```python
import torch

def _softmax(x, axis=-1):
    return torch.softmax(x, dim=axis)
def _gelu(x):
    return torch.nn.functional.gelu(x, approximate="tanh")
def _silu(x):
    return torch.nn.functional.silu(x)
def _sigmoid(x):
    return torch.sigmoid(x)
def _layernorm(x, axis=-1, eps=1e-5):
    m = x.mean(dim=axis, keepdim=True)
    v = x.var(dim=axis, keepdim=True, unbiased=False)
    return (x - m) / torch.sqrt(v + eps)
def _scatter_add(tgt, idx, src, axis=0):
    return tgt.clone().index_add_(axis, idx, src)
def _segment_sum(src, seg, n):
    out = torch.zeros((n,) + src.shape[1:], dtype=src.dtype)
    return out.index_add_(0, seg, src)
def _pad(x, pw):
    flat = tuple(v for pair in reversed(pw) for v in pair)
    return torch.nn.functional.pad(x, flat)

kernel_name = 'interleave_layers__sliding_local_dilated__linear_kernel_hedgehog__prompt'

def setup_inputs():

    _w = (lambda a: (lambda t: t.to(torch.bfloat16) if t.is_floating_point() else t)(torch.as_tensor(a)))
    def _A_setup():
        B, S, D = 1, 2048, 1024
        return {
            "x": _w((lambda *s: torch.randn(*s, dtype=torch.bfloat16) * 0.05)(B, S, D)),
            "wqkv": _w((lambda *s: torch.randn(*s, dtype=torch.bfloat16) * 0.05)(D, 3 * D)),
            "wo": _w((lambda *s: torch.randn(*s, dtype=torch.bfloat16) * 0.05)(D, D)),
        }
    _A_in = _A_setup()

    def _B_setup():
        B, S, D, H = 1, 2048, 1024, 32
        Dh = D // H
        return {
            "x": _w((lambda *s: torch.randn(*s, dtype=torch.bfloat16) * 0.05)(B, S, D)),
            "wqkv": _w((lambda *s: torch.randn(*s, dtype=torch.bfloat16) * 0.4)(D, 3 * D)),
            "w_phi": _w((lambda *s: torch.randn(*s, dtype=torch.bfloat16) * 0.4)(H, Dh, Dh) * (Dh ** -0.5)),
            "wo": _w((lambda *s: torch.randn(*s, dtype=torch.bfloat16) * 0.1)(D, D)),
        }
    _B_in = _B_setup()

    return {
        "A_x": _A_in["x"],
        "A_wqkv": _A_in["wqkv"],
        "A_wo": _A_in["wo"],
        "B_x": _B_in["x"],
        "B_wqkv": _B_in["wqkv"],
        "B_w_phi": _B_in["w_phi"],
        "B_wo": _B_in["wo"],
    }

def reference(A_x, A_wqkv, A_wo, B_x, B_wqkv, B_w_phi, B_wo):
    def _A_ref(x, wqkv, wo):
        B, S, D = x.shape
        H = 32; Dh = D // H
        W, dil = 8, 2
        qkv = (lambda x, *d: x.permute(*d))((x @ wqkv).reshape(B, S, 3, H, Dh), 2, 0, 3, 1, 4)
        q, k, v = qkv[0], qkv[1], qkv[2]
        scale = Dh ** -0.5
        pos = torch.arange(S)
        off = torch.arange(W) * dil
        idx = pos[:, None] - off[None, :]
        mask = idx >= 0
        idx_c = torch.where(mask, idx, 0).reshape(-1)
        kg = k[:, :, idx_c, :].reshape(B, H, S, W, Dh)
        vg = v[:, :, idx_c, :].reshape(B, H, S, W, Dh)
        logits = torch.einsum("bhsd,bhswd->bhsw", q, kg) * scale
        logits = torch.where(mask[None, None, :, :], logits, -1e9)
        attn = _softmax(logits, axis=-1)
        o = torch.einsum("bhsw,bhswd->bhsd", attn, vg)
        return (lambda x, *d: x.permute(*d))(o, 0, 2, 1, 3).reshape(B, S, D) @ wo

    def _B_ref(x, wqkv, w_phi, wo):
        B, S, D = x.shape
        H = 32; Dh = D // H
        qkv = (lambda x, *d: x.permute(*d))((x @ wqkv).reshape(B, S, 3, H, Dh), 2, 0, 3, 1, 4)
        q, k, v = qkv[0], qkv[1], qkv[2]
        pq = torch.einsum("bhsd,hdf->bhsf", q, w_phi)
        pk = torch.einsum("bhsd,hdf->bhsf", k, w_phi)
        phi_q = torch.concatenate([_softmax(pq, axis=-1), _softmax(-pq, axis=-1)], axis=-1)
        phi_k = torch.concatenate([_softmax(pk, axis=-1), _softmax(-pk, axis=-1)], axis=-1)
        kv = torch.einsum("bhsf,bhsd->bhfd", phi_k, v)
        z  = torch.einsum("bhsf->bhf", phi_k)
        num = torch.einsum("bhqf,bhfd->bhqd", phi_q, kv)
        den = torch.einsum("bhqf,bhf->bhq", phi_q, z)[..., None] + 1e-6
        o = num / den
        return (lambda x, *d: x.permute(*d))(o, 0, 2, 1, 3).reshape(B, S, D) @ wo

    x = A_x
    x = x + _A_ref(_layernorm(x, axis=-1), A_wqkv, A_wo)
    x = x + _B_ref(_layernorm(x, axis=-1), B_wqkv, B_w_phi, B_wo)
    x = x + _A_ref(_layernorm(x, axis=-1), A_wqkv, A_wo)
    x = x + _B_ref(_layernorm(x, axis=-1), B_wqkv, B_w_phi, B_wo)
    return x
```

```cpp
#define WSLEEP 0
#include <hip/hip_runtime.h>
#include <hip/hip_bf16.h>
#include <stdint.h>
#include <type_traits>
#pragma clang diagnostic ignored "-Winline-asm"
#pragma clang diagnostic ignored "-Wunused-value"

typedef unsigned short u16;
typedef __attribute__((ext_vector_type(8))) short bf16x8;
typedef __attribute__((ext_vector_type(4))) short bf16x4;
typedef __attribute__((ext_vector_type(4))) float f32x4;
typedef __attribute__((ext_vector_type(4))) unsigned u32x4;
typedef __attribute__((ext_vector_type(2))) unsigned u32x2;
typedef __attribute__((ext_vector_type(8))) float f32x8;
typedef __attribute__((ext_vector_type(16))) unsigned u32x16;
typedef __attribute__((ext_vector_type(16))) float f32x16;
typedef __attribute__((ext_vector_type(32))) float f32x32;
template <int I, typename T> __device__ __forceinline__ f32x4 quad(const T& v) { return __builtin_shufflevector(v, v, 4 * I, 4 * I + 1, 4 * I + 2, 4 * I + 3); }

constexpr int SEQ = 2048, DM = 1024, NH = 32;
#define ST(i)
constexpr int KVP = 64 * 32 + 64;

typedef __attribute__((ext_vector_type(2))) float f32x2;
typedef __attribute__((ext_vector_type(2))) __bf16 bf16x2_t;
__device__ __forceinline__ unsigned pk2(float a, float b) {
  f32x2 v = {a, b};
  bf16x2_t r = __builtin_convertvector(v, bf16x2_t);
  return __builtin_bit_cast(unsigned, r);
}

__device__ __forceinline__ bf16x4 tr_read(const char* p) {
  return __builtin_amdgcn_ds_read_tr16_b64_v4i16((__attribute__((address_space(3))) bf16x4*)p);
}
template <typename T> __device__ __forceinline__ __attribute__((address_space(3))) T& lds(unsigned a) { return *(__attribute__((address_space(3))) T*)(size_t)a; }
__device__ __forceinline__ bf16x4 tr_read(unsigned a) { return __builtin_amdgcn_ds_read_tr16_b64_v4i16(&lds<bf16x4>(a)); }
__device__ __forceinline__ f32x4 xget(unsigned a) { const f32x2 p = lds<f32x2>(a), q = lds<f32x2>(a + 512); return f32x4{p[0], p[1], q[0], q[1]}; }
__device__ __forceinline__ bf16x8 cat8(bf16x4 a, bf16x4 b) {
  bf16x8 f = {a[0], a[1], a[2], a[3], b[0], b[1], b[2], b[3]};
  return f;
}
__device__ __forceinline__ void glds16s(const void* sbase, unsigned voff, unsigned lds_base) {
  asm volatile("s_mov_b32 m0, %2\n\ts_nop 0\n\tglobal_load_lds_dwordx4 %0, %1" :: "v"(voff), "s"(sbase), "s"(lds_base) : "memory", "m0");
}
__device__ __forceinline__ void glds16c(const void* sbase, unsigned voff, unsigned lds_base) {
  asm volatile("s_mov_b32 m0, %2\n\ts_nop 0\n\tglobal_load_lds_dwordx4 %0, %1 sc1" :: "v"(voff), "s"(sbase), "s"(lds_base) : "memory", "m0");
}
#define GL_ "\n\tglobal_load_lds_dwordx4 "
__device__ __forceinline__ void glds4c(const void* sbase, unsigned v, unsigned lds_base) {
  asm volatile("s_mov_b32 m0, %2\n\ts_nop 0" GL_ "%0, %1 sc1" GL_ "%0, %1 offset:1024 sc1" GL_ "%0, %1 offset:2048 sc1" GL_ "%0, %1 offset:3072 sc1"
               :: "v"(v), "s"(sbase), "s"(lds_base) : "memory", "m0");
}
__device__ __forceinline__ void glds4v(const void* sbase, unsigned v0, unsigned v1, unsigned v2, unsigned v3, unsigned lds_base) {
  asm volatile("s_mov_b32 m0, %5\n\ts_nop 0" GL_ "%0, %4" GL_ "%1, %4 offset:1024" GL_ "%2, %4 offset:2048" GL_ "%3, %4 offset:3072"
               :: "v"(v0), "v"(v1), "v"(v2), "v"(v3), "s"(sbase), "s"(lds_base) : "memory", "m0");
}
__device__ __forceinline__ void glds2v(const void* sbase, unsigned v0, unsigned v1, unsigned lds_base) {
  asm volatile("s_mov_b32 m0, %3\n\ts_nop 0" GL_ "%0, %2" GL_ "%1, %2 offset:1024" :: "v"(v0), "v"(v1), "s"(sbase), "s"(lds_base) : "memory", "m0");
}
__device__ __forceinline__ void gld16(u32x4& d, unsigned voff, const void* sbase) {
  asm volatile("global_load_dwordx4 %0, %1, %2" : "=v"(d) : "v"(voff), "s"(sbase) : "memory");
}
__device__ __forceinline__ unsigned lds_addr(const void* p) { return (unsigned)(size_t)p; }
#define WAITV(n) asm volatile("s_waitcnt vmcnt(" #n ")" ::: "memory")
#define WAITVN(n) asm volatile("s_waitcnt vmcnt(%0)" :: "n"(n) : "memory")
#define WAITL0 do { asm volatile("" ::: "memory"); __builtin_amdgcn_s_waitcnt(0xC07F); asm volatile("" ::: "memory"); } while (0)
#define BAR __builtin_amdgcn_s_barrier()
#define ARRIVED(...) do { WAITV(0); __builtin_amdgcn_sched_barrier(0); asm volatile("" : __VA_ARGS__); } while (0)

__device__ __forceinline__ float vmaxf(float a, float b) { float r; asm("v_max_f32 %0, %1, %2" : "=v"(r) : "v"(a), "v"(b)); return r; }
template <int N> __device__ __forceinline__ float dpp_ror(float x) {
  return __uint_as_float(__builtin_amdgcn_update_dpp(0u, __float_as_uint(x), 0x120 + N, 0xf, 0xf, false));
}
__device__ __forceinline__ float r16_sum(float x) { x += dpp_ror<8>(x); x += dpp_ror<4>(x); x += dpp_ror<2>(x); return x + dpp_ror<1>(x); }
__device__ __forceinline__ float vmax3f(float a, float b, float c) { float r; asm("v_max3_f32 %0, %1, %2, %3" : "=v"(r) : "v"(a), "v"(b), "v"(c)); return r; }
__device__ __forceinline__ void max8pn(const f32x4& a, const f32x4& b, float& mx, float& mn) {
  asm("v_max3_f32 %0, %2, %3, %4\n\tv_max3_f32 %1, -%2, -%3, -%4\n\tv_max3_f32 %0, %0, %5, %6\n\tv_max3_f32 %1, %1, -%5, -%6\n\t"
      "v_max3_f32 %0, %0, %7, %8\n\tv_max3_f32 %1, %1, -%7, -%8\n\tv_max_f32 %0, %0, %9\n\tv_max_f32 %1, %1, -%9\n\ts_nop 0"
      : "=&v"(mx), "=&v"(mn) : "v"(a[0]), "v"(a[1]), "v"(a[2]), "v"(a[3]), "v"(b[0]), "v"(b[1]), "v"(b[2]), "v"(b[3]));
}
__device__ __forceinline__ float vmaxf_n(float a, float b) { float r; asm("v_max_f32 %0, %1, %2\n\ts_nop 0" : "=v"(r) : "v"(a), "v"(b)); return r; }
__device__ __forceinline__ float vmax3f_n(float a, float b, float c) { float r; asm("v_max3_f32 %0, %1, %2, %3\n\ts_nop 0" : "=v"(r) : "v"(a), "v"(b), "v"(c)); return r; }
template <bool MAXOP> __device__ __forceinline__ float rop(float a, float b) { return MAXOP ? vmaxf_n(a, b) : a + b; }
template <bool MAXOP> __device__ __forceinline__ void xr2(float& a, float& b) {
  auto r = __builtin_amdgcn_permlane16_swap(__float_as_uint(a), __float_as_uint(b), false, false);
  unsigned u = __float_as_uint(rop<MAXOP>(__uint_as_float(r[0]), __uint_as_float(r[1])));
  auto r2 = __builtin_amdgcn_permlane32_swap(u, u, false, false);
  u = __float_as_uint(rop<MAXOP>(__uint_as_float(r2[0]), __uint_as_float(r2[1])));
  auto r3 = __builtin_amdgcn_permlane16_swap(u, u, false, false);
  a = __uint_as_float(r3[0]); b = __uint_as_float(r3[1]);
}
template <bool MAXOP> __device__ __forceinline__ void xr4(float& a, float& b, float& c, float& d) {
  auto r = __builtin_amdgcn_permlane16_swap(__float_as_uint(a), __float_as_uint(b), false, false);
  auto q = __builtin_amdgcn_permlane16_swap(__float_as_uint(c), __float_as_uint(d), false, false);
  const unsigned ab = __float_as_uint(rop<MAXOP>(__uint_as_float(r[0]), __uint_as_float(r[1])));
  const unsigned cd = __float_as_uint(rop<MAXOP>(__uint_as_float(q[0]), __uint_as_float(q[1])));
  auto t = __builtin_amdgcn_permlane32_swap(ab, cd, false, false);
  const unsigned u = __float_as_uint(rop<MAXOP>(__uint_as_float(t[0]), __uint_as_float(t[1])));
  auto h = __builtin_amdgcn_permlane32_swap(u, u, false, false);
  auto p0 = __builtin_amdgcn_permlane16_swap(h[0], h[0], false, false);
  auto p1 = __builtin_amdgcn_permlane16_swap(h[1], h[1], false, false);
  a = __uint_as_float(p0[0]); b = __uint_as_float(p0[1]); c = __uint_as_float(p1[0]); d = __uint_as_float(p1[1]);
}

__device__ __forceinline__ unsigned xs4(float a, float b, float c, float d) {
  auto r = __builtin_amdgcn_permlane16_swap(__float_as_uint(a), __float_as_uint(b), false, false);
  auto q = __builtin_amdgcn_permlane16_swap(__float_as_uint(c), __float_as_uint(d), false, false);
  auto t = __builtin_amdgcn_permlane32_swap(__float_as_uint(__uint_as_float(r[0]) + __uint_as_float(r[1])), __float_as_uint(__uint_as_float(q[0]) + __uint_as_float(q[1])), false, false);
  return __float_as_uint(__uint_as_float(t[0]) + __uint_as_float(t[1]));
}

__device__ __forceinline__ f32x4 sum4(const u32x4 (&v)[4]) {
  float r0, r1, r2, r3, t0, t1, t2, t3;
  asm("v_add_f32 %0, %8, %12\n\tv_add_f32 %1, %9, %13\n\tv_add_f32 %2, %10, %14\n\tv_add_f32 %3, %11, %15\n\t"
      "v_add_f32 %4, %16, %20\n\tv_add_f32 %5, %17, %21\n\tv_add_f32 %6, %18, %22\n\tv_add_f32 %7, %19, %23\n\t"
      "v_add_f32 %0, %0, %4\n\tv_add_f32 %1, %1, %5\n\tv_add_f32 %2, %2, %6\n\tv_add_f32 %3, %3, %7\n\ts_nop 0"
      : "=&v"(r0), "=&v"(r1), "=&v"(r2), "=&v"(r3), "=&v"(t0), "=&v"(t1), "=&v"(t2), "=&v"(t3)
      : "v"(v[0][0]), "v"(v[0][1]), "v"(v[0][2]), "v"(v[0][3]), "v"(v[1][0]), "v"(v[1][1]), "v"(v[1][2]), "v"(v[1][3]),
        "v"(v[2][0]), "v"(v[2][1]), "v"(v[2][2]), "v"(v[2][3]), "v"(v[3][0]), "v"(v[3][1]), "v"(v[3][2]), "v"(v[3][3]));
  return f32x4{r0, r1, r2, r3};
}
#define MFMA(a, b, c) __builtin_amdgcn_mfma_f32_16x16x32_bf16(a, b, c, 0, 0, 0)

typedef unsigned long long u64;
typedef __attribute__((address_space(1))) unsigned long long gu64;
constexpr unsigned WS_FLAGS = 4096;
constexpr int FLS = 64;
constexpr unsigned WS_G0 = 65536;
constexpr unsigned WS_CSP = WS_G0 + SEQ * 16;
constexpr unsigned WS_ST = WS_CSP + 16 * 16 * 768;
constexpr unsigned WS_X = 1u << 21;
constexpr unsigned WS_O = WS_X + 3u * SEQ * DM * 2;
constexpr unsigned KVPB = KVP * 2, KVFB = KVP * 4;
constexpr unsigned EMPTY = 0xffffffffu;
constexpr unsigned WS_KV = WS_O + 4u * SEQ * DM * 2;
constexpr unsigned KVLB = 16u * NH * KVPB + 16u * KVFB;
constexpr unsigned WS_HL = WS_KV + 2u * KVLB;
constexpr unsigned WS_CSA = WS_HL + 512u * 4096;
constexpr unsigned WS_CPA = WS_CSA + 256u * 1024;
constexpr unsigned WS_END = WS_CPA + 16 * 16 * 768;
constexpr int LDS_TOTAL = 163840;
constexpr int OFF_WO = 49152;
template <int V> using IC = std::integral_constant<int, V>;
__device__ __forceinline__ bf16x8 ones8() { unsigned e = 0x3F803F80u; asm volatile("" : "+v"(e)); return __builtin_bit_cast(bf16x8, u32x4{e, e, e, e}); }
__device__ __forceinline__ u32x4 empty4() { unsigned e = EMPTY; asm volatile("" : "+v"(e)); return u32x4{e, e, e, e}; }
__device__ __forceinline__ u64 korder(int v, int own) {
  const u64 t = v == 0 ? 0x3b210fedca987654ull : v == 1 ? 0xb37654210fedca98ull : v == 2 ? 0x3ba987654210fedcull : 0xb3fedca987654210ull;
  const u64 inv = v == 0 ? 0xa987e6543210fdcbull : v == 1 ? 0x6543f210dcbae987ull : v == 2 ? 0x3210edcba987f654ull : 0xdcbaf9876543e210ull;
  const u64 low = (1ull << (4 * ((inv >> (4 * own)) & 15))) - 1ull;
  return (u64)own | ((t & low) << 4) | (t & ~((low << 4) | 15ull));
}
__device__ __forceinline__ u64 korder0(int rt) {
  const int r = rt & 7;
  const u64 own = (0x765432765432ull >> (4 * (r < 6 ? r : r - 6))) & 0xffffffull, oth = 0x7654321076543210ull >> (4 * r);
  const u64 t = 0x10ull | ((oth & 15ull) << 8) | (own << 12) | ((oth >> 4) << 36);
  return t | (rt < 8 ? 0x8888888000000800ull : 0x0000000888888088ull);
}
__device__ __forceinline__ int knib(u64 t, int s) { return (int)(t >> (4 * s)) & 15; }
__device__ __forceinline__ unsigned kfirst(u64 t) { return (1u << knib(t, 1)) | (1u << knib(t, 2)) | (1u << knib(t, 3)) | (1u << knib(t, 4)); }
__device__ __forceinline__ int kv1(int bid) { return (bid + (0x4924 >> ((bid >> 2) & 14))) & 3; }

template <typename T> __device__ __forceinline__ T* uni(T* p) {
  const unsigned long long v = (unsigned long long)p;
  const unsigned lo = __builtin_amdgcn_readfirstlane((unsigned)v), hi = __builtin_amdgcn_readfirstlane((unsigned)(v >> 32));
  return (T*)(((unsigned long long)hi << 32) | lo);
}
__device__ __forceinline__ int lane_id() {
  int l;
  asm volatile("v_mbcnt_lo_u32_b32 %0, -1, 0\n\tv_mbcnt_hi_u32_b32 %0, -1, %0" : "=v"(l));
  __builtin_assume(l >= 0 && l < 64);
  return l;
}
#define IDS \
  const int lane = lane_id(); \
  asm volatile("" : "+s"(w)); \
  const int tid = w * 64 + lane; \
  int bid = blockIdx.x; asm volatile("" : "+s"(bid)); \
  ws = uni(ws); asm volatile("" : "+s"(ws)); \
  layer = __builtin_amdgcn_readfirstlane(layer); x0 = uni(x0); wo = uni(wo); \
    \
  const int rt = 2 * (bid & 4) + ((bid >> 3) & 7); \
  const int hp = 4 * (bid & 3) + (bid >> 6); \
  const int m0 = rt * 128; \
  const int fr = lane & 15, g = lane >> 4; \
  const int q4 = (lane & 15) >> 2, p4 = lane & 3; \
  const __amdgpu_buffer_rsrc_t rsW = __builtin_amdgcn_make_buffer_rsrc((void*)ws, (short)0, (int)WS_END, 0x00020000); \
  (void)m0; (void)fr; (void)q4; (void)p4; (void)rsW; (void)g;

#define LANES \
  const int lane = lane_id(), tid = w * 64 + lane, fr = lane & 15, g = lane >> 4, q4 = (lane & 15) >> 2, p4 = lane & 3; \
  (void)tid; (void)fr; (void)g; (void)q4; (void)p4;

__device__ __forceinline__ void publish(__amdgpu_buffer_rsrc_t rsW, int tid, unsigned idx, u64 v) {
  unsigned off = WS_FLAGS + idx * FLS;
  asm volatile("" : "+v"(off), "+v"(v));
  WAITV(0);
  WAITL0; BAR;
  if (tid == 192) __builtin_amdgcn_raw_buffer_store_b64(__builtin_bit_cast(u32x2, v), rsW, off, 0, 16);
  asm volatile("" ::: "memory");
}
__device__ __forceinline__ u64 poll_flags(char* ws, u64 E, int set, int rt, int hp, int lane, unsigned n, u64 mask, u64 now = ~0ull) {
  u64 miss = 0;
  const int l = lane & 15, g = lane >> 4;
  const int fi = set * 256 + (g == 2 ? l * 16 + hp : rt * 16 + l);
  const unsigned need = ((mask >> lane) & 1) ? n : 0u;
  gu64* fp = (gu64*)(ws + (need ? WS_FLAGS + fi * FLS : 0));
  _Pragma("unroll 1") for (unsigned spins = 0; spins < (1u << 18); ++spins) {
    const u64 d = __hip_atomic_load(fp, __ATOMIC_RELAXED, __HIP_MEMORY_SCOPE_AGENT) - E;
    const bool ok = (need == 0u) || (d >= (u64)need && d <= 16ull);
    miss = __ballot(!ok);
    if (!(miss & now)) break;
    __builtin_amdgcn_s_sleep(1);
  }
  asm volatile("s_waitcnt vmcnt(0)" ::: "memory");
  return miss;
}

__device__ __forceinline__ u64 poll_eq(const char* ws, unsigned off, u64 want, u64 mask, u64 now) {
  u64 miss;
  _Pragma("unroll 1") for (unsigned spins = 0; spins < (1u << 18); ++spins) {
    u64 d;
    asm volatile("global_load_dwordx2 %0, %1, %2 sc1\n\ts_waitcnt vmcnt(0)" : "=v"(d) : "v"(off), "s"(ws) : "memory");
    miss = __ballot(d != want) & mask;
    if (!(miss & now)) break;
    __builtin_amdgcn_s_sleep(1);
  }
  return miss;
}

#define G1_MACROS \
  ".macro G1M rb, j, s, z\n" \
  " .set g1d, 160+24*(\\rb)+4*(((\\j)+2)-6*((\\j)>>2))\n .set g1a, 80+40*(\\s)+4*(\\rb)\n .set g1b, 96+40*(\\s)+4*(\\j)\n" \
  " .if ((\\j)>>1) == 1\n  .set g1t, g1a\n  .set g1a, g1b\n  .set g1b, g1t\n .endif\n" \
  " .if \\z\n  v_mfma_f32_16x16x32_bf16 v[g1d:g1d+3], v[g1b:g1b+3], v[g1a:g1a+3], 0\n" \
  " .else\n  v_mfma_f32_16x16x32_bf16 v[g1d:g1d+3], v[g1b:g1b+3], v[g1a:g1a+3], v[g1d:g1d+3]\n .endif\n" \
  ".endm\n" \
  ".macro G1RA rb, s, ra, oa\n" \
  " ds_read_b128 v[80+40*(\\s)+4*(\\rb):83+40*(\\s)+4*(\\rb)], \\ra offset:(\\oa)+(\\rb)*2048\n" \
  ".endm\n" \
  ".macro G1RB k, s, r0, r1, ob\n" \
  " .if (\\k) & 2\n  ds_read_b64_tr_b16 v[96+40*(\\s)+2*(\\k):97+40*(\\s)+2*(\\k)], \\r1 offset:(\\ob)+(((\\k)>>2)+1-3*((\\k)>>3))*2048+((\\k)&1)*512\n" \
  " .else\n  ds_read_b64_tr_b16 v[96+40*(\\s)+2*(\\k):97+40*(\\s)+2*(\\k)], \\r0 offset:(\\ob)+(((\\k)>>2)+1-3*((\\k)>>3))*2048+((\\k)&1)*512\n .endif\n" \
  ".endm\n" \
    \
  ".macro G1BK i, g, r0, r1, ob\n G1RB 4*(\\g)+((\\i)&3), (\\i)>>2, \\r0, \\r1, (\\ob)+((\\i)>>2)*12288\n.endm\n" \
  ".macro G1AK rb, s, a0, a1, oa\n .if \\s\n  G1RA \\rb, 1, \\a1, \\oa\n .else\n  G1RA \\rb, 0, \\a0, \\oa\n .endif\n.endm\n" \
  ".macro G1RDB g, r0, r1, ob\n .set g1i, 0\n .rept 8\n  G1BK g1i, \\g, \\r0, \\r1, \\ob\n  .set g1i, g1i+1\n .endr\n.endm\n" \
  ".macro G1RDA a0, a1, oa\n .set g1i, 0\n .rept 8\n  G1AK (g1i>>1)^2, g1i&1, \\a0, \\a1, \\oa\n  .set g1i, g1i+1\n .endr\n.endm\n" \
    \
  ".macro G1Q g, rb, q, i, z\n G1M \\rb, 2*(\\g)+((((\\i)>>1)^(\\q))&1), (((\\i)+1)>>1)&1, (\\z)&&(((\\i)&1)==0)\n.endm\n" \
    \
  ".macro G1SG g, z, rd, a0, a1, r0, r1, o\n" \
  " .set g1i, 0\n .rept 16\n" \
  "  .if ((\\g) == 0) && (g1i == 8) && (\\rd)\n   s_waitcnt lgkmcnt(7)\n   s_nop 0\n  .endif\n" \
  "  .if ((\\g) == 0) && (g1i == 15)\n   s_waitcnt lgkmcnt(0)\n  .endif\n" \
  "  G1Q \\g, (g1i>>2)^(2-((\\g)&1)), g1i>>2, g1i&3, \\z\n" \
  "  .if \\rd\n" \
  "   .if (\\g) == 0\n    .if g1i < 7\n     G1BK g1i, 2, \\r0, \\r1, \\o\n    .elseif g1i == 8\n     G1BK 7, 2, \\r0, \\r1, \\o\n    .endif\n" \
  "   .elseif (\\g) == 1\n    .if g1i < 8\n     G1BK g1i, 0, \\r0, \\r1, \\o\n    .endif\n" \
  "   .elseif (g1i & 2) == 0\n    G1BK (g1i&1)+2*(g1i>>2), 1, \\r0, \\r1, \\o\n" \
  "   .elseif (\\rd) == 1\n    G1AK (g1i>>2)^2, 1-(g1i&1), \\a0, \\a1, \\o\n   .endif\n" \
  "  .endif\n" \
  "  .set g1i, g1i+1\n .endr\n" \
  ".endm\n" \
    \
    \
  ".macro G1W wg, wrb, wq, i, xs\n" \
  " .set g1j, 2*(\\wg)+((((\\i)>>1)^(\\wq))&1)\n .set g1j, g1j+2-6*(g1j>>2)\n .set g1x, 160+24*(\\wrb)+4*g1j+2*((\\i)&1)\n" \
  " ds_write_b64 \\xs, v[g1x:g1x+1] offset:(((\\wrb)-2)*4+g1j)*1024+((\\i)&1)*512\n" \
  ".endm\n" \
  ".macro G1Z g, rb, q, m, wg, wrb, wq, xs, r0, r1\n" \
  " .set g1i, 0\n .rept 4\n" \
  "  .if (\\m) == 2\n   G1W \\wg, \\wrb, \\wq, g1i, \\xs\n  .endif\n" \
  "  .if ((\\m) > 2) && ((g1i & 1) == 0)\n   G1W \\wg, \\wrb, \\wq, 2*((\\m)-3)+(g1i>>1), \\xs\n  .endif\n" \
  "  G1Q \\g, \\rb, \\q, g1i, 0\n" \
  "  .if (\\m) == 1\n   G1BK (\\wg)+g1i, 2, \\r0, \\r1, 0\n  .endif\n" \
  "  .set g1i, g1i+1\n .endr\n" \
  ".endm\n"
#define G1_SLIDE \
  "s_cmp_eq_u32 %[wm], 0\n v_pk_fma_f32 v[240:241], v[96:97], v[138:139], v[240:241] op_sel_hi:[1,0,1]\n " \
  "v_pk_fma_f32 v[242:243], v[98:99], v[138:139], v[242:243] op_sel_hi:[1,0,1]\n " \
  "v_pk_fma_f32 v[244:245], v[100:101], v[138:139], v[244:245] op_sel_hi:[1,0,1]\n " \
  "v_pk_fma_f32 v[246:247], v[102:103], v[138:139], v[246:247] op_sel_hi:[1,0,1]\n v_pk_mul_f32 v[240:241], v[138:139], v[240:241] op_sel:[1,0]\n " \
  "v_pk_mul_f32 v[242:243], v[138:139], v[242:243] op_sel:[1,0]\n v_pk_mul_f32 v[244:245], v[138:139], v[244:245] op_sel:[1,0]\n " \
  "v_pk_mul_f32 v[246:247], v[138:139], v[246:247] op_sel:[1,0]\n v_pk_fma_f32 v[248:249], v[104:105], v[144:145], v[248:249] op_sel_hi:[1,0,1]\n " \
  "v_pk_fma_f32 v[250:251], v[106:107], v[144:145], v[250:251] op_sel_hi:[1,0,1]\n " \
  "v_pk_fma_f32 v[252:253], v[104:105], v[144:145], v[252:253] op_sel:[0,1,0]\n " \
  "v_pk_fma_f32 v[254:255], v[106:107], v[144:145], v[254:255] op_sel:[0,1,0]\n v_pk_mul_f32 v[248:249], v[248:249], v[108:109]\n " \
  "v_pk_mul_f32 v[250:251], v[250:251], v[110:111]\n v_pk_mul_f32 v[252:253], v[252:253], v[108:109]\n v_pk_mul_f32 v[254:255], v[254:255], v[110:111]\n " \
  "v_cvt_pk_bf16_f32 v240, v240, v241\n v_cvt_pk_bf16_f32 v241, v242, v243\n v_cvt_pk_bf16_f32 v242, v244, v245\n v_cvt_pk_bf16_f32 v243, v246, v247\n " \
  "v_cvt_pk_bf16_f32 v248, v248, v249\n v_cvt_pk_bf16_f32 v249, v250, v251\n v_cvt_pk_bf16_f32 v255, v254, v255\n v_cvt_pk_bf16_f32 v254, v252, v253\n " \
  "v_min_u32 v104, -2, v240\n v_min_u32 v105, -2, v241\n v_min_u32 v106, -2, v242\n v_min_u32 v107, -2, v243\n v_min_u32 v108, -2, v248\n " \
  "v_min_u32 v109, -2, v249\n v_min_u32 v110, -2, v254\n v_min_u32 v111, -2, v255\n s_waitcnt lgkmcnt(0)\n s_cbranch_scc1 .Lg1d%=\n " \
  "buffer_store_dwordx4 v[104:107], v55, %[rs], 0 offen sc1\n buffer_store_dwordx4 v[108:111], v55, %[rs], 0 offen offset:1024 sc1\n s_branch .Lg1e%=\n " \
  ".Lg1d%=:\n v_mov_b32 v53, 1\n ds_write_b128 v58, v[104:107]\n ds_write_b128 v58, v[108:111] offset:1024\n ds_write_b32 v59, v53\n .Lg1e%=:\n " \
  "v_pk_fma_f32 v[232:233], v[148:149], v[138:139], v[232:233] op_sel_hi:[1,0,1]\n " \
  "v_pk_fma_f32 v[234:235], v[150:151], v[138:139], v[234:235] op_sel_hi:[1,0,1]\n " \
  "v_pk_fma_f32 v[236:237], v[88:89], v[138:139], v[236:237] op_sel_hi:[1,0,1]\n " \
  "v_pk_fma_f32 v[238:239], v[90:91], v[138:139], v[238:239] op_sel_hi:[1,0,1]\n " \
  "v_pk_fma_f32 v[216:217], v[96:97], v[136:137], v[216:217] op_sel_hi:[1,0,1]\n " \
  "v_pk_fma_f32 v[218:219], v[98:99], v[136:137], v[218:219] op_sel_hi:[1,0,1]\n " \
  "v_pk_fma_f32 v[220:221], v[100:101], v[136:137], v[220:221] op_sel_hi:[1,0,1]\n " \
  "v_pk_fma_f32 v[222:223], v[102:103], v[136:137], v[222:223] op_sel_hi:[1,0,1]\n v_pk_mul_f32 v[216:217], v[136:137], v[216:217] op_sel:[1,0]\n " \
  "v_pk_mul_f32 v[218:219], v[136:137], v[218:219] op_sel:[1,0]\n v_pk_mul_f32 v[220:221], v[136:137], v[220:221] op_sel:[1,0]\n " \
  "v_pk_mul_f32 v[222:223], v[136:137], v[222:223] op_sel:[1,0]\n v_cvt_pk_bf16_f32 v232, v232, v233\n v_cvt_pk_bf16_f32 v233, v234, v235\n " \
  "v_cvt_pk_bf16_f32 v234, v236, v237\n v_cvt_pk_bf16_f32 v235, v238, v239\n v_cvt_pk_bf16_f32 v216, v216, v217\n v_cvt_pk_bf16_f32 v217, v218, v219\n " \
  "v_cvt_pk_bf16_f32 v218, v220, v221\n v_cvt_pk_bf16_f32 v219, v222, v223\n " \
  "v_pk_fma_f32 v[208:209], v[148:149], v[136:137], v[208:209] op_sel_hi:[1,0,1]\n " \
  "v_pk_fma_f32 v[210:211], v[150:151], v[136:137], v[210:211] op_sel_hi:[1,0,1]\n " \
  "v_pk_fma_f32 v[212:213], v[88:89], v[136:137], v[212:213] op_sel_hi:[1,0,1]\n " \
  "v_pk_fma_f32 v[214:215], v[90:91], v[136:137], v[214:215] op_sel_hi:[1,0,1]\n " \
  "v_pk_fma_f32 v[192:193], v[96:97], v[142:143], v[192:193] op_sel_hi:[1,0,1]\n " \
  "v_pk_fma_f32 v[194:195], v[98:99], v[142:143], v[194:195] op_sel_hi:[1,0,1]\n " \
  "v_pk_fma_f32 v[196:197], v[100:101], v[142:143], v[196:197] op_sel_hi:[1,0,1]\n " \
  "v_pk_fma_f32 v[198:199], v[102:103], v[142:143], v[198:199] op_sel_hi:[1,0,1]\n v_pk_mul_f32 v[192:193], v[142:143], v[192:193] op_sel:[1,0]\n " \
  "v_pk_mul_f32 v[194:195], v[142:143], v[194:195] op_sel:[1,0]\n v_pk_mul_f32 v[196:197], v[142:143], v[196:197] op_sel:[1,0]\n " \
  "v_pk_mul_f32 v[198:199], v[142:143], v[198:199] op_sel:[1,0]\n v_mfma_f32_16x16x32_bf16 v[120:123], v[240:243], v[232:235], v[40:43]\n " \
  "v_mfma_f32_16x16x32_bf16 v[236:239], v[216:219], v[232:235], v[36:39]\n v_cvt_pk_bf16_f32 v208, v208, v209\n v_cvt_pk_bf16_f32 v209, v210, v211\n " \
  "v_cvt_pk_bf16_f32 v210, v212, v213\n v_cvt_pk_bf16_f32 v211, v214, v215\n v_cvt_pk_bf16_f32 v192, v192, v193\n v_cvt_pk_bf16_f32 v193, v194, v195\n " \
  "v_cvt_pk_bf16_f32 v194, v196, v197\n v_cvt_pk_bf16_f32 v195, v198, v199\n " \
  "v_pk_fma_f32 v[184:185], v[148:149], v[142:143], v[184:185] op_sel_hi:[1,0,1]\n " \
  "v_pk_fma_f32 v[186:187], v[150:151], v[142:143], v[186:187] op_sel_hi:[1,0,1]\n " \
  "v_pk_fma_f32 v[188:189], v[88:89], v[142:143], v[188:189] op_sel_hi:[1,0,1]\n " \
  "v_pk_fma_f32 v[190:191], v[90:91], v[142:143], v[190:191] op_sel_hi:[1,0,1]\n " \
  "v_pk_fma_f32 v[168:169], v[96:97], v[140:141], v[168:169] op_sel_hi:[1,0,1]\n " \
  "v_pk_fma_f32 v[170:171], v[98:99], v[140:141], v[170:171] op_sel_hi:[1,0,1]\n " \
  "v_pk_fma_f32 v[172:173], v[100:101], v[140:141], v[172:173] op_sel_hi:[1,0,1]\n " \
  "v_pk_fma_f32 v[174:175], v[102:103], v[140:141], v[174:175] op_sel_hi:[1,0,1]\n v_pk_mul_f32 v[168:169], v[140:141], v[168:169] op_sel:[1,0]\n " \
  "v_pk_mul_f32 v[170:171], v[140:141], v[170:171] op_sel:[1,0]\n v_pk_mul_f32 v[172:173], v[140:141], v[172:173] op_sel:[1,0]\n " \
  "v_pk_mul_f32 v[174:175], v[140:141], v[174:175] op_sel:[1,0]\n v_mfma_f32_16x16x32_bf16 v[220:223], v[216:219], v[208:211], v[40:43]\n " \
  "v_mfma_f32_16x16x32_bf16 v[212:215], v[192:195], v[208:211], v[36:39]\n v_cvt_pk_bf16_f32 v184, v184, v185\n v_cvt_pk_bf16_f32 v185, v186, v187\n " \
  "v_cvt_pk_bf16_f32 v186, v188, v189\n v_cvt_pk_bf16_f32 v187, v190, v191\n v_cvt_pk_bf16_f32 v168, v168, v169\n v_cvt_pk_bf16_f32 v169, v170, v171\n " \
  "v_cvt_pk_bf16_f32 v170, v172, v173\n v_cvt_pk_bf16_f32 v171, v174, v175\n " \
  "v_pk_fma_f32 v[160:161], v[148:149], v[140:141], v[160:161] op_sel_hi:[1,0,1]\n " \
  "v_pk_fma_f32 v[162:163], v[150:151], v[140:141], v[162:163] op_sel_hi:[1,0,1]\n " \
  "v_pk_fma_f32 v[164:165], v[88:89], v[140:141], v[164:165] op_sel_hi:[1,0,1]\n " \
  "v_pk_fma_f32 v[166:167], v[90:91], v[140:141], v[166:167] op_sel_hi:[1,0,1]\n " \
  "v_pk_fma_f32 v[176:177], v[128:129], v[144:145], v[176:177] op_sel_hi:[1,0,1]\n " \
  "v_pk_fma_f32 v[178:179], v[130:131], v[144:145], v[178:179] op_sel_hi:[1,0,1]\n " \
  "v_pk_fma_f32 v[180:181], v[128:129], v[144:145], v[180:181] op_sel:[0,1,0]\n " \
  "v_pk_fma_f32 v[182:183], v[130:131], v[144:145], v[182:183] op_sel:[0,1,0]\n v_pk_mul_f32 v[176:177], v[176:177], v[92:93]\n " \
  "v_pk_mul_f32 v[178:179], v[178:179], v[94:95]\n v_pk_mul_f32 v[180:181], v[180:181], v[92:93]\n v_pk_mul_f32 v[182:183], v[182:183], v[94:95]\n " \
  "v_mfma_f32_16x16x32_bf16 v[196:199], v[192:195], v[184:187], v[40:43]\n v_mfma_f32_16x16x32_bf16 v[188:191], v[168:171], v[184:187], v[36:39]\n " \
  "v_cvt_pk_bf16_f32 v160, v160, v161\n v_cvt_pk_bf16_f32 v161, v162, v163\n v_cvt_pk_bf16_f32 v162, v164, v165\n v_cvt_pk_bf16_f32 v163, v166, v167\n " \
  "v_cvt_pk_bf16_f32 v174, v176, v177\n v_cvt_pk_bf16_f32 v175, v178, v179\n v_cvt_pk_bf16_f32 v180, v180, v181\n v_cvt_pk_bf16_f32 v181, v182, v183\n " \
  "v_pk_fma_f32 v[224:225], v[112:113], v[144:145], v[224:225] op_sel_hi:[1,0,1]\n " \
  "v_pk_fma_f32 v[226:227], v[114:115], v[144:145], v[226:227] op_sel_hi:[1,0,1]\n " \
  "v_pk_fma_f32 v[228:229], v[112:113], v[144:145], v[228:229] op_sel:[0,1,0]\n " \
  "v_pk_fma_f32 v[230:231], v[114:115], v[144:145], v[230:231] op_sel:[0,1,0]\n v_pk_mul_f32 v[224:225], v[224:225], v[116:117]\n " \
  "v_pk_mul_f32 v[226:227], v[226:227], v[118:119]\n v_pk_mul_f32 v[228:229], v[228:229], v[116:117]\n v_pk_mul_f32 v[230:231], v[230:231], v[118:119]\n " \
  "v_mfma_f32_16x16x32_bf16 v[124:127], v[168:171], v[160:163], v[40:43]\n v_cvt_pk_bf16_f32 v246, v224, v225\n v_cvt_pk_bf16_f32 v247, v226, v227\n " \
  "v_cvt_pk_bf16_f32 v252, v228, v229\n v_cvt_pk_bf16_f32 v253, v230, v231\n v_max3_f32 v108, v236, v237, v238\n v_max3_f32 v106, v212, v213, v214\n " \
  "v_max3_f32 v109, v239, v120, v121\n v_max3_f32 v107, v215, v220, v221\n v_max3_f32 v108, v108, v122, v123\n v_max3_f32 v106, v106, v222, v223\n " \
  "v_max_f32 v108, v108, v109\n v_max_f32 v106, v106, v107\n v_max3_f32 v104, v188, v189, v190\n v_max3_f32 v105, v191, v196, v197\n " \
  "v_max3_f32 v104, v104, v198, v199\n v_pk_fma_f32 v[200:201], v[152:153], v[144:145], v[200:201] op_sel_hi:[1,0,1]\n " \
  "v_pk_fma_f32 v[202:203], v[154:155], v[144:145], v[202:203] op_sel_hi:[1,0,1]\n v_max_f32 v104, v104, v105\n v_mov_b32 v110, v108\n " \
  "v_pk_fma_f32 v[204:205], v[152:153], v[144:145], v[204:205] op_sel:[0,1,0]\n " \
  "v_pk_fma_f32 v[206:207], v[154:155], v[144:145], v[206:207] op_sel:[0,1,0]\n v_permlane16_swap_b32 v104, v106\n v_permlane16_swap_b32 v108, v110\n " \
  "v_pk_mul_f32 v[200:201], v[200:201], v[156:157]\n v_pk_mul_f32 v[202:203], v[202:203], v[158:159]\n v_max_f32 v104, v104, v106\n " \
  "v_max_f32 v108, v108, v110\n v_pk_mul_f32 v[204:205], v[204:205], v[156:157]\n v_pk_mul_f32 v[206:207], v[206:207], v[158:159]\n " \
  "v_permlane32_swap_b32 v104, v108\n v_mul_f32 v60, 0x3e8293ee, v139\n v_mul_f32 v62, 0x3e8293ee, v137\n v_max_f32 v104, v104, v108\n " \
  "v_mov_b32 v108, v104\n v_mul_f32 v56, 0x3e8293ee, v143\n v_mul_f32 v146, 0x3e8293ee, v141\n v_permlane32_swap_b32 v104, v108\n " \
  "v_mov_b32 v132, 0x3f803f80\n v_mov_b32 v133, 0x3f803f80\n v_mov_b32 v106, v104\n v_mov_b32 v134, 0x3f803f80\n v_mov_b32 v135, 0x3f803f80\n " \
  "v_permlane16_swap_b32 v104, v106\n v_cvt_pk_bf16_f32 v244, v200, v201\n v_cvt_pk_bf16_f32 v245, v202, v203\n v_cvt_pk_bf16_f32 v250, v204, v205\n " \
  "v_cvt_pk_bf16_f32 v251, v206, v207\n v_cvt_pk_bf16_f32 v176, v200, v201\n v_cvt_pk_bf16_f32 v177, v202, v203\n v_cvt_pk_bf16_f32 v182, v204, v205\n " \
  "v_cvt_pk_bf16_f32 v183, v206, v207\n v_mul_f32_e64 v61, v60, -v108\n v_mul_f32_e64 v63, v62, -v106\n v_mul_f32_e64 v57, v56, -v104\n " \
  "v_pk_fma_f32 v[236:237], v[236:237], v[60:61], v[60:61] op_sel:[0,0,1] op_sel_hi:[1,0,1]\n " \
  "v_pk_fma_f32 v[238:239], v[238:239], v[60:61], v[60:61] op_sel:[0,0,1] op_sel_hi:[1,0,1]\n " \
  "v_pk_fma_f32 v[120:121], v[120:121], v[60:61], v[60:61] op_sel:[0,0,1] op_sel_hi:[1,0,1]\n " \
  "v_pk_fma_f32 v[122:123], v[122:123], v[60:61], v[60:61] op_sel:[0,0,1] op_sel_hi:[1,0,1]\n v_exp_f32 v236, v236\n v_exp_f32 v237, v237\n " \
  "v_exp_f32 v238, v238\n v_exp_f32 v239, v239\n v_exp_f32 v120, v120\n v_exp_f32 v121, v121\n v_exp_f32 v122, v122\n v_exp_f32 v123, v123\n " \
  "v_pk_fma_f32 v[212:213], v[212:213], v[62:63], v[62:63] op_sel:[0,0,1] op_sel_hi:[1,0,1]\n " \
  "v_pk_fma_f32 v[214:215], v[214:215], v[62:63], v[62:63] op_sel:[0,0,1] op_sel_hi:[1,0,1]\n " \
  "v_pk_fma_f32 v[220:221], v[220:221], v[62:63], v[62:63] op_sel:[0,0,1] op_sel_hi:[1,0,1]\n " \
  "v_pk_fma_f32 v[222:223], v[222:223], v[62:63], v[62:63] op_sel:[0,0,1] op_sel_hi:[1,0,1]\n v_cvt_pk_bf16_f32 v236, v236, v237\n " \
  "v_cvt_pk_bf16_f32 v237, v238, v239\n v_cvt_pk_bf16_f32 v238, v120, v121\n v_cvt_pk_bf16_f32 v239, v122, v123\n v_exp_f32 v212, v212\n " \
  "v_exp_f32 v213, v213\n v_mfma_f32_16x16x32_bf16 v[120:123], v[132:135], v[236:239], 0\n " \
  "v_mfma_f32_16x16x32_bf16 v[232:235], v[246:249], v[236:239], 0\n v_mfma_f32_16x16x32_bf16 v[240:243], v[252:255], v[236:239], 0\n " \
  "v_exp_f32 v214, v214\n v_exp_f32 v215, v215\n v_exp_f32 v220, v220\n v_exp_f32 v221, v221\n v_exp_f32 v222, v222\n v_exp_f32 v223, v223\n " \
  "v_pk_fma_f32 v[188:189], v[188:189], v[56:57], v[56:57] op_sel:[0,0,1] op_sel_hi:[1,0,1]\n " \
  "v_pk_fma_f32 v[190:191], v[190:191], v[56:57], v[56:57] op_sel:[0,0,1] op_sel_hi:[1,0,1]\n " \
  "v_pk_fma_f32 v[196:197], v[196:197], v[56:57], v[56:57] op_sel:[0,0,1] op_sel_hi:[1,0,1]\n " \
  "v_pk_fma_f32 v[198:199], v[198:199], v[56:57], v[56:57] op_sel:[0,0,1] op_sel_hi:[1,0,1]\n v_cvt_pk_bf16_f32 v212, v212, v213\n " \
  "v_cvt_pk_bf16_f32 v213, v214, v215\n v_cvt_pk_bf16_f32 v214, v220, v221\n v_cvt_pk_bf16_f32 v215, v222, v223\n v_exp_f32 v188, v188\n " \
  "s_cmp_eq_u32 %[wm], 0\n s_cbranch_scc1 .Lg1f%=\n ds_read_b32 v53, v59\n ds_read_b128 v[80:83], v58\n ds_read_b64 v[172:173], v58 offset:1024\n " \
  "ds_read_b64 v[178:179], v58 offset:1032\n s_branch .Lg1g%=\n .Lg1f%=:\n buffer_load_dwordx4 v[80:83], v55, %[rs], 0 offen sc1\n " \
  "buffer_load_dwordx2 v[172:173], v55, %[rs], 0 offen offset:1024 sc1\n buffer_load_dwordx2 v[178:179], v55, %[rs], 0 offen offset:1032 sc1\n " \
  ".Lg1g%=:\n v_exp_f32 v189, v189\n v_mfma_f32_16x16x32_bf16 v[220:223], v[132:135], v[212:215], 0\n " \
  "v_mfma_f32_16x16x32_bf16 v[224:227], v[244:247], v[212:215], 0\n v_mfma_f32_16x16x32_bf16 v[228:231], v[250:253], v[212:215], 0\n " \
  "v_exp_f32 v190, v190\n v_exp_f32 v191, v191\n v_exp_f32 v196, v196\n v_exp_f32 v197, v197\n v_exp_f32 v198, v198\n v_exp_f32 v199, v199\n " \
  "v_rcp_f32 v120, v120\n v_cvt_pk_bf16_f32 v188, v188, v189\n v_pk_mul_f32 v[232:233], v[232:233], v[120:121] op_sel_hi:[1,0]\n " \
  "v_pk_mul_f32 v[234:235], v[234:235], v[120:121] op_sel_hi:[1,0]\n v_pk_mul_f32 v[240:241], v[240:241], v[120:121] op_sel_hi:[1,0]\n " \
  "v_pk_mul_f32 v[242:243], v[242:243], v[120:121] op_sel_hi:[1,0]\n v_cvt_pk_bf16_f32 v189, v190, v191\n v_cvt_pk_bf16_f32 v190, v196, v197\n " \
  "v_cvt_pk_bf16_f32 v191, v198, v199\n v_rcp_f32 v220, v220\n v_cvt_pk_bf16_f32 v232, v232, v233\n v_cvt_pk_bf16_f32 v233, v234, v235\n " \
  "v_mfma_f32_16x16x32_bf16 v[196:199], v[132:135], v[188:191], 0\n v_mfma_f32_16x16x32_bf16 v[200:203], v[174:177], v[188:191], 0\n " \
  "v_mfma_f32_16x16x32_bf16 v[204:207], v[180:183], v[188:191], 0\n v_cvt_pk_bf16_f32 v234, v240, v241\n v_cvt_pk_bf16_f32 v235, v242, v243\n " \
  "buffer_store_dwordx4 v[232:235], v51, %[rs], 0 offen offset:3072 sc1\n ds_write_b128 v50, v[232:235] offset:3072\n " \
  "v_pk_mul_f32 v[224:225], v[224:225], v[220:221] op_sel_hi:[1,0]\n v_pk_mul_f32 v[226:227], v[226:227], v[220:221] op_sel_hi:[1,0]\n " \
  "v_pk_mul_f32 v[228:229], v[228:229], v[220:221] op_sel_hi:[1,0]\n v_pk_mul_f32 v[230:231], v[230:231], v[220:221] op_sel_hi:[1,0]\n " \
  "v_cvt_pk_bf16_f32 v224, v224, v225\n v_cvt_pk_bf16_f32 v225, v226, v227\n v_cvt_pk_bf16_f32 v226, v228, v229\n v_cvt_pk_bf16_f32 v227, v230, v231\n " \
  "v_rcp_f32 v196, v196\n buffer_store_dwordx4 v[224:227], v51, %[rs], 0 offen offset:2048 sc1\n ds_write_b128 v50, v[224:227] offset:2048\n " \
  "v_pk_mul_f32 v[200:201], v[200:201], v[196:197] op_sel_hi:[1,0]\n v_pk_mul_f32 v[202:203], v[202:203], v[196:197] op_sel_hi:[1,0]\n " \
  "v_pk_mul_f32 v[204:205], v[204:205], v[196:197] op_sel_hi:[1,0]\n v_pk_mul_f32 v[206:207], v[206:207], v[196:197] op_sel_hi:[1,0]\n " \
  "v_cvt_pk_bf16_f32 v200, v200, v201\n v_cvt_pk_bf16_f32 v201, v202, v203\n v_cvt_pk_bf16_f32 v202, v204, v205\n v_cvt_pk_bf16_f32 v203, v206, v207\n " \
  "buffer_store_dwordx4 v[200:203], v51, %[rs], 0 offen offset:1024 sc1\n ds_write_b128 v50, v[200:203] offset:1024\n s_mov_b32 vcc_hi, 0x40000\n " \
  "s_cmp_eq_u32 %[wm], 0\n s_cbranch_scc1 .Lg1h%=\n s_waitcnt lgkmcnt(3)\n .Lg1i%=:\n v_readfirstlane_b32 vcc_lo, v53\n s_nop 1\n " \
  "s_cmp_lg_u32 vcc_lo, 0\n s_cbranch_scc1 .Lg1k%=\n ds_read_b32 v53, v59\n ds_read_b128 v[80:83], v58\n ds_read_b64 v[172:173], v58 offset:1024\n " \
  "ds_read_b64 v[178:179], v58 offset:1032\n s_waitcnt lgkmcnt(0)\n s_sub_u32 vcc_hi, vcc_hi, 1\n s_cbranch_scc0 .Lg1i%=\n s_branch .Lg1k%=\n .Lg1h%=:\n " \
  "s_mov_b32 m0, 0x40000\n s_waitcnt vmcnt(3)\n .Lg1j%=:\n v_max3_u32 v104, v80, v81, v82\n v_max3_u32 v105, v83, v172, v173\n " \
  "v_max3_u32 v104, v104, v178, v179\n v_max_u32 v104, v104, v105\n v_cmp_eq_u32 vcc, -1, v104\n s_cbranch_vccz .Lg1k%=\n " \
  "buffer_load_dwordx4 v[80:83], v55, %[rs], 0 offen sc1\n buffer_load_dwordx2 v[172:173], v55, %[rs], 0 offen offset:1024 sc1\n " \
  "buffer_load_dwordx2 v[178:179], v55, %[rs], 0 offen offset:1032 sc1\n s_waitcnt vmcnt(0)\n s_sub_u32 m0, m0, 1\n s_cbranch_scc0 .Lg1j%=\n .Lg1k%=:\n " \
  "v_mfma_f32_16x16x32_bf16 v[164:167], v[80:83], v[160:163], v[44:47]\n s_nop 7\n v_max3_f32 v104, v164, v165, v166\n " \
  "v_max3_f32 v105, v167, v124, v125\n v_max3_f32 v104, v104, v126, v127\n v_max_f32 v104, v104, v105\n v_mov_b32 v105, v104\n s_nop 1\n " \
  "v_permlane16_swap_b32 v104, v105\n v_max_f32 v104, v104, v105\n v_mov_b32 v105, v104\n s_nop 1\n v_permlane32_swap_b32 v104, v105\n " \
  "v_max_f32 v104, v104, v105\n v_mul_f32_e64 v147, v146, -v104\n " \
  "v_pk_fma_f32 v[164:165], v[164:165], v[146:147], v[146:147] op_sel:[0,0,1] op_sel_hi:[1,0,1]\n " \
  "v_pk_fma_f32 v[166:167], v[166:167], v[146:147], v[146:147] op_sel:[0,0,1] op_sel_hi:[1,0,1]\n " \
  "v_pk_fma_f32 v[124:125], v[124:125], v[146:147], v[146:147] op_sel:[0,0,1] op_sel_hi:[1,0,1]\n " \
  "v_pk_fma_f32 v[126:127], v[126:127], v[146:147], v[146:147] op_sel:[0,0,1] op_sel_hi:[1,0,1]\n v_exp_f32 v164, v164\n v_exp_f32 v165, v165\n " \
  "v_exp_f32 v166, v166\n v_exp_f32 v167, v167\n v_exp_f32 v124, v124\n v_exp_f32 v125, v125\n v_exp_f32 v126, v126\n v_exp_f32 v127, v127\n " \
  "v_cvt_pk_bf16_f32 v164, v164, v165\n v_cvt_pk_bf16_f32 v165, v166, v167\n v_cvt_pk_bf16_f32 v166, v124, v125\n v_cvt_pk_bf16_f32 v167, v126, v127\n " \
  "s_nop 1\n v_mfma_f32_16x16x32_bf16 v[124:127], v[132:135], v[164:167], 0\n v_mfma_f32_16x16x32_bf16 v[168:171], v[172:175], v[164:167], 0\n " \
  "v_mfma_f32_16x16x32_bf16 v[84:87], v[178:181], v[164:167], 0\n s_nop 5\n v_rcp_f32 v124, v124\n s_nop 0\n " \
  "v_pk_mul_f32 v[168:169], v[168:169], v[124:125] op_sel_hi:[1,0]\n v_pk_mul_f32 v[170:171], v[170:171], v[124:125] op_sel_hi:[1,0]\n " \
  "v_pk_mul_f32 v[84:85], v[84:85], v[124:125] op_sel_hi:[1,0]\n v_pk_mul_f32 v[86:87], v[86:87], v[124:125] op_sel_hi:[1,0]\n " \
  "v_cvt_pk_bf16_f32 v168, v168, v169\n v_cvt_pk_bf16_f32 v169, v170, v171\n v_cvt_pk_bf16_f32 v170, v84, v85\n v_cvt_pk_bf16_f32 v171, v86, v87\n " \
  "buffer_store_dwordx4 v[168:171], v51, %[rs], 0 offen offset:0 sc1\n ds_write_b128 v50, v[168:171] offset:0\n "
#define G1_PURGE \
  ".purgem G1M\n .purgem G1RA\n .purgem G1RB\n .purgem G1BK\n .purgem G1AK\n .purgem G1RDB\n .purgem G1RDA\n .purgem G1Q\n .purgem G1SG\n .purgem G1W\n .purgem G1Z\n"
#define G1_CLOBBER \
  "v80", "v81", "v82", "v83", "v84", "v85", "v86", "v87", "v88", "v89", "v90", "v91", "v92", "v93", "v94", "v95", "v96", "v97", "v98", "v99", "v100", \
  "v101", "v102", "v103", "v104", "v105", "v106", "v107", "v108", "v109", "v110", "v111", "v112", "v113", "v114", "v115", "v116", "v117", "v118", \
  "v119", "v120", "v121", "v122", "v123", "v124", "v125", "v126", "v127", "v128", "v129", "v130", "v131", "v132", "v133", "v134", "v135", "v136", \
  "v137", "v138", "v139", "v140", "v141", "v142", "v143", "v144", "v145", "v146", "v147", "v148", "v149", "v150", "v151", "v152", "v153", "v154", \
  "v155", "v156", "v157", "v158", "v159"

template <int MODE>
__device__ __forceinline__ void g1_phase(char* smem, char* ws, u64& E, int w, f32x16& xq, int layer, const u16* __restrict__ x0, const u16* __restrict__ W,
                                         const u16* __restrict__ wo, const u16* __restrict__ wphi,
                                         const u16* __restrict__ wn1, const u16* __restrict__ wn2) {
  constexpr bool HH = (MODE == 1);
  constexpr int ABYTES = 128 * 128;
  constexpr int STAGE = ABYTES + 3 * 8192;
  constexpr int OFF_FX = 16384;
  constexpr int OFF_SP = 3 * STAGE;
  constexpr int OFF_RS = !HH ? 159744 : OFF_SP + 16384;
  constexpr int OFF_CS = OFF_RS + (HH ? 1152 : 1024);
  constexpr int OFF_WP = OFF_CS + 1536;
  constexpr int OFF_JK = LDS_TOTAL - 1024;
  constexpr int OFF_PK = 0;
  constexpr int OFF_VF = OFF_WP + 4096;
  constexpr int OFF_KV = 16384;
  constexpr int OFF_R2 = HH ? OFF_VF + 16384 : OFF_CS + 1024;
  const int XS = 98304 + (w & 3) * 8192;
  constexpr int OFF_RD = OFF_CS + 768;
  constexpr int PSTR = 144;
  IDS
  W = uni(W); wphi = uni(wphi); wn1 = uni(wn1); wn2 = uni(wn2);
  const int kg = w >> 2, wm = (w >> 1) & 1, wn = w & 1;
  const bool first = !HH && layer == 0;
  f32x4 acc[2][6];
  u32x4 pw[8];
  ST(layer * 24 + 0)
  const int R0 = wm * 64 + (HH ? kg * 32 : 0);
  u32x16 pa;
#define pcs pa[0]
#define prs pa[1]
#define opo pa[2]
#define ogo pa[3]
#define pkw pa[5]
#define hof pa[7]
#define dm pa[8]
#define dm2 pa[9]
#define pcv pa[4]
#define pwf pa[5]
#define prv pa[6]
#define pvf pa[7]
#define ppk pa[10]
#define ppa pa[11]
#define pvr pa[12]
#define gkv pa[13]
#define gkz pa[14]
#define pwl pa[15]
#define pwm pa[4]
#define plb pa[10]
#define prd pa[11]
#define dm3 pa[12]
  f32x4 bias[3];
  const unsigned sb = lds_addr(smem);
  auto prep = [&]() {

    pcs = sb + OFF_CS + wn * 128 + g * 16;
    prs = sb + OFF_RS + (R0 + fr) * 8;
    opo = (unsigned)(wn * 8192 + R0 * 64 + lane * 16);
    ogo = opo + WS_O + layer * (SEQ * DM * 2) + (unsigned)((rt * NH + 2 * hp) * 8192);
    opo += sb;
    pcv = sb + OFF_CS + (wn * 32 + 128 + fr) * 4;
    prv = sb + OFF_R2 + (R0 + 4 * g) * 4;
    if (layer < 2) {
      constexpr unsigned kvn = WS_KV + MODE * KVLB;
      const int t = HH ? tid : tid & 255;
      dm = kvn + (unsigned)((rt * NH + 2 * hp) * KVPB) + t * 16;
      dm2 = t < 16 ? dm + 8192 : t >= 64 && t < 97 ? kvn + 16u * NH * KVPB + (unsigned)(hp * KVFB + rt * 528 + (t - 64) * 16) : 0x7fff0000u;
      if (!HH) dm3 = WS_HL + (unsigned)((256 + rt * 16 + hp) * 4096 + t * 16);
    }
    if constexpr (!HH) {
      plb = sb + OFF_FX + wn * 2048 + lane * 16;
      prd = sb + OFF_RD + wn * 4;
      hof = m0 + wm == 0 ? 0x7fff0000u : WS_HL + (unsigned)((layer * 128 + rt * 16 - 16 + wm * 16 + hp) * 4096 + wn * 2048 + lane * 16);
      const unsigned out = ~((0x5555u << 2 << fr) >> (4 * g));
#pragma unroll
      for (int i = 0; i < 8; ++i) {
        unsigned t;
        asm("v_bfe_i32 %0, %1, %2, 1" : "=v"(t) : "v"(out), "n"((i >> 2) * 16 + (i & 3)));
        bias[i >> 2][i & 3] = __uint_as_float(t & 0xff61b1e6u);
      }
      bias[2] = m0 + R0 == 0 ? f32x4{-3.0e38f, -3.0e38f, -3.0e38f, -3.0e38f} : bias[0];
    } else {
      const int u = wm * 2 + kg;
      pwf = sb + OFF_WP + wn * 2048 + (4 * g + q4) * 64 + p4 * 8;
      pvf = sb + OFF_VF + w * 2048 + lane * 16;
      ppk = sb + OFF_PK + w * (32 * PSTR) + fr * PSTR + g * 8;
      ppa = sb + OFF_PK + wn * (32 * PSTR) + (4 * g + q4) * PSTR + u * 32 + p4 * 8;
      pvr = sb + OFF_VF + wn * 2048 + lane * 16;
      const unsigned doff = WS_KV + (layer >> 1) * KVLB + (unsigned)((rt * NH + 2 * hp + wn) * KVPB);
      pwl = (unsigned)(((16 * w + (lane >> 3)) * DM + hp * 64 + ((lane & 7) ^ ((lane >> 4) << 1)) * 8) * 2);
      gkv = doff + u * 1024 + lane * 16;
      gkz = fr == 0 ? doff + 4096 + u * 32 + g * 8 : 0x7fff0000u;
    }
    asm volatile("" : "+{v[48:63]}"(pa), "+{v[36:39]}"(bias[0]), "+{v[40:43]}"(bias[1]), "+{v[44:47]}"(bias[2]));
  };

  auto o_put = [&](int rb, const f32x4& a, const f32x4& b) {
    const u32x4 v = {pk2(a[0], a[1]), pk2(a[2], a[3]), pk2(b[0], b[1]), pk2(b[2], b[3])};
    __builtin_amdgcn_raw_buffer_store_b128(v, rsW, ogo + rb * 1024, 0, 16);
    lds<u32x4>(opo + rb * 1024) = v;
  };

  if (kg == 1) {

    const int lw = w & 3;
    const int kB = 16 * lw + (lane >> 3);
    const int fB = (kB >> 1) & 1;
    const int cB0 = ((((lane & 7) >> 1) ^ fB) << 1) | (lane & 1), cB1 = ((((lane & 7) >> 1) ^ (fB | 2)) << 1) | (lane & 1);
    const unsigned vB0 = (unsigned)((kB * 3072 + hp * 64 + cB0 * 8) * 2);
    const unsigned vB1 = (unsigned)(((kB + 8) * 3072 + hp * 64 + cB1 * 8) * 2) - 1024u;

    unsigned vW0, vW1, vP, vX;
    u32x4 jk[2];
    u64 ko2;
    auto ld_pw = [&](int i0) {
#pragma unroll
      for (int i = i0; i < i0 + 4; ++i) gld16(pw[i], vP, wn1 + i * 8 * 3072);
    };
    auto wo_tile = [&](int t) { glds2v(wo + (size_t)knib(ko2, t) * 64 * DM, vW0, vW1, lds_addr(smem) + OFF_WO + t * 8192 + lw * 2048); };
    auto run = [&](auto fst) {
      constexpr bool FIRST = decltype(fst)::value;
      constexpr int N = 10;
      const unsigned vA = FIRST ? (unsigned)(((m0 + lw * 32 + (lane >> 3)) * DM + ((lane & 7) ^ (lane >> 3)) * 8) * 2)
                                : (unsigned)(rt * 262144 + lw * 4096 + lane * 16);
      const char* xa = FIRST ? (const char*)x0 : ws + WS_X + (layer - 1) * (SEQ * DM * 2);
      constexpr int KS = FIRST ? 128 : 16384;
      const u64 kord = FIRST ? korder0(rt) : korder(kv1(bid), hp);
      auto ktile = [&](int ks) { return knib(kord, ks); };
      auto stageA = [&](int ks, int boff) {
        const int kt = ktile(ks);
        const unsigned sA = lds_addr(smem) + boff;
        if (FIRST) glds4v(xa + kt * KS, vA, vA + 15360u, vA + 2 * 15360u, vA + 3 * 15360u, sA + lw * 4096);
        else glds4c(xa + kt * KS, vA, sA + lw * 4096);
      };
      auto stageB = [&](const u16* Wp, int ks, int boff) {
        const int kt = ktile(ks);
        const unsigned sB = lds_addr(smem) + boff + ABYTES;
        const u16* b = Wp + (size_t)kt * 64 * 3072;
        glds4v(b, vB0, vB1, vB0, vB1, sB + lw * 6144);
        glds2v(b + 2048, vB0, vB1, sB + lw * 6144 + 4096);
      };
      bf16x8 sel[3];
      unsigned cso[3], cst;
      if (FIRST) {
        stageA(1, STAGE); stageB(W, 1, STAGE);
        __builtin_amdgcn_sched_barrier(0);
        const int fl = (q4 >> 1) | ((g & 1) << 1), j = 3 * wm + (fr < 3 ? fr : 0);
#pragma unroll
        for (int i = 0; i < 3; ++i) {
          const unsigned o = fr == i ? 0x3F803F80u : 0u;
          sel[i] = __builtin_bit_cast(bf16x8, u32x4{o, o, o, o});
          cso[i] = (unsigned)(2 * STAGE + ABYTES + (g >> 1) * 6144 + (g & 1) * 1024 + q4 * 128 + p4 * 8 + (((wn * 2 + ((wm + i) & 1)) ^ fl) << 5) + ((3 * wm + i) >> 1) * 2048);
          asm volatile("" : "+v"(sel[i]), "+v"(cso[i]));
        }
        cst = fr < 3 ? WS_CPA + (unsigned)(((hp * 16 + rt) * 192 + (j >> 1) * 64 + wn * 32 + (j & 1) * 16 + 4 * g) * 4) : 0x7fff0000u;
        asm volatile("" : "+v"(cst));
        WAITVN(N);
        BAR;
        stageA(2, 2 * STAGE); stageB(W, 2, 2 * STAGE);
      }
      if (!FIRST) {
        stageB(W, 2, 2 * STAGE);
        const char* xa1 = xa + ktile(1) * KS;
        const char* xa2 = xa + ktile(2) * KS;
        unsigned sA = lds_addr(smem) + STAGE + lw * 4096;
        asm volatile("" : "+s"(xa1), "+s"(xa2), "+s"(sA));
        asm volatile("" :: "v"(vA));
        if (lw == 0) {
          const u64 m = kfirst(kord);
          poll_eq(ws, ((m >> lane) & 1) ? WS_FLAGS + (unsigned)(256 + rt * 16 + lane) * FLS : 0u, E + (unsigned)layer, m, ~0ull);
        }
        BAR;
        glds4c(xa1, vA, sA); glds4c(xa2, vA, sA + STAGE);
        __builtin_amdgcn_sched_barrier(0);
      }
      u64 fall, fal2;
      unsigned fao, fa2, vS, vC, dS, dC, dR;
      auto setup = [&]() {
        fao = (unsigned)((256 + rt * 16 + (lane & 15)) * FLS);
        fa2 = (unsigned)(((FIRST ? 512 : 256) + (lane & 15) * 16 + (FIRST ? hp : (hp - ((1 << (lane >> 4)) >> 1)) & 15)) * FLS);
        vP = (unsigned)((rt * 64 + (lane >> 3)) * 6144 + lw * 2048 + ((hp + (1 << lw)) & 15) * 128 + (lane & 7) * 16);
        vS = FIRST ? (unsigned)(WS_G0 + (m0 + lw * 32 + (lane & 31)) * 16)
                   : WS_ST + (unsigned)((layer - 1) * (SEQ * 128) + rt * 16384 + g * 1024 + lw * 256 + fr * 16);
        vC = (FIRST ? WS_CPA : WS_CSP) + (unsigned)(hp * 12288 + g * 768 + lw * 192 + (fr < 12 ? fr : 0) * 16);
        if (!FIRST) vX = HH ? (unsigned)(hp * 4096 + lw * 1024 + lane * 16) : (unsigned)((rt * 16 + hp) * 1024 + lane * 16);
        vW0 = (unsigned)((kB * DM + hp * 64 + ((lane & 7) ^ ((lane >> 4) << 1)) * 8) * 2); vW1 = vW0 + 8 * DM * 2 - 1024u;
        dS = OFF_RS + lw * 256 + (FIRST ? (lane & 31) * 8 : fr * 16 + (g & 1) * 8); dC = OFF_CS + lw * 192 + (fr < 12 ? fr : 0) * 16 + g * 4;
        ko2 = korder(bid & 3, hp);
        dR = OFF_R2 + lw * 128 + (FIRST ? (lane & 31) * 4 : fr * 8 + (g & 1) * 4);
        asm volatile("" : "+v"(dR));
        asm volatile("" : "+v"(vS), "+v"(vX), "+v"(vW0), "+v"(vW1), "+v"(dS));
        asm volatile("" : "+s"(ko2));
        if (FIRST || HH) asm volatile("" : "+v"(vC), "+v"(dC), "+v"(fa2));
        if (FIRST) asm volatile("" : "+v"(vP)); else asm volatile("" : "+v"(fao));
      };
      if (!FIRST) { setup(); prep(); }
      u32x4 sv[4], cv[4];
      auto ld_c = [&](u32x4& d, unsigned v, int so) { asm volatile("buffer_load_dwordx4 %0, %1, %2, %3 offen sc1" : "=v"(d) : "v"(v), "s"(rsW), "s"(so) : "memory"); };
      float tm, tr;
      unsigned tc;
      auto fin = [&]() {
        if (FIRST) {
          const unsigned tag = (unsigned)E + 1u;
          asm volatile("" : "+v"(sv[0]));
          _Pragma("unroll 1") for (unsigned spins = 0; spins < (1u << 18); ++spins) {
            if (__all(sv[0][1] == tag && sv[0][3] == tag)) break;
            __builtin_amdgcn_s_sleep(1);
            asm volatile("buffer_load_dwordx4 %0, %1, %2, 0 offen sc1\n\ts_waitcnt vmcnt(0)" : "=v"(sv[0]) : "v"(vS), "s"(rsW) : "memory");
          }
          tm = __uint_as_float(sv[0][0]); tr = __uint_as_float(sv[0][2]);
        } else {
          asm volatile("" : "+v"(sv[0]), "+v"(sv[1]), "+v"(sv[2]), "+v"(sv[3]));
          const f32x4 t = sum4(sv);
          const unsigned u = xs4(t[0], t[2], t[1], t[3]);
          auto h = __builtin_amdgcn_permlane32_swap(u, u, false, false);
          const float mean = __uint_as_float(h[0]) * (1.0f / 1024.0f);
          const float var = fmaxf(__uint_as_float(h[1]) * (1.0f / 1024.0f) - mean * mean, 0.f);
          tm = -mean; tr = __builtin_amdgcn_rsqf(var + 1e-5f);
        }
      };
      auto cfin = [&]() {
        if (FIRST || HH) {
          asm volatile("" : "+v"(cv[0]), "+v"(cv[1]), "+v"(cv[2]), "+v"(cv[3]));
          const f32x4 t = sum4(cv);
          tc = xs4(t[0], t[1], t[2], t[3]);
        }
      };
      bf16x8 cf[6];
      f32x4 csum = {0, 0, 0, 0};
      if (FIRST) { fall = E + 1u; vX = (unsigned)(bid * 8192 + lw * 2048 + lane * 16); asm volatile("" : "+v"(fall), "+v"(vX)); }
#pragma unroll
      for (int kt = 0; kt < 13; ++kt) {
        const int cur = (kt % 3) * STAGE;
        if (!FIRST && kt == 0) { WAITV(4); }
        else if (FIRST && kt == 4 && lw == 1) { WAITVN(N + 2); }
        else if ((!FIRST && kt == 1 && lw == 3) || ((FIRST || HH) && kt == 10 && lw == 3) || (FIRST && (kt == 2 || kt == 4 || kt == 5))) { WAITVN(N + 1); }
        else { WAITVN(N); }
        if (!FIRST && kt == 2 && lw == 3) {
          asm volatile("" : "+v"(fall));
          if (__any(fall != E + (unsigned)layer)) poll_flags(ws, E, 1, rt, hp, lane, layer, 0xffffull);
        }
        if (HH && kt == 11 && lw == 3) {
          asm volatile("" : "+v"(fal2));
          if (__any(fal2 != E + (unsigned)layer)) {
            _Pragma("unroll 1") for (int j = 0; j < 4; ++j) poll_flags(ws, E, 1, rt, (hp - ((1 << j) >> 1)) & 15, lane, layer, 0xffff00000000ull);
          }
        }
        if (FIRST && kt == 11 && lw == 3) {
          asm volatile("" : "+v"(fal2));
          if (__any(fal2 != E + 1u)) poll_flags(ws, E, 2, rt, hp, lane, 1u, 0xffff00000000ull);
        }
        BAR;
        if (FIRST && kt == 1) {
          asm volatile("" ::: "memory");
#pragma unroll
          for (int i = 0; i < 6; ++i) { const char* t = smem + cso[i >> 1] + (i & 1) * 12288; cf[i] = cat8(tr_read(t), tr_read(t + 512)); }
        }
        if (FIRST && kt >= 2 && kt < 6 && lw == kt - 2) setup();
        if (FIRST && kt >= 6 && kt < 10 && lw == kt - 6) prep();
        stageA(kt + 3, cur);
        if (FIRST && kt == 1) {
          asm volatile("" : "+v"(sel[0]));
#pragma unroll
          for (int i = 0; i < 6; ++i) csum = MFMA(cf[i], sel[i >> 1], csum);
          asm volatile("" : "+v"(csum));
        }
        stageB(W, kt + 3, cur);
        if (FIRST) {
          if (kt == 1) __builtin_amdgcn_raw_buffer_store_b128(__builtin_bit_cast(u32x4, csum), rsW, cst, 0, 16);
          if (kt == 3 && tid == 320) __builtin_amdgcn_raw_buffer_store_b64(__builtin_bit_cast(u32x2, fall), rsW, WS_FLAGS + (unsigned)(512 + rt * 16 + hp) * FLS, 0, 16);
          if (kt == 3 || kt == 4) glds16s(wo + (kt - 3) * 512, vX, lds_addr(smem) + OFF_JK);
        }
        if (!FIRST && kt == 0 && lw == 3) asm volatile("global_load_dwordx2 %0, %1, %2 sc1" : "=v"(fall) : "v"(fao), "s"(ws + WS_FLAGS) : "memory");
        if ((FIRST || HH) && kt == 9 && lw == 3) asm volatile("global_load_dwordx2 %0, %1, %2 sc1" : "=v"(fal2) : "v"(fa2), "s"(ws + WS_FLAGS) : "memory");
      }
      if (FIRST && lw < 3) { ld_pw(0); WAITVN(N + 4); } else { WAITVN(N); }
      BAR;
#pragma unroll
      for (int i = 0; i < (FIRST ? 1 : 4); ++i) ld_c(sv[i], vS, i * 4096);
      if (FIRST || HH) {
#pragma unroll
        for (int i = 0; i < 4; ++i) ld_c(cv[i], vC, i * 3072);
      }
      if (HH) glds16s(wphi, vX, lds_addr(smem) + OFF_WP + lw * 1024);
      else if (!FIRST) glds16c(ws + WS_CSA, vX, lds_addr(smem) + OFF_CS);
      wo_tile(0); wo_tile(1);
      if (HH || (FIRST && lw < 3)) { WAITV(13); } else { WAITV(9); }
      BAR;
      if (HH) { wo_tile(2); wo_tile(3); wo_tile(4); wo_tile(5); }
      if (FIRST) {
        gld16(jk[0], vX, wn2); gld16(jk[1], vX, wn2 + 512);
        if (lw < 3) { ld_pw(4); WAITV(10); } else { WAITV(6); }
      } else {
        WAITVN(HH ? 12 : 4);
      }
      cfin();
      fin();
      asm volatile("" : "+v"(tm), "+v"(tr));
      if (FIRST || HH) asm volatile("" : "+v"(tc));
      if (!FIRST) { WAITV(0); } else if (lw < 3) { WAITV(6); } else { WAITV(2); }
      lds<f32x2>(sb + dS) = f32x2{tm, tr};
      lds<float>(sb + dR) = tm; lds<float>(sb + dR + 512) = tr;
      if (FIRST || HH) lds<unsigned>(sb + dC) = tc;
      if (!HH) lds<unsigned>(prd) = 0u;
      WAITL0;
    };
    typedef std::integral_constant<bool, true> T_;
    typedef std::integral_constant<bool, false> F_;
    __builtin_amdgcn_s_setprio(3);
    if (first) run(T_{}); else run(F_{});
    __builtin_amdgcn_s_setprio(0);
    BAR;
    if constexpr (HH) {
      const unsigned xr = sb + XS + lane * 8;
#pragma unroll
      for (int i = 1; i >= 0; --i)
#pragma unroll
        for (int j = 2; j < 4; ++j) acc[i][j] = xget(xr + (i * 4 + j) * 1024);
      asm volatile("" : "=v"(acc[0][0]), "=v"(acc[0][1]), "=v"(acc[1][0]), "=v"(acc[1][1]));
    } else {
      const int lane = lane_id();
      if (WSLEEP) __builtin_amdgcn_s_sleep(WSLEEP);
#pragma unroll
      for (int t = 2; t < 12; ++t) {
        wo_tile(t);
        if (t == 2 && first) {
        const u32x4 pz = empty4();
        __builtin_amdgcn_raw_buffer_store_b128(pz, rsW, dm, 0, 16);
        __builtin_amdgcn_raw_buffer_store_b128(pz, rsW, dm, 4096, 16);
        __builtin_amdgcn_raw_buffer_store_b128(pz, rsW, dm3, 0, 16);
        __builtin_amdgcn_raw_buffer_store_b128(pz, rsW, dm2, 0, 16);
        if (lw == 3 && lane < 48) __builtin_amdgcn_raw_buffer_store_b128(lds<u32x4>(sb + OFF_CS + lane * 16), rsW, WS_CSA + (unsigned)((rt * 16 + hp) * 1024 + lane * 16), 0, 16);
        }
      }
      BAR;
      if (first) { WAITV(0); asm volatile("" :: "v"(jk[0]), "v"(jk[1])); }
    }
    asm volatile("" : "={v[64:79]}"(xq));
  } else {

    prep();
    if (first) {
      const int row = m0 + hp * 8 + w * 2 + (lane >> 5);
      asm volatile("s_load_dwordx2 %0, %1, 0x0\n\ts_waitcnt lgkmcnt(0)" : "=s"(E) : "s"(ws) : "memory");
      const unsigned tag = (unsigned)E + 1u;
      static_assert(HH || (OFF_SP == 122880 && OFF_R2 + 1024 <= OFF_JK), "addresses used at the top of the kernel");
      WAITV(0);
      u32x4 xs[4];
#pragma unroll
      for (int i = 0; i < 4; ++i) xs[i] = *(const u32x4*)(smem + OFF_SP + w * 4096 + (lane >> 5) * 2048 + (lane & 31) * 16 + i * 512);
      typedef __attribute__((ext_vector_type(2))) __bf16 bf2;
      const bf2 one2 = __builtin_bit_cast(bf2, 0x3F803F80u);
      float sa[2] = {0.f, 0.f}, sq[2] = {0.f, 0.f};
#pragma unroll
      for (int i = 0; i < 16; ++i) {
        const unsigned u = xs[i >> 2][i & 3];
        const bf2 t = __builtin_bit_cast(bf2, u);
        sa[i & 1] = __builtin_amdgcn_fdot2_f32_bf16(t, one2, sa[i & 1], false);
        sq[i & 1] = __builtin_amdgcn_fdot2_f32_bf16(t, t, sq[i & 1], false);
      }
      float sm = r16_sum(sa[0] + sa[1]), m2 = r16_sum(sq[0] + sq[1]);
      {
        auto r = __builtin_amdgcn_permlane16_swap(__float_as_uint(sm), __float_as_uint(sm), false, false);
        auto q = __builtin_amdgcn_permlane16_swap(__float_as_uint(m2), __float_as_uint(m2), false, false);
        sm = __uint_as_float(r[0]) + __uint_as_float(r[1]); m2 = __uint_as_float(q[0]) + __uint_as_float(q[1]);
      }
      const float mean = sm * (1.0f / 1024.0f);
      m2 = fmaxf(m2 - sm * mean, 0.f);
      u32x4 gw = {__float_as_uint(-mean), tag, __float_as_uint(rsqrtf(m2 * (1.0f / 1024.0f) + 1e-5f)), tag};
      if ((lane & 31) == 0) __builtin_amdgcn_raw_buffer_store_b128(gw, rsW, (unsigned)(WS_G0 + row * 16), 0, 16);
      __builtin_amdgcn_raw_buffer_store_b128(empty4(), rsW, WS_HL + (unsigned)((rt * 16 + hp) * 4096 + tid * 16), 0, 16);
    }
    const int fl = (q4 >> 1) | ((g & 1) << 1);
    const unsigned bo = lds_addr(smem) + ABYTES + (g >> 1) * 6144 + (g & 1) * 1024 + q4 * 128 + p4 * 8;
    const unsigned bo0 = bo + (((wn * 2 + 0) ^ fl) << 5), bo1 = bo + (((wn * 2 + 1) ^ fl) << 5);
    const unsigned ar = lds_addr(smem) + (wm * 64 + fr) * 128;
    const unsigned aK0 = ar + ((g ^ (fr & 7)) << 4), aK1 = ar + (((4 + g) ^ (fr & 7)) << 4);
    f32x32 ac0, ac1, ac2;
    asm volatile("" :: "v"(aK0), "v"(aK1), "v"(bo0), "v"(bo1));
    if (first) BAR;
    else publish(rsW, tid, 256 + rt * 16 + hp, E + (unsigned)layer);
#define G1_OUT
#define G1_T(a)
#define G1_TEND
#define G1_TCL
    asm volatile(G1_MACROS
        ".p2align 3\n"
        "G1RDB 0, %[b0], %[b1], 0\n G1RDA %[a0], %[a1], 0\n G1RDB 1, %[b0], %[b1], 0\n"
        "ds_read_b128 v[64:67], %[xa]\n ds_read_b128 v[68:71], %[xa] offset:2048\n"
        "ds_read_b128 v[72:75], %[xa] offset:4096\n ds_read_b128 v[76:79], %[xa] offset:6144\n"
        "s_cmp_eq_u32 %[lay], 0\n s_waitcnt lgkmcnt(12)\n"
        "G1SG 0, 1, 1, %[a0], %[a1], %[b0], %[b1], 0\n"
        "s_mov_b32 vcc_lo, 16\n"
        "G1SG 1, 1, 1, %[a0], %[a1], %[b0], %[b1], 40960\n"
        "G1SG 2, 1, 2, %[a0], %[a1], %[b0], %[b1], 40960\n"
        "s_cbranch_scc1 .Lg1a%=\n"
        "s_barrier\n"
        G1_T(88)
        "s_barrier\n"
        G1_T(92)
        "G1RDA %[a0], %[a1], 40960\n"
        "s_waitcnt lgkmcnt(4)\n"
        "s_branch .Lg1b%=\n"
        ".Lg1a%=:\n"
        G1_T(88)
        "s_barrier\n"
        G1_T(92)
        "G1RDB 0, %[b0], %[b1], 40960\n G1RDA %[a0], %[a1], 40960\n G1RDB 1, %[b0], %[b1], 40960\n"
        ".p2align 3\n s_nop 0\n"
        ".Lg1b%=:\n"
        "s_waitcnt lgkmcnt(8)\n"
        "G1SG 0, 0, 1, %[a0], %[a1], %[b0], %[b1], 40960\n"
        "s_barrier\n"
        "G1SG 1, 0, 1, %[a2], %[a3], %[b2], %[b3], 0\n"
        "G1SG 2, 0, 1, %[a2], %[a3], %[b2], %[b3], 0\n"
        "s_lshr_b32 vcc_lo, vcc_lo, 1\n s_waitcnt vmcnt(0) lgkmcnt(8)\n"
        "G1SG 0, 0, 1, %[a2], %[a3], %[b2], %[b3], 0\n"
        "s_barrier\n"
        "G1SG 1, 0, 1, %[a0], %[a1], %[b0], %[b1], 0\n"
        "G1SG 2, 0, 1, %[a0], %[a1], %[b0], %[b1], 0\n"
        "s_cbranch_scc0 .Lg1c%=\n s_waitcnt lgkmcnt(8)\n"
        "G1SG 0, 0, 1, %[a0], %[a1], %[b0], %[b1], 0\n"
        "s_barrier\n"
        "G1SG 1, 0, 1, %[a0], %[a1], %[b0], %[b1], 40960\n"
        "G1SG 2, 0, 1, %[a0], %[a1], %[b0], %[b1], 40960\n"
        "s_branch .Lg1b%=\n"
        ".Lg1c%=:\n"
        "s_waitcnt lgkmcnt(8)\n"
        ".if %[hh] == 0\n"
        "G1SG 0, 0, 1, %[a0], %[a1], %[b0], %[b1], 0\n s_nop 0\n"
        "G1SG 1, 0, 0, %[a0], %[a1], %[b0], %[b1], 0\n"
        G1_T(96)
        "s_nop 0\n s_barrier\n"
        "G1Q 2, 2, 0, 0, 0\n ds_read2_b64 v[136:139], v49 offset0:32 offset1:48\n"
        "G1Q 2, 2, 0, 1, 0\n ds_read_b128 v[96:99], v48 offset:256\n"
        "G1Q 2, 2, 0, 2, 0\n ds_read_b128 v[100:103], v48 offset:320\n"
        "G1Q 2, 2, 0, 3, 0\n ds_read2_b32 v[144:145], v52 offset1:16\n"
        "G1Q 2, 3, 1, 0, 0\n ds_read_b128 v[104:107], v54 offset:192\n"
        "G1Q 2, 3, 1, 1, 0\n ds_read_b128 v[108:111], v54 offset:704\n"
        "G1Q 2, 3, 1, 2, 0\n ds_read2_b64 v[140:143], v49 offset1:16\n"
        "G1Q 2, 3, 1, 3, 0\n ds_read_b128 v[148:151], v48\n"
        "G1Q 2, 0, 2, 0, 0\n ds_read_b128 v[88:91], v48 offset:64\n"
        "G1Q 2, 0, 2, 1, 0\n ds_read_b128 v[128:131], v54\n"
        "G1Q 2, 0, 2, 2, 0\n ds_read_b128 v[92:95], v54 offset:512\n"
        "G1Q 2, 0, 2, 3, 0\n"
        "G1Q 2, 1, 3, 0, 0\n"
        "G1Q 2, 1, 3, 1, 0\n"
        "G1Q 2, 1, 3, 2, 0\n"
        "G1Q 2, 1, 3, 3, 0\n"
        "ds_read_b128 v[112:115], v54 offset:128\n ds_read_b128 v[116:119], v54 offset:640\n ds_read_b128 v[152:155], v54 offset:64\n ds_read_b128 v[156:159], v54 offset:576\n"
        "s_waitcnt lgkmcnt(4)\n"
        G1_SLIDE
        ".else\n"
        "G1Z 0, 2, 0, 1, 0, 0, 0, %[xs], %[b0], %[b1]\n"
        "G1Z 0, 3, 1, 1, 4, 0, 0, %[xs], %[b0], %[b1]\n"
        "s_waitcnt lgkmcnt(8)\n s_nop 0\n"
        "G1Z 0, 0, 0, 0, 0, 0, 0, %[xs], %[b0], %[b1]\n"
        "G1Z 0, 1, 1, 0, 0, 0, 0, %[xs], %[b0], %[b1]\n"
        "s_waitcnt lgkmcnt(0)\n s_nop 0\n"
        "G1Z 2, 2, 0, 0, 0, 0, 0, %[xs], %[b0], %[b1]\n"
        "G1Z 2, 3, 1, 0, 0, 0, 0, %[xs], %[b0], %[b1]\n"
        "G1Z 1, 3, 0, 0, 0, 0, 0, %[xs], %[b0], %[b1]\n"
        "G1Z 1, 2, 1, 0, 0, 0, 0, %[xs], %[b0], %[b1]\n"
        "G1Z 1, 1, 0, 2, 0, 2, 0, %[xs], %[b0], %[b1]\n"
        "G1Z 1, 0, 1, 2, 0, 3, 1, %[xs], %[b0], %[b1]\n"
        "G1Z 2, 0, 0, 2, 2, 2, 0, %[xs], %[b0], %[b1]\n"
        "G1Z 2, 1, 1, 2, 2, 3, 1, %[xs], %[b0], %[b1]\n"
        G1_T(96)
        "s_waitcnt lgkmcnt(0)\n s_barrier\n"
        "s_nop 7\n"
        ".endif\n"
        G1_TEND G1_PURGE
        : "=&{v[160:191]}"(ac0), "=&{v[192:223]}"(ac1), "=&{v[224:255]}"(ac2), "=&{v[64:79]}"(xq),
          "+{v[48:63]}"(pa), "+{v[36:39]}"(bias[0]), "+{v[40:43]}"(bias[1]), "+{v[44:47]}"(bias[2]) G1_OUT
        : [a0] "v"(aK0), [a1] "v"(aK1), [a2] "v"(aK0 + 2 * STAGE), [a3] "v"(aK1 + 2 * STAGE), [b0] "v"(bo0), [b1] "v"(bo1),
          [b2] "v"(bo0 + 2 * STAGE), [b3] "v"(bo1 + 2 * STAGE), [xs] "v"(lds_addr(smem) + XS + lane * 8), [xa] "v"(wn ? aK1 : aK0),
          [lay] "s"(layer), [hh] "n"(MODE), [rs] "s"(rsW), [wm] "s"(wm)
        : "memory", "scc", "vcc", "m0", G1_TCL G1_CLOBBER);
    acc[0][0] = quad<0>(ac0); acc[0][1] = quad<1>(ac0); acc[0][2] = quad<2>(ac0); acc[0][3] = quad<3>(ac0); acc[0][4] = quad<4>(ac0); acc[0][5] = quad<5>(ac0);
    acc[1][0] = quad<6>(ac0); acc[1][1] = quad<7>(ac0); acc[1][2] = quad<0>(ac1); acc[1][3] = quad<1>(ac1); acc[1][4] = quad<2>(ac1); acc[1][5] = quad<3>(ac1);
    if constexpr (HH) {
      const float cs[2] = {lds<float>(pcv), lds<float>(pcv + 64)};
      f32x4 rm[4], rr[4];
#pragma unroll
      for (int rb = 0; rb < 4; ++rb) { rm[rb] = lds<f32x4>(prv + rb * 64); rr[rb] = lds<f32x4>(prv + 512 + rb * 64); }
#pragma unroll
      for (int h = 0; h < 2; ++h) {
        f32x2 f[8];
#pragma unroll
        for (int i = 0; i < 8; ++i) {
          const int c = i >> 2, rb = (i >> 1) & 1, e = (i & 1) * 2;
          const f32x4 a = h == 0 ? acc[rb][4 + c] : rb ? (c ? quad<7>(ac2) : quad<6>(ac2)) : (c ? quad<1>(ac2) : quad<0>(ac2));
          f[i] = __builtin_elementwise_fma(f32x2{rm[2 * h + rb][e], rm[2 * h + rb][e + 1]}, f32x2{cs[c], cs[c]}, f32x2{a[e], a[e + 1]});
        }
        asm volatile("" : "+v"(f[0]), "+v"(f[1]), "+v"(f[2]), "+v"(f[3]), "+v"(f[4]), "+v"(f[5]), "+v"(f[6]), "+v"(f[7]));
#pragma unroll
        for (int i = 0; i < 8; ++i) f[i] *= f32x2{rr[2 * h + ((i >> 1) & 1)][(i & 1) * 2], rr[2 * h + ((i >> 1) & 1)][(i & 1) * 2 + 1]};
        asm volatile("" : "+v"(f[0]), "+v"(f[1]), "+v"(f[2]), "+v"(f[3]), "+v"(f[4]), "+v"(f[5]), "+v"(f[6]), "+v"(f[7]));
#pragma unroll
        for (int c = 0; c < 2; ++c) lds<u32x4>(pvf + h * 8192 + c * 1024) = u32x4{pk2(f[4 * c][0], f[4 * c][1]), pk2(f[4 * c + 1][0], f[4 * c + 1][1]), pk2(f[4 * c + 2][0], f[4 * c + 2][1]), pk2(f[4 * c + 3][0], f[4 * c + 3][1])};
      }
    }
  }
  ST(layer * 24 + 4)
  {
  const int lane = lane_id(), tid = w * 64 + lane, fr = lane & 15, g = lane >> 4;
  (void)fr; (void)g;
  if constexpr (HH) {
    f32x4 csq[2], csk[2];
    f32x2 st2[2] = {lds<f32x2>(prs), lds<f32x2>(prs + 128)};
#pragma unroll
    for (int c = 0; c < 2; ++c) csk[c] = lds<f32x4>(pcs + 256 + c * 64);
#pragma unroll
    for (int c = 0; c < 2; ++c) csq[c] = lds<f32x4>(pcs + c * 64);
    bf16x8 wf[2];
#pragma unroll
    for (int fb = 0; fb < 2; ++fb) wf[fb] = cat8(tr_read(pwf + fb * 32), tr_read(pwf + fb * 32 + 16 * 64));
    __builtin_amdgcn_sched_barrier(0);
    if (layer == 1) {
      const u32x4 pz = empty4();
      __builtin_amdgcn_raw_buffer_store_b128(pz, rsW, dm, 0, 16);
      if (w < 2) __builtin_amdgcn_raw_buffer_store_b128(pz, rsW, dm2, 0, 16);
    }
    const f32x4 z4 = {0, 0, 0, 0};
    constexpr float L2E = 1.4426950408889634f;
    const unsigned kvo = WS_KV + (layer >> 1) * KVLB, kfo = kvo + 16u * NH * KVPB + hp * KVFB;
    bf16x8 qp[2][2];
    auto phi2 = [&](auto qktag) {
      constexpr int QK = decltype(qktag)::value & 1;
      constexpr int RB0 = (decltype(qktag)::value >> 1) & 1, RB1 = (decltype(qktag)::value & 4) ? RB0 + 1 : 2;
      float pv[2][8];
      f32x4 pm[2][2];
      f32x2 f[2][4];
#pragma unroll
      for (int rb = RB0; rb < RB1; ++rb) {
        asm volatile("" : "+v"(st2[rb]));
        const f32x2 nm = {st2[rb][0], st2[rb][0]};
#pragma unroll
        for (int i = 0; i < 4; ++i) {
          const f32x4& a = acc[rb][2 * QK + (i >> 1)];
          const f32x4& cs = QK ? csk[i >> 1] : csq[i >> 1];
          f[rb][i] = __builtin_elementwise_fma(f32x2{cs[(i & 1) * 2], cs[(i & 1) * 2 + 1]}, nm, f32x2{a[(i & 1) * 2], a[(i & 1) * 2 + 1]});
        }
      }
#pragma unroll
      for (int rb = RB0; rb < RB1; ++rb) asm volatile("" : "+v"(f[rb][0]), "+v"(f[rb][1]), "+v"(f[rb][2]), "+v"(f[rb][3]));
#pragma unroll
      for (int rb = RB0; rb < RB1; ++rb) {
        const bf16x8 xf = __builtin_bit_cast(bf16x8, u32x4{pk2(f[rb][0][0], f[rb][0][1]), pk2(f[rb][1][0], f[rb][1][1]), pk2(f[rb][2][0], f[rb][2][1]), pk2(f[rb][3][0], f[rb][3][1])});
        pm[rb][0] = MFMA(wf[0], xf, z4); pm[rb][1] = MFMA(wf[1], xf, z4);
      }
      if (QK) asm volatile("s_nop 7" : "+v"(pm[0][0]), "+v"(pm[0][1]), "+v"(pm[1][0]), "+v"(pm[1][1]));
      else asm volatile("s_nop 7" : "+v"(pm[RB0][0]), "+v"(pm[RB0][1]));
#pragma unroll
      for (int rb = RB0; rb < RB1; ++rb)
#pragma unroll
        for (int i = 0; i < 4; ++i) { pv[rb][i] = pm[rb][0][i]; pv[rb][4 + i] = pm[rb][1][i]; }
      float mx[2], mn[2];
#pragma unroll
      for (int u = RB0; u < RB1; ++u) max8pn(pm[u][0], pm[u][1], mx[u], mn[u]);
      if (RB1 - RB0 == 2) xr4<true>(mx[0], mn[0], mx[1], mn[1]); else xr2<true>(mx[RB0], mn[RB0]);
      float ep[2][8], en[2][8], sp[2], sn[2];
#pragma unroll
      for (int u = RB0; u < RB1; ++u) {
        const float c = st2[u][1] * L2E, mxs = -mx[u] * c, mns = -mn[u] * c;
#pragma unroll
        for (int i = 0; i < 4; ++i) {
          const f32x2 p2 = {pv[u][2 * i], pv[u][2 * i + 1]};
          const f32x2 tp = __builtin_elementwise_fma(p2, f32x2{c, c}, f32x2{mxs, mxs});
          const f32x2 tn = __builtin_elementwise_fma(-p2, f32x2{c, c}, f32x2{mns, mns});
          ep[u][2 * i] = __builtin_amdgcn_exp2f(tp[0]); ep[u][2 * i + 1] = __builtin_amdgcn_exp2f(tp[1]);
          en[u][2 * i] = __builtin_amdgcn_exp2f(tn[0]); en[u][2 * i + 1] = __builtin_amdgcn_exp2f(tn[1]);
        }
        const f32x2 a2 = (f32x2{ep[u][0], ep[u][1]} + f32x2{ep[u][2], ep[u][3]}) + (f32x2{ep[u][4], ep[u][5]} + f32x2{ep[u][6], ep[u][7]});
        const f32x2 b2 = (f32x2{en[u][0], en[u][1]} + f32x2{en[u][2], en[u][3]}) + (f32x2{en[u][4], en[u][5]} + f32x2{en[u][6], en[u][7]});
        sp[u] = a2[0] + a2[1]; sn[u] = b2[0] + b2[1];
      }
      if (RB1 - RB0 == 2) xr4<false>(sp[0], sn[0], sp[1], sn[1]); else xr2<false>(sp[RB0], sn[RB0]);
#pragma unroll
      for (int rb = RB0; rb < RB1; ++rb) {
        float ip = __builtin_amdgcn_rcpf(sp[rb]), in = __builtin_amdgcn_rcpf(sn[rb]);
        u32x2 o0, o1, o2, o3;
        o0[0] = pk2(ep[rb][0] * ip, ep[rb][1] * ip); o0[1] = pk2(ep[rb][2] * ip, ep[rb][3] * ip);
        o1[0] = pk2(ep[rb][4] * ip, ep[rb][5] * ip); o1[1] = pk2(ep[rb][6] * ip, ep[rb][7] * ip);
        o2[0] = pk2(en[rb][0] * in, en[rb][1] * in); o2[1] = pk2(en[rb][2] * in, en[rb][3] * in);
        o3[0] = pk2(en[rb][4] * in, en[rb][5] * in); o3[1] = pk2(en[rb][6] * in, en[rb][7] * in);
        if (QK == 0) {
          qp[rb][0] = __builtin_bit_cast(bf16x8, (u32x4){o0[0], o0[1], o1[0], o1[1]});
          qp[rb][1] = __builtin_bit_cast(bf16x8, (u32x4){o2[0], o2[1], o3[0], o3[1]});
          asm volatile("" : "+v"(qp[rb][0]), "+v"(qp[rb][1]));
        } else {
          const unsigned dst = ppk + rb * 16 * PSTR;
          lds<u32x2>(dst) = o0; lds<u32x2>(dst + 32) = o1; lds<u32x2>(dst + 64) = o2; lds<u32x2>(dst + 96) = o3;
        }
      }
    };
    phi2(std::integral_constant<int, 1>{});
    pwm = pwl + 8 * DM * 2 - 1024u;
    asm volatile("" : "+v"(pa));
    bf16x8 one8 = ones8();
    asm volatile("" : "+v"(one8));
    WAITL0; BAR;
    ST(layer * 24 + 5)
    {
      bf16x8 af[4], v0[4], v1[4];
      asm volatile("" ::: "memory");
#pragma unroll
      for (int sw = 0; sw < 4; ++sw) {
        af[sw] = cat8(tr_read(ppa + sw * (64 * PSTR)), tr_read(ppa + sw * (64 * PSTR) + 16 * PSTR));
        v0[sw] = lds<bf16x8>(pvr + sw * 4096);
        v1[sw] = lds<bf16x8>(pvr + sw * 4096 + 1024);
      }
      if (kg) {
#pragma unroll
        for (int i = 0; i < 4; ++i) acc[i >> 1][i & 1] = xget(sb + XS + lane * 8 + ((i >> 1) * 4 + (i & 1)) * 1024);
      }
      __builtin_amdgcn_sched_barrier(0);
      f32x4 kv[3] = {z4, z4, z4};
#pragma unroll
      for (int sw = 0; sw < 4; ++sw) {
        kv[0] = MFMA(af[sw], v0[sw], kv[0]);
        kv[1] = MFMA(af[sw], v1[sw], kv[1]);
        kv[2] = MFMA(af[sw], one8, kv[2]);
      }
      auto pc = [](float x, float y) { const unsigned t = pk2(x, y); return t < EMPTY - 1u ? t : EMPTY - 1u; };
      __builtin_amdgcn_raw_buffer_store_b128(u32x4{pc(kv[0][0], kv[0][1]), pc(kv[0][2], kv[0][3]), pc(kv[1][0], kv[1][1]), pc(kv[1][2], kv[1][3])}, rsW, gkv, 0, 16);
      __builtin_amdgcn_raw_buffer_store_b64(u32x2{pc(kv[2][0], kv[2][1]), pc(kv[2][2], kv[2][3])}, rsW, gkz, 0, 16);
    }
    u64 ko = korder(bid & 3, hp);
    auto wo_late = [&](int t) { glds2v(wo + (size_t)knib(ko, t) * 64 * DM, pwl, pwm, sb + OFF_WO + t * 8192 + w * 2048); };
    ST(layer * 24 + 6)
    u32x4 l1[8];
    const bool red = w == 7 || (w == 6 && (rt & 7) == 0);
    unsigned lo = kvo + (unsigned)((2 * hp + (rt >> 3) + (lane >> 5) * 8 * NH) * KVPB) +
                  (w == 7 ? (unsigned)((rt & 4) * 512 + (g & 1) * 1024 + (rt & 3) * 256 + fr * 16) : (unsigned)(4096 + (lane & 7) * 16));
    unsigned fs = w == 7 ? kfo + (unsigned)((rt >> 2) * 2048 + (lane >> 5) * 1024 + (rt & 3) * 256 + fr * 16 + (g & 1) * 8)
                : (lane & 24) ? 0x7fff0000u : kfo + (unsigned)(8192 + (rt >> 3) * 128 + (lane & 4) * 16 + (lane & 1) * 32 + (lane >> 5) * 16 + (lane & 2) * 4);
    auto l1_load = [&]() {
      asm volatile("" ::: "memory");
#pragma unroll
      for (int p = 0; p < 8; ++p) asm volatile("buffer_load_dwordx4 %0, %1, %2, %3 offen sc1" : "=v"(l1[p]) : "v"(lo), "s"(rsW), "s"(p * (NH * KVPB)) : "memory");
      __builtin_amdgcn_sched_barrier(0);
    };
    if (red) asm volatile("" : "+v"(lo), "+v"(fs));
    if (red) { __builtin_amdgcn_s_sleep(13); l1_load(); }
    phi2(std::integral_constant<int, 4>{});
    ST(layer * 24 + 7)
    if (!red) {
      phi2(std::integral_constant<int, 6>{});
    } else {
      typedef __attribute__((ext_vector_type(2))) __bf16 bf2;
      unsigned c0 = 0x00003F80u, c1 = 0x3F800000u;
      asm volatile("" : "+v"(c0), "+v"(c1));
      float s4[4];
      _Pragma("unroll 1") for (unsigned spins = 0; spins < (1u << 18); ++spins) {
        float a[8] = {0.f, 0.f, 0.f, 0.f, 0.f, 0.f, 0.f, 0.f};
        ARRIVED("+v"(l1[0]), "+v"(l1[1]), "+v"(l1[2]), "+v"(l1[3]), "+v"(l1[4]), "+v"(l1[5]), "+v"(l1[6]), "+v"(l1[7]));
#pragma unroll
        for (int p = 0; p < 8; ++p)
#pragma unroll
          for (int e = 0; e < 4; ++e) {
            const unsigned u = l1[p][e];
            a[2 * e] = __builtin_amdgcn_fdot2_f32_bf16(__builtin_bit_cast(bf2, u), __builtin_bit_cast(bf2, c0), a[2 * e], false);
            a[2 * e + 1] = __builtin_amdgcn_fdot2_f32_bf16(__builtin_bit_cast(bf2, u), __builtin_bit_cast(bf2, c1), a[2 * e + 1], false);
          }
#pragma unroll
        for (int i = 0; i < 4; ++i) {
          auto r = __builtin_amdgcn_permlane32_swap(__float_as_uint(a[i]), __float_as_uint(a[i + 4]), false, false);
          s4[i] = __uint_as_float(r[0]) + __uint_as_float(r[1]);
        }
        const float t = s4[0] + s4[2];
        if (__all(t == t)) break;
        unsigned mxw = 0;
#pragma unroll
        for (int p = 0; p < 8; ++p)
#pragma unroll
          for (int e = 0; e < 4; ++e) mxw = l1[p][e] > mxw ? l1[p][e] : mxw;
        if (__all(mxw != EMPTY)) break;
        l1_load();
      }
      WAITV(0);
      unsigned o0 = pk2(s4[0], s4[1]), o1 = pk2(s4[2], s4[3]);
      o0 = o0 < EMPTY - 1u ? o0 : EMPTY - 1u; o1 = o1 < EMPTY - 1u ? o1 : EMPTY - 1u;
      __builtin_amdgcn_raw_buffer_store_b64(u32x2{o0, o1}, rsW, fs, 0, 16);
    }
    if (!red) asm volatile("" : "+s"(ko));
    ST(layer * 24 + 8)
    WAITL0;
    BAR;
    ST(layer * 24 + 9)
    {
      u32x4 fa, fb = {0u, 0u, 0u, 0u};
      unsigned pfo = OFF_KV + wn * 4096 + lane * 16, pzo = OFF_KV + 8192 + wn * 128 + g * 16;
      asm volatile("" : "+v"(pfo), "+v"(pzo));
      const unsigned fo = kfo + (unsigned)(tid * 16), fo2 = kfo + (unsigned)(8192 + (lane & 15) * 16);
      if (red) {
        phi2(std::integral_constant<int, 6>{});
      } else if (w < 4) {
#pragma unroll
        for (int i = 6; i < 12; ++i) wo_late(i);
      } else {
        __builtin_amdgcn_s_sleep(13);
      }
      _Pragma("unroll 1") for (unsigned spins = 0; spins < (1u << 18); ++spins) {
        asm volatile("" ::: "memory");
        fa = __builtin_amdgcn_raw_buffer_load_b128(rsW, fo, 0, 16);
        if (w == 0) fb = __builtin_amdgcn_raw_buffer_load_b128(rsW, fo2, 0, 16);
        unsigned mxw = 0;
#pragma unroll
        for (int e = 0; e < 4; ++e) { mxw = fa[e] > mxw ? fa[e] : mxw; mxw = fb[e] > mxw ? fb[e] : mxw; }
        if (__all(mxw != EMPTY)) break;
        __builtin_amdgcn_s_sleep(1);
      }
      *(u32x4*)(smem + OFF_KV + tid * 16) = fa;
      if (tid < 16) *(u32x4*)(smem + OFF_KV + 8192 + tid * 16) = fb;
      ST(layer * 24 + 10)
      WAITL0; BAR;
      ST(layer * 24 + 11)
      bf16x8 kvf[3][2];
#pragma unroll
      for (int ks = 0; ks < 2; ++ks) {
        kvf[0][ks] = *(const bf16x8*)(smem + pfo + ks * 2048);
        kvf[1][ks] = *(const bf16x8*)(smem + pfo + ks * 2048 + 1024);
        kvf[2][ks] = *(const bf16x8*)(smem + pzo + ks * 64);
      }
#pragma unroll
      for (int rb = 0; rb < 2; ++rb) {
        f32x4 oa[3];
#pragma unroll
        for (int nb = 2; nb >= 0; --nb) {
          oa[nb] = MFMA(kvf[nb][0], qp[rb][0], z4);
          oa[nb] = MFMA(kvf[nb][1], qp[rb][1], oa[nb]);
        }
        float den = oa[2][0];
        den += 1e-6f;
        float inv = __builtin_amdgcn_rcpf(den);
        o_put(rb, oa[0] * inv, oa[1] * inv);
      }
    }
  }
  ST(layer * 24 + 12)
  if (HH) if (w >= 4) { WAITV(0); WAITL0; BAR; }
  if (first && w >= 4 && w < 7) {
    typedef __attribute__((ext_vector_type(2))) __bf16 bf2;
    unsigned c0 = 0x00003F80u, c1 = 0x3F800000u;
    asm volatile("" : "+v"(c0), "+v"(c1));
    const bf2 lo1 = __builtin_bit_cast(bf2, c0), hi1 = __builtin_bit_cast(bf2, c1);
    float a[8] = {0.f, 0.f, 0.f, 0.f, 0.f, 0.f, 0.f, 0.f};
#pragma unroll
    for (int i = 0; i < 8; ++i) {
      asm volatile("" : "+v"(pw[i]));
#pragma unroll
      for (int e = 0; e < 4; ++e) {
        const unsigned u = pw[i][e];
        a[2 * e] = __builtin_amdgcn_fdot2_f32_bf16(__builtin_bit_cast(bf2, u), lo1, a[2 * e], false);
        a[2 * e + 1] = __builtin_amdgcn_fdot2_f32_bf16(__builtin_bit_cast(bf2, u), hi1, a[2 * e + 1], false);
      }
    }
#pragma unroll
    for (int i = 0; i < 4; ++i) {
      auto r = __builtin_amdgcn_permlane32_swap(__float_as_uint(a[i]), __float_as_uint(a[i + 4]), false, false);
      a[i] = __uint_as_float(r[0]) + __uint_as_float(r[1]);
    }
#pragma unroll
    for (int i = 0; i < 2; ++i) {
      auto r = __builtin_amdgcn_permlane16_swap(__float_as_uint(a[i]), __float_as_uint(a[i + 2]), false, false);
      a[i] = __uint_as_float(r[0]) + __uint_as_float(r[1]);
      a[i] += dpp_ror<8>(a[i]);
    }
    if (!(lane & 8))
      __builtin_amdgcn_raw_buffer_store_b64(u32x2{__float_as_uint(a[0]), __float_as_uint(a[1])}, rsW,
          (unsigned)(WS_CSP + ((((hp + (1 << (w - 4))) & 15) * 16 + rt) * 192 + (w - 4) * 64 + (lane & 7) * 8 + (lane >> 5) * 4 + ((lane >> 4) & 1) * 2) * 4), 0, 16);
  }
  }
}

#undef pcs
#undef prs
#undef opo
#undef ogo
#undef pkw
#undef hof
#undef dm
#undef dm2
#undef pcv
#undef pwf
#undef prv
#undef pvf
#undef ppk
#undef ppa
#undef pvr
#undef gkv
#undef gkz
#undef pwl
#undef pwm
#undef plb
#undef prd
#undef dm3

__device__ __forceinline__ void g2_phase(char* smem, char* ws, u64 E, int w, f32x16& xq, int layer, const u16* __restrict__ x0,
                                         const u16* __restrict__ wo, const u16* __restrict__ wnx, u16* __restrict__ out) {
  IDS
  wnx = uni(wnx);
  const unsigned sb = lds_addr(smem);
  const int lw = w & 3, k2 = 16 * lw + (lane >> 3);
  const unsigned vW0 = (unsigned)((k2 * DM + hp * 64 + ((lane & 7) ^ ((lane >> 4) << 1)) * 8) * 2), vW1 = vW0 + 8 * DM * 2 - 1024u;
  const int r0 = w * 16 + (lane >> 3);
  unsigned pxr = sb + r0 * 128 + (lane & 7) * 16;
  const unsigned rmo = (unsigned)(((m0 + r0) * DM + hp * 64 + ((lane & 7) ^ (lane >> 3)) * 8) * 2);
  const unsigned imo = (unsigned)((rt * 16 + hp) * 16384 + r0 * 128 + (lane & 7) * 16);
  constexpr int S0 = 0, S1 = 16384, S2 = 32768, S3 = 147456, S4 = OFF_WO, S5 = 98304, S6 = 114688;
  constexpr int AS[16] = {S0, S1, S2, S3, S0, S1, S4, S2, S3, S0, S1, S4, S5, S3, S0, S6};
  constexpr int OFF_C = 0;
  auto bs = [](int t) { return OFF_WO + (t < 12 ? t : t - 10) * 8192; };
  if (w >= 4) {
    constexpr int WT[15] = {0, 8, 12, 14, 16, 18, 20, 18, 16, 14, 12, 12, 10, 10, 8};
    const char* o = ws + WS_O + layer * (SEQ * DM * 2) + rt * 262144;
    const unsigned vA = (unsigned)(lw * 4096 + lane * 16);
    const u64 kord = korder(bid & 3, hp);
    auto ldA = [&](int s) { glds4c(o + knib(kord, s) * 16384, vA, sb + AS[s] + lw * 4096); };
    auto ldW = [&](int t) { glds2v(wo + (size_t)knib(kord, t) * 64 * DM, vW0, vW1, sb + bs(t) + lw * 2048); };
    const int fB = (k2 >> 1) & 1;
    unsigned vB0 = (unsigned)((k2 * 3072 + hp * 64 + (((((lane & 7) >> 1) ^ fB) << 1) | (lane & 1)) * 8) * 2);
    unsigned vB1 = (unsigned)(((k2 + 8) * 3072 + hp * 64 + (((((lane & 7) >> 1) ^ (fB | 2)) << 1) | (lane & 1)) * 8) * 2) - 1024u;
    int kn1 = knib(korder(kv1(bid), hp), 1);
    asm volatile("" : "+v"(vB0), "+v"(vB1));
    asm volatile("" : "+s"(kn1));
    auto ldB = [&](int s, int part) {
      const u16* b = wnx + (size_t)(s ? kn1 : hp) * 64 * 3072;
      if (part) glds4v(b, vB0, vB1, vB0, vB1, sb + s * 40960 + 16384 + lw * 6144);
      else glds2v(b + 2048, vB0, vB1, sb + s * 40960 + 16384 + lw * 6144 + 4096);
    };
    BAR;
    ldA(1); ldA(2); ldA(3); ldA(4);
    WAITV(12);
    BAR;
    auto step = [&](auto ktc) {
      constexpr int kt = decltype(ktc)::value;
      WAITVN(WT[kt]);
      BAR;
      if constexpr (kt == 1) { ldA(5); ldA(6); }
      if constexpr (kt >= 2 && kt <= 10) ldA(kt + 5);
      if constexpr (kt >= 2 && kt <= 5) ldW(kt + 10);
      if constexpr (kt >= 11) ldB((kt - 11) >> 1, (kt - 11) & 1);
      if constexpr (kt >= 6) { __builtin_amdgcn_s_sleep(kt >= 11 ? 3 : 2); if (layer == 1) __builtin_amdgcn_s_sleep(1); }
    };
    step(IC<1>{}); step(IC<2>{}); step(IC<3>{}); step(IC<4>{}); step(IC<5>{}); step(IC<6>{}); step(IC<7>{});
    step(IC<8>{}); step(IC<9>{}); step(IC<10>{}); step(IC<11>{}); step(IC<12>{}); step(IC<13>{}); step(IC<14>{});
  } else {
    const int wm = w >> 1, wn = w & 1;
    f32x4 acc[4][2];
    unsigned co[2];
#pragma unroll
    for (int cb = 0; cb < 2; ++cb) co[cb] = sb + OFF_C + (unsigned)((wm * 64 + fr) * 128 + (((wn * 4 + cb * 2 + (g >> 1)) ^ (fr & 7)) << 4) + (g & 1) * 8);
    const int fl = (q4 >> 1) | ((g & 1) << 1);
    const int bb0 = (4 * g + q4) * 128 + p4 * 8 + (((wn * 2) ^ fl) << 5), bb1 = (4 * g + q4) * 128 + p4 * 8 + (((wn * 2 + 1) ^ fl) << 5);
    const int abase = wm * 4096 + lane * 16;
    bf16x8 fa[2][2][4], fb[2][2][2];
    unsigned ra[3], rb0[2], rb1[2];
#pragma unroll
    for (int i = 0; i < 3; ++i) { ra[i] = abase + i * 65536; asm volatile("" : "+v"(ra[i])); }
#pragma unroll
    for (int i = 0; i < 2; ++i) { rb0[i] = bb0 + OFF_WO + i * 49152; rb1[i] = bb1 + OFF_WO + i * 49152; asm volatile("" : "+v"(rb0[i]), "+v"(rb1[i])); }
    auto loadf = [&](int kt, bf16x8 (&a)[2][4], bf16x8 (&b)[2][2]) {
#pragma unroll
      for (int s = 0; s < 2; ++s) {
        const char* sA = smem + ra[AS[kt] >> 16] + (AS[kt] & 65535) + s * 8192;
        const int hi = bs(kt) >= OFF_WO + 49152, ob = bs(kt) - OFF_WO - hi * 49152 + s * 4096;
        b[s][0] = cat8(tr_read(smem + rb0[hi] + ob), tr_read(smem + rb0[hi] + ob + 2048));
        b[s][1] = cat8(tr_read(smem + rb1[hi] + ob), tr_read(smem + rb1[hi] + ob + 2048));
#pragma unroll
        for (int rb = 0; rb < 4; ++rb) a[s][rb] = *(const bf16x8*)(sA + rb * 1024);
      }
    };
    auto mma = [&](bf16x8 (&a)[2][4], bf16x8 (&b)[2][2]) {
#pragma unroll
      for (int i = 0; i < 16; ++i) {
        const int rb = i >> 2, cb = ((i >> 1) ^ rb) & 1, s = ((i + 1) >> 1) & 1;
        acc[rb][cb] = MFMA(b[s][cb], a[s][rb], acc[rb][cb]);
      }
    };
    unsigned fo = lane < 16 && lane != hp ? WS_FLAGS + (unsigned)(rt * 16 + lane) * FLS : 0u;
    const u64 ko = korder(bid & 3, hp);
    unsigned kf = kfirst(ko), kf2 = kfirst(ko >> 16) | (1u << knib(ko, 9));
    unsigned jo = (unsigned)((((rt & 7) * 128 + (w - 1) * 64 + lane) * 3072 + (hp & 3) * 1024 + (hp | 3) * 64) * 2);
    unsigned fo1 = ((kf >> (lane & 31)) & 1) ? fo : 0u, fo2 = ((kf2 >> (lane & 31)) & 1) ? fo : 0u;
    fo = (((kf | kf2) >> (lane & 31)) & 1) ? 0u : fo;
    asm volatile("" : "+v"(fo), "+v"(fo1), "+v"(fo2));
    asm volatile("" : "+s"(kf), "+s"(kf2));
    publish(rsW, tid, rt * 16 + hp, E + (unsigned)(layer + 1));
    ST(layer * 24 + 13)
    u64 fl1 = 0;
    if (w == 0) asm volatile("global_load_dwordx2 %0, %1, %2 sc1" : "+v"(fl1) : "v"(fo1), "s"(ws) : "memory");
    if (layer == 0) {
      const unsigned xo = (unsigned)(((m0 + wm * 64 + fr) * DM + hp * 64 + wn * 32 + g * 8) * 2);
      asm volatile("global_load_dwordx4 v[64:67], %1, %3\n\tglobal_load_dwordx4 v[68:71], %1, %4\n\tglobal_load_dwordx4 v[72:75], %2, %3\n\tglobal_load_dwordx4 v[76:79], %2, %4"
                   : "+{v[64:79]}"(xq) : "v"(xo), "v"(xo + 32 * DM * 2), "s"(x0), "s"(x0 + 16 * DM) : "memory");
    }
    loadf(0, fa[0], fb[0]);
    unsigned junk = 0;
    if (layer < 3 && (hp & 7) < 3 && (w == 1 || w == 2))
      asm volatile("global_load_dword %0, %1, %2" : "+v"(junk) : "v"(jo), "s"(wnx) : "memory");
    __builtin_amdgcn_sched_barrier(0);
    bf16x8 id[2];
#pragma unroll
    for (int cb = 0; cb < 2; ++cb) {
      const unsigned one = (g >> 1) == cb && (g & 1) == (fr >> 3) ? 0x3F80u << ((fr & 1) * 16) : 0u;
      u32x4 t;
#pragma unroll
      for (int j = 0; j < 4; ++j) t[j] = ((fr >> 1) & 3) == j ? one : 0u;
      id[cb] = __builtin_bit_cast(bf16x8, t);
      asm volatile("" : "+v"(id[cb]));
    }
    asm volatile("" : "+v"(co[0]), "+v"(co[1]));
#pragma unroll
    for (int rb = 0; rb < 4; ++rb) { acc[rb][0] = f32x4{0, 0, 0, 0}; acc[rb][1] = f32x4{0, 0, 0, 0}; }
    mma(fa[0], fb[0]);
    __builtin_amdgcn_sched_barrier(0);
    WAITL0;
    u64 miss = 0;
    if (w == 0) { ARRIVED("+v"(fl1)); if (__ballot(fl1 != E + (unsigned)(layer + 1)) & kf) poll_eq(ws, fo1, E + (unsigned)(layer + 1), kf, kf); miss = 0xffffu & ~(kf | kf2 | (1u << hp)); }
    ST(layer * 24 + 14)
    __builtin_amdgcn_sched_barrier(0);
    BAR;
    __builtin_amdgcn_sched_barrier(0);
    asm volatile("" ::: "memory");
    if (layer == 0) WAITV(0);
    __builtin_amdgcn_sched_barrier(0);
    asm volatile("" : "+{v[64:79]}"(xq));
    const bf16x8 xf[4] = {__builtin_bit_cast(bf16x8, quad<0>(xq)), __builtin_bit_cast(bf16x8, quad<1>(xq)), __builtin_bit_cast(bf16x8, quad<2>(xq)), __builtin_bit_cast(bf16x8, quad<3>(xq))};
#pragma unroll
    for (int rb = 0; rb < 4; ++rb) { acc[rb][0] = MFMA(id[0], xf[rb], acc[rb][0]); acc[rb][1] = MFMA(id[1], xf[rb], acc[rb][1]); }
    if (w == 0) poll_eq(ws, fo2, E + (unsigned)(layer + 1), kf2, kf2);
    u64 fl2 = 0;
    auto step = [&](auto ktc) {
      constexpr int kt = decltype(ktc)::value;
      __builtin_amdgcn_sched_barrier(0);
      if (kt == 5 && miss) {
        ARRIVED("+v"(fl2));
        if (__ballot(fl2 != E + (unsigned)(layer + 1)) & miss) poll_eq(ws, fo, E + (unsigned)(layer + 1), miss, ~0ull);
      }
      WAITL0;
      BAR;
      if (kt == 2 && miss) asm volatile("global_load_dwordx2 %0, %1, %2 sc1" : "+v"(fl2) : "v"(fo), "s"(ws) : "memory");
      if (kt == 0) { ST(layer * 24 + 15) }
      __builtin_amdgcn_sched_barrier(0);
      loadf(kt + 1, fa[(kt + 1) & 1], fb[(kt + 1) & 1]);
      if (kt == 0) return;
      mma(fa[kt & 1], fb[kt & 1]);
#pragma unroll
      for (int i = 0; i < 4; ++i) {
        __builtin_amdgcn_sched_group_barrier(0x008, 1, 0);
        __builtin_amdgcn_sched_group_barrier(0x100, 1, 0);
        __builtin_amdgcn_sched_group_barrier(0x008, 1, 0);
        __builtin_amdgcn_sched_group_barrier(0x100, 3, 0);
      }
#pragma unroll
      for (int i = 0; i < 8; ++i) __builtin_amdgcn_sched_group_barrier(0x008, 1, 0);
    };
    step(IC<0>{}); step(IC<1>{}); step(IC<2>{}); step(IC<3>{}); step(IC<4>{}); step(IC<5>{}); step(IC<6>{}); step(IC<7>{});
    step(IC<8>{}); step(IC<9>{}); step(IC<10>{}); step(IC<11>{}); step(IC<12>{}); step(IC<13>{}); step(IC<14>{});
    WAITV(0);
    asm volatile("" :: "v"(junk));
    __builtin_amdgcn_sched_barrier(0);
    auto put = [&](int rb) {
#pragma unroll
      for (int cb = 0; cb < 2; ++cb) lds<u32x2>(co[cb] + rb * 2048) = u32x2{pk2(acc[rb][cb][0], acc[rb][cb][1]), pk2(acc[rb][cb][2], acc[rb][cb][3])};
    };
#pragma unroll
    for (int rb = 0; rb < 4; ++rb)
#pragma unroll
      for (int cb = 0; cb < 2; ++cb) acc[rb][cb] = MFMA(fb[1][0][cb], fa[1][0][rb], acc[rb][cb]);
#pragma unroll
    for (int rb = 0; rb < 4; ++rb) {
      __builtin_amdgcn_sched_barrier(0);
#pragma unroll
      for (int cb = 0; cb < 2; ++cb) acc[rb][cb] = MFMA(fb[1][1][cb], fa[1][1][rb], acc[rb][cb]);
      __builtin_amdgcn_sched_barrier(0);
      if (rb >= 2) put(rb - 2);
    }
    __builtin_amdgcn_sched_barrier(0);
    ST(layer * 24 + 16)
    put(2); put(3);
  }
  WAITL0; BAR;
  asm volatile("" ::: "memory");
  u32x4 xn[2];
#pragma unroll
  for (int i = 0; i < 2; ++i) xn[i] = lds<u32x4>(pxr + OFF_C + i * 1024);
  if (layer == 3) {
    const __amdgpu_buffer_rsrc_t rsO = __builtin_amdgcn_make_buffer_rsrc((void*)uni(out), (short)0, SEQ * DM * 2, 0x00020000);
    __builtin_amdgcn_raw_buffer_store_b128(xn[0], rsO, rmo, 0, 16);
    __builtin_amdgcn_raw_buffer_store_b128(xn[1], rsO, rmo + 8 * DM * 2, 0, 16);
  } else {
    __builtin_amdgcn_raw_buffer_store_b128(xn[0], rsW, WS_X + layer * (SEQ * DM * 2) + imo, 0, 16);
    __builtin_amdgcn_raw_buffer_store_b128(xn[1], rsW, WS_X + layer * (SEQ * DM * 2) + imo + 1024, 0, 16);
    float mean[2], m2[2];
#pragma unroll
    for (int i = 0; i < 2; ++i) {
      typedef __attribute__((ext_vector_type(2))) __bf16 bf2;
      const bf2 one2 = __builtin_bit_cast(bf2, 0x3F803F80u);
      float sa = 0.f, sq = 0.f;
#pragma unroll
      for (int e = 0; e < 4; ++e) {
        const unsigned u = xn[i][e];
        const bf2 t = __builtin_bit_cast(bf2, u);
        sa = __builtin_amdgcn_fdot2_f32_bf16(t, one2, sa, false);
        sq = __builtin_amdgcn_fdot2_f32_bf16(t, t, sq, false);
      }
      mean[i] = sa;
      m2[i] = sq;
    }
#define DPA(c) "v_add_f32_dpp %0, %0, %0 " c "\n\tv_add_f32_dpp %1, %1, %1 " c "\n\tv_add_f32_dpp %2, %2, %2 " c "\n\tv_add_f32_dpp %3, %3, %3 " c "\n\t"
    asm volatile("s_nop 3\n\t" DPA("quad_perm:[1,0,3,2] row_mask:0xf bank_mask:0xf") DPA("quad_perm:[2,3,0,1] row_mask:0xf bank_mask:0xf")
                 DPA("row_half_mirror row_mask:0xf bank_mask:0xf") "s_nop 0" : "+v"(mean[0]), "+v"(m2[0]), "+v"(mean[1]), "+v"(m2[1]));
    if ((lane & 7) < 2) {
      const int i = lane & 1;
      const u32x2 sv = {__float_as_uint(i ? mean[1] : mean[0]), __float_as_uint(i ? m2[1] : m2[0])};
      __builtin_amdgcn_raw_buffer_store_b64(sv, rsW, WS_ST + layer * (SEQ * 128) + (unsigned)(((rt * 16 + hp) * 128 + r0 + i * 8) * 8), 0, 16);
    }
  }
  ST(layer * 24 + 17)
  if (layer < 3 && w >= 4) { WAITV(0); BAR; }
  ST(layer * 24 + 18)
}

__global__ __launch_bounds__(512, 2) void mega_kernel(const u16* __restrict__ x0, const u16* __restrict__ wqkvA,
                                                      const u16* __restrict__ woA, const u16* __restrict__ wqkvB,
                                                      const u16* __restrict__ wphi, const u16* __restrict__ woB,
                                                      u16* __restrict__ out, char* __restrict__ ws) {
  extern __shared__ __attribute__((aligned(16))) char smem[];
  const int w = __builtin_amdgcn_readfirstlane(threadIdx.x >> 6);
  u64 E = 0;
  {
    const int bid = blockIdx.x, lane = threadIdx.x & 63;
    const int rt = 2 * (bid & 4) + ((bid >> 3) & 7), hp = 4 * (bid & 3) + (bid >> 6), m0 = rt * 128;
    if (w < 4) {
      if (w == 3) {
        unsigned long long pc;
        unsigned len;
        asm volatile("s_getpc_b64 %0\n.Lmk_pc:\n\ts_mov_b32 %1, .Lmk_end-.Lmk_pc" : "=s"(pc), "=s"(len));
        const unsigned per = ((len >> 10) + 1) * 128, off = ((bid >> 3) & 7) * per + (lane * 128 < per ? lane * 128 : 0);
        asm volatile("s_mov_b32 m0, %2\n\ts_nop 0\n\tglobal_load_lds_dword %0, %1" :: "v"(off < len - 4 ? off : 0u), "s"(pc), "s"(lds_addr(smem) + LDS_TOTAL - 1024) : "memory", "m0");
      }
      const unsigned xo = (unsigned)((m0 + hp * 8 + w * 2) * (DM * 2) + lane * 16);
      glds4v(x0, xo, xo, xo, xo, lds_addr(smem) + 122880 + w * 4096);
    } else {
      const int lw = w & 3;
      const int kB = 16 * lw + (lane >> 3), fB = (kB >> 1) & 1;
      const unsigned vB0 = (unsigned)((kB * 3072 + hp * 64 + (((((lane & 7) >> 1) ^ fB) << 1) | (lane & 1)) * 8) * 2);
      const unsigned vB1 = (unsigned)(((kB + 8) * 3072 + hp * 64 + (((((lane & 7) >> 1) ^ (fB | 2)) << 1) | (lane & 1)) * 8) * 2) - 1024u;
      const unsigned vA = (unsigned)(((m0 + lw * 32 + (lane >> 3)) * DM + ((lane & 7) ^ (lane >> 3)) * 8) * 2);
#pragma unroll
      for (int s = 0; s < 1; ++s) {
        const int kt = (rt & 8) + s;
        const unsigned sA = lds_addr(smem) + s * 40960, sB = sA + 16384;
        glds4v(x0 + kt * 64, vA, vA + 15360u, vA + 2 * 15360u, vA + 3 * 15360u, sA + lw * 4096);
        const u16* b = wqkvA + (size_t)kt * 64 * 3072;
        glds4v(b, vB0, vB1, vB0, vB1, sB + lw * 6144);
        glds2v(b + 2048, vB0, vB1, sB + lw * 6144 + 4096);
      }
      asm volatile("s_load_dwordx2 %0, %1, 0x0\n\ts_waitcnt lgkmcnt(0)" : "=s"(E) : "s"(ws) : "memory");
    }
  }
  f32x16 xq;
#pragma unroll 1
  for (int layer = 0; layer < 4; ++layer) {
    if (layer & 1) g1_phase<1>(smem, ws, E, w, xq, layer, x0, wqkvB, woB, wphi, nullptr, nullptr);
    else g1_phase<0>(smem, ws, E, w, xq, layer, x0, wqkvA, woA, nullptr, wqkvB, woB);
    g2_phase(smem, ws, E, w, xq, layer, x0, (layer & 1) ? woB : woA, (layer & 1) ? wqkvA : wqkvB, out);
  }
  asm volatile(".Lmk_end:");
  WAITV(0);
  if (blockIdx.x == 0 && w == 0 && lane_id() == 0) __hip_atomic_store((gu64*)ws, E + 16u, __ATOMIC_RELAXED, __HIP_MEMORY_SCOPE_AGENT);
}

extern "C" void kernel_launch(void* const* d_in, const int* in_sizes, int n_in,
                              void* d_out, int out_size, void* d_ws, size_t ws_size,
                              hipStream_t stream) {
  hipFuncSetAttribute((const void*)mega_kernel, hipFuncAttributeMaxDynamicSharedMemorySize, LDS_TOTAL);
  mega_kernel<<<256, 512, LDS_TOTAL, stream>>>((const u16*)d_in[0], (const u16*)d_in[1], (const u16*)d_in[2], (const u16*)d_in[4],
                                               (const u16*)d_in[5], (const u16*)d_in[6], (u16*)d_out, (char*)d_ws);
}
```

```cpp
#define RSLEEP 13
#define FSLEEP 0
#define LATE0 6
#define WSLEEP 0
#include <hip/hip_runtime.h>
#include <hip/hip_bf16.h>
#include <stdint.h>
#include <type_traits>
#pragma clang diagnostic ignored "-Winline-asm"
#pragma clang diagnostic ignored "-Wunused-value"

typedef unsigned short u16;
typedef __attribute__((ext_vector_type(8))) short bf16x8;
typedef __attribute__((ext_vector_type(4))) short bf16x4;
typedef __attribute__((ext_vector_type(4))) float f32x4;
typedef __attribute__((ext_vector_type(4))) unsigned u32x4;
typedef __attribute__((ext_vector_type(2))) unsigned u32x2;
typedef __attribute__((ext_vector_type(8))) float f32x8;
typedef __attribute__((ext_vector_type(16))) unsigned u32x16;
typedef __attribute__((ext_vector_type(16))) float f32x16;
typedef __attribute__((ext_vector_type(32))) float f32x32;
template <int I, typename T> __device__ __forceinline__ f32x4 quad(const T& v) { return __builtin_shufflevector(v, v, 4 * I, 4 * I + 1, 4 * I + 2, 4 * I + 3); }

constexpr int SEQ = 2048, DM = 1024, NH = 32;
#define ST(i)
constexpr int KVP = 64 * 32 + 64;

typedef __attribute__((ext_vector_type(2))) float f32x2;
typedef __attribute__((ext_vector_type(2))) __bf16 bf16x2_t;
__device__ __forceinline__ unsigned pk2(float a, float b) {
  f32x2 v = {a, b};
  bf16x2_t r = __builtin_convertvector(v, bf16x2_t);
  return __builtin_bit_cast(unsigned, r);
}

__device__ __forceinline__ bf16x4 tr_read(const char* p) {
  return __builtin_amdgcn_ds_read_tr16_b64_v4i16((__attribute__((address_space(3))) bf16x4*)p);
}
template <typename T> __device__ __forceinline__ __attribute__((address_space(3))) T& lds(unsigned a) { return *(__attribute__((address_space(3))) T*)(size_t)a; }
__device__ __forceinline__ bf16x4 tr_read(unsigned a) { return __builtin_amdgcn_ds_read_tr16_b64_v4i16(&lds<bf16x4>(a)); }
__device__ __forceinline__ f32x4 xget(unsigned a) { const f32x2 p = lds<f32x2>(a), q = lds<f32x2>(a + 512); return f32x4{p[0], p[1], q[0], q[1]}; }
__device__ __forceinline__ bf16x8 cat8(bf16x4 a, bf16x4 b) {
  bf16x8 f = {a[0], a[1], a[2], a[3], b[0], b[1], b[2], b[3]};
  return f;
}
__device__ __forceinline__ void glds16s(const void* sbase, unsigned voff, unsigned lds_base) {
  asm volatile("s_mov_b32 m0, %2\n\ts_nop 0\n\tglobal_load_lds_dwordx4 %0, %1" :: "v"(voff), "s"(sbase), "s"(lds_base) : "memory", "m0");
}
__device__ __forceinline__ void glds16c(const void* sbase, unsigned voff, unsigned lds_base) {
  asm volatile("s_mov_b32 m0, %2\n\ts_nop 0\n\tglobal_load_lds_dwordx4 %0, %1 sc1" :: "v"(voff), "s"(sbase), "s"(lds_base) : "memory", "m0");
}
#define GL_ "\n\tglobal_load_lds_dwordx4 "
__device__ __forceinline__ void glds4c(const void* sbase, unsigned v, unsigned lds_base) {
  asm volatile("s_mov_b32 m0, %2\n\ts_nop 0" GL_ "%0, %1 sc1" GL_ "%0, %1 offset:1024 sc1" GL_ "%0, %1 offset:2048 sc1" GL_ "%0, %1 offset:3072 sc1"
               :: "v"(v), "s"(sbase), "s"(lds_base) : "memory", "m0");
}
__device__ __forceinline__ void glds4v(const void* sbase, unsigned v0, unsigned v1, unsigned v2, unsigned v3, unsigned lds_base) {
  asm volatile("s_mov_b32 m0, %5\n\ts_nop 0" GL_ "%0, %4" GL_ "%1, %4 offset:1024" GL_ "%2, %4 offset:2048" GL_ "%3, %4 offset:3072"
               :: "v"(v0), "v"(v1), "v"(v2), "v"(v3), "s"(sbase), "s"(lds_base) : "memory", "m0");
}
__device__ __forceinline__ void glds2v(const void* sbase, unsigned v0, unsigned v1, unsigned lds_base) {
  asm volatile("s_mov_b32 m0, %3\n\ts_nop 0" GL_ "%0, %2" GL_ "%1, %2 offset:1024" :: "v"(v0), "v"(v1), "s"(sbase), "s"(lds_base) : "memory", "m0");
}
__device__ __forceinline__ void gld16(u32x4& d, unsigned voff, const void* sbase) {
  asm volatile("global_load_dwordx4 %0, %1, %2" : "=v"(d) : "v"(voff), "s"(sbase) : "memory");
}
__device__ __forceinline__ unsigned lds_addr(const void* p) { return (unsigned)(size_t)p; }
#define WAITV(n) asm volatile("s_waitcnt vmcnt(" #n ")" ::: "memory")
#define WAITVN(n) asm volatile("s_waitcnt vmcnt(%0)" :: "n"(n) : "memory")
#define WAITL0 do { asm volatile("" ::: "memory"); __builtin_amdgcn_s_waitcnt(0xC07F); asm volatile("" ::: "memory"); } while (0)
#define BAR __builtin_amdgcn_s_barrier()
#define ARRIVED(...) do { WAITV(0); __builtin_amdgcn_sched_barrier(0); asm volatile("" : __VA_ARGS__); } while (0)

__device__ __forceinline__ float vmaxf(float a, float b) { float r; asm("v_max_f32 %0, %1, %2" : "=v"(r) : "v"(a), "v"(b)); return r; }
template <int N> __device__ __forceinline__ float dpp_ror(float x) {
  return __uint_as_float(__builtin_amdgcn_update_dpp(0u, __float_as_uint(x), 0x120 + N, 0xf, 0xf, false));
}
__device__ __forceinline__ float r16_sum(float x) { x += dpp_ror<8>(x); x += dpp_ror<4>(x); x += dpp_ror<2>(x); return x + dpp_ror<1>(x); }
__device__ __forceinline__ float vmax3f(float a, float b, float c) { float r; asm("v_max3_f32 %0, %1, %2, %3" : "=v"(r) : "v"(a), "v"(b), "v"(c)); return r; }
__device__ __forceinline__ void max8pn(const f32x4& a, const f32x4& b, float& mx, float& mn) {
  asm("v_max3_f32 %0, %2, %3, %4\n\tv_max3_f32 %1, -%2, -%3, -%4\n\tv_max3_f32 %0, %0, %5, %6\n\tv_max3_f32 %1, %1, -%5, -%6\n\t"
      "v_max3_f32 %0, %0, %7, %8\n\tv_max3_f32 %1, %1, -%7, -%8\n\tv_max_f32 %0, %0, %9\n\tv_max_f32 %1, %1, -%9\n\ts_nop 0"
      : "=&v"(mx), "=&v"(mn) : "v"(a[0]), "v"(a[1]), "v"(a[2]), "v"(a[3]), "v"(b[0]), "v"(b[1]), "v"(b[2]), "v"(b[3]));
}
__device__ __forceinline__ float vmaxf_n(float a, float b) { float r; asm("v_max_f32 %0, %1, %2\n\ts_nop 0" : "=v"(r) : "v"(a), "v"(b)); return r; }
__device__ __forceinline__ float vmax3f_n(float a, float b, float c) { float r; asm("v_max3_f32 %0, %1, %2, %3\n\ts_nop 0" : "=v"(r) : "v"(a), "v"(b), "v"(c)); return r; }
template <bool MAXOP> __device__ __forceinline__ float rop(float a, float b) { return MAXOP ? vmaxf_n(a, b) : a + b; }
template <bool MAXOP> __device__ __forceinline__ void xr2(float& a, float& b) {
  auto r = __builtin_amdgcn_permlane16_swap(__float_as_uint(a), __float_as_uint(b), false, false);
  unsigned u = __float_as_uint(rop<MAXOP>(__uint_as_float(r[0]), __uint_as_float(r[1])));
  auto r2 = __builtin_amdgcn_permlane32_swap(u, u, false, false);
  u = __float_as_uint(rop<MAXOP>(__uint_as_float(r2[0]), __uint_as_float(r2[1])));
  auto r3 = __builtin_amdgcn_permlane16_swap(u, u, false, false);
  a = __uint_as_float(r3[0]); b = __uint_as_float(r3[1]);
}
template <bool MAXOP> __device__ __forceinline__ void xr4(float& a, float& b, float& c, float& d) {
  auto r = __builtin_amdgcn_permlane16_swap(__float_as_uint(a), __float_as_uint(b), false, false);
  auto q = __builtin_amdgcn_permlane16_swap(__float_as_uint(c), __float_as_uint(d), false, false);
  const unsigned ab = __float_as_uint(rop<MAXOP>(__uint_as_float(r[0]), __uint_as_float(r[1])));
  const unsigned cd = __float_as_uint(rop<MAXOP>(__uint_as_float(q[0]), __uint_as_float(q[1])));
  auto t = __builtin_amdgcn_permlane32_swap(ab, cd, false, false);
  const unsigned u = __float_as_uint(rop<MAXOP>(__uint_as_float(t[0]), __uint_as_float(t[1])));
  auto h = __builtin_amdgcn_permlane32_swap(u, u, false, false);
  auto p0 = __builtin_amdgcn_permlane16_swap(h[0], h[0], false, false);
  auto p1 = __builtin_amdgcn_permlane16_swap(h[1], h[1], false, false);
  a = __uint_as_float(p0[0]); b = __uint_as_float(p0[1]); c = __uint_as_float(p1[0]); d = __uint_as_float(p1[1]);
}

__device__ __forceinline__ unsigned xs4(float a, float b, float c, float d) {
  auto r = __builtin_amdgcn_permlane16_swap(__float_as_uint(a), __float_as_uint(b), false, false);
  auto q = __builtin_amdgcn_permlane16_swap(__float_as_uint(c), __float_as_uint(d), false, false);
  auto t = __builtin_amdgcn_permlane32_swap(__float_as_uint(__uint_as_float(r[0]) + __uint_as_float(r[1])), __float_as_uint(__uint_as_float(q[0]) + __uint_as_float(q[1])), false, false);
  return __float_as_uint(__uint_as_float(t[0]) + __uint_as_float(t[1]));
}

__device__ __forceinline__ f32x4 sum4(const u32x4 (&v)[4]) {
  float r0, r1, r2, r3, t0, t1, t2, t3;
  asm("v_add_f32 %0, %8, %12\n\tv_add_f32 %1, %9, %13\n\tv_add_f32 %2, %10, %14\n\tv_add_f32 %3, %11, %15\n\t"
      "v_add_f32 %4, %16, %20\n\tv_add_f32 %5, %17, %21\n\tv_add_f32 %6, %18, %22\n\tv_add_f32 %7, %19, %23\n\t"
      "v_add_f32 %0, %0, %4\n\tv_add_f32 %1, %1, %5\n\tv_add_f32 %2, %2, %6\n\tv_add_f32 %3, %3, %7\n\ts_nop 0"
      : "=&v"(r0), "=&v"(r1), "=&v"(r2), "=&v"(r3), "=&v"(t0), "=&v"(t1), "=&v"(t2), "=&v"(t3)
      : "v"(v[0][0]), "v"(v[0][1]), "v"(v[0][2]), "v"(v[0][3]), "v"(v[1][0]), "v"(v[1][1]), "v"(v[1][2]), "v"(v[1][3]),
        "v"(v[2][0]), "v"(v[2][1]), "v"(v[2][2]), "v"(v[2][3]), "v"(v[3][0]), "v"(v[3][1]), "v"(v[3][2]), "v"(v[3][3]));
  return f32x4{r0, r1, r2, r3};
}
#define MFMA(a, b, c) __builtin_amdgcn_mfma_f32_16x16x32_bf16(a, b, c, 0, 0, 0)

typedef unsigned long long u64;
typedef __attribute__((address_space(1))) unsigned long long gu64;
constexpr unsigned WS_FLAGS = 4096;
constexpr int FLS = 64;
constexpr unsigned WS_G0 = 65536;
constexpr unsigned WS_CSP = WS_G0 + SEQ * 16;
constexpr unsigned WS_ST = WS_CSP + 16 * 16 * 768;
constexpr unsigned WS_X = 1u << 21;
constexpr unsigned WS_O = WS_X + 3u * SEQ * DM * 2;
constexpr unsigned KVPB = KVP * 2, KVFB = KVP * 4;
constexpr unsigned EMPTY = 0xffffffffu;
constexpr unsigned WS_KV = WS_O + 4u * SEQ * DM * 2;
constexpr unsigned KVLB = 16u * NH * KVPB + 16u * KVFB;
constexpr unsigned WS_HL = WS_KV + 2u * KVLB;
constexpr unsigned WS_CSA = WS_HL + 512u * 4096;
constexpr unsigned WS_CPA = WS_CSA + 256u * 1024;
constexpr unsigned WS_END = WS_CPA + 16 * 16 * 768;
constexpr int LDS_TOTAL = 163840;
constexpr int OFF_WO = 49152;
template <int V> using IC = std::integral_constant<int, V>;
__device__ __forceinline__ bf16x8 ones8() { unsigned e = 0x3F803F80u; asm volatile("" : "+v"(e)); return __builtin_bit_cast(bf16x8, u32x4{e, e, e, e}); }
__device__ __forceinline__ u32x4 empty4() { unsigned e = EMPTY; asm volatile("" : "+v"(e)); return u32x4{e, e, e, e}; }
__device__ __forceinline__ u64 korder(int v, int own) {
  const u64 t = v == 0 ? 0x3b210fedca987654ull : v == 1 ? 0xb37654210fedca98ull : v == 2 ? 0x3ba987654210fedcull : 0xb3fedca987654210ull;
  const u64 inv = v == 0 ? 0xa987e6543210fdcbull : v == 1 ? 0x6543f210dcbae987ull : v == 2 ? 0x3210edcba987f654ull : 0xdcbaf9876543e210ull;
  const u64 low = (1ull << (4 * ((inv >> (4 * own)) & 15))) - 1ull;
  return (u64)own | ((t & low) << 4) | (t & ~((low << 4) | 15ull));
}
__device__ __forceinline__ u64 korder0(int rt) {
  const int r = rt & 7;
  const u64 own = (0x765432765432ull >> (4 * (r < 6 ? r : r - 6))) & 0xffffffull, oth = 0x7654321076543210ull >> (4 * r);
  const u64 t = 0x10ull | ((oth & 15ull) << 8) | (own << 12) | ((oth >> 4) << 36);
  return t | (rt < 8 ? 0x8888888000000800ull : 0x0000000888888088ull);
}
__device__ __forceinline__ int knib(u64 t, int s) { return (int)(t >> (4 * s)) & 15; }
__device__ __forceinline__ unsigned kfirst(u64 t) { return (1u << knib(t, 1)) | (1u << knib(t, 2)) | (1u << knib(t, 3)) | (1u << knib(t, 4)); }
__device__ __forceinline__ int kv1(int bid) { return (bid + (0x4924 >> ((bid >> 2) & 14))) & 3; }

template <typename T> __device__ __forceinline__ T* uni(T* p) {
  const unsigned long long v = (unsigned long long)p;
  const unsigned lo = __builtin_amdgcn_readfirstlane((unsigned)v), hi = __builtin_amdgcn_readfirstlane((unsigned)(v >> 32));
  return (T*)(((unsigned long long)hi << 32) | lo);
}
__device__ __forceinline__ int lane_id() {
  int l;
  asm volatile("v_mbcnt_lo_u32_b32 %0, -1, 0\n\tv_mbcnt_hi_u32_b32 %0, -1, %0" : "=v"(l));
  __builtin_assume(l >= 0 && l < 64);
  return l;
}
#define IDS \
  const int lane = lane_id(); \
  asm volatile("" : "+s"(w)); \
  const int tid = w * 64 + lane; \
  int bid = blockIdx.x; asm volatile("" : "+s"(bid)); \
  ws = uni(ws); asm volatile("" : "+s"(ws)); \
  layer = __builtin_amdgcn_readfirstlane(layer); x0 = uni(x0); wo = uni(wo); \
    \
  const int rt = 2 * (bid & 4) + ((bid >> 3) & 7); \
  const int hp = 4 * (bid & 3) + (bid >> 6); \
  const int m0 = rt * 128; \
  const int fr = lane & 15, g = lane >> 4; \
  const int q4 = (lane & 15) >> 2, p4 = lane & 3; \
  const __amdgpu_buffer_rsrc_t rsW = __builtin_amdgcn_make_buffer_rsrc((void*)ws, (short)0, (int)WS_END, 0x00020000); \
  (void)m0; (void)fr; (void)q4; (void)p4; (void)rsW; (void)g;

#define LANES \
  const int lane = lane_id(), tid = w * 64 + lane, fr = lane & 15, g = lane >> 4, q4 = (lane & 15) >> 2, p4 = lane & 3; \
  (void)tid; (void)fr; (void)g; (void)q4; (void)p4;

__device__ __forceinline__ void code_touch(unsigned& junk, int bid, int lane) {
  unsigned long long pc;
  unsigned len, d;
  asm volatile("s_getpc_b64 %0\n.Lct%=:\n\ts_mov_b32 %2, .Lct%=-.Lmk_pc\n\ts_mov_b32 %1, .Lmk_end-.Lmk_pc" : "=s"(pc), "=s"(len), "=s"(d));
  pc -= d;
  const unsigned per = ((len >> 10) + 1) * 128, off = ((bid >> 3) & 7) * per + (lane * 128 < per ? lane * 128 : 0);
  asm volatile("global_load_dword %0, %1, %2" : "+v"(junk) : "v"(off < len - 4 ? off : 0u), "s"(pc) : "memory");
}

__device__ __forceinline__ void publish(__amdgpu_buffer_rsrc_t rsW, int tid, unsigned idx, u64 v) {
  unsigned off = WS_FLAGS + idx * FLS;
  asm volatile("" : "+v"(off), "+v"(v));
  WAITV(0);
  WAITL0; BAR;
  if (tid == 192) __builtin_amdgcn_raw_buffer_store_b64(__builtin_bit_cast(u32x2, v), rsW, off, 0, 16);
  asm volatile("" ::: "memory");
}
__device__ __forceinline__ u64 poll_flags(char* ws, u64 E, int set, int rt, int hp, int lane, unsigned n, u64 mask, u64 now = ~0ull) {
  u64 miss = 0;
  const int l = lane & 15, g = lane >> 4;
  const int fi = set * 256 + (g == 2 ? l * 16 + hp : rt * 16 + l);
  const unsigned need = ((mask >> lane) & 1) ? n : 0u;
  gu64* fp = (gu64*)(ws + (need ? WS_FLAGS + fi * FLS : 0));
  _Pragma("unroll 1") for (unsigned spins = 0; spins < (1u << 18); ++spins) {
    const u64 d = __hip_atomic_load(fp, __ATOMIC_RELAXED, __HIP_MEMORY_SCOPE_AGENT) - E;
    const bool ok = (need == 0u) || (d >= (u64)need && d <= 16ull);
    miss = __ballot(!ok);
    if (!(miss & now)) break;
    __builtin_amdgcn_s_sleep(1);
  }
  asm volatile("s_waitcnt vmcnt(0)" ::: "memory");
  return miss;
}

__device__ __forceinline__ u64 poll_eq(const char* ws, unsigned off, u64 want, u64 mask, u64 now) {
  u64 miss;
  _Pragma("unroll 1") for (unsigned spins = 0; spins < (1u << 18); ++spins) {
    u64 d;
    asm volatile("global_load_dwordx2 %0, %1, %2 sc1\n\ts_waitcnt vmcnt(0)" : "=v"(d) : "v"(off), "s"(ws) : "memory");
    miss = __ballot(d != want) & mask;
    if (!(miss & now)) break;
    __builtin_amdgcn_s_sleep(1);
  }
  return miss;
}

#define G1_MACROS \
  ".macro G1M rb, j, s, z\n" \
  " .set g1d, 160+24*(\\rb)+4*(((\\j)+2)-6*((\\j)>>2))\n .set g1a, 80+40*(\\s)+4*(\\rb)\n .set g1b, 96+40*(\\s)+4*(\\j)\n" \
  " .if ((\\j)>>1) == 1\n  .set g1t, g1a\n  .set g1a, g1b\n  .set g1b, g1t\n .endif\n" \
  " .if \\z\n  v_mfma_f32_16x16x32_bf16 v[g1d:g1d+3], v[g1b:g1b+3], v[g1a:g1a+3], 0\n" \
  " .else\n  v_mfma_f32_16x16x32_bf16 v[g1d:g1d+3], v[g1b:g1b+3], v[g1a:g1a+3], v[g1d:g1d+3]\n .endif\n" \
  ".endm\n" \
  ".macro G1RA rb, s, ra, oa\n" \
  " ds_read_b128 v[80+40*(\\s)+4*(\\rb):83+40*(\\s)+4*(\\rb)], \\ra offset:(\\oa)+(\\rb)*2048\n" \
  ".endm\n" \
  ".macro G1RB k, s, r0, r1, ob\n" \
  " .if (\\k) & 2\n  ds_read_b64_tr_b16 v[96+40*(\\s)+2*(\\k):97+40*(\\s)+2*(\\k)], \\r1 offset:(\\ob)+(((\\k)>>2)+1-3*((\\k)>>3))*2048+((\\k)&1)*512\n" \
  " .else\n  ds_read_b64_tr_b16 v[96+40*(\\s)+2*(\\k):97+40*(\\s)+2*(\\k)], \\r0 offset:(\\ob)+(((\\k)>>2)+1-3*((\\k)>>3))*2048+((\\k)&1)*512\n .endif\n" \
  ".endm\n" \
    \
  ".macro G1BK i, g, r0, r1, ob\n G1RB 4*(\\g)+((\\i)&3), (\\i)>>2, \\r0, \\r1, (\\ob)+((\\i)>>2)*12288\n.endm\n" \
  ".macro G1AK rb, s, a0, a1, oa\n .if \\s\n  G1RA \\rb, 1, \\a1, \\oa\n .else\n  G1RA \\rb, 0, \\a0, \\oa\n .endif\n.endm\n" \
  ".macro G1RDB g, r0, r1, ob\n .set g1i, 0\n .rept 8\n  G1BK g1i, \\g, \\r0, \\r1, \\ob\n  .set g1i, g1i+1\n .endr\n.endm\n" \
  ".macro G1RDA a0, a1, oa\n .set g1i, 0\n .rept 8\n  G1AK (g1i>>1)^2, g1i&1, \\a0, \\a1, \\oa\n  .set g1i, g1i+1\n .endr\n.endm\n" \
    \
  ".macro G1Q g, rb, q, i, z\n G1M \\rb, 2*(\\g)+((((\\i)>>1)^(\\q))&1), (((\\i)+1)>>1)&1, (\\z)&&(((\\i)&1)==0)\n.endm\n" \
    \
  ".macro G1SG g, z, rd, a0, a1, r0, r1, o\n" \
  " .set g1i, 0\n .rept 16\n" \
  "  .if ((\\g) == 0) && (g1i == 8) && (\\rd)\n   s_waitcnt lgkmcnt(7)\n   s_nop 0\n  .endif\n" \
  "  .if ((\\g) == 0) && (g1i == 15)\n   s_waitcnt lgkmcnt(0)\n  .endif\n" \
  "  G1Q \\g, (g1i>>2)^(2-((\\g)&1)), g1i>>2, g1i&3, \\z\n" \
  "  .if \\rd\n" \
  "   .if (\\g) == 0\n    .if g1i < 7\n     G1BK g1i, 2, \\r0, \\r1, \\o\n    .elseif g1i == 8\n     G1BK 7, 2, \\r0, \\r1, \\o\n    .endif\n" \
  "   .elseif (\\g) == 1\n    .if g1i < 8\n     G1BK g1i, 0, \\r0, \\r1, \\o\n    .endif\n" \
  "   .elseif (g1i & 2) == 0\n    G1BK (g1i&1)+2*(g1i>>2), 1, \\r0, \\r1, \\o\n" \
  "   .elseif (\\rd) == 1\n    G1AK (g1i>>2)^2, 1-(g1i&1), \\a0, \\a1, \\o\n   .endif\n" \
  "  .endif\n" \
  "  .set g1i, g1i+1\n .endr\n" \
  ".endm\n" \
  ""
#define G1_SLIDE \
  "s_cmp_eq_u32 %[wm], 0\n v_pk_fma_f32 v[240:241], v[96:97], v[138:139], v[240:241] op_sel_hi:[1,0,1]\n " \
  "v_pk_fma_f32 v[242:243], v[98:99], v[138:139], v[242:243] op_sel_hi:[1,0,1]\n " \
  "v_pk_fma_f32 v[244:245], v[100:101], v[138:139], v[244:245] op_sel_hi:[1,0,1]\n " \
  "v_pk_fma_f32 v[246:247], v[102:103], v[138:139], v[246:247] op_sel_hi:[1,0,1]\n v_pk_mul_f32 v[240:241], v[138:139], v[240:241] op_sel:[1,0]\n " \
  "v_pk_mul_f32 v[242:243], v[138:139], v[242:243] op_sel:[1,0]\n v_pk_mul_f32 v[244:245], v[138:139], v[244:245] op_sel:[1,0]\n " \
  "v_pk_mul_f32 v[246:247], v[138:139], v[246:247] op_sel:[1,0]\n v_pk_fma_f32 v[248:249], v[104:105], v[144:145], v[248:249] op_sel_hi:[1,0,1]\n " \
  "v_pk_fma_f32 v[250:251], v[106:107], v[144:145], v[250:251] op_sel_hi:[1,0,1]\n " \
  "v_pk_fma_f32 v[252:253], v[104:105], v[144:145], v[252:253] op_sel:[0,1,0]\n " \
  "v_pk_fma_f32 v[254:255], v[106:107], v[144:145], v[254:255] op_sel:[0,1,0]\n v_pk_mul_f32 v[248:249], v[248:249], v[108:109]\n " \
  "v_pk_mul_f32 v[250:251], v[250:251], v[110:111]\n v_pk_mul_f32 v[252:253], v[252:253], v[108:109]\n v_pk_mul_f32 v[254:255], v[254:255], v[110:111]\n " \
  "v_cvt_pk_bf16_f32 v240, v240, v241\n v_cvt_pk_bf16_f32 v241, v242, v243\n v_cvt_pk_bf16_f32 v242, v244, v245\n v_cvt_pk_bf16_f32 v243, v246, v247\n " \
  "v_cvt_pk_bf16_f32 v248, v248, v249\n v_cvt_pk_bf16_f32 v249, v250, v251\n v_cvt_pk_bf16_f32 v255, v254, v255\n v_cvt_pk_bf16_f32 v254, v252, v253\n " \
  "v_min_u32 v104, -2, v240\n v_min_u32 v105, -2, v241\n v_min_u32 v106, -2, v242\n v_min_u32 v107, -2, v243\n v_min_u32 v108, -2, v248\n " \
  "v_min_u32 v109, -2, v249\n v_min_u32 v110, -2, v254\n v_min_u32 v111, -2, v255\n s_waitcnt lgkmcnt(0)\n s_cbranch_scc1 .Lg1d%=\n " \
  "buffer_store_dwordx4 v[104:107], v55, %[rs], 0 offen sc1\n buffer_store_dwordx4 v[108:111], v55, %[rs], 0 offen offset:1024 sc1\n s_branch .Lg1e%=\n " \
  ".Lg1d%=:\n v_mov_b32 v53, 1\n ds_write_b128 v58, v[104:107]\n ds_write_b128 v58, v[108:111] offset:1024\n ds_write_b32 v59, v53\n .Lg1e%=:\n " \
  "v_pk_fma_f32 v[232:233], v[148:149], v[138:139], v[232:233] op_sel_hi:[1,0,1]\n " \
  "v_pk_fma_f32 v[234:235], v[150:151], v[138:139], v[234:235] op_sel_hi:[1,0,1]\n " \
  "v_pk_fma_f32 v[236:237], v[88:89], v[138:139], v[236:237] op_sel_hi:[1,0,1]\n " \
  "v_pk_fma_f32 v[238:239], v[90:91], v[138:139], v[238:239] op_sel_hi:[1,0,1]\n " \
  "v_pk_fma_f32 v[216:217], v[96:97], v[136:137], v[216:217] op_sel_hi:[1,0,1]\n " \
  "v_pk_fma_f32 v[218:219], v[98:99], v[136:137], v[218:219] op_sel_hi:[1,0,1]\n " \
  "v_pk_fma_f32 v[220:221], v[100:101], v[136:137], v[220:221] op_sel_hi:[1,0,1]\n " \
  "v_pk_fma_f32 v[222:223], v[102:103], v[136:137], v[222:223] op_sel_hi:[1,0,1]\n v_pk_mul_f32 v[216:217], v[136:137], v[216:217] op_sel:[1,0]\n " \
  "v_pk_mul_f32 v[218:219], v[136:137], v[218:219] op_sel:[1,0]\n v_pk_mul_f32 v[220:221], v[136:137], v[220:221] op_sel:[1,0]\n " \
  "v_pk_mul_f32 v[222:223], v[136:137], v[222:223] op_sel:[1,0]\n v_cvt_pk_bf16_f32 v232, v232, v233\n v_cvt_pk_bf16_f32 v233, v234, v235\n " \
  "v_cvt_pk_bf16_f32 v234, v236, v237\n v_cvt_pk_bf16_f32 v235, v238, v239\n v_cvt_pk_bf16_f32 v216, v216, v217\n v_cvt_pk_bf16_f32 v217, v218, v219\n " \
  "v_cvt_pk_bf16_f32 v218, v220, v221\n v_cvt_pk_bf16_f32 v219, v222, v223\n " \
  "v_pk_fma_f32 v[208:209], v[148:149], v[136:137], v[208:209] op_sel_hi:[1,0,1]\n " \
  "v_pk_fma_f32 v[210:211], v[150:151], v[136:137], v[210:211] op_sel_hi:[1,0,1]\n " \
  "v_pk_fma_f32 v[212:213], v[88:89], v[136:137], v[212:213] op_sel_hi:[1,0,1]\n " \
  "v_pk_fma_f32 v[214:215], v[90:91], v[136:137], v[214:215] op_sel_hi:[1,0,1]\n " \
  "v_pk_fma_f32 v[192:193], v[96:97], v[142:143], v[192:193] op_sel_hi:[1,0,1]\n " \
  "v_pk_fma_f32 v[194:195], v[98:99], v[142:143], v[194:195] op_sel_hi:[1,0,1]\n " \
  "v_pk_fma_f32 v[196:197], v[100:101], v[142:143], v[196:197] op_sel_hi:[1,0,1]\n " \
  "v_pk_fma_f32 v[198:199], v[102:103], v[142:143], v[198:199] op_sel_hi:[1,0,1]\n v_pk_mul_f32 v[192:193], v[142:143], v[192:193] op_sel:[1,0]\n " \
  "v_pk_mul_f32 v[194:195], v[142:143], v[194:195] op_sel:[1,0]\n v_pk_mul_f32 v[196:197], v[142:143], v[196:197] op_sel:[1,0]\n " \
  "v_pk_mul_f32 v[198:199], v[142:143], v[198:199] op_sel:[1,0]\n v_mfma_f32_16x16x32_bf16 v[120:123], v[240:243], v[232:235], v[40:43]\n " \
  "v_mfma_f32_16x16x32_bf16 v[236:239], v[216:219], v[232:235], v[36:39]\n v_cvt_pk_bf16_f32 v208, v208, v209\n v_cvt_pk_bf16_f32 v209, v210, v211\n " \
  "v_cvt_pk_bf16_f32 v210, v212, v213\n v_cvt_pk_bf16_f32 v211, v214, v215\n v_cvt_pk_bf16_f32 v192, v192, v193\n v_cvt_pk_bf16_f32 v193, v194, v195\n " \
  "v_cvt_pk_bf16_f32 v194, v196, v197\n v_cvt_pk_bf16_f32 v195, v198, v199\n " \
  "v_pk_fma_f32 v[184:185], v[148:149], v[142:143], v[184:185] op_sel_hi:[1,0,1]\n " \
  "v_pk_fma_f32 v[186:187], v[150:151], v[142:143], v[186:187] op_sel_hi:[1,0,1]\n " \
  "v_pk_fma_f32 v[188:189], v[88:89], v[142:143], v[188:189] op_sel_hi:[1,0,1]\n " \
  "v_pk_fma_f32 v[190:191], v[90:91], v[142:143], v[190:191] op_sel_hi:[1,0,1]\n " \
  "v_pk_fma_f32 v[168:169], v[96:97], v[140:141], v[168:169] op_sel_hi:[1,0,1]\n " \
  "v_pk_fma_f32 v[170:171], v[98:99], v[140:141], v[170:171] op_sel_hi:[1,0,1]\n " \
  "v_pk_fma_f32 v[172:173], v[100:101], v[140:141], v[172:173] op_sel_hi:[1,0,1]\n " \
  "v_pk_fma_f32 v[174:175], v[102:103], v[140:141], v[174:175] op_sel_hi:[1,0,1]\n v_pk_mul_f32 v[168:169], v[140:141], v[168:169] op_sel:[1,0]\n " \
  "v_pk_mul_f32 v[170:171], v[140:141], v[170:171] op_sel:[1,0]\n v_pk_mul_f32 v[172:173], v[140:141], v[172:173] op_sel:[1,0]\n " \
  "v_pk_mul_f32 v[174:175], v[140:141], v[174:175] op_sel:[1,0]\n v_mfma_f32_16x16x32_bf16 v[220:223], v[216:219], v[208:211], v[40:43]\n " \
  "v_mfma_f32_16x16x32_bf16 v[212:215], v[192:195], v[208:211], v[36:39]\n v_cvt_pk_bf16_f32 v184, v184, v185\n v_cvt_pk_bf16_f32 v185, v186, v187\n " \
  "v_cvt_pk_bf16_f32 v186, v188, v189\n v_cvt_pk_bf16_f32 v187, v190, v191\n v_cvt_pk_bf16_f32 v168, v168, v169\n v_cvt_pk_bf16_f32 v169, v170, v171\n " \
  "v_cvt_pk_bf16_f32 v170, v172, v173\n v_cvt_pk_bf16_f32 v171, v174, v175\n " \
  "v_pk_fma_f32 v[160:161], v[148:149], v[140:141], v[160:161] op_sel_hi:[1,0,1]\n " \
  "v_pk_fma_f32 v[162:163], v[150:151], v[140:141], v[162:163] op_sel_hi:[1,0,1]\n " \
  "v_pk_fma_f32 v[164:165], v[88:89], v[140:141], v[164:165] op_sel_hi:[1,0,1]\n " \
  "v_pk_fma_f32 v[166:167], v[90:91], v[140:141], v[166:167] op_sel_hi:[1,0,1]\n " \
  "v_pk_fma_f32 v[176:177], v[128:129], v[144:145], v[176:177] op_sel_hi:[1,0,1]\n " \
  "v_pk_fma_f32 v[178:179], v[130:131], v[144:145], v[178:179] op_sel_hi:[1,0,1]\n " \
  "v_pk_fma_f32 v[180:181], v[128:129], v[144:145], v[180:181] op_sel:[0,1,0]\n " \
  "v_pk_fma_f32 v[182:183], v[130:131], v[144:145], v[182:183] op_sel:[0,1,0]\n v_pk_mul_f32 v[176:177], v[176:177], v[92:93]\n " \
  "v_pk_mul_f32 v[178:179], v[178:179], v[94:95]\n v_pk_mul_f32 v[180:181], v[180:181], v[92:93]\n v_pk_mul_f32 v[182:183], v[182:183], v[94:95]\n " \
  "v_mfma_f32_16x16x32_bf16 v[196:199], v[192:195], v[184:187], v[40:43]\n v_mfma_f32_16x16x32_bf16 v[188:191], v[168:171], v[184:187], v[36:39]\n " \
  "v_cvt_pk_bf16_f32 v160, v160, v161\n v_cvt_pk_bf16_f32 v161, v162, v163\n v_cvt_pk_bf16_f32 v162, v164, v165\n v_cvt_pk_bf16_f32 v163, v166, v167\n " \
  "v_cvt_pk_bf16_f32 v174, v176, v177\n v_cvt_pk_bf16_f32 v175, v178, v179\n v_cvt_pk_bf16_f32 v180, v180, v181\n v_cvt_pk_bf16_f32 v181, v182, v183\n " \
  "v_pk_fma_f32 v[224:225], v[112:113], v[144:145], v[224:225] op_sel_hi:[1,0,1]\n " \
  "v_pk_fma_f32 v[226:227], v[114:115], v[144:145], v[226:227] op_sel_hi:[1,0,1]\n " \
  "v_pk_fma_f32 v[228:229], v[112:113], v[144:145], v[228:229] op_sel:[0,1,0]\n " \
  "v_pk_fma_f32 v[230:231], v[114:115], v[144:145], v[230:231] op_sel:[0,1,0]\n v_pk_mul_f32 v[224:225], v[224:225], v[116:117]\n " \
  "v_pk_mul_f32 v[226:227], v[226:227], v[118:119]\n v_pk_mul_f32 v[228:229], v[228:229], v[116:117]\n v_pk_mul_f32 v[230:231], v[230:231], v[118:119]\n " \
  "v_mfma_f32_16x16x32_bf16 v[124:127], v[168:171], v[160:163], v[40:43]\n v_cvt_pk_bf16_f32 v246, v224, v225\n v_cvt_pk_bf16_f32 v247, v226, v227\n " \
  "v_cvt_pk_bf16_f32 v252, v228, v229\n v_cvt_pk_bf16_f32 v253, v230, v231\n v_max3_f32 v108, v236, v237, v238\n v_max3_f32 v106, v212, v213, v214\n " \
  "v_max3_f32 v109, v239, v120, v121\n v_max3_f32 v107, v215, v220, v221\n v_max3_f32 v108, v108, v122, v123\n v_max3_f32 v106, v106, v222, v223\n " \
  "v_max_f32 v108, v108, v109\n v_max_f32 v106, v106, v107\n v_max3_f32 v104, v188, v189, v190\n v_max3_f32 v105, v191, v196, v197\n " \
  "v_max3_f32 v104, v104, v198, v199\n v_pk_fma_f32 v[200:201], v[152:153], v[144:145], v[200:201] op_sel_hi:[1,0,1]\n " \
  "v_pk_fma_f32 v[202:203], v[154:155], v[144:145], v[202:203] op_sel_hi:[1,0,1]\n v_max_f32 v104, v104, v105\n v_mov_b32 v110, v108\n " \
  "v_pk_fma_f32 v[204:205], v[152:153], v[144:145], v[204:205] op_sel:[0,1,0]\n " \
  "v_pk_fma_f32 v[206:207], v[154:155], v[144:145], v[206:207] op_sel:[0,1,0]\n v_permlane16_swap_b32 v104, v106\n v_permlane16_swap_b32 v108, v110\n " \
  "v_pk_mul_f32 v[200:201], v[200:201], v[156:157]\n v_pk_mul_f32 v[202:203], v[202:203], v[158:159]\n v_max_f32 v104, v104, v106\n " \
  "v_max_f32 v108, v108, v110\n v_pk_mul_f32 v[204:205], v[204:205], v[156:157]\n v_pk_mul_f32 v[206:207], v[206:207], v[158:159]\n " \
  "v_permlane32_swap_b32 v104, v108\n v_mul_f32 v60, 0x3e8293ee, v139\n v_mul_f32 v62, 0x3e8293ee, v137\n v_max_f32 v104, v104, v108\n " \
  "v_mov_b32 v108, v104\n v_mul_f32 v56, 0x3e8293ee, v143\n v_mul_f32 v146, 0x3e8293ee, v141\n v_permlane32_swap_b32 v104, v108\n " \
  "v_mov_b32 v132, 0x3f803f80\n v_mov_b32 v133, 0x3f803f80\n v_mov_b32 v106, v104\n v_mov_b32 v134, 0x3f803f80\n v_mov_b32 v135, 0x3f803f80\n " \
  "v_permlane16_swap_b32 v104, v106\n v_cvt_pk_bf16_f32 v244, v200, v201\n v_cvt_pk_bf16_f32 v245, v202, v203\n v_cvt_pk_bf16_f32 v250, v204, v205\n " \
  "v_cvt_pk_bf16_f32 v251, v206, v207\n v_cvt_pk_bf16_f32 v176, v200, v201\n v_cvt_pk_bf16_f32 v177, v202, v203\n v_cvt_pk_bf16_f32 v182, v204, v205\n " \
  "v_cvt_pk_bf16_f32 v183, v206, v207\n v_mul_f32_e64 v61, v60, -v108\n v_mul_f32_e64 v63, v62, -v106\n v_mul_f32_e64 v57, v56, -v104\n " \
  "v_pk_fma_f32 v[236:237], v[236:237], v[60:61], v[60:61] op_sel:[0,0,1] op_sel_hi:[1,0,1]\n " \
  "v_pk_fma_f32 v[238:239], v[238:239], v[60:61], v[60:61] op_sel:[0,0,1] op_sel_hi:[1,0,1]\n " \
  "v_pk_fma_f32 v[120:121], v[120:121], v[60:61], v[60:61] op_sel:[0,0,1] op_sel_hi:[1,0,1]\n " \
  "v_pk_fma_f32 v[122:123], v[122:123], v[60:61], v[60:61] op_sel:[0,0,1] op_sel_hi:[1,0,1]\n v_exp_f32 v236, v236\n v_exp_f32 v237, v237\n " \
  "v_exp_f32 v238, v238\n v_exp_f32 v239, v239\n v_exp_f32 v120, v120\n v_exp_f32 v121, v121\n v_exp_f32 v122, v122\n v_exp_f32 v123, v123\n " \
  "v_pk_fma_f32 v[212:213], v[212:213], v[62:63], v[62:63] op_sel:[0,0,1] op_sel_hi:[1,0,1]\n " \
  "v_pk_fma_f32 v[214:215], v[214:215], v[62:63], v[62:63] op_sel:[0,0,1] op_sel_hi:[1,0,1]\n " \
  "v_pk_fma_f32 v[220:221], v[220:221], v[62:63], v[62:63] op_sel:[0,0,1] op_sel_hi:[1,0,1]\n " \
  "v_pk_fma_f32 v[222:223], v[222:223], v[62:63], v[62:63] op_sel:[0,0,1] op_sel_hi:[1,0,1]\n v_cvt_pk_bf16_f32 v236, v236, v237\n " \
  "v_cvt_pk_bf16_f32 v237, v238, v239\n v_cvt_pk_bf16_f32 v238, v120, v121\n v_cvt_pk_bf16_f32 v239, v122, v123\n v_exp_f32 v212, v212\n " \
  "v_exp_f32 v213, v213\n v_mfma_f32_16x16x32_bf16 v[120:123], v[132:135], v[236:239], 0\n " \
  "v_mfma_f32_16x16x32_bf16 v[232:235], v[246:249], v[236:239], 0\n v_mfma_f32_16x16x32_bf16 v[240:243], v[252:255], v[236:239], 0\n " \
  "v_exp_f32 v214, v214\n v_exp_f32 v215, v215\n v_exp_f32 v220, v220\n v_exp_f32 v221, v221\n v_exp_f32 v222, v222\n v_exp_f32 v223, v223\n " \
  "v_pk_fma_f32 v[188:189], v[188:189], v[56:57], v[56:57] op_sel:[0,0,1] op_sel_hi:[1,0,1]\n " \
  "v_pk_fma_f32 v[190:191], v[190:191], v[56:57], v[56:57] op_sel:[0,0,1] op_sel_hi:[1,0,1]\n " \
  "v_pk_fma_f32 v[196:197], v[196:197], v[56:57], v[56:57] op_sel:[0,0,1] op_sel_hi:[1,0,1]\n " \
  "v_pk_fma_f32 v[198:199], v[198:199], v[56:57], v[56:57] op_sel:[0,0,1] op_sel_hi:[1,0,1]\n v_cvt_pk_bf16_f32 v212, v212, v213\n " \
  "v_cvt_pk_bf16_f32 v213, v214, v215\n v_cvt_pk_bf16_f32 v214, v220, v221\n v_cvt_pk_bf16_f32 v215, v222, v223\n v_exp_f32 v188, v188\n " \
  "s_cmp_eq_u32 %[wm], 0\n s_cbranch_scc1 .Lg1f%=\n ds_read_b32 v53, v59\n ds_read_b128 v[80:83], v58\n ds_read_b64 v[172:173], v58 offset:1024\n " \
  "ds_read_b64 v[178:179], v58 offset:1032\n s_branch .Lg1g%=\n .Lg1f%=:\n buffer_load_dwordx4 v[80:83], v55, %[rs], 0 offen sc1\n " \
  "buffer_load_dwordx2 v[172:173], v55, %[rs], 0 offen offset:1024 sc1\n buffer_load_dwordx2 v[178:179], v55, %[rs], 0 offen offset:1032 sc1\n " \
  ".Lg1g%=:\n v_exp_f32 v189, v189\n v_mfma_f32_16x16x32_bf16 v[220:223], v[132:135], v[212:215], 0\n " \
  "v_mfma_f32_16x16x32_bf16 v[224:227], v[244:247], v[212:215], 0\n v_mfma_f32_16x16x32_bf16 v[228:231], v[250:253], v[212:215], 0\n " \
  "v_exp_f32 v190, v190\n v_exp_f32 v191, v191\n v_exp_f32 v196, v196\n v_exp_f32 v197, v197\n v_exp_f32 v198, v198\n v_exp_f32 v199, v199\n " \
  "v_rcp_f32 v120, v120\n v_cvt_pk_bf16_f32 v188, v188, v189\n v_pk_mul_f32 v[232:233], v[232:233], v[120:121] op_sel_hi:[1,0]\n " \
  "v_pk_mul_f32 v[234:235], v[234:235], v[120:121] op_sel_hi:[1,0]\n v_pk_mul_f32 v[240:241], v[240:241], v[120:121] op_sel_hi:[1,0]\n " \
  "v_pk_mul_f32 v[242:243], v[242:243], v[120:121] op_sel_hi:[1,0]\n v_cvt_pk_bf16_f32 v189, v190, v191\n v_cvt_pk_bf16_f32 v190, v196, v197\n " \
  "v_cvt_pk_bf16_f32 v191, v198, v199\n v_rcp_f32 v220, v220\n v_cvt_pk_bf16_f32 v232, v232, v233\n v_cvt_pk_bf16_f32 v233, v234, v235\n " \
  "v_mfma_f32_16x16x32_bf16 v[196:199], v[132:135], v[188:191], 0\n v_mfma_f32_16x16x32_bf16 v[200:203], v[174:177], v[188:191], 0\n " \
  "v_mfma_f32_16x16x32_bf16 v[204:207], v[180:183], v[188:191], 0\n v_cvt_pk_bf16_f32 v234, v240, v241\n v_cvt_pk_bf16_f32 v235, v242, v243\n " \
  "buffer_store_dwordx4 v[232:235], v51, %[rs], 0 offen offset:3072 sc1\n ds_write_b128 v50, v[232:235] offset:3072\n " \
  "v_pk_mul_f32 v[224:225], v[224:225], v[220:221] op_sel_hi:[1,0]\n v_pk_mul_f32 v[226:227], v[226:227], v[220:221] op_sel_hi:[1,0]\n " \
  "v_pk_mul_f32 v[228:229], v[228:229], v[220:221] op_sel_hi:[1,0]\n v_pk_mul_f32 v[230:231], v[230:231], v[220:221] op_sel_hi:[1,0]\n " \
  "v_cvt_pk_bf16_f32 v224, v224, v225\n v_cvt_pk_bf16_f32 v225, v226, v227\n v_cvt_pk_bf16_f32 v226, v228, v229\n v_cvt_pk_bf16_f32 v227, v230, v231\n " \
  "v_rcp_f32 v196, v196\n buffer_store_dwordx4 v[224:227], v51, %[rs], 0 offen offset:2048 sc1\n ds_write_b128 v50, v[224:227] offset:2048\n " \
  "v_pk_mul_f32 v[200:201], v[200:201], v[196:197] op_sel_hi:[1,0]\n v_pk_mul_f32 v[202:203], v[202:203], v[196:197] op_sel_hi:[1,0]\n " \
  "v_pk_mul_f32 v[204:205], v[204:205], v[196:197] op_sel_hi:[1,0]\n v_pk_mul_f32 v[206:207], v[206:207], v[196:197] op_sel_hi:[1,0]\n " \
  "v_cvt_pk_bf16_f32 v200, v200, v201\n v_cvt_pk_bf16_f32 v201, v202, v203\n v_cvt_pk_bf16_f32 v202, v204, v205\n v_cvt_pk_bf16_f32 v203, v206, v207\n " \
  "buffer_store_dwordx4 v[200:203], v51, %[rs], 0 offen offset:1024 sc1\n ds_write_b128 v50, v[200:203] offset:1024\n s_mov_b32 vcc_hi, 0x40000\n " \
  "s_cmp_eq_u32 %[wm], 0\n s_cbranch_scc1 .Lg1h%=\n s_waitcnt lgkmcnt(3)\n .Lg1i%=:\n v_readfirstlane_b32 vcc_lo, v53\n s_nop 1\n " \
  "s_cmp_lg_u32 vcc_lo, 0\n s_cbranch_scc1 .Lg1k%=\n ds_read_b32 v53, v59\n ds_read_b128 v[80:83], v58\n ds_read_b64 v[172:173], v58 offset:1024\n " \
  "ds_read_b64 v[178:179], v58 offset:1032\n s_waitcnt lgkmcnt(0)\n s_sub_u32 vcc_hi, vcc_hi, 1\n s_cbranch_scc0 .Lg1i%=\n s_branch .Lg1k%=\n .Lg1h%=:\n " \
  "s_mov_b32 m0, 0x40000\n s_waitcnt vmcnt(3)\n .Lg1j%=:\n v_max3_u32 v104, v80, v81, v82\n v_max3_u32 v105, v83, v172, v173\n " \
  "v_max3_u32 v104, v104, v178, v179\n v_max_u32 v104, v104, v105\n v_cmp_eq_u32 vcc, -1, v104\n s_cbranch_vccz .Lg1k%=\n " \
  "buffer_load_dwordx4 v[80:83], v55, %[rs], 0 offen sc1\n buffer_load_dwordx2 v[172:173], v55, %[rs], 0 offen offset:1024 sc1\n " \
  "buffer_load_dwordx2 v[178:179], v55, %[rs], 0 offen offset:1032 sc1\n s_waitcnt vmcnt(0)\n s_sub_u32 m0, m0, 1\n s_cbranch_scc0 .Lg1j%=\n .Lg1k%=:\n " \
  "v_mfma_f32_16x16x32_bf16 v[164:167], v[80:83], v[160:163], v[44:47]\n s_nop 7\n v_max3_f32 v104, v164, v165, v166\n " \
  "v_max3_f32 v105, v167, v124, v125\n v_max3_f32 v104, v104, v126, v127\n v_max_f32 v104, v104, v105\n v_mov_b32 v105, v104\n s_nop 1\n " \
  "v_permlane16_swap_b32 v104, v105\n v_max_f32 v104, v104, v105\n v_mov_b32 v105, v104\n s_nop 1\n v_permlane32_swap_b32 v104, v105\n " \
  "v_max_f32 v104, v104, v105\n v_mul_f32_e64 v147, v146, -v104\n " \
  "v_pk_fma_f32 v[164:165], v[164:165], v[146:147], v[146:147] op_sel:[0,0,1] op_sel_hi:[1,0,1]\n " \
  "v_pk_fma_f32 v[166:167], v[166:167], v[146:147], v[146:147] op_sel:[0,0,1] op_sel_hi:[1,0,1]\n " \
  "v_pk_fma_f32 v[124:125], v[124:125], v[146:147], v[146:147] op_sel:[0,0,1] op_sel_hi:[1,0,1]\n " \
  "v_pk_fma_f32 v[126:127], v[126:127], v[146:147], v[146:147] op_sel:[0,0,1] op_sel_hi:[1,0,1]\n v_exp_f32 v164, v164\n v_exp_f32 v165, v165\n " \
  "v_exp_f32 v166, v166\n v_exp_f32 v167, v167\n v_exp_f32 v124, v124\n v_exp_f32 v125, v125\n v_exp_f32 v126, v126\n v_exp_f32 v127, v127\n " \
  "v_cvt_pk_bf16_f32 v164, v164, v165\n v_cvt_pk_bf16_f32 v165, v166, v167\n v_cvt_pk_bf16_f32 v166, v124, v125\n v_cvt_pk_bf16_f32 v167, v126, v127\n " \
  "s_nop 1\n v_mfma_f32_16x16x32_bf16 v[124:127], v[132:135], v[164:167], 0\n v_mfma_f32_16x16x32_bf16 v[168:171], v[172:175], v[164:167], 0\n " \
  "v_mfma_f32_16x16x32_bf16 v[84:87], v[178:181], v[164:167], 0\n s_nop 5\n v_rcp_f32 v124, v124\n s_nop 0\n " \
  "v_pk_mul_f32 v[168:169], v[168:169], v[124:125] op_sel_hi:[1,0]\n v_pk_mul_f32 v[170:171], v[170:171], v[124:125] op_sel_hi:[1,0]\n " \
  "v_pk_mul_f32 v[84:85], v[84:85], v[124:125] op_sel_hi:[1,0]\n v_pk_mul_f32 v[86:87], v[86:87], v[124:125] op_sel_hi:[1,0]\n " \
  "v_cvt_pk_bf16_f32 v168, v168, v169\n v_cvt_pk_bf16_f32 v169, v170, v171\n v_cvt_pk_bf16_f32 v170, v84, v85\n v_cvt_pk_bf16_f32 v171, v86, v87\n " \
  "buffer_store_dwordx4 v[168:171], v51, %[rs], 0 offen offset:0 sc1\n ds_write_b128 v50, v[168:171] offset:0\n "
#define G1_HEDGE \
  "G1Q 2, 2, 0, 0, 0\n ds_read2_b64 v[140:143], v49 offset1:16\n G1Q 2, 2, 0, 1, 0\n ds_read_b128 v[96:99], v48 offset:256\n G1Q 2, 2, 0, 2, 0\n " \
  "ds_read_b128 v[100:103], v48 offset:320\n G1Q 2, 2, 0, 3, 0\n ds_read2_b64 v[136:139], v49 offset0:32 offset1:48\n G1Q 2, 3, 1, 0, 0\n " \
  "ds_read_b64_tr_b16 v[104:105], v53\n G1Q 2, 3, 1, 1, 0\n ds_read_b64_tr_b16 v[106:107], v53 offset:1024\n G1Q 2, 3, 1, 2, 0\n " \
  "ds_read_b64_tr_b16 v[108:109], v53 offset:32\n G1Q 2, 3, 1, 3, 0\n ds_read_b64_tr_b16 v[110:111], v53 offset:1056\n G1Q 2, 0, 2, 0, 0\n " \
  "ds_read2_b32 v[144:145], v52 offset1:16\n G1Q 2, 0, 2, 1, 0\n ds_read_b128 v[148:151], v48\n G1Q 2, 0, 2, 2, 0\n " \
  "ds_read_b128 v[88:91], v48 offset:64\n G1Q 2, 0, 2, 3, 0\n ds_read_b128 v[128:131], v54\n G1Q 2, 1, 3, 0, 0\n ds_read_b128 v[92:95], v54 offset:512\n " \
  "G1Q 2, 1, 3, 1, 0\n ds_read_b128 v[132:135], v54 offset:64\n G1Q 2, 1, 3, 2, 0\n G1Q 2, 1, 3, 3, 0\n ds_read_b128 v[112:115], v54 offset:576\n " \
  "ds_read_b128 v[116:119], v54 offset:128\n ds_read_b128 v[152:155], v54 offset:640\n ds_read_b128 v[156:159], v54 offset:192\n " \
  "ds_read_b128 v[120:123], v54 offset:704\n s_waitcnt lgkmcnt(11)\n v_pk_fma_f32 v[168:169], v[96:97], v[140:141], v[168:169] op_sel_hi:[1,0,1]\n " \
  "v_pk_fma_f32 v[170:171], v[98:99], v[140:141], v[170:171] op_sel_hi:[1,0,1]\n " \
  "v_pk_fma_f32 v[172:173], v[100:101], v[140:141], v[172:173] op_sel_hi:[1,0,1]\n " \
  "v_pk_fma_f32 v[174:175], v[102:103], v[140:141], v[174:175] op_sel_hi:[1,0,1]\n " \
  "v_pk_fma_f32 v[192:193], v[96:97], v[142:143], v[192:193] op_sel_hi:[1,0,1]\n " \
  "v_pk_fma_f32 v[194:195], v[98:99], v[142:143], v[194:195] op_sel_hi:[1,0,1]\n " \
  "v_pk_fma_f32 v[196:197], v[100:101], v[142:143], v[196:197] op_sel_hi:[1,0,1]\n " \
  "v_pk_fma_f32 v[198:199], v[102:103], v[142:143], v[198:199] op_sel_hi:[1,0,1]\n v_cvt_pk_bf16_f32 v168, v168, v169\n " \
  "v_cvt_pk_bf16_f32 v169, v170, v171\n v_cvt_pk_bf16_f32 v170, v172, v173\n v_cvt_pk_bf16_f32 v171, v174, v175\n v_cvt_pk_bf16_f32 v192, v192, v193\n " \
  "v_cvt_pk_bf16_f32 v193, v194, v195\n v_cvt_pk_bf16_f32 v194, v196, v197\n v_cvt_pk_bf16_f32 v195, v198, v199\n " \
  "v_pk_fma_f32 v[216:217], v[96:97], v[136:137], v[216:217] op_sel_hi:[1,0,1]\n " \
  "v_pk_fma_f32 v[218:219], v[98:99], v[136:137], v[218:219] op_sel_hi:[1,0,1]\n " \
  "v_pk_fma_f32 v[220:221], v[100:101], v[136:137], v[220:221] op_sel_hi:[1,0,1]\n " \
  "v_pk_fma_f32 v[222:223], v[102:103], v[136:137], v[222:223] op_sel_hi:[1,0,1]\n " \
  "v_pk_fma_f32 v[240:241], v[96:97], v[138:139], v[240:241] op_sel_hi:[1,0,1]\n " \
  "v_pk_fma_f32 v[242:243], v[98:99], v[138:139], v[242:243] op_sel_hi:[1,0,1]\n " \
  "v_pk_fma_f32 v[244:245], v[100:101], v[138:139], v[244:245] op_sel_hi:[1,0,1]\n " \
  "v_pk_fma_f32 v[246:247], v[102:103], v[138:139], v[246:247] op_sel_hi:[1,0,1]\n v_mfma_f32_16x16x32_bf16 v[172:175], v[104:107], v[168:171], 0\n " \
  "v_mfma_f32_16x16x32_bf16 v[80:83], v[108:111], v[168:171], 0\n v_mfma_f32_16x16x32_bf16 v[196:199], v[104:107], v[192:195], 0\n " \
  "v_mfma_f32_16x16x32_bf16 v[84:87], v[108:111], v[192:195], 0\n v_cvt_pk_bf16_f32 v216, v216, v217\n v_cvt_pk_bf16_f32 v217, v218, v219\n " \
  "v_cvt_pk_bf16_f32 v218, v220, v221\n v_cvt_pk_bf16_f32 v219, v222, v223\n v_cvt_pk_bf16_f32 v240, v240, v241\n v_cvt_pk_bf16_f32 v241, v242, v243\n " \
  "v_cvt_pk_bf16_f32 v242, v244, v245\n v_cvt_pk_bf16_f32 v243, v246, v247\n v_mul_f32 v40, 0x3fb8aa3b, v141\n v_mul_f32 v42, 0x3fb8aa3b, v143\n " \
  "v_mul_f32 v44, 0x3fb8aa3b, v137\n v_mul_f32 v46, 0x3fb8aa3b, v139\n v_mfma_f32_16x16x32_bf16 v[220:223], v[104:107], v[216:219], 0\n " \
  "v_mfma_f32_16x16x32_bf16 v[124:127], v[108:111], v[216:219], 0\n v_mfma_f32_16x16x32_bf16 v[244:247], v[104:107], v[240:243], 0\n " \
  "v_mfma_f32_16x16x32_bf16 v[36:39], v[108:111], v[240:243], 0\n v_max3_f32 v168, v172, v173, v174\n v_max3_f32 v169, -v172, -v173, -v174\n " \
  "v_max3_f32 v168, v168, v175, v80\n v_max3_f32 v169, v169, -v175, -v80\n v_max3_f32 v168, v168, v81, v82\n v_max3_f32 v169, v169, -v81, -v82\n " \
  "v_max_f32_e64 v168, v168, v83\n v_max_f32_e64 v169, v169, -v83\n v_max3_f32 v170, v196, v197, v198\n v_max3_f32 v171, -v196, -v197, -v198\n " \
  "v_max3_f32 v170, v170, v199, v84\n v_max3_f32 v171, v171, -v199, -v84\n v_max3_f32 v170, v170, v85, v86\n v_max3_f32 v171, v171, -v85, -v86\n " \
  "v_max_f32_e64 v170, v170, v87\n v_max_f32_e64 v171, v171, -v87\n s_waitcnt lgkmcnt(0)\n " \
  "v_pk_fma_f32 v[176:177], v[128:129], v[144:145], v[176:177] op_sel_hi:[1,0,1]\n " \
  "v_pk_fma_f32 v[178:179], v[130:131], v[144:145], v[178:179] op_sel_hi:[1,0,1]\n " \
  "v_pk_fma_f32 v[180:181], v[128:129], v[144:145], v[180:181] op_sel:[0,1,0]\n " \
  "v_pk_fma_f32 v[182:183], v[130:131], v[144:145], v[182:183] op_sel:[0,1,0]\n v_pk_mul_f32 v[176:177], v[176:177], v[92:93]\n " \
  "v_pk_mul_f32 v[178:179], v[178:179], v[94:95]\n v_pk_mul_f32 v[180:181], v[180:181], v[92:93]\n v_pk_mul_f32 v[182:183], v[182:183], v[94:95]\n " \
  "v_pk_fma_f32 v[200:201], v[132:133], v[144:145], v[200:201] op_sel_hi:[1,0,1]\n " \
  "v_pk_fma_f32 v[202:203], v[134:135], v[144:145], v[202:203] op_sel_hi:[1,0,1]\n " \
  "v_pk_fma_f32 v[204:205], v[132:133], v[144:145], v[204:205] op_sel:[0,1,0]\n " \
  "v_pk_fma_f32 v[206:207], v[134:135], v[144:145], v[206:207] op_sel:[0,1,0]\n v_pk_mul_f32 v[200:201], v[200:201], v[112:113]\n " \
  "v_pk_mul_f32 v[202:203], v[202:203], v[114:115]\n v_pk_mul_f32 v[204:205], v[204:205], v[112:113]\n v_pk_mul_f32 v[206:207], v[206:207], v[114:115]\n " \
  "v_max3_f32 v192, v220, v221, v222\n v_max3_f32 v193, -v220, -v221, -v222\n v_max3_f32 v192, v192, v223, v124\n v_max3_f32 v193, v193, -v223, -v124\n " \
  "v_max3_f32 v192, v192, v125, v126\n v_max3_f32 v193, v193, -v125, -v126\n v_max_f32_e64 v192, v192, v127\n v_max_f32_e64 v193, v193, -v127\n " \
  "v_max3_f32 v194, v244, v245, v246\n v_max3_f32 v195, -v244, -v245, -v246\n v_max3_f32 v194, v194, v247, v36\n v_max3_f32 v195, v195, -v247, -v36\n " \
  "v_max3_f32 v194, v194, v37, v38\n v_max3_f32 v195, v195, -v37, -v38\n v_max_f32_e64 v194, v194, v39\n v_max_f32_e64 v195, v195, -v39\n " \
  "v_permlane16_swap_b32 v168, v169\n v_permlane16_swap_b32 v170, v171\n v_permlane16_swap_b32 v192, v193\n v_permlane16_swap_b32 v194, v195\n " \
  "v_pk_fma_f32 v[224:225], v[116:117], v[144:145], v[224:225] op_sel_hi:[1,0,1]\n " \
  "v_pk_fma_f32 v[226:227], v[118:119], v[144:145], v[226:227] op_sel_hi:[1,0,1]\n v_max_f32 v168, v168, v169\n v_max_f32 v170, v170, v171\n " \
  "v_max_f32 v192, v192, v193\n v_max_f32 v194, v194, v195\n v_pk_fma_f32 v[228:229], v[116:117], v[144:145], v[228:229] op_sel:[0,1,0]\n " \
  "v_pk_fma_f32 v[230:231], v[118:119], v[144:145], v[230:231] op_sel:[0,1,0]\n v_permlane32_swap_b32 v168, v170\n v_permlane32_swap_b32 v192, v194\n " \
  "v_pk_mul_f32 v[224:225], v[224:225], v[152:153]\n v_pk_mul_f32 v[226:227], v[226:227], v[154:155]\n v_max_f32 v168, v168, v170\n " \
  "v_max_f32 v192, v192, v194\n v_pk_mul_f32 v[228:229], v[228:229], v[152:153]\n v_pk_mul_f32 v[230:231], v[230:231], v[154:155]\n " \
  "v_mov_b32 v170, v168\n v_mov_b32 v194, v192\n v_pk_fma_f32 v[248:249], v[156:157], v[144:145], v[248:249] op_sel_hi:[1,0,1]\n " \
  "v_pk_fma_f32 v[250:251], v[158:159], v[144:145], v[250:251] op_sel_hi:[1,0,1]\n v_permlane32_swap_b32 v168, v170\n v_permlane32_swap_b32 v192, v194\n " \
  "v_pk_fma_f32 v[252:253], v[156:157], v[144:145], v[252:253] op_sel:[0,1,0]\n " \
  "v_pk_fma_f32 v[254:255], v[158:159], v[144:145], v[254:255] op_sel:[0,1,0]\n v_mov_b32 v169, v168\n v_mov_b32 v171, v170\n v_mov_b32 v193, v192\n " \
  "v_mov_b32 v195, v194\n v_pk_mul_f32 v[248:249], v[248:249], v[120:121]\n v_pk_mul_f32 v[250:251], v[250:251], v[122:123]\n " \
  "v_permlane16_swap_b32 v168, v169\n v_permlane16_swap_b32 v170, v171\n v_permlane16_swap_b32 v192, v193\n v_permlane16_swap_b32 v194, v195\n " \
  "v_pk_mul_f32 v[252:253], v[252:253], v[120:121]\n v_pk_mul_f32 v[254:255], v[254:255], v[122:123]\n v_cvt_pk_bf16_f32 v176, v176, v177\n " \
  "v_cvt_pk_bf16_f32 v177, v178, v179\n v_cvt_pk_bf16_f32 v178, v200, v201\n v_cvt_pk_bf16_f32 v179, v202, v203\n v_cvt_pk_bf16_f32 v180, v180, v181\n " \
  "v_cvt_pk_bf16_f32 v181, v182, v183\n v_cvt_pk_bf16_f32 v182, v204, v205\n v_cvt_pk_bf16_f32 v183, v206, v207\n v_cvt_pk_bf16_f32 v224, v224, v225\n " \
  "v_cvt_pk_bf16_f32 v225, v226, v227\n v_cvt_pk_bf16_f32 v226, v248, v249\n v_cvt_pk_bf16_f32 v227, v250, v251\n v_cvt_pk_bf16_f32 v228, v228, v229\n " \
  "v_cvt_pk_bf16_f32 v229, v230, v231\n v_cvt_pk_bf16_f32 v230, v252, v253\n v_cvt_pk_bf16_f32 v231, v254, v255\n v_mul_f32_e64 v41, v40, -v168\n " \
  "v_mul_f32_e64 v146, v40, -v169\n v_mul_f32_e64 v43, v42, -v170\n v_mul_f32_e64 v147, v42, -v171\n v_mul_f32_e64 v45, v44, -v192\n " \
  "v_mul_f32_e64 v144, v44, -v193\n v_mul_f32_e64 v47, v46, -v194\n v_mul_f32_e64 v145, v46, -v195\n " \
  "v_pk_fma_f32 v[200:201], v[172:173], v[40:41], v[146:147] op_sel_hi:[1,0,0] neg_lo:[1,0,0] neg_hi:[1,0,0]\n " \
  "v_pk_fma_f32 v[202:203], v[174:175], v[40:41], v[146:147] op_sel_hi:[1,0,0] neg_lo:[1,0,0] neg_hi:[1,0,0]\n " \
  "v_pk_fma_f32 v[204:205], v[80:81], v[40:41], v[146:147] op_sel_hi:[1,0,0] neg_lo:[1,0,0] neg_hi:[1,0,0]\n " \
  "v_pk_fma_f32 v[206:207], v[82:83], v[40:41], v[146:147] op_sel_hi:[1,0,0] neg_lo:[1,0,0] neg_hi:[1,0,0]\n " \
  "v_pk_fma_f32 v[172:173], v[172:173], v[40:41], v[40:41] op_sel:[0,0,1] op_sel_hi:[1,0,1]\n " \
  "v_pk_fma_f32 v[174:175], v[174:175], v[40:41], v[40:41] op_sel:[0,0,1] op_sel_hi:[1,0,1]\n " \
  "v_pk_fma_f32 v[80:81], v[80:81], v[40:41], v[40:41] op_sel:[0,0,1] op_sel_hi:[1,0,1]\n " \
  "v_pk_fma_f32 v[82:83], v[82:83], v[40:41], v[40:41] op_sel:[0,0,1] op_sel_hi:[1,0,1]\n ds_write_b128 v55, v[176:179] offset:0\n " \
  "v_exp_f32 v200, v200\n v_exp_f32 v201, v201\n v_exp_f32 v202, v202\n v_exp_f32 v203, v203\n v_exp_f32 v204, v204\n v_exp_f32 v205, v205\n " \
  "v_exp_f32 v206, v206\n v_exp_f32 v207, v207\n v_exp_f32 v172, v172\n v_exp_f32 v173, v173\n v_exp_f32 v174, v174\n v_exp_f32 v175, v175\n " \
  "v_exp_f32 v80, v80\n v_exp_f32 v81, v81\n v_exp_f32 v82, v82\n v_exp_f32 v83, v83\n " \
  "v_pk_fma_f32 v[248:249], v[196:197], v[42:43], v[146:147] op_sel:[0,0,1] op_sel_hi:[1,0,1] neg_lo:[1,0,0] neg_hi:[1,0,0]\n " \
  "v_pk_fma_f32 v[250:251], v[198:199], v[42:43], v[146:147] op_sel:[0,0,1] op_sel_hi:[1,0,1] neg_lo:[1,0,0] neg_hi:[1,0,0]\n " \
  "v_pk_fma_f32 v[252:253], v[84:85], v[42:43], v[146:147] op_sel:[0,0,1] op_sel_hi:[1,0,1] neg_lo:[1,0,0] neg_hi:[1,0,0]\n " \
  "v_pk_fma_f32 v[254:255], v[86:87], v[42:43], v[146:147] op_sel:[0,0,1] op_sel_hi:[1,0,1] neg_lo:[1,0,0] neg_hi:[1,0,0]\n " \
  "v_pk_fma_f32 v[196:197], v[196:197], v[42:43], v[42:43] op_sel:[0,0,1] op_sel_hi:[1,0,1]\n " \
  "v_pk_fma_f32 v[198:199], v[198:199], v[42:43], v[42:43] op_sel:[0,0,1] op_sel_hi:[1,0,1]\n " \
  "v_pk_fma_f32 v[84:85], v[84:85], v[42:43], v[42:43] op_sel:[0,0,1] op_sel_hi:[1,0,1]\n " \
  "v_pk_fma_f32 v[86:87], v[86:87], v[42:43], v[42:43] op_sel:[0,0,1] op_sel_hi:[1,0,1]\n ds_write_b128 v55, v[180:183] offset:1024\n " \
  "v_exp_f32 v248, v248\n v_exp_f32 v249, v249\n v_exp_f32 v250, v250\n v_exp_f32 v251, v251\n v_exp_f32 v252, v252\n v_exp_f32 v253, v253\n " \
  "v_exp_f32 v254, v254\n v_exp_f32 v255, v255\n v_exp_f32 v196, v196\n v_exp_f32 v197, v197\n v_exp_f32 v198, v198\n v_exp_f32 v199, v199\n " \
  "v_exp_f32 v84, v84\n v_exp_f32 v85, v85\n v_exp_f32 v86, v86\n v_exp_f32 v87, v87\n " \
  "v_pk_fma_f32 v[128:129], v[220:221], v[44:45], v[144:145] op_sel_hi:[1,0,0] neg_lo:[1,0,0] neg_hi:[1,0,0]\n " \
  "v_pk_fma_f32 v[130:131], v[222:223], v[44:45], v[144:145] op_sel_hi:[1,0,0] neg_lo:[1,0,0] neg_hi:[1,0,0]\n " \
  "v_pk_fma_f32 v[132:133], v[124:125], v[44:45], v[144:145] op_sel_hi:[1,0,0] neg_lo:[1,0,0] neg_hi:[1,0,0]\n " \
  "v_pk_fma_f32 v[134:135], v[126:127], v[44:45], v[144:145] op_sel_hi:[1,0,0] neg_lo:[1,0,0] neg_hi:[1,0,0]\n " \
  "v_pk_fma_f32 v[220:221], v[220:221], v[44:45], v[44:45] op_sel:[0,0,1] op_sel_hi:[1,0,1]\n " \
  "v_pk_fma_f32 v[222:223], v[222:223], v[44:45], v[44:45] op_sel:[0,0,1] op_sel_hi:[1,0,1]\n " \
  "v_pk_fma_f32 v[124:125], v[124:125], v[44:45], v[44:45] op_sel:[0,0,1] op_sel_hi:[1,0,1]\n " \
  "v_pk_fma_f32 v[126:127], v[126:127], v[44:45], v[44:45] op_sel:[0,0,1] op_sel_hi:[1,0,1]\n ds_write_b128 v55, v[224:227] offset:8192\n " \
  "v_pk_add_f32 v[216:217], v[172:173], v[174:175]\n v_pk_add_f32 v[218:219], v[80:81], v[82:83]\n v_pk_add_f32 v[216:217], v[216:217], v[218:219]\n " \
  "v_add_f32 v168, v216, v217\n v_pk_add_f32 v[216:217], v[200:201], v[202:203]\n v_pk_add_f32 v[218:219], v[204:205], v[206:207]\n " \
  "v_pk_add_f32 v[216:217], v[216:217], v[218:219]\n v_add_f32 v169, v216, v217\n v_pk_add_f32 v[216:217], v[196:197], v[198:199]\n " \
  "v_pk_add_f32 v[218:219], v[84:85], v[86:87]\n v_pk_add_f32 v[216:217], v[216:217], v[218:219]\n v_add_f32 v170, v216, v217\n " \
  "v_pk_add_f32 v[216:217], v[248:249], v[250:251]\n v_pk_add_f32 v[218:219], v[252:253], v[254:255]\n v_pk_add_f32 v[216:217], v[216:217], v[218:219]\n " \
  "v_add_f32 v171, v216, v217\n v_exp_f32 v128, v128\n v_exp_f32 v129, v129\n v_permlane16_swap_b32 v168, v169\n v_permlane16_swap_b32 v170, v171\n " \
  "v_exp_f32 v130, v130\n v_exp_f32 v131, v131\n v_add_f32 v168, v168, v169\n v_add_f32 v170, v170, v171\n v_exp_f32 v132, v132\n v_exp_f32 v133, v133\n " \
  "v_permlane32_swap_b32 v168, v170\n v_exp_f32 v134, v134\n v_exp_f32 v135, v135\n v_add_f32 v168, v168, v170\n v_exp_f32 v220, v220\n " \
  "v_exp_f32 v221, v221\n v_rcp_f32 v168, v168\n v_exp_f32 v222, v222\n v_exp_f32 v223, v223\n v_mov_b32 v170, v168\n v_exp_f32 v124, v124\n " \
  "v_exp_f32 v125, v125\n v_permlane32_swap_b32 v168, v170\n v_exp_f32 v126, v126\n v_exp_f32 v127, v127\n v_mov_b32 v169, v168\n v_mov_b32 v171, v170\n " \
  "v_pk_fma_f32 v[112:113], v[244:245], v[46:47], v[144:145] op_sel:[0,0,1] op_sel_hi:[1,0,1] neg_lo:[1,0,0] neg_hi:[1,0,0]\n " \
  "v_pk_fma_f32 v[114:115], v[246:247], v[46:47], v[144:145] op_sel:[0,0,1] op_sel_hi:[1,0,1] neg_lo:[1,0,0] neg_hi:[1,0,0]\n " \
  "v_permlane16_swap_b32 v168, v169\n v_permlane16_swap_b32 v170, v171\n " \
  "v_pk_fma_f32 v[116:117], v[36:37], v[46:47], v[144:145] op_sel:[0,0,1] op_sel_hi:[1,0,1] neg_lo:[1,0,0] neg_hi:[1,0,0]\n " \
  "v_pk_fma_f32 v[118:119], v[38:39], v[46:47], v[144:145] op_sel:[0,0,1] op_sel_hi:[1,0,1] neg_lo:[1,0,0] neg_hi:[1,0,0]\n " \
  "v_pk_fma_f32 v[244:245], v[244:245], v[46:47], v[46:47] op_sel:[0,0,1] op_sel_hi:[1,0,1]\n " \
  "v_pk_fma_f32 v[246:247], v[246:247], v[46:47], v[46:47] op_sel:[0,0,1] op_sel_hi:[1,0,1]\n " \
  "v_pk_fma_f32 v[36:37], v[36:37], v[46:47], v[46:47] op_sel:[0,0,1] op_sel_hi:[1,0,1]\n " \
  "v_pk_fma_f32 v[38:39], v[38:39], v[46:47], v[46:47] op_sel:[0,0,1] op_sel_hi:[1,0,1]\n ds_write_b128 v55, v[228:231] offset:9216\n " \
  "v_exp_f32 v112, v112\n v_pk_mul_f32 v[172:173], v[172:173], v[168:169] op_sel_hi:[1,0]\n v_exp_f32 v113, v113\n " \
  "v_pk_mul_f32 v[174:175], v[174:175], v[168:169] op_sel_hi:[1,0]\n v_exp_f32 v114, v114\n v_cvt_pk_bf16_f32 v172, v172, v173\n v_exp_f32 v115, v115\n " \
  "v_cvt_pk_bf16_f32 v173, v174, v175\n v_exp_f32 v116, v116\n ds_write_b64 v58, v[172:173] offset:0\n v_exp_f32 v117, v117\n " \
  "v_pk_mul_f32 v[80:81], v[80:81], v[168:169] op_sel_hi:[1,0]\n v_exp_f32 v118, v118\n v_pk_mul_f32 v[82:83], v[82:83], v[168:169] op_sel_hi:[1,0]\n " \
  "v_exp_f32 v119, v119\n v_cvt_pk_bf16_f32 v80, v80, v81\n v_exp_f32 v244, v244\n v_cvt_pk_bf16_f32 v81, v82, v83\n v_exp_f32 v245, v245\n " \
  "ds_write_b64 v58, v[80:81] offset:32\n v_exp_f32 v246, v246\n v_pk_mul_f32 v[200:201], v[200:201], v[168:169] op_sel:[0,1]\n v_exp_f32 v247, v247\n " \
  "v_pk_mul_f32 v[202:203], v[202:203], v[168:169] op_sel:[0,1]\n v_exp_f32 v36, v36\n v_cvt_pk_bf16_f32 v200, v200, v201\n v_exp_f32 v37, v37\n " \
  "v_cvt_pk_bf16_f32 v201, v202, v203\n v_exp_f32 v38, v38\n ds_write_b64 v58, v[200:201] offset:64\n v_exp_f32 v39, v39\n " \
  "v_pk_mul_f32 v[204:205], v[204:205], v[168:169] op_sel:[0,1]\n v_pk_mul_f32 v[206:207], v[206:207], v[168:169] op_sel:[0,1]\n " \
  "v_cvt_pk_bf16_f32 v204, v204, v205\n v_cvt_pk_bf16_f32 v205, v206, v207\n ds_write_b64 v58, v[204:205] offset:96\n " \
  "v_pk_add_f32 v[216:217], v[220:221], v[222:223]\n v_pk_add_f32 v[218:219], v[124:125], v[126:127]\n v_pk_add_f32 v[216:217], v[216:217], v[218:219]\n " \
  "v_add_f32 v192, v216, v217\n v_pk_add_f32 v[216:217], v[128:129], v[130:131]\n v_pk_add_f32 v[218:219], v[132:133], v[134:135]\n " \
  "v_pk_add_f32 v[216:217], v[216:217], v[218:219]\n v_add_f32 v193, v216, v217\n v_pk_add_f32 v[216:217], v[244:245], v[246:247]\n " \
  "v_pk_add_f32 v[218:219], v[36:37], v[38:39]\n v_pk_add_f32 v[216:217], v[216:217], v[218:219]\n v_add_f32 v194, v216, v217\n " \
  "v_pk_add_f32 v[216:217], v[112:113], v[114:115]\n v_pk_add_f32 v[218:219], v[116:117], v[118:119]\n v_pk_add_f32 v[216:217], v[216:217], v[218:219]\n " \
  "v_add_f32 v195, v216, v217\n v_pk_mul_f32 v[196:197], v[196:197], v[170:171] op_sel_hi:[1,0]\n " \
  "v_pk_mul_f32 v[198:199], v[198:199], v[170:171] op_sel_hi:[1,0]\n v_permlane16_swap_b32 v192, v193\n v_permlane16_swap_b32 v194, v195\n " \
  "v_cvt_pk_bf16_f32 v196, v196, v197\n v_cvt_pk_bf16_f32 v197, v198, v199\n v_add_f32 v192, v192, v193\n v_add_f32 v194, v194, v195\n " \
  "ds_write_b64 v58, v[196:197] offset:2304\n v_pk_mul_f32 v[84:85], v[84:85], v[170:171] op_sel_hi:[1,0]\n v_permlane32_swap_b32 v192, v194\n " \
  "v_pk_mul_f32 v[86:87], v[86:87], v[170:171] op_sel_hi:[1,0]\n v_cvt_pk_bf16_f32 v84, v84, v85\n v_add_f32 v192, v192, v194\n " \
  "v_cvt_pk_bf16_f32 v85, v86, v87\n ds_write_b64 v58, v[84:85] offset:2336\n v_rcp_f32 v192, v192\n " \
  "v_pk_mul_f32 v[248:249], v[248:249], v[170:171] op_sel:[0,1]\n v_pk_mul_f32 v[250:251], v[250:251], v[170:171] op_sel:[0,1]\n v_mov_b32 v194, v192\n " \
  "v_cvt_pk_bf16_f32 v248, v248, v249\n v_cvt_pk_bf16_f32 v249, v250, v251\n v_permlane32_swap_b32 v192, v194\n " \
  "ds_write_b64 v58, v[248:249] offset:2368\n v_pk_mul_f32 v[252:253], v[252:253], v[170:171] op_sel:[0,1]\n v_mov_b32 v193, v192\n " \
  "v_mov_b32 v195, v194\n v_pk_mul_f32 v[254:255], v[254:255], v[170:171] op_sel:[0,1]\n v_cvt_pk_bf16_f32 v252, v252, v253\n " \
  "v_permlane16_swap_b32 v192, v193\n v_permlane16_swap_b32 v194, v195\n v_cvt_pk_bf16_f32 v253, v254, v255\n ds_write_b64 v58, v[252:253] offset:2400\n " \
  "v_pk_mul_f32 v[220:221], v[220:221], v[192:193] op_sel_hi:[1,0]\n v_pk_mul_f32 v[222:223], v[222:223], v[192:193] op_sel_hi:[1,0]\n " \
  "v_cvt_pk_bf16_f32 v220, v220, v221\n v_cvt_pk_bf16_f32 v221, v222, v223\n ds_write_b64 v58, v[220:221] offset:18432\n " \
  "v_pk_mul_f32 v[124:125], v[124:125], v[192:193] op_sel_hi:[1,0]\n v_pk_mul_f32 v[126:127], v[126:127], v[192:193] op_sel_hi:[1,0]\n " \
  "v_cvt_pk_bf16_f32 v124, v124, v125\n v_cvt_pk_bf16_f32 v125, v126, v127\n ds_write_b64 v58, v[124:125] offset:18464\n " \
  "v_pk_mul_f32 v[128:129], v[128:129], v[192:193] op_sel:[0,1]\n v_pk_mul_f32 v[130:131], v[130:131], v[192:193] op_sel:[0,1]\n " \
  "v_cvt_pk_bf16_f32 v128, v128, v129\n v_cvt_pk_bf16_f32 v129, v130, v131\n ds_write_b64 v58, v[128:129] offset:18496\n " \
  "v_pk_mul_f32 v[132:133], v[132:133], v[192:193] op_sel:[0,1]\n v_pk_mul_f32 v[134:135], v[134:135], v[192:193] op_sel:[0,1]\n " \
  "v_cvt_pk_bf16_f32 v132, v132, v133\n v_cvt_pk_bf16_f32 v133, v134, v135\n ds_write_b64 v58, v[132:133] offset:18528\n " \
  "v_pk_mul_f32 v[244:245], v[244:245], v[194:195] op_sel_hi:[1,0]\n v_pk_mul_f32 v[246:247], v[246:247], v[194:195] op_sel_hi:[1,0]\n " \
  "v_cvt_pk_bf16_f32 v244, v244, v245\n v_cvt_pk_bf16_f32 v245, v246, v247\n ds_write_b64 v58, v[244:245] offset:20736\n " \
  "v_pk_mul_f32 v[36:37], v[36:37], v[194:195] op_sel_hi:[1,0]\n v_pk_mul_f32 v[38:39], v[38:39], v[194:195] op_sel_hi:[1,0]\n " \
  "v_cvt_pk_bf16_f32 v36, v36, v37\n v_cvt_pk_bf16_f32 v37, v38, v39\n ds_write_b64 v58, v[36:37] offset:20768\n " \
  "v_pk_mul_f32 v[112:113], v[112:113], v[194:195] op_sel:[0,1]\n v_pk_mul_f32 v[114:115], v[114:115], v[194:195] op_sel:[0,1]\n " \
  "v_cvt_pk_bf16_f32 v112, v112, v113\n v_cvt_pk_bf16_f32 v113, v114, v115\n ds_write_b64 v58, v[112:113] offset:20800\n " \
  "v_pk_mul_f32 v[116:117], v[116:117], v[194:195] op_sel:[0,1]\n v_pk_mul_f32 v[118:119], v[118:119], v[194:195] op_sel:[0,1]\n " \
  "v_cvt_pk_bf16_f32 v116, v116, v117\n v_cvt_pk_bf16_f32 v117, v118, v119\n ds_write_b64 v58, v[116:117] offset:20832\n "
#define G1_PURGE \
  ".purgem G1M\n .purgem G1RA\n .purgem G1RB\n .purgem G1BK\n .purgem G1AK\n .purgem G1RDB\n .purgem G1RDA\n .purgem G1Q\n .purgem G1SG\n "
#define G1_CLOBBER \
  "v80", "v81", "v82", "v83", "v84", "v85", "v86", "v87", "v92", "v93", "v94", "v95", "v96", "v97", "v98", "v99", "v100", "v101", "v102", "v103", \
  "v112", "v113", "v114", "v115", "v116", "v117", "v118", "v119", "v120", "v121", "v122", "v123", "v124", "v125", "v126", "v127", "v128", "v129", \
  "v130", "v131", "v132", "v133", "v134", "v135", "v144", "v145", "v146", "v147", "v152", "v153", "v154", "v155", \
  "v156", "v157", "v158", "v159"

template <int MODE>
__device__ __forceinline__ void g1_phase(char* smem, char* ws, u64& E, int w, f32x16& xq, int layer, const u16* __restrict__ x0, const u16* __restrict__ W,
                                         const u16* __restrict__ wo, const u16* __restrict__ wphi,
                                         const u16* __restrict__ wn1, const u16* __restrict__ wn2) {
  constexpr bool HH = (MODE == 1);
  constexpr int ABYTES = 128 * 128;
  constexpr int STAGE = ABYTES + 3 * 8192;
  constexpr int OFF_FX = 16384;
  constexpr int OFF_SP = 3 * STAGE;
  constexpr int OFF_RS = !HH ? 159744 : OFF_SP + 16384;
  constexpr int OFF_CS = OFF_RS + (HH ? 1152 : 1024);
  constexpr int OFF_WP = OFF_CS + 1536;
  constexpr int OFF_JK = LDS_TOTAL - 1024;
  constexpr int OFF_PK = 0;
  constexpr int OFF_VF = OFF_WP + 4096;
  constexpr int OFF_KV = 16384;
  constexpr int OFF_R2 = HH ? OFF_VF + 16384 : OFF_CS + 1024;
  const int XS = OFF_WO + 9 * 8192 + (w & 3) * 2048;
  constexpr int OFF_RD = OFF_CS + 768;
  constexpr int PSTR = 144;
  IDS
  W = uni(W); wphi = uni(wphi); wn1 = uni(wn1); wn2 = uni(wn2);
  const int kg = w >> 2, wm = (w >> 1) & 1, wn = w & 1;
  const bool first = !HH && layer == 0;
  f32x4 acc[2][2], qh[4], csq[2], st01, st23;
  bf16x8 wf[2];
  u32x4 pw[8];
  ST(layer * 24 + 0)
  const int R0 = wm * 64 + (HH ? kg * 32 : 0);
  u32x16 pa;
#define pcs pa[0]
#define prs pa[1]
#define opo pa[2]
#define ogo pa[3]
#define pkw pa[5]
#define hof pa[7]
#define dm pa[8]
#define dm2 pa[9]
#define pcv pa[4]
#define pwf pa[5]
#define prv pa[6]
#define pvf pa[7]
#define ppk pa[10]
#define ppa pa[11]
#define pvr pa[12]
#define gkv pa[13]
#define gkz pa[14]
#define pwl pa[15]
#define pwm pa[4]
#define plb pa[10]
#define prd pa[11]
#define dm3 pa[12]
  f32x4 bias[3];
  const unsigned sb = lds_addr(smem);
  auto prep = [&]() {

    pcs = sb + OFF_CS + wn * 128 + g * 16;
    prs = sb + OFF_RS + (R0 + fr) * 8;
    opo = (unsigned)(wn * 8192 + R0 * 64 + lane * 16);
    ogo = opo + WS_O + layer * (SEQ * DM * 2) + (unsigned)((rt * NH + 2 * hp) * 8192);
    opo += sb;
    pcv = sb + OFF_CS + (wn * 32 + 128 + fr) * 4;
    prv = sb + OFF_R2 + (R0 + 4 * g) * 4;
    if (layer < 2) {
      constexpr unsigned kvn = WS_KV + MODE * KVLB;
      const int t = tid & 255;
      dm = kvn + (unsigned)((rt * NH + 2 * hp) * KVPB) + t * 16;
      dm2 = t < 16 ? dm + 8192 : t >= 64 && t < 97 ? kvn + 16u * NH * KVPB + (unsigned)(hp * KVFB + rt * 528 + (t - 64) * 16) : 0x7fff0000u;
      if (!HH) dm3 = WS_HL + (unsigned)((256 + rt * 16 + hp) * 4096 + t * 16);
    }
    if constexpr (!HH) {
      plb = sb + OFF_FX + wn * 2048 + lane * 16;
      prd = sb + OFF_RD + wn * 4;
      hof = m0 + wm == 0 ? 0x7fff0000u : WS_HL + (unsigned)((layer * 128 + rt * 16 - 16 + wm * 16 + hp) * 4096 + wn * 2048 + lane * 16);
      const unsigned out = ~((0x5555u << 2 << fr) >> (4 * g));
#pragma unroll
      for (int i = 0; i < 8; ++i) {
        unsigned t;
        asm("v_bfe_i32 %0, %1, %2, 1" : "=v"(t) : "v"(out), "n"((i >> 2) * 16 + (i & 3)));
        bias[i >> 2][i & 3] = __uint_as_float(t & 0xff61b1e6u);
      }
      bias[2] = m0 + R0 == 0 ? f32x4{-3.0e38f, -3.0e38f, -3.0e38f, -3.0e38f} : bias[0];
    } else {
      const int u = wm * 2 + kg;
      pwf = sb + OFF_WP + wn * 2048 + (4 * g + q4) * 64 + p4 * 8;
      pvf = sb + OFF_VF + w * 2048 + lane * 16;
      ppk = sb + OFF_PK + w * (32 * PSTR) + fr * PSTR + g * 8;
      ppa = sb + OFF_PK + wn * (32 * PSTR) + (4 * g + q4) * PSTR + u * 32 + p4 * 8;
      pvr = sb + OFF_VF + wn * 2048 + lane * 16;
      const unsigned doff = WS_KV + (layer >> 1) * KVLB + (unsigned)((rt * NH + 2 * hp + wn) * KVPB);
      pwl = (unsigned)(((16 * (w & 3) + (lane >> 3)) * DM + hp * 64 + ((lane & 7) ^ ((lane >> 4) << 1)) * 8) * 2);
      gkv = doff + u * 1024 + lane * 16;
      gkz = fr == 0 ? doff + 4096 + u * 32 + g * 8 : 0x7fff0000u;
    }
    asm volatile("" : "+{v[48:63]}"(pa), "+{v[36:39]}"(bias[0]), "+{v[40:43]}"(bias[1]), "+{v[44:47]}"(bias[2]));
  };

  auto o_put = [&](int rb, const f32x4& a, const f32x4& b) {
    const u32x4 v = {pk2(a[0], a[1]), pk2(a[2], a[3]), pk2(b[0], b[1]), pk2(b[2], b[3])};
    __builtin_amdgcn_raw_buffer_store_b128(v, rsW, ogo + rb * 1024, 0, 16);
    lds<u32x4>(opo + rb * 1024) = v;
  };

  if (kg == 1) {

    const int lw = w & 3;
    const int kB = 16 * lw + (lane >> 3);
    const int fB = (kB >> 1) & 1;
    const int cB0 = ((((lane & 7) >> 1) ^ fB) << 1) | (lane & 1), cB1 = ((((lane & 7) >> 1) ^ (fB | 2)) << 1) | (lane & 1);
    const unsigned vB0 = (unsigned)((kB * 3072 + hp * 64 + cB0 * 8) * 2);
    const unsigned vB1 = (unsigned)(((kB + 8) * 3072 + hp * 64 + cB1 * 8) * 2) - 1024u;

    unsigned vW0, vW1, vP, vX, vY;
    u32x4 jk[2];
    u64 ko2;
    auto ld_pw = [&](int i0) {
#pragma unroll
      for (int i = i0; i < i0 + 4; ++i) gld16(pw[i], vP, wn1 + i * 8 * 3072);
    };
    auto wo_tile = [&](int t) { glds2v(wo + (size_t)knib(ko2, t) * 64 * DM, vW0, vW1, lds_addr(smem) + OFF_WO + t * 8192 + lw * 2048); };
    auto run = [&](auto fst) {
      constexpr bool FIRST = decltype(fst)::value;
      constexpr int N = 10;
      const unsigned vA = FIRST ? (unsigned)(((m0 + lw * 32 + (lane >> 3)) * DM + ((lane & 7) ^ (lane >> 3)) * 8) * 2)
                                : (unsigned)(rt * 262144 + lw * 4096 + lane * 16);
      const char* xa = FIRST ? (const char*)x0 : ws + WS_X + (layer - 1) * (SEQ * DM * 2);
      constexpr int KS = FIRST ? 128 : 16384;
      const u64 kord = FIRST ? korder0(rt) : korder(kv1(bid), hp);
      auto ktile = [&](int ks) { return knib(kord, ks); };
      auto stageA = [&](int ks, int boff) {
        const int kt = ktile(ks);
        const unsigned sA = lds_addr(smem) + boff;
        if (FIRST) glds4v(xa + kt * KS, vA, vA + 15360u, vA + 2 * 15360u, vA + 3 * 15360u, sA + lw * 4096);
        else glds4c(xa + kt * KS, vA, sA + lw * 4096);
      };
      auto stageB = [&](const u16* Wp, int ks, int boff) {
        const int kt = ktile(ks);
        const unsigned sB = lds_addr(smem) + boff + ABYTES;
        const u16* b = Wp + (size_t)kt * 64 * 3072;
        glds4v(b, vB0, vB1, vB0, vB1, sB + lw * 6144);
        glds2v(b + 2048, vB0, vB1, sB + lw * 6144 + 4096);
      };
      bf16x8 sel[3];
      unsigned cso[3], cst;
      if (FIRST) {
        stageA(1, STAGE); stageB(W, 1, STAGE);
        __builtin_amdgcn_sched_barrier(0);
        const int fl = (q4 >> 1) | ((g & 1) << 1), j = 3 * wm + (fr < 3 ? fr : 0);
#pragma unroll
        for (int i = 0; i < 3; ++i) {
          const unsigned o = fr == i ? 0x3F803F80u : 0u;
          sel[i] = __builtin_bit_cast(bf16x8, u32x4{o, o, o, o});
          cso[i] = (unsigned)(2 * STAGE + ABYTES + (g >> 1) * 6144 + (g & 1) * 1024 + q4 * 128 + p4 * 8 + (((wn * 2 + ((wm + i) & 1)) ^ fl) << 5) + ((3 * wm + i) >> 1) * 2048);
          asm volatile("" : "+v"(sel[i]), "+v"(cso[i]));
        }
        cst = fr < 3 ? WS_CPA + (unsigned)(((hp * 16 + rt) * 192 + (j >> 1) * 64 + wn * 32 + (j & 1) * 16 + 4 * g) * 4) : 0x7fff0000u;
        asm volatile("" : "+v"(cst));
        WAITVN(N);
        BAR;
        stageA(2, 2 * STAGE); stageB(W, 2, 2 * STAGE);
      }
      if (!FIRST) {
        stageB(W, 2, 2 * STAGE);
        const char* xa1 = xa + ktile(1) * KS;
        const char* xa2 = xa + ktile(2) * KS;
        unsigned sA = lds_addr(smem) + STAGE + lw * 4096;
        asm volatile("" : "+s"(xa1), "+s"(xa2), "+s"(sA));
        asm volatile("" :: "v"(vA));
        if (lw == 0) {
          const u64 m = kfirst(kord);
          poll_eq(ws, ((m >> lane) & 1) ? WS_FLAGS + (unsigned)(256 + rt * 16 + lane) * FLS : 0u, E + (unsigned)layer, m, ~0ull);
        }
        BAR;
        glds4c(xa1, vA, sA); glds4c(xa2, vA, sA + STAGE);
        __builtin_amdgcn_sched_barrier(0);
      }
      u64 fall, fal2;
      unsigned fao, fa2, vS, vC, dS, dC, dR;
      auto setup = [&]() {
        fao = (unsigned)((256 + rt * 16 + (lane & 15)) * FLS);
        fa2 = (unsigned)(((FIRST ? 512 : 256) + (lane & 15) * 16 + (FIRST ? hp : (hp - ((1 << (lane >> 4)) >> 1)) & 15)) * FLS);
        vP = (unsigned)((rt * 64 + (lane >> 3)) * 6144 + lw * 2048 + ((hp + (1 << lw)) & 15) * 128 + (lane & 7) * 16);
        vS = FIRST ? (unsigned)(WS_G0 + (m0 + lw * 32 + (lane & 31)) * 16)
                   : WS_ST + (unsigned)((layer - 1) * (SEQ * 128) + rt * 16384 + g * 1024 + lw * 256 + fr * 16);
        vC = (FIRST ? WS_CPA : WS_CSP) + (unsigned)(hp * 12288 + g * 768 + lw * 192 + (fr < 12 ? fr : 0) * 16);
        if (!FIRST) vX = HH ? (unsigned)(hp * 4096 + lw * 1024 + lane * 16) : (unsigned)((rt * 16 + hp) * 1024 + lane * 16);
        if (HH) { vY = (unsigned)(bid * 8192 + lw * 2048 + lane * 16); asm volatile("" : "+v"(vY)); }
        vW0 = (unsigned)((kB * DM + hp * 64 + ((lane & 7) ^ ((lane >> 4) << 1)) * 8) * 2); vW1 = vW0 + 8 * DM * 2 - 1024u;
        dS = OFF_RS + lw * 256 + (FIRST ? (lane & 31) * 8 : fr * 16 + (g & 1) * 8); dC = OFF_CS + lw * 192 + (fr < 12 ? fr : 0) * 16 + g * 4;
        ko2 = korder(bid & 3, hp);
        dR = OFF_R2 + lw * 128 + (FIRST ? (lane & 31) * 4 : fr * 8 + (g & 1) * 4);
        asm volatile("" : "+v"(dR));
        asm volatile("" : "+v"(vS), "+v"(vX), "+v"(vW0), "+v"(vW1), "+v"(dS));
        asm volatile("" : "+s"(ko2));
        if (FIRST || HH) asm volatile("" : "+v"(vC), "+v"(dC), "+v"(fa2));
        if (FIRST) asm volatile("" : "+v"(vP)); else asm volatile("" : "+v"(fao));
      };
      if (!FIRST) { setup(); prep(); }
      u32x4 sv[4], cv[4];
      auto ld_c = [&](u32x4& d, unsigned v, int so) { asm volatile("buffer_load_dwordx4 %0, %1, %2, %3 offen sc1" : "=v"(d) : "v"(v), "s"(rsW), "s"(so) : "memory"); };
      float tm, tr;
      unsigned tc;
      auto fin = [&]() {
        if (FIRST) {
          const unsigned tag = (unsigned)E + 1u;
          asm volatile("" : "+v"(sv[0]));
          _Pragma("unroll 1") for (unsigned spins = 0; spins < (1u << 18); ++spins) {
            if (__all(sv[0][1] == tag && sv[0][3] == tag)) break;
            __builtin_amdgcn_s_sleep(1);
            asm volatile("buffer_load_dwordx4 %0, %1, %2, 0 offen sc1\n\ts_waitcnt vmcnt(0)" : "=v"(sv[0]) : "v"(vS), "s"(rsW) : "memory");
          }
          tm = __uint_as_float(sv[0][0]); tr = __uint_as_float(sv[0][2]);
        } else {
          asm volatile("" : "+v"(sv[0]), "+v"(sv[1]), "+v"(sv[2]), "+v"(sv[3]));
          const f32x4 t = sum4(sv);
          const unsigned u = xs4(t[0], t[2], t[1], t[3]);
          auto h = __builtin_amdgcn_permlane32_swap(u, u, false, false);
          const float mean = __uint_as_float(h[0]) * (1.0f / 1024.0f);
          const float var = fmaxf(__uint_as_float(h[1]) * (1.0f / 1024.0f) - mean * mean, 0.f);
          tm = -mean; tr = __builtin_amdgcn_rsqf(var + 1e-5f);
        }
      };
      auto cfin = [&]() {
        if (FIRST || HH) {
          asm volatile("" : "+v"(cv[0]), "+v"(cv[1]), "+v"(cv[2]), "+v"(cv[3]));
          const f32x4 t = sum4(cv);
          tc = xs4(t[0], t[1], t[2], t[3]);
        }
      };
      bf16x8 cf[6];
      f32x4 csum = {0, 0, 0, 0};
      if (FIRST) { fall = E + 1u; vX = (unsigned)(bid * 8192 + lw * 2048 + lane * 16); asm volatile("" : "+v"(fall), "+v"(vX)); }
#pragma unroll
      for (int kt = 0; kt < 13; ++kt) {
        const int cur = (kt % 3) * STAGE;
        if (!FIRST && kt == 0) { WAITV(4); }
        else if (FIRST && kt == 4 && lw == 1) { WAITVN(N + 2); }
        else if (HH && (kt == 4 || kt == 6) && true) { WAITVN(N + 1); }
        else if (HH && kt == 5 && true) { WAITVN(N + 2); }
        else if ((!FIRST && kt == 1 && lw == 3) || ((FIRST || HH) && kt == 10 && lw == 3) || (FIRST && (kt == 2 || kt == 4 || kt == 5))) { WAITVN(N + 1); }
        else { WAITVN(N); }
        if (!FIRST && kt == 2 && lw == 3) {
          asm volatile("" : "+v"(fall));
          if (__any(fall != E + (unsigned)layer)) poll_flags(ws, E, 1, rt, hp, lane, layer, 0xffffull);
        }
        if (HH && kt == 11 && lw == 3) {
          asm volatile("" : "+v"(fal2));
          if (__any(fal2 != E + (unsigned)layer)) {
            _Pragma("unroll 1") for (int j = 0; j < 4; ++j) poll_flags(ws, E, 1, rt, (hp - ((1 << j) >> 1)) & 15, lane, layer, 0xffff00000000ull);
          }
        }
        if (FIRST && kt == 11 && lw == 3) {
          asm volatile("" : "+v"(fal2));
          if (__any(fal2 != E + 1u)) poll_flags(ws, E, 2, rt, hp, lane, 1u, 0xffff00000000ull);
        }
        BAR;
        if (FIRST && kt == 1) {
          asm volatile("" ::: "memory");
#pragma unroll
          for (int i = 0; i < 6; ++i) { const char* t = smem + cso[i >> 1] + (i & 1) * 12288; cf[i] = cat8(tr_read(t), tr_read(t + 512)); }
        }
        if (FIRST && kt >= 2 && kt < 6 && lw == kt - 2) setup();
        if (FIRST && kt >= 6 && kt < 10 && lw == kt - 6) prep();
        stageA(kt + 3, cur);
        if (FIRST && kt == 1) {
          asm volatile("" : "+v"(sel[0]));
#pragma unroll
          for (int i = 0; i < 6; ++i) csum = MFMA(cf[i], sel[i >> 1], csum);
          asm volatile("" : "+v"(csum));
        }
        stageB(W, kt + 3, cur);
        if (FIRST) {
          if (kt == 1) __builtin_amdgcn_raw_buffer_store_b128(__builtin_bit_cast(u32x4, csum), rsW, cst, 0, 16);
          if (kt == 3 && tid == 320) __builtin_amdgcn_raw_buffer_store_b64(__builtin_bit_cast(u32x2, fall), rsW, WS_FLAGS + (unsigned)(512 + rt * 16 + hp) * FLS, 0, 16);
          if (kt == 3 || kt == 4) glds16s(wo + (kt - 3) * 512, vX, lds_addr(smem) + OFF_JK);
        }
        if (HH && (kt == 3 || kt == 4) && true) glds16s(wo + (kt - 3) * 512, vY, lds_addr(smem) + OFF_JK);
        if (!FIRST && kt == 0 && lw == 3) asm volatile("global_load_dwordx2 %0, %1, %2 sc1" : "=v"(fall) : "v"(fao), "s"(ws + WS_FLAGS) : "memory");
        if ((FIRST || HH) && kt == 9 && lw == 3) asm volatile("global_load_dwordx2 %0, %1, %2 sc1" : "=v"(fal2) : "v"(fa2), "s"(ws + WS_FLAGS) : "memory");
      }
      WAITVN(N);
      BAR;
#pragma unroll
      for (int i = 0; i < (FIRST ? 1 : 4); ++i) ld_c(sv[i], vS, i * 4096);
      if (FIRST || HH) {
#pragma unroll
        for (int i = 0; i < 4; ++i) ld_c(cv[i], vC, i * 3072);
      }
      if (HH) glds16s(wphi, vX, lds_addr(smem) + OFF_WP + lw * 1024);
      else if (!FIRST) glds16c(ws + WS_CSA, vX, lds_addr(smem) + OFF_CS);
      wo_tile(0); wo_tile(1);
      WAITVN(HH ? 13 : 9);
      BAR;
      if (FIRST) {
        if (lw < 3) { ld_pw(0); ld_pw(4); WAITV(12); } else { WAITV(4); }
      } else {
        WAITV(4);
      }
      cfin();
      fin();
      asm volatile("" : "+v"(tm), "+v"(tr));
      if (FIRST || HH) asm volatile("" : "+v"(tc));
      if (FIRST && lw < 3) { WAITV(8); } else { WAITV(0); }
      lds<f32x2>(sb + dS) = f32x2{tm, tr};
      lds<float>(sb + dR) = tm; lds<float>(sb + dR + 512) = tr;
      if (FIRST || HH) lds<unsigned>(sb + dC) = tc;
      if (!HH) lds<unsigned>(prd) = 0u;
      WAITL0;
    };
    typedef std::integral_constant<bool, true> T_;
    typedef std::integral_constant<bool, false> F_;
    __builtin_amdgcn_s_setprio(3);
    if (first) run(T_{}); else run(F_{});
    __builtin_amdgcn_s_setprio(0);
    BAR;
    if constexpr (HH) {
      if (layer == 1) {
        const u32x4 pz = empty4();
        __builtin_amdgcn_raw_buffer_store_b128(pz, rsW, dm, 0, 16);
        __builtin_amdgcn_raw_buffer_store_b128(pz, rsW, dm, 4096, 16);
        __builtin_amdgcn_raw_buffer_store_b128(pz, rsW, dm2, 0, 16);
      }
#pragma unroll
      for (int t = 2; t < 6; ++t) wo_tile(t);
      asm volatile("" : "=v"(csq[0]), "=v"(csq[1]), "=v"(st01), "=v"(st23), "=v"(wf[0]), "=v"(wf[1]));
      asm volatile("" : "=v"(acc[0][0]), "=v"(acc[0][1]), "=v"(acc[1][0]), "=v"(acc[1][1]));
      asm volatile("" : "=v"(qh[0]), "=v"(qh[1]), "=v"(qh[2]), "=v"(qh[3]));
    } else {
      const int lane = lane_id();
      if (WSLEEP) __builtin_amdgcn_s_sleep(WSLEEP);
#pragma unroll
      for (int t = 2; t < 12; ++t) {
        wo_tile(t);
        if (t == 2 && first) {
        const u32x4 pz = empty4();
        __builtin_amdgcn_raw_buffer_store_b128(pz, rsW, dm, 0, 16);
        __builtin_amdgcn_raw_buffer_store_b128(pz, rsW, dm, 4096, 16);
        __builtin_amdgcn_raw_buffer_store_b128(pz, rsW, dm3, 0, 16);
        __builtin_amdgcn_raw_buffer_store_b128(pz, rsW, dm2, 0, 16);
        if (lw == 3 && lane < 48) __builtin_amdgcn_raw_buffer_store_b128(lds<u32x4>(sb + OFF_CS + lane * 16), rsW, WS_CSA + (unsigned)((rt * 16 + hp) * 1024 + lane * 16), 0, 16);
        }
      }
      BAR;
      if (first) { WAITV(0); }
    }
    asm volatile("" : "={v[64:79]}"(xq));
  } else {

    prep();
    if (first) {
      const int row = m0 + hp * 8 + w * 2 + (lane >> 5);
      asm volatile("s_load_dwordx2 %0, %1, 0x0\n\ts_waitcnt lgkmcnt(0)" : "=s"(E) : "s"(ws) : "memory");
      const unsigned tag = (unsigned)E + 1u;
      static_assert(HH || (OFF_SP == 122880 && OFF_R2 + 1024 <= OFF_JK), "addresses used at the top of the kernel");
      WAITV(0);
      u32x4 xs[4];
#pragma unroll
      for (int i = 0; i < 4; ++i) xs[i] = *(const u32x4*)(smem + OFF_SP + w * 4096 + (lane >> 5) * 2048 + (lane & 31) * 16 + i * 512);
      typedef __attribute__((ext_vector_type(2))) __bf16 bf2;
      const bf2 one2 = __builtin_bit_cast(bf2, 0x3F803F80u);
      float sa[2] = {0.f, 0.f}, sq[2] = {0.f, 0.f};
#pragma unroll
      for (int i = 0; i < 16; ++i) {
        const unsigned u = xs[i >> 2][i & 3];
        const bf2 t = __builtin_bit_cast(bf2, u);
        sa[i & 1] = __builtin_amdgcn_fdot2_f32_bf16(t, one2, sa[i & 1], false);
        sq[i & 1] = __builtin_amdgcn_fdot2_f32_bf16(t, t, sq[i & 1], false);
      }
      float sm = r16_sum(sa[0] + sa[1]), m2 = r16_sum(sq[0] + sq[1]);
      {
        auto r = __builtin_amdgcn_permlane16_swap(__float_as_uint(sm), __float_as_uint(sm), false, false);
        auto q = __builtin_amdgcn_permlane16_swap(__float_as_uint(m2), __float_as_uint(m2), false, false);
        sm = __uint_as_float(r[0]) + __uint_as_float(r[1]); m2 = __uint_as_float(q[0]) + __uint_as_float(q[1]);
      }
      const float mean = sm * (1.0f / 1024.0f);
      m2 = fmaxf(m2 - sm * mean, 0.f);
      u32x4 gw = {__float_as_uint(-mean), tag, __float_as_uint(rsqrtf(m2 * (1.0f / 1024.0f) + 1e-5f)), tag};
      if ((lane & 31) == 0) __builtin_amdgcn_raw_buffer_store_b128(gw, rsW, (unsigned)(WS_G0 + row * 16), 0, 16);
      __builtin_amdgcn_raw_buffer_store_b128(empty4(), rsW, WS_HL + (unsigned)((rt * 16 + hp) * 4096 + tid * 16), 0, 16);
    }
    const int fl = (q4 >> 1) | ((g & 1) << 1);
    const unsigned bo = lds_addr(smem) + ABYTES + (g >> 1) * 6144 + (g & 1) * 1024 + q4 * 128 + p4 * 8;
    const unsigned bo0 = bo + (((wn * 2 + 0) ^ fl) << 5), bo1 = bo + (((wn * 2 + 1) ^ fl) << 5);
    const unsigned ar = lds_addr(smem) + (wm * 64 + fr) * 128;
    const unsigned aK0 = ar + ((g ^ (fr & 7)) << 4), aK1 = ar + (((4 + g) ^ (fr & 7)) << 4);
    f32x32 ac0, ac1, ac2;
    asm volatile("" :: "v"(aK0), "v"(aK1), "v"(bo0), "v"(bo1));
    if (first) BAR;
    else publish(rsW, tid, 256 + rt * 16 + hp, E + (unsigned)layer);
#define G1_OUT
#define G1_T(a)
#define G1_TEND
#define G1_TCL
    asm volatile(G1_MACROS
        "s_mov_b32 vcc_hi, %[lay]\n"
        ".if %[hh]\n s_branch .Lg1m\n .else\n"
        ".p2align 3\n .Lg1m:\n"
        "G1RDB 0, v16, v17, 0\n G1RDA v12, v13, 0\n G1RDB 1, v16, v17, 0\n"
        "ds_read_b128 v[64:67], v20\n ds_read_b128 v[68:71], v20 offset:2048\n"
        "ds_read_b128 v[72:75], v20 offset:4096\n ds_read_b128 v[76:79], v20 offset:6144\n"
        "s_cmp_eq_u32 vcc_hi, 0\n s_waitcnt lgkmcnt(12)\n"
        "G1SG 0, 1, 1, v12, v13, v16, v17, 0\n"
        "s_mov_b32 vcc_lo, 16\n"
        "G1SG 1, 1, 1, v12, v13, v16, v17, 40960\n"
        "G1SG 2, 1, 2, v12, v13, v16, v17, 40960\n"
        "s_cbranch_scc1 .Lg1a%=\n"
        "s_barrier\n"
        G1_T(88)
        "s_barrier\n"
        G1_T(92)
        "G1RDA v12, v13, 40960\n"
        "s_waitcnt lgkmcnt(4)\n"
        "s_branch .Lg1b%=\n"
        ".Lg1a%=:\n"
        G1_T(88)
        "s_barrier\n"
        G1_T(92)
        "G1RDB 0, v16, v17, 40960\n G1RDA v12, v13, 40960\n G1RDB 1, v16, v17, 40960\n"
        ".p2align 3\n s_nop 0\n"
        ".Lg1b%=:\n"
        "s_waitcnt lgkmcnt(8)\n"
        "G1SG 0, 0, 1, v12, v13, v16, v17, 40960\n"
        "s_barrier\n"
        "G1SG 1, 0, 1, v14, v15, v18, v19, 0\n"
        "G1SG 2, 0, 1, v14, v15, v18, v19, 0\n"
        "s_lshr_b32 vcc_lo, vcc_lo, 1\n s_waitcnt vmcnt(0) lgkmcnt(8)\n"
        "G1SG 0, 0, 1, v14, v15, v18, v19, 0\n"
        "s_barrier\n"
        "G1SG 1, 0, 1, v12, v13, v16, v17, 0\n"
        "G1SG 2, 0, 1, v12, v13, v16, v17, 0\n"
        "s_cbranch_scc0 .Lg1c%=\n s_waitcnt lgkmcnt(8)\n"
        "G1SG 0, 0, 1, v12, v13, v16, v17, 0\n"
        "s_barrier\n"
        "G1SG 1, 0, 1, v12, v13, v16, v17, 40960\n"
        "G1SG 2, 0, 1, v12, v13, v16, v17, 40960\n"
        "s_branch .Lg1b%=\n"
        ".Lg1c%=:\n"
        "s_waitcnt lgkmcnt(8)\n"
        "G1SG 0, 0, 1, v12, v13, v16, v17, 0\n s_bitcmp1_b32 vcc_hi, 0\n"
        "G1SG 1, 0, 0, v12, v13, v16, v17, 0\n"
        G1_T(96)
        "s_cbranch_scc1 .Lg1n\n s_barrier\n"
        "G1Q 2, 2, 0, 0, 0\n ds_read2_b64 v[136:139], v49 offset0:32 offset1:48\n"
        "G1Q 2, 2, 0, 1, 0\n ds_read_b128 v[96:99], v48 offset:256\n"
        "G1Q 2, 2, 0, 2, 0\n ds_read_b128 v[100:103], v48 offset:320\n"
        "G1Q 2, 2, 0, 3, 0\n ds_read2_b32 v[144:145], v52 offset1:16\n"
        "G1Q 2, 3, 1, 0, 0\n ds_read_b128 v[104:107], v54 offset:192\n"
        "G1Q 2, 3, 1, 1, 0\n ds_read_b128 v[108:111], v54 offset:704\n"
        "G1Q 2, 3, 1, 2, 0\n ds_read2_b64 v[140:143], v49 offset1:16\n"
        "G1Q 2, 3, 1, 3, 0\n ds_read_b128 v[148:151], v48\n"
        "G1Q 2, 0, 2, 0, 0\n ds_read_b128 v[88:91], v48 offset:64\n"
        "G1Q 2, 0, 2, 1, 0\n ds_read_b128 v[128:131], v54\n"
        "G1Q 2, 0, 2, 2, 0\n ds_read_b128 v[92:95], v54 offset:512\n"
        "G1Q 2, 0, 2, 3, 0\n"
        "G1Q 2, 1, 3, 0, 0\n"
        "G1Q 2, 1, 3, 1, 0\n"
        "G1Q 2, 1, 3, 2, 0\n"
        "G1Q 2, 1, 3, 3, 0\n"
        "ds_read_b128 v[112:115], v54 offset:128\n ds_read_b128 v[116:119], v54 offset:640\n ds_read_b128 v[152:155], v54 offset:64\n ds_read_b128 v[156:159], v54 offset:576\n"
        "s_waitcnt lgkmcnt(4)\n"
        G1_SLIDE
        ".endif\n .if %[hh]\n .p2align 3\n s_nop 0\n .Lg1n:\n s_barrier\n"
        G1_HEDGE
        ".endif\n"
        G1_TEND G1_PURGE
        : "=&{v[160:191]}"(ac0), "=&{v[192:223]}"(ac1), "=&{v[224:255]}"(ac2), "=&{v[64:79]}"(xq),
          "=&{v[88:91]}"(csq[1]), "=&{v[148:151]}"(csq[0]), "=&{v[104:107]}"(wf[0]), "=&{v[108:111]}"(wf[1]), "=&{v[140:143]}"(st01), "=&{v[136:139]}"(st23),
          "+{v[48:63]}"(pa), "+{v[36:39]}"(bias[0]), "+{v[40:43]}"(bias[1]), "+{v[44:47]}"(bias[2]) G1_OUT
        : "{v12}"(aK0), "{v13}"(aK1), "{v14}"(aK0 + 2 * STAGE), "{v15}"(aK1 + 2 * STAGE), "{v16}"(bo0), "{v17}"(bo1),
          "{v18}"(bo0 + 2 * STAGE), "{v19}"(bo1 + 2 * STAGE), "{v20}"(wn ? aK1 : aK0),
          [lay] "s"(layer), [hh] "n"(MODE), [rs] "s"(rsW), [wm] "s"(wm)
        : "memory", "scc", "vcc", "m0", G1_TCL G1_CLOBBER);
    acc[0][0] = quad<0>(ac0); acc[0][1] = quad<1>(ac0); acc[1][0] = quad<6>(ac0); acc[1][1] = quad<7>(ac0);
    qh[0] = quad<4>(ac1); qh[1] = quad<5>(ac1); qh[2] = quad<2>(ac2); qh[3] = quad<3>(ac2);
  }
  ST(layer * 24 + 4)
  {
  const int lane = lane_id(), tid = w * 64 + lane, fr = lane & 15, g = lane >> 4;
  (void)fr; (void)g;
  if constexpr (HH) {
    f32x2 st2[2];
    const f32x4 z4 = {0, 0, 0, 0};
    constexpr float L2E = 1.4426950408889634f;
    const unsigned kvo = WS_KV + (layer >> 1) * KVLB, kfo = kvo + 16u * NH * KVPB + hp * KVFB;
    bf16x8 qp[2][2];
    auto phi2 = [&](const f32x4 (&acc)[2][2], auto qktag) {
      constexpr int QK = decltype(qktag)::value & 1;
      constexpr int RB0 = (decltype(qktag)::value >> 1) & 1, RB1 = (decltype(qktag)::value & 4) ? RB0 + 1 : 2;
      float pv[2][8];
      f32x4 pm[2][2];
      f32x2 f[2][4];
#pragma unroll
      for (int rb = RB0; rb < RB1; ++rb) {
        asm volatile("" : "+v"(st2[rb]));
        const f32x2 nm = {st2[rb][0], st2[rb][0]};
#pragma unroll
        for (int i = 0; i < 4; ++i) {
          const f32x4& a = acc[rb][i >> 1];
          const f32x4& cs = csq[i >> 1];
          f[rb][i] = __builtin_elementwise_fma(f32x2{cs[(i & 1) * 2], cs[(i & 1) * 2 + 1]}, nm, f32x2{a[(i & 1) * 2], a[(i & 1) * 2 + 1]});
        }
      }
#pragma unroll
      for (int rb = RB0; rb < RB1; ++rb) asm volatile("" : "+v"(f[rb][0]), "+v"(f[rb][1]), "+v"(f[rb][2]), "+v"(f[rb][3]));
#pragma unroll
      for (int rb = RB0; rb < RB1; ++rb) {
        const bf16x8 xf = __builtin_bit_cast(bf16x8, u32x4{pk2(f[rb][0][0], f[rb][0][1]), pk2(f[rb][1][0], f[rb][1][1]), pk2(f[rb][2][0], f[rb][2][1]), pk2(f[rb][3][0], f[rb][3][1])});
        pm[rb][0] = MFMA(wf[0], xf, z4); pm[rb][1] = MFMA(wf[1], xf, z4);
      }
      asm volatile("s_nop 7" : "+v"(pm[0][0]), "+v"(pm[0][1]), "+v"(pm[1][0]), "+v"(pm[1][1]));
#pragma unroll
      for (int rb = RB0; rb < RB1; ++rb)
#pragma unroll
        for (int i = 0; i < 4; ++i) { pv[rb][i] = pm[rb][0][i]; pv[rb][4 + i] = pm[rb][1][i]; }
      float mx[2], mn[2];
#pragma unroll
      for (int u = RB0; u < RB1; ++u) max8pn(pm[u][0], pm[u][1], mx[u], mn[u]);
      if (RB1 - RB0 == 2) xr4<true>(mx[0], mn[0], mx[1], mn[1]); else xr2<true>(mx[RB0], mn[RB0]);
      float ep[2][8], en[2][8], sp[2], sn[2];
#pragma unroll
      for (int u = RB0; u < RB1; ++u) {
        const float c = st2[u][1] * L2E, mxs = -mx[u] * c, mns = -mn[u] * c;
#pragma unroll
        for (int i = 0; i < 4; ++i) {
          const f32x2 p2 = {pv[u][2 * i], pv[u][2 * i + 1]};
          const f32x2 tp = __builtin_elementwise_fma(p2, f32x2{c, c}, f32x2{mxs, mxs});
          const f32x2 tn = __builtin_elementwise_fma(-p2, f32x2{c, c}, f32x2{mns, mns});
          ep[u][2 * i] = __builtin_amdgcn_exp2f(tp[0]); ep[u][2 * i + 1] = __builtin_amdgcn_exp2f(tp[1]);
          en[u][2 * i] = __builtin_amdgcn_exp2f(tn[0]); en[u][2 * i + 1] = __builtin_amdgcn_exp2f(tn[1]);
        }
        const f32x2 a2 = (f32x2{ep[u][0], ep[u][1]} + f32x2{ep[u][2], ep[u][3]}) + (f32x2{ep[u][4], ep[u][5]} + f32x2{ep[u][6], ep[u][7]});
        const f32x2 b2 = (f32x2{en[u][0], en[u][1]} + f32x2{en[u][2], en[u][3]}) + (f32x2{en[u][4], en[u][5]} + f32x2{en[u][6], en[u][7]});
        sp[u] = a2[0] + a2[1]; sn[u] = b2[0] + b2[1];
      }
      if (RB1 - RB0 == 2) xr4<false>(sp[0], sn[0], sp[1], sn[1]); else xr2<false>(sp[RB0], sn[RB0]);
#pragma unroll
      for (int rb = RB0; rb < RB1; ++rb) {
        float ip = __builtin_amdgcn_rcpf(sp[rb]), in = __builtin_amdgcn_rcpf(sn[rb]);
        u32x2 o0, o1, o2, o3;
        o0[0] = pk2(ep[rb][0] * ip, ep[rb][1] * ip); o0[1] = pk2(ep[rb][2] * ip, ep[rb][3] * ip);
        o1[0] = pk2(ep[rb][4] * ip, ep[rb][5] * ip); o1[1] = pk2(ep[rb][6] * ip, ep[rb][7] * ip);
        o2[0] = pk2(en[rb][0] * in, en[rb][1] * in); o2[1] = pk2(en[rb][2] * in, en[rb][3] * in);
        o3[0] = pk2(en[rb][4] * in, en[rb][5] * in); o3[1] = pk2(en[rb][6] * in, en[rb][7] * in);
        if (QK == 0) {
          qp[rb][0] = __builtin_bit_cast(bf16x8, (u32x4){o0[0], o0[1], o1[0], o1[1]});
          qp[rb][1] = __builtin_bit_cast(bf16x8, (u32x4){o2[0], o2[1], o3[0], o3[1]});
          asm volatile("" : "+v"(qp[rb][0]), "+v"(qp[rb][1]));
        } else {
          const unsigned dst = ppk + rb * 16 * PSTR;
          lds<u32x2>(dst) = o0; lds<u32x2>(dst + 32) = o1; lds<u32x2>(dst + 64) = o2; lds<u32x2>(dst + 96) = o3;
        }
      }
    };
    pwm = pwl + 8 * DM * 2 - 1024u;
    asm volatile("" : "+v"(pa));
    bf16x8 one8 = ones8();
    asm volatile("" : "+v"(one8));
    WAITL0; BAR;
    ST(layer * 24 + 5)
    {
      bf16x8 af[4], v0[4], v1[4];
      asm volatile("" ::: "memory");
#pragma unroll
      for (int sw = 0; sw < 4; ++sw) {
        af[sw] = cat8(tr_read(ppa + sw * (64 * PSTR)), tr_read(ppa + sw * (64 * PSTR) + 16 * PSTR));
        v0[sw] = lds<bf16x8>(pvr + sw * 4096);
        v1[sw] = lds<bf16x8>(pvr + sw * 4096 + 1024);
      }
      __builtin_amdgcn_sched_barrier(0);
      f32x4 kv[3] = {z4, z4, z4};
#pragma unroll
      for (int sw = 0; sw < 4; ++sw) {
        kv[0] = MFMA(af[sw], v0[sw], kv[0]);
        kv[1] = MFMA(af[sw], v1[sw], kv[1]);
        kv[2] = MFMA(af[sw], one8, kv[2]);
      }
      auto pc = [](float x, float y) { const unsigned t = pk2(x, y); return t < EMPTY - 1u ? t : EMPTY - 1u; };
      __builtin_amdgcn_raw_buffer_store_b128(u32x4{pc(kv[0][0], kv[0][1]), pc(kv[0][2], kv[0][3]), pc(kv[1][0], kv[1][1]), pc(kv[1][2], kv[1][3])}, rsW, gkv, 0, 16);
      __builtin_amdgcn_raw_buffer_store_b64(u32x2{pc(kv[2][0], kv[2][1]), pc(kv[2][2], kv[2][3])}, rsW, gkz, 0, 16);
    }
    u64 ko = korder(bid & 3, hp);
    auto wo_late = [&](int t) { glds2v(wo + (size_t)knib(ko, t) * 64 * DM, pwl, pwm, sb + OFF_WO + t * 8192 + (w & 3) * 2048); };
    ST(layer * 24 + 6)
    u32x4 l1[8];
    const bool red = w == 7 || (w == 6 && (rt & 7) == 0);
    unsigned lo = kvo + (unsigned)((2 * hp + (rt >> 3) + (lane >> 5) * 8 * NH) * KVPB) +
                  (w == 7 ? (unsigned)((rt & 4) * 512 + (g & 1) * 1024 + (rt & 3) * 256 + fr * 16) : (unsigned)(4096 + (lane & 7) * 16));
    unsigned fs = w == 7 ? kfo + (unsigned)((rt >> 2) * 2048 + (lane >> 5) * 1024 + (rt & 3) * 256 + fr * 16 + (g & 1) * 8)
                : (lane & 24) ? 0x7fff0000u : kfo + (unsigned)(8192 + (rt >> 3) * 128 + (lane & 4) * 16 + (lane & 1) * 32 + (lane >> 5) * 16 + (lane & 2) * 4);
    auto l1_load = [&]() {
      asm volatile("" ::: "memory");
#pragma unroll
      for (int p = 0; p < 8; ++p) asm volatile("buffer_load_dwordx4 %0, %1, %2, %3 offen sc1" : "=v"(l1[p]) : "v"(lo), "s"(rsW), "s"(p * (NH * KVPB)) : "memory");
      __builtin_amdgcn_sched_barrier(0);
    };
    if (red) asm volatile("" : "+v"(lo), "+v"(fs));
    if (red) { __builtin_amdgcn_s_sleep(RSLEEP); l1_load(); }
    const unsigned pxq = sb + XS + lane * 16;
    if (!kg) {
      const f32x4 qa[2][2] = {{qh[0], qh[1]}, {qh[2], qh[3]}};
      st2[0] = f32x2{st23[0], st23[1]}; st2[1] = f32x2{st23[2], st23[3]};
      phi2(qa, IC<0>{});
#pragma unroll
      for (int i = 0; i < 4; ++i) lds<bf16x8>(pxq + (i >> 1) * 8192 + (i & 1) * 1024) = qp[i >> 1][i & 1];
    } else if (red) {
      typedef __attribute__((ext_vector_type(2))) __bf16 bf2;
      unsigned c0 = 0x00003F80u, c1 = 0x3F800000u;
      asm volatile("" : "+v"(c0), "+v"(c1));
      float s4[4];
      _Pragma("unroll 1") for (unsigned spins = 0; spins < (1u << 18); ++spins) {
        float a[8] = {0.f, 0.f, 0.f, 0.f, 0.f, 0.f, 0.f, 0.f};
        ARRIVED("+v"(l1[0]), "+v"(l1[1]), "+v"(l1[2]), "+v"(l1[3]), "+v"(l1[4]), "+v"(l1[5]), "+v"(l1[6]), "+v"(l1[7]));
#pragma unroll
        for (int p = 0; p < 8; ++p)
#pragma unroll
          for (int e = 0; e < 4; ++e) {
            const unsigned u = l1[p][e];
            a[2 * e] = __builtin_amdgcn_fdot2_f32_bf16(__builtin_bit_cast(bf2, u), __builtin_bit_cast(bf2, c0), a[2 * e], false);
            a[2 * e + 1] = __builtin_amdgcn_fdot2_f32_bf16(__builtin_bit_cast(bf2, u), __builtin_bit_cast(bf2, c1), a[2 * e + 1], false);
          }
#pragma unroll
        for (int i = 0; i < 4; ++i) {
          auto r = __builtin_amdgcn_permlane32_swap(__float_as_uint(a[i]), __float_as_uint(a[i + 4]), false, false);
          s4[i] = __uint_as_float(r[0]) + __uint_as_float(r[1]);
        }
        const float t = s4[0] + s4[2];
        if (__all(t == t)) break;
        unsigned mxw = 0;
#pragma unroll
        for (int p = 0; p < 8; ++p)
#pragma unroll
          for (int e = 0; e < 4; ++e) mxw = l1[p][e] > mxw ? l1[p][e] : mxw;
        if (__all(mxw != EMPTY)) break;
        l1_load();
      }
      WAITV(0);
      unsigned o0 = pk2(s4[0], s4[1]), o1 = pk2(s4[2], s4[3]);
      o0 = o0 < EMPTY - 1u ? o0 : EMPTY - 1u; o1 = o1 < EMPTY - 1u ? o1 : EMPTY - 1u;
      __builtin_amdgcn_raw_buffer_store_b64(u32x2{o0, o1}, rsW, fs, 0, 16);
    }
    asm volatile("" : "+s"(ko));
    ST(layer * 24 + 8)
    WAITL0;
    BAR;
    ST(layer * 24 + 9)
    {
      unsigned pfo = OFF_KV + wn * 4096 + lane * 16, pzo = OFF_KV + 8192 + wn * 128 + g * 16;
      asm volatile("" : "+v"(pfo), "+v"(pzo));
      if (kg) {
        asm volatile("" ::: "memory");
#pragma unroll
        for (int i = 0; i < 4; ++i) qp[i >> 1][i & 1] = lds<bf16x8>(pxq + (i >> 1) * 8192 + (i & 1) * 1024);
        asm volatile("s_waitcnt lgkmcnt(0)" : "+v"(qp[0][0]), "+v"(qp[0][1]), "+v"(qp[1][0]), "+v"(qp[1][1]) :: "memory");
#pragma unroll
        for (int i = LATE0; i < 12; ++i) wo_late(i);
      } else {
        u32x4 fa, fc, fb = {0u, 0u, 0u, 0u};
        const unsigned fo = kfo + (unsigned)(tid * 16), fo2 = kfo + (unsigned)(8192 + (lane & 15) * 16);
        st2[0] = f32x2{st01[0], st01[1]}; st2[1] = f32x2{st01[2], st01[3]};
        phi2(acc, IC<0>{});
        if (FSLEEP) __builtin_amdgcn_s_sleep(FSLEEP);
        _Pragma("unroll 1") for (unsigned spins = 0; spins < (1u << 18); ++spins) {
          asm volatile("" ::: "memory");
          fa = __builtin_amdgcn_raw_buffer_load_b128(rsW, fo, 0, 16);
          fc = __builtin_amdgcn_raw_buffer_load_b128(rsW, fo, 4096, 16);
          if (w == 0) fb = __builtin_amdgcn_raw_buffer_load_b128(rsW, fo2, 0, 16);
          unsigned mxw = 0;
#pragma unroll
          for (int e = 0; e < 4; ++e) { mxw = fa[e] > mxw ? fa[e] : mxw; mxw = fb[e] > mxw ? fb[e] : mxw; mxw = fc[e] > mxw ? fc[e] : mxw; }
          if (__all(mxw != EMPTY)) break;
          __builtin_amdgcn_s_sleep(1);
        }
        *(u32x4*)(smem + OFF_KV + tid * 16) = fa;
        *(u32x4*)(smem + OFF_KV + 4096 + tid * 16) = fc;
        if (tid < 16) *(u32x4*)(smem + OFF_KV + 8192 + tid * 16) = fb;
      }
      ST(layer * 24 + 10)
      WAITL0; BAR;
      ST(layer * 24 + 11)
      bf16x8 kvf[3][2];
#pragma unroll
      for (int ks = 0; ks < 2; ++ks) {
        kvf[0][ks] = *(const bf16x8*)(smem + pfo + ks * 2048);
        kvf[1][ks] = *(const bf16x8*)(smem + pfo + ks * 2048 + 1024);
        kvf[2][ks] = *(const bf16x8*)(smem + pzo + ks * 64);
      }
#pragma unroll
      for (int rb = 0; rb < 2; ++rb) {
        f32x4 oa[3];
#pragma unroll
        for (int nb = 2; nb >= 0; --nb) {
          oa[nb] = MFMA(kvf[nb][0], qp[rb][0], z4);
          oa[nb] = MFMA(kvf[nb][1], qp[rb][1], oa[nb]);
        }
        float den = oa[2][0];
        den += 1e-6f;
        float inv = __builtin_amdgcn_rcpf(den);
        o_put(rb, oa[0] * inv, oa[1] * inv);
      }
    }
  }
  ST(layer * 24 + 12)
  if (HH) if (w >= 4) { WAITV(0); WAITL0; BAR; }
  if (first && w >= 4 && w < 7) {
    typedef __attribute__((ext_vector_type(2))) __bf16 bf2;
    unsigned c0 = 0x00003F80u, c1 = 0x3F800000u;
    asm volatile("" : "+v"(c0), "+v"(c1));
    const bf2 lo1 = __builtin_bit_cast(bf2, c0), hi1 = __builtin_bit_cast(bf2, c1);
    float a[8] = {0.f, 0.f, 0.f, 0.f, 0.f, 0.f, 0.f, 0.f};
#pragma unroll
    for (int i = 0; i < 8; ++i) {
      asm volatile("" : "+v"(pw[i]));
#pragma unroll
      for (int e = 0; e < 4; ++e) {
        const unsigned u = pw[i][e];
        a[2 * e] = __builtin_amdgcn_fdot2_f32_bf16(__builtin_bit_cast(bf2, u), lo1, a[2 * e], false);
        a[2 * e + 1] = __builtin_amdgcn_fdot2_f32_bf16(__builtin_bit_cast(bf2, u), hi1, a[2 * e + 1], false);
      }
    }
#pragma unroll
    for (int i = 0; i < 4; ++i) {
      auto r = __builtin_amdgcn_permlane32_swap(__float_as_uint(a[i]), __float_as_uint(a[i + 4]), false, false);
      a[i] = __uint_as_float(r[0]) + __uint_as_float(r[1]);
    }
#pragma unroll
    for (int i = 0; i < 2; ++i) {
      auto r = __builtin_amdgcn_permlane16_swap(__float_as_uint(a[i]), __float_as_uint(a[i + 2]), false, false);
      a[i] = __uint_as_float(r[0]) + __uint_as_float(r[1]);
      a[i] += dpp_ror<8>(a[i]);
    }
    if (!(lane & 8))
      __builtin_amdgcn_raw_buffer_store_b64(u32x2{__float_as_uint(a[0]), __float_as_uint(a[1])}, rsW,
          (unsigned)(WS_CSP + ((((hp + (1 << (w - 4))) & 15) * 16 + rt) * 192 + (w - 4) * 64 + (lane & 7) * 8 + (lane >> 5) * 4 + ((lane >> 4) & 1) * 2) * 4), 0, 16);
  }
  }
}

#undef pcs
#undef prs
#undef opo
#undef ogo
#undef pkw
#undef hof
#undef dm
#undef dm2
#undef pcv
#undef pwf
#undef prv
#undef pvf
#undef ppk
#undef ppa
#undef pvr
#undef gkv
#undef gkz
#undef pwl
#undef pwm
#undef plb
#undef prd
#undef dm3

__device__ __forceinline__ void g2_phase(char* smem, char* ws, u64 E, int w, f32x16& xq, int layer, const u16* __restrict__ x0,
                                         const u16* __restrict__ wo, const u16* __restrict__ wnx, u16* __restrict__ out) {
  IDS
  wnx = uni(wnx);
  const unsigned sb = lds_addr(smem);
  const int lw = w & 3, k2 = 16 * lw + (lane >> 3);
  const unsigned vW0 = (unsigned)((k2 * DM + hp * 64 + ((lane & 7) ^ ((lane >> 4) << 1)) * 8) * 2), vW1 = vW0 + 8 * DM * 2 - 1024u;
  const int r0 = w * 16 + (lane >> 3);
  unsigned pxr = sb + r0 * 128 + (lane & 7) * 16;
  const unsigned rmo = (unsigned)(((m0 + r0) * DM + hp * 64 + ((lane & 7) ^ (lane >> 3)) * 8) * 2);
  const unsigned imo = (unsigned)((rt * 16 + hp) * 16384 + r0 * 128 + (lane & 7) * 16);
  constexpr int S0 = 0, S1 = 16384, S2 = 32768, S3 = 147456, S4 = OFF_WO, S5 = 98304, S6 = 114688;
  constexpr int AS[16] = {S0, S1, S2, S3, S0, S1, S4, S2, S3, S0, S1, S4, S5, S3, S0, S6};
  constexpr int OFF_C = 0;
  auto bs = [](int t) { return OFF_WO + (t < 12 ? t : t - 10) * 8192; };
  if (w >= 4) {
    constexpr int WT[15] = {0, 8, 12, 14, 16, 18, 20, 18, 16, 14, 12, 12, 10, 10, 8};
    const char* o = ws + WS_O + layer * (SEQ * DM * 2) + rt * 262144;
    const unsigned vA = (unsigned)(lw * 4096 + lane * 16);
    const u64 kord = korder(bid & 3, hp);
    auto ldA = [&](int s) { glds4c(o + knib(kord, s) * 16384, vA, sb + AS[s] + lw * 4096); };
    auto ldW = [&](int t) { glds2v(wo + (size_t)knib(kord, t) * 64 * DM, vW0, vW1, sb + bs(t) + lw * 2048); };
    const int fB = (k2 >> 1) & 1;
    unsigned vB0 = (unsigned)((k2 * 3072 + hp * 64 + (((((lane & 7) >> 1) ^ fB) << 1) | (lane & 1)) * 8) * 2);
    unsigned vB1 = (unsigned)(((k2 + 8) * 3072 + hp * 64 + (((((lane & 7) >> 1) ^ (fB | 2)) << 1) | (lane & 1)) * 8) * 2) - 1024u;
    int kn1 = knib(korder(kv1(bid), hp), 1);
    asm volatile("" : "+v"(vB0), "+v"(vB1));
    asm volatile("" : "+s"(kn1));
    auto ldB = [&](int s, int part) {
      const u16* b = wnx + (size_t)(s ? kn1 : hp) * 64 * 3072;
      if (part) glds4v(b, vB0, vB1, vB0, vB1, sb + s * 40960 + 16384 + lw * 6144);
      else glds2v(b + 2048, vB0, vB1, sb + s * 40960 + 16384 + lw * 6144 + 4096);
    };
    BAR;
    ldA(1); ldA(2); ldA(3); ldA(4);
    WAITV(12);
    BAR;
    auto step = [&](auto ktc) {
      constexpr int kt = decltype(ktc)::value;
      WAITVN(WT[kt]);
      BAR;
      if constexpr (kt == 1) { ldA(5); ldA(6); }
      if constexpr (kt >= 2 && kt <= 10) ldA(kt + 5);
      if constexpr (kt >= 2 && kt <= 5) ldW(kt + 10);
      if constexpr (kt >= 11) ldB((kt - 11) >> 1, (kt - 11) & 1);
      if constexpr (kt >= 6) { __builtin_amdgcn_s_sleep(kt >= 11 ? 3 : 2); if (layer == 1) __builtin_amdgcn_s_sleep(1); }
    };
    step(IC<1>{}); step(IC<2>{}); step(IC<3>{}); step(IC<4>{}); step(IC<5>{}); step(IC<6>{}); step(IC<7>{});
    step(IC<8>{}); step(IC<9>{}); step(IC<10>{}); step(IC<11>{}); step(IC<12>{}); step(IC<13>{}); step(IC<14>{});
  } else {
    const int wm = w >> 1, wn = w & 1;
    f32x4 acc[4][2];
    unsigned co[2];
#pragma unroll
    for (int cb = 0; cb < 2; ++cb) co[cb] = sb + OFF_C + (unsigned)((wm * 64 + fr) * 128 + (((wn * 4 + cb * 2 + (g >> 1)) ^ (fr & 7)) << 4) + (g & 1) * 8);
    const int fl = (q4 >> 1) | ((g & 1) << 1);
    const int bb0 = (4 * g + q4) * 128 + p4 * 8 + (((wn * 2) ^ fl) << 5), bb1 = (4 * g + q4) * 128 + p4 * 8 + (((wn * 2 + 1) ^ fl) << 5);
    const int abase = wm * 4096 + lane * 16;
    bf16x8 fa[2][2][4], fb[2][2][2];
    unsigned ra[3], rb0[2], rb1[2];
#pragma unroll
    for (int i = 0; i < 3; ++i) { ra[i] = abase + i * 65536; asm volatile("" : "+v"(ra[i])); }
#pragma unroll
    for (int i = 0; i < 2; ++i) { rb0[i] = bb0 + OFF_WO + i * 49152; rb1[i] = bb1 + OFF_WO + i * 49152; asm volatile("" : "+v"(rb0[i]), "+v"(rb1[i])); }
    auto loadf = [&](int kt, bf16x8 (&a)[2][4], bf16x8 (&b)[2][2]) {
#pragma unroll
      for (int s = 0; s < 2; ++s) {
        const char* sA = smem + ra[AS[kt] >> 16] + (AS[kt] & 65535) + s * 8192;
        const int hi = bs(kt) >= OFF_WO + 49152, ob = bs(kt) - OFF_WO - hi * 49152 + s * 4096;
        b[s][0] = cat8(tr_read(smem + rb0[hi] + ob), tr_read(smem + rb0[hi] + ob + 2048));
        b[s][1] = cat8(tr_read(smem + rb1[hi] + ob), tr_read(smem + rb1[hi] + ob + 2048));
#pragma unroll
        for (int rb = 0; rb < 4; ++rb) a[s][rb] = *(const bf16x8*)(sA + rb * 1024);
      }
    };
    auto mma = [&](bf16x8 (&a)[2][4], bf16x8 (&b)[2][2]) {
#pragma unroll
      for (int i = 0; i < 16; ++i) {
        const int rb = i >> 2, cb = ((i >> 1) ^ rb) & 1, s = ((i + 1) >> 1) & 1;
        acc[rb][cb] = MFMA(b[s][cb], a[s][rb], acc[rb][cb]);
      }
    };
    unsigned fo = lane < 16 && lane != hp ? WS_FLAGS + (unsigned)(rt * 16 + lane) * FLS : 0u;
    const u64 ko = korder(bid & 3, hp);
    unsigned kf = kfirst(ko), kf2 = kfirst(ko >> 16) | (1u << knib(ko, 9));
    unsigned jo = (unsigned)((((rt & 7) * 128 + (w - 1) * 64 + lane) * 3072 + (hp & 3) * 1024 + (hp | 3) * 64) * 2);
    unsigned fo1 = ((kf >> (lane & 31)) & 1) ? fo : 0u, fo2 = ((kf2 >> (lane & 31)) & 1) ? fo : 0u;
    fo = (((kf | kf2) >> (lane & 31)) & 1) ? 0u : fo;
    asm volatile("" : "+v"(fo), "+v"(fo1), "+v"(fo2));
    asm volatile("" : "+s"(kf), "+s"(kf2));
    publish(rsW, tid, rt * 16 + hp, E + (unsigned)(layer + 1));
    ST(layer * 24 + 13)
    u64 fl1 = 0;
    if (w == 0) asm volatile("global_load_dwordx2 %0, %1, %2 sc1" : "+v"(fl1) : "v"(fo1), "s"(ws) : "memory");
    if (layer == 0) {
      const unsigned xo = (unsigned)(((m0 + wm * 64 + fr) * DM + hp * 64 + wn * 32 + g * 8) * 2);
      asm volatile("global_load_dwordx4 v[64:67], %1, %3\n\tglobal_load_dwordx4 v[68:71], %1, %4\n\tglobal_load_dwordx4 v[72:75], %2, %3\n\tglobal_load_dwordx4 v[76:79], %2, %4"
                   : "+{v[64:79]}"(xq) : "v"(xo), "v"(xo + 32 * DM * 2), "s"(x0), "s"(x0 + 16 * DM) : "memory");
    }
    loadf(0, fa[0], fb[0]);
    unsigned junk = 0;
    if (layer < 2 && w == 3 && (bid >> 6) == 0) code_touch(junk, bid, lane);
    if (layer < 3 && (hp & 7) < 3 && (w == 1 || w == 2))
      asm volatile("global_load_dword %0, %1, %2" : "+v"(junk) : "v"(jo), "s"(wnx) : "memory");
    __builtin_amdgcn_sched_barrier(0);
    bf16x8 id[2];
#pragma unroll
    for (int cb = 0; cb < 2; ++cb) {
      const unsigned one = (g >> 1) == cb && (g & 1) == (fr >> 3) ? 0x3F80u << ((fr & 1) * 16) : 0u;
      u32x4 t;
#pragma unroll
      for (int j = 0; j < 4; ++j) t[j] = ((fr >> 1) & 3) == j ? one : 0u;
      id[cb] = __builtin_bit_cast(bf16x8, t);
      asm volatile("" : "+v"(id[cb]));
    }
    asm volatile("" : "+v"(co[0]), "+v"(co[1]));
#pragma unroll
    for (int rb = 0; rb < 4; ++rb) { acc[rb][0] = f32x4{0, 0, 0, 0}; acc[rb][1] = f32x4{0, 0, 0, 0}; }
    mma(fa[0], fb[0]);
    __builtin_amdgcn_sched_barrier(0);
    WAITL0;
    u64 miss = 0;
    if (w == 0) { ARRIVED("+v"(fl1)); if (__ballot(fl1 != E + (unsigned)(layer + 1)) & kf) poll_eq(ws, fo1, E + (unsigned)(layer + 1), kf, kf); miss = 0xffffu & ~(kf | kf2 | (1u << hp)); }
    ST(layer * 24 + 14)
    __builtin_amdgcn_sched_barrier(0);
    BAR;
    __builtin_amdgcn_sched_barrier(0);
    asm volatile("" ::: "memory");
    if (layer == 0) WAITV(0);
    __builtin_amdgcn_sched_barrier(0);
    asm volatile("" : "+{v[64:79]}"(xq));
    const bf16x8 xf[4] = {__builtin_bit_cast(bf16x8, quad<0>(xq)), __builtin_bit_cast(bf16x8, quad<1>(xq)), __builtin_bit_cast(bf16x8, quad<2>(xq)), __builtin_bit_cast(bf16x8, quad<3>(xq))};
#pragma unroll
    for (int rb = 0; rb < 4; ++rb) { acc[rb][0] = MFMA(id[0], xf[rb], acc[rb][0]); acc[rb][1] = MFMA(id[1], xf[rb], acc[rb][1]); }
    if (w == 0) poll_eq(ws, fo2, E + (unsigned)(layer + 1), kf2, kf2);
    u64 fl2 = 0;
    auto step = [&](auto ktc) {
      constexpr int kt = decltype(ktc)::value;
      __builtin_amdgcn_sched_barrier(0);
      if (kt == 5 && miss) {
        ARRIVED("+v"(fl2));
        if (__ballot(fl2 != E + (unsigned)(layer + 1)) & miss) poll_eq(ws, fo, E + (unsigned)(layer + 1), miss, ~0ull);
      }
      WAITL0;
      BAR;
      if (kt == 2 && miss) asm volatile("global_load_dwordx2 %0, %1, %2 sc1" : "+v"(fl2) : "v"(fo), "s"(ws) : "memory");
      if (kt == 0) { ST(layer * 24 + 15) }
      __builtin_amdgcn_sched_barrier(0);
      loadf(kt + 1, fa[(kt + 1) & 1], fb[(kt + 1) & 1]);
      if (kt == 0) return;
      mma(fa[kt & 1], fb[kt & 1]);
#pragma unroll
      for (int i = 0; i < 4; ++i) {
        __builtin_amdgcn_sched_group_barrier(0x008, 1, 0);
        __builtin_amdgcn_sched_group_barrier(0x100, 1, 0);
        __builtin_amdgcn_sched_group_barrier(0x008, 1, 0);
        __builtin_amdgcn_sched_group_barrier(0x100, 3, 0);
      }
#pragma unroll
      for (int i = 0; i < 8; ++i) __builtin_amdgcn_sched_group_barrier(0x008, 1, 0);
    };
    step(IC<0>{}); step(IC<1>{}); step(IC<2>{}); step(IC<3>{}); step(IC<4>{}); step(IC<5>{}); step(IC<6>{}); step(IC<7>{});
    step(IC<8>{}); step(IC<9>{}); step(IC<10>{}); step(IC<11>{}); step(IC<12>{}); step(IC<13>{}); step(IC<14>{});
    WAITV(0);
    asm volatile("" :: "v"(junk));
    __builtin_amdgcn_sched_barrier(0);
    auto put = [&](int rb) {
#pragma unroll
      for (int cb = 0; cb < 2; ++cb) lds<u32x2>(co[cb] + rb * 2048) = u32x2{pk2(acc[rb][cb][0], acc[rb][cb][1]), pk2(acc[rb][cb][2], acc[rb][cb][3])};
    };
#pragma unroll
    for (int rb = 0; rb < 4; ++rb)
#pragma unroll
      for (int cb = 0; cb < 2; ++cb) acc[rb][cb] = MFMA(fb[1][0][cb], fa[1][0][rb], acc[rb][cb]);
#pragma unroll
    for (int rb = 0; rb < 4; ++rb) {
      __builtin_amdgcn_sched_barrier(0);
#pragma unroll
      for (int cb = 0; cb < 2; ++cb) acc[rb][cb] = MFMA(fb[1][1][cb], fa[1][1][rb], acc[rb][cb]);
      __builtin_amdgcn_sched_barrier(0);
      if (rb >= 2) put(rb - 2);
    }
    __builtin_amdgcn_sched_barrier(0);
    ST(layer * 24 + 16)
    put(2); put(3);
  }
  WAITL0; BAR;
  asm volatile("" ::: "memory");
  u32x4 xn[2];
#pragma unroll
  for (int i = 0; i < 2; ++i) xn[i] = lds<u32x4>(pxr + OFF_C + i * 1024);
  if (layer == 3) {
    const __amdgpu_buffer_rsrc_t rsO = __builtin_amdgcn_make_buffer_rsrc((void*)uni(out), (short)0, SEQ * DM * 2, 0x00020000);
    __builtin_amdgcn_raw_buffer_store_b128(xn[0], rsO, rmo, 0, 16);
    __builtin_amdgcn_raw_buffer_store_b128(xn[1], rsO, rmo + 8 * DM * 2, 0, 16);
  } else {
    __builtin_amdgcn_raw_buffer_store_b128(xn[0], rsW, WS_X + layer * (SEQ * DM * 2) + imo, 0, 16);
    __builtin_amdgcn_raw_buffer_store_b128(xn[1], rsW, WS_X + layer * (SEQ * DM * 2) + imo + 1024, 0, 16);
    float mean[2], m2[2];
#pragma unroll
    for (int i = 0; i < 2; ++i) {
      typedef __attribute__((ext_vector_type(2))) __bf16 bf2;
      const bf2 one2 = __builtin_bit_cast(bf2, 0x3F803F80u);
      float sa = 0.f, sq = 0.f;
#pragma unroll
      for (int e = 0; e < 4; ++e) {
        const unsigned u = xn[i][e];
        const bf2 t = __builtin_bit_cast(bf2, u);
        sa = __builtin_amdgcn_fdot2_f32_bf16(t, one2, sa, false);
        sq = __builtin_amdgcn_fdot2_f32_bf16(t, t, sq, false);
      }
      mean[i] = sa;
      m2[i] = sq;
    }
#define DPA(c) "v_add_f32_dpp %0, %0, %0 " c "\n\tv_add_f32_dpp %1, %1, %1 " c "\n\tv_add_f32_dpp %2, %2, %2 " c "\n\tv_add_f32_dpp %3, %3, %3 " c "\n\t"
    asm volatile("s_nop 3\n\t" DPA("quad_perm:[1,0,3,2] row_mask:0xf bank_mask:0xf") DPA("quad_perm:[2,3,0,1] row_mask:0xf bank_mask:0xf")
                 DPA("row_half_mirror row_mask:0xf bank_mask:0xf") "s_nop 0" : "+v"(mean[0]), "+v"(m2[0]), "+v"(mean[1]), "+v"(m2[1]));
    if ((lane & 7) < 2) {
      const int i = lane & 1;
      const u32x2 sv = {__float_as_uint(i ? mean[1] : mean[0]), __float_as_uint(i ? m2[1] : m2[0])};
      __builtin_amdgcn_raw_buffer_store_b64(sv, rsW, WS_ST + layer * (SEQ * 128) + (unsigned)(((rt * 16 + hp) * 128 + r0 + i * 8) * 8), 0, 16);
    }
  }
  ST(layer * 24 + 17)
  if (layer < 3 && w >= 4) { WAITV(0); BAR; }
  ST(layer * 24 + 18)
}

__global__ __launch_bounds__(512, 2) void mega_kernel(const u16* __restrict__ x0, const u16* __restrict__ wqkvA,
                                                      const u16* __restrict__ woA, const u16* __restrict__ wqkvB,
                                                      const u16* __restrict__ wphi, const u16* __restrict__ woB,
                                                      u16* __restrict__ out, char* __restrict__ ws) {
  extern __shared__ __attribute__((aligned(16))) char smem[];
  const int w = __builtin_amdgcn_readfirstlane(threadIdx.x >> 6);
  u64 E = 0;
  {
    const int bid = blockIdx.x, lane = threadIdx.x & 63;
    const int rt = 2 * (bid & 4) + ((bid >> 3) & 7), hp = 4 * (bid & 3) + (bid >> 6), m0 = rt * 128;
    if (w < 4) {
      if (w == 3) {
        unsigned long long pc;
        unsigned len;
        asm volatile("s_getpc_b64 %0\n.Lmk_pc:\n\ts_mov_b32 %1, .Lmk_end-.Lmk_pc" : "=s"(pc), "=s"(len));
        const unsigned per = ((len >> 10) + 1) * 128, off = ((bid >> 3) & 7) * per + (lane * 128 < per ? lane * 128 : 0);
        asm volatile("s_mov_b32 m0, %2\n\ts_nop 0\n\tglobal_load_lds_dword %0, %1" :: "v"(off < len - 4 ? off : 0u), "s"(pc), "s"(lds_addr(smem) + LDS_TOTAL - 1024) : "memory", "m0");
      }
      const unsigned xo = (unsigned)((m0 + hp * 8 + w * 2) * (DM * 2) + lane * 16);
      glds4v(x0, xo, xo, xo, xo, lds_addr(smem) + 122880 + w * 4096);
    } else {
      const int lw = w & 3;
      const int kB = 16 * lw + (lane >> 3), fB = (kB >> 1) & 1;
      const unsigned vB0 = (unsigned)((kB * 3072 + hp * 64 + (((((lane & 7) >> 1) ^ fB) << 1) | (lane & 1)) * 8) * 2);
      const unsigned vB1 = (unsigned)(((kB + 8) * 3072 + hp * 64 + (((((lane & 7) >> 1) ^ (fB | 2)) << 1) | (lane & 1)) * 8) * 2) - 1024u;
      const unsigned vA = (unsigned)(((m0 + lw * 32 + (lane >> 3)) * DM + ((lane & 7) ^ (lane >> 3)) * 8) * 2);
#pragma unroll
      for (int s = 0; s < 1; ++s) {
        const int kt = (rt & 8) + s;
        const unsigned sA = lds_addr(smem) + s * 40960, sB = sA + 16384;
        glds4v(x0 + kt * 64, vA, vA + 15360u, vA + 2 * 15360u, vA + 3 * 15360u, sA + lw * 4096);
        const u16* b = wqkvA + (size_t)kt * 64 * 3072;
        glds4v(b, vB0, vB1, vB0, vB1, sB + lw * 6144);
        glds2v(b + 2048, vB0, vB1, sB + lw * 6144 + 4096);
      }
      asm volatile("s_load_dwordx2 %0, %1, 0x0\n\ts_waitcnt lgkmcnt(0)" : "=s"(E) : "s"(ws) : "memory");
    }
  }
  f32x16 xq;
#pragma unroll 1
  for (int layer = 0; layer < 4; ++layer) {
    if (layer & 1) g1_phase<1>(smem, ws, E, w, xq, layer, x0, wqkvB, woB, wphi, nullptr, nullptr);
    else g1_phase<0>(smem, ws, E, w, xq, layer, x0, wqkvA, woA, nullptr, wqkvB, woB);
    g2_phase(smem, ws, E, w, xq, layer, x0, (layer & 1) ? woB : woA, (layer & 1) ? wqkvA : wqkvB, out);
  }
  asm volatile(".Lmk_end:");
  WAITV(0);
  if (blockIdx.x == 0 && w == 0 && lane_id() == 0) __hip_atomic_store((gu64*)ws, E + 16u, __ATOMIC_RELAXED, __HIP_MEMORY_SCOPE_AGENT);
}

extern "C" void kernel_launch(void* const* d_in, const int* in_sizes, int n_in,
                              void* d_out, int out_size, void* d_ws, size_t ws_size,
                              hipStream_t stream) {
  hipFuncSetAttribute((const void*)mega_kernel, hipFuncAttributeMaxDynamicSharedMemorySize, LDS_TOTAL);
  mega_kernel<<<256, 512, LDS_TOTAL, stream>>>((const u16*)d_in[0], (const u16*)d_in[1], (const u16*)d_in[2], (const u16*)d_in[4],
                                               (const u16*)d_in[5], (const u16*)d_in[6], (u16*)d_out, (char*)d_ws);
}
```

```cpp
#define RSLEEP 13
#define FSLEEP 0
#define LATE0 6
#define WSLEEP 0
#include <hip/hip_runtime.h>
#include <hip/hip_bf16.h>
#include <stdint.h>
#include <type_traits>
#pragma clang diagnostic ignored "-Winline-asm"
#pragma clang diagnostic ignored "-Wunused-value"

typedef unsigned short u16;
typedef __attribute__((ext_vector_type(8))) short bf16x8;
typedef __attribute__((ext_vector_type(4))) short bf16x4;
typedef __attribute__((ext_vector_type(4))) float f32x4;
typedef __attribute__((ext_vector_type(4))) unsigned u32x4;
typedef __attribute__((ext_vector_type(2))) unsigned u32x2;
typedef __attribute__((ext_vector_type(8))) float f32x8;
typedef __attribute__((ext_vector_type(16))) unsigned u32x16;
typedef __attribute__((ext_vector_type(16))) float f32x16;
typedef __attribute__((ext_vector_type(32))) float f32x32;
template <int I, typename T> __device__ __forceinline__ f32x4 quad(const T& v) { return __builtin_shufflevector(v, v, 4 * I, 4 * I + 1, 4 * I + 2, 4 * I + 3); }

constexpr int SEQ = 2048, DM = 1024, NH = 32;
#define ST(i)
constexpr int KVP = 64 * 32 + 64;

typedef __attribute__((ext_vector_type(2))) float f32x2;
typedef __attribute__((ext_vector_type(2))) __bf16 bf16x2_t;
__device__ __forceinline__ unsigned pk2(float a, float b) {
  f32x2 v = {a, b};
  bf16x2_t r = __builtin_convertvector(v, bf16x2_t);
  return __builtin_bit_cast(unsigned, r);
}

__device__ __forceinline__ bf16x4 tr_read(const char* p) {
  return __builtin_amdgcn_ds_read_tr16_b64_v4i16((__attribute__((address_space(3))) bf16x4*)p);
}
template <typename T> __device__ __forceinline__ __attribute__((address_space(3))) T& lds(unsigned a) { return *(__attribute__((address_space(3))) T*)(size_t)a; }
__device__ __forceinline__ bf16x4 tr_read(unsigned a) { return __builtin_amdgcn_ds_read_tr16_b64_v4i16(&lds<bf16x4>(a)); }
__device__ __forceinline__ f32x4 xget(unsigned a) { const f32x2 p = lds<f32x2>(a), q = lds<f32x2>(a + 512); return f32x4{p[0], p[1], q[0], q[1]}; }
__device__ __forceinline__ bf16x8 cat8(bf16x4 a, bf16x4 b) {
  bf16x8 f = {a[0], a[1], a[2], a[3], b[0], b[1], b[2], b[3]};
  return f;
}
__device__ __forceinline__ void glds16s(const void* sbase, unsigned voff, unsigned lds_base) {
  asm volatile("s_mov_b32 m0, %2\n\ts_nop 0\n\tglobal_load_lds_dwordx4 %0, %1" :: "v"(voff), "s"(sbase), "s"(lds_base) : "memory", "m0");
}
__device__ __forceinline__ void glds16c(const void* sbase, unsigned voff, unsigned lds_base) {
  asm volatile("s_mov_b32 m0, %2\n\ts_nop 0\n\tglobal_load_lds_dwordx4 %0, %1 sc1" :: "v"(voff), "s"(sbase), "s"(lds_base) : "memory", "m0");
}
#define GL_ "\n\tglobal_load_lds_dwordx4 "
__device__ __forceinline__ void glds4c(const void* sbase, unsigned v, unsigned lds_base) {
  asm volatile("s_mov_b32 m0, %2\n\ts_nop 0" GL_ "%0, %1 sc1" GL_ "%0, %1 offset:1024 sc1" GL_ "%0, %1 offset:2048 sc1" GL_ "%0, %1 offset:3072 sc1"
               :: "v"(v), "s"(sbase), "s"(lds_base) : "memory", "m0");
}
__device__ __forceinline__ void glds4v(const void* sbase, unsigned v0, unsigned v1, unsigned v2, unsigned v3, unsigned lds_base) {
  asm volatile("s_mov_b32 m0, %5\n\ts_nop 0" GL_ "%0, %4 sc1" GL_ "%1, %4 offset:1024 sc1" GL_ "%2, %4 offset:2048 sc1" GL_ "%3, %4 offset:3072 sc1"
               :: "v"(v0), "v"(v1), "v"(v2), "v"(v3), "s"(sbase), "s"(lds_base) : "memory", "m0");
}
__device__ __forceinline__ void glds2v(const void* sbase, unsigned v0, unsigned v1, unsigned lds_base) {
  asm volatile("s_mov_b32 m0, %3\n\ts_nop 0" GL_ "%0, %2 sc1" GL_ "%1, %2 offset:1024 sc1" :: "v"(v0), "v"(v1), "s"(sbase), "s"(lds_base) : "memory", "m0");
}
__device__ __forceinline__ void gld16(u32x4& d, unsigned voff, const void* sbase) {
  asm volatile("global_load_dwordx4 %0, %1, %2" : "=v"(d) : "v"(voff), "s"(sbase) : "memory");
}
__device__ __forceinline__ unsigned lds_addr(const void* p) { return (unsigned)(size_t)p; }
#define WAITV(n) asm volatile("s_waitcnt vmcnt(" #n ")" ::: "memory")
#define WAITVN(n) asm volatile("s_waitcnt vmcnt(%0)" :: "n"(n) : "memory")
#define WAITL0 do { asm volatile("" ::: "memory"); __builtin_amdgcn_s_waitcnt(0xC07F); asm volatile("" ::: "memory"); } while (0)
#define BAR __builtin_amdgcn_s_barrier()
#define ARRIVED(...) do { WAITV(0); __builtin_amdgcn_sched_barrier(0); asm volatile("" : __VA_ARGS__); } while (0)

__device__ __forceinline__ float vmaxf(float a, float b) { float r; asm("v_max_f32 %0, %1, %2" : "=v"(r) : "v"(a), "v"(b)); return r; }
template <int N> __device__ __forceinline__ float dpp_ror(float x) {
  return __uint_as_float(__builtin_amdgcn_update_dpp(0u, __float_as_uint(x), 0x120 + N, 0xf, 0xf, false));
}
__device__ __forceinline__ float r16_sum(float x) { x += dpp_ror<8>(x); x += dpp_ror<4>(x); x += dpp_ror<2>(x); return x + dpp_ror<1>(x); }
__device__ __forceinline__ float vmax3f(float a, float b, float c) { float r; asm("v_max3_f32 %0, %1, %2, %3" : "=v"(r) : "v"(a), "v"(b), "v"(c)); return r; }
__device__ __forceinline__ void max8pn(const f32x4& a, const f32x4& b, float& mx, float& mn) {
  asm("v_max3_f32 %0, %2, %3, %4\n\tv_max3_f32 %1, -%2, -%3, -%4\n\tv_max3_f32 %0, %0, %5, %6\n\tv_max3_f32 %1, %1, -%5, -%6\n\t"
      "v_max3_f32 %0, %0, %7, %8\n\tv_max3_f32 %1, %1, -%7, -%8\n\tv_max_f32 %0, %0, %9\n\tv_max_f32 %1, %1, -%9\n\ts_nop 0"
      : "=&v"(mx), "=&v"(mn) : "v"(a[0]), "v"(a[1]), "v"(a[2]), "v"(a[3]), "v"(b[0]), "v"(b[1]), "v"(b[2]), "v"(b[3]));
}
__device__ __forceinline__ float vmaxf_n(float a, float b) { float r; asm("v_max_f32 %0, %1, %2\n\ts_nop 0" : "=v"(r) : "v"(a), "v"(b)); return r; }
__device__ __forceinline__ float vmax3f_n(float a, float b, float c) { float r; asm("v_max3_f32 %0, %1, %2, %3\n\ts_nop 0" : "=v"(r) : "v"(a), "v"(b), "v"(c)); return r; }
template <bool MAXOP> __device__ __forceinline__ float rop(float a, float b) { return MAXOP ? vmaxf_n(a, b) : a + b; }
template <bool MAXOP> __device__ __forceinline__ void xr2(float& a, float& b) {
  auto r = __builtin_amdgcn_permlane16_swap(__float_as_uint(a), __float_as_uint(b), false, false);
  unsigned u = __float_as_uint(rop<MAXOP>(__uint_as_float(r[0]), __uint_as_float(r[1])));
  auto r2 = __builtin_amdgcn_permlane32_swap(u, u, false, false);
  u = __float_as_uint(rop<MAXOP>(__uint_as_float(r2[0]), __uint_as_float(r2[1])));
  auto r3 = __builtin_amdgcn_permlane16_swap(u, u, false, false);
  a = __uint_as_float(r3[0]); b = __uint_as_float(r3[1]);
}
template <bool MAXOP> __device__ __forceinline__ void xr4(float& a, float& b, float& c, float& d) {
  auto r = __builtin_amdgcn_permlane16_swap(__float_as_uint(a), __float_as_uint(b), false, false);
  auto q = __builtin_amdgcn_permlane16_swap(__float_as_uint(c), __float_as_uint(d), false, false);
  const unsigned ab = __float_as_uint(rop<MAXOP>(__uint_as_float(r[0]), __uint_as_float(r[1])));
  const unsigned cd = __float_as_uint(rop<MAXOP>(__uint_as_float(q[0]), __uint_as_float(q[1])));
  auto t = __builtin_amdgcn_permlane32_swap(ab, cd, false, false);
  const unsigned u = __float_as_uint(rop<MAXOP>(__uint_as_float(t[0]), __uint_as_float(t[1])));
  auto h = __builtin_amdgcn_permlane32_swap(u, u, false, false);
  auto p0 = __builtin_amdgcn_permlane16_swap(h[0], h[0], false, false);
  auto p1 = __builtin_amdgcn_permlane16_swap(h[1], h[1], false, false);
  a = __uint_as_float(p0[0]); b = __uint_as_float(p0[1]); c = __uint_as_float(p1[0]); d = __uint_as_float(p1[1]);
}

__device__ __forceinline__ unsigned xs4(float a, float b, float c, float d) {
  auto r = __builtin_amdgcn_permlane16_swap(__float_as_uint(a), __float_as_uint(b), false, false);
  auto q = __builtin_amdgcn_permlane16_swap(__float_as_uint(c), __float_as_uint(d), false, false);
  auto t = __builtin_amdgcn_permlane32_swap(__float_as_uint(__uint_as_float(r[0]) + __uint_as_float(r[1])), __float_as_uint(__uint_as_float(q[0]) + __uint_as_float(q[1])), false, false);
  return __float_as_uint(__uint_as_float(t[0]) + __uint_as_float(t[1]));
}

__device__ __forceinline__ f32x4 sum4(const u32x4 (&v)[4]) {
  float r0, r1, r2, r3, t0, t1, t2, t3;
  asm("v_add_f32 %0, %8, %12\n\tv_add_f32 %1, %9, %13\n\tv_add_f32 %2, %10, %14\n\tv_add_f32 %3, %11, %15\n\t"
      "v_add_f32 %4, %16, %20\n\tv_add_f32 %5, %17, %21\n\tv_add_f32 %6, %18, %22\n\tv_add_f32 %7, %19, %23\n\t"
      "v_add_f32 %0, %0, %4\n\tv_add_f32 %1, %1, %5\n\tv_add_f32 %2, %2, %6\n\tv_add_f32 %3, %3, %7\n\ts_nop 0"
      : "=&v"(r0), "=&v"(r1), "=&v"(r2), "=&v"(r3), "=&v"(t0), "=&v"(t1), "=&v"(t2), "=&v"(t3)
      : "v"(v[0][0]), "v"(v[0][1]), "v"(v[0][2]), "v"(v[0][3]), "v"(v[1][0]), "v"(v[1][1]), "v"(v[1][2]), "v"(v[1][3]),
        "v"(v[2][0]), "v"(v[2][1]), "v"(v[2][2]), "v"(v[2][3]), "v"(v[3][0]), "v"(v[3][1]), "v"(v[3][2]), "v"(v[3][3]));
  return f32x4{r0, r1, r2, r3};
}
#define MFMA(a, b, c) __builtin_amdgcn_mfma_f32_16x16x32_bf16(a, b, c, 0, 0, 0)

typedef unsigned long long u64;
typedef __attribute__((address_space(1))) unsigned long long gu64;
constexpr unsigned WS_FLAGS = 4096;
constexpr int FLS = 64;
constexpr unsigned WS_G0 = 65536;
constexpr unsigned WS_CSP = WS_G0 + SEQ * 16;
constexpr unsigned WS_ST = WS_CSP + 16 * 16 * 768;
constexpr unsigned WS_X = 1u << 21;
constexpr unsigned WS_O = WS_X + 3u * SEQ * DM * 2;
constexpr unsigned KVPB = KVP * 2, KVFB = KVP * 4;
constexpr unsigned EMPTY = 0xffffffffu;
constexpr unsigned WS_KV = WS_O + 4u * SEQ * DM * 2;
constexpr unsigned KVLB = 16u * NH * KVPB + 16u * KVFB;
constexpr unsigned WS_HL = WS_KV + 2u * KVLB;
constexpr unsigned WS_CSA = WS_HL + 512u * 4096;
constexpr unsigned WS_CPA = WS_CSA + 256u * 1024;
constexpr unsigned WS_IMG = WS_CPA + 16 * 16 * 768;
constexpr unsigned WS_END = WS_IMG + 2048 * 1024;
constexpr int LDS_TOTAL = 163840;
constexpr int OFF_WO = 49152;
struct Next { u64 kord; const char* wq; unsigned tw; };
template <int V> using IC = std::integral_constant<int, V>;
__device__ __forceinline__ bf16x8 ones8() { unsigned e = 0x3F803F80u; asm volatile("" : "+v"(e)); return __builtin_bit_cast(bf16x8, u32x4{e, e, e, e}); }
__device__ __forceinline__ u32x4 empty4() { unsigned e = EMPTY; asm volatile("" : "+v"(e)); return u32x4{e, e, e, e}; }
__device__ __forceinline__ u64 korder(int v, int own) {
  const u64 t = v == 0 ? 0x3b210fedca987654ull : v == 1 ? 0xb37654210fedca98ull : v == 2 ? 0x3ba987654210fedcull : 0xb3fedca987654210ull;
  const u64 inv = v == 0 ? 0xa987e6543210fdcbull : v == 1 ? 0x6543f210dcbae987ull : v == 2 ? 0x3210edcba987f654ull : 0xdcbaf9876543e210ull;
  const u64 low = (1ull << (4 * ((inv >> (4 * own)) & 15))) - 1ull;
  return (u64)own | ((t & low) << 4) | (t & ~((low << 4) | 15ull));
}
__device__ __forceinline__ u64 korder0(int rt) {
  const int r = rt & 7;
  const u64 own = (0x765432765432ull >> (4 * (r < 6 ? r : r - 6))) & 0xffffffull, oth = 0x7654321076543210ull >> (4 * r);
  const u64 t = 0x10ull | ((oth & 15ull) << 8) | (own << 12) | ((oth >> 4) << 36);
  return t | (rt < 8 ? 0x8888888000000800ull : 0x0000000888888088ull);
}
__device__ __forceinline__ int knib(u64 t, int s) { return (int)(t >> (4 * s)) & 15; }
__device__ __forceinline__ unsigned kfirst(u64 t) { return (1u << knib(t, 1)) | (1u << knib(t, 2)) | (1u << knib(t, 3)) | (1u << knib(t, 4)); }
__device__ __forceinline__ int kv1(int bid) { return (bid + (0x4924 >> ((bid >> 2) & 14))) & 3; }

__device__ __forceinline__ bool img(int layer, int hp) { return layer >= 2 && (hp & 7) == 3; }
__device__ __forceinline__ const char* wq_src(const char* ws, const void* W, int layer, int hp) { return img(layer, hp) ? ws + WS_IMG + ((layer & 1) * 768 + (hp >> 3) * 384) * 1024 : (const char*)W; }
__device__ __forceinline__ const char* wo_src(const char* ws, const void* W, int layer, int hp) { return img(layer, hp) ? ws + WS_IMG + (1536 + (layer & 1) * 256 + (hp >> 3) * 128) * 1024 : (const char*)W; }

template <typename T> __device__ __forceinline__ T* uni(T* p) {
  const unsigned long long v = (unsigned long long)p;
  const unsigned lo = __builtin_amdgcn_readfirstlane((unsigned)v), hi = __builtin_amdgcn_readfirstlane((unsigned)(v >> 32));
  return (T*)(((unsigned long long)hi << 32) | lo);
}
__device__ __forceinline__ int lane_id() {
  int l;
  asm volatile("v_mbcnt_lo_u32_b32 %0, -1, 0\n\tv_mbcnt_hi_u32_b32 %0, -1, %0" : "=v"(l));
  __builtin_assume(l >= 0 && l < 64);
  return l;
}
#define IDS \
  const int lane = lane_id(); \
  asm volatile("" : "+s"(w)); \
  const int tid = w * 64 + lane; \
  int bid = blockIdx.x; asm volatile("" : "+s"(bid)); \
  ws = uni(ws); asm volatile("" : "+s"(ws)); \
  layer = __builtin_amdgcn_readfirstlane(layer); x0 = uni(x0); wo = uni(wo); \
    \
  const int rt = 2 * (bid & 4) + ((bid >> 3) & 7); \
  const int hp = 4 * (bid & 3) + (bid >> 6); \
  const int m0 = rt * 128; \
  const int fr = lane & 15, g = lane >> 4; \
  const int q4 = (lane & 15) >> 2, p4 = lane & 3; \
  const __amdgpu_buffer_rsrc_t rsW = __builtin_amdgcn_make_buffer_rsrc((void*)ws, (short)0, (int)WS_END, 0x00020000); \
  (void)m0; (void)fr; (void)q4; (void)p4; (void)rsW; (void)g;

#define LANES \
  const int lane = lane_id(), tid = w * 64 + lane, fr = lane & 15, g = lane >> 4, q4 = (lane & 15) >> 2, p4 = lane & 3; \
  (void)tid; (void)fr; (void)g; (void)q4; (void)p4;

__device__ __forceinline__ void code_touch(unsigned& junk, int bid, int lane) {
  unsigned long long pc;
  unsigned len, d;
  asm volatile("s_getpc_b64 %0\n.Lct%=:\n\ts_mov_b32 %2, .Lct%=-.Lmk_pc\n\ts_mov_b32 %1, .Lmk_end-.Lmk_pc" : "=s"(pc), "=s"(len), "=s"(d));
  pc -= d;
  const unsigned per = ((len >> 10) + 1) * 128, off = ((bid >> 3) & 7) * per + (lane * 128 < per ? lane * 128 : 0);
  asm volatile("global_load_dword %0, %1, %2" : "+v"(junk) : "v"(off < len - 4 ? off : 0u), "s"(pc) : "memory");
}

__device__ __forceinline__ void publish(__amdgpu_buffer_rsrc_t rsW, int tid, unsigned idx, u64 v) {
  unsigned off = WS_FLAGS + idx * FLS;
  asm volatile("" : "+v"(off), "+v"(v));
  WAITV(0);
  WAITL0; BAR;
  if (tid == 192) __builtin_amdgcn_raw_buffer_store_b64(__builtin_bit_cast(u32x2, v), rsW, off, 0, 16);
  asm volatile("" ::: "memory");
}
__device__ __forceinline__ u64 poll_flags(char* ws, u64 E, int set, int rt, int hp, int lane, unsigned n, u64 mask, u64 now = ~0ull) {
  u64 miss = 0;
  const int l = lane & 15, g = lane >> 4;
  const int fi = set * 256 + (g == 2 ? l * 16 + hp : rt * 16 + l);
  const unsigned need = ((mask >> lane) & 1) ? n : 0u;
  gu64* fp = (gu64*)(ws + (need ? WS_FLAGS + fi * FLS : 0));
  _Pragma("unroll 1") for (unsigned spins = 0; spins < (1u << 18); ++spins) {
    const u64 d = __hip_atomic_load(fp, __ATOMIC_RELAXED, __HIP_MEMORY_SCOPE_AGENT) - E;
    const bool ok = (need == 0u) || (d >= (u64)need && d <= 16ull);
    miss = __ballot(!ok);
    if (!(miss & now)) break;
    __builtin_amdgcn_s_sleep(1);
  }
  asm volatile("s_waitcnt vmcnt(0)" ::: "memory");
  return miss;
}

__device__ __forceinline__ u64 poll_eq(const char* ws, unsigned off, u64 want, u64 mask, u64 now) {
  u64 miss;
  _Pragma("unroll 1") for (unsigned spins = 0; spins < (1u << 18); ++spins) {
    u64 d;
    asm volatile("global_load_dwordx2 %0, %1, %2 sc1\n\ts_waitcnt vmcnt(0)" : "=v"(d) : "v"(off), "s"(ws) : "memory");
    miss = __ballot(d != want) & mask;
    if (!(miss & now)) break;
    __builtin_amdgcn_s_sleep(1);
  }
  return miss;
}

#define G1_MACROS \
  ".macro G1M rb, j, s, z\n" \
  " .set g1d, 160+24*(\\rb)+4*(((\\j)+2)-6*((\\j)>>2))\n .set g1a, 80+40*(\\s)+4*(\\rb)\n .set g1b, 96+40*(\\s)+4*(\\j)\n" \
  " .if ((\\j)>>1) == 1\n  .set g1t, g1a\n  .set g1a, g1b\n  .set g1b, g1t\n .endif\n" \
  " .if \\z\n  v_mfma_f32_16x16x32_bf16 v[g1d:g1d+3], v[g1b:g1b+3], v[g1a:g1a+3], 0\n" \
  " .else\n  v_mfma_f32_16x16x32_bf16 v[g1d:g1d+3], v[g1b:g1b+3], v[g1a:g1a+3], v[g1d:g1d+3]\n .endif\n" \
  ".endm\n" \
  ".macro G1RA rb, s, ra, oa\n" \
  " ds_read_b128 v[80+40*(\\s)+4*(\\rb):83+40*(\\s)+4*(\\rb)], \\ra offset:(\\oa)+(\\rb)*2048\n" \
  ".endm\n" \
  ".macro G1RB k, s, r0, r1, ob\n" \
  " .if (\\k) & 2\n  ds_read_b64_tr_b16 v[96+40*(\\s)+2*(\\k):97+40*(\\s)+2*(\\k)], \\r1 offset:(\\ob)+(((\\k)>>2)+1-3*((\\k)>>3))*2048+((\\k)&1)*512\n" \
  " .else\n  ds_read_b64_tr_b16 v[96+40*(\\s)+2*(\\k):97+40*(\\s)+2*(\\k)], \\r0 offset:(\\ob)+(((\\k)>>2)+1-3*((\\k)>>3))*2048+((\\k)&1)*512\n .endif\n" \
  ".endm\n" \
    \
  ".macro G1BK i, g, r0, r1, ob\n G1RB 4*(\\g)+((\\i)&3), (\\i)>>2, \\r0, \\r1, (\\ob)+((\\i)>>2)*12288\n.endm\n" \
  ".macro G1AK rb, s, a0, a1, oa\n .if \\s\n  G1RA \\rb, 1, \\a1, \\oa\n .else\n  G1RA \\rb, 0, \\a0, \\oa\n .endif\n.endm\n" \
  ".macro G1RDB g, r0, r1, ob\n .set g1i, 0\n .rept 8\n  G1BK g1i, \\g, \\r0, \\r1, \\ob\n  .set g1i, g1i+1\n .endr\n.endm\n" \
  ".macro G1RDA a0, a1, oa\n .set g1i, 0\n .rept 8\n  G1AK (g1i>>1)^2, g1i&1, \\a0, \\a1, \\oa\n  .set g1i, g1i+1\n .endr\n.endm\n" \
    \
  ".macro G1Q g, rb, q, i, z\n G1M \\rb, 2*(\\g)+((((\\i)>>1)^(\\q))&1), (((\\i)+1)>>1)&1, (\\z)&&(((\\i)&1)==0)\n.endm\n" \
    \
  ".macro G1SG g, z, rd, a0, a1, r0, r1, o\n" \
  " .set g1i, 0\n .rept 16\n" \
  "  .if ((\\g) == 0) && (g1i == 8) && (\\rd)\n   s_waitcnt lgkmcnt(7)\n   s_nop 0\n  .endif\n" \
  "  .if ((\\g) == 0) && (g1i == 15)\n   s_waitcnt lgkmcnt(0)\n  .endif\n" \
  "  G1Q \\g, (g1i>>2)^(2-((\\g)&1)), g1i>>2, g1i&3, \\z\n" \
  "  .if \\rd\n" \
  "   .if (\\g) == 0\n    .if g1i < 7\n     G1BK g1i, 2, \\r0, \\r1, \\o\n    .elseif g1i == 8\n     G1BK 7, 2, \\r0, \\r1, \\o\n    .endif\n" \
  "   .elseif (\\g) == 1\n    .if g1i < 8\n     G1BK g1i, 0, \\r0, \\r1, \\o\n    .endif\n" \
  "   .elseif (g1i & 2) == 0\n    G1BK (g1i&1)+2*(g1i>>2), 1, \\r0, \\r1, \\o\n" \
  "   .elseif (\\rd) == 1\n    G1AK (g1i>>2)^2, 1-(g1i&1), \\a0, \\a1, \\o\n   .endif\n" \
  "  .endif\n" \
  "  .set g1i, g1i+1\n .endr\n" \
  ".endm\n" \
  ""
#define G1_SLIDE \
  "s_cmp_eq_u32 %[wm], 0\n v_pk_fma_f32 v[240:241], v[96:97], v[138:139], v[240:241] op_sel_hi:[1,0,1]\n " \
  "v_pk_fma_f32 v[242:243], v[98:99], v[138:139], v[242:243] op_sel_hi:[1,0,1]\n " \
  "v_pk_fma_f32 v[244:245], v[100:101], v[138:139], v[244:245] op_sel_hi:[1,0,1]\n " \
  "v_pk_fma_f32 v[246:247], v[102:103], v[138:139], v[246:247] op_sel_hi:[1,0,1]\n v_pk_mul_f32 v[240:241], v[138:139], v[240:241] op_sel:[1,0]\n " \
  "v_pk_mul_f32 v[242:243], v[138:139], v[242:243] op_sel:[1,0]\n v_pk_mul_f32 v[244:245], v[138:139], v[244:245] op_sel:[1,0]\n " \
  "v_pk_mul_f32 v[246:247], v[138:139], v[246:247] op_sel:[1,0]\n v_pk_fma_f32 v[248:249], v[104:105], v[144:145], v[248:249] op_sel_hi:[1,0,1]\n " \
  "v_pk_fma_f32 v[250:251], v[106:107], v[144:145], v[250:251] op_sel_hi:[1,0,1]\n " \
  "v_pk_fma_f32 v[252:253], v[104:105], v[144:145], v[252:253] op_sel:[0,1,0]\n " \
  "v_pk_fma_f32 v[254:255], v[106:107], v[144:145], v[254:255] op_sel:[0,1,0]\n v_pk_mul_f32 v[248:249], v[248:249], v[108:109]\n " \
  "v_pk_mul_f32 v[250:251], v[250:251], v[110:111]\n v_pk_mul_f32 v[252:253], v[252:253], v[108:109]\n v_pk_mul_f32 v[254:255], v[254:255], v[110:111]\n " \
  "v_cvt_pk_bf16_f32 v240, v240, v241\n v_cvt_pk_bf16_f32 v241, v242, v243\n v_cvt_pk_bf16_f32 v242, v244, v245\n v_cvt_pk_bf16_f32 v243, v246, v247\n " \
  "v_cvt_pk_bf16_f32 v248, v248, v249\n v_cvt_pk_bf16_f32 v249, v250, v251\n v_cvt_pk_bf16_f32 v255, v254, v255\n v_cvt_pk_bf16_f32 v254, v252, v253\n " \
  "v_min_u32 v104, -2, v240\n v_min_u32 v105, -2, v241\n v_min_u32 v106, -2, v242\n v_min_u32 v107, -2, v243\n v_min_u32 v108, -2, v248\n " \
  "v_min_u32 v109, -2, v249\n v_min_u32 v110, -2, v254\n v_min_u32 v111, -2, v255\n s_waitcnt lgkmcnt(0)\n s_cbranch_scc1 .Lg1d%=\n " \
  "buffer_store_dwordx4 v[104:107], v55, %[rs], 0 offen sc1\n buffer_store_dwordx4 v[108:111], v55, %[rs], 0 offen offset:1024 sc1\n s_branch .Lg1e%=\n " \
  ".Lg1d%=:\n v_mov_b32 v53, 1\n ds_write_b128 v58, v[104:107]\n ds_write_b128 v58, v[108:111] offset:1024\n ds_write_b32 v59, v53\n .Lg1e%=:\n " \
  "v_pk_fma_f32 v[232:233], v[148:149], v[138:139], v[232:233] op_sel_hi:[1,0,1]\n " \
  "v_pk_fma_f32 v[234:235], v[150:151], v[138:139], v[234:235] op_sel_hi:[1,0,1]\n " \
  "v_pk_fma_f32 v[236:237], v[88:89], v[138:139], v[236:237] op_sel_hi:[1,0,1]\n " \
  "v_pk_fma_f32 v[238:239], v[90:91], v[138:139], v[238:239] op_sel_hi:[1,0,1]\n " \
  "v_pk_fma_f32 v[216:217], v[96:97], v[136:137], v[216:217] op_sel_hi:[1,0,1]\n " \
  "v_pk_fma_f32 v[218:219], v[98:99], v[136:137], v[218:219] op_sel_hi:[1,0,1]\n " \
  "v_pk_fma_f32 v[220:221], v[100:101], v[136:137], v[220:221] op_sel_hi:[1,0,1]\n " \
  "v_pk_fma_f32 v[222:223], v[102:103], v[136:137], v[222:223] op_sel_hi:[1,0,1]\n v_pk_mul_f32 v[216:217], v[136:137], v[216:217] op_sel:[1,0]\n " \
  "v_pk_mul_f32 v[218:219], v[136:137], v[218:219] op_sel:[1,0]\n v_pk_mul_f32 v[220:221], v[136:137], v[220:221] op_sel:[1,0]\n " \
  "v_pk_mul_f32 v[222:223], v[136:137], v[222:223] op_sel:[1,0]\n v_cvt_pk_bf16_f32 v232, v232, v233\n v_cvt_pk_bf16_f32 v233, v234, v235\n " \
  "v_cvt_pk_bf16_f32 v234, v236, v237\n v_cvt_pk_bf16_f32 v235, v238, v239\n v_cvt_pk_bf16_f32 v216, v216, v217\n v_cvt_pk_bf16_f32 v217, v218, v219\n " \
  "v_cvt_pk_bf16_f32 v218, v220, v221\n v_cvt_pk_bf16_f32 v219, v222, v223\n " \
  "v_pk_fma_f32 v[208:209], v[148:149], v[136:137], v[208:209] op_sel_hi:[1,0,1]\n " \
  "v_pk_fma_f32 v[210:211], v[150:151], v[136:137], v[210:211] op_sel_hi:[1,0,1]\n " \
  "v_pk_fma_f32 v[212:213], v[88:89], v[136:137], v[212:213] op_sel_hi:[1,0,1]\n " \
  "v_pk_fma_f32 v[214:215], v[90:91], v[136:137], v[214:215] op_sel_hi:[1,0,1]\n " \
  "v_pk_fma_f32 v[192:193], v[96:97], v[142:143], v[192:193] op_sel_hi:[1,0,1]\n " \
  "v_pk_fma_f32 v[194:195], v[98:99], v[142:143], v[194:195] op_sel_hi:[1,0,1]\n " \
  "v_pk_fma_f32 v[196:197], v[100:101], v[142:143], v[196:197] op_sel_hi:[1,0,1]\n " \
  "v_pk_fma_f32 v[198:199], v[102:103], v[142:143], v[198:199] op_sel_hi:[1,0,1]\n v_pk_mul_f32 v[192:193], v[142:143], v[192:193] op_sel:[1,0]\n " \
  "v_pk_mul_f32 v[194:195], v[142:143], v[194:195] op_sel:[1,0]\n v_pk_mul_f32 v[196:197], v[142:143], v[196:197] op_sel:[1,0]\n " \
  "v_pk_mul_f32 v[198:199], v[142:143], v[198:199] op_sel:[1,0]\n v_mfma_f32_16x16x32_bf16 v[120:123], v[240:243], v[232:235], v[40:43]\n " \
  "v_mfma_f32_16x16x32_bf16 v[236:239], v[216:219], v[232:235], v[36:39]\n v_cvt_pk_bf16_f32 v208, v208, v209\n v_cvt_pk_bf16_f32 v209, v210, v211\n " \
  "v_cvt_pk_bf16_f32 v210, v212, v213\n v_cvt_pk_bf16_f32 v211, v214, v215\n v_cvt_pk_bf16_f32 v192, v192, v193\n v_cvt_pk_bf16_f32 v193, v194, v195\n " \
  "v_cvt_pk_bf16_f32 v194, v196, v197\n v_cvt_pk_bf16_f32 v195, v198, v199\n " \
  "v_pk_fma_f32 v[184:185], v[148:149], v[142:143], v[184:185] op_sel_hi:[1,0,1]\n " \
  "v_pk_fma_f32 v[186:187], v[150:151], v[142:143], v[186:187] op_sel_hi:[1,0,1]\n " \
  "v_pk_fma_f32 v[188:189], v[88:89], v[142:143], v[188:189] op_sel_hi:[1,0,1]\n " \
  "v_pk_fma_f32 v[190:191], v[90:91], v[142:143], v[190:191] op_sel_hi:[1,0,1]\n " \
  "v_pk_fma_f32 v[168:169], v[96:97], v[140:141], v[168:169] op_sel_hi:[1,0,1]\n " \
  "v_pk_fma_f32 v[170:171], v[98:99], v[140:141], v[170:171] op_sel_hi:[1,0,1]\n " \
  "v_pk_fma_f32 v[172:173], v[100:101], v[140:141], v[172:173] op_sel_hi:[1,0,1]\n " \
  "v_pk_fma_f32 v[174:175], v[102:103], v[140:141], v[174:175] op_sel_hi:[1,0,1]\n v_pk_mul_f32 v[168:169], v[140:141], v[168:169] op_sel:[1,0]\n " \
  "v_pk_mul_f32 v[170:171], v[140:141], v[170:171] op_sel:[1,0]\n v_pk_mul_f32 v[172:173], v[140:141], v[172:173] op_sel:[1,0]\n " \
  "v_pk_mul_f32 v[174:175], v[140:141], v[174:175] op_sel:[1,0]\n v_mfma_f32_16x16x32_bf16 v[220:223], v[216:219], v[208:211], v[40:43]\n " \
  "v_mfma_f32_16x16x32_bf16 v[212:215], v[192:195], v[208:211], v[36:39]\n v_cvt_pk_bf16_f32 v184, v184, v185\n v_cvt_pk_bf16_f32 v185, v186, v187\n " \
  "v_cvt_pk_bf16_f32 v186, v188, v189\n v_cvt_pk_bf16_f32 v187, v190, v191\n v_cvt_pk_bf16_f32 v168, v168, v169\n v_cvt_pk_bf16_f32 v169, v170, v171\n " \
  "v_cvt_pk_bf16_f32 v170, v172, v173\n v_cvt_pk_bf16_f32 v171, v174, v175\n " \
  "v_pk_fma_f32 v[160:161], v[148:149], v[140:141], v[160:161] op_sel_hi:[1,0,1]\n " \
  "v_pk_fma_f32 v[162:163], v[150:151], v[140:141], v[162:163] op_sel_hi:[1,0,1]\n " \
  "v_pk_fma_f32 v[164:165], v[88:89], v[140:141], v[164:165] op_sel_hi:[1,0,1]\n " \
  "v_pk_fma_f32 v[166:167], v[90:91], v[140:141], v[166:167] op_sel_hi:[1,0,1]\n " \
  "v_pk_fma_f32 v[176:177], v[128:129], v[144:145], v[176:177] op_sel_hi:[1,0,1]\n " \
  "v_pk_fma_f32 v[178:179], v[130:131], v[144:145], v[178:179] op_sel_hi:[1,0,1]\n " \
  "v_pk_fma_f32 v[180:181], v[128:129], v[144:145], v[180:181] op_sel:[0,1,0]\n " \
  "v_pk_fma_f32 v[182:183], v[130:131], v[144:145], v[182:183] op_sel:[0,1,0]\n v_pk_mul_f32 v[176:177], v[176:177], v[92:93]\n " \
  "v_pk_mul_f32 v[178:179], v[178:179], v[94:95]\n v_pk_mul_f32 v[180:181], v[180:181], v[92:93]\n v_pk_mul_f32 v[182:183], v[182:183], v[94:95]\n " \
  "v_mfma_f32_16x16x32_bf16 v[196:199], v[192:195], v[184:187], v[40:43]\n v_mfma_f32_16x16x32_bf16 v[188:191], v[168:171], v[184:187], v[36:39]\n " \
  "v_cvt_pk_bf16_f32 v160, v160, v161\n v_cvt_pk_bf16_f32 v161, v162, v163\n v_cvt_pk_bf16_f32 v162, v164, v165\n v_cvt_pk_bf16_f32 v163, v166, v167\n " \
  "v_cvt_pk_bf16_f32 v174, v176, v177\n v_cvt_pk_bf16_f32 v175, v178, v179\n v_cvt_pk_bf16_f32 v180, v180, v181\n v_cvt_pk_bf16_f32 v181, v182, v183\n " \
  "v_pk_fma_f32 v[224:225], v[112:113], v[144:145], v[224:225] op_sel_hi:[1,0,1]\n " \
  "v_pk_fma_f32 v[226:227], v[114:115], v[144:145], v[226:227] op_sel_hi:[1,0,1]\n " \
  "v_pk_fma_f32 v[228:229], v[112:113], v[144:145], v[228:229] op_sel:[0,1,0]\n " \
  "v_pk_fma_f32 v[230:231], v[114:115], v[144:145], v[230:231] op_sel:[0,1,0]\n v_pk_mul_f32 v[224:225], v[224:225], v[116:117]\n " \
  "v_pk_mul_f32 v[226:227], v[226:227], v[118:119]\n v_pk_mul_f32 v[228:229], v[228:229], v[116:117]\n v_pk_mul_f32 v[230:231], v[230:231], v[118:119]\n " \
  "v_mfma_f32_16x16x32_bf16 v[124:127], v[168:171], v[160:163], v[40:43]\n v_cvt_pk_bf16_f32 v246, v224, v225\n v_cvt_pk_bf16_f32 v247, v226, v227\n " \
  "v_cvt_pk_bf16_f32 v252, v228, v229\n v_cvt_pk_bf16_f32 v253, v230, v231\n v_max3_f32 v108, v236, v237, v238\n v_max3_f32 v106, v212, v213, v214\n " \
  "v_max3_f32 v109, v239, v120, v121\n v_max3_f32 v107, v215, v220, v221\n v_max3_f32 v108, v108, v122, v123\n v_max3_f32 v106, v106, v222, v223\n " \
  "v_max_f32 v108, v108, v109\n v_max_f32 v106, v106, v107\n v_max3_f32 v104, v188, v189, v190\n v_max3_f32 v105, v191, v196, v197\n " \
  "v_max3_f32 v104, v104, v198, v199\n v_pk_fma_f32 v[200:201], v[152:153], v[144:145], v[200:201] op_sel_hi:[1,0,1]\n " \
  "v_pk_fma_f32 v[202:203], v[154:155], v[144:145], v[202:203] op_sel_hi:[1,0,1]\n v_max_f32 v104, v104, v105\n v_mov_b32 v110, v108\n " \
  "v_pk_fma_f32 v[204:205], v[152:153], v[144:145], v[204:205] op_sel:[0,1,0]\n " \
  "v_pk_fma_f32 v[206:207], v[154:155], v[144:145], v[206:207] op_sel:[0,1,0]\n v_permlane16_swap_b32 v104, v106\n v_permlane16_swap_b32 v108, v110\n " \
  "v_pk_mul_f32 v[200:201], v[200:201], v[156:157]\n v_pk_mul_f32 v[202:203], v[202:203], v[158:159]\n v_max_f32 v104, v104, v106\n " \
  "v_max_f32 v108, v108, v110\n v_pk_mul_f32 v[204:205], v[204:205], v[156:157]\n v_pk_mul_f32 v[206:207], v[206:207], v[158:159]\n " \
  "v_permlane32_swap_b32 v104, v108\n v_mul_f32 v60, 0x3e8293ee, v139\n v_mul_f32 v62, 0x3e8293ee, v137\n v_max_f32 v104, v104, v108\n " \
  "v_mov_b32 v108, v104\n v_mul_f32 v56, 0x3e8293ee, v143\n v_mul_f32 v146, 0x3e8293ee, v141\n v_permlane32_swap_b32 v104, v108\n " \
  "v_mov_b32 v132, 0x3f803f80\n v_mov_b32 v133, 0x3f803f80\n v_mov_b32 v106, v104\n v_mov_b32 v134, 0x3f803f80\n v_mov_b32 v135, 0x3f803f80\n " \
  "v_permlane16_swap_b32 v104, v106\n v_cvt_pk_bf16_f32 v244, v200, v201\n v_cvt_pk_bf16_f32 v245, v202, v203\n v_cvt_pk_bf16_f32 v250, v204, v205\n " \
  "v_cvt_pk_bf16_f32 v251, v206, v207\n v_cvt_pk_bf16_f32 v176, v200, v201\n v_cvt_pk_bf16_f32 v177, v202, v203\n v_cvt_pk_bf16_f32 v182, v204, v205\n " \
  "v_cvt_pk_bf16_f32 v183, v206, v207\n v_mul_f32_e64 v61, v60, -v108\n v_mul_f32_e64 v63, v62, -v106\n v_mul_f32_e64 v57, v56, -v104\n " \
  "v_pk_fma_f32 v[236:237], v[236:237], v[60:61], v[60:61] op_sel:[0,0,1] op_sel_hi:[1,0,1]\n " \
  "v_pk_fma_f32 v[238:239], v[238:239], v[60:61], v[60:61] op_sel:[0,0,1] op_sel_hi:[1,0,1]\n " \
  "v_pk_fma_f32 v[120:121], v[120:121], v[60:61], v[60:61] op_sel:[0,0,1] op_sel_hi:[1,0,1]\n " \
  "v_pk_fma_f32 v[122:123], v[122:123], v[60:61], v[60:61] op_sel:[0,0,1] op_sel_hi:[1,0,1]\n v_exp_f32 v236, v236\n v_exp_f32 v237, v237\n " \
  "v_exp_f32 v238, v238\n v_exp_f32 v239, v239\n v_exp_f32 v120, v120\n v_exp_f32 v121, v121\n v_exp_f32 v122, v122\n v_exp_f32 v123, v123\n " \
  "v_pk_fma_f32 v[212:213], v[212:213], v[62:63], v[62:63] op_sel:[0,0,1] op_sel_hi:[1,0,1]\n " \
  "v_pk_fma_f32 v[214:215], v[214:215], v[62:63], v[62:63] op_sel:[0,0,1] op_sel_hi:[1,0,1]\n " \
  "v_pk_fma_f32 v[220:221], v[220:221], v[62:63], v[62:63] op_sel:[0,0,1] op_sel_hi:[1,0,1]\n " \
  "v_pk_fma_f32 v[222:223], v[222:223], v[62:63], v[62:63] op_sel:[0,0,1] op_sel_hi:[1,0,1]\n v_cvt_pk_bf16_f32 v236, v236, v237\n " \
  "v_cvt_pk_bf16_f32 v237, v238, v239\n v_cvt_pk_bf16_f32 v238, v120, v121\n v_cvt_pk_bf16_f32 v239, v122, v123\n v_exp_f32 v212, v212\n " \
  "v_exp_f32 v213, v213\n v_mfma_f32_16x16x32_bf16 v[120:123], v[132:135], v[236:239], 0\n " \
  "v_mfma_f32_16x16x32_bf16 v[232:235], v[246:249], v[236:239], 0\n v_mfma_f32_16x16x32_bf16 v[240:243], v[252:255], v[236:239], 0\n " \
  "v_exp_f32 v214, v214\n v_exp_f32 v215, v215\n v_exp_f32 v220, v220\n v_exp_f32 v221, v221\n v_exp_f32 v222, v222\n v_exp_f32 v223, v223\n " \
  "v_pk_fma_f32 v[188:189], v[188:189], v[56:57], v[56:57] op_sel:[0,0,1] op_sel_hi:[1,0,1]\n " \
  "v_pk_fma_f32 v[190:191], v[190:191], v[56:57], v[56:57] op_sel:[0,0,1] op_sel_hi:[1,0,1]\n " \
  "v_pk_fma_f32 v[196:197], v[196:197], v[56:57], v[56:57] op_sel:[0,0,1] op_sel_hi:[1,0,1]\n " \
  "v_pk_fma_f32 v[198:199], v[198:199], v[56:57], v[56:57] op_sel:[0,0,1] op_sel_hi:[1,0,1]\n v_cvt_pk_bf16_f32 v212, v212, v213\n " \
  "v_cvt_pk_bf16_f32 v213, v214, v215\n v_cvt_pk_bf16_f32 v214, v220, v221\n v_cvt_pk_bf16_f32 v215, v222, v223\n v_exp_f32 v188, v188\n " \
  "s_cmp_eq_u32 %[wm], 0\n s_cbranch_scc1 .Lg1f%=\n ds_read_b32 v53, v59\n ds_read_b128 v[80:83], v58\n ds_read_b64 v[172:173], v58 offset:1024\n " \
  "ds_read_b64 v[178:179], v58 offset:1032\n s_branch .Lg1g%=\n .Lg1f%=:\n buffer_load_dwordx4 v[80:83], v55, %[rs], 0 offen sc1\n " \
  "buffer_load_dwordx2 v[172:173], v55, %[rs], 0 offen offset:1024 sc1\n buffer_load_dwordx2 v[178:179], v55, %[rs], 0 offen offset:1032 sc1\n " \
  ".Lg1g%=:\n v_exp_f32 v189, v189\n v_mfma_f32_16x16x32_bf16 v[220:223], v[132:135], v[212:215], 0\n " \
  "v_mfma_f32_16x16x32_bf16 v[224:227], v[244:247], v[212:215], 0\n v_mfma_f32_16x16x32_bf16 v[228:231], v[250:253], v[212:215], 0\n " \
  "v_exp_f32 v190, v190\n v_exp_f32 v191, v191\n v_exp_f32 v196, v196\n v_exp_f32 v197, v197\n v_exp_f32 v198, v198\n v_exp_f32 v199, v199\n " \
  "v_rcp_f32 v120, v120\n v_cvt_pk_bf16_f32 v188, v188, v189\n v_pk_mul_f32 v[232:233], v[232:233], v[120:121] op_sel_hi:[1,0]\n " \
  "v_pk_mul_f32 v[234:235], v[234:235], v[120:121] op_sel_hi:[1,0]\n v_pk_mul_f32 v[240:241], v[240:241], v[120:121] op_sel_hi:[1,0]\n " \
  "v_pk_mul_f32 v[242:243], v[242:243], v[120:121] op_sel_hi:[1,0]\n v_cvt_pk_bf16_f32 v189, v190, v191\n v_cvt_pk_bf16_f32 v190, v196, v197\n " \
  "v_cvt_pk_bf16_f32 v191, v198, v199\n v_rcp_f32 v220, v220\n v_cvt_pk_bf16_f32 v232, v232, v233\n v_cvt_pk_bf16_f32 v233, v234, v235\n " \
  "v_mfma_f32_16x16x32_bf16 v[196:199], v[132:135], v[188:191], 0\n v_mfma_f32_16x16x32_bf16 v[200:203], v[174:177], v[188:191], 0\n " \
  "v_mfma_f32_16x16x32_bf16 v[204:207], v[180:183], v[188:191], 0\n v_cvt_pk_bf16_f32 v234, v240, v241\n v_cvt_pk_bf16_f32 v235, v242, v243\n " \
  "buffer_store_dwordx4 v[232:235], v51, %[rs], 0 offen offset:3072 sc1\n ds_write_b128 v50, v[232:235] offset:3072\n " \
  "v_pk_mul_f32 v[224:225], v[224:225], v[220:221] op_sel_hi:[1,0]\n v_pk_mul_f32 v[226:227], v[226:227], v[220:221] op_sel_hi:[1,0]\n " \
  "v_pk_mul_f32 v[228:229], v[228:229], v[220:221] op_sel_hi:[1,0]\n v_pk_mul_f32 v[230:231], v[230:231], v[220:221] op_sel_hi:[1,0]\n " \
  "v_cvt_pk_bf16_f32 v224, v224, v225\n v_cvt_pk_bf16_f32 v225, v226, v227\n v_cvt_pk_bf16_f32 v226, v228, v229\n v_cvt_pk_bf16_f32 v227, v230, v231\n " \
  "v_rcp_f32 v196, v196\n buffer_store_dwordx4 v[224:227], v51, %[rs], 0 offen offset:2048 sc1\n ds_write_b128 v50, v[224:227] offset:2048\n " \
  "v_pk_mul_f32 v[200:201], v[200:201], v[196:197] op_sel_hi:[1,0]\n v_pk_mul_f32 v[202:203], v[202:203], v[196:197] op_sel_hi:[1,0]\n " \
  "v_pk_mul_f32 v[204:205], v[204:205], v[196:197] op_sel_hi:[1,0]\n v_pk_mul_f32 v[206:207], v[206:207], v[196:197] op_sel_hi:[1,0]\n " \
  "v_cvt_pk_bf16_f32 v200, v200, v201\n v_cvt_pk_bf16_f32 v201, v202, v203\n v_cvt_pk_bf16_f32 v202, v204, v205\n v_cvt_pk_bf16_f32 v203, v206, v207\n " \
  "buffer_store_dwordx4 v[200:203], v51, %[rs], 0 offen offset:1024 sc1\n ds_write_b128 v50, v[200:203] offset:1024\n s_mov_b32 vcc_hi, 0x40000\n " \
  "s_cmp_eq_u32 %[wm], 0\n s_cbranch_scc1 .Lg1h%=\n s_waitcnt lgkmcnt(3)\n .Lg1i%=:\n v_readfirstlane_b32 vcc_lo, v53\n s_nop 1\n " \
  "s_cmp_lg_u32 vcc_lo, 0\n s_cbranch_scc1 .Lg1k%=\n ds_read_b32 v53, v59\n ds_read_b128 v[80:83], v58\n ds_read_b64 v[172:173], v58 offset:1024\n " \
  "ds_read_b64 v[178:179], v58 offset:1032\n s_waitcnt lgkmcnt(0)\n s_sub_u32 vcc_hi, vcc_hi, 1\n s_cbranch_scc0 .Lg1i%=\n s_branch .Lg1k%=\n .Lg1h%=:\n " \
  "s_mov_b32 m0, 0x40000\n s_waitcnt vmcnt(3)\n .Lg1j%=:\n v_max3_u32 v104, v80, v81, v82\n v_max3_u32 v105, v83, v172, v173\n " \
  "v_max3_u32 v104, v104, v178, v179\n v_max_u32 v104, v104, v105\n v_cmp_eq_u32 vcc, -1, v104\n s_cbranch_vccz .Lg1k%=\n " \
  "buffer_load_dwordx4 v[80:83], v55, %[rs], 0 offen sc1\n buffer_load_dwordx2 v[172:173], v55, %[rs], 0 offen offset:1024 sc1\n " \
  "buffer_load_dwordx2 v[178:179], v55, %[rs], 0 offen offset:1032 sc1\n s_waitcnt vmcnt(0)\n s_sub_u32 m0, m0, 1\n s_cbranch_scc0 .Lg1j%=\n .Lg1k%=:\n " \
  "v_mfma_f32_16x16x32_bf16 v[164:167], v[80:83], v[160:163], v[44:47]\n s_nop 7\n v_max3_f32 v104, v164, v165, v166\n " \
  "v_max3_f32 v105, v167, v124, v125\n v_max3_f32 v104, v104, v126, v127\n v_max_f32 v104, v104, v105\n v_mov_b32 v105, v104\n s_nop 1\n " \
  "v_permlane16_swap_b32 v104, v105\n v_max_f32 v104, v104, v105\n v_mov_b32 v105, v104\n s_nop 1\n v_permlane32_swap_b32 v104, v105\n " \
  "v_max_f32 v104, v104, v105\n v_mul_f32_e64 v147, v146, -v104\n " \
  "v_pk_fma_f32 v[164:165], v[164:165], v[146:147], v[146:147] op_sel:[0,0,1] op_sel_hi:[1,0,1]\n " \
  "v_pk_fma_f32 v[166:167], v[166:167], v[146:147], v[146:147] op_sel:[0,0,1] op_sel_hi:[1,0,1]\n " \
  "v_pk_fma_f32 v[124:125], v[124:125], v[146:147], v[146:147] op_sel:[0,0,1] op_sel_hi:[1,0,1]\n " \
  "v_pk_fma_f32 v[126:127], v[126:127], v[146:147], v[146:147] op_sel:[0,0,1] op_sel_hi:[1,0,1]\n v_exp_f32 v164, v164\n v_exp_f32 v165, v165\n " \
  "v_exp_f32 v166, v166\n v_exp_f32 v167, v167\n v_exp_f32 v124, v124\n v_exp_f32 v125, v125\n v_exp_f32 v126, v126\n v_exp_f32 v127, v127\n " \
  "v_cvt_pk_bf16_f32 v164, v164, v165\n v_cvt_pk_bf16_f32 v165, v166, v167\n v_cvt_pk_bf16_f32 v166, v124, v125\n v_cvt_pk_bf16_f32 v167, v126, v127\n " \
  "s_nop 1\n v_mfma_f32_16x16x32_bf16 v[124:127], v[132:135], v[164:167], 0\n v_mfma_f32_16x16x32_bf16 v[168:171], v[172:175], v[164:167], 0\n " \
  "v_mfma_f32_16x16x32_bf16 v[84:87], v[178:181], v[164:167], 0\n s_nop 5\n v_rcp_f32 v124, v124\n s_nop 0\n " \
  "v_pk_mul_f32 v[168:169], v[168:169], v[124:125] op_sel_hi:[1,0]\n v_pk_mul_f32 v[170:171], v[170:171], v[124:125] op_sel_hi:[1,0]\n " \
  "v_pk_mul_f32 v[84:85], v[84:85], v[124:125] op_sel_hi:[1,0]\n v_pk_mul_f32 v[86:87], v[86:87], v[124:125] op_sel_hi:[1,0]\n " \
  "v_cvt_pk_bf16_f32 v168, v168, v169\n v_cvt_pk_bf16_f32 v169, v170, v171\n v_cvt_pk_bf16_f32 v170, v84, v85\n v_cvt_pk_bf16_f32 v171, v86, v87\n " \
  "buffer_store_dwordx4 v[168:171], v51, %[rs], 0 offen offset:0 sc1\n ds_write_b128 v50, v[168:171] offset:0\n "
#define G1_HEDGE \
  "G1Q 2, 2, 0, 0, 0\n ds_read2_b64 v[140:143], v49 offset1:16\n G1Q 2, 2, 0, 1, 0\n ds_read_b128 v[96:99], v48 offset:256\n G1Q 2, 2, 0, 2, 0\n " \
  "ds_read_b128 v[100:103], v48 offset:320\n G1Q 2, 2, 0, 3, 0\n ds_read2_b64 v[136:139], v49 offset0:32 offset1:48\n G1Q 2, 3, 1, 0, 0\n " \
  "ds_read_b64_tr_b16 v[104:105], v53\n G1Q 2, 3, 1, 1, 0\n ds_read_b64_tr_b16 v[106:107], v53 offset:1024\n G1Q 2, 3, 1, 2, 0\n " \
  "ds_read_b64_tr_b16 v[108:109], v53 offset:32\n G1Q 2, 3, 1, 3, 0\n ds_read_b64_tr_b16 v[110:111], v53 offset:1056\n G1Q 2, 0, 2, 0, 0\n " \
  "ds_read2_b32 v[144:145], v52 offset1:16\n G1Q 2, 0, 2, 1, 0\n ds_read_b128 v[148:151], v48\n G1Q 2, 0, 2, 2, 0\n " \
  "ds_read_b128 v[88:91], v48 offset:64\n G1Q 2, 0, 2, 3, 0\n ds_read_b128 v[128:131], v54\n G1Q 2, 1, 3, 0, 0\n ds_read_b128 v[92:95], v54 offset:512\n " \
  "G1Q 2, 1, 3, 1, 0\n ds_read_b128 v[132:135], v54 offset:64\n G1Q 2, 1, 3, 2, 0\n G1Q 2, 1, 3, 3, 0\n ds_read_b128 v[112:115], v54 offset:576\n " \
  "ds_read_b128 v[116:119], v54 offset:128\n ds_read_b128 v[152:155], v54 offset:640\n ds_read_b128 v[156:159], v54 offset:192\n " \
  "ds_read_b128 v[120:123], v54 offset:704\n s_waitcnt lgkmcnt(11)\n v_pk_fma_f32 v[168:169], v[96:97], v[140:141], v[168:169] op_sel_hi:[1,0,1]\n " \
  "v_pk_fma_f32 v[170:171], v[98:99], v[140:141], v[170:171] op_sel_hi:[1,0,1]\n " \
  "v_pk_fma_f32 v[172:173], v[100:101], v[140:141], v[172:173] op_sel_hi:[1,0,1]\n " \
  "v_pk_fma_f32 v[174:175], v[102:103], v[140:141], v[174:175] op_sel_hi:[1,0,1]\n " \
  "v_pk_fma_f32 v[192:193], v[96:97], v[142:143], v[192:193] op_sel_hi:[1,0,1]\n " \
  "v_pk_fma_f32 v[194:195], v[98:99], v[142:143], v[194:195] op_sel_hi:[1,0,1]\n " \
  "v_pk_fma_f32 v[196:197], v[100:101], v[142:143], v[196:197] op_sel_hi:[1,0,1]\n " \
  "v_pk_fma_f32 v[198:199], v[102:103], v[142:143], v[198:199] op_sel_hi:[1,0,1]\n v_cvt_pk_bf16_f32 v168, v168, v169\n " \
  "v_cvt_pk_bf16_f32 v169, v170, v171\n v_cvt_pk_bf16_f32 v170, v172, v173\n v_cvt_pk_bf16_f32 v171, v174, v175\n v_cvt_pk_bf16_f32 v192, v192, v193\n " \
  "v_cvt_pk_bf16_f32 v193, v194, v195\n v_cvt_pk_bf16_f32 v194, v196, v197\n v_cvt_pk_bf16_f32 v195, v198, v199\n " \
  "v_pk_fma_f32 v[216:217], v[96:97], v[136:137], v[216:217] op_sel_hi:[1,0,1]\n " \
  "v_pk_fma_f32 v[218:219], v[98:99], v[136:137], v[218:219] op_sel_hi:[1,0,1]\n " \
  "v_pk_fma_f32 v[220:221], v[100:101], v[136:137], v[220:221] op_sel_hi:[1,0,1]\n " \
  "v_pk_fma_f32 v[222:223], v[102:103], v[136:137], v[222:223] op_sel_hi:[1,0,1]\n " \
  "v_pk_fma_f32 v[240:241], v[96:97], v[138:139], v[240:241] op_sel_hi:[1,0,1]\n " \
  "v_pk_fma_f32 v[242:243], v[98:99], v[138:139], v[242:243] op_sel_hi:[1,0,1]\n " \
  "v_pk_fma_f32 v[244:245], v[100:101], v[138:139], v[244:245] op_sel_hi:[1,0,1]\n " \
  "v_pk_fma_f32 v[246:247], v[102:103], v[138:139], v[246:247] op_sel_hi:[1,0,1]\n v_mfma_f32_16x16x32_bf16 v[172:175], v[104:107], v[168:171], 0\n " \
  "v_mfma_f32_16x16x32_bf16 v[80:83], v[108:111], v[168:171], 0\n v_mfma_f32_16x16x32_bf16 v[196:199], v[104:107], v[192:195], 0\n " \
  "v_mfma_f32_16x16x32_bf16 v[84:87], v[108:111], v[192:195], 0\n v_cvt_pk_bf16_f32 v216, v216, v217\n v_cvt_pk_bf16_f32 v217, v218, v219\n " \
  "v_cvt_pk_bf16_f32 v218, v220, v221\n v_cvt_pk_bf16_f32 v219, v222, v223\n v_cvt_pk_bf16_f32 v240, v240, v241\n v_cvt_pk_bf16_f32 v241, v242, v243\n " \
  "v_cvt_pk_bf16_f32 v242, v244, v245\n v_cvt_pk_bf16_f32 v243, v246, v247\n v_mul_f32 v40, 0x3fb8aa3b, v141\n v_mul_f32 v42, 0x3fb8aa3b, v143\n " \
  "v_mul_f32 v44, 0x3fb8aa3b, v137\n v_mul_f32 v46, 0x3fb8aa3b, v139\n v_mfma_f32_16x16x32_bf16 v[220:223], v[104:107], v[216:219], 0\n " \
  "v_mfma_f32_16x16x32_bf16 v[124:127], v[108:111], v[216:219], 0\n v_mfma_f32_16x16x32_bf16 v[244:247], v[104:107], v[240:243], 0\n " \
  "v_mfma_f32_16x16x32_bf16 v[36:39], v[108:111], v[240:243], 0\n v_max3_f32 v168, v172, v173, v174\n v_max3_f32 v169, -v172, -v173, -v174\n " \
  "v_max3_f32 v168, v168, v175, v80\n v_max3_f32 v169, v169, -v175, -v80\n v_max3_f32 v168, v168, v81, v82\n v_max3_f32 v169, v169, -v81, -v82\n " \
  "v_max_f32_e64 v168, v168, v83\n v_max_f32_e64 v169, v169, -v83\n v_max3_f32 v170, v196, v197, v198\n v_max3_f32 v171, -v196, -v197, -v198\n " \
  "v_max3_f32 v170, v170, v199, v84\n v_max3_f32 v171, v171, -v199, -v84\n v_max3_f32 v170, v170, v85, v86\n v_max3_f32 v171, v171, -v85, -v86\n " \
  "v_max_f32_e64 v170, v170, v87\n v_max_f32_e64 v171, v171, -v87\n s_waitcnt lgkmcnt(0)\n " \
  "v_pk_fma_f32 v[176:177], v[128:129], v[144:145], v[176:177] op_sel_hi:[1,0,1]\n " \
  "v_pk_fma_f32 v[178:179], v[130:131], v[144:145], v[178:179] op_sel_hi:[1,0,1]\n " \
  "v_pk_fma_f32 v[180:181], v[128:129], v[144:145], v[180:181] op_sel:[0,1,0]\n " \
  "v_pk_fma_f32 v[182:183], v[130:131], v[144:145], v[182:183] op_sel:[0,1,0]\n v_pk_mul_f32 v[176:177], v[176:177], v[92:93]\n " \
  "v_pk_mul_f32 v[178:179], v[178:179], v[94:95]\n v_pk_mul_f32 v[180:181], v[180:181], v[92:93]\n v_pk_mul_f32 v[182:183], v[182:183], v[94:95]\n " \
  "v_pk_fma_f32 v[200:201], v[132:133], v[144:145], v[200:201] op_sel_hi:[1,0,1]\n " \
  "v_pk_fma_f32 v[202:203], v[134:135], v[144:145], v[202:203] op_sel_hi:[1,0,1]\n " \
  "v_pk_fma_f32 v[204:205], v[132:133], v[144:145], v[204:205] op_sel:[0,1,0]\n " \
  "v_pk_fma_f32 v[206:207], v[134:135], v[144:145], v[206:207] op_sel:[0,1,0]\n v_pk_mul_f32 v[200:201], v[200:201], v[112:113]\n " \
  "v_pk_mul_f32 v[202:203], v[202:203], v[114:115]\n v_pk_mul_f32 v[204:205], v[204:205], v[112:113]\n v_pk_mul_f32 v[206:207], v[206:207], v[114:115]\n " \
  "v_max3_f32 v192, v220, v221, v222\n v_max3_f32 v193, -v220, -v221, -v222\n v_max3_f32 v192, v192, v223, v124\n v_max3_f32 v193, v193, -v223, -v124\n " \
  "v_max3_f32 v192, v192, v125, v126\n v_max3_f32 v193, v193, -v125, -v126\n v_max_f32_e64 v192, v192, v127\n v_max_f32_e64 v193, v193, -v127\n " \
  "v_max3_f32 v194, v244, v245, v246\n v_max3_f32 v195, -v244, -v245, -v246\n v_max3_f32 v194, v194, v247, v36\n v_max3_f32 v195, v195, -v247, -v36\n " \
  "v_max3_f32 v194, v194, v37, v38\n v_max3_f32 v195, v195, -v37, -v38\n v_max_f32_e64 v194, v194, v39\n v_max_f32_e64 v195, v195, -v39\n " \
  "v_permlane16_swap_b32 v168, v169\n v_permlane16_swap_b32 v170, v171\n v_permlane16_swap_b32 v192, v193\n v_permlane16_swap_b32 v194, v195\n " \
  "v_pk_fma_f32 v[224:225], v[116:117], v[144:145], v[224:225] op_sel_hi:[1,0,1]\n " \
  "v_pk_fma_f32 v[226:227], v[118:119], v[144:145], v[226:227] op_sel_hi:[1,0,1]\n v_max_f32 v168, v168, v169\n v_max_f32 v170, v170, v171\n " \
  "v_max_f32 v192, v192, v193\n v_max_f32 v194, v194, v195\n v_pk_fma_f32 v[228:229], v[116:117], v[144:145], v[228:229] op_sel:[0,1,0]\n " \
  "v_pk_fma_f32 v[230:231], v[118:119], v[144:145], v[230:231] op_sel:[0,1,0]\n v_permlane32_swap_b32 v168, v170\n v_permlane32_swap_b32 v192, v194\n " \
  "v_pk_mul_f32 v[224:225], v[224:225], v[152:153]\n v_pk_mul_f32 v[226:227], v[226:227], v[154:155]\n v_max_f32 v168, v168, v170\n " \
  "v_max_f32 v192, v192, v194\n v_pk_mul_f32 v[228:229], v[228:229], v[152:153]\n v_pk_mul_f32 v[230:231], v[230:231], v[154:155]\n " \
  "v_mov_b32 v170, v168\n v_mov_b32 v194, v192\n v_pk_fma_f32 v[248:249], v[156:157], v[144:145], v[248:249] op_sel_hi:[1,0,1]\n " \
  "v_pk_fma_f32 v[250:251], v[158:159], v[144:145], v[250:251] op_sel_hi:[1,0,1]\n v_permlane32_swap_b32 v168, v170\n v_permlane32_swap_b32 v192, v194\n " \
  "v_pk_fma_f32 v[252:253], v[156:157], v[144:145], v[252:253] op_sel:[0,1,0]\n " \
  "v_pk_fma_f32 v[254:255], v[158:159], v[144:145], v[254:255] op_sel:[0,1,0]\n v_mov_b32 v169, v168\n v_mov_b32 v171, v170\n v_mov_b32 v193, v192\n " \
  "v_mov_b32 v195, v194\n v_pk_mul_f32 v[248:249], v[248:249], v[120:121]\n v_pk_mul_f32 v[250:251], v[250:251], v[122:123]\n " \
  "v_permlane16_swap_b32 v168, v169\n v_permlane16_swap_b32 v170, v171\n v_permlane16_swap_b32 v192, v193\n v_permlane16_swap_b32 v194, v195\n " \
  "v_pk_mul_f32 v[252:253], v[252:253], v[120:121]\n v_pk_mul_f32 v[254:255], v[254:255], v[122:123]\n v_cvt_pk_bf16_f32 v176, v176, v177\n " \
  "v_cvt_pk_bf16_f32 v177, v178, v179\n v_cvt_pk_bf16_f32 v178, v200, v201\n v_cvt_pk_bf16_f32 v179, v202, v203\n v_cvt_pk_bf16_f32 v180, v180, v181\n " \
  "v_cvt_pk_bf16_f32 v181, v182, v183\n v_cvt_pk_bf16_f32 v182, v204, v205\n v_cvt_pk_bf16_f32 v183, v206, v207\n v_cvt_pk_bf16_f32 v224, v224, v225\n " \
  "v_cvt_pk_bf16_f32 v225, v226, v227\n v_cvt_pk_bf16_f32 v226, v248, v249\n v_cvt_pk_bf16_f32 v227, v250, v251\n v_cvt_pk_bf16_f32 v228, v228, v229\n " \
  "v_cvt_pk_bf16_f32 v229, v230, v231\n v_cvt_pk_bf16_f32 v230, v252, v253\n v_cvt_pk_bf16_f32 v231, v254, v255\n v_mul_f32_e64 v41, v40, -v168\n " \
  "v_mul_f32_e64 v146, v40, -v169\n v_mul_f32_e64 v43, v42, -v170\n v_mul_f32_e64 v147, v42, -v171\n v_mul_f32_e64 v45, v44, -v192\n " \
  "v_mul_f32_e64 v144, v44, -v193\n v_mul_f32_e64 v47, v46, -v194\n v_mul_f32_e64 v145, v46, -v195\n " \
  "v_pk_fma_f32 v[200:201], v[172:173], v[40:41], v[146:147] op_sel_hi:[1,0,0] neg_lo:[1,0,0] neg_hi:[1,0,0]\n " \
  "v_pk_fma_f32 v[202:203], v[174:175], v[40:41], v[146:147] op_sel_hi:[1,0,0] neg_lo:[1,0,0] neg_hi:[1,0,0]\n " \
  "v_pk_fma_f32 v[204:205], v[80:81], v[40:41], v[146:147] op_sel_hi:[1,0,0] neg_lo:[1,0,0] neg_hi:[1,0,0]\n " \
  "v_pk_fma_f32 v[206:207], v[82:83], v[40:41], v[146:147] op_sel_hi:[1,0,0] neg_lo:[1,0,0] neg_hi:[1,0,0]\n " \
  "v_pk_fma_f32 v[172:173], v[172:173], v[40:41], v[40:41] op_sel:[0,0,1] op_sel_hi:[1,0,1]\n " \
  "v_pk_fma_f32 v[174:175], v[174:175], v[40:41], v[40:41] op_sel:[0,0,1] op_sel_hi:[1,0,1]\n " \
  "v_pk_fma_f32 v[80:81], v[80:81], v[40:41], v[40:41] op_sel:[0,0,1] op_sel_hi:[1,0,1]\n " \
  "v_pk_fma_f32 v[82:83], v[82:83], v[40:41], v[40:41] op_sel:[0,0,1] op_sel_hi:[1,0,1]\n ds_write_b128 v55, v[176:179] offset:0\n " \
  "v_exp_f32 v200, v200\n v_exp_f32 v201, v201\n v_exp_f32 v202, v202\n v_exp_f32 v203, v203\n v_exp_f32 v204, v204\n v_exp_f32 v205, v205\n " \
  "v_exp_f32 v206, v206\n v_exp_f32 v207, v207\n v_exp_f32 v172, v172\n v_exp_f32 v173, v173\n v_exp_f32 v174, v174\n v_exp_f32 v175, v175\n " \
  "v_exp_f32 v80, v80\n v_exp_f32 v81, v81\n v_exp_f32 v82, v82\n v_exp_f32 v83, v83\n " \
  "v_pk_fma_f32 v[248:249], v[196:197], v[42:43], v[146:147] op_sel:[0,0,1] op_sel_hi:[1,0,1] neg_lo:[1,0,0] neg_hi:[1,0,0]\n " \
  "v_pk_fma_f32 v[250:251], v[198:199], v[42:43], v[146:147] op_sel:[0,0,1] op_sel_hi:[1,0,1] neg_lo:[1,0,0] neg_hi:[1,0,0]\n " \
  "v_pk_fma_f32 v[252:253], v[84:85], v[42:43], v[146:147] op_sel:[0,0,1] op_sel_hi:[1,0,1] neg_lo:[1,0,0] neg_hi:[1,0,0]\n " \
  "v_pk_fma_f32 v[254:255], v[86:87], v[42:43], v[146:147] op_sel:[0,0,1] op_sel_hi:[1,0,1] neg_lo:[1,0,0] neg_hi:[1,0,0]\n " \
  "v_pk_fma_f32 v[196:197], v[196:197], v[42:43], v[42:43] op_sel:[0,0,1] op_sel_hi:[1,0,1]\n " \
  "v_pk_fma_f32 v[198:199], v[198:199], v[42:43], v[42:43] op_sel:[0,0,1] op_sel_hi:[1,0,1]\n " \
  "v_pk_fma_f32 v[84:85], v[84:85], v[42:43], v[42:43] op_sel:[0,0,1] op_sel_hi:[1,0,1]\n " \
  "v_pk_fma_f32 v[86:87], v[86:87], v[42:43], v[42:43] op_sel:[0,0,1] op_sel_hi:[1,0,1]\n ds_write_b128 v55, v[180:183] offset:1024\n " \
  "v_exp_f32 v248, v248\n v_exp_f32 v249, v249\n v_exp_f32 v250, v250\n v_exp_f32 v251, v251\n v_exp_f32 v252, v252\n v_exp_f32 v253, v253\n " \
  "v_exp_f32 v254, v254\n v_exp_f32 v255, v255\n v_exp_f32 v196, v196\n v_exp_f32 v197, v197\n v_exp_f32 v198, v198\n v_exp_f32 v199, v199\n " \
  "v_exp_f32 v84, v84\n v_exp_f32 v85, v85\n v_exp_f32 v86, v86\n v_exp_f32 v87, v87\n " \
  "v_pk_fma_f32 v[128:129], v[220:221], v[44:45], v[144:145] op_sel_hi:[1,0,0] neg_lo:[1,0,0] neg_hi:[1,0,0]\n " \
  "v_pk_fma_f32 v[130:131], v[222:223], v[44:45], v[144:145] op_sel_hi:[1,0,0] neg_lo:[1,0,0] neg_hi:[1,0,0]\n " \
  "v_pk_fma_f32 v[132:133], v[124:125], v[44:45], v[144:145] op_sel_hi:[1,0,0] neg_lo:[1,0,0] neg_hi:[1,0,0]\n " \
  "v_pk_fma_f32 v[134:135], v[126:127], v[44:45], v[144:145] op_sel_hi:[1,0,0] neg_lo:[1,0,0] neg_hi:[1,0,0]\n " \
  "v_pk_fma_f32 v[220:221], v[220:221], v[44:45], v[44:45] op_sel:[0,0,1] op_sel_hi:[1,0,1]\n " \
  "v_pk_fma_f32 v[222:223], v[222:223], v[44:45], v[44:45] op_sel:[0,0,1] op_sel_hi:[1,0,1]\n " \
  "v_pk_fma_f32 v[124:125], v[124:125], v[44:45], v[44:45] op_sel:[0,0,1] op_sel_hi:[1,0,1]\n " \
  "v_pk_fma_f32 v[126:127], v[126:127], v[44:45], v[44:45] op_sel:[0,0,1] op_sel_hi:[1,0,1]\n ds_write_b128 v55, v[224:227] offset:8192\n " \
  "v_pk_add_f32 v[216:217], v[172:173], v[174:175]\n v_pk_add_f32 v[218:219], v[80:81], v[82:83]\n v_pk_add_f32 v[216:217], v[216:217], v[218:219]\n " \
  "v_add_f32 v168, v216, v217\n v_pk_add_f32 v[216:217], v[200:201], v[202:203]\n v_pk_add_f32 v[218:219], v[204:205], v[206:207]\n " \
  "v_pk_add_f32 v[216:217], v[216:217], v[218:219]\n v_add_f32 v169, v216, v217\n v_pk_add_f32 v[216:217], v[196:197], v[198:199]\n " \
  "v_pk_add_f32 v[218:219], v[84:85], v[86:87]\n v_pk_add_f32 v[216:217], v[216:217], v[218:219]\n v_add_f32 v170, v216, v217\n " \
  "v_pk_add_f32 v[216:217], v[248:249], v[250:251]\n v_pk_add_f32 v[218:219], v[252:253], v[254:255]\n v_pk_add_f32 v[216:217], v[216:217], v[218:219]\n " \
  "v_add_f32 v171, v216, v217\n v_exp_f32 v128, v128\n v_exp_f32 v129, v129\n v_permlane16_swap_b32 v168, v169\n v_permlane16_swap_b32 v170, v171\n " \
  "v_exp_f32 v130, v130\n v_exp_f32 v131, v131\n v_add_f32 v168, v168, v169\n v_add_f32 v170, v170, v171\n v_exp_f32 v132, v132\n v_exp_f32 v133, v133\n " \
  "v_permlane32_swap_b32 v168, v170\n v_exp_f32 v134, v134\n v_exp_f32 v135, v135\n v_add_f32 v168, v168, v170\n v_exp_f32 v220, v220\n " \
  "v_exp_f32 v221, v221\n v_rcp_f32 v168, v168\n v_exp_f32 v222, v222\n v_exp_f32 v223, v223\n v_mov_b32 v170, v168\n v_exp_f32 v124, v124\n " \
  "v_exp_f32 v125, v125\n v_permlane32_swap_b32 v168, v170\n v_exp_f32 v126, v126\n v_exp_f32 v127, v127\n v_mov_b32 v169, v168\n v_mov_b32 v171, v170\n " \
  "v_pk_fma_f32 v[112:113], v[244:245], v[46:47], v[144:145] op_sel:[0,0,1] op_sel_hi:[1,0,1] neg_lo:[1,0,0] neg_hi:[1,0,0]\n " \
  "v_pk_fma_f32 v[114:115], v[246:247], v[46:47], v[144:145] op_sel:[0,0,1] op_sel_hi:[1,0,1] neg_lo:[1,0,0] neg_hi:[1,0,0]\n " \
  "v_permlane16_swap_b32 v168, v169\n v_permlane16_swap_b32 v170, v171\n " \
  "v_pk_fma_f32 v[116:117], v[36:37], v[46:47], v[144:145] op_sel:[0,0,1] op_sel_hi:[1,0,1] neg_lo:[1,0,0] neg_hi:[1,0,0]\n " \
  "v_pk_fma_f32 v[118:119], v[38:39], v[46:47], v[144:145] op_sel:[0,0,1] op_sel_hi:[1,0,1] neg_lo:[1,0,0] neg_hi:[1,0,0]\n " \
  "v_pk_fma_f32 v[244:245], v[244:245], v[46:47], v[46:47] op_sel:[0,0,1] op_sel_hi:[1,0,1]\n " \
  "v_pk_fma_f32 v[246:247], v[246:247], v[46:47], v[46:47] op_sel:[0,0,1] op_sel_hi:[1,0,1]\n " \
  "v_pk_fma_f32 v[36:37], v[36:37], v[46:47], v[46:47] op_sel:[0,0,1] op_sel_hi:[1,0,1]\n " \
  "v_pk_fma_f32 v[38:39], v[38:39], v[46:47], v[46:47] op_sel:[0,0,1] op_sel_hi:[1,0,1]\n ds_write_b128 v55, v[228:231] offset:9216\n " \
  "v_exp_f32 v112, v112\n v_pk_mul_f32 v[172:173], v[172:173], v[168:169] op_sel_hi:[1,0]\n v_exp_f32 v113, v113\n " \
  "v_pk_mul_f32 v[174:175], v[174:175], v[168:169] op_sel_hi:[1,0]\n v_exp_f32 v114, v114\n v_cvt_pk_bf16_f32 v172, v172, v173\n v_exp_f32 v115, v115\n " \
  "v_cvt_pk_bf16_f32 v173, v174, v175\n v_exp_f32 v116, v116\n ds_write_b64 v58, v[172:173] offset:0\n v_exp_f32 v117, v117\n " \
  "v_pk_mul_f32 v[80:81], v[80:81], v[168:169] op_sel_hi:[1,0]\n v_exp_f32 v118, v118\n v_pk_mul_f32 v[82:83], v[82:83], v[168:169] op_sel_hi:[1,0]\n " \
  "v_exp_f32 v119, v119\n v_cvt_pk_bf16_f32 v80, v80, v81\n v_exp_f32 v244, v244\n v_cvt_pk_bf16_f32 v81, v82, v83\n v_exp_f32 v245, v245\n " \
  "ds_write_b64 v58, v[80:81] offset:32\n v_exp_f32 v246, v246\n v_pk_mul_f32 v[200:201], v[200:201], v[168:169] op_sel:[0,1]\n v_exp_f32 v247, v247\n " \
  "v_pk_mul_f32 v[202:203], v[202:203], v[168:169] op_sel:[0,1]\n v_exp_f32 v36, v36\n v_cvt_pk_bf16_f32 v200, v200, v201\n v_exp_f32 v37, v37\n " \
  "v_cvt_pk_bf16_f32 v201, v202, v203\n v_exp_f32 v38, v38\n ds_write_b64 v58, v[200:201] offset:64\n v_exp_f32 v39, v39\n " \
  "v_pk_mul_f32 v[204:205], v[204:205], v[168:169] op_sel:[0,1]\n v_pk_mul_f32 v[206:207], v[206:207], v[168:169] op_sel:[0,1]\n " \
  "v_cvt_pk_bf16_f32 v204, v204, v205\n v_cvt_pk_bf16_f32 v205, v206, v207\n ds_write_b64 v58, v[204:205] offset:96\n " \
  "v_pk_add_f32 v[216:217], v[220:221], v[222:223]\n v_pk_add_f32 v[218:219], v[124:125], v[126:127]\n v_pk_add_f32 v[216:217], v[216:217], v[218:219]\n " \
  "v_add_f32 v192, v216, v217\n v_pk_add_f32 v[216:217], v[128:129], v[130:131]\n v_pk_add_f32 v[218:219], v[132:133], v[134:135]\n " \
  "v_pk_add_f32 v[216:217], v[216:217], v[218:219]\n v_add_f32 v193, v216, v217\n v_pk_add_f32 v[216:217], v[244:245], v[246:247]\n " \
  "v_pk_add_f32 v[218:219], v[36:37], v[38:39]\n v_pk_add_f32 v[216:217], v[216:217], v[218:219]\n v_add_f32 v194, v216, v217\n " \
  "v_pk_add_f32 v[216:217], v[112:113], v[114:115]\n v_pk_add_f32 v[218:219], v[116:117], v[118:119]\n v_pk_add_f32 v[216:217], v[216:217], v[218:219]\n " \
  "v_add_f32 v195, v216, v217\n v_pk_mul_f32 v[196:197], v[196:197], v[170:171] op_sel_hi:[1,0]\n " \
  "v_pk_mul_f32 v[198:199], v[198:199], v[170:171] op_sel_hi:[1,0]\n v_permlane16_swap_b32 v192, v193\n v_permlane16_swap_b32 v194, v195\n " \
  "v_cvt_pk_bf16_f32 v196, v196, v197\n v_cvt_pk_bf16_f32 v197, v198, v199\n v_add_f32 v192, v192, v193\n v_add_f32 v194, v194, v195\n " \
  "ds_write_b64 v58, v[196:197] offset:2304\n v_pk_mul_f32 v[84:85], v[84:85], v[170:171] op_sel_hi:[1,0]\n v_permlane32_swap_b32 v192, v194\n " \
  "v_pk_mul_f32 v[86:87], v[86:87], v[170:171] op_sel_hi:[1,0]\n v_cvt_pk_bf16_f32 v84, v84, v85\n v_add_f32 v192, v192, v194\n " \
  "v_cvt_pk_bf16_f32 v85, v86, v87\n ds_write_b64 v58, v[84:85] offset:2336\n v_rcp_f32 v192, v192\n " \
  "v_pk_mul_f32 v[248:249], v[248:249], v[170:171] op_sel:[0,1]\n v_pk_mul_f32 v[250:251], v[250:251], v[170:171] op_sel:[0,1]\n v_mov_b32 v194, v192\n " \
  "v_cvt_pk_bf16_f32 v248, v248, v249\n v_cvt_pk_bf16_f32 v249, v250, v251\n v_permlane32_swap_b32 v192, v194\n " \
  "ds_write_b64 v58, v[248:249] offset:2368\n v_pk_mul_f32 v[252:253], v[252:253], v[170:171] op_sel:[0,1]\n v_mov_b32 v193, v192\n " \
  "v_mov_b32 v195, v194\n v_pk_mul_f32 v[254:255], v[254:255], v[170:171] op_sel:[0,1]\n v_cvt_pk_bf16_f32 v252, v252, v253\n " \
  "v_permlane16_swap_b32 v192, v193\n v_permlane16_swap_b32 v194, v195\n v_cvt_pk_bf16_f32 v253, v254, v255\n ds_write_b64 v58, v[252:253] offset:2400\n " \
  "v_pk_mul_f32 v[220:221], v[220:221], v[192:193] op_sel_hi:[1,0]\n v_pk_mul_f32 v[222:223], v[222:223], v[192:193] op_sel_hi:[1,0]\n " \
  "v_cvt_pk_bf16_f32 v220, v220, v221\n v_cvt_pk_bf16_f32 v221, v222, v223\n ds_write_b64 v58, v[220:221] offset:18432\n " \
  "v_pk_mul_f32 v[124:125], v[124:125], v[192:193] op_sel_hi:[1,0]\n v_pk_mul_f32 v[126:127], v[126:127], v[192:193] op_sel_hi:[1,0]\n " \
  "v_cvt_pk_bf16_f32 v124, v124, v125\n v_cvt_pk_bf16_f32 v125, v126, v127\n ds_write_b64 v58, v[124:125] offset:18464\n " \
  "v_pk_mul_f32 v[128:129], v[128:129], v[192:193] op_sel:[0,1]\n v_pk_mul_f32 v[130:131], v[130:131], v[192:193] op_sel:[0,1]\n " \
  "v_cvt_pk_bf16_f32 v128, v128, v129\n v_cvt_pk_bf16_f32 v129, v130, v131\n ds_write_b64 v58, v[128:129] offset:18496\n " \
  "v_pk_mul_f32 v[132:133], v[132:133], v[192:193] op_sel:[0,1]\n v_pk_mul_f32 v[134:135], v[134:135], v[192:193] op_sel:[0,1]\n " \
  "v_cvt_pk_bf16_f32 v132, v132, v133\n v_cvt_pk_bf16_f32 v133, v134, v135\n ds_write_b64 v58, v[132:133] offset:18528\n " \
  "v_pk_mul_f32 v[244:245], v[244:245], v[194:195] op_sel_hi:[1,0]\n v_pk_mul_f32 v[246:247], v[246:247], v[194:195] op_sel_hi:[1,0]\n " \
  "v_cvt_pk_bf16_f32 v244, v244, v245\n v_cvt_pk_bf16_f32 v245, v246, v247\n ds_write_b64 v58, v[244:245] offset:20736\n " \
  "v_pk_mul_f32 v[36:37], v[36:37], v[194:195] op_sel_hi:[1,0]\n v_pk_mul_f32 v[38:39], v[38:39], v[194:195] op_sel_hi:[1,0]\n " \
  "v_cvt_pk_bf16_f32 v36, v36, v37\n v_cvt_pk_bf16_f32 v37, v38, v39\n ds_write_b64 v58, v[36:37] offset:20768\n " \
  "v_pk_mul_f32 v[112:113], v[112:113], v[194:195] op_sel:[0,1]\n v_pk_mul_f32 v[114:115], v[114:115], v[194:195] op_sel:[0,1]\n " \
  "v_cvt_pk_bf16_f32 v112, v112, v113\n v_cvt_pk_bf16_f32 v113, v114, v115\n ds_write_b64 v58, v[112:113] offset:20800\n " \
  "v_pk_mul_f32 v[116:117], v[116:117], v[194:195] op_sel:[0,1]\n v_pk_mul_f32 v[118:119], v[118:119], v[194:195] op_sel:[0,1]\n " \
  "v_cvt_pk_bf16_f32 v116, v116, v117\n v_cvt_pk_bf16_f32 v117, v118, v119\n ds_write_b64 v58, v[116:117] offset:20832\n "
#define G1_PURGE \
  ".purgem G1M\n .purgem G1RA\n .purgem G1RB\n .purgem G1BK\n .purgem G1AK\n .purgem G1RDB\n .purgem G1RDA\n .purgem G1Q\n .purgem G1SG\n "
#define G1_CLOBBER \
  "v80", "v81", "v82", "v83", "v84", "v85", "v86", "v87", "v92", "v93", "v94", "v95", "v96", "v97", "v98", "v99", "v100", "v101", "v102", "v103", \
  "v112", "v113", "v114", "v115", "v116", "v117", "v118", "v119", "v120", "v121", "v122", "v123", "v124", "v125", "v126", "v127", "v128", "v129", \
  "v130", "v131", "v132", "v133", "v134", "v135", "v144", "v145", "v146", "v147", "v152", "v153", "v154", "v155", \
  "v156", "v157", "v158", "v159"

template <int MODE>
__device__ __forceinline__ void g1_phase(char* smem, char* ws, u64& E, int w, f32x16& xq, Next& nx, int layer, const u16* __restrict__ x0, const u16* __restrict__ W,
                                         const u16* __restrict__ wo, const u16* __restrict__ wphi,
                                         const u16* __restrict__ wn1, const u16* __restrict__ wn2) {
  constexpr bool HH = (MODE == 1);
  constexpr int ABYTES = 128 * 128;
  constexpr int STAGE = ABYTES + 3 * 8192;
  constexpr int OFF_FX = 16384;
  constexpr int OFF_SP = 3 * STAGE;
  constexpr int OFF_RS = !HH ? 159744 : OFF_SP + 16384;
  constexpr int OFF_CS = OFF_RS + (HH ? 1152 : 1024);
  constexpr int OFF_WP = OFF_CS + 1536;
  constexpr int OFF_JK = LDS_TOTAL - 1024;
  constexpr int OFF_PK = 0;
  constexpr int OFF_VF = OFF_WP + 4096;
  constexpr int OFF_KV = 16384;
  constexpr int OFF_R2 = HH ? OFF_VF + 16384 : OFF_CS + 1024;
  const int XS = OFF_WO + 9 * 8192 + (w & 3) * 2048;
  constexpr int OFF_RD = OFF_CS + 768;
  constexpr int PSTR = 144;
  IDS
  W = uni(W); wphi = uni(wphi); wn1 = uni(wn1); wn2 = uni(wn2);
  const int kg = w >> 2, wm = (w >> 1) & 1, wn = w & 1;
  const bool first = !HH && layer == 0;
  f32x4 acc[2][2], qh[4], csq[2], st01, st23;
  bf16x8 wf[2];
  u32x4 pw[8];
  ST(layer * 24 + 0)
  const int R0 = wm * 64 + (HH ? kg * 32 : 0);
  u32x16 pa;
#define pcs pa[0]
#define prs pa[1]
#define opo pa[2]
#define ogo pa[3]
#define pkw pa[5]
#define hof pa[7]
#define dm pa[8]
#define dm2 pa[9]
#define pcv pa[4]
#define pwf pa[5]
#define prv pa[6]
#define pvf pa[7]
#define ppk pa[10]
#define ppa pa[11]
#define pvr pa[12]
#define gkv pa[13]
#define gkz pa[14]
#define pwl pa[15]
#define pwm pa[4]
#define plb pa[10]
#define prd pa[11]
#define dm3 pa[12]
  f32x4 bias[3];
  const char* wos = wo_src(ws, wo, layer, hp);
  const unsigned tO = img(layer, hp) ? 8192u : 131072u;
  const unsigned sb = lds_addr(smem);
  auto prep = [&]() {

    pcs = sb + OFF_CS + wn * 128 + g * 16;
    prs = sb + OFF_RS + (R0 + fr) * 8;
    opo = (unsigned)(wn * 8192 + R0 * 64 + lane * 16);
    ogo = opo + WS_O + layer * (SEQ * DM * 2) + (unsigned)((rt * NH + 2 * hp) * 8192);
    opo += sb;
    pcv = sb + OFF_CS + (wn * 32 + 128 + fr) * 4;
    prv = sb + OFF_R2 + (R0 + 4 * g) * 4;
    if (layer < 2) {
      constexpr unsigned kvn = WS_KV + MODE * KVLB;
      const int t = tid & 255;
      dm = kvn + (unsigned)((rt * NH + 2 * hp) * KVPB) + t * 16;
      dm2 = t < 16 ? dm + 8192 : t >= 64 && t < 97 ? kvn + 16u * NH * KVPB + (unsigned)(hp * KVFB + rt * 528 + (t - 64) * 16) : 0x7fff0000u;
      if (!HH) dm3 = WS_HL + (unsigned)((256 + rt * 16 + hp) * 4096 + t * 16);
    }
    if constexpr (!HH) {
      plb = sb + OFF_FX + wn * 2048 + lane * 16;
      prd = sb + OFF_RD + wn * 4;
      hof = m0 + wm == 0 ? 0x7fff0000u : WS_HL + (unsigned)((layer * 128 + rt * 16 - 16 + wm * 16 + hp) * 4096 + wn * 2048 + lane * 16);
      const unsigned out = ~((0x5555u << 2 << fr) >> (4 * g));
#pragma unroll
      for (int i = 0; i < 8; ++i) {
        unsigned t;
        asm("v_bfe_i32 %0, %1, %2, 1" : "=v"(t) : "v"(out), "n"((i >> 2) * 16 + (i & 3)));
        bias[i >> 2][i & 3] = __uint_as_float(t & 0xff61b1e6u);
      }
      bias[2] = m0 + R0 == 0 ? f32x4{-3.0e38f, -3.0e38f, -3.0e38f, -3.0e38f} : bias[0];
    } else {
      const int u = wm * 2 + kg;
      pwf = sb + OFF_WP + wn * 2048 + (4 * g + q4) * 64 + p4 * 8;
      pvf = sb + OFF_VF + w * 2048 + lane * 16;
      ppk = sb + OFF_PK + w * (32 * PSTR) + fr * PSTR + g * 8;
      ppa = sb + OFF_PK + wn * (32 * PSTR) + (4 * g + q4) * PSTR + u * 32 + p4 * 8;
      pvr = sb + OFF_VF + wn * 2048 + lane * 16;
      const unsigned doff = WS_KV + (layer >> 1) * KVLB + (unsigned)((rt * NH + 2 * hp + wn) * KVPB);
      pwl = img(layer, hp) ? (unsigned)((w & 3) * 2048 + lane * 16) : (unsigned)(((16 * (w & 3) + (lane >> 3)) * DM + hp * 64 + ((lane & 7) ^ ((lane >> 4) << 1)) * 8) * 2);
      gkv = doff + u * 1024 + lane * 16;
      gkz = fr == 0 ? doff + 4096 + u * 32 + g * 8 : 0x7fff0000u;
    }
    asm volatile("" : "+{v[48:63]}"(pa), "+{v[36:39]}"(bias[0]), "+{v[40:43]}"(bias[1]), "+{v[44:47]}"(bias[2]));
  };

  auto o_put = [&](int rb, const f32x4& a, const f32x4& b) {
    const u32x4 v = {pk2(a[0], a[1]), pk2(a[2], a[3]), pk2(b[0], b[1]), pk2(b[2], b[3])};
    __builtin_amdgcn_raw_buffer_store_b128(v, rsW, ogo + rb * 1024, 0, 16);
    lds<u32x4>(opo + rb * 1024) = v;
  };

  if (kg == 1) {

    const int lw = w & 3;
    const int kB = 16 * lw + (lane >> 3);
    const int fB = (kB >> 1) & 1;
    const int cB0 = ((((lane & 7) >> 1) ^ fB) << 1) | (lane & 1), cB1 = ((((lane & 7) >> 1) ^ (fB | 2)) << 1) | (lane & 1);
    const bool im = img(layer, hp);

    unsigned vW0, vW1, vP, vX, vY;
    u32x4 jk[2];
    u64 ko2;
    auto ld_pw = [&](int i0) {
#pragma unroll
      for (int i = i0; i < i0 + 4; ++i) gld16(pw[i], vP, wn1 + i * 8 * 3072);
    };
    auto wo_tile = [&](int t) { glds2v(wos + knib(ko2, t) * tO, vW0, vW1, lds_addr(smem) + OFF_WO + t * 8192 + lw * 2048); };
    auto run = [&](auto fst) {
      constexpr bool FIRST = decltype(fst)::value;
      constexpr int N = 10;
      const unsigned vA = FIRST ? (unsigned)(((m0 + lw * 32 + (lane >> 3)) * DM + ((lane & 7) ^ (lane >> 3)) * 8) * 2)
                                : (unsigned)(rt * 262144 + lw * 4096 + lane * 16);
      unsigned imv = !FIRST && im ? ~0u : 0u;
      if (!FIRST) asm volatile("" : "+v"(imv));
      const unsigned vB0 = ((unsigned)(lw * 6144 + lane * 16) & imv) | ((unsigned)((kB * 3072 + hp * 64 + cB0 * 8) * 2) & ~imv);
      const unsigned vB1 = ((unsigned)(lw * 6144 + lane * 16) & imv) | (((unsigned)(((kB + 8) * 3072 + hp * 64 + cB1 * 8) * 2) - 1024u) & ~imv);
      const char* xa = FIRST ? (const char*)x0 : ws + WS_X + (layer - 1) * (SEQ * DM * 2);
      constexpr int KS = FIRST ? 128 : 16384;
      const u64 kord = FIRST ? korder0(rt) : nx.kord;
      auto ktile = [&](int ks) { return knib(kord, ks); };
      auto stageA = [&](int ks, int boff) {
        const int kt = ktile(ks);
        const unsigned sA = lds_addr(smem) + boff;
        if (FIRST) glds4v(xa + kt * KS, vA, vA + 15360u, vA + 2 * 15360u, vA + 3 * 15360u, sA + lw * 4096);
        else glds4c(xa + kt * KS, vA, sA + lw * 4096);
      };
      auto stageB = [&](const u16* Wp, int ks, int boff) {
        const int kt = ktile(ks);
        const unsigned sB = lds_addr(smem) + boff + ABYTES;
        const char* b = FIRST ? (const char*)(Wp + (size_t)kt * 64 * 3072) : nx.wq + kt * nx.tw;
        glds4v(b, vB0, vB1, vB0, vB1, sB + lw * 6144);
        glds2v(b + 4096, vB0, vB1, sB + lw * 6144 + 4096);
      };
      bf16x8 sel[3];
      unsigned cso[3], cst;
      if (FIRST) {
        stageA(1, STAGE); stageB(W, 1, STAGE);
        __builtin_amdgcn_sched_barrier(0);
        const int fl = (q4 >> 1) | ((g & 1) << 1), j = 3 * wm + (fr < 3 ? fr : 0);
#pragma unroll
        for (int i = 0; i < 3; ++i) {
          const unsigned o = fr == i ? 0x3F803F80u : 0u;
          sel[i] = __builtin_bit_cast(bf16x8, u32x4{o, o, o, o});
          cso[i] = (unsigned)(2 * STAGE + ABYTES + (g >> 1) * 6144 + (g & 1) * 1024 + q4 * 128 + p4 * 8 + (((wn * 2 + ((wm + i) & 1)) ^ fl) << 5) + ((3 * wm + i) >> 1) * 2048);
          asm volatile("" : "+v"(sel[i]), "+v"(cso[i]));
        }
        cst = fr < 3 ? WS_CPA + (unsigned)(((hp * 16 + rt) * 192 + (j >> 1) * 64 + wn * 32 + (j & 1) * 16 + 4 * g) * 4) : 0x7fff0000u;
        asm volatile("" : "+v"(cst));
        WAITVN(N);
        BAR;
        stageA(2, 2 * STAGE); stageB(W, 2, 2 * STAGE);
      }
      u64 fall, fal2;
      unsigned fao, fa2, vS, vC, dS, dC, dR;
      auto setup = [&]() {
        fao = (unsigned)((256 + rt * 16 + (lane & 15)) * FLS);
        fa2 = (unsigned)(((FIRST ? 512 : 256) + (lane & 15) * 16 + (FIRST ? hp : (hp - ((1 << (lane >> 4)) >> 1)) & 15)) * FLS);
        vP = (unsigned)((rt * 64 + (lane >> 3)) * 6144 + lw * 2048 + ((hp + (1 << lw)) & 15) * 128 + (lane & 7) * 16);
        vS = FIRST ? (unsigned)(WS_G0 + (m0 + lw * 32 + (lane & 31)) * 16)
                   : WS_ST + (unsigned)((layer - 1) * (SEQ * 128) + rt * 16384 + g * 1024 + lw * 256 + fr * 16);
        vC = (FIRST ? WS_CPA : WS_CSP) + (unsigned)(hp * 12288 + g * 768 + lw * 192 + (fr < 12 ? fr : 0) * 16);
        if (!FIRST) vX = HH ? (unsigned)(hp * 4096 + lw * 1024 + lane * 16) : (unsigned)((rt * 16 + hp) * 1024 + lane * 16);
        if (HH) { vY = (unsigned)(bid * 8192 + lw * 2048 + lane * 16); asm volatile("" : "+v"(vY)); }
        vW0 = ((unsigned)(lw * 2048 + lane * 16) & imv) | ((unsigned)((kB * DM + hp * 64 + ((lane & 7) ^ ((lane >> 4) << 1)) * 8) * 2) & ~imv); vW1 = vW0 + ((8 * DM * 2 - 1024u) & ~imv);
        dS = OFF_RS + lw * 256 + (FIRST ? (lane & 31) * 8 : fr * 16 + (g & 1) * 8); dC = OFF_CS + lw * 192 + (fr < 12 ? fr : 0) * 16 + g * 4;
        ko2 = korder(bid & 3, hp);
        dR = OFF_R2 + lw * 128 + (FIRST ? (lane & 31) * 4 : fr * 8 + (g & 1) * 4);
        asm volatile("" : "+v"(dR));
        asm volatile("" : "+v"(vS), "+v"(vX), "+v"(vW0), "+v"(vW1), "+v"(dS));
        asm volatile("" : "+s"(ko2));
        if (FIRST || HH) asm volatile("" : "+v"(vC), "+v"(dC), "+v"(fa2));
        if (FIRST) asm volatile("" : "+v"(vP)); else asm volatile("" : "+v"(fao));
      };
      if (!FIRST) setup();
      u32x4 sv[4], cv[4];
      auto ld_c = [&](u32x4& d, unsigned v, int so) { asm volatile("buffer_load_dwordx4 %0, %1, %2, %3 offen sc1" : "=v"(d) : "v"(v), "s"(rsW), "s"(so) : "memory"); };
      float tm, tr;
      unsigned tc;
      auto fin = [&]() {
        if (FIRST) {
          const unsigned tag = (unsigned)E + 1u;
          asm volatile("" : "+v"(sv[0]));
          _Pragma("unroll 1") for (unsigned spins = 0; spins < (1u << 18); ++spins) {
            if (__all(sv[0][1] == tag && sv[0][3] == tag)) break;
            __builtin_amdgcn_s_sleep(1);
            asm volatile("buffer_load_dwordx4 %0, %1, %2, 0 offen sc1\n\ts_waitcnt vmcnt(0)" : "=v"(sv[0]) : "v"(vS), "s"(rsW) : "memory");
          }
          tm = __uint_as_float(sv[0][0]); tr = __uint_as_float(sv[0][2]);
        } else {
          asm volatile("" : "+v"(sv[0]), "+v"(sv[1]), "+v"(sv[2]), "+v"(sv[3]));
          const f32x4 t = sum4(sv);
          const unsigned u = xs4(t[0], t[2], t[1], t[3]);
          auto h = __builtin_amdgcn_permlane32_swap(u, u, false, false);
          const float mean = __uint_as_float(h[0]) * (1.0f / 1024.0f);
          const float var = fmaxf(__uint_as_float(h[1]) * (1.0f / 1024.0f) - mean * mean, 0.f);
          tm = -mean; tr = __builtin_amdgcn_rsqf(var + 1e-5f);
        }
      };
      auto cfin = [&]() {
        if (FIRST || HH) {
          asm volatile("" : "+v"(cv[0]), "+v"(cv[1]), "+v"(cv[2]), "+v"(cv[3]));
          const f32x4 t = sum4(cv);
          tc = xs4(t[0], t[1], t[2], t[3]);
        }
      };
      bf16x8 cf[6];
      f32x4 csum = {0, 0, 0, 0};
      if (FIRST) { fall = E + 1u; vX = (unsigned)(bid * 8192 + lw * 2048 + lane * 16); asm volatile("" : "+v"(fall), "+v"(vX)); }
#pragma unroll
      for (int kt = 0; kt < 13; ++kt) {
        const int cur = (kt % 3) * STAGE;
        if (!FIRST && kt == 0) { WAITV(4); }
        else if (FIRST && kt == 4 && lw == 1) { WAITVN(N + 2); }
        else if (HH && (kt == 4 || kt == 6) && true) { WAITVN(N + 1); }
        else if (HH && kt == 5 && true) { WAITVN(N + 2); }
        else if ((!FIRST && kt == 1 && lw == 3) || ((FIRST || HH) && kt == 10 && lw == 3) || (FIRST && (kt == 2 || kt == 4 || kt == 5))) { WAITVN(N + 1); }
        else { WAITVN(N); }
        if (!FIRST && kt == 2 && lw == 3) {
          asm volatile("" : "+v"(fall));
          if (__any(fall != E + (unsigned)layer)) poll_flags(ws, E, 1, rt, hp, lane, layer, 0xffffull);
        }
        if (HH && kt == 11 && lw == 3) {
          asm volatile("" : "+v"(fal2));
          if (__any(fal2 != E + (unsigned)layer)) {
            _Pragma("unroll 1") for (int j = 0; j < 4; ++j) poll_flags(ws, E, 1, rt, (hp - ((1 << j) >> 1)) & 15, lane, layer, 0xffff00000000ull);
          }
        }
        if (FIRST && kt == 11 && lw == 3) {
          asm volatile("" : "+v"(fal2));
          if (__any(fal2 != E + 1u)) poll_flags(ws, E, 2, rt, hp, lane, 1u, 0xffff00000000ull);
        }
        BAR;
        if (FIRST && kt == 1) {
          asm volatile("" ::: "memory");
#pragma unroll
          for (int i = 0; i < 6; ++i) { const char* t = smem + cso[i >> 1] + (i & 1) * 12288; cf[i] = cat8(tr_read(t), tr_read(t + 512)); }
        }
        if (FIRST && kt >= 2 && kt < 6 && lw == kt - 2) setup();
        if (kt >= 6 && kt < 10 && lw == kt - 6) prep();
        stageA(kt + 3, cur);
        if (FIRST && kt == 1) {
          asm volatile("" : "+v"(sel[0]));
#pragma unroll
          for (int i = 0; i < 6; ++i) csum = MFMA(cf[i], sel[i >> 1], csum);
          asm volatile("" : "+v"(csum));
        }
        stageB(W, kt + 3, cur);
        if (FIRST) {
          if (kt == 1) __builtin_amdgcn_raw_buffer_store_b128(__builtin_bit_cast(u32x4, csum), rsW, cst, 0, 16);
          if (kt == 3 && tid == 320) __builtin_amdgcn_raw_buffer_store_b64(__builtin_bit_cast(u32x2, fall), rsW, WS_FLAGS + (unsigned)(512 + rt * 16 + hp) * FLS, 0, 16);
          if (kt == 3 || kt == 4) glds16s(wo + (kt - 3) * 512, vX, lds_addr(smem) + OFF_JK);
        }
        if (HH && (kt == 3 || kt == 4) && true) glds16s(wo + (kt - 3) * 512, vY, lds_addr(smem) + OFF_JK);
        if (!FIRST && kt == 0 && lw == 3) asm volatile("global_load_dwordx2 %0, %1, %2 sc1" : "=v"(fall) : "v"(fao), "s"(ws + WS_FLAGS) : "memory");
        if ((FIRST || HH) && kt == 9 && lw == 3) asm volatile("global_load_dwordx2 %0, %1, %2 sc1" : "=v"(fal2) : "v"(fa2), "s"(ws + WS_FLAGS) : "memory");
      }
      WAITVN(N);
      BAR;
#pragma unroll
      for (int i = 0; i < (FIRST ? 1 : 4); ++i) ld_c(sv[i], vS, i * 4096);
      if (FIRST || HH) {
#pragma unroll
        for (int i = 0; i < 4; ++i) ld_c(cv[i], vC, i * 3072);
      }
      if (HH) glds16s(wphi, vX, lds_addr(smem) + OFF_WP + lw * 1024);
      else if (!FIRST) glds16c(ws + WS_CSA, vX, lds_addr(smem) + OFF_CS);
      wo_tile(0); wo_tile(1);
      WAITVN(HH ? 13 : 9);
      BAR;
      if (FIRST) {
        if (lw < 3) { ld_pw(0); ld_pw(4); WAITV(12); } else { WAITV(4); }
      } else {
        WAITV(4);
      }
      cfin();
      fin();
      asm volatile("" : "+v"(tm), "+v"(tr));
      if (FIRST || HH) asm volatile("" : "+v"(tc));
      if (FIRST && lw < 3) { WAITV(8); } else { WAITV(0); }
      lds<f32x2>(sb + dS) = f32x2{tm, tr};
      lds<float>(sb + dR) = tm; lds<float>(sb + dR + 512) = tr;
      if (FIRST || HH) lds<unsigned>(sb + dC) = tc;
      if (!HH) lds<unsigned>(prd) = 0u;
      WAITL0;
    };
    typedef std::integral_constant<bool, true> T_;
    typedef std::integral_constant<bool, false> F_;
    __builtin_amdgcn_s_setprio(3);
    if (first) run(T_{}); else run(F_{});
    __builtin_amdgcn_s_setprio(0);
    BAR;
    if constexpr (HH) {
      u32x4 c0, c1;
      if (layer == 1) {
        const int n = bid * 8 + lw * 2;
        const void* p;
        unsigned a0, a1;
        if (n < 1536) {
          const int r = n % 768, q = r % 384, e = q % 24;
          p = n < 768 ? wn1 : W;
          a0 = (unsigned)((((q / 24) * 64 + (e / 6) * 16 + (lane >> 3)) * 3072 + ((e % 6) >> 1) * 1024 + (r < 384 ? 192 : 704)) * 2);
          a1 = a0 + 8 * 6144 + cB1 * 16; a0 += cB0 * 16;
        } else {
          p = n < 1792 ? wn2 : wo;
          a0 = (unsigned)((((n & 127) * 8 + (lane >> 3)) * DM + ((n & 128) ? 704 : 192) + ((lane & 7) ^ ((lane >> 4) << 1)) * 8) * 2);
          a1 = a0 + 8 * DM * 2;
        }
        p = uni(p);
        gld16(c0, a0, p); gld16(c1, a1, p);
        const u32x4 pz = empty4();
        __builtin_amdgcn_raw_buffer_store_b128(pz, rsW, dm, 0, 16);
        __builtin_amdgcn_raw_buffer_store_b128(pz, rsW, dm, 4096, 16);
        __builtin_amdgcn_raw_buffer_store_b128(pz, rsW, dm2, 0, 16);
      }
#pragma unroll
      for (int t = 2; t < 6; ++t) wo_tile(t);
      if (layer == 1) {
        WAITV(11);
        __builtin_amdgcn_sched_barrier(0);
        asm volatile("" : "+v"(c0), "+v"(c1));
        __builtin_amdgcn_raw_buffer_store_b128(c0, rsW, WS_IMG + (unsigned)(bid * 8192 + lw * 2048 + lane * 16), 0, 16);
        __builtin_amdgcn_raw_buffer_store_b128(c1, rsW, WS_IMG + (unsigned)(bid * 8192 + lw * 2048 + 1024 + lane * 16), 0, 16);
        WAITV(0);
      }
      asm volatile("" : "=v"(csq[0]), "=v"(csq[1]), "=v"(st01), "=v"(st23), "=v"(wf[0]), "=v"(wf[1]));
      asm volatile("" : "=v"(acc[0][0]), "=v"(acc[0][1]), "=v"(acc[1][0]), "=v"(acc[1][1]));
      asm volatile("" : "=v"(qh[0]), "=v"(qh[1]), "=v"(qh[2]), "=v"(qh[3]));
    } else {
      const int lane = lane_id();
      if (WSLEEP) __builtin_amdgcn_s_sleep(WSLEEP);
#pragma unroll
      for (int t = 2; t < 12; ++t) {
        wo_tile(t);
        if (t == 2 && first) {
        const u32x4 pz = empty4();
        __builtin_amdgcn_raw_buffer_store_b128(pz, rsW, dm, 0, 16);
        __builtin_amdgcn_raw_buffer_store_b128(pz, rsW, dm, 4096, 16);
        __builtin_amdgcn_raw_buffer_store_b128(pz, rsW, dm3, 0, 16);
        __builtin_amdgcn_raw_buffer_store_b128(pz, rsW, dm2, 0, 16);
        if (lw == 3 && lane < 48) __builtin_amdgcn_raw_buffer_store_b128(lds<u32x4>(sb + OFF_CS + lane * 16), rsW, WS_CSA + (unsigned)((rt * 16 + hp) * 1024 + lane * 16), 0, 16);
        }
      }
      BAR;
      if (first) { WAITV(0); }
    }
    asm volatile("" : "={v[64:79]}"(xq));
  } else {

    prep();
    if (first) {
      const int row = m0 + hp * 8 + w * 2 + (lane >> 5);
      asm volatile("s_load_dwordx2 %0, %1, 0x0\n\ts_waitcnt lgkmcnt(0)" : "=s"(E) : "s"(ws) : "memory");
      const unsigned tag = (unsigned)E + 1u;
      static_assert(HH || (OFF_SP == 122880 && OFF_R2 + 1024 <= OFF_JK), "addresses used at the top of the kernel");
      WAITV(0);
      u32x4 xs[4];
#pragma unroll
      for (int i = 0; i < 4; ++i) xs[i] = *(const u32x4*)(smem + OFF_SP + w * 4096 + (lane >> 5) * 2048 + (lane & 31) * 16 + i * 512);
      typedef __attribute__((ext_vector_type(2))) __bf16 bf2;
      const bf2 one2 = __builtin_bit_cast(bf2, 0x3F803F80u);
      float sa[2] = {0.f, 0.f}, sq[2] = {0.f, 0.f};
#pragma unroll
      for (int i = 0; i < 16; ++i) {
        const unsigned u = xs[i >> 2][i & 3];
        const bf2 t = __builtin_bit_cast(bf2, u);
        sa[i & 1] = __builtin_amdgcn_fdot2_f32_bf16(t, one2, sa[i & 1], false);
        sq[i & 1] = __builtin_amdgcn_fdot2_f32_bf16(t, t, sq[i & 1], false);
      }
      float sm = r16_sum(sa[0] + sa[1]), m2 = r16_sum(sq[0] + sq[1]);
      {
        auto r = __builtin_amdgcn_permlane16_swap(__float_as_uint(sm), __float_as_uint(sm), false, false);
        auto q = __builtin_amdgcn_permlane16_swap(__float_as_uint(m2), __float_as_uint(m2), false, false);
        sm = __uint_as_float(r[0]) + __uint_as_float(r[1]); m2 = __uint_as_float(q[0]) + __uint_as_float(q[1]);
      }
      const float mean = sm * (1.0f / 1024.0f);
      m2 = fmaxf(m2 - sm * mean, 0.f);
      u32x4 gw = {__float_as_uint(-mean), tag, __float_as_uint(rsqrtf(m2 * (1.0f / 1024.0f) + 1e-5f)), tag};
      if ((lane & 31) == 0) __builtin_amdgcn_raw_buffer_store_b128(gw, rsW, (unsigned)(WS_G0 + row * 16), 0, 16);
      __builtin_amdgcn_raw_buffer_store_b128(empty4(), rsW, WS_HL + (unsigned)((rt * 16 + hp) * 4096 + tid * 16), 0, 16);
    }
    const int fl = (q4 >> 1) | ((g & 1) << 1);
    const unsigned bo = lds_addr(smem) + ABYTES + (g >> 1) * 6144 + (g & 1) * 1024 + q4 * 128 + p4 * 8;
    const unsigned bo0 = bo + (((wn * 2 + 0) ^ fl) << 5), bo1 = bo + (((wn * 2 + 1) ^ fl) << 5);
    const unsigned ar = lds_addr(smem) + (wm * 64 + fr) * 128;
    const unsigned aK0 = ar + ((g ^ (fr & 7)) << 4), aK1 = ar + (((4 + g) ^ (fr & 7)) << 4);
    f32x32 ac0, ac1, ac2;
    asm volatile("" :: "v"(aK0), "v"(aK1), "v"(bo0), "v"(bo1));
    if (first) BAR;
    else publish(rsW, tid, 256 + rt * 16 + hp, E + (unsigned)layer);
#define G1_OUT
#define G1_T(a)
#define G1_TEND
#define G1_TCL
    asm volatile(G1_MACROS
        "s_mov_b32 vcc_hi, %[lay]\n"
        ".if %[hh]\n s_branch .Lg1m\n .else\n"
        ".p2align 3\n .Lg1m:\n"
        "G1RDB 0, v16, v17, 0\n G1RDA v12, v13, 0\n G1RDB 1, v16, v17, 0\n"
        "ds_read_b128 v[64:67], v20\n ds_read_b128 v[68:71], v20 offset:2048\n"
        "ds_read_b128 v[72:75], v20 offset:4096\n ds_read_b128 v[76:79], v20 offset:6144\n"
        "s_cmp_eq_u32 vcc_hi, 0\n s_waitcnt lgkmcnt(12)\n"
        "G1SG 0, 1, 1, v12, v13, v16, v17, 0\n"
        "s_mov_b32 vcc_lo, 16\n"
        "G1SG 1, 1, 1, v12, v13, v16, v17, 40960\n"
        "G1SG 2, 1, 2, v12, v13, v16, v17, 40960\n"
        "s_cbranch_scc1 .Lg1a%=\n"
        "s_barrier\n"
        G1_T(88)
        "s_barrier\n"
        G1_T(92)
        "G1RDA v12, v13, 40960\n"
        "s_waitcnt lgkmcnt(4)\n"
        "s_branch .Lg1b%=\n"
        ".Lg1a%=:\n"
        G1_T(88)
        "s_barrier\n"
        G1_T(92)
        "G1RDB 0, v16, v17, 40960\n G1RDA v12, v13, 40960\n G1RDB 1, v16, v17, 40960\n"
        ".p2align 3\n s_nop 0\n"
        ".Lg1b%=:\n"
        "s_waitcnt lgkmcnt(8)\n"
        "G1SG 0, 0, 1, v12, v13, v16, v17, 40960\n"
        "s_barrier\n"
        "G1SG 1, 0, 1, v14, v15, v18, v19, 0\n"
        "G1SG 2, 0, 1, v14, v15, v18, v19, 0\n"
        "s_lshr_b32 vcc_lo, vcc_lo, 1\n s_waitcnt vmcnt(0) lgkmcnt(8)\n"
        "G1SG 0, 0, 1, v14, v15, v18, v19, 0\n"
        "s_barrier\n"
        "G1SG 1, 0, 1, v12, v13, v16, v17, 0\n"
        "G1SG 2, 0, 1, v12, v13, v16, v17, 0\n"
        "s_cbranch_scc0 .Lg1c%=\n s_waitcnt lgkmcnt(8)\n"
        "G1SG 0, 0, 1, v12, v13, v16, v17, 0\n"
        "s_barrier\n"
        "G1SG 1, 0, 1, v12, v13, v16, v17, 40960\n"
        "G1SG 2, 0, 1, v12, v13, v16, v17, 40960\n"
        "s_branch .Lg1b%=\n"
        ".Lg1c%=:\n"
        "s_waitcnt lgkmcnt(8)\n"
        "G1SG 0, 0, 1, v12, v13, v16, v17, 0\n s_bitcmp1_b32 vcc_hi, 0\n"
        "G1SG 1, 0, 0, v12, v13, v16, v17, 0\n"
        G1_T(96)
        "s_cbranch_scc1 .Lg1n\n s_barrier\n"
        "G1Q 2, 2, 0, 0, 0\n ds_read2_b64 v[136:139], v49 offset0:32 offset1:48\n"
        "G1Q 2, 2, 0, 1, 0\n ds_read_b128 v[96:99], v48 offset:256\n"
        "G1Q 2, 2, 0, 2, 0\n ds_read_b128 v[100:103], v48 offset:320\n"
        "G1Q 2, 2, 0, 3, 0\n ds_read2_b32 v[144:145], v52 offset1:16\n"
        "G1Q 2, 3, 1, 0, 0\n ds_read_b128 v[104:107], v54 offset:192\n"
        "G1Q 2, 3, 1, 1, 0\n ds_read_b128 v[108:111], v54 offset:704\n"
        "G1Q 2, 3, 1, 2, 0\n ds_read2_b64 v[140:143], v49 offset1:16\n"
        "G1Q 2, 3, 1, 3, 0\n ds_read_b128 v[148:151], v48\n"
        "G1Q 2, 0, 2, 0, 0\n ds_read_b128 v[88:91], v48 offset:64\n"
        "G1Q 2, 0, 2, 1, 0\n ds_read_b128 v[128:131], v54\n"
        "G1Q 2, 0, 2, 2, 0\n ds_read_b128 v[92:95], v54 offset:512\n"
        "G1Q 2, 0, 2, 3, 0\n"
        "G1Q 2, 1, 3, 0, 0\n"
        "G1Q 2, 1, 3, 1, 0\n"
        "G1Q 2, 1, 3, 2, 0\n"
        "G1Q 2, 1, 3, 3, 0\n"
        "ds_read_b128 v[112:115], v54 offset:128\n ds_read_b128 v[116:119], v54 offset:640\n ds_read_b128 v[152:155], v54 offset:64\n ds_read_b128 v[156:159], v54 offset:576\n"
        "s_waitcnt lgkmcnt(4)\n"
        G1_SLIDE
        ".endif\n .if %[hh]\n .p2align 3\n s_nop 0\n .Lg1n:\n s_barrier\n"
        G1_HEDGE
        ".endif\n"
        G1_TEND G1_PURGE
        : "=&{v[160:191]}"(ac0), "=&{v[192:223]}"(ac1), "=&{v[224:255]}"(ac2), "=&{v[64:79]}"(xq),
          "=&{v[88:91]}"(csq[1]), "=&{v[148:151]}"(csq[0]), "=&{v[104:107]}"(wf[0]), "=&{v[108:111]}"(wf[1]), "=&{v[140:143]}"(st01), "=&{v[136:139]}"(st23),
          "+{v[48:63]}"(pa), "+{v[36:39]}"(bias[0]), "+{v[40:43]}"(bias[1]), "+{v[44:47]}"(bias[2]) G1_OUT
        : "{v12}"(aK0), "{v13}"(aK1), "{v14}"(aK0 + 2 * STAGE), "{v15}"(aK1 + 2 * STAGE), "{v16}"(bo0), "{v17}"(bo1),
          "{v18}"(bo0 + 2 * STAGE), "{v19}"(bo1 + 2 * STAGE), "{v20}"(wn ? aK1 : aK0),
          [lay] "s"(layer), [hh] "n"(MODE), [rs] "s"(rsW), [wm] "s"(wm)
        : "memory", "scc", "vcc", "m0", G1_TCL G1_CLOBBER);
    acc[0][0] = quad<0>(ac0); acc[0][1] = quad<1>(ac0); acc[1][0] = quad<6>(ac0); acc[1][1] = quad<7>(ac0);
    qh[0] = quad<4>(ac1); qh[1] = quad<5>(ac1); qh[2] = quad<2>(ac2); qh[3] = quad<3>(ac2);
  }
  ST(layer * 24 + 4)
  {
  const int lane = lane_id(), tid = w * 64 + lane, fr = lane & 15, g = lane >> 4;
  (void)fr; (void)g;
  if constexpr (HH) {
    f32x2 st2[2];
    const f32x4 z4 = {0, 0, 0, 0};
    constexpr float L2E = 1.4426950408889634f;
    const unsigned kvo = WS_KV + (layer >> 1) * KVLB, kfo = kvo + 16u * NH * KVPB + hp * KVFB;
    bf16x8 qp[2][2];
    auto phi2 = [&](const f32x4 (&acc)[2][2], auto qktag) {
      constexpr int QK = decltype(qktag)::value & 1;
      constexpr int RB0 = (decltype(qktag)::value >> 1) & 1, RB1 = (decltype(qktag)::value & 4) ? RB0 + 1 : 2;
      float pv[2][8];
      f32x4 pm[2][2];
      f32x2 f[2][4];
#pragma unroll
      for (int rb = RB0; rb < RB1; ++rb) {
        asm volatile("" : "+v"(st2[rb]));
        const f32x2 nm = {st2[rb][0], st2[rb][0]};
#pragma unroll
        for (int i = 0; i < 4; ++i) {
          const f32x4& a = acc[rb][i >> 1];
          const f32x4& cs = csq[i >> 1];
          f[rb][i] = __builtin_elementwise_fma(f32x2{cs[(i & 1) * 2], cs[(i & 1) * 2 + 1]}, nm, f32x2{a[(i & 1) * 2], a[(i & 1) * 2 + 1]});
        }
      }
#pragma unroll
      for (int rb = RB0; rb < RB1; ++rb) asm volatile("" : "+v"(f[rb][0]), "+v"(f[rb][1]), "+v"(f[rb][2]), "+v"(f[rb][3]));
#pragma unroll
      for (int rb = RB0; rb < RB1; ++rb) {
        const bf16x8 xf = __builtin_bit_cast(bf16x8, u32x4{pk2(f[rb][0][0], f[rb][0][1]), pk2(f[rb][1][0], f[rb][1][1]), pk2(f[rb][2][0], f[rb][2][1]), pk2(f[rb][3][0], f[rb][3][1])});
        pm[rb][0] = MFMA(wf[0], xf, z4); pm[rb][1] = MFMA(wf[1], xf, z4);
      }
      asm volatile("s_nop 7" : "+v"(pm[0][0]), "+v"(pm[0][1]), "+v"(pm[1][0]), "+v"(pm[1][1]));
#pragma unroll
      for (int rb = RB0; rb < RB1; ++rb)
#pragma unroll
        for (int i = 0; i < 4; ++i) { pv[rb][i] = pm[rb][0][i]; pv[rb][4 + i] = pm[rb][1][i]; }
      float mx[2], mn[2];
#pragma unroll
      for (int u = RB0; u < RB1; ++u) max8pn(pm[u][0], pm[u][1], mx[u], mn[u]);
      if (RB1 - RB0 == 2) xr4<true>(mx[0], mn[0], mx[1], mn[1]); else xr2<true>(mx[RB0], mn[RB0]);
      float ep[2][8], en[2][8], sp[2], sn[2];
#pragma unroll
      for (int u = RB0; u < RB1; ++u) {
        const float c = st2[u][1] * L2E, mxs = -mx[u] * c, mns = -mn[u] * c;
#pragma unroll
        for (int i = 0; i < 4; ++i) {
          const f32x2 p2 = {pv[u][2 * i], pv[u][2 * i + 1]};
          const f32x2 tp = __builtin_elementwise_fma(p2, f32x2{c, c}, f32x2{mxs, mxs});
          const f32x2 tn = __builtin_elementwise_fma(-p2, f32x2{c, c}, f32x2{mns, mns});
          ep[u][2 * i] = __builtin_amdgcn_exp2f(tp[0]); ep[u][2 * i + 1] = __builtin_amdgcn_exp2f(tp[1]);
          en[u][2 * i] = __builtin_amdgcn_exp2f(tn[0]); en[u][2 * i + 1] = __builtin_amdgcn_exp2f(tn[1]);
        }
        const f32x2 a2 = (f32x2{ep[u][0], ep[u][1]} + f32x2{ep[u][2], ep[u][3]}) + (f32x2{ep[u][4], ep[u][5]} + f32x2{ep[u][6], ep[u][7]});
        const f32x2 b2 = (f32x2{en[u][0], en[u][1]} + f32x2{en[u][2], en[u][3]}) + (f32x2{en[u][4], en[u][5]} + f32x2{en[u][6], en[u][7]});
        sp[u] = a2[0] + a2[1]; sn[u] = b2[0] + b2[1];
      }
      if (RB1 - RB0 == 2) xr4<false>(sp[0], sn[0], sp[1], sn[1]); else xr2<false>(sp[RB0], sn[RB0]);
#pragma unroll
      for (int rb = RB0; rb < RB1; ++rb) {
        float ip = __builtin_amdgcn_rcpf(sp[rb]), in = __builtin_amdgcn_rcpf(sn[rb]);
        u32x2 o0, o1, o2, o3;
        o0[0] = pk2(ep[rb][0] * ip, ep[rb][1] * ip); o0[1] = pk2(ep[rb][2] * ip, ep[rb][3] * ip);
        o1[0] = pk2(ep[rb][4] * ip, ep[rb][5] * ip); o1[1] = pk2(ep[rb][6] * ip, ep[rb][7] * ip);
        o2[0] = pk2(en[rb][0] * in, en[rb][1] * in); o2[1] = pk2(en[rb][2] * in, en[rb][3] * in);
        o3[0] = pk2(en[rb][4] * in, en[rb][5] * in); o3[1] = pk2(en[rb][6] * in, en[rb][7] * in);
        if (QK == 0) {
          qp[rb][0] = __builtin_bit_cast(bf16x8, (u32x4){o0[0], o0[1], o1[0], o1[1]});
          qp[rb][1] = __builtin_bit_cast(bf16x8, (u32x4){o2[0], o2[1], o3[0], o3[1]});
          asm volatile("" : "+v"(qp[rb][0]), "+v"(qp[rb][1]));
        } else {
          const unsigned dst = ppk + rb * 16 * PSTR;
          lds<u32x2>(dst) = o0; lds<u32x2>(dst + 32) = o1; lds<u32x2>(dst + 64) = o2; lds<u32x2>(dst + 96) = o3;
        }
      }
    };
    pwm = pwl + (img(layer, hp) ? 0u : 8 * DM * 2 - 1024u);
    asm volatile("" : "+v"(pa));
    bf16x8 one8 = ones8();
    asm volatile("" : "+v"(one8));
    WAITL0; BAR;
    ST(layer * 24 + 5)
    {
      bf16x8 af[4], v0[4], v1[4];
      asm volatile("" ::: "memory");
#pragma unroll
      for (int sw = 0; sw < 4; ++sw) {
        af[sw] = cat8(tr_read(ppa + sw * (64 * PSTR)), tr_read(ppa + sw * (64 * PSTR) + 16 * PSTR));
        v0[sw] = lds<bf16x8>(pvr + sw * 4096);
        v1[sw] = lds<bf16x8>(pvr + sw * 4096 + 1024);
      }
      __builtin_amdgcn_sched_barrier(0);
      f32x4 kv[3] = {z4, z4, z4};
#pragma unroll
      for (int sw = 0; sw < 4; ++sw) {
        kv[0] = MFMA(af[sw], v0[sw], kv[0]);
        kv[1] = MFMA(af[sw], v1[sw], kv[1]);
        kv[2] = MFMA(af[sw], one8, kv[2]);
      }
      auto pc = [](float x, float y) { const unsigned t = pk2(x, y); return t < EMPTY - 1u ? t : EMPTY - 1u; };
      __builtin_amdgcn_raw_buffer_store_b128(u32x4{pc(kv[0][0], kv[0][1]), pc(kv[0][2], kv[0][3]), pc(kv[1][0], kv[1][1]), pc(kv[1][2], kv[1][3])}, rsW, gkv, 0, 16);
      __builtin_amdgcn_raw_buffer_store_b64(u32x2{pc(kv[2][0], kv[2][1]), pc(kv[2][2], kv[2][3])}, rsW, gkz, 0, 16);
    }
    u64 ko = korder(bid & 3, hp);
    auto wo_late = [&](int t) { glds2v(wos + knib(ko, t) * tO, pwl, pwm, sb + OFF_WO + t * 8192 + (w & 3) * 2048); };
    ST(layer * 24 + 6)
    u32x4 l1[8];
    const bool red = w == 7 || (w == 6 && (rt & 7) == 0);
    unsigned lo = kvo + (unsigned)((2 * hp + (rt >> 3) + (lane >> 5) * 8 * NH) * KVPB) +
                  (w == 7 ? (unsigned)((rt & 4) * 512 + (g & 1) * 1024 + (rt & 3) * 256 + fr * 16) : (unsigned)(4096 + (lane & 7) * 16));
    unsigned fs = w == 7 ? kfo + (unsigned)((rt >> 2) * 2048 + (lane >> 5) * 1024 + (rt & 3) * 256 + fr * 16 + (g & 1) * 8)
                : (lane & 24) ? 0x7fff0000u : kfo + (unsigned)(8192 + (rt >> 3) * 128 + (lane & 4) * 16 + (lane & 1) * 32 + (lane >> 5) * 16 + (lane & 2) * 4);
    auto l1_load = [&]() {
      asm volatile("" ::: "memory");
#pragma unroll
      for (int p = 0; p < 8; ++p) asm volatile("buffer_load_dwordx4 %0, %1, %2, %3 offen sc1" : "=v"(l1[p]) : "v"(lo), "s"(rsW), "s"(p * (NH * KVPB)) : "memory");
      __builtin_amdgcn_sched_barrier(0);
    };
    if (red) asm volatile("" : "+v"(lo), "+v"(fs));
    if (red) { __builtin_amdgcn_s_sleep(RSLEEP); l1_load(); }
    const unsigned pxq = sb + XS + lane * 16;
    if (!kg) {
      const f32x4 qa[2][2] = {{qh[0], qh[1]}, {qh[2], qh[3]}};
      st2[0] = f32x2{st23[0], st23[1]}; st2[1] = f32x2{st23[2], st23[3]};
      phi2(qa, IC<0>{});
#pragma unroll
      for (int i = 0; i < 4; ++i) lds<bf16x8>(pxq + (i >> 1) * 8192 + (i & 1) * 1024) = qp[i >> 1][i & 1];
    } else if (red) {
      typedef __attribute__((ext_vector_type(2))) __bf16 bf2;
      unsigned c0 = 0x00003F80u, c1 = 0x3F800000u;
      asm volatile("" : "+v"(c0), "+v"(c1));
      float s4[4];
      _Pragma("unroll 1") for (unsigned spins = 0; spins < (1u << 18); ++spins) {
        float a[8] = {0.f, 0.f, 0.f, 0.f, 0.f, 0.f, 0.f, 0.f};
        ARRIVED("+v"(l1[0]), "+v"(l1[1]), "+v"(l1[2]), "+v"(l1[3]), "+v"(l1[4]), "+v"(l1[5]), "+v"(l1[6]), "+v"(l1[7]));
#pragma unroll
        for (int p = 0; p < 8; ++p)
#pragma unroll
          for (int e = 0; e < 4; ++e) {
            const unsigned u = l1[p][e];
            a[2 * e] = __builtin_amdgcn_fdot2_f32_bf16(__builtin_bit_cast(bf2, u), __builtin_bit_cast(bf2, c0), a[2 * e], false);
            a[2 * e + 1] = __builtin_amdgcn_fdot2_f32_bf16(__builtin_bit_cast(bf2, u), __builtin_bit_cast(bf2, c1), a[2 * e + 1], false);
          }
#pragma unroll
        for (int i = 0; i < 4; ++i) {
          auto r = __builtin_amdgcn_permlane32_swap(__float_as_uint(a[i]), __float_as_uint(a[i + 4]), false, false);
          s4[i] = __uint_as_float(r[0]) + __uint_as_float(r[1]);
        }
        const float t = s4[0] + s4[2];
        if (__all(t == t)) break;
        unsigned mxw = 0;
#pragma unroll
        for (int p = 0; p < 8; ++p)
#pragma unroll
          for (int e = 0; e < 4; ++e) mxw = l1[p][e] > mxw ? l1[p][e] : mxw;
        if (__all(mxw != EMPTY)) break;
        l1_load();
      }
      WAITV(0);
      unsigned o0 = pk2(s4[0], s4[1]), o1 = pk2(s4[2], s4[3]);
      o0 = o0 < EMPTY - 1u ? o0 : EMPTY - 1u; o1 = o1 < EMPTY - 1u ? o1 : EMPTY - 1u;
      __builtin_amdgcn_raw_buffer_store_b64(u32x2{o0, o1}, rsW, fs, 0, 16);
    }
    asm volatile("" : "+s"(ko));
    ST(layer * 24 + 8)
    WAITL0;
    BAR;
    ST(layer * 24 + 9)
    {
      unsigned pfo = OFF_KV + wn * 4096 + lane * 16, pzo = OFF_KV + 8192 + wn * 128 + g * 16;
      asm volatile("" : "+v"(pfo), "+v"(pzo));
      if (kg) {
        asm volatile("" ::: "memory");
#pragma unroll
        for (int i = 0; i < 4; ++i) qp[i >> 1][i & 1] = lds<bf16x8>(pxq + (i >> 1) * 8192 + (i & 1) * 1024);
        asm volatile("s_waitcnt lgkmcnt(0)" : "+v"(qp[0][0]), "+v"(qp[0][1]), "+v"(qp[1][0]), "+v"(qp[1][1]) :: "memory");
#pragma unroll
        for (int i = LATE0; i < 12; ++i) wo_late(i);
      } else {
        u32x4 fa, fc, fb;
        const unsigned fo = kfo + (unsigned)(tid * 16), fo2 = w == 0 ? kfo + (unsigned)(8192 + (lane & 15) * 16) : 0x7fff0000u;
        st2[0] = f32x2{st01[0], st01[1]}; st2[1] = f32x2{st01[2], st01[3]};
        phi2(acc, IC<0>{});
        if (FSLEEP) __builtin_amdgcn_s_sleep(FSLEEP);
        _Pragma("unroll 1") for (unsigned spins = 0; spins < (1u << 18); ++spins) {
          asm volatile("" ::: "memory");
          fa = __builtin_amdgcn_raw_buffer_load_b128(rsW, fo, 0, 16);
          fc = __builtin_amdgcn_raw_buffer_load_b128(rsW, fo, 4096, 16);
          fb = __builtin_amdgcn_raw_buffer_load_b128(rsW, fo2, 0, 16);
          unsigned mxw = 0;
#pragma unroll
          for (int e = 0; e < 4; ++e) { mxw = fa[e] > mxw ? fa[e] : mxw; mxw = fb[e] > mxw ? fb[e] : mxw; mxw = fc[e] > mxw ? fc[e] : mxw; }
          if (__all(mxw != EMPTY)) break;
          __builtin_amdgcn_s_sleep(1);
        }
        *(u32x4*)(smem + OFF_KV + tid * 16) = fa;
        *(u32x4*)(smem + OFF_KV + 4096 + tid * 16) = fc;
        if (tid < 16) *(u32x4*)(smem + OFF_KV + 8192 + tid * 16) = fb;
      }
      ST(layer * 24 + 10)
      WAITL0; BAR;
      ST(layer * 24 + 11)
      bf16x8 kvf[3][2];
#pragma unroll
      for (int ks = 0; ks < 2; ++ks) {
        kvf[0][ks] = *(const bf16x8*)(smem + pfo + ks * 2048);
        kvf[1][ks] = *(const bf16x8*)(smem + pfo + ks * 2048 + 1024);
        kvf[2][ks] = *(const bf16x8*)(smem + pzo + ks * 64);
      }
#pragma unroll
      for (int rb = 0; rb < 2; ++rb) {
        f32x4 oa[3];
#pragma unroll
        for (int nb = 2; nb >= 0; --nb) {
          oa[nb] = MFMA(kvf[nb][0], qp[rb][0], z4);
          oa[nb] = MFMA(kvf[nb][1], qp[rb][1], oa[nb]);
        }
        float den = oa[2][0];
        den += 1e-6f;
        float inv = __builtin_amdgcn_rcpf(den);
        o_put(rb, oa[0] * inv, oa[1] * inv);
      }
    }
  }
  ST(layer * 24 + 12)
  if (HH) if (w >= 4) { WAITV(0); WAITL0; BAR; }
  if (first && w >= 4 && w < 7) {
    typedef __attribute__((ext_vector_type(2))) __bf16 bf2;
    unsigned c0 = 0x00003F80u, c1 = 0x3F800000u;
    asm volatile("" : "+v"(c0), "+v"(c1));
    const bf2 lo1 = __builtin_bit_cast(bf2, c0), hi1 = __builtin_bit_cast(bf2, c1);
    float a[8] = {0.f, 0.f, 0.f, 0.f, 0.f, 0.f, 0.f, 0.f};
#pragma unroll
    for (int i = 0; i < 8; ++i) {
      asm volatile("" : "+v"(pw[i]));
#pragma unroll
      for (int e = 0; e < 4; ++e) {
        const unsigned u = pw[i][e];
        a[2 * e] = __builtin_amdgcn_fdot2_f32_bf16(__builtin_bit_cast(bf2, u), lo1, a[2 * e], false);
        a[2 * e + 1] = __builtin_amdgcn_fdot2_f32_bf16(__builtin_bit_cast(bf2, u), hi1, a[2 * e + 1], false);
      }
    }
#pragma unroll
    for (int i = 0; i < 4; ++i) {
      auto r = __builtin_amdgcn_permlane32_swap(__float_as_uint(a[i]), __float_as_uint(a[i + 4]), false, false);
      a[i] = __uint_as_float(r[0]) + __uint_as_float(r[1]);
    }
#pragma unroll
    for (int i = 0; i < 2; ++i) {
      auto r = __builtin_amdgcn_permlane16_swap(__float_as_uint(a[i]), __float_as_uint(a[i + 2]), false, false);
      a[i] = __uint_as_float(r[0]) + __uint_as_float(r[1]);
      a[i] += dpp_ror<8>(a[i]);
    }
    if (!(lane & 8))
      __builtin_amdgcn_raw_buffer_store_b64(u32x2{__float_as_uint(a[0]), __float_as_uint(a[1])}, rsW,
          (unsigned)(WS_CSP + ((((hp + (1 << (w - 4))) & 15) * 16 + rt) * 192 + (w - 4) * 64 + (lane & 7) * 8 + (lane >> 5) * 4 + ((lane >> 4) & 1) * 2) * 4), 0, 16);
  }
  }
}

#undef pcs
#undef prs
#undef opo
#undef ogo
#undef pkw
#undef hof
#undef dm
#undef dm2
#undef pcv
#undef pwf
#undef prv
#undef pvf
#undef ppk
#undef ppa
#undef pvr
#undef gkv
#undef gkz
#undef pwl
#undef pwm
#undef plb
#undef prd
#undef dm3

__device__ __forceinline__ void g2_phase(char* smem, char* ws, u64 E, int w, f32x16& xq, Next& nx, int layer, const u16* __restrict__ x0,
                                         const u16* __restrict__ wo, const u16* __restrict__ wnx, u16* __restrict__ out) {
  IDS
  wnx = uni(wnx);
  const unsigned sb = lds_addr(smem);
  const int lw = w & 3, k2 = 16 * lw + (lane >> 3);
  const int r0 = w * 16 + (lane >> 3);
  unsigned pxr = sb + r0 * 128 + (lane & 7) * 16;
  const unsigned rmo = (unsigned)(((m0 + r0) * DM + hp * 64 + ((lane & 7) ^ (lane >> 3)) * 8) * 2);
  const unsigned imo = (unsigned)((rt * 16 + hp) * 16384 + r0 * 128 + (lane & 7) * 16);
  constexpr int S0 = 0, S1 = 16384, S2 = 32768, S3 = 147456, S4 = OFF_WO, S5 = 98304, S6 = 114688;
  constexpr int AS[16] = {S0, S1, S2, S3, S0, S1, S4, S2, S3, S0, S1, S4, S5, S3, S0, S6};
  constexpr int OFF_C = 0;
  auto bs = [](int t) { return OFF_WO + (t < 12 ? t : t - 10) * 8192; };
  const char *xa1, *xa2, *wq2;
  u64 fm;
  unsigned vB0, vB1, vXA, po;
  if (w >= 4) {
    constexpr int WT[15] = {0, 8, 12, 14, 16, 18, 20, 18, 16, 14, 12, 12, 10, 10, 8};
    const char* o = ws + WS_O + layer * (SEQ * DM * 2) + rt * 262144;
    const unsigned vA = (unsigned)(lw * 4096 + lane * 16);
    const u64 kord = korder(bid & 3, hp);
    auto ldA = [&](int s) { glds4c(o + knib(kord, s) * 16384, vA, sb + AS[s] + lw * 4096); };
    const bool im = img(layer, hp), in = img(layer + 1, hp);
    unsigned vW0 = im ? (unsigned)(lw * 2048 + lane * 16) : (unsigned)((k2 * DM + hp * 64 + ((lane & 7) ^ ((lane >> 4) << 1)) * 8) * 2), vW1 = vW0 + (im ? 0u : 8 * DM * 2 - 1024u);
    const char *wos = wo_src(ws, wo, layer, hp), *wqs = wq_src(ws, wnx, layer + 1, hp);
    unsigned tO = im ? 8192u : 131072u;
    asm volatile("" : "+v"(vW0), "+v"(vW1));
    asm volatile("" : "+s"(wos), "+s"(tO));
    auto ldW = [&](int t) { glds2v(wos + knib(kord, t) * tO, vW0, vW1, sb + bs(t) + lw * 2048); };
    const int fB = (k2 >> 1) & 1;
    vB0 = in ? (unsigned)(lw * 6144 + lane * 16) : (unsigned)((k2 * 3072 + hp * 64 + (((((lane & 7) >> 1) ^ fB) << 1) | (lane & 1)) * 8) * 2);
    vB1 = in ? vB0 : (unsigned)(((k2 + 8) * 3072 + hp * 64 + (((((lane & 7) >> 1) ^ (fB | 2)) << 1) | (lane & 1)) * 8) * 2) - 1024u;
    const u64 kn = nx.kord = korder(kv1(bid), hp);
    nx.wq = wqs; nx.tw = in ? 24576u : 393216u;
    const char *wq0 = wqs + hp * (in ? 24576u : 393216u), *wq1 = wqs + knib(kn, 1) * (in ? 24576u : 393216u);
    wq2 = wqs + knib(kn, 2) * (in ? 24576u : 393216u);
    xa1 = ws + WS_X + layer * (SEQ * DM * 2) + knib(kn, 1) * 16384; xa2 = ws + WS_X + layer * (SEQ * DM * 2) + knib(kn, 2) * 16384;
    vXA = (unsigned)(rt * 262144 + lw * 4096 + lane * 16);
    fm = kfirst(kn);
    po = ((fm >> lane) & 1) ? WS_FLAGS + (unsigned)(256 + rt * 16 + lane) * FLS : 0u;
    asm volatile("" : "+v"(vB0), "+v"(vB1), "+v"(vXA), "+v"(po));
    asm volatile("" : "+s"(xa1), "+s"(xa2), "+s"(wq2), "+s"(fm), "+s"(nx.kord), "+s"(nx.wq), "+s"(nx.tw));
    asm volatile("" : "+s"(wq0), "+s"(wq1));
    auto ldB = [&](int s, int part) {
      const char* b = s ? wq1 : wq0;
      if (part) glds4v(b, vB0, vB1, vB0, vB1, sb + s * 40960 + 16384 + lw * 6144);
      else glds2v(b + 4096, vB0, vB1, sb + s * 40960 + 16384 + lw * 6144 + 4096);
    };
    BAR;
    ldA(1); __builtin_amdgcn_s_sleep(3); ldA(2); __builtin_amdgcn_s_sleep(3); ldA(3); __builtin_amdgcn_s_sleep(3); ldA(4);
    WAITV(12);
    BAR;
    auto step = [&](auto ktc) {
      constexpr int kt = decltype(ktc)::value;
      WAITVN(WT[kt]);
      BAR;
      if constexpr (kt == 1) { ldA(5); ldA(6); }
      if constexpr (kt >= 2 && kt <= 10) ldA(kt + 5);
      if constexpr (kt >= 2 && kt <= 5) ldW(kt + 10);
      if constexpr (kt >= 11) ldB((kt - 11) >> 1, (kt - 11) & 1);
      if constexpr (kt >= 6) { __builtin_amdgcn_s_sleep(kt >= 11 ? 3 : 2); if (layer == 1) __builtin_amdgcn_s_sleep(1); }
    };
    step(IC<1>{}); step(IC<2>{}); step(IC<3>{}); step(IC<4>{}); step(IC<5>{}); step(IC<6>{}); step(IC<7>{});
    step(IC<8>{}); step(IC<9>{}); step(IC<10>{}); step(IC<11>{}); step(IC<12>{}); step(IC<13>{}); step(IC<14>{});
  } else {
    asm volatile("" : "=v"(vB0), "=v"(vB1), "=v"(vXA), "=v"(po));
    asm volatile("" : "=s"(xa1), "=s"(xa2), "=s"(wq2), "=s"(fm), "=s"(nx.kord), "=s"(nx.wq), "=s"(nx.tw));
    const int wm = w >> 1, wn = w & 1;
    f32x4 acc[4][2];
    unsigned co[2];
#pragma unroll
    for (int cb = 0; cb < 2; ++cb) co[cb] = sb + OFF_C + (unsigned)((wm * 64 + fr) * 128 + (((wn * 4 + cb * 2 + (g >> 1)) ^ (fr & 7)) << 4) + (g & 1) * 8);
    const int fl = (q4 >> 1) | ((g & 1) << 1);
    const int bb0 = (4 * g + q4) * 128 + p4 * 8 + (((wn * 2) ^ fl) << 5), bb1 = (4 * g + q4) * 128 + p4 * 8 + (((wn * 2 + 1) ^ fl) << 5);
    const int abase = wm * 4096 + lane * 16;
    bf16x8 fa[2][2][4], fb[2][2][2];
    unsigned ra[3], rb0[2], rb1[2];
#pragma unroll
    for (int i = 0; i < 3; ++i) { ra[i] = abase + i * 65536; asm volatile("" : "+v"(ra[i])); }
#pragma unroll
    for (int i = 0; i < 2; ++i) { rb0[i] = bb0 + OFF_WO + i * 49152; rb1[i] = bb1 + OFF_WO + i * 49152; asm volatile("" : "+v"(rb0[i]), "+v"(rb1[i])); }
    auto loadf = [&](int kt, bf16x8 (&a)[2][4], bf16x8 (&b)[2][2]) {
#pragma unroll
      for (int s = 0; s < 2; ++s) {
        const char* sA = smem + ra[AS[kt] >> 16] + (AS[kt] & 65535) + s * 8192;
        const int hi = bs(kt) >= OFF_WO + 49152, ob = bs(kt) - OFF_WO - hi * 49152 + s * 4096;
        b[s][0] = cat8(tr_read(smem + rb0[hi] + ob), tr_read(smem + rb0[hi] + ob + 2048));
        b[s][1] = cat8(tr_read(smem + rb1[hi] + ob), tr_read(smem + rb1[hi] + ob + 2048));
#pragma unroll
        for (int rb = 0; rb < 4; ++rb) a[s][rb] = *(const bf16x8*)(sA + rb * 1024);
      }
    };
    auto mma = [&](bf16x8 (&a)[2][4], bf16x8 (&b)[2][2]) {
#pragma unroll
      for (int i = 0; i < 16; ++i) {
        const int rb = i >> 2, cb = ((i >> 1) ^ rb) & 1, s = ((i + 1) >> 1) & 1;
        acc[rb][cb] = MFMA(b[s][cb], a[s][rb], acc[rb][cb]);
      }
    };
    unsigned fo = lane < 16 && lane != hp ? WS_FLAGS + (unsigned)(rt * 16 + lane) * FLS : 0u;
    const u64 ko = korder(bid & 3, hp);
    unsigned kf = kfirst(ko), kf2 = kfirst(ko >> 16) | (1u << knib(ko, 9));
    unsigned jo = (unsigned)((((rt & 7) * 128 + (w - 1) * 64 + lane) * 3072 + (hp & 3) * 1024 + (hp | 3) * 64) * 2);
    unsigned fo1 = ((kf >> (lane & 31)) & 1) ? fo : 0u, fo2 = ((kf2 >> (lane & 31)) & 1) ? fo : 0u;
    fo = (((kf | kf2) >> (lane & 31)) & 1) ? 0u : fo;
    asm volatile("" : "+v"(fo), "+v"(fo1), "+v"(fo2));
    asm volatile("" : "+s"(kf), "+s"(kf2));
    publish(rsW, tid, rt * 16 + hp, E + (unsigned)(layer + 1));
    ST(layer * 24 + 13)
    u64 fl1 = 0;
    if (w == 0) asm volatile("global_load_dwordx2 %0, %1, %2 sc1" : "+v"(fl1) : "v"(fo1), "s"(ws) : "memory");
    if (layer == 0) {
      const unsigned xo = (unsigned)(((m0 + wm * 64 + fr) * DM + hp * 64 + wn * 32 + g * 8) * 2);
      asm volatile("global_load_dwordx4 v[64:67], %1, %3\n\tglobal_load_dwordx4 v[68:71], %1, %4\n\tglobal_load_dwordx4 v[72:75], %2, %3\n\tglobal_load_dwordx4 v[76:79], %2, %4"
                   : "+{v[64:79]}"(xq) : "v"(xo), "v"(xo + 32 * DM * 2), "s"(x0), "s"(x0 + 16 * DM) : "memory");
    }
    loadf(0, fa[0], fb[0]);
    unsigned junk = 0;
    if (layer < 2 && w == 3 && (bid >> 6) == 0) code_touch(junk, bid, lane);
    if (layer == 0 && (hp & 7) < 3 && (w == 1 || w == 2))
      asm volatile("global_load_dword %0, %1, %2" : "+v"(junk) : "v"(jo), "s"(wnx) : "memory");
    __builtin_amdgcn_sched_barrier(0);
    bf16x8 id[2];
#pragma unroll
    for (int cb = 0; cb < 2; ++cb) {
      const unsigned one = (g >> 1) == cb && (g & 1) == (fr >> 3) ? 0x3F80u << ((fr & 1) * 16) : 0u;
      u32x4 t;
#pragma unroll
      for (int j = 0; j < 4; ++j) t[j] = ((fr >> 1) & 3) == j ? one : 0u;
      id[cb] = __builtin_bit_cast(bf16x8, t);
      asm volatile("" : "+v"(id[cb]));
    }
    asm volatile("" : "+v"(co[0]), "+v"(co[1]));
#pragma unroll
    for (int rb = 0; rb < 4; ++rb) { acc[rb][0] = f32x4{0, 0, 0, 0}; acc[rb][1] = f32x4{0, 0, 0, 0}; }
    mma(fa[0], fb[0]);
    __builtin_amdgcn_sched_barrier(0);
    WAITL0;
    u64 miss = 0;
    if (w == 0) { ARRIVED("+v"(fl1)); if (__ballot(fl1 != E + (unsigned)(layer + 1)) & kf) poll_eq(ws, fo1, E + (unsigned)(layer + 1), kf, kf); miss = 0xffffu & ~(kf | kf2 | (1u << hp)); }
    ST(layer * 24 + 14)
    __builtin_amdgcn_sched_barrier(0);
    BAR;
    __builtin_amdgcn_sched_barrier(0);
    asm volatile("" ::: "memory");
    if (layer == 0) WAITV(0);
    __builtin_amdgcn_sched_barrier(0);
    asm volatile("" : "+{v[64:79]}"(xq));
    const bf16x8 xf[4] = {__builtin_bit_cast(bf16x8, quad<0>(xq)), __builtin_bit_cast(bf16x8, quad<1>(xq)), __builtin_bit_cast(bf16x8, quad<2>(xq)), __builtin_bit_cast(bf16x8, quad<3>(xq))};
#pragma unroll
    for (int rb = 0; rb < 4; ++rb) { acc[rb][0] = MFMA(id[0], xf[rb], acc[rb][0]); acc[rb][1] = MFMA(id[1], xf[rb], acc[rb][1]); }
    if (w == 0) poll_eq(ws, fo2, E + (unsigned)(layer + 1), kf2, kf2);
    u64 fl2 = 0;
    auto step = [&](auto ktc) {
      constexpr int kt = decltype(ktc)::value;
      __builtin_amdgcn_sched_barrier(0);
      if (kt == 5 && miss) {
        ARRIVED("+v"(fl2));
        if (__ballot(fl2 != E + (unsigned)(layer + 1)) & miss) poll_eq(ws, fo, E + (unsigned)(layer + 1), miss, ~0ull);
      }
      WAITL0;
      BAR;
      if (kt == 2 && miss) asm volatile("global_load_dwordx2 %0, %1, %2 sc1" : "+v"(fl2) : "v"(fo), "s"(ws) : "memory");
      if (kt == 0) { ST(layer * 24 + 15) }
      __builtin_amdgcn_sched_barrier(0);
      loadf(kt + 1, fa[(kt + 1) & 1], fb[(kt + 1) & 1]);
      if (kt == 0) return;
      mma(fa[kt & 1], fb[kt & 1]);
#pragma unroll
      for (int i = 0; i < 4; ++i) {
        __builtin_amdgcn_sched_group_barrier(0x008, 1, 0);
        __builtin_amdgcn_sched_group_barrier(0x100, 1, 0);
        __builtin_amdgcn_sched_group_barrier(0x008, 1, 0);
        __builtin_amdgcn_sched_group_barrier(0x100, 3, 0);
      }
#pragma unroll
      for (int i = 0; i < 8; ++i) __builtin_amdgcn_sched_group_barrier(0x008, 1, 0);
    };
    step(IC<0>{}); step(IC<1>{}); step(IC<2>{}); step(IC<3>{}); step(IC<4>{}); step(IC<5>{}); step(IC<6>{}); step(IC<7>{});
    step(IC<8>{}); step(IC<9>{}); step(IC<10>{}); step(IC<11>{}); step(IC<12>{}); step(IC<13>{}); step(IC<14>{});
    WAITV(0);
    asm volatile("" :: "v"(junk));
    __builtin_amdgcn_sched_barrier(0);
    auto put = [&](int rb) {
#pragma unroll
      for (int cb = 0; cb < 2; ++cb) lds<u32x2>(co[cb] + rb * 2048) = u32x2{pk2(acc[rb][cb][0], acc[rb][cb][1]), pk2(acc[rb][cb][2], acc[rb][cb][3])};
    };
#pragma unroll
    for (int rb = 0; rb < 4; ++rb)
#pragma unroll
      for (int cb = 0; cb < 2; ++cb) acc[rb][cb] = MFMA(fb[1][0][cb], fa[1][0][rb], acc[rb][cb]);
#pragma unroll
    for (int rb = 0; rb < 4; ++rb) {
      __builtin_amdgcn_sched_barrier(0);
#pragma unroll
      for (int cb = 0; cb < 2; ++cb) acc[rb][cb] = MFMA(fb[1][1][cb], fa[1][1][rb], acc[rb][cb]);
      __builtin_amdgcn_sched_barrier(0);
      if (rb >= 2) put(rb - 2);
    }
    __builtin_amdgcn_sched_barrier(0);
    ST(layer * 24 + 16)
    put(2); put(3);
  }
  WAITL0; BAR;
  asm volatile("" ::: "memory");
  u32x4 xn[2];
#pragma unroll
  for (int i = 0; i < 2; ++i) xn[i] = lds<u32x4>(pxr + OFF_C + i * 1024);
  if (layer == 3) {
    const __amdgpu_buffer_rsrc_t rsO = __builtin_amdgcn_make_buffer_rsrc((void*)uni(out), (short)0, SEQ * DM * 2, 0x00020000);
    __builtin_amdgcn_raw_buffer_store_b128(xn[0], rsO, rmo, 0, 16);
    __builtin_amdgcn_raw_buffer_store_b128(xn[1], rsO, rmo + 8 * DM * 2, 0, 16);
  } else {
    __builtin_amdgcn_raw_buffer_store_b128(xn[0], rsW, WS_X + layer * (SEQ * DM * 2) + imo, 0, 16);
    __builtin_amdgcn_raw_buffer_store_b128(xn[1], rsW, WS_X + layer * (SEQ * DM * 2) + imo + 1024, 0, 16);
    float mean[2], m2[2];
#pragma unroll
    for (int i = 0; i < 2; ++i) {
      typedef __attribute__((ext_vector_type(2))) __bf16 bf2;
      const bf2 one2 = __builtin_bit_cast(bf2, 0x3F803F80u);
      float sa = 0.f, sq = 0.f;
#pragma unroll
      for (int e = 0; e < 4; ++e) {
        const unsigned u = xn[i][e];
        const bf2 t = __builtin_bit_cast(bf2, u);
        sa = __builtin_amdgcn_fdot2_f32_bf16(t, one2, sa, false);
        sq = __builtin_amdgcn_fdot2_f32_bf16(t, t, sq, false);
      }
      mean[i] = sa;
      m2[i] = sq;
    }
#define DPA(c) "v_add_f32_dpp %0, %0, %0 " c "\n\tv_add_f32_dpp %1, %1, %1 " c "\n\tv_add_f32_dpp %2, %2, %2 " c "\n\tv_add_f32_dpp %3, %3, %3 " c "\n\t"
    asm volatile("s_nop 3\n\t" DPA("quad_perm:[1,0,3,2] row_mask:0xf bank_mask:0xf") DPA("quad_perm:[2,3,0,1] row_mask:0xf bank_mask:0xf")
                 DPA("row_half_mirror row_mask:0xf bank_mask:0xf") "s_nop 0" : "+v"(mean[0]), "+v"(m2[0]), "+v"(mean[1]), "+v"(m2[1]));
    if ((lane & 7) < 2) {
      const int i = lane & 1;
      const u32x2 sv = {__float_as_uint(i ? mean[1] : mean[0]), __float_as_uint(i ? m2[1] : m2[0])};
      __builtin_amdgcn_raw_buffer_store_b64(sv, rsW, WS_ST + layer * (SEQ * 128) + (unsigned)(((rt * 16 + hp) * 128 + r0 + i * 8) * 8), 0, 16);
    }
  }
  ST(layer * 24 + 17)
  if (layer < 3 && w >= 4) {
    WAITV(0); BAR;
    u64 fl = 0;
    if (lw == 0) { __builtin_amdgcn_s_sleep(7); asm volatile("global_load_dwordx2 %0, %1, %2 sc1" : "+v"(fl) : "v"(po), "s"(ws) : "memory"); }
    glds4v(wq2, vB0, vB1, vB0, vB1, sb + 2 * 40960 + 16384 + lw * 6144);
    glds2v(wq2 + 4096, vB0, vB1, sb + 2 * 40960 + 16384 + lw * 6144 + 4096);
    if (lw == 0) {
      WAITV(6);
      __builtin_amdgcn_sched_barrier(0);
      asm volatile("" : "+v"(fl));
      if (__ballot(fl != E + (unsigned)(layer + 1)) & fm) poll_eq(ws, po, E + (unsigned)(layer + 1), fm, ~0ull);
    }
    BAR;
    glds4c(xa1, vXA, sb + 40960 + lw * 4096); glds4c(xa2, vXA, sb + 2 * 40960 + lw * 4096);
  }
  ST(layer * 24 + 18)
}

__global__ __launch_bounds__(512, 2) void mega_kernel(const u16* __restrict__ x0, const u16* __restrict__ wqkvA,
                                                      const u16* __restrict__ woA, const u16* __restrict__ wqkvB,
                                                      const u16* __restrict__ wphi, const u16* __restrict__ woB,
                                                      u16* __restrict__ out, char* __restrict__ ws) {
  extern __shared__ __attribute__((aligned(16))) char smem[];
  const int w = __builtin_amdgcn_readfirstlane(threadIdx.x >> 6);
  u64 E = 0;
  {
    const int bid = blockIdx.x, lane = threadIdx.x & 63;
    const int rt = 2 * (bid & 4) + ((bid >> 3) & 7), hp = 4 * (bid & 3) + (bid >> 6), m0 = rt * 128;
    if (w < 4) {
      if (w == 3) {
        unsigned long long pc;
        unsigned len;
        asm volatile("s_getpc_b64 %0\n.Lmk_pc:\n\ts_mov_b32 %1, .Lmk_end-.Lmk_pc" : "=s"(pc), "=s"(len));
        const unsigned per = ((len >> 10) + 1) * 128, off = ((bid >> 3) & 7) * per + (lane * 128 < per ? lane * 128 : 0);
        asm volatile("s_mov_b32 m0, %2\n\ts_nop 0\n\tglobal_load_lds_dword %0, %1" :: "v"(off < len - 4 ? off : 0u), "s"(pc), "s"(lds_addr(smem) + LDS_TOTAL - 1024) : "memory", "m0");
      }
      const unsigned xo = (unsigned)((m0 + hp * 8 + w * 2) * (DM * 2) + lane * 16);
      glds4v(x0, xo, xo, xo, xo, lds_addr(smem) + 122880 + w * 4096);
    } else {
      const int lw = w & 3;
      const int kB = 16 * lw + (lane >> 3), fB = (kB >> 1) & 1;
      const unsigned vB0 = (unsigned)((kB * 3072 + hp * 64 + (((((lane & 7) >> 1) ^ fB) << 1) | (lane & 1)) * 8) * 2);
      const unsigned vB1 = (unsigned)(((kB + 8) * 3072 + hp * 64 + (((((lane & 7) >> 1) ^ (fB | 2)) << 1) | (lane & 1)) * 8) * 2) - 1024u;
      const unsigned vA = (unsigned)(((m0 + lw * 32 + (lane >> 3)) * DM + ((lane & 7) ^ (lane >> 3)) * 8) * 2);
#pragma unroll
      for (int s = 0; s < 1; ++s) {
        const int kt = (rt & 8) + s;
        const unsigned sA = lds_addr(smem) + s * 40960, sB = sA + 16384;
        glds4v(x0 + kt * 64, vA, vA + 15360u, vA + 2 * 15360u, vA + 3 * 15360u, sA + lw * 4096);
        const u16* b = wqkvA + (size_t)kt * 64 * 3072;
        glds4v(b, vB0, vB1, vB0, vB1, sB + lw * 6144);
        glds2v(b + 2048, vB0, vB1, sB + lw * 6144 + 4096);
      }
      asm volatile("s_load_dwordx2 %0, %1, 0x0\n\ts_waitcnt lgkmcnt(0)" : "=s"(E) : "s"(ws) : "memory");
    }
  }
  f32x16 xq;
  Next nx;
  asm volatile("" : "=s"(nx.kord), "=s"(nx.wq), "=s"(nx.tw));
#pragma unroll 1
  for (int layer = 0; layer < 4; ++layer) {
    if (layer & 1) g1_phase<1>(smem, ws, E, w, xq, nx, layer, x0, wqkvB, woB, wphi, wqkvA, woA);
    else g1_phase<0>(smem, ws, E, w, xq, nx, layer, x0, wqkvA, woA, nullptr, wqkvB, woB);
    g2_phase(smem, ws, E, w, xq, nx, layer, x0, (layer & 1) ? woB : woA, (layer & 1) ? wqkvA : wqkvB, out);
  }
  asm volatile(".Lmk_end:");
  WAITV(0);
  if (blockIdx.x == 0 && w == 0 && lane_id() == 0) __hip_atomic_store((gu64*)ws, E + 16u, __ATOMIC_RELAXED, __HIP_MEMORY_SCOPE_AGENT);
}

extern "C" void kernel_launch(void* const* d_in, const int* in_sizes, int n_in,
                              void* d_out, int out_size, void* d_ws, size_t ws_size,
                              hipStream_t stream) {
  hipFuncSetAttribute((const void*)mega_kernel, hipFuncAttributeMaxDynamicSharedMemorySize, LDS_TOTAL);
  mega_kernel<<<256, 512, LDS_TOTAL, stream>>>((const u16*)d_in[0], (const u16*)d_in[1], (const u16*)d_in[2], (const u16*)d_in[4],
                                               (const u16*)d_in[5], (const u16*)d_in[6], (u16*)d_out, (char*)d_ws);
}
```

```cpp
#include <hip/hip_runtime.h>
#include <hip/hip_bf16.h>
#include <stdint.h>
#include <type_traits>
#pragma clang diagnostic ignored "-Winline-asm"
#pragma clang diagnostic ignored "-Wunused-value"

typedef unsigned short u16;
typedef __attribute__((ext_vector_type(8))) short bf16x8;
typedef __attribute__((ext_vector_type(4))) short bf16x4;
typedef __attribute__((ext_vector_type(4))) float f32x4;
typedef __attribute__((ext_vector_type(4))) unsigned u32x4;
typedef __attribute__((ext_vector_type(2))) unsigned u32x2;
typedef __attribute__((ext_vector_type(8))) float f32x8;
typedef __attribute__((ext_vector_type(16))) unsigned u32x16;
typedef __attribute__((ext_vector_type(16))) float f32x16;
typedef __attribute__((ext_vector_type(32))) float f32x32;
template <int I, typename T> __device__ __forceinline__ f32x4 quad(const T& v) { return __builtin_shufflevector(v, v, 4 * I, 4 * I + 1, 4 * I + 2, 4 * I + 3); }

constexpr int SEQ = 2048, DM = 1024, NH = 32;
#define ST(i)
constexpr int KVP = 64 * 32 + 64;

typedef __attribute__((ext_vector_type(2))) float f32x2;
typedef __attribute__((ext_vector_type(2))) __bf16 bf16x2_t;
__device__ __forceinline__ unsigned pk2(float a, float b) {
  f32x2 v = {a, b};
  bf16x2_t r = __builtin_convertvector(v, bf16x2_t);
  return __builtin_bit_cast(unsigned, r);
}

__device__ __forceinline__ bf16x4 tr_read(const char* p) {
  return __builtin_amdgcn_ds_read_tr16_b64_v4i16((__attribute__((address_space(3))) bf16x4*)p);
}
template <typename T> __device__ __forceinline__ __attribute__((address_space(3))) T& lds(unsigned a) { return *(__attribute__((address_space(3))) T*)(size_t)a; }
__device__ __forceinline__ bf16x4 tr_read(unsigned a) { return __builtin_amdgcn_ds_read_tr16_b64_v4i16(&lds<bf16x4>(a)); }
__device__ __forceinline__ bf16x8 cat8(bf16x4 a, bf16x4 b) {
  bf16x8 f = {a[0], a[1], a[2], a[3], b[0], b[1], b[2], b[3]};
  return f;
}
__device__ __forceinline__ void glds16s(const void* sbase, unsigned voff, unsigned lds_base) {
  asm volatile("s_mov_b32 m0, %2\n\ts_nop 0\n\tglobal_load_lds_dwordx4 %0, %1" :: "v"(voff), "s"(sbase), "s"(lds_base) : "memory", "m0");
}
__device__ __forceinline__ void glds16c(const void* sbase, unsigned voff, unsigned lds_base) {
  asm volatile("s_mov_b32 m0, %2\n\ts_nop 0\n\tglobal_load_lds_dwordx4 %0, %1 sc1" :: "v"(voff), "s"(sbase), "s"(lds_base) : "memory", "m0");
}
#define GL_ "\n\tglobal_load_lds_dwordx4 "
__device__ __forceinline__ void glds4c(const void* sbase, unsigned v, unsigned lds_base) {
  asm volatile("s_mov_b32 m0, %2\n\ts_nop 0" GL_ "%0, %1 sc1" GL_ "%0, %1 offset:1024 sc1" GL_ "%0, %1 offset:2048 sc1" GL_ "%0, %1 offset:3072 sc1"
               :: "v"(v), "s"(sbase), "s"(lds_base) : "memory", "m0");
}
__device__ __forceinline__ void glds4v(const void* sbase, unsigned v0, unsigned v1, unsigned v2, unsigned v3, unsigned lds_base) {
  asm volatile("s_mov_b32 m0, %5\n\ts_nop 0" GL_ "%0, %4 sc1" GL_ "%1, %4 offset:1024 sc1" GL_ "%2, %4 offset:2048 sc1" GL_ "%3, %4 offset:3072 sc1"
               :: "v"(v0), "v"(v1), "v"(v2), "v"(v3), "s"(sbase), "s"(lds_base) : "memory", "m0");
}
__device__ __forceinline__ void glds2v(const void* sbase, unsigned v0, unsigned v1, unsigned lds_base) {
  asm volatile("s_mov_b32 m0, %3\n\ts_nop 0" GL_ "%0, %2 sc1" GL_ "%1, %2 offset:1024 sc1" :: "v"(v0), "v"(v1), "s"(sbase), "s"(lds_base) : "memory", "m0");
}
__device__ __forceinline__ void gld16(u32x4& d, unsigned voff, const void* sbase) {
  asm volatile("global_load_dwordx4 %0, %1, %2" : "=v"(d) : "v"(voff), "s"(sbase) : "memory");
}
__device__ __forceinline__ unsigned lds_addr(const void* p) { return (unsigned)(size_t)p; }
#define WAITV(n) asm volatile("s_waitcnt vmcnt(" #n ")" ::: "memory")
#define WAITVN(n) asm volatile("s_waitcnt vmcnt(%0)" :: "n"(n) : "memory")
#define WAITL0 do { asm volatile("" ::: "memory"); __builtin_amdgcn_s_waitcnt(0xC07F); asm volatile("" ::: "memory"); } while (0)
#define BAR __builtin_amdgcn_s_barrier()
#define ARRIVED(...) do { WAITV(0); __builtin_amdgcn_sched_barrier(0); asm volatile("" : __VA_ARGS__); } while (0)

template <int N> __device__ __forceinline__ float dpp_ror(float x) {
  return __uint_as_float(__builtin_amdgcn_update_dpp(0u, __float_as_uint(x), 0x120 + N, 0xf, 0xf, false));
}
__device__ __forceinline__ float r16_sum(float x) { x += dpp_ror<8>(x); x += dpp_ror<4>(x); x += dpp_ror<2>(x); return x + dpp_ror<1>(x); }
__device__ __forceinline__ void max8pn(const f32x4& a, const f32x4& b, float& mx, float& mn) {
  asm("v_max3_f32 %0, %2, %3, %4\n\tv_max3_f32 %1, -%2, -%3, -%4\n\tv_max3_f32 %0, %0, %5, %6\n\tv_max3_f32 %1, %1, -%5, -%6\n\t"
      "v_max3_f32 %0, %0, %7, %8\n\tv_max3_f32 %1, %1, -%7, -%8\n\tv_max_f32 %0, %0, %9\n\tv_max_f32 %1, %1, -%9\n\ts_nop 0"
      : "=&v"(mx), "=&v"(mn) : "v"(a[0]), "v"(a[1]), "v"(a[2]), "v"(a[3]), "v"(b[0]), "v"(b[1]), "v"(b[2]), "v"(b[3]));
}
__device__ __forceinline__ float vmaxf_n(float a, float b) { float r; asm("v_max_f32 %0, %1, %2\n\ts_nop 0" : "=v"(r) : "v"(a), "v"(b)); return r; }
template <bool MAXOP> __device__ __forceinline__ float rop(float a, float b) { return MAXOP ? vmaxf_n(a, b) : a + b; }
template <bool MAXOP> __device__ __forceinline__ void xr4(float& a, float& b, float& c, float& d) {
  auto r = __builtin_amdgcn_permlane16_swap(__float_as_uint(a), __float_as_uint(b), false, false);
  auto q = __builtin_amdgcn_permlane16_swap(__float_as_uint(c), __float_as_uint(d), false, false);
  const unsigned ab = __float_as_uint(rop<MAXOP>(__uint_as_float(r[0]), __uint_as_float(r[1])));
  const unsigned cd = __float_as_uint(rop<MAXOP>(__uint_as_float(q[0]), __uint_as_float(q[1])));
  auto t = __builtin_amdgcn_permlane32_swap(ab, cd, false, false);
  const unsigned u = __float_as_uint(rop<MAXOP>(__uint_as_float(t[0]), __uint_as_float(t[1])));
  auto h = __builtin_amdgcn_permlane32_swap(u, u, false, false);
  auto p0 = __builtin_amdgcn_permlane16_swap(h[0], h[0], false, false);
  auto p1 = __builtin_amdgcn_permlane16_swap(h[1], h[1], false, false);
  a = __uint_as_float(p0[0]); b = __uint_as_float(p0[1]); c = __uint_as_float(p1[0]); d = __uint_as_float(p1[1]);
}

__device__ __forceinline__ unsigned xs4(float a, float b, float c, float d) {
  auto r = __builtin_amdgcn_permlane16_swap(__float_as_uint(a), __float_as_uint(b), false, false);
  auto q = __builtin_amdgcn_permlane16_swap(__float_as_uint(c), __float_as_uint(d), false, false);
  auto t = __builtin_amdgcn_permlane32_swap(__float_as_uint(__uint_as_float(r[0]) + __uint_as_float(r[1])), __float_as_uint(__uint_as_float(q[0]) + __uint_as_float(q[1])), false, false);
  return __float_as_uint(__uint_as_float(t[0]) + __uint_as_float(t[1]));
}

__device__ __forceinline__ f32x4 sum4(const u32x4 (&v)[4]) {
  float r0, r1, r2, r3, t0, t1, t2, t3;
  asm("v_add_f32 %0, %8, %12\n\tv_add_f32 %1, %9, %13\n\tv_add_f32 %2, %10, %14\n\tv_add_f32 %3, %11, %15\n\t"
      "v_add_f32 %4, %16, %20\n\tv_add_f32 %5, %17, %21\n\tv_add_f32 %6, %18, %22\n\tv_add_f32 %7, %19, %23\n\t"
      "v_add_f32 %0, %0, %4\n\tv_add_f32 %1, %1, %5\n\tv_add_f32 %2, %2, %6\n\tv_add_f32 %3, %3, %7\n\ts_nop 0"
      : "=&v"(r0), "=&v"(r1), "=&v"(r2), "=&v"(r3), "=&v"(t0), "=&v"(t1), "=&v"(t2), "=&v"(t3)
      : "v"(v[0][0]), "v"(v[0][1]), "v"(v[0][2]), "v"(v[0][3]), "v"(v[1][0]), "v"(v[1][1]), "v"(v[1][2]), "v"(v[1][3]),
        "v"(v[2][0]), "v"(v[2][1]), "v"(v[2][2]), "v"(v[2][3]), "v"(v[3][0]), "v"(v[3][1]), "v"(v[3][2]), "v"(v[3][3]));
  return f32x4{r0, r1, r2, r3};
}
#define MFMA(a, b, c) __builtin_amdgcn_mfma_f32_16x16x32_bf16(a, b, c, 0, 0, 0)

typedef unsigned long long u64;
typedef __attribute__((address_space(1))) unsigned long long gu64;
constexpr unsigned WS_FLAGS = 4096;
constexpr int FLS = 64;
constexpr unsigned WS_G0 = 65536;
constexpr unsigned WS_CSP = WS_G0 + SEQ * 16;
constexpr unsigned WS_ST = WS_CSP + 16 * 16 * 768;
constexpr unsigned WS_X = 1u << 21;
constexpr unsigned WS_O = WS_X + 3u * SEQ * DM * 2;
constexpr unsigned KVPB = KVP * 2, KVFB = KVP * 4;
constexpr unsigned EMPTY = 0xffffffffu;
constexpr unsigned WS_KV = WS_O + 4u * SEQ * DM * 2;
constexpr unsigned KVLB = 16u * NH * KVPB + 16u * KVFB;
constexpr unsigned WS_HL = WS_KV + 2u * KVLB;
constexpr unsigned WS_CSA = WS_HL + 512u * 4096;
constexpr unsigned WS_CPA = WS_CSA + 256u * 1024;
constexpr unsigned WS_IMG = WS_CPA + 16 * 16 * 768;
constexpr unsigned WS_END = WS_IMG + 2048 * 1024;
constexpr int LDS_TOTAL = 163840;
constexpr int OFF_WO = 49152;
struct Next { u64 kord; const char* wq; unsigned tw; };
template <int V> using IC = std::integral_constant<int, V>;
__device__ __forceinline__ bf16x8 ones8() { unsigned e = 0x3F803F80u; asm volatile("" : "+v"(e)); return __builtin_bit_cast(bf16x8, u32x4{e, e, e, e}); }
__device__ __forceinline__ u32x4 empty4() { unsigned e = EMPTY; asm volatile("" : "+v"(e)); return u32x4{e, e, e, e}; }
__device__ __forceinline__ u64 korder(int v, int own) {
  const u64 t = v == 0 ? 0x3b210fedca987654ull : v == 1 ? 0xb37654210fedca98ull : v == 2 ? 0x3ba987654210fedcull : 0xb3fedca987654210ull;
  const u64 inv = v == 0 ? 0xa987e6543210fdcbull : v == 1 ? 0x6543f210dcbae987ull : v == 2 ? 0x3210edcba987f654ull : 0xdcbaf9876543e210ull;
  const u64 low = (1ull << (4 * ((inv >> (4 * own)) & 15))) - 1ull;
  return (u64)own | ((t & low) << 4) | (t & ~((low << 4) | 15ull));
}
__device__ __forceinline__ u64 korder0(int rt) {
  const int r = rt & 7;
  const u64 own = (0x765432765432ull >> (4 * (r < 6 ? r : r - 6))) & 0xffffffull, oth = 0x7654321076543210ull >> (4 * r);
  const u64 t = 0x10ull | ((oth & 15ull) << 8) | (own << 12) | ((oth >> 4) << 36);
  return t | (rt < 8 ? 0x8888888000000800ull : 0x0000000888888088ull);
}
__device__ __forceinline__ int knib(u64 t, int s) { return (int)(t >> (4 * s)) & 15; }
__device__ __forceinline__ unsigned kfirst(u64 t) { return (1u << knib(t, 1)) | (1u << knib(t, 2)) | (1u << knib(t, 3)) | (1u << knib(t, 4)); }
__device__ __forceinline__ int kv1(int bid) { return (bid + (0x4924 >> ((bid >> 2) & 14))) & 3; }

__device__ __forceinline__ bool img(int layer, int hp) { return layer >= 2 && (hp & 7) == 3; }
__device__ __forceinline__ const char* wq_src(const char* ws, const void* W, int layer, int hp) { return img(layer, hp) ? ws + WS_IMG + ((layer & 1) * 768 + (hp >> 3) * 384) * 1024 : (const char*)W; }
__device__ __forceinline__ const char* wo_src(const char* ws, const void* W, int layer, int hp) { return img(layer, hp) ? ws + WS_IMG + (1536 + (layer & 1) * 256 + (hp >> 3) * 128) * 1024 : (const char*)W; }

template <typename T> __device__ __forceinline__ T* uni(T* p) {
  const unsigned long long v = (unsigned long long)p;
  const unsigned lo = __builtin_amdgcn_readfirstlane((unsigned)v), hi = __builtin_amdgcn_readfirstlane((unsigned)(v >> 32));
  return (T*)(((unsigned long long)hi << 32) | lo);
}
__device__ __forceinline__ int lane_id() {
  int l;
  asm volatile("v_mbcnt_lo_u32_b32 %0, -1, 0\n\tv_mbcnt_hi_u32_b32 %0, -1, %0" : "=v"(l));
  __builtin_assume(l >= 0 && l < 64);
  return l;
}
#define IDS \
  const int lane = lane_id(); \
  asm volatile("" : "+s"(w)); \
  const int tid = w * 64 + lane; \
  int bid = blockIdx.x; asm volatile("" : "+s"(bid)); \
  ws = uni(ws); asm volatile("" : "+s"(ws)); \
  layer = __builtin_amdgcn_readfirstlane(layer); x0 = uni(x0); wo = uni(wo); \
    \
  const int rt = 2 * (bid & 4) + ((bid >> 3) & 7); \
  const int hp = 4 * (bid & 3) + (bid >> 6); \
  const int m0 = rt * 128; \
  const int fr = lane & 15, g = lane >> 4; \
  const int q4 = (lane & 15) >> 2, p4 = lane & 3; \
  const __amdgpu_buffer_rsrc_t rsW = __builtin_amdgcn_make_buffer_rsrc((void*)ws, (short)0, (int)WS_END, 0x00020000); \
  (void)m0; (void)fr; (void)q4; (void)p4; (void)rsW; (void)g;

__device__ __forceinline__ void code_touch(unsigned& junk, int bid, int lane) {
  unsigned long long pc;
  unsigned len, d;
  asm volatile("s_getpc_b64 %0\n.Lct%=:\n\ts_mov_b32 %2, .Lct%=-.Lmk_pc\n\ts_mov_b32 %1, .Lmk_end-.Lmk_pc" : "=s"(pc), "=s"(len), "=s"(d));
  pc -= d;
  const unsigned per = ((len >> 10) + 1) * 128, off = ((bid >> 3) & 7) * per + (lane * 128 < per ? lane * 128 : 0);
  asm volatile("global_load_dword %0, %1, %2" : "+v"(junk) : "v"(off < len - 4 ? off : 0u), "s"(pc) : "memory");
}

__device__ __forceinline__ void publish(__amdgpu_buffer_rsrc_t rsW, int tid, unsigned idx, u64 v) {
  unsigned off = WS_FLAGS + idx * FLS;
  asm volatile("" : "+v"(off), "+v"(v));
  WAITV(0);
  WAITL0; BAR;
  if (tid == 192) __builtin_amdgcn_raw_buffer_store_b64(__builtin_bit_cast(u32x2, v), rsW, off, 0, 16);
  asm volatile("" ::: "memory");
}
__device__ __forceinline__ u64 poll_flags(char* ws, u64 E, int set, int rt, int hp, int lane, unsigned n, u64 mask, u64 now = ~0ull) {
  u64 miss = 0;
  const int l = lane & 15, g = lane >> 4;
  const int fi = set * 256 + (g == 2 ? l * 16 + hp : rt * 16 + l);
  const unsigned need = ((mask >> lane) & 1) ? n : 0u;
  gu64* fp = (gu64*)(ws + (need ? WS_FLAGS + fi * FLS : 0));
  _Pragma("unroll 1") for (unsigned spins = 0; spins < (1u << 18); ++spins) {
    const u64 d = __hip_atomic_load(fp, __ATOMIC_RELAXED, __HIP_MEMORY_SCOPE_AGENT) - E;
    const bool ok = (need == 0u) || (d >= (u64)need && d <= 16ull);
    miss = __ballot(!ok);
    if (!(miss & now)) break;
    __builtin_amdgcn_s_sleep(1);
  }
  asm volatile("s_waitcnt vmcnt(0)" ::: "memory");
  return miss;
}

__device__ __forceinline__ u64 poll_eq(const char* ws, unsigned off, u64 want, u64 mask, u64 now) {
  u64 miss;
  _Pragma("unroll 1") for (unsigned spins = 0; spins < (1u << 18); ++spins) {
    u64 d;
    asm volatile("global_load_dwordx2 %0, %1, %2 sc1\n\ts_waitcnt vmcnt(0)" : "=v"(d) : "v"(off), "s"(ws) : "memory");
    miss = __ballot(d != want) & mask;
    if (!(miss & now)) break;
    __builtin_amdgcn_s_sleep(1);
  }
  return miss;
}

#define G1_MACROS \
  ".macro G1M rb, j, s, z\n" \
  " .set g1d, 160+24*(\\rb)+4*(((\\j)+2)-6*((\\j)>>2))\n .set g1a, 80+40*(\\s)+4*(\\rb)\n .set g1b, 96+40*(\\s)+4*(\\j)\n" \
  " .if ((\\j)>>1) == 1\n  .set g1t, g1a\n  .set g1a, g1b\n  .set g1b, g1t\n .endif\n" \
  " .if \\z\n  v_mfma_f32_16x16x32_bf16 v[g1d:g1d+3], v[g1b:g1b+3], v[g1a:g1a+3], 0\n" \
  " .else\n  v_mfma_f32_16x16x32_bf16 v[g1d:g1d+3], v[g1b:g1b+3], v[g1a:g1a+3], v[g1d:g1d+3]\n .endif\n" \
  ".endm\n" \
  ".macro G1RA rb, s, ra, oa\n" \
  " ds_read_b128 v[80+40*(\\s)+4*(\\rb):83+40*(\\s)+4*(\\rb)], \\ra offset:(\\oa)+(\\rb)*2048\n" \
  ".endm\n" \
  ".macro G1RB k, s, r0, r1, ob\n" \
  " .if (\\k) & 2\n  ds_read_b64_tr_b16 v[96+40*(\\s)+2*(\\k):97+40*(\\s)+2*(\\k)], \\r1 offset:(\\ob)+(((\\k)>>2)+1-3*((\\k)>>3))*2048+((\\k)&1)*512\n" \
  " .else\n  ds_read_b64_tr_b16 v[96+40*(\\s)+2*(\\k):97+40*(\\s)+2*(\\k)], \\r0 offset:(\\ob)+(((\\k)>>2)+1-3*((\\k)>>3))*2048+((\\k)&1)*512\n .endif\n" \
  ".endm\n" \
    \
  ".macro G1BK i, g, r0, r1, ob\n G1RB 4*(\\g)+((\\i)&3), (\\i)>>2, \\r0, \\r1, (\\ob)+((\\i)>>2)*12288\n.endm\n" \
  ".macro G1AK rb, s, a0, a1, oa\n .if \\s\n  G1RA \\rb, 1, \\a1, \\oa\n .else\n  G1RA \\rb, 0, \\a0, \\oa\n .endif\n.endm\n" \
  ".macro G1RDB g, r0, r1, ob\n .set g1i, 0\n .rept 8\n  G1BK g1i, \\g, \\r0, \\r1, \\ob\n  .set g1i, g1i+1\n .endr\n.endm\n" \
  ".macro G1RDA a0, a1, oa\n .set g1i, 0\n .rept 8\n  G1AK (g1i>>1)^2, g1i&1, \\a0, \\a1, \\oa\n  .set g1i, g1i+1\n .endr\n.endm\n" \
    \
  ".macro G1Q g, rb, q, i, z\n G1M \\rb, 2*(\\g)+((((\\i)>>1)^(\\q))&1), (((\\i)+1)>>1)&1, (\\z)&&(((\\i)&1)==0)\n.endm\n" \
    \
  ".macro G1SG g, z, rd, a0, a1, r0, r1, o\n" \
  " .set g1i, 0\n .rept 16\n" \
  "  .if ((\\g) == 0) && (g1i == 8) && (\\rd)\n   s_waitcnt lgkmcnt(7)\n   s_nop 0\n  .endif\n" \
  "  .if ((\\g) == 0) && (g1i == 15)\n   s_waitcnt lgkmcnt(0)\n  .endif\n" \
  "  G1Q \\g, (g1i>>2)^(2-((\\g)&1)), g1i>>2, g1i&3, \\z\n" \
  "  .if \\rd\n" \
  "   .if (\\g) == 0\n    .if g1i < 7\n     G1BK g1i, 2, \\r0, \\r1, \\o\n    .elseif g1i == 8\n     G1BK 7, 2, \\r0, \\r1, \\o\n    .endif\n" \
  "   .elseif (\\g) == 1\n    .if g1i < 8\n     G1BK g1i, 0, \\r0, \\r1, \\o\n    .endif\n" \
  "   .elseif (g1i & 2) == 0\n    G1BK (g1i&1)+2*(g1i>>2), 1, \\r0, \\r1, \\o\n" \
  "   .elseif (\\rd) == 1\n    G1AK (g1i>>2)^2, 1-(g1i&1), \\a0, \\a1, \\o\n   .endif\n" \
  "  .endif\n" \
  "  .set g1i, g1i+1\n .endr\n" \
  ".endm\n" \
  ""
#define G1_SLIDE \
  "s_cmp_eq_u32 %[wm], 0\n v_pk_fma_f32 v[240:241], v[96:97], v[138:139], v[240:241] op_sel_hi:[1,0,1]\n " \
  "v_pk_fma_f32 v[242:243], v[98:99], v[138:139], v[242:243] op_sel_hi:[1,0,1]\n " \
  "v_pk_fma_f32 v[244:245], v[100:101], v[138:139], v[244:245] op_sel_hi:[1,0,1]\n " \
  "v_pk_fma_f32 v[246:247], v[102:103], v[138:139], v[246:247] op_sel_hi:[1,0,1]\n v_pk_mul_f32 v[240:241], v[138:139], v[240:241] op_sel:[1,0]\n " \
  "v_pk_mul_f32 v[242:243], v[138:139], v[242:243] op_sel:[1,0]\n v_pk_mul_f32 v[244:245], v[138:139], v[244:245] op_sel:[1,0]\n " \
  "v_pk_mul_f32 v[246:247], v[138:139], v[246:247] op_sel:[1,0]\n v_pk_fma_f32 v[248:249], v[104:105], v[144:145], v[248:249] op_sel_hi:[1,0,1]\n " \
  "v_pk_fma_f32 v[250:251], v[106:107], v[144:145], v[250:251] op_sel_hi:[1,0,1]\n " \
  "v_pk_fma_f32 v[252:253], v[104:105], v[144:145], v[252:253] op_sel:[0,1,0]\n " \
  "v_pk_fma_f32 v[254:255], v[106:107], v[144:145], v[254:255] op_sel:[0,1,0]\n v_pk_mul_f32 v[248:249], v[248:249], v[108:109]\n " \
  "v_pk_mul_f32 v[250:251], v[250:251], v[110:111]\n v_pk_mul_f32 v[252:253], v[252:253], v[108:109]\n v_pk_mul_f32 v[254:255], v[254:255], v[110:111]\n " \
  "v_cvt_pk_bf16_f32 v240, v240, v241\n v_cvt_pk_bf16_f32 v241, v242, v243\n v_cvt_pk_bf16_f32 v242, v244, v245\n v_cvt_pk_bf16_f32 v243, v246, v247\n " \
  "v_cvt_pk_bf16_f32 v248, v248, v249\n v_cvt_pk_bf16_f32 v249, v250, v251\n v_cvt_pk_bf16_f32 v255, v254, v255\n v_cvt_pk_bf16_f32 v254, v252, v253\n " \
  "v_min_u32 v104, -2, v240\n v_min_u32 v105, -2, v241\n v_min_u32 v106, -2, v242\n v_min_u32 v107, -2, v243\n v_min_u32 v108, -2, v248\n " \
  "v_min_u32 v109, -2, v249\n v_min_u32 v110, -2, v254\n v_min_u32 v111, -2, v255\n s_waitcnt lgkmcnt(0)\n s_cbranch_scc1 .Lg1d%=\n " \
  "buffer_store_dwordx4 v[104:107], v55, %[rs], 0 offen sc1\n buffer_store_dwordx4 v[108:111], v55, %[rs], 0 offen offset:1024 sc1\n s_branch .Lg1e%=\n " \
  ".Lg1d%=:\n v_mov_b32 v53, 1\n ds_write_b128 v58, v[104:107]\n ds_write_b128 v58, v[108:111] offset:1024\n ds_write_b32 v59, v53\n .Lg1e%=:\n " \
  "v_pk_fma_f32 v[232:233], v[148:149], v[138:139], v[232:233] op_sel_hi:[1,0,1]\n " \
  "v_pk_fma_f32 v[234:235], v[150:151], v[138:139], v[234:235] op_sel_hi:[1,0,1]\n " \
  "v_pk_fma_f32 v[236:237], v[88:89], v[138:139], v[236:237] op_sel_hi:[1,0,1]\n " \
  "v_pk_fma_f32 v[238:239], v[90:91], v[138:139], v[238:239] op_sel_hi:[1,0,1]\n " \
  "v_pk_fma_f32 v[216:217], v[96:97], v[136:137], v[216:217] op_sel_hi:[1,0,1]\n " \
  "v_pk_fma_f32 v[218:219], v[98:99], v[136:137], v[218:219] op_sel_hi:[1,0,1]\n " \
  "v_pk_fma_f32 v[220:221], v[100:101], v[136:137], v[220:221] op_sel_hi:[1,0,1]\n " \
  "v_pk_fma_f32 v[222:223], v[102:103], v[136:137], v[222:223] op_sel_hi:[1,0,1]\n v_pk_mul_f32 v[216:217], v[136:137], v[216:217] op_sel:[1,0]\n " \
  "v_pk_mul_f32 v[218:219], v[136:137], v[218:219] op_sel:[1,0]\n v_pk_mul_f32 v[220:221], v[136:137], v[220:221] op_sel:[1,0]\n " \
  "v_pk_mul_f32 v[222:223], v[136:137], v[222:223] op_sel:[1,0]\n v_cvt_pk_bf16_f32 v232, v232, v233\n v_cvt_pk_bf16_f32 v233, v234, v235\n " \
  "v_cvt_pk_bf16_f32 v234, v236, v237\n v_cvt_pk_bf16_f32 v235, v238, v239\n v_cvt_pk_bf16_f32 v216, v216, v217\n v_cvt_pk_bf16_f32 v217, v218, v219\n " \
  "v_cvt_pk_bf16_f32 v218, v220, v221\n v_cvt_pk_bf16_f32 v219, v222, v223\n " \
  "v_pk_fma_f32 v[208:209], v[148:149], v[136:137], v[208:209] op_sel_hi:[1,0,1]\n " \
  "v_pk_fma_f32 v[210:211], v[150:151], v[136:137], v[210:211] op_sel_hi:[1,0,1]\n " \
  "v_pk_fma_f32 v[212:213], v[88:89], v[136:137], v[212:213] op_sel_hi:[1,0,1]\n " \
  "v_pk_fma_f32 v[214:215], v[90:91], v[136:137], v[214:215] op_sel_hi:[1,0,1]\n " \
  "v_pk_fma_f32 v[192:193], v[96:97], v[142:143], v[192:193] op_sel_hi:[1,0,1]\n " \
  "v_pk_fma_f32 v[194:195], v[98:99], v[142:143], v[194:195] op_sel_hi:[1,0,1]\n " \
  "v_pk_fma_f32 v[196:197], v[100:101], v[142:143], v[196:197] op_sel_hi:[1,0,1]\n " \
  "v_pk_fma_f32 v[198:199], v[102:103], v[142:143], v[198:199] op_sel_hi:[1,0,1]\n v_pk_mul_f32 v[192:193], v[142:143], v[192:193] op_sel:[1,0]\n " \
  "v_pk_mul_f32 v[194:195], v[142:143], v[194:195] op_sel:[1,0]\n v_pk_mul_f32 v[196:197], v[142:143], v[196:197] op_sel:[1,0]\n " \
  "v_pk_mul_f32 v[198:199], v[142:143], v[198:199] op_sel:[1,0]\n v_mfma_f32_16x16x32_bf16 v[120:123], v[240:243], v[232:235], v[40:43]\n " \
  "v_mfma_f32_16x16x32_bf16 v[236:239], v[216:219], v[232:235], v[36:39]\n v_cvt_pk_bf16_f32 v208, v208, v209\n v_cvt_pk_bf16_f32 v209, v210, v211\n " \
  "v_cvt_pk_bf16_f32 v210, v212, v213\n v_cvt_pk_bf16_f32 v211, v214, v215\n v_cvt_pk_bf16_f32 v192, v192, v193\n v_cvt_pk_bf16_f32 v193, v194, v195\n " \
  "v_cvt_pk_bf16_f32 v194, v196, v197\n v_cvt_pk_bf16_f32 v195, v198, v199\n " \
  "v_pk_fma_f32 v[184:185], v[148:149], v[142:143], v[184:185] op_sel_hi:[1,0,1]\n " \
  "v_pk_fma_f32 v[186:187], v[150:151], v[142:143], v[186:187] op_sel_hi:[1,0,1]\n " \
  "v_pk_fma_f32 v[188:189], v[88:89], v[142:143], v[188:189] op_sel_hi:[1,0,1]\n " \
  "v_pk_fma_f32 v[190:191], v[90:91], v[142:143], v[190:191] op_sel_hi:[1,0,1]\n " \
  "v_pk_fma_f32 v[168:169], v[96:97], v[140:141], v[168:169] op_sel_hi:[1,0,1]\n " \
  "v_pk_fma_f32 v[170:171], v[98:99], v[140:141], v[170:171] op_sel_hi:[1,0,1]\n " \
  "v_pk_fma_f32 v[172:173], v[100:101], v[140:141], v[172:173] op_sel_hi:[1,0,1]\n " \
  "v_pk_fma_f32 v[174:175], v[102:103], v[140:141], v[174:175] op_sel_hi:[1,0,1]\n v_pk_mul_f32 v[168:169], v[140:141], v[168:169] op_sel:[1,0]\n " \
  "v_pk_mul_f32 v[170:171], v[140:141], v[170:171] op_sel:[1,0]\n v_pk_mul_f32 v[172:173], v[140:141], v[172:173] op_sel:[1,0]\n " \
  "v_pk_mul_f32 v[174:175], v[140:141], v[174:175] op_sel:[1,0]\n v_mfma_f32_16x16x32_bf16 v[220:223], v[216:219], v[208:211], v[40:43]\n " \
  "v_mfma_f32_16x16x32_bf16 v[212:215], v[192:195], v[208:211], v[36:39]\n v_cvt_pk_bf16_f32 v184, v184, v185\n v_cvt_pk_bf16_f32 v185, v186, v187\n " \
  "v_cvt_pk_bf16_f32 v186, v188, v189\n v_cvt_pk_bf16_f32 v187, v190, v191\n v_cvt_pk_bf16_f32 v168, v168, v169\n v_cvt_pk_bf16_f32 v169, v170, v171\n " \
  "v_cvt_pk_bf16_f32 v170, v172, v173\n v_cvt_pk_bf16_f32 v171, v174, v175\n " \
  "v_pk_fma_f32 v[160:161], v[148:149], v[140:141], v[160:161] op_sel_hi:[1,0,1]\n " \
  "v_pk_fma_f32 v[162:163], v[150:151], v[140:141], v[162:163] op_sel_hi:[1,0,1]\n " \
  "v_pk_fma_f32 v[164:165], v[88:89], v[140:141], v[164:165] op_sel_hi:[1,0,1]\n " \
  "v_pk_fma_f32 v[166:167], v[90:91], v[140:141], v[166:167] op_sel_hi:[1,0,1]\n " \
  "v_pk_fma_f32 v[176:177], v[128:129], v[144:145], v[176:177] op_sel_hi:[1,0,1]\n " \
  "v_pk_fma_f32 v[178:179], v[130:131], v[144:145], v[178:179] op_sel_hi:[1,0,1]\n " \
  "v_pk_fma_f32 v[180:181], v[128:129], v[144:145], v[180:181] op_sel:[0,1,0]\n " \
  "v_pk_fma_f32 v[182:183], v[130:131], v[144:145], v[182:183] op_sel:[0,1,0]\n v_pk_mul_f32 v[176:177], v[176:177], v[92:93]\n " \
  "v_pk_mul_f32 v[178:179], v[178:179], v[94:95]\n v_pk_mul_f32 v[180:181], v[180:181], v[92:93]\n v_pk_mul_f32 v[182:183], v[182:183], v[94:95]\n " \
  "v_mfma_f32_16x16x32_bf16 v[196:199], v[192:195], v[184:187], v[40:43]\n v_mfma_f32_16x16x32_bf16 v[188:191], v[168:171], v[184:187], v[36:39]\n " \
  "v_cvt_pk_bf16_f32 v160, v160, v161\n v_cvt_pk_bf16_f32 v161, v162, v163\n v_cvt_pk_bf16_f32 v162, v164, v165\n v_cvt_pk_bf16_f32 v163, v166, v167\n " \
  "v_cvt_pk_bf16_f32 v174, v176, v177\n v_cvt_pk_bf16_f32 v175, v178, v179\n v_cvt_pk_bf16_f32 v180, v180, v181\n v_cvt_pk_bf16_f32 v181, v182, v183\n " \
  "v_pk_fma_f32 v[224:225], v[112:113], v[144:145], v[224:225] op_sel_hi:[1,0,1]\n " \
  "v_pk_fma_f32 v[226:227], v[114:115], v[144:145], v[226:227] op_sel_hi:[1,0,1]\n " \
  "v_pk_fma_f32 v[228:229], v[112:113], v[144:145], v[228:229] op_sel:[0,1,0]\n " \
  "v_pk_fma_f32 v[230:231], v[114:115], v[144:145], v[230:231] op_sel:[0,1,0]\n v_pk_mul_f32 v[224:225], v[224:225], v[116:117]\n " \
  "v_pk_mul_f32 v[226:227], v[226:227], v[118:119]\n v_pk_mul_f32 v[228:229], v[228:229], v[116:117]\n v_pk_mul_f32 v[230:231], v[230:231], v[118:119]\n " \
  "v_mfma_f32_16x16x32_bf16 v[124:127], v[168:171], v[160:163], v[40:43]\n v_cvt_pk_bf16_f32 v246, v224, v225\n v_cvt_pk_bf16_f32 v247, v226, v227\n " \
  "v_cvt_pk_bf16_f32 v252, v228, v229\n v_cvt_pk_bf16_f32 v253, v230, v231\n v_max3_f32 v108, v236, v237, v238\n v_max3_f32 v106, v212, v213, v214\n " \
  "v_max3_f32 v109, v239, v120, v121\n v_max3_f32 v107, v215, v220, v221\n v_max3_f32 v108, v108, v122, v123\n v_max3_f32 v106, v106, v222, v223\n " \
  "v_max_f32 v108, v108, v109\n v_max_f32 v106, v106, v107\n v_max3_f32 v104, v188, v189, v190\n v_max3_f32 v105, v191, v196, v197\n " \
  "v_max3_f32 v104, v104, v198, v199\n v_pk_fma_f32 v[200:201], v[152:153], v[144:145], v[200:201] op_sel_hi:[1,0,1]\n " \
  "v_pk_fma_f32 v[202:203], v[154:155], v[144:145], v[202:203] op_sel_hi:[1,0,1]\n v_max_f32 v104, v104, v105\n v_mov_b32 v110, v108\n " \
  "v_pk_fma_f32 v[204:205], v[152:153], v[144:145], v[204:205] op_sel:[0,1,0]\n " \
  "v_pk_fma_f32 v[206:207], v[154:155], v[144:145], v[206:207] op_sel:[0,1,0]\n v_permlane16_swap_b32 v104, v106\n v_permlane16_swap_b32 v108, v110\n " \
  "v_pk_mul_f32 v[200:201], v[200:201], v[156:157]\n v_pk_mul_f32 v[202:203], v[202:203], v[158:159]\n v_max_f32 v104, v104, v106\n " \
  "v_max_f32 v108, v108, v110\n v_pk_mul_f32 v[204:205], v[204:205], v[156:157]\n v_pk_mul_f32 v[206:207], v[206:207], v[158:159]\n " \
  "v_permlane32_swap_b32 v104, v108\n v_mul_f32 v60, 0x3e8293ee, v139\n v_mul_f32 v62, 0x3e8293ee, v137\n v_max_f32 v104, v104, v108\n " \
  "v_mov_b32 v108, v104\n v_mul_f32 v56, 0x3e8293ee, v143\n v_mul_f32 v146, 0x3e8293ee, v141\n v_permlane32_swap_b32 v104, v108\n " \
  "v_mov_b32 v132, 0x3f803f80\n v_mov_b32 v133, 0x3f803f80\n v_mov_b32 v106, v104\n v_mov_b32 v134, 0x3f803f80\n v_mov_b32 v135, 0x3f803f80\n " \
  "v_permlane16_swap_b32 v104, v106\n v_cvt_pk_bf16_f32 v244, v200, v201\n v_cvt_pk_bf16_f32 v245, v202, v203\n v_cvt_pk_bf16_f32 v250, v204, v205\n " \
  "v_cvt_pk_bf16_f32 v251, v206, v207\n v_cvt_pk_bf16_f32 v176, v200, v201\n v_cvt_pk_bf16_f32 v177, v202, v203\n v_cvt_pk_bf16_f32 v182, v204, v205\n " \
  "v_cvt_pk_bf16_f32 v183, v206, v207\n v_mul_f32_e64 v61, v60, -v108\n v_mul_f32_e64 v63, v62, -v106\n v_mul_f32_e64 v57, v56, -v104\n " \
  "v_pk_fma_f32 v[236:237], v[236:237], v[60:61], v[60:61] op_sel:[0,0,1] op_sel_hi:[1,0,1]\n " \
  "v_pk_fma_f32 v[238:239], v[238:239], v[60:61], v[60:61] op_sel:[0,0,1] op_sel_hi:[1,0,1]\n " \
  "v_pk_fma_f32 v[120:121], v[120:121], v[60:61], v[60:61] op_sel:[0,0,1] op_sel_hi:[1,0,1]\n " \
  "v_pk_fma_f32 v[122:123], v[122:123], v[60:61], v[60:61] op_sel:[0,0,1] op_sel_hi:[1,0,1]\n v_exp_f32 v236, v236\n v_exp_f32 v237, v237\n " \
  "v_exp_f32 v238, v238\n v_exp_f32 v239, v239\n v_exp_f32 v120, v120\n v_exp_f32 v121, v121\n v_exp_f32 v122, v122\n v_exp_f32 v123, v123\n " \
  "v_pk_fma_f32 v[212:213], v[212:213], v[62:63], v[62:63] op_sel:[0,0,1] op_sel_hi:[1,0,1]\n " \
  "v_pk_fma_f32 v[214:215], v[214:215], v[62:63], v[62:63] op_sel:[0,0,1] op_sel_hi:[1,0,1]\n " \
  "v_pk_fma_f32 v[220:221], v[220:221], v[62:63], v[62:63] op_sel:[0,0,1] op_sel_hi:[1,0,1]\n " \
  "v_pk_fma_f32 v[222:223], v[222:223], v[62:63], v[62:63] op_sel:[0,0,1] op_sel_hi:[1,0,1]\n v_cvt_pk_bf16_f32 v236, v236, v237\n " \
  "v_cvt_pk_bf16_f32 v237, v238, v239\n v_cvt_pk_bf16_f32 v238, v120, v121\n v_cvt_pk_bf16_f32 v239, v122, v123\n v_exp_f32 v212, v212\n " \
  "v_exp_f32 v213, v213\n v_mfma_f32_16x16x32_bf16 v[120:123], v[132:135], v[236:239], 0\n " \
  "v_mfma_f32_16x16x32_bf16 v[232:235], v[246:249], v[236:239], 0\n v_mfma_f32_16x16x32_bf16 v[240:243], v[252:255], v[236:239], 0\n " \
  "v_exp_f32 v214, v214\n v_exp_f32 v215, v215\n v_exp_f32 v220, v220\n v_exp_f32 v221, v221\n v_exp_f32 v222, v222\n v_exp_f32 v223, v223\n " \
  "v_pk_fma_f32 v[188:189], v[188:189], v[56:57], v[56:57] op_sel:[0,0,1] op_sel_hi:[1,0,1]\n " \
  "v_pk_fma_f32 v[190:191], v[190:191], v[56:57], v[56:57] op_sel:[0,0,1] op_sel_hi:[1,0,1]\n " \
  "v_pk_fma_f32 v[196:197], v[196:197], v[56:57], v[56:57] op_sel:[0,0,1] op_sel_hi:[1,0,1]\n " \
  "v_pk_fma_f32 v[198:199], v[198:199], v[56:57], v[56:57] op_sel:[0,0,1] op_sel_hi:[1,0,1]\n v_cvt_pk_bf16_f32 v212, v212, v213\n " \
  "v_cvt_pk_bf16_f32 v213, v214, v215\n v_cvt_pk_bf16_f32 v214, v220, v221\n v_cvt_pk_bf16_f32 v215, v222, v223\n v_exp_f32 v188, v188\n " \
  "s_cmp_eq_u32 %[wm], 0\n s_cbranch_scc1 .Lg1f%=\n ds_read_b32 v53, v59\n ds_read_b128 v[80:83], v58\n ds_read_b64 v[172:173], v58 offset:1024\n " \
  "ds_read_b64 v[178:179], v58 offset:1032\n s_branch .Lg1g%=\n .Lg1f%=:\n buffer_load_dwordx4 v[80:83], v55, %[rs], 0 offen sc1\n " \
  "buffer_load_dwordx2 v[172:173], v55, %[rs], 0 offen offset:1024 sc1\n buffer_load_dwordx2 v[178:179], v55, %[rs], 0 offen offset:1032 sc1\n " \
  ".Lg1g%=:\n v_exp_f32 v189, v189\n v_mfma_f32_16x16x32_bf16 v[220:223], v[132:135], v[212:215], 0\n " \
  "v_mfma_f32_16x16x32_bf16 v[224:227], v[244:247], v[212:215], 0\n v_mfma_f32_16x16x32_bf16 v[228:231], v[250:253], v[212:215], 0\n " \
  "v_exp_f32 v190, v190\n v_exp_f32 v191, v191\n v_exp_f32 v196, v196\n v_exp_f32 v197, v197\n v_exp_f32 v198, v198\n v_exp_f32 v199, v199\n " \
  "v_rcp_f32 v120, v120\n v_cvt_pk_bf16_f32 v188, v188, v189\n v_pk_mul_f32 v[232:233], v[232:233], v[120:121] op_sel_hi:[1,0]\n " \
  "v_pk_mul_f32 v[234:235], v[234:235], v[120:121] op_sel_hi:[1,0]\n v_pk_mul_f32 v[240:241], v[240:241], v[120:121] op_sel_hi:[1,0]\n " \
  "v_pk_mul_f32 v[242:243], v[242:243], v[120:121] op_sel_hi:[1,0]\n v_cvt_pk_bf16_f32 v189, v190, v191\n v_cvt_pk_bf16_f32 v190, v196, v197\n " \
  "v_cvt_pk_bf16_f32 v191, v198, v199\n v_rcp_f32 v220, v220\n v_cvt_pk_bf16_f32 v232, v232, v233\n v_cvt_pk_bf16_f32 v233, v234, v235\n " \
  "v_mfma_f32_16x16x32_bf16 v[196:199], v[132:135], v[188:191], 0\n v_mfma_f32_16x16x32_bf16 v[200:203], v[174:177], v[188:191], 0\n " \
  "v_mfma_f32_16x16x32_bf16 v[204:207], v[180:183], v[188:191], 0\n v_cvt_pk_bf16_f32 v234, v240, v241\n v_cvt_pk_bf16_f32 v235, v242, v243\n " \
  "buffer_store_dwordx4 v[232:235], v51, %[rs], 0 offen offset:3072 sc1\n ds_write_b128 v50, v[232:235] offset:3072\n " \
  "v_pk_mul_f32 v[224:225], v[224:225], v[220:221] op_sel_hi:[1,0]\n v_pk_mul_f32 v[226:227], v[226:227], v[220:221] op_sel_hi:[1,0]\n " \
  "v_pk_mul_f32 v[228:229], v[228:229], v[220:221] op_sel_hi:[1,0]\n v_pk_mul_f32 v[230:231], v[230:231], v[220:221] op_sel_hi:[1,0]\n " \
  "v_cvt_pk_bf16_f32 v224, v224, v225\n v_cvt_pk_bf16_f32 v225, v226, v227\n v_cvt_pk_bf16_f32 v226, v228, v229\n v_cvt_pk_bf16_f32 v227, v230, v231\n " \
  "v_rcp_f32 v196, v196\n buffer_store_dwordx4 v[224:227], v51, %[rs], 0 offen offset:2048 sc1\n ds_write_b128 v50, v[224:227] offset:2048\n " \
  "v_pk_mul_f32 v[200:201], v[200:201], v[196:197] op_sel_hi:[1,0]\n v_pk_mul_f32 v[202:203], v[202:203], v[196:197] op_sel_hi:[1,0]\n " \
  "v_pk_mul_f32 v[204:205], v[204:205], v[196:197] op_sel_hi:[1,0]\n v_pk_mul_f32 v[206:207], v[206:207], v[196:197] op_sel_hi:[1,0]\n " \
  "v_cvt_pk_bf16_f32 v200, v200, v201\n v_cvt_pk_bf16_f32 v201, v202, v203\n v_cvt_pk_bf16_f32 v202, v204, v205\n v_cvt_pk_bf16_f32 v203, v206, v207\n " \
  "buffer_store_dwordx4 v[200:203], v51, %[rs], 0 offen offset:1024 sc1\n ds_write_b128 v50, v[200:203] offset:1024\n s_mov_b32 vcc_hi, 0x40000\n " \
  "s_cmp_eq_u32 %[wm], 0\n s_cbranch_scc1 .Lg1h%=\n s_waitcnt lgkmcnt(3)\n .Lg1i%=:\n v_readfirstlane_b32 vcc_lo, v53\n s_nop 1\n " \
  "s_cmp_lg_u32 vcc_lo, 0\n s_cbranch_scc1 .Lg1k%=\n ds_read_b32 v53, v59\n ds_read_b128 v[80:83], v58\n ds_read_b64 v[172:173], v58 offset:1024\n " \
  "ds_read_b64 v[178:179], v58 offset:1032\n s_waitcnt lgkmcnt(0)\n s_sub_u32 vcc_hi, vcc_hi, 1\n s_cbranch_scc0 .Lg1i%=\n s_branch .Lg1k%=\n .Lg1h%=:\n " \
  "s_mov_b32 m0, 0x40000\n s_waitcnt vmcnt(3)\n .Lg1j%=:\n v_max3_u32 v104, v80, v81, v82\n v_max3_u32 v105, v83, v172, v173\n " \
  "v_max3_u32 v104, v104, v178, v179\n v_max_u32 v104, v104, v105\n v_cmp_eq_u32 vcc, -1, v104\n s_cbranch_vccz .Lg1k%=\n " \
  "buffer_load_dwordx4 v[80:83], v55, %[rs], 0 offen sc1\n buffer_load_dwordx2 v[172:173], v55, %[rs], 0 offen offset:1024 sc1\n " \
  "buffer_load_dwordx2 v[178:179], v55, %[rs], 0 offen offset:1032 sc1\n s_waitcnt vmcnt(0)\n s_sub_u32 m0, m0, 1\n s_cbranch_scc0 .Lg1j%=\n .Lg1k%=:\n " \
  "v_mfma_f32_16x16x32_bf16 v[164:167], v[80:83], v[160:163], v[44:47]\n s_nop 7\n v_max3_f32 v104, v164, v165, v166\n " \
  "v_max3_f32 v105, v167, v124, v125\n v_max3_f32 v104, v104, v126, v127\n v_max_f32 v104, v104, v105\n v_mov_b32 v105, v104\n s_nop 1\n " \
  "v_permlane16_swap_b32 v104, v105\n v_max_f32 v104, v104, v105\n v_mov_b32 v105, v104\n s_nop 1\n v_permlane32_swap_b32 v104, v105\n " \
  "v_max_f32 v104, v104, v105\n v_mul_f32_e64 v147, v146, -v104\n " \
  "v_pk_fma_f32 v[164:165], v[164:165], v[146:147], v[146:147] op_sel:[0,0,1] op_sel_hi:[1,0,1]\n " \
  "v_pk_fma_f32 v[166:167], v[166:167], v[146:147], v[146:147] op_sel:[0,0,1] op_sel_hi:[1,0,1]\n " \
  "v_pk_fma_f32 v[124:125], v[124:125], v[146:147], v[146:147] op_sel:[0,0,1] op_sel_hi:[1,0,1]\n " \
  "v_pk_fma_f32 v[126:127], v[126:127], v[146:147], v[146:147] op_sel:[0,0,1] op_sel_hi:[1,0,1]\n v_exp_f32 v164, v164\n v_exp_f32 v165, v165\n " \
  "v_exp_f32 v166, v166\n v_exp_f32 v167, v167\n v_exp_f32 v124, v124\n v_exp_f32 v125, v125\n v_exp_f32 v126, v126\n v_exp_f32 v127, v127\n " \
  "v_cvt_pk_bf16_f32 v164, v164, v165\n v_cvt_pk_bf16_f32 v165, v166, v167\n v_cvt_pk_bf16_f32 v166, v124, v125\n v_cvt_pk_bf16_f32 v167, v126, v127\n " \
  "s_nop 1\n v_mfma_f32_16x16x32_bf16 v[124:127], v[132:135], v[164:167], 0\n v_mfma_f32_16x16x32_bf16 v[168:171], v[172:175], v[164:167], 0\n " \
  "v_mfma_f32_16x16x32_bf16 v[84:87], v[178:181], v[164:167], 0\n s_nop 5\n v_rcp_f32 v124, v124\n s_nop 0\n " \
  "v_pk_mul_f32 v[168:169], v[168:169], v[124:125] op_sel_hi:[1,0]\n v_pk_mul_f32 v[170:171], v[170:171], v[124:125] op_sel_hi:[1,0]\n " \
  "v_pk_mul_f32 v[84:85], v[84:85], v[124:125] op_sel_hi:[1,0]\n v_pk_mul_f32 v[86:87], v[86:87], v[124:125] op_sel_hi:[1,0]\n " \
  "v_cvt_pk_bf16_f32 v168, v168, v169\n v_cvt_pk_bf16_f32 v169, v170, v171\n v_cvt_pk_bf16_f32 v170, v84, v85\n v_cvt_pk_bf16_f32 v171, v86, v87\n " \
  "buffer_store_dwordx4 v[168:171], v51, %[rs], 0 offen offset:0 sc1\n ds_write_b128 v50, v[168:171] offset:0\n "
#define G1_HEDGE \
  "G1Q 2, 2, 0, 0, 0\n ds_read2_b64 v[140:143], v49 offset1:16\n G1Q 2, 2, 0, 1, 0\n ds_read_b128 v[96:99], v48 offset:256\n G1Q 2, 2, 0, 2, 0\n " \
  "ds_read_b128 v[100:103], v48 offset:320\n G1Q 2, 2, 0, 3, 0\n ds_read2_b64 v[136:139], v49 offset0:32 offset1:48\n G1Q 2, 3, 1, 0, 0\n " \
  "ds_read_b64_tr_b16 v[104:105], v53\n G1Q 2, 3, 1, 1, 0\n ds_read_b64_tr_b16 v[106:107], v53 offset:1024\n G1Q 2, 3, 1, 2, 0\n " \
  "ds_read_b64_tr_b16 v[108:109], v53 offset:32\n G1Q 2, 3, 1, 3, 0\n ds_read_b64_tr_b16 v[110:111], v53 offset:1056\n G1Q 2, 0, 2, 0, 0\n " \
  "ds_read2_b32 v[144:145], v52 offset1:16\n G1Q 2, 0, 2, 1, 0\n ds_read_b128 v[148:151], v48\n G1Q 2, 0, 2, 2, 0\n " \
  "ds_read_b128 v[88:91], v48 offset:64\n G1Q 2, 0, 2, 3, 0\n ds_read_b128 v[128:131], v54\n G1Q 2, 1, 3, 0, 0\n ds_read_b128 v[92:95], v54 offset:512\n " \
  "G1Q 2, 1, 3, 1, 0\n ds_read_b128 v[132:135], v54 offset:64\n G1Q 2, 1, 3, 2, 0\n G1Q 2, 1, 3, 3, 0\n ds_read_b128 v[112:115], v54 offset:576\n " \
  "ds_read_b128 v[116:119], v54 offset:128\n ds_read_b128 v[152:155], v54 offset:640\n ds_read_b128 v[156:159], v54 offset:192\n " \
  "ds_read_b128 v[120:123], v54 offset:704\n s_waitcnt lgkmcnt(11)\n v_pk_fma_f32 v[168:169], v[96:97], v[140:141], v[168:169] op_sel_hi:[1,0,1]\n " \
  "v_pk_fma_f32 v[170:171], v[98:99], v[140:141], v[170:171] op_sel_hi:[1,0,1]\n " \
  "v_pk_fma_f32 v[172:173], v[100:101], v[140:141], v[172:173] op_sel_hi:[1,0,1]\n " \
  "v_pk_fma_f32 v[174:175], v[102:103], v[140:141], v[174:175] op_sel_hi:[1,0,1]\n " \
  "v_pk_fma_f32 v[192:193], v[96:97], v[142:143], v[192:193] op_sel_hi:[1,0,1]\n " \
  "v_pk_fma_f32 v[194:195], v[98:99], v[142:143], v[194:195] op_sel_hi:[1,0,1]\n " \
  "v_pk_fma_f32 v[196:197], v[100:101], v[142:143], v[196:197] op_sel_hi:[1,0,1]\n " \
  "v_pk_fma_f32 v[198:199], v[102:103], v[142:143], v[198:199] op_sel_hi:[1,0,1]\n v_cvt_pk_bf16_f32 v168, v168, v169\n " \
  "v_cvt_pk_bf16_f32 v169, v170, v171\n v_cvt_pk_bf16_f32 v170, v172, v173\n v_cvt_pk_bf16_f32 v171, v174, v175\n v_cvt_pk_bf16_f32 v192, v192, v193\n " \
  "v_cvt_pk_bf16_f32 v193, v194, v195\n v_cvt_pk_bf16_f32 v194, v196, v197\n v_cvt_pk_bf16_f32 v195, v198, v199\n " \
  "v_pk_fma_f32 v[216:217], v[96:97], v[136:137], v[216:217] op_sel_hi:[1,0,1]\n " \
  "v_pk_fma_f32 v[218:219], v[98:99], v[136:137], v[218:219] op_sel_hi:[1,0,1]\n " \
  "v_pk_fma_f32 v[220:221], v[100:101], v[136:137], v[220:221] op_sel_hi:[1,0,1]\n " \
  "v_pk_fma_f32 v[222:223], v[102:103], v[136:137], v[222:223] op_sel_hi:[1,0,1]\n " \
  "v_pk_fma_f32 v[240:241], v[96:97], v[138:139], v[240:241] op_sel_hi:[1,0,1]\n " \
  "v_pk_fma_f32 v[242:243], v[98:99], v[138:139], v[242:243] op_sel_hi:[1,0,1]\n " \
  "v_pk_fma_f32 v[244:245], v[100:101], v[138:139], v[244:245] op_sel_hi:[1,0,1]\n " \
  "v_pk_fma_f32 v[246:247], v[102:103], v[138:139], v[246:247] op_sel_hi:[1,0,1]\n v_mfma_f32_16x16x32_bf16 v[172:175], v[104:107], v[168:171], 0\n " \
  "v_mfma_f32_16x16x32_bf16 v[80:83], v[108:111], v[168:171], 0\n v_mfma_f32_16x16x32_bf16 v[196:199], v[104:107], v[192:195], 0\n " \
  "v_mfma_f32_16x16x32_bf16 v[84:87], v[108:111], v[192:195], 0\n v_cvt_pk_bf16_f32 v216, v216, v217\n v_cvt_pk_bf16_f32 v217, v218, v219\n " \
  "v_cvt_pk_bf16_f32 v218, v220, v221\n v_cvt_pk_bf16_f32 v219, v222, v223\n v_cvt_pk_bf16_f32 v240, v240, v241\n v_cvt_pk_bf16_f32 v241, v242, v243\n " \
  "v_cvt_pk_bf16_f32 v242, v244, v245\n v_cvt_pk_bf16_f32 v243, v246, v247\n v_mul_f32 v40, 0x3fb8aa3b, v141\n v_mul_f32 v42, 0x3fb8aa3b, v143\n " \
  "v_mul_f32 v44, 0x3fb8aa3b, v137\n v_mul_f32 v46, 0x3fb8aa3b, v139\n v_mfma_f32_16x16x32_bf16 v[220:223], v[104:107], v[216:219], 0\n " \
  "v_mfma_f32_16x16x32_bf16 v[124:127], v[108:111], v[216:219], 0\n v_mfma_f32_16x16x32_bf16 v[244:247], v[104:107], v[240:243], 0\n " \
  "v_mfma_f32_16x16x32_bf16 v[36:39], v[108:111], v[240:243], 0\n v_max3_f32 v168, v172, v173, v174\n v_max3_f32 v169, -v172, -v173, -v174\n " \
  "v_max3_f32 v168, v168, v175, v80\n v_max3_f32 v169, v169, -v175, -v80\n v_max3_f32 v168, v168, v81, v82\n v_max3_f32 v169, v169, -v81, -v82\n " \
  "v_max_f32_e64 v168, v168, v83\n v_max_f32_e64 v169, v169, -v83\n v_max3_f32 v170, v196, v197, v198\n v_max3_f32 v171, -v196, -v197, -v198\n " \
  "v_max3_f32 v170, v170, v199, v84\n v_max3_f32 v171, v171, -v199, -v84\n v_max3_f32 v170, v170, v85, v86\n v_max3_f32 v171, v171, -v85, -v86\n " \
  "v_max_f32_e64 v170, v170, v87\n v_max_f32_e64 v171, v171, -v87\n s_waitcnt lgkmcnt(0)\n " \
  "v_pk_fma_f32 v[176:177], v[128:129], v[144:145], v[176:177] op_sel_hi:[1,0,1]\n " \
  "v_pk_fma_f32 v[178:179], v[130:131], v[144:145], v[178:179] op_sel_hi:[1,0,1]\n " \
  "v_pk_fma_f32 v[180:181], v[128:129], v[144:145], v[180:181] op_sel:[0,1,0]\n " \
  "v_pk_fma_f32 v[182:183], v[130:131], v[144:145], v[182:183] op_sel:[0,1,0]\n v_pk_mul_f32 v[176:177], v[176:177], v[92:93]\n " \
  "v_pk_mul_f32 v[178:179], v[178:179], v[94:95]\n v_pk_mul_f32 v[180:181], v[180:181], v[92:93]\n v_pk_mul_f32 v[182:183], v[182:183], v[94:95]\n " \
  "v_pk_fma_f32 v[200:201], v[132:133], v[144:145], v[200:201] op_sel_hi:[1,0,1]\n " \
  "v_pk_fma_f32 v[202:203], v[134:135], v[144:145], v[202:203] op_sel_hi:[1,0,1]\n " \
  "v_pk_fma_f32 v[204:205], v[132:133], v[144:145], v[204:205] op_sel:[0,1,0]\n " \
  "v_pk_fma_f32 v[206:207], v[134:135], v[144:145], v[206:207] op_sel:[0,1,0]\n v_pk_mul_f32 v[200:201], v[200:201], v[112:113]\n " \
  "v_pk_mul_f32 v[202:203], v[202:203], v[114:115]\n v_pk_mul_f32 v[204:205], v[204:205], v[112:113]\n v_pk_mul_f32 v[206:207], v[206:207], v[114:115]\n " \
  "v_max3_f32 v192, v220, v221, v222\n v_max3_f32 v193, -v220, -v221, -v222\n v_max3_f32 v192, v192, v223, v124\n v_max3_f32 v193, v193, -v223, -v124\n " \
  "v_max3_f32 v192, v192, v125, v126\n v_max3_f32 v193, v193, -v125, -v126\n v_max_f32_e64 v192, v192, v127\n v_max_f32_e64 v193, v193, -v127\n " \
  "v_max3_f32 v194, v244, v245, v246\n v_max3_f32 v195, -v244, -v245, -v246\n v_max3_f32 v194, v194, v247, v36\n v_max3_f32 v195, v195, -v247, -v36\n " \
  "v_max3_f32 v194, v194, v37, v38\n v_max3_f32 v195, v195, -v37, -v38\n v_max_f32_e64 v194, v194, v39\n v_max_f32_e64 v195, v195, -v39\n " \
  "v_permlane16_swap_b32 v168, v169\n v_permlane16_swap_b32 v170, v171\n v_permlane16_swap_b32 v192, v193\n v_permlane16_swap_b32 v194, v195\n " \
  "v_pk_fma_f32 v[224:225], v[116:117], v[144:145], v[224:225] op_sel_hi:[1,0,1]\n " \
  "v_pk_fma_f32 v[226:227], v[118:119], v[144:145], v[226:227] op_sel_hi:[1,0,1]\n v_max_f32 v168, v168, v169\n v_max_f32 v170, v170, v171\n " \
  "v_max_f32 v192, v192, v193\n v_max_f32 v194, v194, v195\n v_pk_fma_f32 v[228:229], v[116:117], v[144:145], v[228:229] op_sel:[0,1,0]\n " \
  "v_pk_fma_f32 v[230:231], v[118:119], v[144:145], v[230:231] op_sel:[0,1,0]\n v_permlane32_swap_b32 v168, v170\n v_permlane32_swap_b32 v192, v194\n " \
  "v_pk_mul_f32 v[224:225], v[224:225], v[152:153]\n v_pk_mul_f32 v[226:227], v[226:227], v[154:155]\n v_max_f32 v168, v168, v170\n " \
  "v_max_f32 v192, v192, v194\n v_pk_mul_f32 v[228:229], v[228:229], v[152:153]\n v_pk_mul_f32 v[230:231], v[230:231], v[154:155]\n " \
  "v_mov_b32 v170, v168\n v_mov_b32 v194, v192\n v_pk_fma_f32 v[248:249], v[156:157], v[144:145], v[248:249] op_sel_hi:[1,0,1]\n " \
  "v_pk_fma_f32 v[250:251], v[158:159], v[144:145], v[250:251] op_sel_hi:[1,0,1]\n v_permlane32_swap_b32 v168, v170\n v_permlane32_swap_b32 v192, v194\n " \
  "v_pk_fma_f32 v[252:253], v[156:157], v[144:145], v[252:253] op_sel:[0,1,0]\n " \
  "v_pk_fma_f32 v[254:255], v[158:159], v[144:145], v[254:255] op_sel:[0,1,0]\n v_mov_b32 v169, v168\n v_mov_b32 v171, v170\n v_mov_b32 v193, v192\n " \
  "v_mov_b32 v195, v194\n v_pk_mul_f32 v[248:249], v[248:249], v[120:121]\n v_pk_mul_f32 v[250:251], v[250:251], v[122:123]\n " \
  "v_permlane16_swap_b32 v168, v169\n v_permlane16_swap_b32 v170, v171\n v_permlane16_swap_b32 v192, v193\n v_permlane16_swap_b32 v194, v195\n " \
  "v_pk_mul_f32 v[252:253], v[252:253], v[120:121]\n v_pk_mul_f32 v[254:255], v[254:255], v[122:123]\n v_cvt_pk_bf16_f32 v176, v176, v177\n " \
  "v_cvt_pk_bf16_f32 v177, v178, v179\n v_cvt_pk_bf16_f32 v178, v200, v201\n v_cvt_pk_bf16_f32 v179, v202, v203\n v_cvt_pk_bf16_f32 v180, v180, v181\n " \
  "v_cvt_pk_bf16_f32 v181, v182, v183\n v_cvt_pk_bf16_f32 v182, v204, v205\n v_cvt_pk_bf16_f32 v183, v206, v207\n v_cvt_pk_bf16_f32 v224, v224, v225\n " \
  "v_cvt_pk_bf16_f32 v225, v226, v227\n v_cvt_pk_bf16_f32 v226, v248, v249\n v_cvt_pk_bf16_f32 v227, v250, v251\n v_cvt_pk_bf16_f32 v228, v228, v229\n " \
  "v_cvt_pk_bf16_f32 v229, v230, v231\n v_cvt_pk_bf16_f32 v230, v252, v253\n v_cvt_pk_bf16_f32 v231, v254, v255\n v_mul_f32_e64 v41, v40, -v168\n " \
  "v_mul_f32_e64 v146, v40, -v169\n v_mul_f32_e64 v43, v42, -v170\n v_mul_f32_e64 v147, v42, -v171\n v_mul_f32_e64 v45, v44, -v192\n " \
  "v_mul_f32_e64 v144, v44, -v193\n v_mul_f32_e64 v47, v46, -v194\n v_mul_f32_e64 v145, v46, -v195\n " \
  "v_pk_fma_f32 v[200:201], v[172:173], v[40:41], v[146:147] op_sel_hi:[1,0,0] neg_lo:[1,0,0] neg_hi:[1,0,0]\n " \
  "v_pk_fma_f32 v[202:203], v[174:175], v[40:41], v[146:147] op_sel_hi:[1,0,0] neg_lo:[1,0,0] neg_hi:[1,0,0]\n " \
  "v_pk_fma_f32 v[204:205], v[80:81], v[40:41], v[146:147] op_sel_hi:[1,0,0] neg_lo:[1,0,0] neg_hi:[1,0,0]\n " \
  "v_pk_fma_f32 v[206:207], v[82:83], v[40:41], v[146:147] op_sel_hi:[1,0,0] neg_lo:[1,0,0] neg_hi:[1,0,0]\n " \
  "v_pk_fma_f32 v[172:173], v[172:173], v[40:41], v[40:41] op_sel:[0,0,1] op_sel_hi:[1,0,1]\n " \
  "v_pk_fma_f32 v[174:175], v[174:175], v[40:41], v[40:41] op_sel:[0,0,1] op_sel_hi:[1,0,1]\n " \
  "v_pk_fma_f32 v[80:81], v[80:81], v[40:41], v[40:41] op_sel:[0,0,1] op_sel_hi:[1,0,1]\n " \
  "v_pk_fma_f32 v[82:83], v[82:83], v[40:41], v[40:41] op_sel:[0,0,1] op_sel_hi:[1,0,1]\n ds_write_b128 v55, v[176:179] offset:0\n " \
  "v_exp_f32 v200, v200\n v_exp_f32 v201, v201\n v_exp_f32 v202, v202\n v_exp_f32 v203, v203\n v_exp_f32 v204, v204\n v_exp_f32 v205, v205\n " \
  "v_exp_f32 v206, v206\n v_exp_f32 v207, v207\n v_exp_f32 v172, v172\n v_exp_f32 v173, v173\n v_exp_f32 v174, v174\n v_exp_f32 v175, v175\n " \
  "v_exp_f32 v80, v80\n v_exp_f32 v81, v81\n v_exp_f32 v82, v82\n v_exp_f32 v83, v83\n " \
  "v_pk_fma_f32 v[248:249], v[196:197], v[42:43], v[146:147] op_sel:[0,0,1] op_sel_hi:[1,0,1] neg_lo:[1,0,0] neg_hi:[1,0,0]\n " \
  "v_pk_fma_f32 v[250:251], v[198:199], v[42:43], v[146:147] op_sel:[0,0,1] op_sel_hi:[1,0,1] neg_lo:[1,0,0] neg_hi:[1,0,0]\n " \
  "v_pk_fma_f32 v[252:253], v[84:85], v[42:43], v[146:147] op_sel:[0,0,1] op_sel_hi:[1,0,1] neg_lo:[1,0,0] neg_hi:[1,0,0]\n " \
  "v_pk_fma_f32 v[254:255], v[86:87], v[42:43], v[146:147] op_sel:[0,0,1] op_sel_hi:[1,0,1] neg_lo:[1,0,0] neg_hi:[1,0,0]\n " \
  "v_pk_fma_f32 v[196:197], v[196:197], v[42:43], v[42:43] op_sel:[0,0,1] op_sel_hi:[1,0,1]\n " \
  "v_pk_fma_f32 v[198:199], v[198:199], v[42:43], v[42:43] op_sel:[0,0,1] op_sel_hi:[1,0,1]\n " \
  "v_pk_fma_f32 v[84:85], v[84:85], v[42:43], v[42:43] op_sel:[0,0,1] op_sel_hi:[1,0,1]\n " \
  "v_pk_fma_f32 v[86:87], v[86:87], v[42:43], v[42:43] op_sel:[0,0,1] op_sel_hi:[1,0,1]\n ds_write_b128 v55, v[180:183] offset:1024\n " \
  "v_exp_f32 v248, v248\n v_exp_f32 v249, v249\n v_exp_f32 v250, v250\n v_exp_f32 v251, v251\n v_exp_f32 v252, v252\n v_exp_f32 v253, v253\n " \
  "v_exp_f32 v254, v254\n v_exp_f32 v255, v255\n v_exp_f32 v196, v196\n v_exp_f32 v197, v197\n v_exp_f32 v198, v198\n v_exp_f32 v199, v199\n " \
  "v_exp_f32 v84, v84\n v_exp_f32 v85, v85\n v_exp_f32 v86, v86\n v_exp_f32 v87, v87\n " \
  "v_pk_fma_f32 v[128:129], v[220:221], v[44:45], v[144:145] op_sel_hi:[1,0,0] neg_lo:[1,0,0] neg_hi:[1,0,0]\n " \
  "v_pk_fma_f32 v[130:131], v[222:223], v[44:45], v[144:145] op_sel_hi:[1,0,0] neg_lo:[1,0,0] neg_hi:[1,0,0]\n " \
  "v_pk_fma_f32 v[132:133], v[124:125], v[44:45], v[144:145] op_sel_hi:[1,0,0] neg_lo:[1,0,0] neg_hi:[1,0,0]\n " \
  "v_pk_fma_f32 v[134:135], v[126:127], v[44:45], v[144:145] op_sel_hi:[1,0,0] neg_lo:[1,0,0] neg_hi:[1,0,0]\n " \
  "v_pk_fma_f32 v[220:221], v[220:221], v[44:45], v[44:45] op_sel:[0,0,1] op_sel_hi:[1,0,1]\n " \
  "v_pk_fma_f32 v[222:223], v[222:223], v[44:45], v[44:45] op_sel:[0,0,1] op_sel_hi:[1,0,1]\n " \
  "v_pk_fma_f32 v[124:125], v[124:125], v[44:45], v[44:45] op_sel:[0,0,1] op_sel_hi:[1,0,1]\n " \
  "v_pk_fma_f32 v[126:127], v[126:127], v[44:45], v[44:45] op_sel:[0,0,1] op_sel_hi:[1,0,1]\n ds_write_b128 v55, v[224:227] offset:8192\n " \
  "v_pk_add_f32 v[216:217], v[172:173], v[174:175]\n v_pk_add_f32 v[218:219], v[80:81], v[82:83]\n v_pk_add_f32 v[216:217], v[216:217], v[218:219]\n " \
  "v_add_f32 v168, v216, v217\n v_pk_add_f32 v[216:217], v[200:201], v[202:203]\n v_pk_add_f32 v[218:219], v[204:205], v[206:207]\n " \
  "v_pk_add_f32 v[216:217], v[216:217], v[218:219]\n v_add_f32 v169, v216, v217\n v_pk_add_f32 v[216:217], v[196:197], v[198:199]\n " \
  "v_pk_add_f32 v[218:219], v[84:85], v[86:87]\n v_pk_add_f32 v[216:217], v[216:217], v[218:219]\n v_add_f32 v170, v216, v217\n " \
  "v_pk_add_f32 v[216:217], v[248:249], v[250:251]\n v_pk_add_f32 v[218:219], v[252:253], v[254:255]\n v_pk_add_f32 v[216:217], v[216:217], v[218:219]\n " \
  "v_add_f32 v171, v216, v217\n v_exp_f32 v128, v128\n v_exp_f32 v129, v129\n v_permlane16_swap_b32 v168, v169\n v_permlane16_swap_b32 v170, v171\n " \
  "v_exp_f32 v130, v130\n v_exp_f32 v131, v131\n v_add_f32 v168, v168, v169\n v_add_f32 v170, v170, v171\n v_exp_f32 v132, v132\n v_exp_f32 v133, v133\n " \
  "v_permlane32_swap_b32 v168, v170\n v_exp_f32 v134, v134\n v_exp_f32 v135, v135\n v_add_f32 v168, v168, v170\n v_exp_f32 v220, v220\n " \
  "v_exp_f32 v221, v221\n v_rcp_f32 v168, v168\n v_exp_f32 v222, v222\n v_exp_f32 v223, v223\n v_mov_b32 v170, v168\n v_exp_f32 v124, v124\n " \
  "v_exp_f32 v125, v125\n v_permlane32_swap_b32 v168, v170\n v_exp_f32 v126, v126\n v_exp_f32 v127, v127\n v_mov_b32 v169, v168\n v_mov_b32 v171, v170\n " \
  "v_pk_fma_f32 v[112:113], v[244:245], v[46:47], v[144:145] op_sel:[0,0,1] op_sel_hi:[1,0,1] neg_lo:[1,0,0] neg_hi:[1,0,0]\n " \
  "v_pk_fma_f32 v[114:115], v[246:247], v[46:47], v[144:145] op_sel:[0,0,1] op_sel_hi:[1,0,1] neg_lo:[1,0,0] neg_hi:[1,0,0]\n " \
  "v_permlane16_swap_b32 v168, v169\n v_permlane16_swap_b32 v170, v171\n " \
  "v_pk_fma_f32 v[116:117], v[36:37], v[46:47], v[144:145] op_sel:[0,0,1] op_sel_hi:[1,0,1] neg_lo:[1,0,0] neg_hi:[1,0,0]\n " \
  "v_pk_fma_f32 v[118:119], v[38:39], v[46:47], v[144:145] op_sel:[0,0,1] op_sel_hi:[1,0,1] neg_lo:[1,0,0] neg_hi:[1,0,0]\n " \
  "v_pk_fma_f32 v[244:245], v[244:245], v[46:47], v[46:47] op_sel:[0,0,1] op_sel_hi:[1,0,1]\n " \
  "v_pk_fma_f32 v[246:247], v[246:247], v[46:47], v[46:47] op_sel:[0,0,1] op_sel_hi:[1,0,1]\n " \
  "v_pk_fma_f32 v[36:37], v[36:37], v[46:47], v[46:47] op_sel:[0,0,1] op_sel_hi:[1,0,1]\n " \
  "v_pk_fma_f32 v[38:39], v[38:39], v[46:47], v[46:47] op_sel:[0,0,1] op_sel_hi:[1,0,1]\n ds_write_b128 v55, v[228:231] offset:9216\n " \
  "v_exp_f32 v112, v112\n v_pk_mul_f32 v[172:173], v[172:173], v[168:169] op_sel_hi:[1,0]\n v_exp_f32 v113, v113\n " \
  "v_pk_mul_f32 v[174:175], v[174:175], v[168:169] op_sel_hi:[1,0]\n v_exp_f32 v114, v114\n v_cvt_pk_bf16_f32 v172, v172, v173\n v_exp_f32 v115, v115\n " \
  "v_cvt_pk_bf16_f32 v173, v174, v175\n v_exp_f32 v116, v116\n ds_write_b64 v58, v[172:173] offset:0\n v_exp_f32 v117, v117\n " \
  "v_pk_mul_f32 v[80:81], v[80:81], v[168:169] op_sel_hi:[1,0]\n v_exp_f32 v118, v118\n v_pk_mul_f32 v[82:83], v[82:83], v[168:169] op_sel_hi:[1,0]\n " \
  "v_exp_f32 v119, v119\n v_cvt_pk_bf16_f32 v80, v80, v81\n v_exp_f32 v244, v244\n v_cvt_pk_bf16_f32 v81, v82, v83\n v_exp_f32 v245, v245\n " \
  "ds_write_b64 v58, v[80:81] offset:32\n v_exp_f32 v246, v246\n v_pk_mul_f32 v[200:201], v[200:201], v[168:169] op_sel:[0,1]\n v_exp_f32 v247, v247\n " \
  "v_pk_mul_f32 v[202:203], v[202:203], v[168:169] op_sel:[0,1]\n v_exp_f32 v36, v36\n v_cvt_pk_bf16_f32 v200, v200, v201\n v_exp_f32 v37, v37\n " \
  "v_cvt_pk_bf16_f32 v201, v202, v203\n v_exp_f32 v38, v38\n ds_write_b64 v58, v[200:201] offset:64\n v_exp_f32 v39, v39\n " \
  "v_pk_mul_f32 v[204:205], v[204:205], v[168:169] op_sel:[0,1]\n v_pk_mul_f32 v[206:207], v[206:207], v[168:169] op_sel:[0,1]\n " \
  "v_cvt_pk_bf16_f32 v204, v204, v205\n v_cvt_pk_bf16_f32 v205, v206, v207\n ds_write_b64 v58, v[204:205] offset:96\n " \
  "v_pk_add_f32 v[216:217], v[220:221], v[222:223]\n v_pk_add_f32 v[218:219], v[124:125], v[126:127]\n v_pk_add_f32 v[216:217], v[216:217], v[218:219]\n " \
  "v_add_f32 v192, v216, v217\n v_pk_add_f32 v[216:217], v[128:129], v[130:131]\n v_pk_add_f32 v[218:219], v[132:133], v[134:135]\n " \
  "v_pk_add_f32 v[216:217], v[216:217], v[218:219]\n v_add_f32 v193, v216, v217\n v_pk_add_f32 v[216:217], v[244:245], v[246:247]\n " \
  "v_pk_add_f32 v[218:219], v[36:37], v[38:39]\n v_pk_add_f32 v[216:217], v[216:217], v[218:219]\n v_add_f32 v194, v216, v217\n " \
  "v_pk_add_f32 v[216:217], v[112:113], v[114:115]\n v_pk_add_f32 v[218:219], v[116:117], v[118:119]\n v_pk_add_f32 v[216:217], v[216:217], v[218:219]\n " \
  "v_add_f32 v195, v216, v217\n v_pk_mul_f32 v[196:197], v[196:197], v[170:171] op_sel_hi:[1,0]\n " \
  "v_pk_mul_f32 v[198:199], v[198:199], v[170:171] op_sel_hi:[1,0]\n v_permlane16_swap_b32 v192, v193\n v_permlane16_swap_b32 v194, v195\n " \
  "v_cvt_pk_bf16_f32 v196, v196, v197\n v_cvt_pk_bf16_f32 v197, v198, v199\n v_add_f32 v192, v192, v193\n v_add_f32 v194, v194, v195\n " \
  "ds_write_b64 v58, v[196:197] offset:2304\n v_pk_mul_f32 v[84:85], v[84:85], v[170:171] op_sel_hi:[1,0]\n v_permlane32_swap_b32 v192, v194\n " \
  "v_pk_mul_f32 v[86:87], v[86:87], v[170:171] op_sel_hi:[1,0]\n v_cvt_pk_bf16_f32 v84, v84, v85\n v_add_f32 v192, v192, v194\n " \
  "v_cvt_pk_bf16_f32 v85, v86, v87\n ds_write_b64 v58, v[84:85] offset:2336\n v_rcp_f32 v192, v192\n " \
  "v_pk_mul_f32 v[248:249], v[248:249], v[170:171] op_sel:[0,1]\n v_pk_mul_f32 v[250:251], v[250:251], v[170:171] op_sel:[0,1]\n v_mov_b32 v194, v192\n " \
  "v_cvt_pk_bf16_f32 v248, v248, v249\n v_cvt_pk_bf16_f32 v249, v250, v251\n v_permlane32_swap_b32 v192, v194\n " \
  "ds_write_b64 v58, v[248:249] offset:2368\n v_pk_mul_f32 v[252:253], v[252:253], v[170:171] op_sel:[0,1]\n v_mov_b32 v193, v192\n " \
  "v_mov_b32 v195, v194\n v_pk_mul_f32 v[254:255], v[254:255], v[170:171] op_sel:[0,1]\n v_cvt_pk_bf16_f32 v252, v252, v253\n " \
  "v_permlane16_swap_b32 v192, v193\n v_permlane16_swap_b32 v194, v195\n v_cvt_pk_bf16_f32 v253, v254, v255\n ds_write_b64 v58, v[252:253] offset:2400\n " \
  "v_pk_mul_f32 v[220:221], v[220:221], v[192:193] op_sel_hi:[1,0]\n v_pk_mul_f32 v[222:223], v[222:223], v[192:193] op_sel_hi:[1,0]\n " \
  "v_cvt_pk_bf16_f32 v220, v220, v221\n v_cvt_pk_bf16_f32 v221, v222, v223\n ds_write_b64 v58, v[220:221] offset:18432\n " \
  "v_pk_mul_f32 v[124:125], v[124:125], v[192:193] op_sel_hi:[1,0]\n v_pk_mul_f32 v[126:127], v[126:127], v[192:193] op_sel_hi:[1,0]\n " \
  "v_cvt_pk_bf16_f32 v124, v124, v125\n v_cvt_pk_bf16_f32 v125, v126, v127\n ds_write_b64 v58, v[124:125] offset:18464\n " \
  "v_pk_mul_f32 v[128:129], v[128:129], v[192:193] op_sel:[0,1]\n v_pk_mul_f32 v[130:131], v[130:131], v[192:193] op_sel:[0,1]\n " \
  "v_cvt_pk_bf16_f32 v128, v128, v129\n v_cvt_pk_bf16_f32 v129, v130, v131\n ds_write_b64 v58, v[128:129] offset:18496\n " \
  "v_pk_mul_f32 v[132:133], v[132:133], v[192:193] op_sel:[0,1]\n v_pk_mul_f32 v[134:135], v[134:135], v[192:193] op_sel:[0,1]\n " \
  "v_cvt_pk_bf16_f32 v132, v132, v133\n v_cvt_pk_bf16_f32 v133, v134, v135\n ds_write_b64 v58, v[132:133] offset:18528\n " \
  "v_pk_mul_f32 v[244:245], v[244:245], v[194:195] op_sel_hi:[1,0]\n v_pk_mul_f32 v[246:247], v[246:247], v[194:195] op_sel_hi:[1,0]\n " \
  "v_cvt_pk_bf16_f32 v244, v244, v245\n v_cvt_pk_bf16_f32 v245, v246, v247\n ds_write_b64 v58, v[244:245] offset:20736\n " \
  "v_pk_mul_f32 v[36:37], v[36:37], v[194:195] op_sel_hi:[1,0]\n v_pk_mul_f32 v[38:39], v[38:39], v[194:195] op_sel_hi:[1,0]\n " \
  "v_cvt_pk_bf16_f32 v36, v36, v37\n v_cvt_pk_bf16_f32 v37, v38, v39\n ds_write_b64 v58, v[36:37] offset:20768\n " \
  "v_pk_mul_f32 v[112:113], v[112:113], v[194:195] op_sel:[0,1]\n v_pk_mul_f32 v[114:115], v[114:115], v[194:195] op_sel:[0,1]\n " \
  "v_cvt_pk_bf16_f32 v112, v112, v113\n v_cvt_pk_bf16_f32 v113, v114, v115\n ds_write_b64 v58, v[112:113] offset:20800\n " \
  "v_pk_mul_f32 v[116:117], v[116:117], v[194:195] op_sel:[0,1]\n v_pk_mul_f32 v[118:119], v[118:119], v[194:195] op_sel:[0,1]\n " \
  "v_cvt_pk_bf16_f32 v116, v116, v117\n v_cvt_pk_bf16_f32 v117, v118, v119\n ds_write_b64 v58, v[116:117] offset:20832\n "
#define G1_OUTPUT \
  "s_mov_b32 m0, 0x40000\n s_branch .Lg1p%=\n .Lg1q%=:\n s_sleep 1\n s_sub_u32 m0, m0, 1\n s_cbranch_scc1 .Lg1r%=\n .Lg1p%=:\n " \
  "buffer_load_dwordx4 v[216:219], %[fz], %[rs], 0 offen sc1\n buffer_load_dwordx4 v[220:223], %[fz], %[rs], 0 offen offset:64 sc1\n " \
  "buffer_load_dwordx4 v[200:203], %[fo], %[rs], 0 offen offset:0 sc1\n buffer_load_dwordx4 v[204:207], %[fo], %[rs], 0 offen offset:2048 sc1\n " \
  "buffer_load_dwordx4 v[208:211], %[fo], %[rs], 0 offen offset:1024 sc1\n buffer_load_dwordx4 v[212:215], %[fo], %[rs], 0 offen offset:3072 sc1\n " \
  "s_waitcnt vmcnt(5)\n v_max_u32 v152, v216, v217\n v_max3_u32 v152, v218, v219, v152\n s_waitcnt vmcnt(4)\n v_max3_u32 v152, v220, v221, v152\n " \
  "v_max3_u32 v152, v222, v223, v152\n s_waitcnt vmcnt(3)\n v_max3_u32 v152, v200, v201, v152\n v_max3_u32 v152, v202, v203, v152\n s_waitcnt vmcnt(2)\n " \
  "v_max3_u32 v152, v204, v205, v152\n v_max3_u32 v152, v206, v207, v152\n s_waitcnt vmcnt(1)\n v_max3_u32 v152, v208, v209, v152\n " \
  "v_max3_u32 v152, v210, v211, v152\n s_waitcnt vmcnt(0)\n v_max3_u32 v152, v212, v213, v152\n v_max3_u32 v152, v214, v215, v152\n " \
  "v_cmp_eq_u32 vcc, -1, v152\n s_cbranch_vccnz .Lg1q%=\n .Lg1r%=:\n v_mfma_f32_16x16x32_bf16 v[160:163], v[216:219], v[224:227], 0\n " \
  "v_mfma_f32_16x16x32_bf16 v[160:163], v[220:223], v[228:231], v[160:163]\n v_mfma_f32_16x16x32_bf16 v[152:155], v[200:203], v[224:227], 0\n " \
  "v_mfma_f32_16x16x32_bf16 v[152:155], v[204:207], v[228:231], v[152:155]\n v_mfma_f32_16x16x32_bf16 v[156:159], v[208:211], v[224:227], 0\n " \
  "v_mfma_f32_16x16x32_bf16 v[156:159], v[212:215], v[228:231], v[156:159]\n v_mfma_f32_16x16x32_bf16 v[172:175], v[216:219], v[232:235], 0\n " \
  "v_mfma_f32_16x16x32_bf16 v[172:175], v[220:223], v[236:239], v[172:175]\n v_mfma_f32_16x16x32_bf16 v[164:167], v[200:203], v[232:235], 0\n " \
  "v_mfma_f32_16x16x32_bf16 v[164:167], v[204:207], v[236:239], v[164:167]\n v_mfma_f32_16x16x32_bf16 v[168:171], v[208:211], v[232:235], 0\n " \
  "v_add_f32 v160, 0x358637bd, v160\n v_rcp_f32 v160, v160\n v_mfma_f32_16x16x32_bf16 v[168:171], v[212:215], v[236:239], v[168:171]\n " \
  "v_mul_f32 v152, v160, v152\n v_mfma_f32_16x16x32_bf16 v[184:187], v[216:219], v[240:243], 0\n v_mul_f32 v153, v160, v153\n " \
  "v_cvt_pk_bf16_f32 v152, v152, v153\n v_mul_f32 v154, v160, v154\n v_mfma_f32_16x16x32_bf16 v[184:187], v[220:223], v[244:247], v[184:187]\n " \
  "v_mul_f32 v155, v160, v155\n v_cvt_pk_bf16_f32 v153, v154, v155\n v_mul_f32 v156, v160, v156\n " \
  "v_mfma_f32_16x16x32_bf16 v[176:179], v[200:203], v[240:243], 0\n v_mul_f32 v157, v160, v157\n v_cvt_pk_bf16_f32 v154, v156, v157\n " \
  "v_mul_f32 v158, v160, v158\n v_mfma_f32_16x16x32_bf16 v[176:179], v[204:207], v[244:247], v[176:179]\n v_mul_f32 v159, v160, v159\n " \
  "v_cvt_pk_bf16_f32 v155, v158, v159\n buffer_store_dwordx4 v[152:155], %[go], %[rs], 0 offen offset:0 sc1\n " \
  "v_mfma_f32_16x16x32_bf16 v[180:183], v[208:211], v[240:243], 0\n ds_write_b128 %[lo], v[152:155] offset:0\n v_add_f32 v172, 0x358637bd, v172\n " \
  "v_mfma_f32_16x16x32_bf16 v[180:183], v[212:215], v[244:247], v[180:183]\n v_rcp_f32 v172, v172\n " \
  "v_mfma_f32_16x16x32_bf16 v[196:199], v[216:219], v[248:251], 0\n v_mul_f32 v164, v172, v164\n v_mul_f32 v165, v172, v165\n " \
  "v_cvt_pk_bf16_f32 v164, v164, v165\n v_mfma_f32_16x16x32_bf16 v[196:199], v[220:223], v[252:255], v[196:199]\n v_mul_f32 v166, v172, v166\n " \
  "v_mul_f32 v167, v172, v167\n v_cvt_pk_bf16_f32 v165, v166, v167\n v_mfma_f32_16x16x32_bf16 v[188:191], v[200:203], v[248:251], 0\n " \
  "v_mul_f32 v168, v172, v168\n v_mul_f32 v169, v172, v169\n v_cvt_pk_bf16_f32 v166, v168, v169\n " \
  "v_mfma_f32_16x16x32_bf16 v[188:191], v[204:207], v[252:255], v[188:191]\n v_mul_f32 v170, v172, v170\n v_mul_f32 v171, v172, v171\n " \
  "v_cvt_pk_bf16_f32 v167, v170, v171\n v_mfma_f32_16x16x32_bf16 v[192:195], v[208:211], v[248:251], 0\n " \
  "buffer_store_dwordx4 v[164:167], %[go], %[rs], 0 offen offset:1024 sc1\n ds_write_b128 %[lo], v[164:167] offset:1024\n " \
  "v_mfma_f32_16x16x32_bf16 v[192:195], v[212:215], v[252:255], v[192:195]\n v_add_f32 v184, 0x358637bd, v184\n v_rcp_f32 v184, v184\n s_nop 0\n " \
  "v_pk_mul_f32 v[176:177], v[176:177], v[184:185] op_sel_hi:[1,0]\n v_pk_mul_f32 v[178:179], v[178:179], v[184:185] op_sel_hi:[1,0]\n " \
  "v_pk_mul_f32 v[180:181], v[180:181], v[184:185] op_sel_hi:[1,0]\n v_pk_mul_f32 v[182:183], v[182:183], v[184:185] op_sel_hi:[1,0]\n " \
  "v_cvt_pk_bf16_f32 v176, v176, v177\n v_cvt_pk_bf16_f32 v177, v178, v179\n v_cvt_pk_bf16_f32 v178, v180, v181\n v_cvt_pk_bf16_f32 v179, v182, v183\n " \
  "buffer_store_dwordx4 v[176:179], %[go], %[rs], 0 offen offset:2048 sc1\n ds_write_b128 %[lo], v[176:179] offset:2048\n " \
  "v_add_f32 v196, 0x358637bd, v196\n v_rcp_f32 v196, v196\n s_nop 0\n v_pk_mul_f32 v[188:189], v[188:189], v[196:197] op_sel_hi:[1,0]\n " \
  "v_pk_mul_f32 v[190:191], v[190:191], v[196:197] op_sel_hi:[1,0]\n v_pk_mul_f32 v[192:193], v[192:193], v[196:197] op_sel_hi:[1,0]\n " \
  "v_pk_mul_f32 v[194:195], v[194:195], v[196:197] op_sel_hi:[1,0]\n v_cvt_pk_bf16_f32 v188, v188, v189\n v_cvt_pk_bf16_f32 v189, v190, v191\n " \
  "v_cvt_pk_bf16_f32 v190, v192, v193\n v_cvt_pk_bf16_f32 v191, v194, v195\n buffer_store_dwordx4 v[188:191], %[go], %[rs], 0 offen offset:3072 sc1\n " \
  "ds_write_b128 %[lo], v[188:191] offset:3072\n "
#define G1_PURGE \
  ".purgem G1M\n .purgem G1RA\n .purgem G1RB\n .purgem G1BK\n .purgem G1AK\n .purgem G1RDB\n .purgem G1RDA\n .purgem G1Q\n .purgem G1SG\n "
#define G1_CLOBBER \
  "v80", "v81", "v82", "v83", "v84", "v85", "v86", "v87", "v92", "v93", "v94", "v95", "v96", "v97", "v98", "v99", "v100", "v101", "v102", "v103", \
  "v112", "v113", "v114", "v115", "v116", "v117", "v118", "v119", "v120", "v121", "v122", "v123", "v124", "v125", "v126", "v127", "v128", "v129", \
  "v130", "v131", "v132", "v133", "v134", "v135", "v144", "v145", "v146", "v147", "v152", "v153", "v154", "v155", \
  "v156", "v157", "v158", "v159"

template <int MODE>
__device__ __forceinline__ void g1_phase(char* smem, char* ws, u64& E, int w, f32x16& xq, Next& nx, int layer, const u16* __restrict__ x0, const u16* __restrict__ W,
                                         const u16* __restrict__ wo, const u16* __restrict__ wphi,
                                         const u16* __restrict__ wn1, const u16* __restrict__ wn2) {
  constexpr bool HH = (MODE == 1);
  constexpr int ABYTES = 128 * 128;
  constexpr int STAGE = ABYTES + 3 * 8192;
  constexpr int OFF_FX = 16384;
  constexpr int OFF_SP = 3 * STAGE;
  constexpr int OFF_RS = !HH ? 159744 : OFF_SP + 16384;
  constexpr int OFF_CS = OFF_RS + (HH ? 1152 : 1024);
  constexpr int OFF_WP = OFF_CS + 1536;
  constexpr int OFF_JK = LDS_TOTAL - 1024;
  constexpr int OFF_PK = 0;
  constexpr int OFF_VF = OFF_WP + 4096;
  constexpr int OFF_R2 = HH ? OFF_VF + 16384 : OFF_CS + 1024;
  constexpr int OFF_RD = OFF_CS + 768;
  constexpr int PSTR = 144;
  IDS
  W = uni(W); wphi = uni(wphi); wn1 = uni(wn1); wn2 = uni(wn2);
  const int kg = w >> 2, wm = (w >> 1) & 1, wn = w & 1;
  const bool first = !HH && layer == 0;
  f32x4 acc[2][2], qh[2][2], csq[2], st01, st23;
  bf16x8 wf[2];
  u64 kol;
  u32x4 pw[8];
  ST(layer * 24 + 0)
  const int R0 = wm * 64;
  u32x16 pa;
#define pcs pa[0]
#define prs pa[1]
#define opo pa[2]
#define ogo pa[3]
#define hof pa[7]
#define dm pa[8]
#define dm2 pa[9]
#define pcv pa[4]
#define pwf pa[5]
#define prv pa[6]
#define pvf pa[7]
#define ppk pa[10]
#define ppa pa[11]
#define pvr pa[12]
#define gkv pa[13]
#define gkz pa[14]
#define pwl pa[15]
#define pwm pa[4]
#define plb pa[10]
#define prd pa[11]
#define dm3 pa[12]
  f32x4 bias[3];
  const char* wos = wo_src(ws, wo, layer, hp);
  const unsigned tO = img(layer, hp) ? 8192u : 131072u;
  const unsigned sb = lds_addr(smem);
  auto prep = [&](auto ldr) {
    constexpr bool LDR = decltype(ldr)::value;
    if constexpr (!LDR) {
      pcs = sb + OFF_CS + wn * 128 + g * 16;
      prs = sb + OFF_RS + (R0 + fr) * 8;
      opo = (unsigned)(wn * 8192 + R0 * 64 + lane * 16);
      ogo = opo + WS_O + layer * (SEQ * DM * 2) + (unsigned)((rt * NH + 2 * hp) * 8192);
      opo += sb;
      pcv = sb + OFF_CS + (wn * 32 + 128 + fr) * 4;
      prv = sb + OFF_R2 + (R0 + 4 * g) * 4;
    } else {
      constexpr unsigned kvn = WS_KV + MODE * KVLB;
      const int t = tid & 255;
      dm = kvn + (unsigned)((rt * NH + 2 * hp) * KVPB) + t * 16;
      dm2 = t < 16 ? dm + 8192 : t >= 64 && t < 97 ? kvn + 16u * NH * KVPB + (unsigned)(hp * KVFB + rt * 528 + (t - 64) * 16) : 0x7fff0000u;
      if (!HH) dm3 = WS_HL + (unsigned)((256 + rt * 16 + hp) * 4096 + t * 16);
    }
    if constexpr (!HH) {
      prd = sb + OFF_RD + wn * 4;
      if constexpr (!LDR) {
        plb = sb + OFF_FX + wn * 2048 + lane * 16;
        hof = m0 + wm == 0 ? 0x7fff0000u : WS_HL + (unsigned)((layer * 128 + rt * 16 - 16 + wm * 16 + hp) * 4096 + wn * 2048 + lane * 16);
        const unsigned out = ~((0x5555u << 2 << fr) >> (4 * g));
#pragma unroll
        for (int i = 0; i < 8; ++i) {
          unsigned t;
          asm("v_bfe_i32 %0, %1, %2, 1" : "=v"(t) : "v"(out), "n"((i >> 2) * 16 + (i & 3)));
          bias[i >> 2][i & 3] = __uint_as_float(t & 0xff61b1e6u);
        }
        bias[2] = m0 + R0 == 0 ? f32x4{-3.0e38f, -3.0e38f, -3.0e38f, -3.0e38f} : bias[0];
        asm volatile("" : "+{v[36:39]}"(bias[0]), "+{v[40:43]}"(bias[1]), "+{v[44:47]}"(bias[2]));
      }
    } else {
      const int u = wm * 2 + kg;
      if constexpr (!LDR) {
        pwf = sb + OFF_WP + wn * 2048 + (4 * g + q4) * 64 + p4 * 8;
        pvf = sb + OFF_VF + w * 2048 + lane * 16;
        ppk = sb + OFF_PK + w * (32 * PSTR) + fr * PSTR + g * 8;
      }
      ppa = sb + OFF_PK + wn * (32 * PSTR) + (4 * g + q4) * PSTR + u * 32 + p4 * 8;
      pvr = sb + OFF_VF + wn * 2048 + lane * 16;
      const unsigned doff = WS_KV + (layer >> 1) * KVLB + (unsigned)((rt * NH + 2 * hp + wn) * KVPB);
      gkv = doff + u * 1024 + lane * 16;
      gkz = fr == 0 ? doff + 4096 + u * 32 + g * 8 : 0x7fff0000u;
    }
    asm volatile("" : "+{v[48:63]}"(pa));
  };

  if (kg == 1) {

    const int lw = w & 3;
    const int kB = 16 * lw + (lane >> 3);
    const int fB = (kB >> 1) & 1;
    const int cB0 = ((((lane & 7) >> 1) ^ fB) << 1) | (lane & 1), cB1 = ((((lane & 7) >> 1) ^ (fB | 2)) << 1) | (lane & 1);
    const bool im = img(layer, hp);

    unsigned vW0, vW1, vP, vX, vY;
    u64 ko2;
    auto ld_pw = [&](int i0) {
#pragma unroll
      for (int i = i0; i < i0 + 4; ++i) gld16(pw[i], vP, wn1 + i * 8 * 3072);
    };
    auto wo_tile = [&](int t) { glds2v(wos + knib(ko2, t) * tO, vW0, vW1, lds_addr(smem) + OFF_WO + t * 8192 + lw * 2048); };
    auto run = [&](auto fst) {
      constexpr bool FIRST = decltype(fst)::value;
      constexpr int N = 10;
      const unsigned vA = FIRST ? (unsigned)(((m0 + lw * 32 + (lane >> 3)) * DM + ((lane & 7) ^ (lane >> 3)) * 8) * 2)
                                : (unsigned)(rt * 262144 + lw * 4096 + lane * 16);
      unsigned imv = !FIRST && im ? ~0u : 0u;
      if (!FIRST) asm volatile("" : "+v"(imv));
      const unsigned vB0 = ((unsigned)(lw * 6144 + lane * 16) & imv) | ((unsigned)((kB * 3072 + hp * 64 + cB0 * 8) * 2) & ~imv);
      const unsigned vB1 = ((unsigned)(lw * 6144 + lane * 16) & imv) | (((unsigned)(((kB + 8) * 3072 + hp * 64 + cB1 * 8) * 2) - 1024u) & ~imv);
      const char* xa = FIRST ? (const char*)x0 : ws + WS_X + (layer - 1) * (SEQ * DM * 2);
      constexpr int KS = FIRST ? 128 : 16384;
      const u64 kord = FIRST ? korder0(rt) : nx.kord;
      auto ktile = [&](int ks) { return knib(kord, ks); };
      auto stageA = [&](int ks, int boff) {
        const int kt = ktile(ks);
        const unsigned sA = lds_addr(smem) + boff;
        if (FIRST) glds4v(xa + kt * KS, vA, vA + 15360u, vA + 2 * 15360u, vA + 3 * 15360u, sA + lw * 4096);
        else glds4c(xa + kt * KS, vA, sA + lw * 4096);
      };
      auto stageB = [&](const u16* Wp, int ks, int boff) {
        const int kt = ktile(ks);
        const unsigned sB = lds_addr(smem) + boff + ABYTES;
        const char* b = FIRST ? (const char*)(Wp + (size_t)kt * 64 * 3072) : nx.wq + kt * nx.tw;
        glds4v(b, vB0, vB1, vB0, vB1, sB + lw * 6144);
        glds2v(b + 4096, vB0, vB1, sB + lw * 6144 + 4096);
      };
      bf16x8 sel[3];
      unsigned cso[3], cst;
      if (FIRST) {
        stageA(1, STAGE); stageB(W, 1, STAGE);
        __builtin_amdgcn_sched_barrier(0);
        const int fl = (q4 >> 1) | ((g & 1) << 1), j = 3 * wm + (fr < 3 ? fr : 0);
#pragma unroll
        for (int i = 0; i < 3; ++i) {
          const unsigned o = fr == i ? 0x3F803F80u : 0u;
          sel[i] = __builtin_bit_cast(bf16x8, u32x4{o, o, o, o});
          cso[i] = (unsigned)(2 * STAGE + ABYTES + (g >> 1) * 6144 + (g & 1) * 1024 + q4 * 128 + p4 * 8 + (((wn * 2 + ((wm + i) & 1)) ^ fl) << 5) + ((3 * wm + i) >> 1) * 2048);
          asm volatile("" : "+v"(sel[i]), "+v"(cso[i]));
        }
        cst = fr < 3 ? WS_CPA + (unsigned)(((hp * 16 + rt) * 192 + (j >> 1) * 64 + wn * 32 + (j & 1) * 16 + 4 * g) * 4) : 0x7fff0000u;
        asm volatile("" : "+v"(cst));
        WAITVN(N);
        BAR;
        stageA(2, 2 * STAGE); stageB(W, 2, 2 * STAGE);
      }
      u64 fall, fal2;
      unsigned fao, fa2, vS, vC, dS, dC, dR;
      auto setup = [&]() {
        fao = (unsigned)((256 + rt * 16 + (lane & 15)) * FLS);
        fa2 = (unsigned)(((FIRST ? 512 : 256) + (lane & 15) * 16 + (FIRST ? hp : (hp - ((1 << (lane >> 4)) >> 1)) & 15)) * FLS);
        vP = (unsigned)((rt * 64 + (lane >> 3)) * 6144 + lw * 2048 + ((hp + (1 << lw)) & 15) * 128 + (lane & 7) * 16);
        vS = FIRST ? (unsigned)(WS_G0 + (m0 + lw * 32 + (lane & 31)) * 16)
                   : WS_ST + (unsigned)((layer - 1) * (SEQ * 128) + rt * 16384 + g * 1024 + lw * 256 + fr * 16);
        vC = (FIRST ? WS_CPA : WS_CSP) + (unsigned)(hp * 12288 + g * 768 + lw * 192 + (fr < 12 ? fr : 0) * 16);
        if (!FIRST) vX = HH ? (unsigned)(hp * 4096 + lw * 1024 + lane * 16) : (unsigned)((rt * 16 + hp) * 1024 + lane * 16);
        if (HH) { vY = (unsigned)(bid * 8192 + lw * 2048 + lane * 16); asm volatile("" : "+v"(vY)); }
        vW0 = ((unsigned)(lw * 2048 + lane * 16) & imv) | ((unsigned)((kB * DM + hp * 64 + ((lane & 7) ^ ((lane >> 4) << 1)) * 8) * 2) & ~imv); vW1 = vW0 + ((8 * DM * 2 - 1024u) & ~imv);
        dS = OFF_RS + lw * 256 + (FIRST ? (lane & 31) * 8 : fr * 16 + (g & 1) * 8); dC = OFF_CS + lw * 192 + (fr < 12 ? fr : 0) * 16 + g * 4;
        ko2 = korder(bid & 3, hp);
        dR = OFF_R2 + lw * 128 + (FIRST ? (lane & 31) * 4 : fr * 8 + (g & 1) * 4);
        asm volatile("" : "+v"(dR));
        asm volatile("" : "+v"(vS), "+v"(vX), "+v"(vW0), "+v"(vW1), "+v"(dS));
        asm volatile("" : "+s"(ko2));
        if (FIRST || HH) asm volatile("" : "+v"(vC), "+v"(dC), "+v"(fa2));
        if (FIRST) asm volatile("" : "+v"(vP)); else asm volatile("" : "+v"(fao));
      };
      if (!FIRST) setup();
      u32x4 sv[4], cv[4];
      auto ld_c = [&](u32x4& d, unsigned v, int so) { asm volatile("buffer_load_dwordx4 %0, %1, %2, %3 offen sc1" : "=v"(d) : "v"(v), "s"(rsW), "s"(so) : "memory"); };
      float tm, tr;
      unsigned tc;
      auto fin = [&]() {
        if (FIRST) {
          const unsigned tag = (unsigned)E + 1u;
          asm volatile("" : "+v"(sv[0]));
          _Pragma("unroll 1") for (unsigned spins = 0; spins < (1u << 18); ++spins) {
            if (__all(sv[0][1] == tag && sv[0][3] == tag)) break;
            __builtin_amdgcn_s_sleep(1);
            asm volatile("buffer_load_dwordx4 %0, %1, %2, 0 offen sc1\n\ts_waitcnt vmcnt(0)" : "=v"(sv[0]) : "v"(vS), "s"(rsW) : "memory");
          }
          tm = __uint_as_float(sv[0][0]); tr = __uint_as_float(sv[0][2]);
        } else {
          asm volatile("" : "+v"(sv[0]), "+v"(sv[1]), "+v"(sv[2]), "+v"(sv[3]));
          const f32x4 t = sum4(sv);
          const unsigned u = xs4(t[0], t[2], t[1], t[3]);
          auto h = __builtin_amdgcn_permlane32_swap(u, u, false, false);
          const float mean = __uint_as_float(h[0]) * (1.0f / 1024.0f);
          const float var = fmaxf(__uint_as_float(h[1]) * (1.0f / 1024.0f) - mean * mean, 0.f);
          tm = -mean; tr = __builtin_amdgcn_rsqf(var + 1e-5f);
        }
      };
      auto cfin = [&]() {
        if (FIRST || HH) {
          asm volatile("" : "+v"(cv[0]), "+v"(cv[1]), "+v"(cv[2]), "+v"(cv[3]));
          const f32x4 t = sum4(cv);
          tc = xs4(t[0], t[1], t[2], t[3]);
        }
      };
      bf16x8 cf[6];
      f32x4 csum = {0, 0, 0, 0};
      if (FIRST) { fall = E + 1u; vX = (unsigned)(bid * 8192 + lw * 2048 + lane * 16); asm volatile("" : "+v"(fall), "+v"(vX)); }
#pragma unroll
      for (int kt = 0; kt < 13; ++kt) {
        const int cur = (kt % 3) * STAGE;
        if (!FIRST && kt == 0) { WAITV(4); }
        else if (FIRST && kt == 4 && lw == 1) { WAITVN(N + 2); }
        else if (HH && (kt == 4 || kt == 6) && true) { WAITVN(N + 1); }
        else if (HH && kt == 5 && true) { WAITVN(N + 2); }
        else if ((!FIRST && kt == 1 && lw == 3) || ((FIRST || HH) && kt == 10 && lw == 3) || (FIRST && (kt == 2 || kt == 4 || kt == 5))) { WAITVN(N + 1); }
        else { WAITVN(N); }
        if (!FIRST && kt == 2 && lw == 3) {
          asm volatile("" : "+v"(fall));
          if (__any(fall != E + (unsigned)layer)) poll_flags(ws, E, 1, rt, hp, lane, layer, 0xffffull);
        }
        if (HH && kt == 11 && lw == 3) {
          asm volatile("" : "+v"(fal2));
          if (__any(fal2 != E + (unsigned)layer)) {
            _Pragma("unroll 1") for (int j = 0; j < 4; ++j) poll_flags(ws, E, 1, rt, (hp - ((1 << j) >> 1)) & 15, lane, layer, 0xffff00000000ull);
          }
        }
        if (FIRST && kt == 11 && lw == 3) {
          asm volatile("" : "+v"(fal2));
          if (__any(fal2 != E + 1u)) poll_flags(ws, E, 2, rt, hp, lane, 1u, 0xffff00000000ull);
        }
        BAR;
        if (FIRST && kt == 1) {
          asm volatile("" ::: "memory");
#pragma unroll
          for (int i = 0; i < 6; ++i) { const char* t = smem + cso[i >> 1] + (i & 1) * 12288; cf[i] = cat8(tr_read(t), tr_read(t + 512)); }
        }
        if (FIRST && kt >= 2 && kt < 6 && lw == kt - 2) setup();
        if (kt >= 6 && kt < 10 && lw == kt - 6) prep(IC<1>{});
        stageA(kt + 3, cur);
        if (FIRST && kt == 1) {
          asm volatile("" : "+v"(sel[0]));
#pragma unroll
          for (int i = 0; i < 6; ++i) csum = MFMA(cf[i], sel[i >> 1], csum);
          asm volatile("" : "+v"(csum));
        }
        stageB(W, kt + 3, cur);
        if (FIRST) {
          if (kt == 1) __builtin_amdgcn_raw_buffer_store_b128(__builtin_bit_cast(u32x4, csum), rsW, cst, 0, 16);
          if (kt == 3 && tid == 320) __builtin_amdgcn_raw_buffer_store_b64(__builtin_bit_cast(u32x2, fall), rsW, WS_FLAGS + (unsigned)(512 + rt * 16 + hp) * FLS, 0, 16);
          if (kt == 3 || kt == 4) glds16s(wo + (kt - 3) * 512, vX, lds_addr(smem) + OFF_JK);
        }
        if (HH && (kt == 3 || kt == 4) && true) glds16s(wo + (kt - 3) * 512, vY, lds_addr(smem) + OFF_JK);
        if (!FIRST && kt == 0 && lw == 3) asm volatile("global_load_dwordx2 %0, %1, %2 sc1" : "=v"(fall) : "v"(fao), "s"(ws + WS_FLAGS) : "memory");
        if ((FIRST || HH) && kt == 9 && lw == 3) asm volatile("global_load_dwordx2 %0, %1, %2 sc1" : "=v"(fal2) : "v"(fa2), "s"(ws + WS_FLAGS) : "memory");
      }
      WAITVN(N);
      BAR;
#pragma unroll
      for (int i = 0; i < (FIRST ? 1 : 4); ++i) ld_c(sv[i], vS, i * 4096);
      if (FIRST || HH) {
#pragma unroll
        for (int i = 0; i < 4; ++i) ld_c(cv[i], vC, i * 3072);
      }
      if (HH) glds16s(wphi, vX, lds_addr(smem) + OFF_WP + lw * 1024);
      else if (!FIRST) glds16c(ws + WS_CSA, vX, lds_addr(smem) + OFF_CS);
      wo_tile(0); wo_tile(1);
      WAITVN(HH ? 13 : 9);
      BAR;
      if (FIRST) {
        if (lw < 3) { ld_pw(0); ld_pw(4); WAITV(12); } else { WAITV(4); }
      } else {
        WAITV(4);
      }
      cfin();
      fin();
      asm volatile("" : "+v"(tm), "+v"(tr));
      if (FIRST || HH) asm volatile("" : "+v"(tc));
      if (FIRST && lw < 3) { WAITV(8); } else { WAITV(0); }
      lds<f32x2>(sb + dS) = f32x2{tm, tr};
      lds<float>(sb + dR) = tm; lds<float>(sb + dR + 512) = tr;
      if (FIRST || HH) lds<unsigned>(sb + dC) = tc;
      if (!HH) lds<unsigned>(prd) = 0u;
      WAITL0;
    };
    typedef std::integral_constant<bool, true> T_;
    typedef std::integral_constant<bool, false> F_;
    __builtin_amdgcn_s_setprio(3);
    if (first) run(T_{}); else run(F_{});
    __builtin_amdgcn_s_setprio(0);
    BAR;
    if constexpr (HH) {
      u32x4 c0, c1;
      if (layer == 1) {
        const int n = bid * 8 + lw * 2;
        const void* p;
        unsigned a0, a1;
        if (n < 1536) {
          const int r = n % 768, q = r % 384, e = q % 24;
          p = n < 768 ? wn1 : W;
          a0 = (unsigned)((((q / 24) * 64 + (e / 6) * 16 + (lane >> 3)) * 3072 + ((e % 6) >> 1) * 1024 + (r < 384 ? 192 : 704)) * 2);
          a1 = a0 + 8 * 6144 + cB1 * 16; a0 += cB0 * 16;
        } else {
          p = n < 1792 ? wn2 : wo;
          a0 = (unsigned)((((n & 127) * 8 + (lane >> 3)) * DM + ((n & 128) ? 704 : 192) + ((lane & 7) ^ ((lane >> 4) << 1)) * 8) * 2);
          a1 = a0 + 8 * DM * 2;
        }
        p = uni(p);
        gld16(c0, a0, p); gld16(c1, a1, p);
        const u32x4 pz = empty4();
        __builtin_amdgcn_raw_buffer_store_b128(pz, rsW, dm, 0, 16);
        __builtin_amdgcn_raw_buffer_store_b128(pz, rsW, dm, 4096, 16);
        __builtin_amdgcn_raw_buffer_store_b128(pz, rsW, dm2, 0, 16);
      }
#pragma unroll
      for (int t = 2; t < 6; ++t) wo_tile(t);
      if (layer == 1) {
        WAITV(11);
        __builtin_amdgcn_sched_barrier(0);
        asm volatile("" : "+v"(c0), "+v"(c1));
        __builtin_amdgcn_raw_buffer_store_b128(c0, rsW, WS_IMG + (unsigned)(bid * 8192 + lw * 2048 + lane * 16), 0, 16);
        __builtin_amdgcn_raw_buffer_store_b128(c1, rsW, WS_IMG + (unsigned)(bid * 8192 + lw * 2048 + 1024 + lane * 16), 0, 16);
        WAITV(0);
      }
      pwl = vW0; pwm = vW1; kol = ko2;
      asm volatile("" : "=v"(csq[0]), "=v"(csq[1]), "=v"(st01), "=v"(st23), "=v"(wf[0]), "=v"(wf[1]));
      asm volatile("" : "=v"(acc[0][0]), "=v"(acc[0][1]), "=v"(acc[1][0]), "=v"(acc[1][1]));
      asm volatile("" : "=v"(qh[0][0]), "=v"(qh[0][1]), "=v"(qh[1][0]), "=v"(qh[1][1]));
    } else {
      const int lane = lane_id();
#pragma unroll
      for (int t = 2; t < 12; ++t) {
        wo_tile(t);
        if (t == 2 && first) {
        const u32x4 pz = empty4();
        __builtin_amdgcn_raw_buffer_store_b128(pz, rsW, dm, 0, 16);
        __builtin_amdgcn_raw_buffer_store_b128(pz, rsW, dm, 4096, 16);
        __builtin_amdgcn_raw_buffer_store_b128(pz, rsW, dm3, 0, 16);
        __builtin_amdgcn_raw_buffer_store_b128(pz, rsW, dm2, 0, 16);
        if (lw == 3 && lane < 48) __builtin_amdgcn_raw_buffer_store_b128(lds<u32x4>(sb + OFF_CS + lane * 16), rsW, WS_CSA + (unsigned)((rt * 16 + hp) * 1024 + lane * 16), 0, 16);
        }
      }
      BAR;
      if (first) { WAITV(0); }
    }
    asm volatile("" : "={v[64:79]}"(xq));
  } else {

    prep(IC<0>{});
    if (first) {
      const int row = m0 + hp * 8 + w * 2 + (lane >> 5);
      asm volatile("s_load_dwordx2 %0, %1, 0x0\n\ts_waitcnt lgkmcnt(0)" : "=s"(E) : "s"(ws) : "memory");
      const unsigned tag = (unsigned)E + 1u;
      static_assert(HH || (OFF_SP == 122880 && OFF_R2 + 1024 <= OFF_JK), "addresses used at the top of the kernel");
      WAITV(0);
      u32x4 xs[4];
#pragma unroll
      for (int i = 0; i < 4; ++i) xs[i] = *(const u32x4*)(smem + OFF_SP + w * 4096 + (lane >> 5) * 2048 + (lane & 31) * 16 + i * 512);
      typedef __attribute__((ext_vector_type(2))) __bf16 bf2;
      const bf2 one2 = __builtin_bit_cast(bf2, 0x3F803F80u);
      float sa[2] = {0.f, 0.f}, sq[2] = {0.f, 0.f};
#pragma unroll
      for (int i = 0; i < 16; ++i) {
        const unsigned u = xs[i >> 2][i & 3];
        const bf2 t = __builtin_bit_cast(bf2, u);
        sa[i & 1] = __builtin_amdgcn_fdot2_f32_bf16(t, one2, sa[i & 1], false);
        sq[i & 1] = __builtin_amdgcn_fdot2_f32_bf16(t, t, sq[i & 1], false);
      }
      float sm = r16_sum(sa[0] + sa[1]), m2 = r16_sum(sq[0] + sq[1]);
      {
        auto r = __builtin_amdgcn_permlane16_swap(__float_as_uint(sm), __float_as_uint(sm), false, false);
        auto q = __builtin_amdgcn_permlane16_swap(__float_as_uint(m2), __float_as_uint(m2), false, false);
        sm = __uint_as_float(r[0]) + __uint_as_float(r[1]); m2 = __uint_as_float(q[0]) + __uint_as_float(q[1]);
      }
      const float mean = sm * (1.0f / 1024.0f);
      m2 = fmaxf(m2 - sm * mean, 0.f);
      u32x4 gw = {__float_as_uint(-mean), tag, __float_as_uint(rsqrtf(m2 * (1.0f / 1024.0f) + 1e-5f)), tag};
      if ((lane & 31) == 0) __builtin_amdgcn_raw_buffer_store_b128(gw, rsW, (unsigned)(WS_G0 + row * 16), 0, 16);
      __builtin_amdgcn_raw_buffer_store_b128(empty4(), rsW, WS_HL + (unsigned)((rt * 16 + hp) * 4096 + tid * 16), 0, 16);
    }
    const int fl = (q4 >> 1) | ((g & 1) << 1);
    const unsigned bo = lds_addr(smem) + ABYTES + (g >> 1) * 6144 + (g & 1) * 1024 + q4 * 128 + p4 * 8;
    const unsigned bo0 = bo + (((wn * 2 + 0) ^ fl) << 5), bo1 = bo + (((wn * 2 + 1) ^ fl) << 5);
    const unsigned ar = lds_addr(smem) + (wm * 64 + fr) * 128;
    const unsigned aK0 = ar + ((g ^ (fr & 7)) << 4), aK1 = ar + (((4 + g) ^ (fr & 7)) << 4);
    f32x32 ac0, ac1, ac2;
    asm volatile("" :: "v"(aK0), "v"(aK1), "v"(bo0), "v"(bo1));
    if (first) BAR;
    else publish(rsW, tid, 256 + rt * 16 + hp, E + (unsigned)layer);
#define G1_OUT
#define G1_T(a)
#define G1_TEND
#define G1_TCL
    asm volatile(G1_MACROS
        "s_mov_b32 vcc_hi, %[lay]\n"
        ".if %[hh]\n s_branch .Lg1m\n .else\n"
        ".p2align 3\n .Lg1m:\n"
        "G1RDB 0, v16, v17, 0\n G1RDA v12, v13, 0\n G1RDB 1, v16, v17, 0\n"
        "ds_read_b128 v[64:67], v20\n ds_read_b128 v[68:71], v20 offset:2048\n"
        "ds_read_b128 v[72:75], v20 offset:4096\n ds_read_b128 v[76:79], v20 offset:6144\n"
        "s_cmp_eq_u32 vcc_hi, 0\n s_waitcnt lgkmcnt(12)\n"
        "G1SG 0, 1, 1, v12, v13, v16, v17, 0\n"
        "s_mov_b32 vcc_lo, 16\n"
        "G1SG 1, 1, 1, v12, v13, v16, v17, 40960\n"
        "G1SG 2, 1, 2, v12, v13, v16, v17, 40960\n"
        "s_cbranch_scc1 .Lg1a%=\n"
        "s_barrier\n"
        G1_T(88)
        "s_barrier\n"
        G1_T(92)
        "G1RDA v12, v13, 40960\n"
        "s_waitcnt lgkmcnt(4)\n"
        "s_branch .Lg1b%=\n"
        ".Lg1a%=:\n"
        G1_T(88)
        "s_barrier\n"
        G1_T(92)
        "G1RDB 0, v16, v17, 40960\n G1RDA v12, v13, 40960\n G1RDB 1, v16, v17, 40960\n"
        ".p2align 3\n s_nop 0\n"
        ".Lg1b%=:\n"
        "s_waitcnt lgkmcnt(8)\n"
        "G1SG 0, 0, 1, v12, v13, v16, v17, 40960\n"
        "s_barrier\n"
        "G1SG 1, 0, 1, v14, v15, v18, v19, 0\n"
        "G1SG 2, 0, 1, v14, v15, v18, v19, 0\n"
        "s_lshr_b32 vcc_lo, vcc_lo, 1\n s_waitcnt vmcnt(0) lgkmcnt(8)\n"
        "G1SG 0, 0, 1, v14, v15, v18, v19, 0\n"
        "s_barrier\n"
        "G1SG 1, 0, 1, v12, v13, v16, v17, 0\n"
        "G1SG 2, 0, 1, v12, v13, v16, v17, 0\n"
        "s_cbranch_scc0 .Lg1c%=\n s_waitcnt lgkmcnt(8)\n"
        "G1SG 0, 0, 1, v12, v13, v16, v17, 0\n"
        "s_barrier\n"
        "G1SG 1, 0, 1, v12, v13, v16, v17, 40960\n"
        "G1SG 2, 0, 1, v12, v13, v16, v17, 40960\n"
        "s_branch .Lg1b%=\n"
        ".Lg1c%=:\n"
        "s_waitcnt lgkmcnt(8)\n"
        "G1SG 0, 0, 1, v12, v13, v16, v17, 0\n s_bitcmp1_b32 vcc_hi, 0\n"
        "G1SG 1, 0, 0, v12, v13, v16, v17, 0\n"
        G1_T(96)
        "s_cbranch_scc1 .Lg1n\n s_barrier\n"
        "G1Q 2, 2, 0, 0, 0\n ds_read2_b64 v[136:139], v49 offset0:32 offset1:48\n"
        "G1Q 2, 2, 0, 1, 0\n ds_read_b128 v[96:99], v48 offset:256\n"
        "G1Q 2, 2, 0, 2, 0\n ds_read_b128 v[100:103], v48 offset:320\n"
        "G1Q 2, 2, 0, 3, 0\n ds_read2_b32 v[144:145], v52 offset1:16\n"
        "G1Q 2, 3, 1, 0, 0\n ds_read_b128 v[104:107], v54 offset:192\n"
        "G1Q 2, 3, 1, 1, 0\n ds_read_b128 v[108:111], v54 offset:704\n"
        "G1Q 2, 3, 1, 2, 0\n ds_read2_b64 v[140:143], v49 offset1:16\n"
        "G1Q 2, 3, 1, 3, 0\n ds_read_b128 v[148:151], v48\n"
        "G1Q 2, 0, 2, 0, 0\n ds_read_b128 v[88:91], v48 offset:64\n"
        "G1Q 2, 0, 2, 1, 0\n ds_read_b128 v[128:131], v54\n"
        "G1Q 2, 0, 2, 2, 0\n ds_read_b128 v[92:95], v54 offset:512\n"
        "G1Q 2, 0, 2, 3, 0\n"
        "G1Q 2, 1, 3, 0, 0\n"
        "G1Q 2, 1, 3, 1, 0\n"
        "G1Q 2, 1, 3, 2, 0\n"
        "G1Q 2, 1, 3, 3, 0\n"
        "ds_read_b128 v[112:115], v54 offset:128\n ds_read_b128 v[116:119], v54 offset:640\n ds_read_b128 v[152:155], v54 offset:64\n ds_read_b128 v[156:159], v54 offset:576\n"
        "s_waitcnt lgkmcnt(4)\n"
        G1_SLIDE
        ".endif\n .if %[hh]\n .p2align 3\n s_nop 0\n .Lg1n:\n s_barrier\n"
        G1_HEDGE
        ".endif\n"
        G1_TEND G1_PURGE
        : "=&{v[160:191]}"(ac0), "=&{v[192:223]}"(ac1), "=&{v[224:255]}"(ac2), "=&{v[64:79]}"(xq),
          "=&{v[88:91]}"(csq[1]), "=&{v[148:151]}"(csq[0]), "=&{v[104:107]}"(wf[0]), "=&{v[108:111]}"(wf[1]), "=&{v[140:143]}"(st01), "=&{v[136:139]}"(st23),
          "+{v[48:63]}"(pa), "+{v[36:39]}"(bias[0]), "+{v[40:43]}"(bias[1]), "+{v[44:47]}"(bias[2]) G1_OUT
        : "{v12}"(aK0), "{v13}"(aK1), "{v14}"(aK0 + 2 * STAGE), "{v15}"(aK1 + 2 * STAGE), "{v16}"(bo0), "{v17}"(bo1),
          "{v18}"(bo0 + 2 * STAGE), "{v19}"(bo1 + 2 * STAGE), "{v20}"(wn ? aK1 : aK0),
          [lay] "s"(layer), [hh] "n"(MODE), [rs] "s"(rsW), [wm] "s"(wm)
        : "memory", "scc", "vcc", "m0", G1_TCL G1_CLOBBER);
    acc[0][0] = quad<0>(ac0); acc[0][1] = quad<1>(ac0); acc[1][0] = quad<6>(ac0); acc[1][1] = quad<7>(ac0);
    asm volatile("" : "=s"(kol));
    qh[0][0] = quad<4>(ac1); qh[0][1] = quad<5>(ac1); qh[1][0] = quad<2>(ac2); qh[1][1] = quad<3>(ac2);
  }
  ST(layer * 24 + 4)
  {
  const int lane = lane_id(), tid = w * 64 + lane, fr = lane & 15, g = lane >> 4;
  (void)fr; (void)g;
  if constexpr (HH) {
    const f32x4 z4 = {0, 0, 0, 0};
    constexpr float L2E = 1.4426950408889634f;
    const unsigned kvo = WS_KV + (layer >> 1) * KVLB, kfo = kvo + 16u * NH * KVPB + hp * KVFB;
    auto phi2 = [&](const f32x4 (&a)[2][2], f32x4 st, bf16x8 (&qp)[2][2]) {
      f32x4 pm[2][2];
      f32x2 f[2][4];
      asm volatile("" : "+v"(st));
#pragma unroll
      for (int i = 0; i < 8; ++i) {
        const int rb = i >> 2, c = (i >> 1) & 1, e = (i & 1) * 2;
        f[rb][i & 3] = __builtin_elementwise_fma(f32x2{csq[c][e], csq[c][e + 1]}, f32x2{st[2 * rb], st[2 * rb]}, f32x2{a[rb][c][e], a[rb][c][e + 1]});
      }
#pragma unroll
      for (int rb = 0; rb < 2; ++rb) asm volatile("" : "+v"(f[rb][0]), "+v"(f[rb][1]), "+v"(f[rb][2]), "+v"(f[rb][3]));
#pragma unroll
      for (int rb = 0; rb < 2; ++rb) {
        const bf16x8 xf = __builtin_bit_cast(bf16x8, u32x4{pk2(f[rb][0][0], f[rb][0][1]), pk2(f[rb][1][0], f[rb][1][1]), pk2(f[rb][2][0], f[rb][2][1]), pk2(f[rb][3][0], f[rb][3][1])});
        pm[rb][0] = MFMA(wf[0], xf, z4); pm[rb][1] = MFMA(wf[1], xf, z4);
      }
      asm volatile("s_nop 7" : "+v"(pm[0][0]), "+v"(pm[0][1]), "+v"(pm[1][0]), "+v"(pm[1][1]));
      float mx[2], mn[2];
#pragma unroll
      for (int u = 0; u < 2; ++u) max8pn(pm[u][0], pm[u][1], mx[u], mn[u]);
      xr4<true>(mx[0], mn[0], mx[1], mn[1]);
      float ep[2][8], en[2][8], sp[2], sn[2];
#pragma unroll
      for (int u = 0; u < 2; ++u) {
        const float c = st[2 * u + 1] * L2E, mxs = -mx[u] * c, mns = -mn[u] * c;
#pragma unroll
        for (int i = 0; i < 4; ++i) {
          const f32x2 p2 = {pm[u][i >> 1][(i & 1) * 2], pm[u][i >> 1][(i & 1) * 2 + 1]};
          const f32x2 tp = __builtin_elementwise_fma(p2, f32x2{c, c}, f32x2{mxs, mxs});
          const f32x2 tn = __builtin_elementwise_fma(-p2, f32x2{c, c}, f32x2{mns, mns});
          ep[u][2 * i] = __builtin_amdgcn_exp2f(tp[0]); ep[u][2 * i + 1] = __builtin_amdgcn_exp2f(tp[1]);
          en[u][2 * i] = __builtin_amdgcn_exp2f(tn[0]); en[u][2 * i + 1] = __builtin_amdgcn_exp2f(tn[1]);
        }
        const f32x2 a2 = (f32x2{ep[u][0], ep[u][1]} + f32x2{ep[u][2], ep[u][3]}) + (f32x2{ep[u][4], ep[u][5]} + f32x2{ep[u][6], ep[u][7]});
        const f32x2 b2 = (f32x2{en[u][0], en[u][1]} + f32x2{en[u][2], en[u][3]}) + (f32x2{en[u][4], en[u][5]} + f32x2{en[u][6], en[u][7]});
        sp[u] = a2[0] + a2[1]; sn[u] = b2[0] + b2[1];
      }
      xr4<false>(sp[0], sn[0], sp[1], sn[1]);
#pragma unroll
      for (int rb = 0; rb < 2; ++rb) {
        const float ip = __builtin_amdgcn_rcpf(sp[rb]), in = __builtin_amdgcn_rcpf(sn[rb]);
        qp[rb][0] = __builtin_bit_cast(bf16x8, u32x4{pk2(ep[rb][0] * ip, ep[rb][1] * ip), pk2(ep[rb][2] * ip, ep[rb][3] * ip), pk2(ep[rb][4] * ip, ep[rb][5] * ip), pk2(ep[rb][6] * ip, ep[rb][7] * ip)});
        qp[rb][1] = __builtin_bit_cast(bf16x8, u32x4{pk2(en[rb][0] * in, en[rb][1] * in), pk2(en[rb][2] * in, en[rb][3] * in), pk2(en[rb][4] * in, en[rb][5] * in), pk2(en[rb][6] * in, en[rb][7] * in)});
        asm volatile("" : "+v"(qp[rb][0]), "+v"(qp[rb][1]));
      }
    };
    bf16x8 one8 = ones8();
    asm volatile("" : "+v"(one8));
    WAITL0; BAR;
    ST(layer * 24 + 5)
    {
      bf16x8 af[4], v0[4], v1[4];
      asm volatile("" ::: "memory");
#pragma unroll
      for (int sw = 0; sw < 4; ++sw) {
        af[sw] = cat8(tr_read(ppa + sw * (64 * PSTR)), tr_read(ppa + sw * (64 * PSTR) + 16 * PSTR));
        v0[sw] = lds<bf16x8>(pvr + sw * 4096);
        v1[sw] = lds<bf16x8>(pvr + sw * 4096 + 1024);
      }
      __builtin_amdgcn_sched_barrier(0);
      f32x4 kv[3] = {z4, z4, z4};
#pragma unroll
      for (int sw = 0; sw < 4; ++sw) {
        kv[0] = MFMA(af[sw], v0[sw], kv[0]);
        kv[1] = MFMA(af[sw], v1[sw], kv[1]);
        kv[2] = MFMA(af[sw], one8, kv[2]);
      }
      auto pc = [](float x, float y) { const unsigned t = pk2(x, y); return t < EMPTY - 1u ? t : EMPTY - 1u; };
      __builtin_amdgcn_raw_buffer_store_b128(u32x4{pc(kv[0][0], kv[0][1]), pc(kv[0][2], kv[0][3]), pc(kv[1][0], kv[1][1]), pc(kv[1][2], kv[1][3])}, rsW, gkv, 0, 16);
      __builtin_amdgcn_raw_buffer_store_b64(u32x2{pc(kv[2][0], kv[2][1]), pc(kv[2][2], kv[2][3])}, rsW, gkz, 0, 16);
    }
    auto wo_late = [&](int t) { glds2v(wos + knib(kol, t) * tO, pwl, pwm, sb + OFF_WO + t * 8192 + (w & 3) * 2048); };
    ST(layer * 24 + 6)
    u32x4 l1[8];
    const bool red = w == 7 || (w == 6 && (rt & 7) == 0);
    unsigned lo = kvo + (unsigned)((2 * hp + (rt >> 3) + (lane >> 5) * 8 * NH) * KVPB) +
                  (w == 7 ? (unsigned)((rt & 4) * 512 + (g & 1) * 1024 + (rt & 3) * 256 + fr * 16) : (unsigned)(4096 + (lane & 7) * 16));
    unsigned fs = w == 7 ? kfo + (unsigned)((rt >> 2) * 2048 + (lane >> 5) * 1024 + (rt & 3) * 256 + fr * 16 + (g & 1) * 8)
                : (lane & 24) ? 0x7fff0000u : kfo + (unsigned)(8192 + (rt >> 3) * 128 + (lane & 4) * 16 + (lane & 1) * 32 + (lane >> 5) * 16 + (lane & 2) * 4);
    auto l1_load = [&]() {
      asm volatile("" ::: "memory");
#pragma unroll
      for (int p = 0; p < 8; ++p) asm volatile("buffer_load_dwordx4 %0, %1, %2, %3 offen sc1" : "=v"(l1[p]) : "v"(lo), "s"(rsW), "s"(p * (NH * KVPB)) : "memory");
      __builtin_amdgcn_sched_barrier(0);
    };
    if (red) asm volatile("" : "+v"(lo), "+v"(fs));
    if (red) { __builtin_amdgcn_s_sleep(13); l1_load(); }
    bf16x8 qp2[2][2];
    if (!kg) {
      phi2(qh, st23, qp2);
    } else if (red) {
      typedef __attribute__((ext_vector_type(2))) __bf16 bf2;
      unsigned c0 = 0x00003F80u, c1 = 0x3F800000u;
      asm volatile("" : "+v"(c0), "+v"(c1));
      float s4[4];
      _Pragma("unroll 1") for (unsigned spins = 0; spins < (1u << 18); ++spins) {
        float a[8] = {0.f, 0.f, 0.f, 0.f, 0.f, 0.f, 0.f, 0.f};
        ARRIVED("+v"(l1[0]), "+v"(l1[1]), "+v"(l1[2]), "+v"(l1[3]), "+v"(l1[4]), "+v"(l1[5]), "+v"(l1[6]), "+v"(l1[7]));
#pragma unroll
        for (int p = 0; p < 8; ++p)
#pragma unroll
          for (int e = 0; e < 4; ++e) {
            const unsigned u = l1[p][e];
            a[2 * e] = __builtin_amdgcn_fdot2_f32_bf16(__builtin_bit_cast(bf2, u), __builtin_bit_cast(bf2, c0), a[2 * e], false);
            a[2 * e + 1] = __builtin_amdgcn_fdot2_f32_bf16(__builtin_bit_cast(bf2, u), __builtin_bit_cast(bf2, c1), a[2 * e + 1], false);
          }
#pragma unroll
        for (int i = 0; i < 4; ++i) {
          auto r = __builtin_amdgcn_permlane32_swap(__float_as_uint(a[i]), __float_as_uint(a[i + 4]), false, false);
          s4[i] = __uint_as_float(r[0]) + __uint_as_float(r[1]);
        }
        const float t = s4[0] + s4[2];
        if (__all(t == t)) break;
        unsigned mxw = 0;
#pragma unroll
        for (int p = 0; p < 8; ++p)
#pragma unroll
          for (int e = 0; e < 4; ++e) mxw = l1[p][e] > mxw ? l1[p][e] : mxw;
        if (__all(mxw != EMPTY)) break;
        l1_load();
      }
      WAITV(0);
      unsigned o0 = pk2(s4[0], s4[1]), o1 = pk2(s4[2], s4[3]);
      o0 = o0 < EMPTY - 1u ? o0 : EMPTY - 1u; o1 = o1 < EMPTY - 1u ? o1 : EMPTY - 1u;
      __builtin_amdgcn_raw_buffer_store_b64(u32x2{o0, o1}, rsW, fs, 0, 16);
    }
    ST(layer * 24 + 8)
    WAITL0;
    BAR;
    ST(layer * 24 + 9)
    if (kg) {
#pragma unroll
      for (int i = 6; i < 12; ++i) wo_late(i);
    } else {
      unsigned fo = kfo + (unsigned)(wn * 4096 + lane * 16), fz = kfo + (unsigned)(8192 + wn * 128 + g * 16);
      asm volatile("" : "+v"(fo), "+v"(fz));
      bf16x8 qp[2][2];
      phi2(acc, st01, qp);
      asm volatile(G1_OUTPUT
          :: "{v[224:227]}"(qp[0][0]), "{v[228:231]}"(qp[0][1]), "{v[232:235]}"(qp[1][0]), "{v[236:239]}"(qp[1][1]),
             "{v[240:243]}"(qp2[0][0]), "{v[244:247]}"(qp2[0][1]), "{v[248:251]}"(qp2[1][0]), "{v[252:255]}"(qp2[1][1]),
             [fo] "v"(fo), [fz] "v"(fz), [go] "v"(ogo), [lo] "v"(opo), [rs] "s"(rsW)
          : "memory", "vcc", "scc", "m0", "v152", "v153", "v154", "v155", "v156", "v157", "v158", "v159", "v160", "v161", "v162", "v163", "v164", "v165", "v166", "v167", "v168", "v169", "v170", "v171", "v172", "v173", "v174", "v175", "v176", "v177", "v178", "v179", "v180", "v181", "v182", "v183", "v184", "v185", "v186", "v187", "v188", "v189", "v190", "v191", "v192", "v193", "v194", "v195", "v196", "v197", "v198", "v199", "v200", "v201", "v202", "v203", "v204", "v205", "v206", "v207", "v208", "v209", "v210", "v211", "v212", "v213", "v214", "v215", "v216", "v217", "v218", "v219", "v220", "v221", "v222", "v223");
    }
  }
  ST(layer * 24 + 12)
  if (HH) if (w >= 4) BAR;
  if (first && w >= 4 && w < 7) {
    typedef __attribute__((ext_vector_type(2))) __bf16 bf2;
    unsigned c0 = 0x00003F80u, c1 = 0x3F800000u;
    asm volatile("" : "+v"(c0), "+v"(c1));
    const bf2 lo1 = __builtin_bit_cast(bf2, c0), hi1 = __builtin_bit_cast(bf2, c1);
    float a[8] = {0.f, 0.f, 0.f, 0.f, 0.f, 0.f, 0.f, 0.f};
#pragma unroll
    for (int i = 0; i < 8; ++i) {
      asm volatile("" : "+v"(pw[i]));
#pragma unroll
      for (int e = 0; e < 4; ++e) {
        const unsigned u = pw[i][e];
        a[2 * e] = __builtin_amdgcn_fdot2_f32_bf16(__builtin_bit_cast(bf2, u), lo1, a[2 * e], false);
        a[2 * e + 1] = __builtin_amdgcn_fdot2_f32_bf16(__builtin_bit_cast(bf2, u), hi1, a[2 * e + 1], false);
      }
    }
#pragma unroll
    for (int i = 0; i < 4; ++i) {
      auto r = __builtin_amdgcn_permlane32_swap(__float_as_uint(a[i]), __float_as_uint(a[i + 4]), false, false);
      a[i] = __uint_as_float(r[0]) + __uint_as_float(r[1]);
    }
#pragma unroll
    for (int i = 0; i < 2; ++i) {
      auto r = __builtin_amdgcn_permlane16_swap(__float_as_uint(a[i]), __float_as_uint(a[i + 2]), false, false);
      a[i] = __uint_as_float(r[0]) + __uint_as_float(r[1]);
      a[i] += dpp_ror<8>(a[i]);
    }
    if (!(lane & 8))
      __builtin_amdgcn_raw_buffer_store_b64(u32x2{__float_as_uint(a[0]), __float_as_uint(a[1])}, rsW,
          (unsigned)(WS_CSP + ((((hp + (1 << (w - 4))) & 15) * 16 + rt) * 192 + (w - 4) * 64 + (lane & 7) * 8 + (lane >> 5) * 4 + ((lane >> 4) & 1) * 2) * 4), 0, 16);
  }
  }
}

#undef pcs
#undef prs
#undef opo
#undef ogo
#undef hof
#undef dm
#undef dm2
#undef pcv
#undef pwf
#undef prv
#undef pvf
#undef ppk
#undef ppa
#undef pvr
#undef gkv
#undef gkz
#undef pwl
#undef pwm
#undef plb
#undef prd
#undef dm3

__device__ __forceinline__ void g2_phase(char* smem, char* ws, u64 E, int w, f32x16& xq, Next& nx, int layer, const u16* __restrict__ x0,
                                         const u16* __restrict__ wo, const u16* __restrict__ wnx, u16* __restrict__ out) {
  IDS
  wnx = uni(wnx);
  const unsigned sb = lds_addr(smem);
  const int lw = w & 3, k2 = 16 * lw + (lane >> 3);
  const int r0 = w * 16 + (lane >> 3);
  unsigned pxr = sb + r0 * 128 + (lane & 7) * 16;
  const unsigned rmo = (unsigned)(((m0 + r0) * DM + hp * 64 + ((lane & 7) ^ (lane >> 3)) * 8) * 2);
  const unsigned imo = (unsigned)((rt * 16 + hp) * 16384 + r0 * 128 + (lane & 7) * 16);
  constexpr int S0 = 0, S1 = 16384, S2 = 32768, S3 = 147456, S4 = OFF_WO, S5 = 98304, S6 = 114688;
  constexpr int AS[16] = {S0, S1, S2, S3, S0, S1, S4, S2, S3, S0, S1, S4, S5, S3, S0, S6};
  constexpr int OFF_C = 0;
  auto bs = [](int t) { return OFF_WO + (t < 12 ? t : t - 10) * 8192; };
  const char *xa1, *xa2, *wq2;
  u64 fm;
  unsigned vB0, vB1, vXA, po;
  if (w >= 4) {
    constexpr int WT[15] = {0, 8, 12, 14, 16, 18, 20, 18, 16, 14, 12, 12, 10, 10, 8};
    const char* o = ws + WS_O + layer * (SEQ * DM * 2) + rt * 262144;
    const unsigned vA = (unsigned)(lw * 4096 + lane * 16);
    const u64 kord = korder(bid & 3, hp);
    auto ldA = [&](int s) { glds4c(o + knib(kord, s) * 16384, vA, sb + AS[s] + lw * 4096); };
    const bool im = img(layer, hp), in = img(layer + 1, hp);
    unsigned vW0 = im ? (unsigned)(lw * 2048 + lane * 16) : (unsigned)((k2 * DM + hp * 64 + ((lane & 7) ^ ((lane >> 4) << 1)) * 8) * 2), vW1 = vW0 + (im ? 0u : 8 * DM * 2 - 1024u);
    const char *wos = wo_src(ws, wo, layer, hp), *wqs = wq_src(ws, wnx, layer + 1, hp);
    unsigned tO = im ? 8192u : 131072u;
    asm volatile("" : "+v"(vW0), "+v"(vW1));
    asm volatile("" : "+s"(wos), "+s"(tO));
    auto ldW = [&](int t) { glds2v(wos + knib(kord, t) * tO, vW0, vW1, sb + bs(t) + lw * 2048); };
    const int fB = (k2 >> 1) & 1;
    vB0 = in ? (unsigned)(lw * 6144 + lane * 16) : (unsigned)((k2 * 3072 + hp * 64 + (((((lane & 7) >> 1) ^ fB) << 1) | (lane & 1)) * 8) * 2);
    vB1 = in ? vB0 : (unsigned)(((k2 + 8) * 3072 + hp * 64 + (((((lane & 7) >> 1) ^ (fB | 2)) << 1) | (lane & 1)) * 8) * 2) - 1024u;
    const u64 kn = nx.kord = korder(kv1(bid), hp);
    nx.wq = wqs; nx.tw = in ? 24576u : 393216u;
    const char *wq0 = wqs + hp * (in ? 24576u : 393216u), *wq1 = wqs + knib(kn, 1) * (in ? 24576u : 393216u);
    wq2 = wqs + knib(kn, 2) * (in ? 24576u : 393216u);
    xa1 = ws + WS_X + layer * (SEQ * DM * 2) + knib(kn, 1) * 16384; xa2 = ws + WS_X + layer * (SEQ * DM * 2) + knib(kn, 2) * 16384;
    vXA = (unsigned)(rt * 262144 + lw * 4096 + lane * 16);
    fm = kfirst(kn);
    po = ((fm >> lane) & 1) ? WS_FLAGS + (unsigned)(256 + rt * 16 + lane) * FLS : 0u;
    asm volatile("" : "+v"(vB0), "+v"(vB1), "+v"(vXA), "+v"(po));
    asm volatile("" : "+s"(xa1), "+s"(xa2), "+s"(wq2), "+s"(fm), "+s"(nx.kord), "+s"(nx.wq), "+s"(nx.tw));
    asm volatile("" : "+s"(wq0), "+s"(wq1));
    auto ldB = [&](int s, int part) {
      const char* b = s ? wq1 : wq0;
      if (part) glds4v(b, vB0, vB1, vB0, vB1, sb + s * 40960 + 16384 + lw * 6144);
      else glds2v(b + 4096, vB0, vB1, sb + s * 40960 + 16384 + lw * 6144 + 4096);
    };
    BAR;
    ldA(1); __builtin_amdgcn_s_sleep(3); ldA(2); __builtin_amdgcn_s_sleep(3); ldA(3); __builtin_amdgcn_s_sleep(3); ldA(4);
    WAITV(12);
    BAR;
    auto step = [&](auto ktc) {
      constexpr int kt = decltype(ktc)::value;
      WAITVN(WT[kt]);
      BAR;
      if constexpr (kt == 1) { ldA(5); ldA(6); }
      if constexpr (kt >= 2 && kt <= 10) ldA(kt + 5);
      if constexpr (kt >= 2 && kt <= 5) ldW(kt + 10);
      if constexpr (kt >= 11) ldB((kt - 11) >> 1, (kt - 11) & 1);
      if constexpr (kt >= 6) { __builtin_amdgcn_s_sleep(kt >= 11 ? 3 : 2); if (layer == 1) __builtin_amdgcn_s_sleep(1); }
    };
    step(IC<1>{}); step(IC<2>{}); step(IC<3>{}); step(IC<4>{}); step(IC<5>{}); step(IC<6>{}); step(IC<7>{});
    step(IC<8>{}); step(IC<9>{}); step(IC<10>{}); step(IC<11>{}); step(IC<12>{}); step(IC<13>{}); step(IC<14>{});
  } else {
    asm volatile("" : "=v"(vB0), "=v"(vB1), "=v"(vXA), "=v"(po));
    asm volatile("" : "=s"(xa1), "=s"(xa2), "=s"(wq2), "=s"(fm), "=s"(nx.kord), "=s"(nx.wq), "=s"(nx.tw));
    const int wm = w >> 1, wn = w & 1;
    f32x4 acc[4][2];
    unsigned co[2];
#pragma unroll
    for (int cb = 0; cb < 2; ++cb) co[cb] = sb + OFF_C + (unsigned)((wm * 64 + fr) * 128 + (((wn * 4 + cb * 2 + (g >> 1)) ^ (fr & 7)) << 4) + (g & 1) * 8);
    const int fl = (q4 >> 1) | ((g & 1) << 1);
    const int bb0 = (4 * g + q4) * 128 + p4 * 8 + (((wn * 2) ^ fl) << 5), bb1 = (4 * g + q4) * 128 + p4 * 8 + (((wn * 2 + 1) ^ fl) << 5);
    const int abase = wm * 4096 + lane * 16;
    bf16x8 fa[2][2][4], fb[2][2][2];
    unsigned ra[3], rb0[2], rb1[2];
#pragma unroll
    for (int i = 0; i < 3; ++i) { ra[i] = abase + i * 65536; asm volatile("" : "+v"(ra[i])); }
#pragma unroll
    for (int i = 0; i < 2; ++i) { rb0[i] = bb0 + OFF_WO + i * 49152; rb1[i] = bb1 + OFF_WO + i * 49152; asm volatile("" : "+v"(rb0[i]), "+v"(rb1[i])); }
    auto loadf = [&](int kt, bf16x8 (&a)[2][4], bf16x8 (&b)[2][2]) {
#pragma unroll
      for (int s = 0; s < 2; ++s) {
        const char* sA = smem + ra[AS[kt] >> 16] + (AS[kt] & 65535) + s * 8192;
        const int hi = bs(kt) >= OFF_WO + 49152, ob = bs(kt) - OFF_WO - hi * 49152 + s * 4096;
        b[s][0] = cat8(tr_read(smem + rb0[hi] + ob), tr_read(smem + rb0[hi] + ob + 2048));
        b[s][1] = cat8(tr_read(smem + rb1[hi] + ob), tr_read(smem + rb1[hi] + ob + 2048));
#pragma unroll
        for (int rb = 0; rb < 4; ++rb) a[s][rb] = *(const bf16x8*)(sA + rb * 1024);
      }
    };
    auto mma = [&](bf16x8 (&a)[2][4], bf16x8 (&b)[2][2]) {
#pragma unroll
      for (int i = 0; i < 16; ++i) {
        const int rb = i >> 2, cb = ((i >> 1) ^ rb) & 1, s = ((i + 1) >> 1) & 1;
        acc[rb][cb] = MFMA(b[s][cb], a[s][rb], acc[rb][cb]);
      }
    };
    unsigned fo = lane < 16 && lane != hp ? WS_FLAGS + (unsigned)(rt * 16 + lane) * FLS : 0u;
    const u64 ko = korder(bid & 3, hp);
    unsigned kf = kfirst(ko), kf2 = kfirst(ko >> 16) | (1u << knib(ko, 9));
    unsigned jo = (unsigned)((((rt & 7) * 128 + (w - 1) * 64 + lane) * 3072 + (hp & 3) * 1024 + (hp | 3) * 64) * 2);
    unsigned fo1 = ((kf >> (lane & 31)) & 1) ? fo : 0u, fo2 = ((kf2 >> (lane & 31)) & 1) ? fo : 0u;
    fo = (((kf | kf2) >> (lane & 31)) & 1) ? 0u : fo;
    asm volatile("" : "+v"(fo), "+v"(fo1), "+v"(fo2));
    asm volatile("" : "+s"(kf), "+s"(kf2));
    publish(rsW, tid, rt * 16 + hp, E + (unsigned)(layer + 1));
    ST(layer * 24 + 13)
    u64 fl1 = 0;
    if (w == 0) asm volatile("global_load_dwordx2 %0, %1, %2 sc1" : "+v"(fl1) : "v"(fo1), "s"(ws) : "memory");
    if (layer == 0) {
      const unsigned xo = (unsigned)(((m0 + wm * 64 + fr) * DM + hp * 64 + wn * 32 + g * 8) * 2);
      asm volatile("global_load_dwordx4 v[64:67], %1, %3\n\tglobal_load_dwordx4 v[68:71], %1, %4\n\tglobal_load_dwordx4 v[72:75], %2, %3\n\tglobal_load_dwordx4 v[76:79], %2, %4"
                   : "+{v[64:79]}"(xq) : "v"(xo), "v"(xo + 32 * DM * 2), "s"(x0), "s"(x0 + 16 * DM) : "memory");
    }
    loadf(0, fa[0], fb[0]);
    unsigned junk = 0;
    if (layer < 2 && w == 3 && (bid >> 6) == 0) code_touch(junk, bid, lane);
    if (layer == 0 && (hp & 7) < 3 && (w == 1 || w == 2))
      asm volatile("global_load_dword %0, %1, %2" : "+v"(junk) : "v"(jo), "s"(wnx) : "memory");
    __builtin_amdgcn_sched_barrier(0);
    bf16x8 id[2];
#pragma unroll
    for (int cb = 0; cb < 2; ++cb) {
      const unsigned one = (g >> 1) == cb && (g & 1) == (fr >> 3) ? 0x3F80u << ((fr & 1) * 16) : 0u;
      u32x4 t;
#pragma unroll
      for (int j = 0; j < 4; ++j) t[j] = ((fr >> 1) & 3) == j ? one : 0u;
      id[cb] = __builtin_bit_cast(bf16x8, t);
      asm volatile("" : "+v"(id[cb]));
    }
    asm volatile("" : "+v"(co[0]), "+v"(co[1]));
#pragma unroll
    for (int rb = 0; rb < 4; ++rb) { acc[rb][0] = f32x4{0, 0, 0, 0}; acc[rb][1] = f32x4{0, 0, 0, 0}; }
    mma(fa[0], fb[0]);
    __builtin_amdgcn_sched_barrier(0);
    WAITL0;
    u64 miss = 0;
    if (w == 0) { ARRIVED("+v"(fl1)); if (__ballot(fl1 != E + (unsigned)(layer + 1)) & kf) poll_eq(ws, fo1, E + (unsigned)(layer + 1), kf, kf); miss = 0xffffu & ~(kf | kf2 | (1u << hp)); }
    ST(layer * 24 + 14)
    __builtin_amdgcn_sched_barrier(0);
    BAR;
    __builtin_amdgcn_sched_barrier(0);
    asm volatile("" ::: "memory");
    if (layer == 0) WAITV(0);
    __builtin_amdgcn_sched_barrier(0);
    asm volatile("" : "+{v[64:79]}"(xq));
    const bf16x8 xf[4] = {__builtin_bit_cast(bf16x8, quad<0>(xq)), __builtin_bit_cast(bf16x8, quad<1>(xq)), __builtin_bit_cast(bf16x8, quad<2>(xq)), __builtin_bit_cast(bf16x8, quad<3>(xq))};
#pragma unroll
    for (int rb = 0; rb < 4; ++rb) { acc[rb][0] = MFMA(id[0], xf[rb], acc[rb][0]); acc[rb][1] = MFMA(id[1], xf[rb], acc[rb][1]); }
    if (w == 0) poll_eq(ws, fo2, E + (unsigned)(layer + 1), kf2, kf2);
    u64 fl2 = 0;
    auto step = [&](auto ktc) {
      constexpr int kt = decltype(ktc)::value;
      __builtin_amdgcn_sched_barrier(0);
      if (kt == 5 && miss) {
        ARRIVED("+v"(fl2));
        if (__ballot(fl2 != E + (unsigned)(layer + 1)) & miss) poll_eq(ws, fo, E + (unsigned)(layer + 1), miss, ~0ull);
      }
      WAITL0;
      BAR;
      if (kt == 2 && miss) asm volatile("global_load_dwordx2 %0, %1, %2 sc1" : "+v"(fl2) : "v"(fo), "s"(ws) : "memory");
      if (kt == 0) { ST(layer * 24 + 15) }
      __builtin_amdgcn_sched_barrier(0);
      loadf(kt + 1, fa[(kt + 1) & 1], fb[(kt + 1) & 1]);
      if (kt == 0) return;
      mma(fa[kt & 1], fb[kt & 1]);
#pragma unroll
      for (int i = 0; i < 4; ++i) {
        __builtin_amdgcn_sched_group_barrier(0x008, 1, 0);
        __builtin_amdgcn_sched_group_barrier(0x100, 1, 0);
        __builtin_amdgcn_sched_group_barrier(0x008, 1, 0);
        __builtin_amdgcn_sched_group_barrier(0x100, 3, 0);
      }
#pragma unroll
      for (int i = 0; i < 8; ++i) __builtin_amdgcn_sched_group_barrier(0x008, 1, 0);
    };
    step(IC<0>{}); step(IC<1>{}); step(IC<2>{}); step(IC<3>{}); step(IC<4>{}); step(IC<5>{}); step(IC<6>{}); step(IC<7>{});
    step(IC<8>{}); step(IC<9>{}); step(IC<10>{}); step(IC<11>{}); step(IC<12>{}); step(IC<13>{}); step(IC<14>{});
    WAITV(0);
    asm volatile("" :: "v"(junk));
    __builtin_amdgcn_sched_barrier(0);
    auto put = [&](int rb) {
#pragma unroll
      for (int cb = 0; cb < 2; ++cb) lds<u32x2>(co[cb] + rb * 2048) = u32x2{pk2(acc[rb][cb][0], acc[rb][cb][1]), pk2(acc[rb][cb][2], acc[rb][cb][3])};
    };
#pragma unroll
    for (int rb = 0; rb < 4; ++rb)
#pragma unroll
      for (int cb = 0; cb < 2; ++cb) acc[rb][cb] = MFMA(fb[1][0][cb], fa[1][0][rb], acc[rb][cb]);
#pragma unroll
    for (int rb = 0; rb < 4; ++rb) {
      __builtin_amdgcn_sched_barrier(0);
#pragma unroll
      for (int cb = 0; cb < 2; ++cb) acc[rb][cb] = MFMA(fb[1][1][cb], fa[1][1][rb], acc[rb][cb]);
      __builtin_amdgcn_sched_barrier(0);
      if (rb >= 2) put(rb - 2);
    }
    __builtin_amdgcn_sched_barrier(0);
    ST(layer * 24 + 16)
    put(2); put(3);
  }
  WAITL0; BAR;
  asm volatile("" ::: "memory");
  u32x4 xn[2];
#pragma unroll
  for (int i = 0; i < 2; ++i) xn[i] = lds<u32x4>(pxr + OFF_C + i * 1024);
  if (layer == 3) {
    const __amdgpu_buffer_rsrc_t rsO = __builtin_amdgcn_make_buffer_rsrc((void*)uni(out), (short)0, SEQ * DM * 2, 0x00020000);
    __builtin_amdgcn_raw_buffer_store_b128(xn[0], rsO, rmo, 0, 16);
    __builtin_amdgcn_raw_buffer_store_b128(xn[1], rsO, rmo + 8 * DM * 2, 0, 16);
  } else {
    __builtin_amdgcn_raw_buffer_store_b128(xn[0], rsW, WS_X + layer * (SEQ * DM * 2) + imo, 0, 16);
    __builtin_amdgcn_raw_buffer_store_b128(xn[1], rsW, WS_X + layer * (SEQ * DM * 2) + imo + 1024, 0, 16);
    float mean[2], m2[2];
#pragma unroll
    for (int i = 0; i < 2; ++i) {
      typedef __attribute__((ext_vector_type(2))) __bf16 bf2;
      const bf2 one2 = __builtin_bit_cast(bf2, 0x3F803F80u);
      float sa = 0.f, sq = 0.f;
#pragma unroll
      for (int e = 0; e < 4; ++e) {
        const unsigned u = xn[i][e];
        const bf2 t = __builtin_bit_cast(bf2, u);
        sa = __builtin_amdgcn_fdot2_f32_bf16(t, one2, sa, false);
        sq = __builtin_amdgcn_fdot2_f32_bf16(t, t, sq, false);
      }
      mean[i] = sa;
      m2[i] = sq;
    }
#define DPA(c) "v_add_f32_dpp %0, %0, %0 " c "\n\tv_add_f32_dpp %1, %1, %1 " c "\n\tv_add_f32_dpp %2, %2, %2 " c "\n\tv_add_f32_dpp %3, %3, %3 " c "\n\t"
    asm volatile("s_nop 3\n\t" DPA("quad_perm:[1,0,3,2] row_mask:0xf bank_mask:0xf") DPA("quad_perm:[2,3,0,1] row_mask:0xf bank_mask:0xf")
                 DPA("row_half_mirror row_mask:0xf bank_mask:0xf") "s_nop 0" : "+v"(mean[0]), "+v"(m2[0]), "+v"(mean[1]), "+v"(m2[1]));
    if ((lane & 7) < 2) {
      const int i = lane & 1;
      const u32x2 sv = {__float_as_uint(i ? mean[1] : mean[0]), __float_as_uint(i ? m2[1] : m2[0])};
      __builtin_amdgcn_raw_buffer_store_b64(sv, rsW, WS_ST + layer * (SEQ * 128) + (unsigned)(((rt * 16 + hp) * 128 + r0 + i * 8) * 8), 0, 16);
    }
  }
  ST(layer * 24 + 17)
  if (layer < 3 && w >= 4) {
    WAITV(0); BAR;
    u64 fl = 0;
    if (lw == 0) { __builtin_amdgcn_s_sleep(7); asm volatile("global_load_dwordx2 %0, %1, %2 sc1" : "+v"(fl) : "v"(po), "s"(ws) : "memory"); }
    glds4v(wq2, vB0, vB1, vB0, vB1, sb + 2 * 40960 + 16384 + lw * 6144);
    glds2v(wq2 + 4096, vB0, vB1, sb + 2 * 40960 + 16384 + lw * 6144 + 4096);
    if (lw == 0) {
      WAITV(6);
      __builtin_amdgcn_sched_barrier(0);
      asm volatile("" : "+v"(fl));
      if (__ballot(fl != E + (unsigned)(layer + 1)) & fm) poll_eq(ws, po, E + (unsigned)(layer + 1), fm, ~0ull);
    }
    BAR;
    glds4c(xa1, vXA, sb + 40960 + lw * 4096); glds4c(xa2, vXA, sb + 2 * 40960 + lw * 4096);
  }
  ST(layer * 24 + 18)
}

__global__ __launch_bounds__(512, 2) void mega_kernel(const u16* __restrict__ x0, const u16* __restrict__ wqkvA,
                                                      const u16* __restrict__ woA, const u16* __restrict__ wqkvB,
                                                      const u16* __restrict__ wphi, const u16* __restrict__ woB,
                                                      u16* __restrict__ out, char* __restrict__ ws) {
  extern __shared__ __attribute__((aligned(16))) char smem[];
  const int w = __builtin_amdgcn_readfirstlane(threadIdx.x >> 6);
  u64 E = 0;
  {
    const int bid = blockIdx.x, lane = threadIdx.x & 63;
    const int rt = 2 * (bid & 4) + ((bid >> 3) & 7), hp = 4 * (bid & 3) + (bid >> 6), m0 = rt * 128;
    if (w < 4) {
      if (w == 3) {
        unsigned long long pc;
        unsigned len;
        asm volatile("s_getpc_b64 %0\n.Lmk_pc:\n\ts_mov_b32 %1, .Lmk_end-.Lmk_pc" : "=s"(pc), "=s"(len));
        const unsigned per = ((len >> 10) + 1) * 128, off = ((bid >> 3) & 7) * per + (lane * 128 < per ? lane * 128 : 0);
        asm volatile("s_mov_b32 m0, %2\n\ts_nop 0\n\tglobal_load_lds_dword %0, %1" :: "v"(off < len - 4 ? off : 0u), "s"(pc), "s"(lds_addr(smem) + LDS_TOTAL - 1024) : "memory", "m0");
      }
      const unsigned xo = (unsigned)((m0 + hp * 8 + w * 2) * (DM * 2) + lane * 16);
      glds4v(x0, xo, xo, xo, xo, lds_addr(smem) + 122880 + w * 4096);
    } else {
      const int lw = w & 3;
      const int kB = 16 * lw + (lane >> 3), fB = (kB >> 1) & 1;
      const unsigned vB0 = (unsigned)((kB * 3072 + hp * 64 + (((((lane & 7) >> 1) ^ fB) << 1) | (lane & 1)) * 8) * 2);
      const unsigned vB1 = (unsigned)(((kB + 8) * 3072 + hp * 64 + (((((lane & 7) >> 1) ^ (fB | 2)) << 1) | (lane & 1)) * 8) * 2) - 1024u;
      const unsigned vA = (unsigned)(((m0 + lw * 32 + (lane >> 3)) * DM + ((lane & 7) ^ (lane >> 3)) * 8) * 2);
#pragma unroll
      for (int s = 0; s < 1; ++s) {
        const int kt = (rt & 8) + s;
        const unsigned sA = lds_addr(smem) + s * 40960, sB = sA + 16384;
        glds4v(x0 + kt * 64, vA, vA + 15360u, vA + 2 * 15360u, vA + 3 * 15360u, sA + lw * 4096);
        const u16* b = wqkvA + (size_t)kt * 64 * 3072;
        glds4v(b, vB0, vB1, vB0, vB1, sB + lw * 6144);
        glds2v(b + 2048, vB0, vB1, sB + lw * 6144 + 4096);
      }
      asm volatile("s_load_dwordx2 %0, %1, 0x0\n\ts_waitcnt lgkmcnt(0)" : "=s"(E) : "s"(ws) : "memory");
    }
  }
  f32x16 xq;
  Next nx;
  asm volatile("" : "=s"(nx.kord), "=s"(nx.wq), "=s"(nx.tw));
#pragma unroll 1
  for (int layer = 0; layer < 4; ++layer) {
    if (layer & 1) g1_phase<1>(smem, ws, E, w, xq, nx, layer, x0, wqkvB, woB, wphi, wqkvA, woA);
    else g1_phase<0>(smem, ws, E, w, xq, nx, layer, x0, wqkvA, woA, nullptr, wqkvB, woB);
    g2_phase(smem, ws, E, w, xq, nx, layer, x0, (layer & 1) ? woB : woA, (layer & 1) ? wqkvA : wqkvB, out);
  }
  asm volatile(".Lmk_end:");
  WAITV(0);
  if (blockIdx.x == 0 && w == 0 && lane_id() == 0) __hip_atomic_store((gu64*)ws, E + 16u, __ATOMIC_RELAXED, __HIP_MEMORY_SCOPE_AGENT);
}

extern "C" void kernel_launch(void* const* d_in, const int* in_sizes, int n_in,
                              void* d_out, int out_size, void* d_ws, size_t ws_size,
                              hipStream_t stream) {
  hipFuncSetAttribute((const void*)mega_kernel, hipFuncAttributeMaxDynamicSharedMemorySize, LDS_TOTAL);
  mega_kernel<<<256, 512, LDS_TOTAL, stream>>>((const u16*)d_in[0], (const u16*)d_in[1], (const u16*)d_in[2], (const u16*)d_in[4],
                                               (const u16*)d_in[5], (const u16*)d_in[6], (u16*)d_out, (char*)d_ws);
}
```

```cpp
#include <hip/hip_runtime.h>
#include <hip/hip_bf16.h>
#include <stdint.h>
#include <type_traits>
#pragma clang diagnostic ignored "-Winline-asm"
#pragma clang diagnostic ignored "-Wunused-value"

typedef unsigned short u16;
typedef __attribute__((ext_vector_type(8))) short bf16x8;
typedef __attribute__((ext_vector_type(4))) short bf16x4;
typedef __attribute__((ext_vector_type(4))) float f32x4;
typedef __attribute__((ext_vector_type(4))) unsigned u32x4;
typedef __attribute__((ext_vector_type(2))) unsigned u32x2;
typedef __attribute__((ext_vector_type(8))) float f32x8;
typedef __attribute__((ext_vector_type(16))) unsigned u32x16;
typedef __attribute__((ext_vector_type(16))) float f32x16;
typedef __attribute__((ext_vector_type(32))) float f32x32;
template <int I, typename T> __device__ __forceinline__ f32x4 quad(const T& v) { return __builtin_shufflevector(v, v, 4 * I, 4 * I + 1, 4 * I + 2, 4 * I + 3); }

constexpr int SEQ = 2048, DM = 1024, NH = 32;
#define ST(i)
constexpr int KVP = 64 * 32 + 64;

typedef __attribute__((ext_vector_type(2))) float f32x2;
typedef __attribute__((ext_vector_type(2))) __bf16 bf16x2_t;
__device__ __forceinline__ unsigned pk2(float a, float b) {
  f32x2 v = {a, b};
  bf16x2_t r = __builtin_convertvector(v, bf16x2_t);
  return __builtin_bit_cast(unsigned, r);
}

__device__ __forceinline__ bf16x4 tr_read(const char* p) {
  return __builtin_amdgcn_ds_read_tr16_b64_v4i16((__attribute__((address_space(3))) bf16x4*)p);
}
template <typename T> __device__ __forceinline__ __attribute__((address_space(3))) T& lds(unsigned a) { return *(__attribute__((address_space(3))) T*)(size_t)a; }
__device__ __forceinline__ bf16x4 tr_read(unsigned a) { return __builtin_amdgcn_ds_read_tr16_b64_v4i16(&lds<bf16x4>(a)); }
__device__ __forceinline__ bf16x8 cat8(bf16x4 a, bf16x4 b) {
  bf16x8 f = {a[0], a[1], a[2], a[3], b[0], b[1], b[2], b[3]};
  return f;
}
__device__ __forceinline__ void glds16s(const void* sbase, unsigned voff, unsigned lds_base) {
  asm volatile("s_mov_b32 m0, %2\n\ts_nop 0\n\tglobal_load_lds_dwordx4 %0, %1" :: "v"(voff), "s"(sbase), "s"(lds_base) : "memory", "m0");
}
__device__ __forceinline__ void glds16c(const void* sbase, unsigned voff, unsigned lds_base) {
  asm volatile("s_mov_b32 m0, %2\n\ts_nop 0\n\tglobal_load_lds_dwordx4 %0, %1 sc1" :: "v"(voff), "s"(sbase), "s"(lds_base) : "memory", "m0");
}
#define GL_ "\n\tglobal_load_lds_dwordx4 "
__device__ __forceinline__ void glds4c(const void* sbase, unsigned v, unsigned lds_base) {
  asm volatile("s_mov_b32 m0, %2\n\ts_nop 0" GL_ "%0, %1 sc1" GL_ "%0, %1 offset:1024 sc1" GL_ "%0, %1 offset:2048 sc1" GL_ "%0, %1 offset:3072 sc1"
               :: "v"(v), "s"(sbase), "s"(lds_base) : "memory", "m0");
}
__device__ __forceinline__ void glds4v(const void* sbase, unsigned v0, unsigned v1, unsigned v2, unsigned v3, unsigned lds_base) {
  asm volatile("s_mov_b32 m0, %5\n\ts_nop 0" GL_ "%0, %4 sc1" GL_ "%1, %4 offset:1024 sc1" GL_ "%2, %4 offset:2048 sc1" GL_ "%3, %4 offset:3072 sc1"
               :: "v"(v0), "v"(v1), "v"(v2), "v"(v3), "s"(sbase), "s"(lds_base) : "memory", "m0");
}
__device__ __forceinline__ void glds2v(const void* sbase, unsigned v0, unsigned v1, unsigned lds_base) {
  asm volatile("s_mov_b32 m0, %3\n\ts_nop 0" GL_ "%0, %2 sc1" GL_ "%1, %2 offset:1024 sc1" :: "v"(v0), "v"(v1), "s"(sbase), "s"(lds_base) : "memory", "m0");
}
__device__ __forceinline__ void gld16(u32x4& d, unsigned voff, const void* sbase) {
  asm volatile("global_load_dwordx4 %0, %1, %2" : "=v"(d) : "v"(voff), "s"(sbase) : "memory");
}
__device__ __forceinline__ unsigned lds_addr(const void* p) { return (unsigned)(size_t)p; }
#define WAITV(n) asm volatile("s_waitcnt vmcnt(" #n ")" ::: "memory")
#define WAITVN(n) asm volatile("s_waitcnt vmcnt(%0)" :: "n"(n) : "memory")
#define WAITL0 do { asm volatile("" ::: "memory"); __builtin_amdgcn_s_waitcnt(0xC07F); asm volatile("" ::: "memory"); } while (0)
#define BAR __builtin_amdgcn_s_barrier()
#define ARRIVED(...) do { WAITV(0); __builtin_amdgcn_sched_barrier(0); asm volatile("" : __VA_ARGS__); } while (0)

template <int N> __device__ __forceinline__ float dpp_ror(float x) {
  return __uint_as_float(__builtin_amdgcn_update_dpp(0u, __float_as_uint(x), 0x120 + N, 0xf, 0xf, false));
}
__device__ __forceinline__ float r16_sum(float x) { x += dpp_ror<8>(x); x += dpp_ror<4>(x); x += dpp_ror<2>(x); return x + dpp_ror<1>(x); }
__device__ __forceinline__ void max8pn(const f32x4& a, const f32x4& b, float& mx, float& mn) {
  asm("v_max3_f32 %0, %2, %3, %4\n\tv_max3_f32 %1, -%2, -%3, -%4\n\tv_max3_f32 %0, %0, %5, %6\n\tv_max3_f32 %1, %1, -%5, -%6\n\t"
      "v_max3_f32 %0, %0, %7, %8\n\tv_max3_f32 %1, %1, -%7, -%8\n\tv_max_f32 %0, %0, %9\n\tv_max_f32 %1, %1, -%9\n\ts_nop 0"
      : "=&v"(mx), "=&v"(mn) : "v"(a[0]), "v"(a[1]), "v"(a[2]), "v"(a[3]), "v"(b[0]), "v"(b[1]), "v"(b[2]), "v"(b[3]));
}
__device__ __forceinline__ float vmaxf_n(float a, float b) { float r; asm("v_max_f32 %0, %1, %2\n\ts_nop 0" : "=v"(r) : "v"(a), "v"(b)); return r; }
template <bool MAXOP> __device__ __forceinline__ float rop(float a, float b) { return MAXOP ? vmaxf_n(a, b) : a + b; }
template <bool MAXOP> __device__ __forceinline__ void xr4(float& a, float& b, float& c, float& d) {
  auto r = __builtin_amdgcn_permlane16_swap(__float_as_uint(a), __float_as_uint(b), false, false);
  auto q = __builtin_amdgcn_permlane16_swap(__float_as_uint(c), __float_as_uint(d), false, false);
  const unsigned ab = __float_as_uint(rop<MAXOP>(__uint_as_float(r[0]), __uint_as_float(r[1])));
  const unsigned cd = __float_as_uint(rop<MAXOP>(__uint_as_float(q[0]), __uint_as_float(q[1])));
  auto t = __builtin_amdgcn_permlane32_swap(ab, cd, false, false);
  const unsigned u = __float_as_uint(rop<MAXOP>(__uint_as_float(t[0]), __uint_as_float(t[1])));
  auto h = __builtin_amdgcn_permlane32_swap(u, u, false, false);
  auto p0 = __builtin_amdgcn_permlane16_swap(h[0], h[0], false, false);
  auto p1 = __builtin_amdgcn_permlane16_swap(h[1], h[1], false, false);
  a = __uint_as_float(p0[0]); b = __uint_as_float(p0[1]); c = __uint_as_float(p1[0]); d = __uint_as_float(p1[1]);
}

__device__ __forceinline__ unsigned xs4(float a, float b, float c, float d) {
  auto r = __builtin_amdgcn_permlane16_swap(__float_as_uint(a), __float_as_uint(b), false, false);
  auto q = __builtin_amdgcn_permlane16_swap(__float_as_uint(c), __float_as_uint(d), false, false);
  auto t = __builtin_amdgcn_permlane32_swap(__float_as_uint(__uint_as_float(r[0]) + __uint_as_float(r[1])), __float_as_uint(__uint_as_float(q[0]) + __uint_as_float(q[1])), false, false);
  return __float_as_uint(__uint_as_float(t[0]) + __uint_as_float(t[1]));
}

__device__ __forceinline__ f32x4 sum4(const u32x4 (&v)[4]) {
  float r0, r1, r2, r3, t0, t1, t2, t3;
  asm("v_add_f32 %0, %8, %12\n\tv_add_f32 %1, %9, %13\n\tv_add_f32 %2, %10, %14\n\tv_add_f32 %3, %11, %15\n\t"
      "v_add_f32 %4, %16, %20\n\tv_add_f32 %5, %17, %21\n\tv_add_f32 %6, %18, %22\n\tv_add_f32 %7, %19, %23\n\t"
      "v_add_f32 %0, %0, %4\n\tv_add_f32 %1, %1, %5\n\tv_add_f32 %2, %2, %6\n\tv_add_f32 %3, %3, %7\n\ts_nop 0"
      : "=&v"(r0), "=&v"(r1), "=&v"(r2), "=&v"(r3), "=&v"(t0), "=&v"(t1), "=&v"(t2), "=&v"(t3)
      : "v"(v[0][0]), "v"(v[0][1]), "v"(v[0][2]), "v"(v[0][3]), "v"(v[1][0]), "v"(v[1][1]), "v"(v[1][2]), "v"(v[1][3]),
        "v"(v[2][0]), "v"(v[2][1]), "v"(v[2][2]), "v"(v[2][3]), "v"(v[3][0]), "v"(v[3][1]), "v"(v[3][2]), "v"(v[3][3]));
  return f32x4{r0, r1, r2, r3};
}
#define MFMA(a, b, c) __builtin_amdgcn_mfma_f32_16x16x32_bf16(a, b, c, 0, 0, 0)

typedef unsigned long long u64;
typedef __attribute__((address_space(1))) unsigned long long gu64;
constexpr unsigned WS_FLAGS = 4096;
constexpr int FLS = 64;
constexpr unsigned WS_G0 = 65536;
constexpr unsigned WS_CSP = WS_G0 + SEQ * 16;
constexpr unsigned WS_ST = WS_CSP + 16 * 16 * 768;
constexpr unsigned WS_X = 1u << 21;
constexpr unsigned WS_O = WS_X + 3u * SEQ * DM * 2;
constexpr unsigned KVPB = KVP * 2, KVFB = KVP * 4;
constexpr unsigned EMPTY = 0xffffffffu;
constexpr unsigned WS_KV = WS_O + 4u * SEQ * DM * 2;
constexpr unsigned KVLB = 16u * NH * KVPB + 16u * KVFB;
constexpr unsigned WS_HL = WS_KV + 2u * KVLB;
constexpr unsigned WS_CSA = WS_HL + 512u * 4096;
constexpr unsigned WS_CPA = WS_CSA + 256u * 1024;
constexpr unsigned WS_IMG = WS_CPA + 16 * 16 * 768;
constexpr unsigned WS_END = WS_IMG + 2048 * 1024;
constexpr int LDS_TOTAL = 163840;
constexpr int OFF_WO = 49152;
struct Next { u64 kord; const char* wq; unsigned tw; };
template <int V> using IC = std::integral_constant<int, V>;
__device__ __forceinline__ bf16x8 ones8() { unsigned e = 0x3F803F80u; asm volatile("" : "+v"(e)); return __builtin_bit_cast(bf16x8, u32x4{e, e, e, e}); }
__device__ __forceinline__ u32x4 empty4() { unsigned e = EMPTY; asm volatile("" : "+v"(e)); return u32x4{e, e, e, e}; }
__device__ __forceinline__ u64 korder(int v, int own) {
  const u64 t = v == 0 ? 0x3b210fedca987654ull : v == 1 ? 0xb37654210fedca98ull : v == 2 ? 0x3ba987654210fedcull : 0xb3fedca987654210ull;
  const u64 inv = v == 0 ? 0xa987e6543210fdcbull : v == 1 ? 0x6543f210dcbae987ull : v == 2 ? 0x3210edcba987f654ull : 0xdcbaf9876543e210ull;
  const u64 low = (1ull << (4 * ((inv >> (4 * own)) & 15))) - 1ull;
  return (u64)own | ((t & low) << 4) | (t & ~((low << 4) | 15ull));
}
__device__ __forceinline__ u64 korder0(int rt) {
  const int r = rt & 7;
  const u64 own = (0x765432765432ull >> (4 * (r < 6 ? r : r - 6))) & 0xffffffull, oth = 0x7654321076543210ull >> (4 * r);
  const u64 t = 0x10ull | ((oth & 15ull) << 8) | (own << 12) | ((oth >> 4) << 36);
  return t | (rt < 8 ? 0x8888888000000800ull : 0x0000000888888088ull);
}
__device__ __forceinline__ int knib(u64 t, int s) { return (int)(t >> (4 * s)) & 15; }
__device__ __forceinline__ unsigned kfirst(u64 t) { return (1u << knib(t, 1)) | (1u << knib(t, 2)) | (1u << knib(t, 3)) | (1u << knib(t, 4)); }
__device__ __forceinline__ int kv1(int bid) { return (bid + (0x4924 >> ((bid >> 2) & 14))) & 3; }

__device__ __forceinline__ bool img(int layer, int hp) { return layer >= 2 && (hp & 7) == 3; }
__device__ __forceinline__ const char* wq_src(const char* ws, const void* W, int layer, int hp) { return img(layer, hp) ? ws + WS_IMG + ((layer & 1) * 768 + (hp >> 3) * 384) * 1024 : (const char*)W; }
__device__ __forceinline__ const char* wo_src(const char* ws, const void* W, int layer, int hp) { return img(layer, hp) ? ws + WS_IMG + (1536 + (layer & 1) * 256 + (hp >> 3) * 128) * 1024 : (const char*)W; }

template <typename T> __device__ __forceinline__ T* uni(T* p) {
  const unsigned long long v = (unsigned long long)p;
  const unsigned lo = __builtin_amdgcn_readfirstlane((unsigned)v), hi = __builtin_amdgcn_readfirstlane((unsigned)(v >> 32));
  return (T*)(((unsigned long long)hi << 32) | lo);
}
__device__ __forceinline__ int lane_id() {
  int l;
  asm volatile("v_mbcnt_lo_u32_b32 %0, -1, 0\n\tv_mbcnt_hi_u32_b32 %0, -1, %0" : "=v"(l));
  __builtin_assume(l >= 0 && l < 64);
  return l;
}
#define IDS \
  const int lane = lane_id(); \
  asm volatile("" : "+s"(w)); \
  const int tid = w * 64 + lane; \
  int bid = blockIdx.x; asm volatile("" : "+s"(bid)); \
  ws = uni(ws); asm volatile("" : "+s"(ws)); \
  layer = __builtin_amdgcn_readfirstlane(layer); x0 = uni(x0); wo = uni(wo); \
    \
  const int rt = 2 * (bid & 4) + ((bid >> 3) & 7); \
  const int hp = 4 * (bid & 3) + (bid >> 6); \
  const int m0 = rt * 128; \
  const int fr = lane & 15, g = lane >> 4; \
  const int q4 = (lane & 15) >> 2, p4 = lane & 3; \
  const __amdgpu_buffer_rsrc_t rsW = __builtin_amdgcn_make_buffer_rsrc((void*)ws, (short)0, (int)WS_END, 0x00020000); \
  (void)m0; (void)fr; (void)q4; (void)p4; (void)rsW; (void)g;

__device__ __forceinline__ void code_touch(unsigned& junk, int bid, int lane) {
  unsigned long long pc;
  unsigned len, d;
  asm volatile("s_getpc_b64 %0\n.Lct%=:\n\ts_mov_b32 %2, .Lct%=-.Lmk_pc\n\ts_mov_b32 %1, .Lmk_end-.Lmk_pc" : "=s"(pc), "=s"(len), "=s"(d));
  pc -= d;
  const unsigned per = ((len >> 10) + 1) * 128, off = ((bid >> 3) & 7) * per + (lane * 128 < per ? lane * 128 : 0);
  asm volatile("global_load_dword %0, %1, %2" : "+v"(junk) : "v"(off < len - 4 ? off : 0u), "s"(pc) : "memory");
}

__device__ __forceinline__ void publish(__amdgpu_buffer_rsrc_t rsW, int tid, unsigned idx, u64 v) {
  unsigned off = WS_FLAGS + idx * FLS;
  asm volatile("" : "+v"(off), "+v"(v));
  WAITV(0);
  WAITL0; BAR;
  if (tid == 192) __builtin_amdgcn_raw_buffer_store_b64(__builtin_bit_cast(u32x2, v), rsW, off, 0, 16);
  asm volatile("" ::: "memory");
}
__device__ __forceinline__ u64 poll_flags(char* ws, u64 E, int set, int rt, int hp, int lane, unsigned n, u64 mask, u64 now = ~0ull) {
  u64 miss = 0;
  const int l = lane & 15, g = lane >> 4;
  const int fi = set * 256 + (g == 2 ? l * 16 + hp : rt * 16 + l);
  const unsigned need = ((mask >> lane) & 1) ? n : 0u;
  gu64* fp = (gu64*)(ws + (need ? WS_FLAGS + fi * FLS : 0));
  _Pragma("unroll 1") for (unsigned spins = 0; spins < (1u << 18); ++spins) {
    const u64 d = __hip_atomic_load(fp, __ATOMIC_RELAXED, __HIP_MEMORY_SCOPE_AGENT) - E;
    const bool ok = (need == 0u) || (d >= (u64)need && d <= 16ull);
    miss = __ballot(!ok);
    if (!(miss & now)) break;
    __builtin_amdgcn_s_sleep(1);
  }
  asm volatile("s_waitcnt vmcnt(0)" ::: "memory");
  return miss;
}

__device__ __forceinline__ u64 poll_eq(const char* ws, unsigned off, u64 want, u64 mask, u64 now) {
  u64 miss;
  _Pragma("unroll 1") for (unsigned spins = 0; spins < (1u << 18); ++spins) {
    u64 d;
    asm volatile("global_load_dwordx2 %0, %1, %2 sc1\n\ts_waitcnt vmcnt(0)" : "=v"(d) : "v"(off), "s"(ws) : "memory");
    miss = __ballot(d != want) & mask;
    if (!(miss & now)) break;
    __builtin_amdgcn_s_sleep(1);
  }
  return miss;
}

#define G1_MACROS \
  ".macro G1M rb, j, s, z\n" \
  " .set g1d, 160+24*(\\rb)+4*(((\\j)+2)-6*((\\j)>>2))\n .set g1a, 80+40*(\\s)+4*(\\rb)\n .set g1b, 96+40*(\\s)+4*(\\j)\n" \
  " .if ((\\j)>>1) == 1\n  .set g1t, g1a\n  .set g1a, g1b\n  .set g1b, g1t\n .endif\n" \
  " .if \\z\n  v_mfma_f32_16x16x32_bf16 v[g1d:g1d+3], v[g1b:g1b+3], v[g1a:g1a+3], 0\n" \
  " .else\n  v_mfma_f32_16x16x32_bf16 v[g1d:g1d+3], v[g1b:g1b+3], v[g1a:g1a+3], v[g1d:g1d+3]\n .endif\n" \
  ".endm\n" \
  ".macro G1RA rb, s, ra, oa\n" \
  " ds_read_b128 v[80+40*(\\s)+4*(\\rb):83+40*(\\s)+4*(\\rb)], \\ra offset:(\\oa)+(\\rb)*2048\n" \
  ".endm\n" \
  ".macro G1RB k, s, r0, r1, ob\n" \
  " .if (\\k) & 2\n  ds_read_b64_tr_b16 v[96+40*(\\s)+2*(\\k):97+40*(\\s)+2*(\\k)], \\r1 offset:(\\ob)+(((\\k)>>2)+1-3*((\\k)>>3))*2048+((\\k)&1)*512\n" \
  " .else\n  ds_read_b64_tr_b16 v[96+40*(\\s)+2*(\\k):97+40*(\\s)+2*(\\k)], \\r0 offset:(\\ob)+(((\\k)>>2)+1-3*((\\k)>>3))*2048+((\\k)&1)*512\n .endif\n" \
  ".endm\n" \
    \
  ".macro G1BK i, g, r0, r1, ob\n G1RB 4*(\\g)+((\\i)&3), (\\i)>>2, \\r0, \\r1, (\\ob)+((\\i)>>2)*12288\n.endm\n" \
  ".macro G1AK rb, s, a0, a1, oa\n .if \\s\n  G1RA \\rb, 1, \\a1, \\oa\n .else\n  G1RA \\rb, 0, \\a0, \\oa\n .endif\n.endm\n" \
  ".macro G1RDB g, r0, r1, ob\n .set g1i, 0\n .rept 8\n  G1BK g1i, \\g, \\r0, \\r1, \\ob\n  .set g1i, g1i+1\n .endr\n.endm\n" \
  ".macro G1RDA a0, a1, oa\n .set g1i, 0\n .rept 8\n  G1AK (g1i>>1)^2, g1i&1, \\a0, \\a1, \\oa\n  .set g1i, g1i+1\n .endr\n.endm\n" \
    \
  ".macro G1Q g, rb, q, i, z\n G1M \\rb, 2*(\\g)+((((\\i)>>1)^(\\q))&1), (((\\i)+1)>>1)&1, (\\z)&&(((\\i)&1)==0)\n.endm\n" \
    \
  ".macro G1SG g, z, rd, a0, a1, r0, r1, o\n" \
  " .set g1i, 0\n .rept 16\n" \
  "  .if ((\\g) == 0) && (g1i == 8) && (\\rd)\n   s_waitcnt lgkmcnt(7)\n   s_nop 0\n  .endif\n" \
  "  .if ((\\g) == 0) && (g1i == 15)\n   s_waitcnt lgkmcnt(0)\n  .endif\n" \
  "  G1Q \\g, (g1i>>2)^(2-((\\g)&1)), g1i>>2, g1i&3, \\z\n" \
  "  .if \\rd\n" \
  "   .if (\\g) == 0\n    .if g1i < 7\n     G1BK g1i, 2, \\r0, \\r1, \\o\n    .elseif g1i == 8\n     G1BK 7, 2, \\r0, \\r1, \\o\n    .endif\n" \
  "   .elseif (\\g) == 1\n    .if g1i < 8\n     G1BK g1i, 0, \\r0, \\r1, \\o\n    .endif\n" \
  "   .elseif (g1i & 2) == 0\n    G1BK (g1i&1)+2*(g1i>>2), 1, \\r0, \\r1, \\o\n" \
  "   .elseif (\\rd) == 1\n    G1AK (g1i>>2)^2, 1-(g1i&1), \\a0, \\a1, \\o\n   .endif\n" \
  "  .endif\n" \
  "  .set g1i, g1i+1\n .endr\n" \
  ".endm\n" \
  ""
#define G1_SLIDE \
  "s_cmp_eq_u32 %[wm], 0\n v_pk_fma_f32 v[240:241], v[96:97], v[138:139], v[240:241] op_sel_hi:[1,0,1]\n " \
  "v_pk_fma_f32 v[242:243], v[98:99], v[138:139], v[242:243] op_sel_hi:[1,0,1]\n " \
  "v_pk_fma_f32 v[244:245], v[100:101], v[138:139], v[244:245] op_sel_hi:[1,0,1]\n " \
  "v_pk_fma_f32 v[246:247], v[102:103], v[138:139], v[246:247] op_sel_hi:[1,0,1]\n v_pk_mul_f32 v[240:241], v[138:139], v[240:241] op_sel:[1,0]\n " \
  "v_pk_mul_f32 v[242:243], v[138:139], v[242:243] op_sel:[1,0]\n v_pk_mul_f32 v[244:245], v[138:139], v[244:245] op_sel:[1,0]\n " \
  "v_pk_mul_f32 v[246:247], v[138:139], v[246:247] op_sel:[1,0]\n v_pk_fma_f32 v[248:249], v[104:105], v[144:145], v[248:249] op_sel_hi:[1,0,1]\n " \
  "v_pk_fma_f32 v[250:251], v[106:107], v[144:145], v[250:251] op_sel_hi:[1,0,1]\n " \
  "v_pk_fma_f32 v[252:253], v[104:105], v[144:145], v[252:253] op_sel:[0,1,0]\n " \
  "v_pk_fma_f32 v[254:255], v[106:107], v[144:145], v[254:255] op_sel:[0,1,0]\n v_pk_mul_f32 v[248:249], v[248:249], v[108:109]\n " \
  "v_pk_mul_f32 v[250:251], v[250:251], v[110:111]\n v_pk_mul_f32 v[252:253], v[252:253], v[108:109]\n v_pk_mul_f32 v[254:255], v[254:255], v[110:111]\n " \
  "v_cvt_pk_bf16_f32 v240, v240, v241\n v_cvt_pk_bf16_f32 v241, v242, v243\n v_cvt_pk_bf16_f32 v242, v244, v245\n v_cvt_pk_bf16_f32 v243, v246, v247\n " \
  "v_cvt_pk_bf16_f32 v248, v248, v249\n v_cvt_pk_bf16_f32 v249, v250, v251\n v_cvt_pk_bf16_f32 v255, v254, v255\n v_cvt_pk_bf16_f32 v254, v252, v253\n " \
  "v_min_u32 v104, -2, v240\n v_min_u32 v105, -2, v241\n v_min_u32 v106, -2, v242\n v_min_u32 v107, -2, v243\n v_min_u32 v108, -2, v248\n " \
  "v_min_u32 v109, -2, v249\n v_min_u32 v110, -2, v254\n v_min_u32 v111, -2, v255\n s_waitcnt lgkmcnt(0)\n s_cbranch_scc1 .Lg1d%=\n " \
  "buffer_store_dwordx4 v[104:107], v55, %[rs], 0 offen sc1\n buffer_store_dwordx4 v[108:111], v55, %[rs], 0 offen offset:1024 sc1\n s_branch .Lg1e%=\n " \
  ".Lg1d%=:\n v_mov_b32 v53, 1\n ds_write_b128 v58, v[104:107]\n ds_write_b128 v58, v[108:111] offset:1024\n ds_write_b32 v59, v53\n .Lg1e%=:\n " \
  "v_pk_fma_f32 v[232:233], v[148:149], v[138:139], v[232:233] op_sel_hi:[1,0,1]\n " \
  "v_pk_fma_f32 v[234:235], v[150:151], v[138:139], v[234:235] op_sel_hi:[1,0,1]\n " \
  "v_pk_fma_f32 v[236:237], v[88:89], v[138:139], v[236:237] op_sel_hi:[1,0,1]\n " \
  "v_pk_fma_f32 v[238:239], v[90:91], v[138:139], v[238:239] op_sel_hi:[1,0,1]\n " \
  "v_pk_fma_f32 v[216:217], v[96:97], v[136:137], v[216:217] op_sel_hi:[1,0,1]\n " \
  "v_pk_fma_f32 v[218:219], v[98:99], v[136:137], v[218:219] op_sel_hi:[1,0,1]\n " \
  "v_pk_fma_f32 v[220:221], v[100:101], v[136:137], v[220:221] op_sel_hi:[1,0,1]\n " \
  "v_pk_fma_f32 v[222:223], v[102:103], v[136:137], v[222:223] op_sel_hi:[1,0,1]\n v_pk_mul_f32 v[216:217], v[136:137], v[216:217] op_sel:[1,0]\n " \
  "v_pk_mul_f32 v[218:219], v[136:137], v[218:219] op_sel:[1,0]\n v_pk_mul_f32 v[220:221], v[136:137], v[220:221] op_sel:[1,0]\n " \
  "v_pk_mul_f32 v[222:223], v[136:137], v[222:223] op_sel:[1,0]\n v_cvt_pk_bf16_f32 v232, v232, v233\n v_cvt_pk_bf16_f32 v233, v234, v235\n " \
  "v_cvt_pk_bf16_f32 v234, v236, v237\n v_cvt_pk_bf16_f32 v235, v238, v239\n v_cvt_pk_bf16_f32 v216, v216, v217\n v_cvt_pk_bf16_f32 v217, v218, v219\n " \
  "v_cvt_pk_bf16_f32 v218, v220, v221\n v_cvt_pk_bf16_f32 v219, v222, v223\n " \
  "v_pk_fma_f32 v[208:209], v[148:149], v[136:137], v[208:209] op_sel_hi:[1,0,1]\n " \
  "v_pk_fma_f32 v[210:211], v[150:151], v[136:137], v[210:211] op_sel_hi:[1,0,1]\n " \
  "v_pk_fma_f32 v[212:213], v[88:89], v[136:137], v[212:213] op_sel_hi:[1,0,1]\n " \
  "v_pk_fma_f32 v[214:215], v[90:91], v[136:137], v[214:215] op_sel_hi:[1,0,1]\n " \
  "v_pk_fma_f32 v[192:193], v[96:97], v[142:143], v[192:193] op_sel_hi:[1,0,1]\n " \
  "v_pk_fma_f32 v[194:195], v[98:99], v[142:143], v[194:195] op_sel_hi:[1,0,1]\n " \
  "v_pk_fma_f32 v[196:197], v[100:101], v[142:143], v[196:197] op_sel_hi:[1,0,1]\n " \
  "v_pk_fma_f32 v[198:199], v[102:103], v[142:143], v[198:199] op_sel_hi:[1,0,1]\n v_pk_mul_f32 v[192:193], v[142:143], v[192:193] op_sel:[1,0]\n " \
  "v_pk_mul_f32 v[194:195], v[142:143], v[194:195] op_sel:[1,0]\n v_pk_mul_f32 v[196:197], v[142:143], v[196:197] op_sel:[1,0]\n " \
  "v_pk_mul_f32 v[198:199], v[142:143], v[198:199] op_sel:[1,0]\n v_mfma_f32_16x16x32_bf16 v[120:123], v[240:243], v[232:235], v[40:43]\n " \
  "v_mfma_f32_16x16x32_bf16 v[236:239], v[216:219], v[232:235], v[36:39]\n v_cvt_pk_bf16_f32 v208, v208, v209\n v_cvt_pk_bf16_f32 v209, v210, v211\n " \
  "v_cvt_pk_bf16_f32 v210, v212, v213\n v_cvt_pk_bf16_f32 v211, v214, v215\n v_cvt_pk_bf16_f32 v192, v192, v193\n v_cvt_pk_bf16_f32 v193, v194, v195\n " \
  "v_cvt_pk_bf16_f32 v194, v196, v197\n v_cvt_pk_bf16_f32 v195, v198, v199\n " \
  "v_pk_fma_f32 v[184:185], v[148:149], v[142:143], v[184:185] op_sel_hi:[1,0,1]\n " \
  "v_pk_fma_f32 v[186:187], v[150:151], v[142:143], v[186:187] op_sel_hi:[1,0,1]\n " \
  "v_pk_fma_f32 v[188:189], v[88:89], v[142:143], v[188:189] op_sel_hi:[1,0,1]\n " \
  "v_pk_fma_f32 v[190:191], v[90:91], v[142:143], v[190:191] op_sel_hi:[1,0,1]\n " \
  "v_pk_fma_f32 v[168:169], v[96:97], v[140:141], v[168:169] op_sel_hi:[1,0,1]\n " \
  "v_pk_fma_f32 v[170:171], v[98:99], v[140:141], v[170:171] op_sel_hi:[1,0,1]\n " \
  "v_pk_fma_f32 v[172:173], v[100:101], v[140:141], v[172:173] op_sel_hi:[1,0,1]\n " \
  "v_pk_fma_f32 v[174:175], v[102:103], v[140:141], v[174:175] op_sel_hi:[1,0,1]\n v_pk_mul_f32 v[168:169], v[140:141], v[168:169] op_sel:[1,0]\n " \
  "v_pk_mul_f32 v[170:171], v[140:141], v[170:171] op_sel:[1,0]\n v_pk_mul_f32 v[172:173], v[140:141], v[172:173] op_sel:[1,0]\n " \
  "v_pk_mul_f32 v[174:175], v[140:141], v[174:175] op_sel:[1,0]\n v_mfma_f32_16x16x32_bf16 v[220:223], v[216:219], v[208:211], v[40:43]\n " \
  "v_mfma_f32_16x16x32_bf16 v[212:215], v[192:195], v[208:211], v[36:39]\n v_cvt_pk_bf16_f32 v184, v184, v185\n v_cvt_pk_bf16_f32 v185, v186, v187\n " \
  "v_cvt_pk_bf16_f32 v186, v188, v189\n v_cvt_pk_bf16_f32 v187, v190, v191\n v_cvt_pk_bf16_f32 v168, v168, v169\n v_cvt_pk_bf16_f32 v169, v170, v171\n " \
  "v_cvt_pk_bf16_f32 v170, v172, v173\n v_cvt_pk_bf16_f32 v171, v174, v175\n " \
  "v_pk_fma_f32 v[160:161], v[148:149], v[140:141], v[160:161] op_sel_hi:[1,0,1]\n " \
  "v_pk_fma_f32 v[162:163], v[150:151], v[140:141], v[162:163] op_sel_hi:[1,0,1]\n " \
  "v_pk_fma_f32 v[164:165], v[88:89], v[140:141], v[164:165] op_sel_hi:[1,0,1]\n " \
  "v_pk_fma_f32 v[166:167], v[90:91], v[140:141], v[166:167] op_sel_hi:[1,0,1]\n " \
  "v_pk_fma_f32 v[176:177], v[128:129], v[144:145], v[176:177] op_sel_hi:[1,0,1]\n " \
  "v_pk_fma_f32 v[178:179], v[130:131], v[144:145], v[178:179] op_sel_hi:[1,0,1]\n " \
  "v_pk_fma_f32 v[180:181], v[128:129], v[144:145], v[180:181] op_sel:[0,1,0]\n " \
  "v_pk_fma_f32 v[182:183], v[130:131], v[144:145], v[182:183] op_sel:[0,1,0]\n v_pk_mul_f32 v[176:177], v[176:177], v[92:93]\n " \
  "v_pk_mul_f32 v[178:179], v[178:179], v[94:95]\n v_pk_mul_f32 v[180:181], v[180:181], v[92:93]\n v_pk_mul_f32 v[182:183], v[182:183], v[94:95]\n " \
  "v_mfma_f32_16x16x32_bf16 v[196:199], v[192:195], v[184:187], v[40:43]\n v_mfma_f32_16x16x32_bf16 v[188:191], v[168:171], v[184:187], v[36:39]\n " \
  "v_cvt_pk_bf16_f32 v160, v160, v161\n v_cvt_pk_bf16_f32 v161, v162, v163\n v_cvt_pk_bf16_f32 v162, v164, v165\n v_cvt_pk_bf16_f32 v163, v166, v167\n " \
  "v_cvt_pk_bf16_f32 v174, v176, v177\n v_cvt_pk_bf16_f32 v175, v178, v179\n v_cvt_pk_bf16_f32 v180, v180, v181\n v_cvt_pk_bf16_f32 v181, v182, v183\n " \
  "v_pk_fma_f32 v[224:225], v[112:113], v[144:145], v[224:225] op_sel_hi:[1,0,1]\n " \
  "v_pk_fma_f32 v[226:227], v[114:115], v[144:145], v[226:227] op_sel_hi:[1,0,1]\n " \
  "v_pk_fma_f32 v[228:229], v[112:113], v[144:145], v[228:229] op_sel:[0,1,0]\n " \
  "v_pk_fma_f32 v[230:231], v[114:115], v[144:145], v[230:231] op_sel:[0,1,0]\n v_pk_mul_f32 v[224:225], v[224:225], v[116:117]\n " \
  "v_pk_mul_f32 v[226:227], v[226:227], v[118:119]\n v_pk_mul_f32 v[228:229], v[228:229], v[116:117]\n v_pk_mul_f32 v[230:231], v[230:231], v[118:119]\n " \
  "v_mfma_f32_16x16x32_bf16 v[124:127], v[168:171], v[160:163], v[40:43]\n v_cvt_pk_bf16_f32 v246, v224, v225\n v_cvt_pk_bf16_f32 v247, v226, v227\n " \
  "v_cvt_pk_bf16_f32 v252, v228, v229\n v_cvt_pk_bf16_f32 v253, v230, v231\n v_max3_f32 v108, v236, v237, v238\n v_max3_f32 v106, v212, v213, v214\n " \
  "v_max3_f32 v109, v239, v120, v121\n v_max3_f32 v107, v215, v220, v221\n v_max3_f32 v108, v108, v122, v123\n v_max3_f32 v106, v106, v222, v223\n " \
  "v_max_f32 v108, v108, v109\n v_max_f32 v106, v106, v107\n v_max3_f32 v104, v188, v189, v190\n v_max3_f32 v105, v191, v196, v197\n " \
  "v_max3_f32 v104, v104, v198, v199\n v_pk_fma_f32 v[200:201], v[152:153], v[144:145], v[200:201] op_sel_hi:[1,0,1]\n " \
  "v_pk_fma_f32 v[202:203], v[154:155], v[144:145], v[202:203] op_sel_hi:[1,0,1]\n v_max_f32 v104, v104, v105\n v_mov_b32 v110, v108\n " \
  "v_pk_fma_f32 v[204:205], v[152:153], v[144:145], v[204:205] op_sel:[0,1,0]\n " \
  "v_pk_fma_f32 v[206:207], v[154:155], v[144:145], v[206:207] op_sel:[0,1,0]\n v_permlane16_swap_b32 v104, v106\n v_permlane16_swap_b32 v108, v110\n " \
  "v_pk_mul_f32 v[200:201], v[200:201], v[156:157]\n v_pk_mul_f32 v[202:203], v[202:203], v[158:159]\n v_max_f32 v104, v104, v106\n " \
  "v_max_f32 v108, v108, v110\n v_pk_mul_f32 v[204:205], v[204:205], v[156:157]\n v_pk_mul_f32 v[206:207], v[206:207], v[158:159]\n " \
  "v_permlane32_swap_b32 v104, v108\n v_mul_f32 v60, 0x3e8293ee, v139\n v_mul_f32 v62, 0x3e8293ee, v137\n v_max_f32 v104, v104, v108\n " \
  "v_mov_b32 v108, v104\n v_mul_f32 v56, 0x3e8293ee, v143\n v_mul_f32 v146, 0x3e8293ee, v141\n v_permlane32_swap_b32 v104, v108\n " \
  "v_mov_b32 v132, 0x3f803f80\n v_mov_b32 v133, 0x3f803f80\n v_mov_b32 v106, v104\n v_mov_b32 v134, 0x3f803f80\n v_mov_b32 v135, 0x3f803f80\n " \
  "v_permlane16_swap_b32 v104, v106\n v_cvt_pk_bf16_f32 v244, v200, v201\n v_cvt_pk_bf16_f32 v245, v202, v203\n v_cvt_pk_bf16_f32 v250, v204, v205\n " \
  "v_cvt_pk_bf16_f32 v251, v206, v207\n v_cvt_pk_bf16_f32 v176, v200, v201\n v_cvt_pk_bf16_f32 v177, v202, v203\n v_cvt_pk_bf16_f32 v182, v204, v205\n " \
  "v_cvt_pk_bf16_f32 v183, v206, v207\n v_mul_f32_e64 v61, v60, -v108\n v_mul_f32_e64 v63, v62, -v106\n v_mul_f32_e64 v57, v56, -v104\n " \
  "v_pk_fma_f32 v[236:237], v[236:237], v[60:61], v[60:61] op_sel:[0,0,1] op_sel_hi:[1,0,1]\n " \
  "v_pk_fma_f32 v[238:239], v[238:239], v[60:61], v[60:61] op_sel:[0,0,1] op_sel_hi:[1,0,1]\n " \
  "v_pk_fma_f32 v[120:121], v[120:121], v[60:61], v[60:61] op_sel:[0,0,1] op_sel_hi:[1,0,1]\n " \
  "v_pk_fma_f32 v[122:123], v[122:123], v[60:61], v[60:61] op_sel:[0,0,1] op_sel_hi:[1,0,1]\n v_exp_f32 v236, v236\n v_exp_f32 v237, v237\n " \
  "v_exp_f32 v238, v238\n v_exp_f32 v239, v239\n v_exp_f32 v120, v120\n v_exp_f32 v121, v121\n v_exp_f32 v122, v122\n v_exp_f32 v123, v123\n " \
  "v_pk_fma_f32 v[212:213], v[212:213], v[62:63], v[62:63] op_sel:[0,0,1] op_sel_hi:[1,0,1]\n " \
  "v_pk_fma_f32 v[214:215], v[214:215], v[62:63], v[62:63] op_sel:[0,0,1] op_sel_hi:[1,0,1]\n " \
  "v_pk_fma_f32 v[220:221], v[220:221], v[62:63], v[62:63] op_sel:[0,0,1] op_sel_hi:[1,0,1]\n " \
  "v_pk_fma_f32 v[222:223], v[222:223], v[62:63], v[62:63] op_sel:[0,0,1] op_sel_hi:[1,0,1]\n v_cvt_pk_bf16_f32 v236, v236, v237\n " \
  "v_cvt_pk_bf16_f32 v237, v238, v239\n v_cvt_pk_bf16_f32 v238, v120, v121\n v_cvt_pk_bf16_f32 v239, v122, v123\n v_exp_f32 v212, v212\n " \
  "v_exp_f32 v213, v213\n v_mfma_f32_16x16x32_bf16 v[120:123], v[132:135], v[236:239], 0\n " \
  "v_mfma_f32_16x16x32_bf16 v[232:235], v[246:249], v[236:239], 0\n v_mfma_f32_16x16x32_bf16 v[240:243], v[252:255], v[236:239], 0\n " \
  "v_exp_f32 v214, v214\n v_exp_f32 v215, v215\n v_exp_f32 v220, v220\n v_exp_f32 v221, v221\n v_exp_f32 v222, v222\n v_exp_f32 v223, v223\n " \
  "v_pk_fma_f32 v[188:189], v[188:189], v[56:57], v[56:57] op_sel:[0,0,1] op_sel_hi:[1,0,1]\n " \
  "v_pk_fma_f32 v[190:191], v[190:191], v[56:57], v[56:57] op_sel:[0,0,1] op_sel_hi:[1,0,1]\n " \
  "v_pk_fma_f32 v[196:197], v[196:197], v[56:57], v[56:57] op_sel:[0,0,1] op_sel_hi:[1,0,1]\n " \
  "v_pk_fma_f32 v[198:199], v[198:199], v[56:57], v[56:57] op_sel:[0,0,1] op_sel_hi:[1,0,1]\n v_cvt_pk_bf16_f32 v212, v212, v213\n " \
  "v_cvt_pk_bf16_f32 v213, v214, v215\n v_cvt_pk_bf16_f32 v214, v220, v221\n v_cvt_pk_bf16_f32 v215, v222, v223\n v_exp_f32 v188, v188\n " \
  "s_cmp_eq_u32 %[wm], 0\n s_cbranch_scc1 .Lg1f%=\n ds_read_b32 v53, v59\n ds_read_b128 v[80:83], v58\n ds_read_b64 v[172:173], v58 offset:1024\n " \
  "ds_read_b64 v[178:179], v58 offset:1032\n s_branch .Lg1g%=\n .Lg1f%=:\n buffer_load_dwordx4 v[80:83], v55, %[rs], 0 offen sc1\n " \
  "buffer_load_dwordx2 v[172:173], v55, %[rs], 0 offen offset:1024 sc1\n buffer_load_dwordx2 v[178:179], v55, %[rs], 0 offen offset:1032 sc1\n " \
  ".Lg1g%=:\n v_exp_f32 v189, v189\n v_mfma_f32_16x16x32_bf16 v[220:223], v[132:135], v[212:215], 0\n " \
  "v_mfma_f32_16x16x32_bf16 v[224:227], v[244:247], v[212:215], 0\n v_mfma_f32_16x16x32_bf16 v[228:231], v[250:253], v[212:215], 0\n " \
  "v_exp_f32 v190, v190\n v_exp_f32 v191, v191\n v_exp_f32 v196, v196\n v_exp_f32 v197, v197\n v_exp_f32 v198, v198\n v_exp_f32 v199, v199\n " \
  "v_rcp_f32 v120, v120\n v_cvt_pk_bf16_f32 v188, v188, v189\n v_pk_mul_f32 v[232:233], v[232:233], v[120:121] op_sel_hi:[1,0]\n " \
  "v_pk_mul_f32 v[234:235], v[234:235], v[120:121] op_sel_hi:[1,0]\n v_pk_mul_f32 v[240:241], v[240:241], v[120:121] op_sel_hi:[1,0]\n " \
  "v_pk_mul_f32 v[242:243], v[242:243], v[120:121] op_sel_hi:[1,0]\n v_cvt_pk_bf16_f32 v189, v190, v191\n v_cvt_pk_bf16_f32 v190, v196, v197\n " \
  "v_cvt_pk_bf16_f32 v191, v198, v199\n v_rcp_f32 v220, v220\n v_cvt_pk_bf16_f32 v232, v232, v233\n v_cvt_pk_bf16_f32 v233, v234, v235\n " \
  "v_mfma_f32_16x16x32_bf16 v[196:199], v[132:135], v[188:191], 0\n v_mfma_f32_16x16x32_bf16 v[200:203], v[174:177], v[188:191], 0\n " \
  "v_mfma_f32_16x16x32_bf16 v[204:207], v[180:183], v[188:191], 0\n v_cvt_pk_bf16_f32 v234, v240, v241\n v_cvt_pk_bf16_f32 v235, v242, v243\n " \
  "buffer_store_dwordx4 v[232:235], v51, %[rs], 0 offen offset:3072 sc1\n ds_write_b128 v50, v[232:235] offset:3072\n " \
  "v_pk_mul_f32 v[224:225], v[224:225], v[220:221] op_sel_hi:[1,0]\n v_pk_mul_f32 v[226:227], v[226:227], v[220:221] op_sel_hi:[1,0]\n " \
  "v_pk_mul_f32 v[228:229], v[228:229], v[220:221] op_sel_hi:[1,0]\n v_pk_mul_f32 v[230:231], v[230:231], v[220:221] op_sel_hi:[1,0]\n " \
  "v_cvt_pk_bf16_f32 v224, v224, v225\n v_cvt_pk_bf16_f32 v225, v226, v227\n v_cvt_pk_bf16_f32 v226, v228, v229\n v_cvt_pk_bf16_f32 v227, v230, v231\n " \
  "v_rcp_f32 v196, v196\n buffer_store_dwordx4 v[224:227], v51, %[rs], 0 offen offset:2048 sc1\n ds_write_b128 v50, v[224:227] offset:2048\n " \
  "v_pk_mul_f32 v[200:201], v[200:201], v[196:197] op_sel_hi:[1,0]\n v_pk_mul_f32 v[202:203], v[202:203], v[196:197] op_sel_hi:[1,0]\n " \
  "v_pk_mul_f32 v[204:205], v[204:205], v[196:197] op_sel_hi:[1,0]\n v_pk_mul_f32 v[206:207], v[206:207], v[196:197] op_sel_hi:[1,0]\n " \
  "v_cvt_pk_bf16_f32 v200, v200, v201\n v_cvt_pk_bf16_f32 v201, v202, v203\n v_cvt_pk_bf16_f32 v202, v204, v205\n v_cvt_pk_bf16_f32 v203, v206, v207\n " \
  "buffer_store_dwordx4 v[200:203], v51, %[rs], 0 offen offset:1024 sc1\n ds_write_b128 v50, v[200:203] offset:1024\n s_mov_b32 vcc_hi, 0x40000\n " \
  "s_cmp_eq_u32 %[wm], 0\n s_cbranch_scc1 .Lg1h%=\n s_waitcnt lgkmcnt(3)\n .Lg1i%=:\n v_readfirstlane_b32 vcc_lo, v53\n s_nop 1\n " \
  "s_cmp_lg_u32 vcc_lo, 0\n s_cbranch_scc1 .Lg1k%=\n ds_read_b32 v53, v59\n ds_read_b128 v[80:83], v58\n ds_read_b64 v[172:173], v58 offset:1024\n " \
  "ds_read_b64 v[178:179], v58 offset:1032\n s_waitcnt lgkmcnt(0)\n s_sub_u32 vcc_hi, vcc_hi, 1\n s_cbranch_scc0 .Lg1i%=\n s_branch .Lg1k%=\n .Lg1h%=:\n " \
  "s_mov_b32 m0, 0x40000\n s_waitcnt vmcnt(3)\n .Lg1j%=:\n v_max3_u32 v104, v80, v81, v82\n v_max3_u32 v105, v83, v172, v173\n " \
  "v_max3_u32 v104, v104, v178, v179\n v_max_u32 v104, v104, v105\n v_cmp_eq_u32 vcc, -1, v104\n s_cbranch_vccz .Lg1k%=\n " \
  "buffer_load_dwordx4 v[80:83], v55, %[rs], 0 offen sc1\n buffer_load_dwordx2 v[172:173], v55, %[rs], 0 offen offset:1024 sc1\n " \
  "buffer_load_dwordx2 v[178:179], v55, %[rs], 0 offen offset:1032 sc1\n s_waitcnt vmcnt(0)\n s_sub_u32 m0, m0, 1\n s_cbranch_scc0 .Lg1j%=\n .Lg1k%=:\n " \
  "v_mfma_f32_16x16x32_bf16 v[164:167], v[80:83], v[160:163], v[44:47]\n s_nop 7\n v_max3_f32 v104, v164, v165, v166\n " \
  "v_max3_f32 v105, v167, v124, v125\n v_max3_f32 v104, v104, v126, v127\n v_max_f32 v104, v104, v105\n v_mov_b32 v105, v104\n s_nop 1\n " \
  "v_permlane16_swap_b32 v104, v105\n v_max_f32 v104, v104, v105\n v_mov_b32 v105, v104\n s_nop 1\n v_permlane32_swap_b32 v104, v105\n " \
  "v_max_f32 v104, v104, v105\n v_mul_f32_e64 v147, v146, -v104\n " \
  "v_pk_fma_f32 v[164:165], v[164:165], v[146:147], v[146:147] op_sel:[0,0,1] op_sel_hi:[1,0,1]\n " \
  "v_pk_fma_f32 v[166:167], v[166:167], v[146:147], v[146:147] op_sel:[0,0,1] op_sel_hi:[1,0,1]\n " \
  "v_pk_fma_f32 v[124:125], v[124:125], v[146:147], v[146:147] op_sel:[0,0,1] op_sel_hi:[1,0,1]\n " \
  "v_pk_fma_f32 v[126:127], v[126:127], v[146:147], v[146:147] op_sel:[0,0,1] op_sel_hi:[1,0,1]\n v_exp_f32 v164, v164\n v_exp_f32 v165, v165\n " \
  "v_exp_f32 v166, v166\n v_exp_f32 v167, v167\n v_exp_f32 v124, v124\n v_exp_f32 v125, v125\n v_exp_f32 v126, v126\n v_exp_f32 v127, v127\n " \
  "v_cvt_pk_bf16_f32 v164, v164, v165\n v_cvt_pk_bf16_f32 v165, v166, v167\n v_cvt_pk_bf16_f32 v166, v124, v125\n v_cvt_pk_bf16_f32 v167, v126, v127\n " \
  "s_nop 1\n v_mfma_f32_16x16x32_bf16 v[124:127], v[132:135], v[164:167], 0\n v_mfma_f32_16x16x32_bf16 v[168:171], v[172:175], v[164:167], 0\n " \
  "v_mfma_f32_16x16x32_bf16 v[84:87], v[178:181], v[164:167], 0\n s_nop 5\n v_rcp_f32 v124, v124\n s_nop 0\n " \
  "v_pk_mul_f32 v[168:169], v[168:169], v[124:125] op_sel_hi:[1,0]\n v_pk_mul_f32 v[170:171], v[170:171], v[124:125] op_sel_hi:[1,0]\n " \
  "v_pk_mul_f32 v[84:85], v[84:85], v[124:125] op_sel_hi:[1,0]\n v_pk_mul_f32 v[86:87], v[86:87], v[124:125] op_sel_hi:[1,0]\n " \
  "v_cvt_pk_bf16_f32 v168, v168, v169\n v_cvt_pk_bf16_f32 v169, v170, v171\n v_cvt_pk_bf16_f32 v170, v84, v85\n v_cvt_pk_bf16_f32 v171, v86, v87\n " \
  "buffer_store_dwordx4 v[168:171], v51, %[rs], 0 offen offset:0 sc1\n ds_write_b128 v50, v[168:171] offset:0\n "
#define G1_HEDGE \
  "G1Q 2, 2, 0, 0, 0\n ds_read2_b64 v[140:143], v49 offset1:16\n G1Q 2, 2, 0, 1, 0\n ds_read_b128 v[96:99], v48 offset:256\n G1Q 2, 2, 0, 2, 0\n " \
  "ds_read_b128 v[100:103], v48 offset:320\n G1Q 2, 2, 0, 3, 0\n ds_read2_b64 v[136:139], v49 offset0:32 offset1:48\n G1Q 2, 3, 1, 0, 0\n " \
  "ds_read_b64_tr_b16 v[104:105], v53\n G1Q 2, 3, 1, 1, 0\n ds_read_b64_tr_b16 v[106:107], v53 offset:1024\n G1Q 2, 3, 1, 2, 0\n " \
  "ds_read_b64_tr_b16 v[108:109], v53 offset:32\n G1Q 2, 3, 1, 3, 0\n ds_read_b64_tr_b16 v[110:111], v53 offset:1056\n G1Q 2, 0, 2, 0, 0\n " \
  "ds_read2_b32 v[144:145], v52 offset1:16\n G1Q 2, 0, 2, 1, 0\n ds_read_b128 v[148:151], v48\n G1Q 2, 0, 2, 2, 0\n " \
  "ds_read_b128 v[88:91], v48 offset:64\n G1Q 2, 0, 2, 3, 0\n ds_read_b128 v[128:131], v54\n G1Q 2, 1, 3, 0, 0\n ds_read_b128 v[92:95], v54 offset:512\n " \
  "G1Q 2, 1, 3, 1, 0\n ds_read_b128 v[132:135], v54 offset:64\n G1Q 2, 1, 3, 2, 0\n G1Q 2, 1, 3, 3, 0\n ds_read_b128 v[112:115], v54 offset:576\n " \
  "ds_read_b128 v[116:119], v54 offset:128\n ds_read_b128 v[152:155], v54 offset:640\n ds_read_b128 v[156:159], v54 offset:192\n " \
  "ds_read_b128 v[120:123], v54 offset:704\n s_waitcnt lgkmcnt(11)\n v_pk_fma_f32 v[168:169], v[96:97], v[140:141], v[168:169] op_sel_hi:[1,0,1]\n " \
  "v_pk_fma_f32 v[170:171], v[98:99], v[140:141], v[170:171] op_sel_hi:[1,0,1]\n " \
  "v_pk_fma_f32 v[172:173], v[100:101], v[140:141], v[172:173] op_sel_hi:[1,0,1]\n " \
  "v_pk_fma_f32 v[174:175], v[102:103], v[140:141], v[174:175] op_sel_hi:[1,0,1]\n " \
  "v_pk_fma_f32 v[192:193], v[96:97], v[142:143], v[192:193] op_sel_hi:[1,0,1]\n " \
  "v_pk_fma_f32 v[194:195], v[98:99], v[142:143], v[194:195] op_sel_hi:[1,0,1]\n " \
  "v_pk_fma_f32 v[196:197], v[100:101], v[142:143], v[196:197] op_sel_hi:[1,0,1]\n " \
  "v_pk_fma_f32 v[198:199], v[102:103], v[142:143], v[198:199] op_sel_hi:[1,0,1]\n v_cvt_pk_bf16_f32 v168, v168, v169\n " \
  "v_cvt_pk_bf16_f32 v169, v170, v171\n v_cvt_pk_bf16_f32 v170, v172, v173\n v_cvt_pk_bf16_f32 v171, v174, v175\n v_cvt_pk_bf16_f32 v192, v192, v193\n " \
  "v_cvt_pk_bf16_f32 v193, v194, v195\n v_cvt_pk_bf16_f32 v194, v196, v197\n v_cvt_pk_bf16_f32 v195, v198, v199\n " \
  "v_pk_fma_f32 v[216:217], v[96:97], v[136:137], v[216:217] op_sel_hi:[1,0,1]\n " \
  "v_pk_fma_f32 v[218:219], v[98:99], v[136:137], v[218:219] op_sel_hi:[1,0,1]\n " \
  "v_pk_fma_f32 v[220:221], v[100:101], v[136:137], v[220:221] op_sel_hi:[1,0,1]\n " \
  "v_pk_fma_f32 v[222:223], v[102:103], v[136:137], v[222:223] op_sel_hi:[1,0,1]\n " \
  "v_pk_fma_f32 v[240:241], v[96:97], v[138:139], v[240:241] op_sel_hi:[1,0,1]\n " \
  "v_pk_fma_f32 v[242:243], v[98:99], v[138:139], v[242:243] op_sel_hi:[1,0,1]\n " \
  "v_pk_fma_f32 v[244:245], v[100:101], v[138:139], v[244:245] op_sel_hi:[1,0,1]\n " \
  "v_pk_fma_f32 v[246:247], v[102:103], v[138:139], v[246:247] op_sel_hi:[1,0,1]\n v_mfma_f32_16x16x32_bf16 v[172:175], v[104:107], v[168:171], 0\n " \
  "v_mfma_f32_16x16x32_bf16 v[80:83], v[108:111], v[168:171], 0\n v_mfma_f32_16x16x32_bf16 v[196:199], v[104:107], v[192:195], 0\n " \
  "v_mfma_f32_16x16x32_bf16 v[84:87], v[108:111], v[192:195], 0\n v_cvt_pk_bf16_f32 v216, v216, v217\n v_cvt_pk_bf16_f32 v217, v218, v219\n " \
  "v_cvt_pk_bf16_f32 v218, v220, v221\n v_cvt_pk_bf16_f32 v219, v222, v223\n v_cvt_pk_bf16_f32 v240, v240, v241\n v_cvt_pk_bf16_f32 v241, v242, v243\n " \
  "v_cvt_pk_bf16_f32 v242, v244, v245\n v_cvt_pk_bf16_f32 v243, v246, v247\n v_mul_f32 v40, 0x3fb8aa3b, v141\n v_mul_f32 v42, 0x3fb8aa3b, v143\n " \
  "v_mul_f32 v44, 0x3fb8aa3b, v137\n v_mul_f32 v46, 0x3fb8aa3b, v139\n v_mfma_f32_16x16x32_bf16 v[220:223], v[104:107], v[216:219], 0\n " \
  "v_mfma_f32_16x16x32_bf16 v[124:127], v[108:111], v[216:219], 0\n v_mfma_f32_16x16x32_bf16 v[244:247], v[104:107], v[240:243], 0\n " \
  "v_mfma_f32_16x16x32_bf16 v[36:39], v[108:111], v[240:243], 0\n v_max3_f32 v168, v172, v173, v174\n v_max3_f32 v169, -v172, -v173, -v174\n " \
  "v_max3_f32 v168, v168, v175, v80\n v_max3_f32 v169, v169, -v175, -v80\n v_max3_f32 v168, v168, v81, v82\n v_max3_f32 v169, v169, -v81, -v82\n " \
  "v_max_f32_e64 v168, v168, v83\n v_max_f32_e64 v169, v169, -v83\n v_max3_f32 v170, v196, v197, v198\n v_max3_f32 v171, -v196, -v197, -v198\n " \
  "v_max3_f32 v170, v170, v199, v84\n v_max3_f32 v171, v171, -v199, -v84\n v_max3_f32 v170, v170, v85, v86\n v_max3_f32 v171, v171, -v85, -v86\n " \
  "v_max_f32_e64 v170, v170, v87\n v_max_f32_e64 v171, v171, -v87\n s_waitcnt lgkmcnt(0)\n " \
  "v_pk_fma_f32 v[176:177], v[128:129], v[144:145], v[176:177] op_sel_hi:[1,0,1]\n " \
  "v_pk_fma_f32 v[178:179], v[130:131], v[144:145], v[178:179] op_sel_hi:[1,0,1]\n " \
  "v_pk_fma_f32 v[180:181], v[128:129], v[144:145], v[180:181] op_sel:[0,1,0]\n " \
  "v_pk_fma_f32 v[182:183], v[130:131], v[144:145], v[182:183] op_sel:[0,1,0]\n v_pk_mul_f32 v[176:177], v[176:177], v[92:93]\n " \
  "v_pk_mul_f32 v[178:179], v[178:179], v[94:95]\n v_pk_mul_f32 v[180:181], v[180:181], v[92:93]\n v_pk_mul_f32 v[182:183], v[182:183], v[94:95]\n " \
  "v_pk_fma_f32 v[200:201], v[132:133], v[144:145], v[200:201] op_sel_hi:[1,0,1]\n " \
  "v_pk_fma_f32 v[202:203], v[134:135], v[144:145], v[202:203] op_sel_hi:[1,0,1]\n " \
  "v_pk_fma_f32 v[204:205], v[132:133], v[144:145], v[204:205] op_sel:[0,1,0]\n " \
  "v_pk_fma_f32 v[206:207], v[134:135], v[144:145], v[206:207] op_sel:[0,1,0]\n v_pk_mul_f32 v[200:201], v[200:201], v[112:113]\n " \
  "v_pk_mul_f32 v[202:203], v[202:203], v[114:115]\n v_pk_mul_f32 v[204:205], v[204:205], v[112:113]\n v_pk_mul_f32 v[206:207], v[206:207], v[114:115]\n " \
  "v_max3_f32 v192, v220, v221, v222\n v_max3_f32 v193, -v220, -v221, -v222\n v_max3_f32 v192, v192, v223, v124\n v_max3_f32 v193, v193, -v223, -v124\n " \
  "v_max3_f32 v192, v192, v125, v126\n v_max3_f32 v193, v193, -v125, -v126\n v_max_f32_e64 v192, v192, v127\n v_max_f32_e64 v193, v193, -v127\n " \
  "v_max3_f32 v194, v244, v245, v246\n v_max3_f32 v195, -v244, -v245, -v246\n v_max3_f32 v194, v194, v247, v36\n v_max3_f32 v195, v195, -v247, -v36\n " \
  "v_max3_f32 v194, v194, v37, v38\n v_max3_f32 v195, v195, -v37, -v38\n v_max_f32_e64 v194, v194, v39\n v_max_f32_e64 v195, v195, -v39\n " \
  "v_permlane16_swap_b32 v168, v169\n v_permlane16_swap_b32 v170, v171\n v_permlane16_swap_b32 v192, v193\n v_permlane16_swap_b32 v194, v195\n " \
  "v_pk_fma_f32 v[224:225], v[116:117], v[144:145], v[224:225] op_sel_hi:[1,0,1]\n " \
  "v_pk_fma_f32 v[226:227], v[118:119], v[144:145], v[226:227] op_sel_hi:[1,0,1]\n v_max_f32 v168, v168, v169\n v_max_f32 v170, v170, v171\n " \
  "v_max_f32 v192, v192, v193\n v_max_f32 v194, v194, v195\n v_pk_fma_f32 v[228:229], v[116:117], v[144:145], v[228:229] op_sel:[0,1,0]\n " \
  "v_pk_fma_f32 v[230:231], v[118:119], v[144:145], v[230:231] op_sel:[0,1,0]\n v_permlane32_swap_b32 v168, v170\n v_permlane32_swap_b32 v192, v194\n " \
  "v_pk_mul_f32 v[224:225], v[224:225], v[152:153]\n v_pk_mul_f32 v[226:227], v[226:227], v[154:155]\n v_max_f32 v168, v168, v170\n " \
  "v_max_f32 v192, v192, v194\n v_pk_mul_f32 v[228:229], v[228:229], v[152:153]\n v_pk_mul_f32 v[230:231], v[230:231], v[154:155]\n " \
  "v_mov_b32 v170, v168\n v_mov_b32 v194, v192\n v_pk_fma_f32 v[248:249], v[156:157], v[144:145], v[248:249] op_sel_hi:[1,0,1]\n " \
  "v_pk_fma_f32 v[250:251], v[158:159], v[144:145], v[250:251] op_sel_hi:[1,0,1]\n v_permlane32_swap_b32 v168, v170\n v_permlane32_swap_b32 v192, v194\n " \
  "v_pk_fma_f32 v[252:253], v[156:157], v[144:145], v[252:253] op_sel:[0,1,0]\n " \
  "v_pk_fma_f32 v[254:255], v[158:159], v[144:145], v[254:255] op_sel:[0,1,0]\n v_mov_b32 v169, v168\n v_mov_b32 v171, v170\n v_mov_b32 v193, v192\n " \
  "v_mov_b32 v195, v194\n v_pk_mul_f32 v[248:249], v[248:249], v[120:121]\n v_pk_mul_f32 v[250:251], v[250:251], v[122:123]\n " \
  "v_permlane16_swap_b32 v168, v169\n v_permlane16_swap_b32 v170, v171\n v_permlane16_swap_b32 v192, v193\n v_permlane16_swap_b32 v194, v195\n " \
  "v_pk_mul_f32 v[252:253], v[252:253], v[120:121]\n v_pk_mul_f32 v[254:255], v[254:255], v[122:123]\n v_cvt_pk_bf16_f32 v176, v176, v177\n " \
  "v_cvt_pk_bf16_f32 v177, v178, v179\n v_cvt_pk_bf16_f32 v178, v200, v201\n v_cvt_pk_bf16_f32 v179, v202, v203\n v_cvt_pk_bf16_f32 v180, v180, v181\n " \
  "v_cvt_pk_bf16_f32 v181, v182, v183\n v_cvt_pk_bf16_f32 v182, v204, v205\n v_cvt_pk_bf16_f32 v183, v206, v207\n v_cvt_pk_bf16_f32 v224, v224, v225\n " \
  "v_cvt_pk_bf16_f32 v225, v226, v227\n v_cvt_pk_bf16_f32 v226, v248, v249\n v_cvt_pk_bf16_f32 v227, v250, v251\n v_cvt_pk_bf16_f32 v228, v228, v229\n " \
  "v_cvt_pk_bf16_f32 v229, v230, v231\n v_cvt_pk_bf16_f32 v230, v252, v253\n v_cvt_pk_bf16_f32 v231, v254, v255\n v_mul_f32_e64 v41, v40, -v168\n " \
  "v_mul_f32_e64 v146, v40, -v169\n v_mul_f32_e64 v43, v42, -v170\n v_mul_f32_e64 v147, v42, -v171\n v_mul_f32_e64 v45, v44, -v192\n " \
  "v_mul_f32_e64 v144, v44, -v193\n v_mul_f32_e64 v47, v46, -v194\n v_mul_f32_e64 v145, v46, -v195\n " \
  "v_pk_fma_f32 v[200:201], v[172:173], v[40:41], v[146:147] op_sel_hi:[1,0,0] neg_lo:[1,0,0] neg_hi:[1,0,0]\n " \
  "v_pk_fma_f32 v[202:203], v[174:175], v[40:41], v[146:147] op_sel_hi:[1,0,0] neg_lo:[1,0,0] neg_hi:[1,0,0]\n " \
  "v_pk_fma_f32 v[204:205], v[80:81], v[40:41], v[146:147] op_sel_hi:[1,0,0] neg_lo:[1,0,0] neg_hi:[1,0,0]\n " \
  "v_pk_fma_f32 v[206:207], v[82:83], v[40:41], v[146:147] op_sel_hi:[1,0,0] neg_lo:[1,0,0] neg_hi:[1,0,0]\n " \
  "v_pk_fma_f32 v[172:173], v[172:173], v[40:41], v[40:41] op_sel:[0,0,1] op_sel_hi:[1,0,1]\n " \
  "v_pk_fma_f32 v[174:175], v[174:175], v[40:41], v[40:41] op_sel:[0,0,1] op_sel_hi:[1,0,1]\n " \
  "v_pk_fma_f32 v[80:81], v[80:81], v[40:41], v[40:41] op_sel:[0,0,1] op_sel_hi:[1,0,1]\n " \
  "v_pk_fma_f32 v[82:83], v[82:83], v[40:41], v[40:41] op_sel:[0,0,1] op_sel_hi:[1,0,1]\n ds_write_b128 v55, v[176:179] offset:0\n " \
  "v_exp_f32 v200, v200\n v_exp_f32 v201, v201\n v_exp_f32 v202, v202\n v_exp_f32 v203, v203\n v_exp_f32 v204, v204\n v_exp_f32 v205, v205\n " \
  "v_exp_f32 v206, v206\n v_exp_f32 v207, v207\n v_exp_f32 v172, v172\n v_exp_f32 v173, v173\n v_exp_f32 v174, v174\n v_exp_f32 v175, v175\n " \
  "v_exp_f32 v80, v80\n v_exp_f32 v81, v81\n v_exp_f32 v82, v82\n v_exp_f32 v83, v83\n " \
  "v_pk_fma_f32 v[248:249], v[196:197], v[42:43], v[146:147] op_sel:[0,0,1] op_sel_hi:[1,0,1] neg_lo:[1,0,0] neg_hi:[1,0,0]\n " \
  "v_pk_fma_f32 v[250:251], v[198:199], v[42:43], v[146:147] op_sel:[0,0,1] op_sel_hi:[1,0,1] neg_lo:[1,0,0] neg_hi:[1,0,0]\n " \
  "v_pk_fma_f32 v[252:253], v[84:85], v[42:43], v[146:147] op_sel:[0,0,1] op_sel_hi:[1,0,1] neg_lo:[1,0,0] neg_hi:[1,0,0]\n " \
  "v_pk_fma_f32 v[254:255], v[86:87], v[42:43], v[146:147] op_sel:[0,0,1] op_sel_hi:[1,0,1] neg_lo:[1,0,0] neg_hi:[1,0,0]\n " \
  "v_pk_fma_f32 v[196:197], v[196:197], v[42:43], v[42:43] op_sel:[0,0,1] op_sel_hi:[1,0,1]\n " \
  "v_pk_fma_f32 v[198:199], v[198:199], v[42:43], v[42:43] op_sel:[0,0,1] op_sel_hi:[1,0,1]\n " \
  "v_pk_fma_f32 v[84:85], v[84:85], v[42:43], v[42:43] op_sel:[0,0,1] op_sel_hi:[1,0,1]\n " \
  "v_pk_fma_f32 v[86:87], v[86:87], v[42:43], v[42:43] op_sel:[0,0,1] op_sel_hi:[1,0,1]\n ds_write_b128 v55, v[180:183] offset:1024\n " \
  "v_exp_f32 v248, v248\n v_exp_f32 v249, v249\n v_exp_f32 v250, v250\n v_exp_f32 v251, v251\n v_exp_f32 v252, v252\n v_exp_f32 v253, v253\n " \
  "v_exp_f32 v254, v254\n v_exp_f32 v255, v255\n v_exp_f32 v196, v196\n v_exp_f32 v197, v197\n v_exp_f32 v198, v198\n v_exp_f32 v199, v199\n " \
  "v_exp_f32 v84, v84\n v_exp_f32 v85, v85\n v_exp_f32 v86, v86\n v_exp_f32 v87, v87\n " \
  "v_pk_fma_f32 v[128:129], v[220:221], v[44:45], v[144:145] op_sel_hi:[1,0,0] neg_lo:[1,0,0] neg_hi:[1,0,0]\n " \
  "v_pk_fma_f32 v[130:131], v[222:223], v[44:45], v[144:145] op_sel_hi:[1,0,0] neg_lo:[1,0,0] neg_hi:[1,0,0]\n " \
  "v_pk_fma_f32 v[132:133], v[124:125], v[44:45], v[144:145] op_sel_hi:[1,0,0] neg_lo:[1,0,0] neg_hi:[1,0,0]\n " \
  "v_pk_fma_f32 v[134:135], v[126:127], v[44:45], v[144:145] op_sel_hi:[1,0,0] neg_lo:[1,0,0] neg_hi:[1,0,0]\n " \
  "v_pk_fma_f32 v[220:221], v[220:221], v[44:45], v[44:45] op_sel:[0,0,1] op_sel_hi:[1,0,1]\n " \
  "v_pk_fma_f32 v[222:223], v[222:223], v[44:45], v[44:45] op_sel:[0,0,1] op_sel_hi:[1,0,1]\n " \
  "v_pk_fma_f32 v[124:125], v[124:125], v[44:45], v[44:45] op_sel:[0,0,1] op_sel_hi:[1,0,1]\n " \
  "v_pk_fma_f32 v[126:127], v[126:127], v[44:45], v[44:45] op_sel:[0,0,1] op_sel_hi:[1,0,1]\n ds_write_b128 v55, v[224:227] offset:8192\n " \
  "v_pk_add_f32 v[216:217], v[172:173], v[174:175]\n v_pk_add_f32 v[218:219], v[80:81], v[82:83]\n v_pk_add_f32 v[216:217], v[216:217], v[218:219]\n " \
  "v_add_f32 v168, v216, v217\n v_pk_add_f32 v[216:217], v[200:201], v[202:203]\n v_pk_add_f32 v[218:219], v[204:205], v[206:207]\n " \
  "v_pk_add_f32 v[216:217], v[216:217], v[218:219]\n v_add_f32 v169, v216, v217\n v_pk_add_f32 v[216:217], v[196:197], v[198:199]\n " \
  "v_pk_add_f32 v[218:219], v[84:85], v[86:87]\n v_pk_add_f32 v[216:217], v[216:217], v[218:219]\n v_add_f32 v170, v216, v217\n " \
  "v_pk_add_f32 v[216:217], v[248:249], v[250:251]\n v_pk_add_f32 v[218:219], v[252:253], v[254:255]\n v_pk_add_f32 v[216:217], v[216:217], v[218:219]\n " \
  "v_add_f32 v171, v216, v217\n v_exp_f32 v128, v128\n v_exp_f32 v129, v129\n v_permlane16_swap_b32 v168, v169\n v_permlane16_swap_b32 v170, v171\n " \
  "v_exp_f32 v130, v130\n v_exp_f32 v131, v131\n v_add_f32 v168, v168, v169\n v_add_f32 v170, v170, v171\n v_exp_f32 v132, v132\n v_exp_f32 v133, v133\n " \
  "v_permlane32_swap_b32 v168, v170\n v_exp_f32 v134, v134\n v_exp_f32 v135, v135\n v_add_f32 v168, v168, v170\n v_exp_f32 v220, v220\n " \
  "v_exp_f32 v221, v221\n v_rcp_f32 v168, v168\n v_exp_f32 v222, v222\n v_exp_f32 v223, v223\n v_mov_b32 v170, v168\n v_exp_f32 v124, v124\n " \
  "v_exp_f32 v125, v125\n v_permlane32_swap_b32 v168, v170\n v_exp_f32 v126, v126\n v_exp_f32 v127, v127\n v_mov_b32 v169, v168\n v_mov_b32 v171, v170\n " \
  "v_pk_fma_f32 v[112:113], v[244:245], v[46:47], v[144:145] op_sel:[0,0,1] op_sel_hi:[1,0,1] neg_lo:[1,0,0] neg_hi:[1,0,0]\n " \
  "v_pk_fma_f32 v[114:115], v[246:247], v[46:47], v[144:145] op_sel:[0,0,1] op_sel_hi:[1,0,1] neg_lo:[1,0,0] neg_hi:[1,0,0]\n " \
  "v_permlane16_swap_b32 v168, v169\n v_permlane16_swap_b32 v170, v171\n " \
  "v_pk_fma_f32 v[116:117], v[36:37], v[46:47], v[144:145] op_sel:[0,0,1] op_sel_hi:[1,0,1] neg_lo:[1,0,0] neg_hi:[1,0,0]\n " \
  "v_pk_fma_f32 v[118:119], v[38:39], v[46:47], v[144:145] op_sel:[0,0,1] op_sel_hi:[1,0,1] neg_lo:[1,0,0] neg_hi:[1,0,0]\n " \
  "v_pk_fma_f32 v[244:245], v[244:245], v[46:47], v[46:47] op_sel:[0,0,1] op_sel_hi:[1,0,1]\n " \
  "v_pk_fma_f32 v[246:247], v[246:247], v[46:47], v[46:47] op_sel:[0,0,1] op_sel_hi:[1,0,1]\n " \
  "v_pk_fma_f32 v[36:37], v[36:37], v[46:47], v[46:47] op_sel:[0,0,1] op_sel_hi:[1,0,1]\n " \
  "v_pk_fma_f32 v[38:39], v[38:39], v[46:47], v[46:47] op_sel:[0,0,1] op_sel_hi:[1,0,1]\n ds_write_b128 v55, v[228:231] offset:9216\n " \
  "v_exp_f32 v112, v112\n v_pk_mul_f32 v[172:173], v[172:173], v[168:169] op_sel_hi:[1,0]\n v_exp_f32 v113, v113\n " \
  "v_pk_mul_f32 v[174:175], v[174:175], v[168:169] op_sel_hi:[1,0]\n v_exp_f32 v114, v114\n v_cvt_pk_bf16_f32 v172, v172, v173\n v_exp_f32 v115, v115\n " \
  "v_cvt_pk_bf16_f32 v173, v174, v175\n v_exp_f32 v116, v116\n ds_write_b64 v58, v[172:173] offset:0\n v_exp_f32 v117, v117\n " \
  "v_pk_mul_f32 v[80:81], v[80:81], v[168:169] op_sel_hi:[1,0]\n v_exp_f32 v118, v118\n v_pk_mul_f32 v[82:83], v[82:83], v[168:169] op_sel_hi:[1,0]\n " \
  "v_exp_f32 v119, v119\n v_cvt_pk_bf16_f32 v80, v80, v81\n v_exp_f32 v244, v244\n v_cvt_pk_bf16_f32 v81, v82, v83\n v_exp_f32 v245, v245\n " \
  "ds_write_b64 v58, v[80:81] offset:32\n v_exp_f32 v246, v246\n v_pk_mul_f32 v[200:201], v[200:201], v[168:169] op_sel:[0,1]\n v_exp_f32 v247, v247\n " \
  "v_pk_mul_f32 v[202:203], v[202:203], v[168:169] op_sel:[0,1]\n v_exp_f32 v36, v36\n v_cvt_pk_bf16_f32 v200, v200, v201\n v_exp_f32 v37, v37\n " \
  "v_cvt_pk_bf16_f32 v201, v202, v203\n v_exp_f32 v38, v38\n ds_write_b64 v58, v[200:201] offset:64\n v_exp_f32 v39, v39\n " \
  "v_pk_mul_f32 v[204:205], v[204:205], v[168:169] op_sel:[0,1]\n v_pk_mul_f32 v[206:207], v[206:207], v[168:169] op_sel:[0,1]\n " \
  "v_cvt_pk_bf16_f32 v204, v204, v205\n v_cvt_pk_bf16_f32 v205, v206, v207\n ds_write_b64 v58, v[204:205] offset:96\n " \
  "v_pk_add_f32 v[216:217], v[220:221], v[222:223]\n v_pk_add_f32 v[218:219], v[124:125], v[126:127]\n v_pk_add_f32 v[216:217], v[216:217], v[218:219]\n " \
  "v_add_f32 v192, v216, v217\n v_pk_add_f32 v[216:217], v[128:129], v[130:131]\n v_pk_add_f32 v[218:219], v[132:133], v[134:135]\n " \
  "v_pk_add_f32 v[216:217], v[216:217], v[218:219]\n v_add_f32 v193, v216, v217\n v_pk_add_f32 v[216:217], v[244:245], v[246:247]\n " \
  "v_pk_add_f32 v[218:219], v[36:37], v[38:39]\n v_pk_add_f32 v[216:217], v[216:217], v[218:219]\n v_add_f32 v194, v216, v217\n " \
  "v_pk_add_f32 v[216:217], v[112:113], v[114:115]\n v_pk_add_f32 v[218:219], v[116:117], v[118:119]\n v_pk_add_f32 v[216:217], v[216:217], v[218:219]\n " \
  "v_add_f32 v195, v216, v217\n v_pk_mul_f32 v[196:197], v[196:197], v[170:171] op_sel_hi:[1,0]\n " \
  "v_pk_mul_f32 v[198:199], v[198:199], v[170:171] op_sel_hi:[1,0]\n v_permlane16_swap_b32 v192, v193\n v_permlane16_swap_b32 v194, v195\n " \
  "v_cvt_pk_bf16_f32 v196, v196, v197\n v_cvt_pk_bf16_f32 v197, v198, v199\n v_add_f32 v192, v192, v193\n v_add_f32 v194, v194, v195\n " \
  "ds_write_b64 v58, v[196:197] offset:2304\n v_pk_mul_f32 v[84:85], v[84:85], v[170:171] op_sel_hi:[1,0]\n v_permlane32_swap_b32 v192, v194\n " \
  "v_pk_mul_f32 v[86:87], v[86:87], v[170:171] op_sel_hi:[1,0]\n v_cvt_pk_bf16_f32 v84, v84, v85\n v_add_f32 v192, v192, v194\n " \
  "v_cvt_pk_bf16_f32 v85, v86, v87\n ds_write_b64 v58, v[84:85] offset:2336\n v_rcp_f32 v192, v192\n " \
  "v_pk_mul_f32 v[248:249], v[248:249], v[170:171] op_sel:[0,1]\n v_pk_mul_f32 v[250:251], v[250:251], v[170:171] op_sel:[0,1]\n v_mov_b32 v194, v192\n " \
  "v_cvt_pk_bf16_f32 v248, v248, v249\n v_cvt_pk_bf16_f32 v249, v250, v251\n v_permlane32_swap_b32 v192, v194\n " \
  "ds_write_b64 v58, v[248:249] offset:2368\n v_pk_mul_f32 v[252:253], v[252:253], v[170:171] op_sel:[0,1]\n v_mov_b32 v193, v192\n " \
  "v_mov_b32 v195, v194\n v_pk_mul_f32 v[254:255], v[254:255], v[170:171] op_sel:[0,1]\n v_cvt_pk_bf16_f32 v252, v252, v253\n " \
  "v_permlane16_swap_b32 v192, v193\n v_permlane16_swap_b32 v194, v195\n v_cvt_pk_bf16_f32 v253, v254, v255\n ds_write_b64 v58, v[252:253] offset:2400\n " \
  "v_pk_mul_f32 v[220:221], v[220:221], v[192:193] op_sel_hi:[1,0]\n v_pk_mul_f32 v[222:223], v[222:223], v[192:193] op_sel_hi:[1,0]\n " \
  "v_cvt_pk_bf16_f32 v220, v220, v221\n v_cvt_pk_bf16_f32 v221, v222, v223\n ds_write_b64 v58, v[220:221] offset:18432\n " \
  "v_pk_mul_f32 v[124:125], v[124:125], v[192:193] op_sel_hi:[1,0]\n v_pk_mul_f32 v[126:127], v[126:127], v[192:193] op_sel_hi:[1,0]\n " \
  "v_cvt_pk_bf16_f32 v124, v124, v125\n v_cvt_pk_bf16_f32 v125, v126, v127\n ds_write_b64 v58, v[124:125] offset:18464\n " \
  "v_pk_mul_f32 v[128:129], v[128:129], v[192:193] op_sel:[0,1]\n v_pk_mul_f32 v[130:131], v[130:131], v[192:193] op_sel:[0,1]\n " \
  "v_cvt_pk_bf16_f32 v128, v128, v129\n v_cvt_pk_bf16_f32 v129, v130, v131\n ds_write_b64 v58, v[128:129] offset:18496\n " \
  "v_pk_mul_f32 v[132:133], v[132:133], v[192:193] op_sel:[0,1]\n v_pk_mul_f32 v[134:135], v[134:135], v[192:193] op_sel:[0,1]\n " \
  "v_cvt_pk_bf16_f32 v132, v132, v133\n v_cvt_pk_bf16_f32 v133, v134, v135\n ds_write_b64 v58, v[132:133] offset:18528\n " \
  "v_pk_mul_f32 v[244:245], v[244:245], v[194:195] op_sel_hi:[1,0]\n v_pk_mul_f32 v[246:247], v[246:247], v[194:195] op_sel_hi:[1,0]\n " \
  "v_cvt_pk_bf16_f32 v244, v244, v245\n v_cvt_pk_bf16_f32 v245, v246, v247\n ds_write_b64 v58, v[244:245] offset:20736\n " \
  "v_pk_mul_f32 v[36:37], v[36:37], v[194:195] op_sel_hi:[1,0]\n v_pk_mul_f32 v[38:39], v[38:39], v[194:195] op_sel_hi:[1,0]\n " \
  "v_cvt_pk_bf16_f32 v36, v36, v37\n v_cvt_pk_bf16_f32 v37, v38, v39\n ds_write_b64 v58, v[36:37] offset:20768\n " \
  "v_pk_mul_f32 v[112:113], v[112:113], v[194:195] op_sel:[0,1]\n v_pk_mul_f32 v[114:115], v[114:115], v[194:195] op_sel:[0,1]\n " \
  "v_cvt_pk_bf16_f32 v112, v112, v113\n v_cvt_pk_bf16_f32 v113, v114, v115\n ds_write_b64 v58, v[112:113] offset:20800\n " \
  "v_pk_mul_f32 v[116:117], v[116:117], v[194:195] op_sel:[0,1]\n v_pk_mul_f32 v[118:119], v[118:119], v[194:195] op_sel:[0,1]\n " \
  "v_cvt_pk_bf16_f32 v116, v116, v117\n v_cvt_pk_bf16_f32 v117, v118, v119\n ds_write_b64 v58, v[116:117] offset:20832\n "
#define G1_OUTPUT \
  "s_mov_b32 m0, 0x40000\n s_branch .Lg1p%=\n .Lg1q%=:\n s_sleep 1\n s_sub_u32 m0, m0, 1\n s_cbranch_scc1 .Lg1r%=\n .Lg1p%=:\n " \
  "buffer_load_dwordx4 v[216:219], %[fz], %[rs], 0 offen sc1\n buffer_load_dwordx4 v[220:223], %[fz], %[rs], 0 offen offset:64 sc1\n " \
  "buffer_load_dwordx4 v[200:203], %[fo], %[rs], 0 offen offset:0 sc1\n buffer_load_dwordx4 v[204:207], %[fo], %[rs], 0 offen offset:2048 sc1\n " \
  "buffer_load_dwordx4 v[208:211], %[fo], %[rs], 0 offen offset:1024 sc1\n buffer_load_dwordx4 v[212:215], %[fo], %[rs], 0 offen offset:3072 sc1\n " \
  "s_waitcnt vmcnt(5)\n v_max_u32 v152, v216, v217\n v_max3_u32 v152, v218, v219, v152\n s_waitcnt vmcnt(4)\n v_max3_u32 v152, v220, v221, v152\n " \
  "v_max3_u32 v152, v222, v223, v152\n s_waitcnt vmcnt(3)\n v_max3_u32 v152, v200, v201, v152\n v_max3_u32 v152, v202, v203, v152\n s_waitcnt vmcnt(2)\n " \
  "v_max3_u32 v152, v204, v205, v152\n v_max3_u32 v152, v206, v207, v152\n s_waitcnt vmcnt(1)\n v_max3_u32 v152, v208, v209, v152\n " \
  "v_max3_u32 v152, v210, v211, v152\n s_waitcnt vmcnt(0)\n v_max3_u32 v152, v212, v213, v152\n v_max3_u32 v152, v214, v215, v152\n " \
  "v_cmp_eq_u32 vcc, -1, v152\n s_cbranch_vccnz .Lg1q%=\n .Lg1r%=:\n v_mfma_f32_16x16x32_bf16 v[160:163], v[216:219], v[224:227], 0\n " \
  "v_mfma_f32_16x16x32_bf16 v[160:163], v[220:223], v[228:231], v[160:163]\n v_mfma_f32_16x16x32_bf16 v[152:155], v[200:203], v[224:227], 0\n " \
  "v_mfma_f32_16x16x32_bf16 v[152:155], v[204:207], v[228:231], v[152:155]\n v_mfma_f32_16x16x32_bf16 v[156:159], v[208:211], v[224:227], 0\n " \
  "v_mfma_f32_16x16x32_bf16 v[156:159], v[212:215], v[228:231], v[156:159]\n v_mfma_f32_16x16x32_bf16 v[172:175], v[216:219], v[232:235], 0\n " \
  "v_mfma_f32_16x16x32_bf16 v[172:175], v[220:223], v[236:239], v[172:175]\n v_mfma_f32_16x16x32_bf16 v[164:167], v[200:203], v[232:235], 0\n " \
  "v_mfma_f32_16x16x32_bf16 v[164:167], v[204:207], v[236:239], v[164:167]\n v_mfma_f32_16x16x32_bf16 v[168:171], v[208:211], v[232:235], 0\n " \
  "v_add_f32 v160, 0x358637bd, v160\n v_rcp_f32 v160, v160\n v_mfma_f32_16x16x32_bf16 v[168:171], v[212:215], v[236:239], v[168:171]\n " \
  "v_mul_f32 v152, v160, v152\n v_mfma_f32_16x16x32_bf16 v[184:187], v[216:219], v[240:243], 0\n v_mul_f32 v153, v160, v153\n " \
  "v_cvt_pk_bf16_f32 v152, v152, v153\n v_mul_f32 v154, v160, v154\n v_mfma_f32_16x16x32_bf16 v[184:187], v[220:223], v[244:247], v[184:187]\n " \
  "v_mul_f32 v155, v160, v155\n v_cvt_pk_bf16_f32 v153, v154, v155\n v_mul_f32 v156, v160, v156\n " \
  "v_mfma_f32_16x16x32_bf16 v[176:179], v[200:203], v[240:243], 0\n v_mul_f32 v157, v160, v157\n v_cvt_pk_bf16_f32 v154, v156, v157\n " \
  "v_mul_f32 v158, v160, v158\n v_mfma_f32_16x16x32_bf16 v[176:179], v[204:207], v[244:247], v[176:179]\n v_mul_f32 v159, v160, v159\n " \
  "v_cvt_pk_bf16_f32 v155, v158, v159\n buffer_store_dwordx4 v[152:155], %[go], %[rs], 0 offen offset:0 sc1\n " \
  "v_mfma_f32_16x16x32_bf16 v[180:183], v[208:211], v[240:243], 0\n ds_write_b128 %[lo], v[152:155] offset:0\n v_add_f32 v172, 0x358637bd, v172\n " \
  "v_mfma_f32_16x16x32_bf16 v[180:183], v[212:215], v[244:247], v[180:183]\n v_rcp_f32 v172, v172\n " \
  "v_mfma_f32_16x16x32_bf16 v[196:199], v[216:219], v[248:251], 0\n v_mul_f32 v164, v172, v164\n v_mul_f32 v165, v172, v165\n " \
  "v_cvt_pk_bf16_f32 v164, v164, v165\n v_mfma_f32_16x16x32_bf16 v[196:199], v[220:223], v[252:255], v[196:199]\n v_mul_f32 v166, v172, v166\n " \
  "v_mul_f32 v167, v172, v167\n v_cvt_pk_bf16_f32 v165, v166, v167\n v_mfma_f32_16x16x32_bf16 v[188:191], v[200:203], v[248:251], 0\n " \
  "v_mul_f32 v168, v172, v168\n v_mul_f32 v169, v172, v169\n v_cvt_pk_bf16_f32 v166, v168, v169\n " \
  "v_mfma_f32_16x16x32_bf16 v[188:191], v[204:207], v[252:255], v[188:191]\n v_mul_f32 v170, v172, v170\n v_mul_f32 v171, v172, v171\n " \
  "v_cvt_pk_bf16_f32 v167, v170, v171\n v_mfma_f32_16x16x32_bf16 v[192:195], v[208:211], v[248:251], 0\n " \
  "buffer_store_dwordx4 v[164:167], %[go], %[rs], 0 offen offset:1024 sc1\n ds_write_b128 %[lo], v[164:167] offset:1024\n " \
  "v_mfma_f32_16x16x32_bf16 v[192:195], v[212:215], v[252:255], v[192:195]\n v_add_f32 v184, 0x358637bd, v184\n v_rcp_f32 v184, v184\n s_nop 0\n " \
  "v_pk_mul_f32 v[176:177], v[176:177], v[184:185] op_sel_hi:[1,0]\n v_pk_mul_f32 v[178:179], v[178:179], v[184:185] op_sel_hi:[1,0]\n " \
  "v_pk_mul_f32 v[180:181], v[180:181], v[184:185] op_sel_hi:[1,0]\n v_pk_mul_f32 v[182:183], v[182:183], v[184:185] op_sel_hi:[1,0]\n " \
  "v_cvt_pk_bf16_f32 v176, v176, v177\n v_cvt_pk_bf16_f32 v177, v178, v179\n v_cvt_pk_bf16_f32 v178, v180, v181\n v_cvt_pk_bf16_f32 v179, v182, v183\n " \
  "buffer_store_dwordx4 v[176:179], %[go], %[rs], 0 offen offset:2048 sc1\n ds_write_b128 %[lo], v[176:179] offset:2048\n " \
  "v_add_f32 v196, 0x358637bd, v196\n v_rcp_f32 v196, v196\n s_nop 0\n v_pk_mul_f32 v[188:189], v[188:189], v[196:197] op_sel_hi:[1,0]\n " \
  "v_pk_mul_f32 v[190:191], v[190:191], v[196:197] op_sel_hi:[1,0]\n v_pk_mul_f32 v[192:193], v[192:193], v[196:197] op_sel_hi:[1,0]\n " \
  "v_pk_mul_f32 v[194:195], v[194:195], v[196:197] op_sel_hi:[1,0]\n v_cvt_pk_bf16_f32 v188, v188, v189\n v_cvt_pk_bf16_f32 v189, v190, v191\n " \
  "v_cvt_pk_bf16_f32 v190, v192, v193\n v_cvt_pk_bf16_f32 v191, v194, v195\n buffer_store_dwordx4 v[188:191], %[go], %[rs], 0 offen offset:3072 sc1\n " \
  "ds_write_b128 %[lo], v[188:191] offset:3072\n "
#define G1_PURGE \
  ".purgem G1M\n .purgem G1RA\n .purgem G1RB\n .purgem G1BK\n .purgem G1AK\n .purgem G1RDB\n .purgem G1RDA\n .purgem G1Q\n .purgem G1SG\n "
#define G1_CLOBBER \
  "v80", "v81", "v82", "v83", "v84", "v85", "v86", "v87", "v92", "v93", "v94", "v95", "v96", "v97", "v98", "v99", "v100", "v101", "v102", "v103", \
  "v112", "v113", "v114", "v115", "v116", "v117", "v118", "v119", "v120", "v121", "v122", "v123", "v124", "v125", "v126", "v127", "v128", "v129", \
  "v130", "v131", "v132", "v133", "v134", "v135", "v144", "v145", "v146", "v147", "v152", "v153", "v154", "v155", \
  "v156", "v157", "v158", "v159"

template <int MODE>
__device__ __forceinline__ void g1_phase(char* smem, char* ws, u64& E, int w, f32x16& xq, Next& nx, int layer, const u16* __restrict__ x0, const u16* __restrict__ W,
                                         const u16* __restrict__ wo, const u16* __restrict__ wphi,
                                         const u16* __restrict__ wn1, const u16* __restrict__ wn2) {
  constexpr bool HH = (MODE == 1);
  constexpr int ABYTES = 128 * 128;
  constexpr int STAGE = ABYTES + 3 * 8192;
  constexpr int OFF_FX = 16384;
  constexpr int OFF_SP = 3 * STAGE;
  constexpr int OFF_RS = !HH ? 159744 : OFF_SP + 16384;
  constexpr int OFF_CS = OFF_RS + (HH ? 1152 : 1024);
  constexpr int OFF_WP = OFF_CS + 1536;
  constexpr int OFF_JK = LDS_TOTAL - 1024;
  constexpr int OFF_PK = 0;
  constexpr int OFF_VF = OFF_WP + 4096;
  constexpr int OFF_R2 = HH ? OFF_VF + 16384 : OFF_CS + 1024;
  constexpr int OFF_RD = OFF_CS + 768;
  constexpr int PSTR = 144;
  IDS
  W = uni(W); wphi = uni(wphi); wn1 = uni(wn1); wn2 = uni(wn2);
  const int kg = w >> 2, wm = (w >> 1) & 1, wn = w & 1;
  const bool first = !HH && layer == 0;
  f32x4 acc[2][2], qh[2][2], csq[2], st01, st23;
  bf16x8 wf[2];
  u64 kol;
  u32x4 pw[8];
  ST(layer * 24 + 0)
  const int R0 = wm * 64;
  u32x16 pa;
#define pcs pa[0]
#define prs pa[1]
#define opo pa[2]
#define ogo pa[3]
#define hof pa[7]
#define dm pa[8]
#define dm2 pa[9]
#define pcv pa[4]
#define pwf pa[5]
#define prv pa[6]
#define pvf pa[7]
#define ppk pa[10]
#define ppa pa[11]
#define pvr pa[12]
#define gkv pa[13]
#define gkz pa[14]
#define pwl pa[15]
#define pwm pa[4]
#define plb pa[10]
#define prd pa[11]
#define dm3 pa[12]
  f32x4 bias[3];
  const char* wos = wo_src(ws, wo, layer, hp);
  const unsigned tO = img(layer, hp) ? 8192u : 131072u;
  const unsigned sb = lds_addr(smem);
  auto prep = [&](auto ldr) {
    constexpr bool LDR = decltype(ldr)::value;
    if constexpr (!LDR) {
      pcs = sb + OFF_CS + wn * 128 + g * 16;
      prs = sb + OFF_RS + (R0 + fr) * 8;
      opo = (unsigned)(wn * 8192 + R0 * 64 + lane * 16);
      ogo = opo + WS_O + layer * (SEQ * DM * 2) + (unsigned)((rt * NH + 2 * hp) * 8192);
      opo += sb;
      pcv = sb + OFF_CS + (wn * 32 + 128 + fr) * 4;
      prv = sb + OFF_R2 + (R0 + 4 * g) * 4;
    } else {
      constexpr unsigned kvn = WS_KV + MODE * KVLB;
      const int t = tid & 255;
      dm = kvn + (unsigned)((rt * NH + 2 * hp) * KVPB) + t * 16;
      dm2 = t < 16 ? dm + 8192 : t >= 64 && t < 97 ? kvn + 16u * NH * KVPB + (unsigned)(hp * KVFB + rt * 528 + (t - 64) * 16) : 0x7fff0000u;
      if (!HH) dm3 = WS_HL + (unsigned)((256 + rt * 16 + hp) * 4096 + t * 16);
    }
    if constexpr (!HH) {
      prd = sb + OFF_RD + wn * 4;
      if constexpr (!LDR) {
        plb = sb + OFF_FX + wn * 2048 + lane * 16;
        hof = m0 + wm == 0 ? 0x7fff0000u : WS_HL + (unsigned)((layer * 128 + rt * 16 - 16 + wm * 16 + hp) * 4096 + wn * 2048 + lane * 16);
        const unsigned out = ~((0x5555u << 2 << fr) >> (4 * g));
#pragma unroll
        for (int i = 0; i < 8; ++i) {
          unsigned t;
          asm("v_bfe_i32 %0, %1, %2, 1" : "=v"(t) : "v"(out), "n"((i >> 2) * 16 + (i & 3)));
          bias[i >> 2][i & 3] = __uint_as_float(t & 0xff61b1e6u);
        }
        bias[2] = m0 + R0 == 0 ? f32x4{-3.0e38f, -3.0e38f, -3.0e38f, -3.0e38f} : bias[0];
        asm volatile("" : "+{v[36:39]}"(bias[0]), "+{v[40:43]}"(bias[1]), "+{v[44:47]}"(bias[2]));
      }
    } else {
      const int u = wm * 2 + kg;
      if constexpr (!LDR) {
        pwf = sb + OFF_WP + wn * 2048 + (4 * g + q4) * 64 + p4 * 8;
        pvf = sb + OFF_VF + w * 2048 + lane * 16;
        ppk = sb + OFF_PK + w * (32 * PSTR) + fr * PSTR + g * 8;
      }
      ppa = sb + OFF_PK + wn * (32 * PSTR) + (4 * g + q4) * PSTR + u * 32 + p4 * 8;
      pvr = sb + OFF_VF + wn * 2048 + lane * 16;
      const unsigned doff = WS_KV + (layer >> 1) * KVLB + (unsigned)((rt * NH + 2 * hp + wn) * KVPB);
      gkv = doff + u * 1024 + lane * 16;
      gkz = fr == 0 ? doff + 4096 + u * 32 + g * 8 : 0x7fff0000u;
    }
    asm volatile("" : "+{v[48:63]}"(pa));
  };

  if (kg == 1) {

    const int lw = w & 3;
    const int kB = 16 * lw + (lane >> 3);
    const int fB = (kB >> 1) & 1;
    const int cB0 = ((((lane & 7) >> 1) ^ fB) << 1) | (lane & 1), cB1 = ((((lane & 7) >> 1) ^ (fB | 2)) << 1) | (lane & 1);
    const bool im = img(layer, hp);

    unsigned vW0, vW1, vP, vX, vY;
    u64 ko2;
    auto ld_pw = [&](int i0) {
#pragma unroll
      for (int i = i0; i < i0 + 4; ++i) gld16(pw[i], vP, wn1 + i * 8 * 3072);
    };
    auto wo_tile = [&](int t) { glds2v(wos + knib(ko2, t) * tO, vW0, vW1, lds_addr(smem) + OFF_WO + t * 8192 + lw * 2048); };
    auto run = [&](auto fst) {
      constexpr bool FIRST = decltype(fst)::value;
      constexpr int N = 10;
      const unsigned vA = FIRST ? (unsigned)(((m0 + lw * 32 + (lane >> 3)) * DM + ((lane & 7) ^ (lane >> 3)) * 8) * 2)
                                : (unsigned)(rt * 262144 + lw * 4096 + lane * 16);
      unsigned imv = !FIRST && im ? ~0u : 0u;
      if (!FIRST) asm volatile("" : "+v"(imv));
      const unsigned vB0 = ((unsigned)(lw * 6144 + lane * 16) & imv) | ((unsigned)((kB * 3072 + hp * 64 + cB0 * 8) * 2) & ~imv);
      const unsigned vB1 = ((unsigned)(lw * 6144 + lane * 16) & imv) | (((unsigned)(((kB + 8) * 3072 + hp * 64 + cB1 * 8) * 2) - 1024u) & ~imv);
      const char* xa = FIRST ? (const char*)x0 : ws + WS_X + (layer - 1) * (SEQ * DM * 2);
      constexpr int KS = FIRST ? 128 : 16384;
      const u64 kord = FIRST ? korder0(rt) : nx.kord;
      auto ktile = [&](int ks) { return knib(kord, ks); };
      auto stageA = [&](int ks, int boff) {
        const int kt = ktile(ks);
        const unsigned sA = lds_addr(smem) + boff;
        if (FIRST) glds4v(xa + kt * KS, vA, vA + 15360u, vA + 2 * 15360u, vA + 3 * 15360u, sA + lw * 4096);
        else glds4c(xa + kt * KS, vA, sA + lw * 4096);
      };
      auto stageB = [&](const u16* Wp, int ks, int boff) {
        const int kt = ktile(ks);
        const unsigned sB = lds_addr(smem) + boff + ABYTES;
        const char* b = FIRST ? (const char*)(Wp + (size_t)kt * 64 * 3072) : nx.wq + kt * nx.tw;
        glds4v(b, vB0, vB1, vB0, vB1, sB + lw * 6144);
        glds2v(b + 4096, vB0, vB1, sB + lw * 6144 + 4096);
      };
      bf16x8 sel[3];
      unsigned cso[3], cst;
      if (FIRST) {
        stageA(1, STAGE); stageB(W, 1, STAGE);
        __builtin_amdgcn_sched_barrier(0);
        const int fl = (q4 >> 1) | ((g & 1) << 1), j = 3 * wm + (fr < 3 ? fr : 0);
#pragma unroll
        for (int i = 0; i < 3; ++i) {
          const unsigned o = fr == i ? 0x3F803F80u : 0u;
          sel[i] = __builtin_bit_cast(bf16x8, u32x4{o, o, o, o});
          cso[i] = (unsigned)(2 * STAGE + ABYTES + (g >> 1) * 6144 + (g & 1) * 1024 + q4 * 128 + p4 * 8 + (((wn * 2 + ((wm + i) & 1)) ^ fl) << 5) + ((3 * wm + i) >> 1) * 2048);
          asm volatile("" : "+v"(sel[i]), "+v"(cso[i]));
        }
        cst = fr < 3 ? WS_CPA + (unsigned)(((hp * 16 + rt) * 192 + (j >> 1) * 64 + wn * 32 + (j & 1) * 16 + 4 * g) * 4) : 0x7fff0000u;
        asm volatile("" : "+v"(cst));
        WAITVN(N);
        BAR;
        stageA(2, 2 * STAGE); stageB(W, 2, 2 * STAGE);
      }
      u64 fall, fal2;
      unsigned fao, fa2, vS, vC, dS, dC, dR;
      auto setup = [&]() {
        fao = (unsigned)((256 + rt * 16 + (lane & 15)) * FLS);
        fa2 = (unsigned)(((FIRST ? 512 : 256) + (lane & 15) * 16 + (FIRST ? hp : (hp - ((1 << (lane >> 4)) >> 1)) & 15)) * FLS);
        vP = (unsigned)((rt * 64 + (lane >> 3)) * 6144 + lw * 2048 + ((hp + (1 << lw)) & 15) * 128 + (lane & 7) * 16);
        vS = FIRST ? (unsigned)(WS_G0 + (m0 + lw * 32 + (lane & 31)) * 16)
                   : WS_ST + (unsigned)((layer - 1) * (SEQ * 128) + rt * 16384 + g * 1024 + lw * 256 + fr * 16);
        vC = (FIRST ? WS_CPA : WS_CSP) + (unsigned)(hp * 12288 + g * 768 + lw * 192 + (fr < 12 ? fr : 0) * 16);
        if (!FIRST) vX = HH ? (unsigned)(hp * 4096 + lw * 1024 + lane * 16) : (unsigned)((rt * 16 + hp) * 1024 + lane * 16);
        if (HH) { vY = (unsigned)(bid * 8192 + lw * 2048 + lane * 16); asm volatile("" : "+v"(vY)); }
        vW0 = ((unsigned)(lw * 2048 + lane * 16) & imv) | ((unsigned)((kB * DM + hp * 64 + ((lane & 7) ^ ((lane >> 4) << 1)) * 8) * 2) & ~imv); vW1 = vW0 + ((8 * DM * 2 - 1024u) & ~imv);
        dS = OFF_RS + lw * 256 + (FIRST ? (lane & 31) * 8 : fr * 16 + (g & 1) * 8); dC = OFF_CS + lw * 192 + (fr < 12 ? fr : 0) * 16 + g * 4;
        ko2 = korder(bid & 3, hp);
        dR = OFF_R2 + lw * 128 + (FIRST ? (lane & 31) * 4 : fr * 8 + (g & 1) * 4);
        asm volatile("" : "+v"(dR));
        asm volatile("" : "+v"(vS), "+v"(vX), "+v"(vW0), "+v"(vW1), "+v"(dS));
        asm volatile("" : "+s"(ko2));
        if (FIRST || HH) asm volatile("" : "+v"(vC), "+v"(dC), "+v"(fa2));
        if (FIRST) asm volatile("" : "+v"(vP)); else asm volatile("" : "+v"(fao));
      };
      if (!FIRST) setup();
      u32x4 sv[4], cv[4];
      auto ld_c = [&](u32x4& d, unsigned v, int so) { asm volatile("buffer_load_dwordx4 %0, %1, %2, %3 offen sc1" : "=v"(d) : "v"(v), "s"(rsW), "s"(so) : "memory"); };
      float tm, tr;
      unsigned tc;
      auto fin = [&]() {
        if (FIRST) {
          const unsigned tag = (unsigned)E + 1u;
          asm volatile("" : "+v"(sv[0]));
          _Pragma("unroll 1") for (unsigned spins = 0; spins < (1u << 18); ++spins) {
            if (__all(sv[0][1] == tag && sv[0][3] == tag)) break;
            __builtin_amdgcn_s_sleep(1);
            asm volatile("buffer_load_dwordx4 %0, %1, %2, 0 offen sc1\n\ts_waitcnt vmcnt(0)" : "=v"(sv[0]) : "v"(vS), "s"(rsW) : "memory");
          }
          tm = __uint_as_float(sv[0][0]); tr = __uint_as_float(sv[0][2]);
        } else {
          asm volatile("" : "+v"(sv[0]), "+v"(sv[1]), "+v"(sv[2]), "+v"(sv[3]));
          const f32x4 t = sum4(sv);
          const unsigned u = xs4(t[0], t[2], t[1], t[3]);
          auto h = __builtin_amdgcn_permlane32_swap(u, u, false, false);
          const float mean = __uint_as_float(h[0]) * (1.0f / 1024.0f);
          const float var = fmaxf(__uint_as_float(h[1]) * (1.0f / 1024.0f) - mean * mean, 0.f);
          tm = -mean; tr = __builtin_amdgcn_rsqf(var + 1e-5f);
        }
      };
      auto cfin = [&]() {
        if (FIRST || HH) {
          asm volatile("" : "+v"(cv[0]), "+v"(cv[1]), "+v"(cv[2]), "+v"(cv[3]));
          const f32x4 t = sum4(cv);
          tc = xs4(t[0], t[1], t[2], t[3]);
        }
      };
      bf16x8 cf[6];
      f32x4 csum = {0, 0, 0, 0};
      if (FIRST) { fall = E + 1u; vX = (unsigned)(bid * 8192 + lw * 2048 + lane * 16); asm volatile("" : "+v"(fall), "+v"(vX)); }
#pragma unroll
      for (int kt = 0; kt < 13; ++kt) {
        const int cur = (kt % 3) * STAGE;
        if (!FIRST && kt == 0) { WAITV(4); }
        else if (FIRST && kt == 4 && lw == 1) { WAITVN(N + 2); }
        else if (HH && (kt == 4 || kt == 6) && true) { WAITVN(N + 1); }
        else if (HH && kt == 5 && true) { WAITVN(N + 2); }
        else if ((!FIRST && kt == 1 && lw == 3) || ((FIRST || HH) && kt == 10 && lw == 3) || (FIRST && (kt == 2 || kt == 4 || kt == 5))) { WAITVN(N + 1); }
        else { WAITVN(N); }
        if (!FIRST && kt == 2 && lw == 3) {
          asm volatile("" : "+v"(fall));
          if (__any(fall != E + (unsigned)layer)) poll_flags(ws, E, 1, rt, hp, lane, layer, 0xffffull);
        }
        if (HH && kt == 11 && lw == 3) {
          asm volatile("" : "+v"(fal2));
          if (__any(fal2 != E + (unsigned)layer)) {
            _Pragma("unroll 1") for (int j = 0; j < 4; ++j) poll_flags(ws, E, 1, rt, (hp - ((1 << j) >> 1)) & 15, lane, layer, 0xffff00000000ull);
          }
        }
        if (FIRST && kt == 11 && lw == 3) {
          asm volatile("" : "+v"(fal2));
          if (__any(fal2 != E + 1u)) poll_flags(ws, E, 2, rt, hp, lane, 1u, 0xffff00000000ull);
        }
        BAR;
        if (FIRST && kt == 1) {
          asm volatile("" ::: "memory");
#pragma unroll
          for (int i = 0; i < 6; ++i) { const char* t = smem + cso[i >> 1] + (i & 1) * 12288; cf[i] = cat8(tr_read(t), tr_read(t + 512)); }
        }
        if (FIRST && kt >= 2 && kt < 6 && lw == kt - 2) setup();
        if (kt >= 6 && kt < 10 && lw == kt - 6) prep(IC<1>{});
        stageA(kt + 3, cur);
        if (FIRST && kt == 1) {
          asm volatile("" : "+v"(sel[0]));
#pragma unroll
          for (int i = 0; i < 6; ++i) csum = MFMA(cf[i], sel[i >> 1], csum);
          asm volatile("" : "+v"(csum));
        }
        stageB(W, kt + 3, cur);
        if (FIRST) {
          if (kt == 1) __builtin_amdgcn_raw_buffer_store_b128(__builtin_bit_cast(u32x4, csum), rsW, cst, 0, 16);
          if (kt == 3 && tid == 320) __builtin_amdgcn_raw_buffer_store_b64(__builtin_bit_cast(u32x2, fall), rsW, WS_FLAGS + (unsigned)(512 + rt * 16 + hp) * FLS, 0, 16);
          if (kt == 3 || kt == 4) glds16s(wo + (kt - 3) * 512, vX, lds_addr(smem) + OFF_JK);
        }
        if (HH && (kt == 3 || kt == 4) && true) glds16s(wo + (kt - 3) * 512, vY, lds_addr(smem) + OFF_JK);
        if (!FIRST && kt == 0 && lw == 3) asm volatile("global_load_dwordx2 %0, %1, %2 sc1" : "=v"(fall) : "v"(fao), "s"(ws + WS_FLAGS) : "memory");
        if ((FIRST || HH) && kt == 9 && lw == 3) asm volatile("global_load_dwordx2 %0, %1, %2 sc1" : "=v"(fal2) : "v"(fa2), "s"(ws + WS_FLAGS) : "memory");
      }
      WAITVN(N);
      BAR;
#pragma unroll
      for (int i = 0; i < (FIRST ? 1 : 4); ++i) ld_c(sv[i], vS, i * 4096);
      if (FIRST || HH) {
#pragma unroll
        for (int i = 0; i < 4; ++i) ld_c(cv[i], vC, i * 3072);
      }
      if (HH) glds16s(wphi, vX, lds_addr(smem) + OFF_WP + lw * 1024);
      else if (!FIRST) glds16c(ws + WS_CSA, vX, lds_addr(smem) + OFF_CS);
      wo_tile(0); wo_tile(1);
      WAITVN(HH ? 13 : 9);
      BAR;
      if (FIRST) {
        if (lw < 3) { ld_pw(0); ld_pw(4); WAITV(12); } else { WAITV(4); }
      } else {
        WAITV(4);
      }
      cfin();
      fin();
      asm volatile("" : "+v"(tm), "+v"(tr));
      if (FIRST || HH) asm volatile("" : "+v"(tc));
      if (FIRST && lw < 3) { WAITV(8); } else { WAITV(0); }
      lds<f32x2>(sb + dS) = f32x2{tm, tr};
      lds<float>(sb + dR) = tm; lds<float>(sb + dR + 512) = tr;
      if (FIRST || HH) lds<unsigned>(sb + dC) = tc;
      if (!HH) lds<unsigned>(prd) = 0u;
      WAITL0;
    };
    typedef std::integral_constant<bool, true> T_;
    typedef std::integral_constant<bool, false> F_;
    __builtin_amdgcn_s_setprio(3);
    if (first) run(T_{}); else run(F_{});
    __builtin_amdgcn_s_setprio(0);
    BAR;
    if constexpr (HH) {
      u32x4 c0, c1;
      if (layer == 1) {
        const int n = bid * 8 + lw * 2;
        const void* p;
        unsigned a0, a1;
        if (n < 1536) {
          const int r = n % 768, q = r % 384, e = q % 24;
          p = n < 768 ? wn1 : W;
          a0 = (unsigned)((((q / 24) * 64 + (e / 6) * 16 + (lane >> 3)) * 3072 + ((e % 6) >> 1) * 1024 + (r < 384 ? 192 : 704)) * 2);
          a1 = a0 + 8 * 6144 + cB1 * 16; a0 += cB0 * 16;
        } else {
          p = n < 1792 ? wn2 : wo;
          a0 = (unsigned)((((n & 127) * 8 + (lane >> 3)) * DM + ((n & 128) ? 704 : 192) + ((lane & 7) ^ ((lane >> 4) << 1)) * 8) * 2);
          a1 = a0 + 8 * DM * 2;
        }
        p = uni(p);
        gld16(c0, a0, p); gld16(c1, a1, p);
        const u32x4 pz = empty4();
        __builtin_amdgcn_raw_buffer_store_b128(pz, rsW, dm, 0, 16);
        __builtin_amdgcn_raw_buffer_store_b128(pz, rsW, dm, 4096, 16);
        __builtin_amdgcn_raw_buffer_store_b128(pz, rsW, dm2, 0, 16);
      }
#pragma unroll
      for (int t = 2; t < 6; ++t) wo_tile(t);
      if (layer == 1) {
        WAITV(11);
        __builtin_amdgcn_sched_barrier(0);
        asm volatile("" : "+v"(c0), "+v"(c1));
        __builtin_amdgcn_raw_buffer_store_b128(c0, rsW, WS_IMG + (unsigned)(bid * 8192 + lw * 2048 + lane * 16), 0, 16);
        __builtin_amdgcn_raw_buffer_store_b128(c1, rsW, WS_IMG + (unsigned)(bid * 8192 + lw * 2048 + 1024 + lane * 16), 0, 16);
        WAITV(0);
      }
      pwl = vW0; pwm = vW1; kol = ko2;
      asm volatile("" : "=v"(csq[0]), "=v"(csq[1]), "=v"(st01), "=v"(st23), "=v"(wf[0]), "=v"(wf[1]));
      asm volatile("" : "=v"(acc[0][0]), "=v"(acc[0][1]), "=v"(acc[1][0]), "=v"(acc[1][1]));
      asm volatile("" : "=v"(qh[0][0]), "=v"(qh[0][1]), "=v"(qh[1][0]), "=v"(qh[1][1]));
    } else {
      const int lane = lane_id();
#pragma unroll
      for (int t = 2; t < 12; ++t) {
        wo_tile(t);
        if (t == 2 && first) {
        const u32x4 pz = empty4();
        __builtin_amdgcn_raw_buffer_store_b128(pz, rsW, dm, 0, 16);
        __builtin_amdgcn_raw_buffer_store_b128(pz, rsW, dm, 4096, 16);
        __builtin_amdgcn_raw_buffer_store_b128(pz, rsW, dm3, 0, 16);
        __builtin_amdgcn_raw_buffer_store_b128(pz, rsW, dm2, 0, 16);
        if (lw == 3 && lane < 48) __builtin_amdgcn_raw_buffer_store_b128(lds<u32x4>(sb + OFF_CS + lane * 16), rsW, WS_CSA + (unsigned)((rt * 16 + hp) * 1024 + lane * 16), 0, 16);
        }
      }
      if (first) { BAR;   WAITV(0); }
    }
    asm volatile("" : "={v[64:79]}"(xq));
  } else {

    prep(IC<0>{});
    if (first) {
      const int row = m0 + hp * 8 + w * 2 + (lane >> 5);
      asm volatile("s_load_dwordx2 %0, %1, 0x0\n\ts_waitcnt lgkmcnt(0)" : "=s"(E) : "s"(ws) : "memory");
      const unsigned tag = (unsigned)E + 1u;
      static_assert(HH || (OFF_SP == 122880 && OFF_R2 + 1024 <= OFF_JK), "addresses used at the top of the kernel");
      WAITV(0);
      u32x4 xs[4];
#pragma unroll
      for (int i = 0; i < 4; ++i) xs[i] = *(const u32x4*)(smem + OFF_SP + w * 4096 + (lane >> 5) * 2048 + (lane & 31) * 16 + i * 512);
      typedef __attribute__((ext_vector_type(2))) __bf16 bf2;
      const bf2 one2 = __builtin_bit_cast(bf2, 0x3F803F80u);
      float sa[2] = {0.f, 0.f}, sq[2] = {0.f, 0.f};
#pragma unroll
      for (int i = 0; i < 16; ++i) {
        const unsigned u = xs[i >> 2][i & 3];
        const bf2 t = __builtin_bit_cast(bf2, u);
        sa[i & 1] = __builtin_amdgcn_fdot2_f32_bf16(t, one2, sa[i & 1], false);
        sq[i & 1] = __builtin_amdgcn_fdot2_f32_bf16(t, t, sq[i & 1], false);
      }
      float sm = r16_sum(sa[0] + sa[1]), m2 = r16_sum(sq[0] + sq[1]);
      {
        auto r = __builtin_amdgcn_permlane16_swap(__float_as_uint(sm), __float_as_uint(sm), false, false);
        auto q = __builtin_amdgcn_permlane16_swap(__float_as_uint(m2), __float_as_uint(m2), false, false);
        sm = __uint_as_float(r[0]) + __uint_as_float(r[1]); m2 = __uint_as_float(q[0]) + __uint_as_float(q[1]);
      }
      const float mean = sm * (1.0f / 1024.0f);
      m2 = fmaxf(m2 - sm * mean, 0.f);
      u32x4 gw = {__float_as_uint(-mean), tag, __float_as_uint(rsqrtf(m2 * (1.0f / 1024.0f) + 1e-5f)), tag};
      if ((lane & 31) == 0) __builtin_amdgcn_raw_buffer_store_b128(gw, rsW, (unsigned)(WS_G0 + row * 16), 0, 16);
      __builtin_amdgcn_raw_buffer_store_b128(empty4(), rsW, WS_HL + (unsigned)((rt * 16 + hp) * 4096 + tid * 16), 0, 16);
    }
    const int fl = (q4 >> 1) | ((g & 1) << 1);
    const unsigned bo = lds_addr(smem) + ABYTES + (g >> 1) * 6144 + (g & 1) * 1024 + q4 * 128 + p4 * 8;
    const unsigned bo0 = bo + (((wn * 2 + 0) ^ fl) << 5), bo1 = bo + (((wn * 2 + 1) ^ fl) << 5);
    const unsigned ar = lds_addr(smem) + (wm * 64 + fr) * 128;
    const unsigned aK0 = ar + ((g ^ (fr & 7)) << 4), aK1 = ar + (((4 + g) ^ (fr & 7)) << 4);
    f32x32 ac0, ac1, ac2;
    asm volatile("" :: "v"(aK0), "v"(aK1), "v"(bo0), "v"(bo1));
    if (first) BAR;
    else publish(rsW, tid, 256 + rt * 16 + hp, E + (unsigned)layer);
#define G1_OUT
#define G1_T(a)
#define G1_TEND
#define G1_TCL
    asm volatile(G1_MACROS
        "s_mov_b32 vcc_hi, %[lay]\n"
        ".if %[hh]\n s_branch .Lg1m\n .else\n"
        ".p2align 3\n .Lg1m:\n"
        "G1RDB 0, v16, v17, 0\n G1RDA v12, v13, 0\n G1RDB 1, v16, v17, 0\n"
        "ds_read_b128 v[64:67], v20\n ds_read_b128 v[68:71], v20 offset:2048\n"
        "ds_read_b128 v[72:75], v20 offset:4096\n ds_read_b128 v[76:79], v20 offset:6144\n"
        "s_cmp_eq_u32 vcc_hi, 0\n s_waitcnt lgkmcnt(12)\n"
        "G1SG 0, 1, 1, v12, v13, v16, v17, 0\n"
        "s_mov_b32 vcc_lo, 16\n"
        "G1SG 1, 1, 1, v12, v13, v16, v17, 40960\n"
        "G1SG 2, 1, 2, v12, v13, v16, v17, 40960\n"
        "s_cbranch_scc1 .Lg1a%=\n"
        "s_barrier\n"
        G1_T(88)
        "s_barrier\n"
        G1_T(92)
        "G1RDA v12, v13, 40960\n"
        "s_waitcnt lgkmcnt(4)\n"
        "s_branch .Lg1b%=\n"
        ".Lg1a%=:\n"
        G1_T(88)
        "s_barrier\n"
        G1_T(92)
        "G1RDB 0, v16, v17, 40960\n G1RDA v12, v13, 40960\n G1RDB 1, v16, v17, 40960\n"
        ".p2align 3\n s_nop 0\n"
        ".Lg1b%=:\n"
        "s_waitcnt lgkmcnt(8)\n"
        "G1SG 0, 0, 1, v12, v13, v16, v17, 40960\n"
        "s_barrier\n"
        "G1SG 1, 0, 1, v14, v15, v18, v19, 0\n"
        "G1SG 2, 0, 1, v14, v15, v18, v19, 0\n"
        "s_lshr_b32 vcc_lo, vcc_lo, 1\n s_waitcnt vmcnt(0) lgkmcnt(8)\n"
        "G1SG 0, 0, 1, v14, v15, v18, v19, 0\n"
        "s_barrier\n"
        "G1SG 1, 0, 1, v12, v13, v16, v17, 0\n"
        "G1SG 2, 0, 1, v12, v13, v16, v17, 0\n"
        "s_cbranch_scc0 .Lg1c%=\n s_waitcnt lgkmcnt(8)\n"
        "G1SG 0, 0, 1, v12, v13, v16, v17, 0\n"
        "s_barrier\n"
        "G1SG 1, 0, 1, v12, v13, v16, v17, 40960\n"
        "G1SG 2, 0, 1, v12, v13, v16, v17, 40960\n"
        "s_branch .Lg1b%=\n"
        ".Lg1c%=:\n"
        "s_waitcnt lgkmcnt(8)\n"
        "G1SG 0, 0, 1, v12, v13, v16, v17, 0\n s_bitcmp1_b32 vcc_hi, 0\n"
        "G1SG 1, 0, 0, v12, v13, v16, v17, 0\n"
        G1_T(96)
        "s_cbranch_scc1 .Lg1n\n s_barrier\n"
        "G1Q 2, 2, 0, 0, 0\n ds_read2_b64 v[136:139], v49 offset0:32 offset1:48\n"
        "G1Q 2, 2, 0, 1, 0\n ds_read_b128 v[96:99], v48 offset:256\n"
        "G1Q 2, 2, 0, 2, 0\n ds_read_b128 v[100:103], v48 offset:320\n"
        "G1Q 2, 2, 0, 3, 0\n ds_read2_b32 v[144:145], v52 offset1:16\n"
        "G1Q 2, 3, 1, 0, 0\n ds_read_b128 v[104:107], v54 offset:192\n"
        "G1Q 2, 3, 1, 1, 0\n ds_read_b128 v[108:111], v54 offset:704\n"
        "G1Q 2, 3, 1, 2, 0\n ds_read2_b64 v[140:143], v49 offset1:16\n"
        "G1Q 2, 3, 1, 3, 0\n ds_read_b128 v[148:151], v48\n"
        "G1Q 2, 0, 2, 0, 0\n ds_read_b128 v[88:91], v48 offset:64\n"
        "G1Q 2, 0, 2, 1, 0\n ds_read_b128 v[128:131], v54\n"
        "G1Q 2, 0, 2, 2, 0\n ds_read_b128 v[92:95], v54 offset:512\n"
        "G1Q 2, 0, 2, 3, 0\n"
        "G1Q 2, 1, 3, 0, 0\n"
        "G1Q 2, 1, 3, 1, 0\n"
        "G1Q 2, 1, 3, 2, 0\n"
        "G1Q 2, 1, 3, 3, 0\n"
        "ds_read_b128 v[112:115], v54 offset:128\n ds_read_b128 v[116:119], v54 offset:640\n ds_read_b128 v[152:155], v54 offset:64\n ds_read_b128 v[156:159], v54 offset:576\n"
        "s_waitcnt lgkmcnt(4)\n"
        G1_SLIDE
        ".endif\n .if %[hh]\n .p2align 3\n s_nop 0\n .Lg1n:\n s_barrier\n"
        G1_HEDGE
        ".endif\n"
        G1_TEND G1_PURGE
        : "=&{v[160:191]}"(ac0), "=&{v[192:223]}"(ac1), "=&{v[224:255]}"(ac2), "=&{v[64:79]}"(xq),
          "=&{v[88:91]}"(csq[1]), "=&{v[148:151]}"(csq[0]), "=&{v[104:107]}"(wf[0]), "=&{v[108:111]}"(wf[1]), "=&{v[140:143]}"(st01), "=&{v[136:139]}"(st23),
          "+{v[48:63]}"(pa), "+{v[36:39]}"(bias[0]), "+{v[40:43]}"(bias[1]), "+{v[44:47]}"(bias[2]) G1_OUT
        : "{v12}"(aK0), "{v13}"(aK1), "{v14}"(aK0 + 2 * STAGE), "{v15}"(aK1 + 2 * STAGE), "{v16}"(bo0), "{v17}"(bo1),
          "{v18}"(bo0 + 2 * STAGE), "{v19}"(bo1 + 2 * STAGE), "{v20}"(wn ? aK1 : aK0),
          [lay] "s"(layer), [hh] "n"(MODE), [rs] "s"(rsW), [wm] "s"(wm)
        : "memory", "scc", "vcc", "m0", G1_TCL G1_CLOBBER);
    acc[0][0] = quad<0>(ac0); acc[0][1] = quad<1>(ac0); acc[1][0] = quad<6>(ac0); acc[1][1] = quad<7>(ac0);
    asm volatile("" : "=s"(kol));
    qh[0][0] = quad<4>(ac1); qh[0][1] = quad<5>(ac1); qh[1][0] = quad<2>(ac2); qh[1][1] = quad<3>(ac2);
  }
  ST(layer * 24 + 4)
  {
  const int lane = lane_id(), tid = w * 64 + lane, fr = lane & 15, g = lane >> 4;
  (void)fr; (void)g;
  if constexpr (HH) {
    const f32x4 z4 = {0, 0, 0, 0};
    constexpr float L2E = 1.4426950408889634f;
    const unsigned kvo = WS_KV + (layer >> 1) * KVLB, kfo = kvo + 16u * NH * KVPB + hp * KVFB;
    auto phi2 = [&](const f32x4 (&a)[2][2], f32x4 st, bf16x8 (&qp)[2][2]) {
      f32x4 pm[2][2];
      f32x2 f[2][4];
      asm volatile("" : "+v"(st));
#pragma unroll
      for (int i = 0; i < 8; ++i) {
        const int rb = i >> 2, c = (i >> 1) & 1, e = (i & 1) * 2;
        f[rb][i & 3] = __builtin_elementwise_fma(f32x2{csq[c][e], csq[c][e + 1]}, f32x2{st[2 * rb], st[2 * rb]}, f32x2{a[rb][c][e], a[rb][c][e + 1]});
      }
#pragma unroll
      for (int rb = 0; rb < 2; ++rb) asm volatile("" : "+v"(f[rb][0]), "+v"(f[rb][1]), "+v"(f[rb][2]), "+v"(f[rb][3]));
#pragma unroll
      for (int rb = 0; rb < 2; ++rb) {
        const bf16x8 xf = __builtin_bit_cast(bf16x8, u32x4{pk2(f[rb][0][0], f[rb][0][1]), pk2(f[rb][1][0], f[rb][1][1]), pk2(f[rb][2][0], f[rb][2][1]), pk2(f[rb][3][0], f[rb][3][1])});
        pm[rb][0] = MFMA(wf[0], xf, z4); pm[rb][1] = MFMA(wf[1], xf, z4);
      }
      asm volatile("s_nop 7" : "+v"(pm[0][0]), "+v"(pm[0][1]), "+v"(pm[1][0]), "+v"(pm[1][1]));
      float mx[2], mn[2];
#pragma unroll
      for (int u = 0; u < 2; ++u) max8pn(pm[u][0], pm[u][1], mx[u], mn[u]);
      xr4<true>(mx[0], mn[0], mx[1], mn[1]);
      float ep[2][8], en[2][8], sp[2], sn[2];
#pragma unroll
      for (int u = 0; u < 2; ++u) {
        const float c = st[2 * u + 1] * L2E, mxs = -mx[u] * c, mns = -mn[u] * c;
#pragma unroll
        for (int i = 0; i < 4; ++i) {
          const f32x2 p2 = {pm[u][i >> 1][(i & 1) * 2], pm[u][i >> 1][(i & 1) * 2 + 1]};
          const f32x2 tp = __builtin_elementwise_fma(p2, f32x2{c, c}, f32x2{mxs, mxs});
          const f32x2 tn = __builtin_elementwise_fma(-p2, f32x2{c, c}, f32x2{mns, mns});
          ep[u][2 * i] = __builtin_amdgcn_exp2f(tp[0]); ep[u][2 * i + 1] = __builtin_amdgcn_exp2f(tp[1]);
          en[u][2 * i] = __builtin_amdgcn_exp2f(tn[0]); en[u][2 * i + 1] = __builtin_amdgcn_exp2f(tn[1]);
        }
        const f32x2 a2 = (f32x2{ep[u][0], ep[u][1]} + f32x2{ep[u][2], ep[u][3]}) + (f32x2{ep[u][4], ep[u][5]} + f32x2{ep[u][6], ep[u][7]});
        const f32x2 b2 = (f32x2{en[u][0], en[u][1]} + f32x2{en[u][2], en[u][3]}) + (f32x2{en[u][4], en[u][5]} + f32x2{en[u][6], en[u][7]});
        sp[u] = a2[0] + a2[1]; sn[u] = b2[0] + b2[1];
      }
      xr4<false>(sp[0], sn[0], sp[1], sn[1]);
#pragma unroll
      for (int rb = 0; rb < 2; ++rb) {
        const float ip = __builtin_amdgcn_rcpf(sp[rb]), in = __builtin_amdgcn_rcpf(sn[rb]);
        qp[rb][0] = __builtin_bit_cast(bf16x8, u32x4{pk2(ep[rb][0] * ip, ep[rb][1] * ip), pk2(ep[rb][2] * ip, ep[rb][3] * ip), pk2(ep[rb][4] * ip, ep[rb][5] * ip), pk2(ep[rb][6] * ip, ep[rb][7] * ip)});
        qp[rb][1] = __builtin_bit_cast(bf16x8, u32x4{pk2(en[rb][0] * in, en[rb][1] * in), pk2(en[rb][2] * in, en[rb][3] * in), pk2(en[rb][4] * in, en[rb][5] * in), pk2(en[rb][6] * in, en[rb][7] * in)});
        asm volatile("" : "+v"(qp[rb][0]), "+v"(qp[rb][1]));
      }
    };
    bf16x8 one8 = ones8();
    asm volatile("" : "+v"(one8));
    WAITL0; BAR;
    ST(layer * 24 + 5)
    {
      bf16x8 af[4], v0[4], v1[4];
      asm volatile("" ::: "memory");
#pragma unroll
      for (int sw = 0; sw < 4; ++sw) {
        af[sw] = cat8(tr_read(ppa + sw * (64 * PSTR)), tr_read(ppa + sw * (64 * PSTR) + 16 * PSTR));
        v0[sw] = lds<bf16x8>(pvr + sw * 4096);
        v1[sw] = lds<bf16x8>(pvr + sw * 4096 + 1024);
      }
      __builtin_amdgcn_sched_barrier(0);
      f32x4 kv[3] = {z4, z4, z4};
#pragma unroll
      for (int sw = 0; sw < 4; ++sw) {
        kv[0] = MFMA(af[sw], v0[sw], kv[0]);
        kv[1] = MFMA(af[sw], v1[sw], kv[1]);
        kv[2] = MFMA(af[sw], one8, kv[2]);
      }
      auto pc = [](float x, float y) { const unsigned t = pk2(x, y); return t < EMPTY - 1u ? t : EMPTY - 1u; };
      __builtin_amdgcn_raw_buffer_store_b128(u32x4{pc(kv[0][0], kv[0][1]), pc(kv[0][2], kv[0][3]), pc(kv[1][0], kv[1][1]), pc(kv[1][2], kv[1][3])}, rsW, gkv, 0, 16);
      __builtin_amdgcn_raw_buffer_store_b64(u32x2{pc(kv[2][0], kv[2][1]), pc(kv[2][2], kv[2][3])}, rsW, gkz, 0, 16);
    }
    auto wo_late = [&](int t) { glds2v(wos + knib(kol, t) * tO, pwl, pwm, sb + OFF_WO + t * 8192 + (w & 3) * 2048); };
    ST(layer * 24 + 6)
    u32x4 l1[8];
    const bool red = w == 7 || (w == 6 && (rt & 7) == 0);
    unsigned lo = kvo + (unsigned)((2 * hp + (rt >> 3) + (lane >> 5) * 8 * NH) * KVPB) +
                  (w == 7 ? (unsigned)((rt & 4) * 512 + (g & 1) * 1024 + (rt & 3) * 256 + fr * 16) : (unsigned)(4096 + (lane & 7) * 16));
    unsigned fs = w == 7 ? kfo + (unsigned)((rt >> 2) * 2048 + (lane >> 5) * 1024 + (rt & 3) * 256 + fr * 16 + (g & 1) * 8)
                : (lane & 24) ? 0x7fff0000u : kfo + (unsigned)(8192 + (rt >> 3) * 128 + (lane & 4) * 16 + (lane & 1) * 32 + (lane >> 5) * 16 + (lane & 2) * 4);
    auto l1_load = [&]() {
      asm volatile("" ::: "memory");
#pragma unroll
      for (int p = 0; p < 8; ++p) asm volatile("buffer_load_dwordx4 %0, %1, %2, %3 offen sc1" : "=v"(l1[p]) : "v"(lo), "s"(rsW), "s"(p * (NH * KVPB)) : "memory");
      __builtin_amdgcn_sched_barrier(0);
    };
    if (red) asm volatile("" : "+v"(lo), "+v"(fs));
    if (red) { __builtin_amdgcn_s_sleep(13); l1_load(); }
    bf16x8 qp2[2][2];
    if (!kg) {
      phi2(qh, st23, qp2);
    } else if (red) {
      typedef __attribute__((ext_vector_type(2))) __bf16 bf2;
      unsigned c0 = 0x00003F80u, c1 = 0x3F800000u;
      asm volatile("" : "+v"(c0), "+v"(c1));
      float s4[4];
      _Pragma("unroll 1") for (unsigned spins = 0; spins < (1u << 18); ++spins) {
        float a[8] = {0.f, 0.f, 0.f, 0.f, 0.f, 0.f, 0.f, 0.f};
        ARRIVED("+v"(l1[0]), "+v"(l1[1]), "+v"(l1[2]), "+v"(l1[3]), "+v"(l1[4]), "+v"(l1[5]), "+v"(l1[6]), "+v"(l1[7]));
#pragma unroll
        for (int p = 0; p < 8; ++p)
#pragma unroll
          for (int e = 0; e < 4; ++e) {
            const unsigned u = l1[p][e];
            a[2 * e] = __builtin_amdgcn_fdot2_f32_bf16(__builtin_bit_cast(bf2, u), __builtin_bit_cast(bf2, c0), a[2 * e], false);
            a[2 * e + 1] = __builtin_amdgcn_fdot2_f32_bf16(__builtin_bit_cast(bf2, u), __builtin_bit_cast(bf2, c1), a[2 * e + 1], false);
          }
#pragma unroll
        for (int i = 0; i < 4; ++i) {
          auto r = __builtin_amdgcn_permlane32_swap(__float_as_uint(a[i]), __float_as_uint(a[i + 4]), false, false);
          s4[i] = __uint_as_float(r[0]) + __uint_as_float(r[1]);
        }
        const float t = s4[0] + s4[2];
        if (__all(t == t)) break;
        unsigned mxw = 0;
#pragma unroll
        for (int p = 0; p < 8; ++p)
#pragma unroll
          for (int e = 0; e < 4; ++e) mxw = l1[p][e] > mxw ? l1[p][e] : mxw;
        if (__all(mxw != EMPTY)) break;
        l1_load();
      }
      WAITV(0);
      unsigned o0 = pk2(s4[0], s4[1]), o1 = pk2(s4[2], s4[3]);
      o0 = o0 < EMPTY - 1u ? o0 : EMPTY - 1u; o1 = o1 < EMPTY - 1u ? o1 : EMPTY - 1u;
      __builtin_amdgcn_raw_buffer_store_b64(u32x2{o0, o1}, rsW, fs, 0, 16);
    }
    ST(layer * 24 + 8)
    WAITL0;
    BAR;
    ST(layer * 24 + 9)
    if (kg) {
#pragma unroll
      for (int i = 6; i < 12; ++i) wo_late(i);
    } else {
      unsigned fo = kfo + (unsigned)(wn * 4096 + lane * 16), fz = kfo + (unsigned)(8192 + wn * 128 + g * 16);
      asm volatile("" : "+v"(fo), "+v"(fz));
      bf16x8 qp[2][2];
      phi2(acc, st01, qp);
      asm volatile(G1_OUTPUT
          :: "{v[224:227]}"(qp[0][0]), "{v[228:231]}"(qp[0][1]), "{v[232:235]}"(qp[1][0]), "{v[236:239]}"(qp[1][1]),
             "{v[240:243]}"(qp2[0][0]), "{v[244:247]}"(qp2[0][1]), "{v[248:251]}"(qp2[1][0]), "{v[252:255]}"(qp2[1][1]),
             [fo] "v"(fo), [fz] "v"(fz), [go] "v"(ogo), [lo] "v"(opo), [rs] "s"(rsW)
          : "memory", "vcc", "scc", "m0", "v152", "v153", "v154", "v155", "v156", "v157", "v158", "v159", "v160", "v161", "v162", "v163", "v164", "v165", "v166", "v167", "v168", "v169", "v170", "v171", "v172", "v173", "v174", "v175", "v176", "v177", "v178", "v179", "v180", "v181", "v182", "v183", "v184", "v185", "v186", "v187", "v188", "v189", "v190", "v191", "v192", "v193", "v194", "v195", "v196", "v197", "v198", "v199", "v200", "v201", "v202", "v203", "v204", "v205", "v206", "v207", "v208", "v209", "v210", "v211", "v212", "v213", "v214", "v215", "v216", "v217", "v218", "v219", "v220", "v221", "v222", "v223");
    }
  }
  ST(layer * 24 + 12)
  if (first && w >= 4 && w < 7) {
    typedef __attribute__((ext_vector_type(2))) __bf16 bf2;
    unsigned c0 = 0x00003F80u, c1 = 0x3F800000u;
    asm volatile("" : "+v"(c0), "+v"(c1));
    const bf2 lo1 = __builtin_bit_cast(bf2, c0), hi1 = __builtin_bit_cast(bf2, c1);
    float a[8] = {0.f, 0.f, 0.f, 0.f, 0.f, 0.f, 0.f, 0.f};
#pragma unroll
    for (int i = 0; i < 8; ++i) {
      asm volatile("" : "+v"(pw[i]));
#pragma unroll
      for (int e = 0; e < 4; ++e) {
        const unsigned u = pw[i][e];
        a[2 * e] = __builtin_amdgcn_fdot2_f32_bf16(__builtin_bit_cast(bf2, u), lo1, a[2 * e], false);
        a[2 * e + 1] = __builtin_amdgcn_fdot2_f32_bf16(__builtin_bit_cast(bf2, u), hi1, a[2 * e + 1], false);
      }
    }
#pragma unroll
    for (int i = 0; i < 4; ++i) {
      auto r = __builtin_amdgcn_permlane32_swap(__float_as_uint(a[i]), __float_as_uint(a[i + 4]), false, false);
      a[i] = __uint_as_float(r[0]) + __uint_as_float(r[1]);
    }
#pragma unroll
    for (int i = 0; i < 2; ++i) {
      auto r = __builtin_amdgcn_permlane16_swap(__float_as_uint(a[i]), __float_as_uint(a[i + 2]), false, false);
      a[i] = __uint_as_float(r[0]) + __uint_as_float(r[1]);
      a[i] += dpp_ror<8>(a[i]);
    }
    if (!(lane & 8))
      __builtin_amdgcn_raw_buffer_store_b64(u32x2{__float_as_uint(a[0]), __float_as_uint(a[1])}, rsW,
          (unsigned)(WS_CSP + ((((hp + (1 << (w - 4))) & 15) * 16 + rt) * 192 + (w - 4) * 64 + (lane & 7) * 8 + (lane >> 5) * 4 + ((lane >> 4) & 1) * 2) * 4), 0, 16);
  }
  }
}

#undef pcs
#undef prs
#undef opo
#undef ogo
#undef hof
#undef dm
#undef dm2
#undef pcv
#undef pwf
#undef prv
#undef pvf
#undef ppk
#undef ppa
#undef pvr
#undef gkv
#undef gkz
#undef pwl
#undef pwm
#undef plb
#undef prd
#undef dm3

__device__ __forceinline__ void g2_phase(char* smem, char* ws, u64 E, int w, f32x16& xq, Next& nx, int layer, const u16* __restrict__ x0,
                                         const u16* __restrict__ wo, const u16* __restrict__ wnx, u16* __restrict__ out) {
  IDS
  wnx = uni(wnx);
  const unsigned sb = lds_addr(smem);
  const int lw = w & 3, k2 = 16 * lw + (lane >> 3);
  const int r0 = w * 16 + (lane >> 3);
  unsigned pxr = sb + r0 * 128 + (lane & 7) * 16;
  const unsigned rmo = (unsigned)(((m0 + r0) * DM + hp * 64 + ((lane & 7) ^ (lane >> 3)) * 8) * 2);
  const unsigned imo = (unsigned)((rt * 16 + hp) * 16384 + r0 * 128 + (lane & 7) * 16);
  constexpr int S0 = 0, S1 = 16384, S2 = 32768, S3 = 147456, S4 = OFF_WO, S5 = 98304, S6 = 114688;
  constexpr int AS[16] = {S0, S1, S2, S3, S0, S1, S4, S2, S3, S0, S1, S4, S5, S3, S0, S6};
  constexpr int OFF_C = 0;
  auto bs = [](int t) { return OFF_WO + (t < 12 ? t : t - 10) * 8192; };
  const char *xa1, *xa2, *wq2;
  u64 fm;
  unsigned vB0, vB1, vXA, po;
  if (w >= 4) {
    constexpr int WT[15] = {0, 8, 12, 14, 16, 18, 20, 18, 16, 14, 12, 12, 10, 10, 8};
    const char* o = ws + WS_O + layer * (SEQ * DM * 2) + rt * 262144;
    const unsigned vA = (unsigned)(lw * 4096 + lane * 16);
    u64 kord = korder(bid & 3, hp);
    auto ldA = [&](int s) { glds4c(o + knib(kord, s) * 16384, vA, sb + AS[s] + lw * 4096); };
    const bool im = img(layer, hp), in = img(layer + 1, hp);
    unsigned vW0 = im ? (unsigned)(lw * 2048 + lane * 16) : (unsigned)((k2 * DM + hp * 64 + ((lane & 7) ^ ((lane >> 4) << 1)) * 8) * 2), vW1 = vW0 + (im ? 0u : 8 * DM * 2 - 1024u);
    const char *wos = wo_src(ws, wo, layer, hp), *wqs = wq_src(ws, wnx, layer + 1, hp);
    unsigned tO = im ? 8192u : 131072u;
    asm volatile("" : "+v"(vW0), "+v"(vW1));
    asm volatile("" : "+s"(wos), "+s"(tO));
    auto ldW = [&](int t) { glds2v(wos + knib(kord, t) * tO, vW0, vW1, sb + bs(t) + lw * 2048); };
    const int fB = (k2 >> 1) & 1;
    vB0 = in ? (unsigned)(lw * 6144 + lane * 16) : (unsigned)((k2 * 3072 + hp * 64 + (((((lane & 7) >> 1) ^ fB) << 1) | (lane & 1)) * 8) * 2);
    vB1 = in ? vB0 : (unsigned)(((k2 + 8) * 3072 + hp * 64 + (((((lane & 7) >> 1) ^ (fB | 2)) << 1) | (lane & 1)) * 8) * 2) - 1024u;
    const u64 kn = nx.kord = korder(kv1(bid), hp);
    nx.wq = wqs; nx.tw = in ? 24576u : 393216u;
    const char *wq0 = wqs + hp * (in ? 24576u : 393216u), *wq1 = wqs + knib(kn, 1) * (in ? 24576u : 393216u);
    wq2 = wqs + knib(kn, 2) * (in ? 24576u : 393216u);
    xa1 = ws + WS_X + layer * (SEQ * DM * 2) + knib(kn, 1) * 16384; xa2 = ws + WS_X + layer * (SEQ * DM * 2) + knib(kn, 2) * 16384;
    vXA = (unsigned)(rt * 262144 + lw * 4096 + lane * 16);
    fm = kfirst(kn);
    po = ((fm >> lane) & 1) ? WS_FLAGS + (unsigned)(256 + rt * 16 + lane) * FLS : 0u;
    asm volatile("" : "+v"(vB0), "+v"(vB1), "+v"(vXA), "+v"(po));
    asm volatile("" : "+s"(xa1), "+s"(xa2), "+s"(wq2), "+s"(fm), "+s"(nx.kord), "+s"(nx.wq), "+s"(nx.tw));
    asm volatile("" : "+s"(wq0), "+s"(wq1));
    auto ldB = [&](int s, int part) {
      const char* b = s ? wq1 : wq0;
      if (part) glds4v(b, vB0, vB1, vB0, vB1, sb + s * 40960 + 16384 + lw * 6144);
      else glds2v(b + 4096, vB0, vB1, sb + s * 40960 + 16384 + lw * 6144 + 4096);
    };
    const char* o1 = o + knib(kord, 1) * 16384;
    asm volatile("" : "+s"(o1), "+s"(kord), "+s"(o));
    asm volatile("" :: "v"(vA));
    if (layer) { BAR;   }
    BAR;
    glds4c(o1, vA, sb + AS[1] + lw * 4096); __builtin_amdgcn_s_sleep(3); ldA(2); __builtin_amdgcn_s_sleep(3); ldA(3); __builtin_amdgcn_s_sleep(3); ldA(4);
    WAITV(12);
    BAR;
    auto step = [&](auto ktc) {
      constexpr int kt = decltype(ktc)::value;
      WAITVN(WT[kt]);
      BAR;
      if constexpr (kt == 1) { ldA(5); ldA(6); }
      if constexpr (kt >= 2 && kt <= 10) ldA(kt + 5);
      if constexpr (kt >= 2 && kt <= 5) ldW(kt + 10);
      if constexpr (kt >= 11) ldB((kt - 11) >> 1, (kt - 11) & 1);
      if constexpr (kt >= 6) { __builtin_amdgcn_s_sleep(kt >= 11 ? 3 : 2); if (layer == 1) __builtin_amdgcn_s_sleep(1); }
    };
    step(IC<1>{}); step(IC<2>{}); step(IC<3>{}); step(IC<4>{}); step(IC<5>{}); step(IC<6>{}); step(IC<7>{});
    step(IC<8>{}); step(IC<9>{}); step(IC<10>{}); step(IC<11>{}); step(IC<12>{}); step(IC<13>{}); step(IC<14>{});
  } else {
    asm volatile("" : "=v"(vB0), "=v"(vB1), "=v"(vXA), "=v"(po));
    asm volatile("" : "=s"(xa1), "=s"(xa2), "=s"(wq2), "=s"(fm), "=s"(nx.kord), "=s"(nx.wq), "=s"(nx.tw));
    const int wm = w >> 1, wn = w & 1;
    f32x4 acc[4][2];
    unsigned co[2];
#pragma unroll
    for (int cb = 0; cb < 2; ++cb) co[cb] = sb + OFF_C + (unsigned)((wm * 64 + fr) * 128 + (((wn * 4 + cb * 2 + (g >> 1)) ^ (fr & 7)) << 4) + (g & 1) * 8);
    const int fl = (q4 >> 1) | ((g & 1) << 1);
    const int bb0 = (4 * g + q4) * 128 + p4 * 8 + (((wn * 2) ^ fl) << 5), bb1 = (4 * g + q4) * 128 + p4 * 8 + (((wn * 2 + 1) ^ fl) << 5);
    const int abase = wm * 4096 + lane * 16;
    bf16x8 fa[2][2][4], fb[2][2][2];
    unsigned ra[3], rb0[2], rb1[2];
#pragma unroll
    for (int i = 0; i < 3; ++i) { ra[i] = abase + i * 65536; asm volatile("" : "+v"(ra[i])); }
#pragma unroll
    for (int i = 0; i < 2; ++i) { rb0[i] = bb0 + OFF_WO + i * 49152; rb1[i] = bb1 + OFF_WO + i * 49152; asm volatile("" : "+v"(rb0[i]), "+v"(rb1[i])); }
    auto loadf = [&](int kt, bf16x8 (&a)[2][4], bf16x8 (&b)[2][2]) {
#pragma unroll
      for (int s = 0; s < 2; ++s) {
        const char* sA = smem + ra[AS[kt] >> 16] + (AS[kt] & 65535) + s * 8192;
        const int hi = bs(kt) >= OFF_WO + 49152, ob = bs(kt) - OFF_WO - hi * 49152 + s * 4096;
        b[s][0] = cat8(tr_read(smem + rb0[hi] + ob), tr_read(smem + rb0[hi] + ob + 2048));
        b[s][1] = cat8(tr_read(smem + rb1[hi] + ob), tr_read(smem + rb1[hi] + ob + 2048));
#pragma unroll
        for (int rb = 0; rb < 4; ++rb) a[s][rb] = *(const bf16x8*)(sA + rb * 1024);
      }
    };
    auto mma = [&](bf16x8 (&a)[2][4], bf16x8 (&b)[2][2]) {
#pragma unroll
      for (int i = 0; i < 16; ++i) {
        const int rb = i >> 2, cb = ((i >> 1) ^ rb) & 1, s = ((i + 1) >> 1) & 1;
        acc[rb][cb] = MFMA(b[s][cb], a[s][rb], acc[rb][cb]);
      }
    };
    unsigned fo = lane < 16 && lane != hp ? WS_FLAGS + (unsigned)(rt * 16 + lane) * FLS : 0u;
    const u64 ko = korder(bid & 3, hp);
    unsigned kf = kfirst(ko), kf2 = kfirst(ko >> 16) | (1u << knib(ko, 9));
    unsigned jo = (unsigned)((((rt & 7) * 128 + (w - 1) * 64 + lane) * 3072 + (hp & 3) * 1024 + (hp | 3) * 64) * 2);
    unsigned fo1 = ((kf >> (lane & 31)) & 1) ? fo : 0u, fo2 = ((kf2 >> (lane & 31)) & 1) ? fo : 0u;
    fo = (((kf | kf2) >> (lane & 31)) & 1) ? 0u : fo;
    asm volatile("" : "+v"(fo), "+v"(fo1), "+v"(fo2));
    asm volatile("" : "+s"(kf), "+s"(kf2));
    publish(rsW, tid, rt * 16 + hp, E + (unsigned)(layer + 1));
    ST(layer * 24 + 13)
    u64 fl1 = 0;
    if (w == 0) asm volatile("global_load_dwordx2 %0, %1, %2 sc1" : "+v"(fl1) : "v"(fo1), "s"(ws) : "memory");
    if (layer == 0) {
      const unsigned xo = (unsigned)(((m0 + wm * 64 + fr) * DM + hp * 64 + wn * 32 + g * 8) * 2);
      asm volatile("global_load_dwordx4 v[64:67], %1, %3\n\tglobal_load_dwordx4 v[68:71], %1, %4\n\tglobal_load_dwordx4 v[72:75], %2, %3\n\tglobal_load_dwordx4 v[76:79], %2, %4"
                   : "+{v[64:79]}"(xq) : "v"(xo), "v"(xo + 32 * DM * 2), "s"(x0), "s"(x0 + 16 * DM) : "memory");
    }
    loadf(0, fa[0], fb[0]);
    unsigned junk = 0;
    if (layer < 2 && w == 3 && (bid >> 6) == 0) code_touch(junk, bid, lane);
    if (layer == 0 && (hp & 7) < 3 && (w == 1 || w == 2))
      asm volatile("global_load_dword %0, %1, %2" : "+v"(junk) : "v"(jo), "s"(wnx) : "memory");
    __builtin_amdgcn_sched_barrier(0);
    bf16x8 id[2];
#pragma unroll
    for (int cb = 0; cb < 2; ++cb) {
      const unsigned one = (g >> 1) == cb && (g & 1) == (fr >> 3) ? 0x3F80u << ((fr & 1) * 16) : 0u;
      u32x4 t;
#pragma unroll
      for (int j = 0; j < 4; ++j) t[j] = ((fr >> 1) & 3) == j ? one : 0u;
      id[cb] = __builtin_bit_cast(bf16x8, t);
      asm volatile("" : "+v"(id[cb]));
    }
    asm volatile("" : "+v"(co[0]), "+v"(co[1]));
#pragma unroll
    for (int rb = 0; rb < 4; ++rb) { acc[rb][0] = f32x4{0, 0, 0, 0}; acc[rb][1] = f32x4{0, 0, 0, 0}; }
    mma(fa[0], fb[0]);
    __builtin_amdgcn_sched_barrier(0);
    WAITL0;
    u64 miss = 0;
    if (w == 0) { ARRIVED("+v"(fl1)); if (__ballot(fl1 != E + (unsigned)(layer + 1)) & kf) poll_eq(ws, fo1, E + (unsigned)(layer + 1), kf, kf); miss = 0xffffu & ~(kf | kf2 | (1u << hp)); }
    ST(layer * 24 + 14)
    __builtin_amdgcn_sched_barrier(0);
    BAR;
    __builtin_amdgcn_sched_barrier(0);
    asm volatile("" ::: "memory");
    if (layer == 0) WAITV(0);
    __builtin_amdgcn_sched_barrier(0);
    asm volatile("" : "+{v[64:79]}"(xq));
    const bf16x8 xf[4] = {__builtin_bit_cast(bf16x8, quad<0>(xq)), __builtin_bit_cast(bf16x8, quad<1>(xq)), __builtin_bit_cast(bf16x8, quad<2>(xq)), __builtin_bit_cast(bf16x8, quad<3>(xq))};
#pragma unroll
    for (int rb = 0; rb < 4; ++rb) { acc[rb][0] = MFMA(id[0], xf[rb], acc[rb][0]); acc[rb][1] = MFMA(id[1], xf[rb], acc[rb][1]); }
    if (w == 0) poll_eq(ws, fo2, E + (unsigned)(layer + 1), kf2, kf2);
    u64 fl2 = 0;
    auto step = [&](auto ktc) {
      constexpr int kt = decltype(ktc)::value;
      __builtin_amdgcn_sched_barrier(0);
      if (kt == 5 && miss) {
        ARRIVED("+v"(fl2));
        if (__ballot(fl2 != E + (unsigned)(layer + 1)) & miss) poll_eq(ws, fo, E + (unsigned)(layer + 1), miss, ~0ull);
      }
      WAITL0;
      BAR;
      if (kt == 2 && miss) asm volatile("global_load_dwordx2 %0, %1, %2 sc1" : "+v"(fl2) : "v"(fo), "s"(ws) : "memory");
      if (kt == 0) { ST(layer * 24 + 15) }
      __builtin_amdgcn_sched_barrier(0);
      loadf(kt + 1, fa[(kt + 1) & 1], fb[(kt + 1) & 1]);
      if (kt == 0) return;
      mma(fa[kt & 1], fb[kt & 1]);
#pragma unroll
      for (int i = 0; i < 4; ++i) {
        __builtin_amdgcn_sched_group_barrier(0x008, 1, 0);
        __builtin_amdgcn_sched_group_barrier(0x100, 1, 0);
        __builtin_amdgcn_sched_group_barrier(0x008, 1, 0);
        __builtin_amdgcn_sched_group_barrier(0x100, 3, 0);
      }
#pragma unroll
      for (int i = 0; i < 8; ++i) __builtin_amdgcn_sched_group_barrier(0x008, 1, 0);
    };
    step(IC<0>{}); step(IC<1>{}); step(IC<2>{}); step(IC<3>{}); step(IC<4>{}); step(IC<5>{}); step(IC<6>{}); step(IC<7>{});
    step(IC<8>{}); step(IC<9>{}); step(IC<10>{}); step(IC<11>{}); step(IC<12>{}); step(IC<13>{}); step(IC<14>{});
    WAITV(0);
    asm volatile("" :: "v"(junk));
    __builtin_amdgcn_sched_barrier(0);
    auto put = [&](int rb) {
#pragma unroll
      for (int cb = 0; cb < 2; ++cb) lds<u32x2>(co[cb] + rb * 2048) = u32x2{pk2(acc[rb][cb][0], acc[rb][cb][1]), pk2(acc[rb][cb][2], acc[rb][cb][3])};
    };
#pragma unroll
    for (int rb = 0; rb < 4; ++rb)
#pragma unroll
      for (int cb = 0; cb < 2; ++cb) acc[rb][cb] = MFMA(fb[1][0][cb], fa[1][0][rb], acc[rb][cb]);
#pragma unroll
    for (int rb = 0; rb < 4; ++rb) {
      __builtin_amdgcn_sched_barrier(0);
#pragma unroll
      for (int cb = 0; cb < 2; ++cb) acc[rb][cb] = MFMA(fb[1][1][cb], fa[1][1][rb], acc[rb][cb]);
      __builtin_amdgcn_sched_barrier(0);
      if (rb >= 2) put(rb - 2);
    }
    __builtin_amdgcn_sched_barrier(0);
    ST(layer * 24 + 16)
    put(2); put(3);
  }
  WAITL0; BAR;
  asm volatile("" ::: "memory");
  u32x4 xn[2];
#pragma unroll
  for (int i = 0; i < 2; ++i) xn[i] = lds<u32x4>(pxr + OFF_C + i * 1024);
  if (layer == 3) {
    const __amdgpu_buffer_rsrc_t rsO = __builtin_amdgcn_make_buffer_rsrc((void*)uni(out), (short)0, SEQ * DM * 2, 0x00020000);
    __builtin_amdgcn_raw_buffer_store_b128(xn[0], rsO, rmo, 0, 16);
    __builtin_amdgcn_raw_buffer_store_b128(xn[1], rsO, rmo + 8 * DM * 2, 0, 16);
  } else {
    __builtin_amdgcn_raw_buffer_store_b128(xn[0], rsW, WS_X + layer * (SEQ * DM * 2) + imo, 0, 16);
    __builtin_amdgcn_raw_buffer_store_b128(xn[1], rsW, WS_X + layer * (SEQ * DM * 2) + imo + 1024, 0, 16);
    float mean[2], m2[2];
#pragma unroll
    for (int i = 0; i < 2; ++i) {
      typedef __attribute__((ext_vector_type(2))) __bf16 bf2;
      const bf2 one2 = __builtin_bit_cast(bf2, 0x3F803F80u);
      float sa = 0.f, sq = 0.f;
#pragma unroll
      for (int e = 0; e < 4; ++e) {
        const unsigned u = xn[i][e];
        const bf2 t = __builtin_bit_cast(bf2, u);
        sa = __builtin_amdgcn_fdot2_f32_bf16(t, one2, sa, false);
        sq = __builtin_amdgcn_fdot2_f32_bf16(t, t, sq, false);
      }
      mean[i] = sa;
      m2[i] = sq;
    }
#define DPA(c) "v_add_f32_dpp %0, %0, %0 " c "\n\tv_add_f32_dpp %1, %1, %1 " c "\n\tv_add_f32_dpp %2, %2, %2 " c "\n\tv_add_f32_dpp %3, %3, %3 " c "\n\t"
    asm volatile("s_nop 3\n\t" DPA("quad_perm:[1,0,3,2] row_mask:0xf bank_mask:0xf") DPA("quad_perm:[2,3,0,1] row_mask:0xf bank_mask:0xf")
                 DPA("row_half_mirror row_mask:0xf bank_mask:0xf") "s_nop 0" : "+v"(mean[0]), "+v"(m2[0]), "+v"(mean[1]), "+v"(m2[1]));
    if ((lane & 7) < 2) {
      const int i = lane & 1;
      const u32x2 sv = {__float_as_uint(i ? mean[1] : mean[0]), __float_as_uint(i ? m2[1] : m2[0])};
      __builtin_amdgcn_raw_buffer_store_b64(sv, rsW, WS_ST + layer * (SEQ * 128) + (unsigned)(((rt * 16 + hp) * 128 + r0 + i * 8) * 8), 0, 16);
    }
  }
  ST(layer * 24 + 17)
  if (layer < 3 && w >= 4) {
    WAITV(0); BAR;
    u64 fl = 0;
    if (lw == 0) { __builtin_amdgcn_s_sleep(7); asm volatile("global_load_dwordx2 %0, %1, %2 sc1" : "+v"(fl) : "v"(po), "s"(ws) : "memory"); }
    glds4v(wq2, vB0, vB1, vB0, vB1, sb + 2 * 40960 + 16384 + lw * 6144);
    glds2v(wq2 + 4096, vB0, vB1, sb + 2 * 40960 + 16384 + lw * 6144 + 4096);
    if (lw == 0) {
      WAITV(6);
      __builtin_amdgcn_sched_barrier(0);
      asm volatile("" : "+v"(fl));
      if (__ballot(fl != E + (unsigned)(layer + 1)) & fm) poll_eq(ws, po, E + (unsigned)(layer + 1), fm, ~0ull);
    }
    BAR;
    glds4c(xa1, vXA, sb + 40960 + lw * 4096); glds4c(xa2, vXA, sb + 2 * 40960 + lw * 4096);
  }
  ST(layer * 24 + 18)
}

__global__ __launch_bounds__(512, 2) void mega_kernel(const u16* __restrict__ x0, const u16* __restrict__ wqkvA,
                                                      const u16* __restrict__ woA, const u16* __restrict__ wqkvB,
                                                      const u16* __restrict__ wphi, const u16* __restrict__ woB,
                                                      u16* __restrict__ out, char* __restrict__ ws) {
  extern __shared__ __attribute__((aligned(16))) char smem[];
  const int w = __builtin_amdgcn_readfirstlane(threadIdx.x >> 6);
  u64 E = 0;
  {
    const int bid = blockIdx.x, lane = threadIdx.x & 63;
    const int rt = 2 * (bid & 4) + ((bid >> 3) & 7), hp = 4 * (bid & 3) + (bid >> 6), m0 = rt * 128;
    if (w < 4) {
      if (w == 3) {
        unsigned long long pc;
        unsigned len;
        asm volatile("s_getpc_b64 %0\n.Lmk_pc:\n\ts_mov_b32 %1, .Lmk_end-.Lmk_pc" : "=s"(pc), "=s"(len));
        const unsigned per = ((len >> 10) + 1) * 128, off = ((bid >> 3) & 7) * per + (lane * 128 < per ? lane * 128 : 0);
        asm volatile("s_mov_b32 m0, %2\n\ts_nop 0\n\tglobal_load_lds_dword %0, %1" :: "v"(off < len - 4 ? off : 0u), "s"(pc), "s"(lds_addr(smem) + LDS_TOTAL - 1024) : "memory", "m0");
      }
      const unsigned xo = (unsigned)((m0 + hp * 8 + w * 2) * (DM * 2) + lane * 16);
      glds4v(x0, xo, xo, xo, xo, lds_addr(smem) + 122880 + w * 4096);
    } else {
      const int lw = w & 3;
      const int kB = 16 * lw + (lane >> 3), fB = (kB >> 1) & 1;
      const unsigned vB0 = (unsigned)((kB * 3072 + hp * 64 + (((((lane & 7) >> 1) ^ fB) << 1) | (lane & 1)) * 8) * 2);
      const unsigned vB1 = (unsigned)(((kB + 8) * 3072 + hp * 64 + (((((lane & 7) >> 1) ^ (fB | 2)) << 1) | (lane & 1)) * 8) * 2) - 1024u;
      const unsigned vA = (unsigned)(((m0 + lw * 32 + (lane >> 3)) * DM + ((lane & 7) ^ (lane >> 3)) * 8) * 2);
#pragma unroll
      for (int s = 0; s < 1; ++s) {
        const int kt = (rt & 8) + s;
        const unsigned sA = lds_addr(smem) + s * 40960, sB = sA + 16384;
        glds4v(x0 + kt * 64, vA, vA + 15360u, vA + 2 * 15360u, vA + 3 * 15360u, sA + lw * 4096);
        const u16* b = wqkvA + (size_t)kt * 64 * 3072;
        glds4v(b, vB0, vB1, vB0, vB1, sB + lw * 6144);
        glds2v(b + 2048, vB0, vB1, sB + lw * 6144 + 4096);
      }
      asm volatile("s_load_dwordx2 %0, %1, 0x0\n\ts_waitcnt lgkmcnt(0)" : "=s"(E) : "s"(ws) : "memory");
    }
  }
  f32x16 xq;
  Next nx;
  asm volatile("" : "=s"(nx.kord), "=s"(nx.wq), "=s"(nx.tw));
#pragma unroll 1
  for (int layer = 0; layer < 4; ++layer) {
    if (layer & 1) g1_phase<1>(smem, ws, E, w, xq, nx, layer, x0, wqkvB, woB, wphi, wqkvA, woA);
    else g1_phase<0>(smem, ws, E, w, xq, nx, layer, x0, wqkvA, woA, nullptr, wqkvB, woB);
    g2_phase(smem, ws, E, w, xq, nx, layer, x0, (layer & 1) ? woB : woA, (layer & 1) ? wqkvA : wqkvB, out);
  }
  asm volatile(".Lmk_end:");
  WAITV(0);
  if (blockIdx.x == 0 && w == 0 && lane_id() == 0) __hip_atomic_store((gu64*)ws, E + 16u, __ATOMIC_RELAXED, __HIP_MEMORY_SCOPE_AGENT);
}

extern "C" void kernel_launch(void* const* d_in, const int* in_sizes, int n_in,
                              void* d_out, int out_size, void* d_ws, size_t ws_size,
                              hipStream_t stream) {
  hipFuncSetAttribute((const void*)mega_kernel, hipFuncAttributeMaxDynamicSharedMemorySize, LDS_TOTAL);
  mega_kernel<<<256, 512, LDS_TOTAL, stream>>>((const u16*)d_in[0], (const u16*)d_in[1], (const u16*)d_in[2], (const u16*)d_in[4],
                                               (const u16*)d_in[5], (const u16*)d_in[6], (u16*)d_out, (char*)d_ws);
}
```

```cpp
#include <hip/hip_runtime.h>
#include <hip/hip_bf16.h>
#include <stdint.h>
#include <type_traits>
#pragma clang diagnostic ignored "-Winline-asm"
#pragma clang diagnostic ignored "-Wunused-value"

typedef unsigned short u16;
typedef __attribute__((ext_vector_type(8))) short bf16x8;
typedef __attribute__((ext_vector_type(4))) short bf16x4;
typedef __attribute__((ext_vector_type(4))) float f32x4;
typedef __attribute__((ext_vector_type(4))) unsigned u32x4;
typedef __attribute__((ext_vector_type(2))) unsigned u32x2;
typedef __attribute__((ext_vector_type(8))) float f32x8;
typedef __attribute__((ext_vector_type(16))) unsigned u32x16;
typedef __attribute__((ext_vector_type(16))) float f32x16;
typedef __attribute__((ext_vector_type(32))) float f32x32;
template <int I, typename T> __device__ __forceinline__ f32x4 quad(const T& v) { return __builtin_shufflevector(v, v, 4 * I, 4 * I + 1, 4 * I + 2, 4 * I + 3); }

constexpr int SEQ = 2048, DM = 1024, NH = 32;
#define ST(i)
constexpr int KVP = 64 * 32 + 64;

typedef __attribute__((ext_vector_type(2))) float f32x2;
typedef __attribute__((ext_vector_type(2))) __bf16 bf16x2_t;
__device__ __forceinline__ unsigned pk2(float a, float b) {
  f32x2 v = {a, b};
  bf16x2_t r = __builtin_convertvector(v, bf16x2_t);
  return __builtin_bit_cast(unsigned, r);
}

__device__ __forceinline__ bf16x4 tr_read(const char* p) {
  return __builtin_amdgcn_ds_read_tr16_b64_v4i16((__attribute__((address_space(3))) bf16x4*)p);
}
template <typename T> __device__ __forceinline__ __attribute__((address_space(3))) T& lds(unsigned a) { return *(__attribute__((address_space(3))) T*)(size_t)a; }
__device__ __forceinline__ bf16x4 tr_read(unsigned a) { return __builtin_amdgcn_ds_read_tr16_b64_v4i16(&lds<bf16x4>(a)); }
__device__ __forceinline__ bf16x8 cat8(bf16x4 a, bf16x4 b) {
  bf16x8 f = {a[0], a[1], a[2], a[3], b[0], b[1], b[2], b[3]};
  return f;
}
__device__ __forceinline__ void glds16s(const void* sbase, unsigned voff, unsigned lds_base) {
  asm volatile("s_mov_b32 m0, %2\n\ts_nop 0\n\tglobal_load_lds_dwordx4 %0, %1" :: "v"(voff), "s"(sbase), "s"(lds_base) : "memory", "m0");
}
__device__ __forceinline__ void glds16c(const void* sbase, unsigned voff, unsigned lds_base) {
  asm volatile("s_mov_b32 m0, %2\n\ts_nop 0\n\tglobal_load_lds_dwordx4 %0, %1 sc1" :: "v"(voff), "s"(sbase), "s"(lds_base) : "memory", "m0");
}
#define GL_ "\n\tglobal_load_lds_dwordx4 "
__device__ __forceinline__ void glds4c(const void* sbase, unsigned v, unsigned lds_base) {
  asm volatile("s_mov_b32 m0, %2\n\ts_nop 0" GL_ "%0, %1 sc1" GL_ "%0, %1 offset:1024 sc1" GL_ "%0, %1 offset:2048 sc1" GL_ "%0, %1 offset:3072 sc1"
               :: "v"(v), "s"(sbase), "s"(lds_base) : "memory", "m0");
}
__device__ __forceinline__ void glds4v(const void* sbase, unsigned v0, unsigned v1, unsigned v2, unsigned v3, unsigned lds_base) {
  asm volatile("s_mov_b32 m0, %5\n\ts_nop 0" GL_ "%0, %4 sc1" GL_ "%1, %4 offset:1024 sc1" GL_ "%2, %4 offset:2048 sc1" GL_ "%3, %4 offset:3072 sc1"
               :: "v"(v0), "v"(v1), "v"(v2), "v"(v3), "s"(sbase), "s"(lds_base) : "memory", "m0");
}
__device__ __forceinline__ void glds2v(const void* sbase, unsigned v0, unsigned v1, unsigned lds_base) {
  asm volatile("s_mov_b32 m0, %3\n\ts_nop 0" GL_ "%0, %2 sc1" GL_ "%1, %2 offset:1024 sc1" :: "v"(v0), "v"(v1), "s"(sbase), "s"(lds_base) : "memory", "m0");
}
__device__ __forceinline__ void gld16(u32x4& d, unsigned voff, const void* sbase) {
  asm volatile("global_load_dwordx4 %0, %1, %2" : "=v"(d) : "v"(voff), "s"(sbase) : "memory");
}
__device__ __forceinline__ unsigned lds_addr(const void* p) { return (unsigned)(size_t)p; }
#define WAITV(n) asm volatile("s_waitcnt vmcnt(" #n ")" ::: "memory")
#define WAITVN(n) asm volatile("s_waitcnt vmcnt(%0)" :: "n"(n) : "memory")
#define WAITL0 do { asm volatile("" ::: "memory"); __builtin_amdgcn_s_waitcnt(0xC07F); asm volatile("" ::: "memory"); } while (0)
#define BAR __builtin_amdgcn_s_barrier()
#define ARRIVED(...) do { WAITV(0); __builtin_amdgcn_sched_barrier(0); asm volatile("" : __VA_ARGS__); } while (0)

template <int N> __device__ __forceinline__ float dpp_ror(float x) {
  return __uint_as_float(__builtin_amdgcn_update_dpp(0u, __float_as_uint(x), 0x120 + N, 0xf, 0xf, false));
}
__device__ __forceinline__ float r16_sum(float x) { x += dpp_ror<8>(x); x += dpp_ror<4>(x); x += dpp_ror<2>(x); return x + dpp_ror<1>(x); }
__device__ __forceinline__ void max8pn(const f32x4& a, const f32x4& b, float& mx, float& mn) {
  asm("v_max3_f32 %0, %2, %3, %4\n\tv_max3_f32 %1, -%2, -%3, -%4\n\tv_max3_f32 %0, %0, %5, %6\n\tv_max3_f32 %1, %1, -%5, -%6\n\t"
      "v_max3_f32 %0, %0, %7, %8\n\tv_max3_f32 %1, %1, -%7, -%8\n\tv_max_f32 %0, %0, %9\n\tv_max_f32 %1, %1, -%9\n\ts_nop 0"
      : "=&v"(mx), "=&v"(mn) : "v"(a[0]), "v"(a[1]), "v"(a[2]), "v"(a[3]), "v"(b[0]), "v"(b[1]), "v"(b[2]), "v"(b[3]));
}
__device__ __forceinline__ float vmaxf_n(float a, float b) { float r; asm("v_max_f32 %0, %1, %2\n\ts_nop 0" : "=v"(r) : "v"(a), "v"(b)); return r; }
template <bool MAXOP> __device__ __forceinline__ float rop(float a, float b) { return MAXOP ? vmaxf_n(a, b) : a + b; }
template <bool MAXOP> __device__ __forceinline__ void xr4(float& a, float& b, float& c, float& d) {
  auto r = __builtin_amdgcn_permlane16_swap(__float_as_uint(a), __float_as_uint(b), false, false);
  auto q = __builtin_amdgcn_permlane16_swap(__float_as_uint(c), __float_as_uint(d), false, false);
  const unsigned ab = __float_as_uint(rop<MAXOP>(__uint_as_float(r[0]), __uint_as_float(r[1])));
  const unsigned cd = __float_as_uint(rop<MAXOP>(__uint_as_float(q[0]), __uint_as_float(q[1])));
  auto t = __builtin_amdgcn_permlane32_swap(ab, cd, false, false);
  const unsigned u = __float_as_uint(rop<MAXOP>(__uint_as_float(t[0]), __uint_as_float(t[1])));
  auto h = __builtin_amdgcn_permlane32_swap(u, u, false, false);
  auto p0 = __builtin_amdgcn_permlane16_swap(h[0], h[0], false, false);
  auto p1 = __builtin_amdgcn_permlane16_swap(h[1], h[1], false, false);
  a = __uint_as_float(p0[0]); b = __uint_as_float(p0[1]); c = __uint_as_float(p1[0]); d = __uint_as_float(p1[1]);
}

__device__ __forceinline__ unsigned xs4(float a, float b, float c, float d) {
  auto r = __builtin_amdgcn_permlane16_swap(__float_as_uint(a), __float_as_uint(b), false, false);
  auto q = __builtin_amdgcn_permlane16_swap(__float_as_uint(c), __float_as_uint(d), false, false);
  auto t = __builtin_amdgcn_permlane32_swap(__float_as_uint(__uint_as_float(r[0]) + __uint_as_float(r[1])), __float_as_uint(__uint_as_float(q[0]) + __uint_as_float(q[1])), false, false);
  return __float_as_uint(__uint_as_float(t[0]) + __uint_as_float(t[1]));
}

__device__ __forceinline__ f32x4 sum4(const u32x4 (&v)[4]) {
  float r0, r1, r2, r3, t0, t1, t2, t3;
  asm("v_add_f32 %0, %8, %12\n\tv_add_f32 %1, %9, %13\n\tv_add_f32 %2, %10, %14\n\tv_add_f32 %3, %11, %15\n\t"
      "v_add_f32 %4, %16, %20\n\tv_add_f32 %5, %17, %21\n\tv_add_f32 %6, %18, %22\n\tv_add_f32 %7, %19, %23\n\t"
      "v_add_f32 %0, %0, %4\n\tv_add_f32 %1, %1, %5\n\tv_add_f32 %2, %2, %6\n\tv_add_f32 %3, %3, %7\n\ts_nop 0"
      : "=&v"(r0), "=&v"(r1), "=&v"(r2), "=&v"(r3), "=&v"(t0), "=&v"(t1), "=&v"(t2), "=&v"(t3)
      : "v"(v[0][0]), "v"(v[0][1]), "v"(v[0][2]), "v"(v[0][3]), "v"(v[1][0]), "v"(v[1][1]), "v"(v[1][2]), "v"(v[1][3]),
        "v"(v[2][0]), "v"(v[2][1]), "v"(v[2][2]), "v"(v[2][3]), "v"(v[3][0]), "v"(v[3][1]), "v"(v[3][2]), "v"(v[3][3]));
  return f32x4{r0, r1, r2, r3};
}
#define MFMA(a, b, c) __builtin_amdgcn_mfma_f32_16x16x32_bf16(a, b, c, 0, 0, 0)

typedef unsigned long long u64;
typedef __attribute__((address_space(1))) unsigned long long gu64;
constexpr unsigned WS_FLAGS = 4096;
constexpr int FLS = 64;
constexpr unsigned WS_G0 = 65536;
constexpr unsigned WS_CSP = WS_G0 + SEQ * 16;
constexpr unsigned WS_ST = WS_CSP + 16 * 16 * 768;
constexpr unsigned WS_X = 1u << 21;
constexpr unsigned WS_O = WS_X + 3u * SEQ * DM * 2;
constexpr unsigned KVPB = KVP * 2, KVFB = KVP * 4;
constexpr unsigned EMPTY = 0xffffffffu;
constexpr unsigned WS_KV = WS_O + 4u * SEQ * DM * 2;
constexpr unsigned KVLB = 16u * NH * KVPB + 16u * KVFB;
constexpr unsigned WS_HL = WS_KV + 2u * KVLB;
constexpr unsigned WS_CSA = WS_HL + 512u * 4096;
constexpr unsigned WS_CPA = WS_CSA + 256u * 1024;
constexpr unsigned WS_IMG = WS_CPA + 16 * 16 * 768;
constexpr unsigned WS_END = WS_IMG + 2048 * 1024;
constexpr int LDS_TOTAL = 163840;
constexpr int OFF_WO = 49152;
struct Next { u64 kord; const char* wq; unsigned tw; };
template <int V> using IC = std::integral_constant<int, V>;
__device__ __forceinline__ bf16x8 ones8() { unsigned e = 0x3F803F80u; asm volatile("" : "+v"(e)); return __builtin_bit_cast(bf16x8, u32x4{e, e, e, e}); }
__device__ __forceinline__ u32x4 empty4() { unsigned e = EMPTY; asm volatile("" : "+v"(e)); return u32x4{e, e, e, e}; }
__device__ __forceinline__ u64 korder(int v, int own) {
  const u64 t = v == 0 ? 0x3b210fedca987654ull : v == 1 ? 0xb37654210fedca98ull : v == 2 ? 0x3ba987654210fedcull : 0xb3fedca987654210ull;
  const u64 inv = v == 0 ? 0xa987e6543210fdcbull : v == 1 ? 0x6543f210dcbae987ull : v == 2 ? 0x3210edcba987f654ull : 0xdcbaf9876543e210ull;
  const u64 low = (1ull << (4 * ((inv >> (4 * own)) & 15))) - 1ull;
  return (u64)own | ((t & low) << 4) | (t & ~((low << 4) | 15ull));
}
__device__ __forceinline__ u64 korder0(int rt) {
  const int r = rt & 7;
  const u64 own = (0x765432765432ull >> (4 * (r < 6 ? r : r - 6))) & 0xffffffull, oth = 0x7654321076543210ull >> (4 * r);
  const u64 t = 0x10ull | ((oth & 15ull) << 8) | (own << 12) | ((oth >> 4) << 36);
  return t | (rt < 8 ? 0x8888888000000800ull : 0x0000000888888088ull);
}
__device__ __forceinline__ int knib(u64 t, int s) { return (int)(t >> (4 * s)) & 15; }
__device__ __forceinline__ unsigned kfirst(u64 t) { return (1u << knib(t, 1)) | (1u << knib(t, 2)) | (1u << knib(t, 3)) | (1u << knib(t, 4)); }
__device__ __forceinline__ int kv1(int bid) { return (bid + (0x4924 >> ((bid >> 2) & 14))) & 3; }

__device__ __forceinline__ bool img(int layer, int hp) { return layer >= 2 && (hp & 7) == 3; }
__device__ __forceinline__ const char* wq_src(const char* ws, const void* W, int layer, int hp) { return img(layer, hp) ? ws + WS_IMG + ((layer & 1) * 768 + (hp >> 3) * 384) * 1024 : (const char*)W; }
__device__ __forceinline__ const char* wo_src(const char* ws, const void* W, int layer, int hp) { return img(layer, hp) ? ws + WS_IMG + (1536 + (layer & 1) * 256 + (hp >> 3) * 128) * 1024 : (const char*)W; }

template <typename T> __device__ __forceinline__ T* uni(T* p) {
  const unsigned long long v = (unsigned long long)p;
  const unsigned lo = __builtin_amdgcn_readfirstlane((unsigned)v), hi = __builtin_amdgcn_readfirstlane((unsigned)(v >> 32));
  return (T*)(((unsigned long long)hi << 32) | lo);
}
__device__ __forceinline__ int lane_id() {
  int l;
  asm volatile("v_mbcnt_lo_u32_b32 %0, -1, 0\n\tv_mbcnt_hi_u32_b32 %0, -1, %0" : "=v"(l));
  __builtin_assume(l >= 0 && l < 64);
  return l;
}
#define IDS \
  const int lane = lane_id(); \
  asm volatile("" : "+s"(w)); \
  const int tid = w * 64 + lane; \
  int bid = blockIdx.x; asm volatile("" : "+s"(bid)); \
  ws = uni(ws); asm volatile("" : "+s"(ws)); \
  layer = __builtin_amdgcn_readfirstlane(layer); x0 = uni(x0); wo = uni(wo); \
    \
  const int rt = 2 * (bid & 4) + ((bid >> 3) & 7); \
  const int hp = 4 * (bid & 3) + (bid >> 6); \
  const int m0 = rt * 128; \
  const int fr = lane & 15, g = lane >> 4; \
  const int q4 = (lane & 15) >> 2, p4 = lane & 3; \
  const __amdgpu_buffer_rsrc_t rsW = __builtin_amdgcn_make_buffer_rsrc((void*)ws, (short)0, (int)WS_END, 0x00020000); \
  (void)m0; (void)fr; (void)q4; (void)p4; (void)rsW; (void)g;

__device__ __forceinline__ void code_touch(unsigned& junk, int bid, int lane) {
  unsigned long long pc;
  unsigned len, d;
  asm volatile("s_getpc_b64 %0\n.Lct%=:\n\ts_mov_b32 %2, .Lct%=-.Lmk_pc\n\ts_mov_b32 %1, .Lmk_end-.Lmk_pc" : "=s"(pc), "=s"(len), "=s"(d));
  pc -= d;
  const unsigned per = ((len >> 10) + 1) * 128, off = ((bid >> 3) & 7) * per + (lane * 128 < per ? lane * 128 : 0);
  asm volatile("global_load_dword %0, %1, %2" : "+v"(junk) : "v"(off < len - 4 ? off : 0u), "s"(pc) : "memory");
}

__device__ __forceinline__ void publish(__amdgpu_buffer_rsrc_t rsW, int tid, unsigned idx, u64 v) {
  unsigned off = WS_FLAGS + idx * FLS;
  asm volatile("" : "+v"(off), "+v"(v));
  WAITV(0);
  WAITL0; BAR;
  if (tid == 192) __builtin_amdgcn_raw_buffer_store_b64(__builtin_bit_cast(u32x2, v), rsW, off, 0, 16);
  asm volatile("" ::: "memory");
}
__device__ __forceinline__ u64 poll_flags(char* ws, u64 E, int set, int rt, int hp, int lane, unsigned n, u64 mask, u64 now = ~0ull) {
  u64 miss = 0;
  const int l = lane & 15, g = lane >> 4;
  const int fi = set * 256 + (g == 2 ? l * 16 + hp : rt * 16 + l);
  const unsigned need = ((mask >> lane) & 1) ? n : 0u;
  gu64* fp = (gu64*)(ws + (need ? WS_FLAGS + fi * FLS : 0));
  _Pragma("unroll 1") for (unsigned spins = 0; spins < (1u << 18); ++spins) {
    const u64 d = __hip_atomic_load(fp, __ATOMIC_RELAXED, __HIP_MEMORY_SCOPE_AGENT) - E;
    const bool ok = (need == 0u) || (d >= (u64)need && d <= 16ull);
    miss = __ballot(!ok);
    if (!(miss & now)) break;
    __builtin_amdgcn_s_sleep(1);
  }
  asm volatile("s_waitcnt vmcnt(0)" ::: "memory");
  return miss;
}

__device__ __forceinline__ u64 poll_eq(const char* ws, unsigned off, u64 want, u64 mask, u64 now) {
  u64 miss;
  _Pragma("unroll 1") for (unsigned spins = 0; spins < (1u << 18); ++spins) {
    u64 d;
    asm volatile("global_load_dwordx2 %0, %1, %2 sc1\n\ts_waitcnt vmcnt(0)" : "=v"(d) : "v"(off), "s"(ws) : "memory");
    miss = __ballot(d != want) & mask;
    if (!(miss & now)) break;
    __builtin_amdgcn_s_sleep(1);
  }
  return miss;
}

#define G1_MACROS \
  ".macro G1M rb, j, s, z\n" \
  " .set g1d, 160+24*(\\rb)+4*(((\\j)+2)-6*((\\j)>>2))\n .set g1a, 80+40*(\\s)+4*(\\rb)\n .set g1b, 96+40*(\\s)+4*(\\j)\n" \
  " .if ((\\j)>>1) == 1\n  .set g1t, g1a\n  .set g1a, g1b\n  .set g1b, g1t\n .endif\n" \
  " .if \\z\n  v_mfma_f32_16x16x32_bf16 v[g1d:g1d+3], v[g1b:g1b+3], v[g1a:g1a+3], 0\n" \
  " .else\n  v_mfma_f32_16x16x32_bf16 v[g1d:g1d+3], v[g1b:g1b+3], v[g1a:g1a+3], v[g1d:g1d+3]\n .endif\n" \
  ".endm\n" \
  ".macro G1RA rb, s, ra, oa\n" \
  " ds_read_b128 v[80+40*(\\s)+4*(\\rb):83+40*(\\s)+4*(\\rb)], \\ra offset:(\\oa)+(\\rb)*2048\n" \
  ".endm\n" \
  ".macro G1RB k, s, r0, r1, ob\n" \
  " .if (\\k) & 2\n  ds_read_b64_tr_b16 v[96+40*(\\s)+2*(\\k):97+40*(\\s)+2*(\\k)], \\r1 offset:(\\ob)+(((\\k)>>2)+1-3*((\\k)>>3))*2048+((\\k)&1)*512\n" \
  " .else\n  ds_read_b64_tr_b16 v[96+40*(\\s)+2*(\\k):97+40*(\\s)+2*(\\k)], \\r0 offset:(\\ob)+(((\\k)>>2)+1-3*((\\k)>>3))*2048+((\\k)&1)*512\n .endif\n" \
  ".endm\n" \
    \
  ".macro G1BK i, g, r0, r1, ob\n G1RB 4*(\\g)+((\\i)&3), (\\i)>>2, \\r0, \\r1, (\\ob)+((\\i)>>2)*12288\n.endm\n" \
  ".macro G1AK rb, s, a0, a1, oa\n .if \\s\n  G1RA \\rb, 1, \\a1, \\oa\n .else\n  G1RA \\rb, 0, \\a0, \\oa\n .endif\n.endm\n" \
  ".macro G1RDB g, r0, r1, ob\n .set g1i, 0\n .rept 8\n  G1BK g1i, \\g, \\r0, \\r1, \\ob\n  .set g1i, g1i+1\n .endr\n.endm\n" \
  ".macro G1RDA a0, a1, oa\n .set g1i, 0\n .rept 8\n  G1AK (g1i>>1)^2, g1i&1, \\a0, \\a1, \\oa\n  .set g1i, g1i+1\n .endr\n.endm\n" \
    \
  ".macro G1Q g, rb, q, i, z\n G1M \\rb, 2*(\\g)+((((\\i)>>1)^(\\q))&1), (((\\i)+1)>>1)&1, (\\z)&&(((\\i)&1)==0)\n.endm\n" \
    \
  ".macro G1SG g, z, rd, a0, a1, r0, r1, o\n" \
  " .set g1i, 0\n .rept 16\n" \
  "  .if ((\\g) == 0) && (g1i == 8) && (\\rd)\n   s_waitcnt lgkmcnt(7)\n   s_nop 0\n  .endif\n" \
  "  .if ((\\g) == 0) && (g1i == 15)\n   s_waitcnt lgkmcnt(0)\n  .endif\n" \
  "  G1Q \\g, (g1i>>2)^(2-((\\g)&1)), g1i>>2, g1i&3, \\z\n" \
  "  .if \\rd\n" \
  "   .if (\\g) == 0\n    .if g1i < 7\n     G1BK g1i, 2, \\r0, \\r1, \\o\n    .elseif g1i == 8\n     G1BK 7, 2, \\r0, \\r1, \\o\n    .endif\n" \
  "   .elseif (\\g) == 1\n    .if g1i < 8\n     G1BK g1i, 0, \\r0, \\r1, \\o\n    .endif\n" \
  "   .elseif (g1i & 2) == 0\n    G1BK (g1i&1)+2*(g1i>>2), 1, \\r0, \\r1, \\o\n" \
  "   .elseif (\\rd) == 1\n    G1AK (g1i>>2)^2, 1-(g1i&1), \\a0, \\a1, \\o\n   .endif\n" \
  "  .endif\n" \
  "  .set g1i, g1i+1\n .endr\n" \
  ".endm\n" \
  ""
#define G1_SLIDE \
  "G1Q 1, 3, 0, 0, 0\n ds_read2_b64 v[60:63], v49 offset0:32 offset1:48\n ds_read_b128 v[96:99], v48 offset:256\n G1Q 1, 3, 0, 1, 0\n " \
  "ds_read_b128 v[100:103], v48 offset:320\n ds_read2_b32 v[56:57], v52 offset1:16\n G1Q 1, 3, 0, 2, 0\n ds_read_b128 v[136:139], v54 offset:192\n " \
  "ds_read_b128 v[140:143], v54 offset:704\n G1Q 1, 3, 0, 3, 0\n G1Q 1, 2, 1, 0, 0\n G1Q 1, 2, 1, 1, 0\n s_waitcnt lgkmcnt(3)\n " \
  "v_fmac_f32 v240, v96, v62\n G1Q 1, 2, 1, 2, 0\n v_fma_f32 v241, v97, v62, v241\n v_fma_f32 v242, v98, v62, v242\n G1Q 1, 2, 1, 3, 0\n " \
  "v_fma_f32 v243, v99, v62, v243\n v_fma_f32 v244, v100, v62, v244\n G1Q 1, 1, 2, 0, 0\n v_fma_f32 v245, v101, v62, v245\n " \
  "v_fma_f32 v246, v102, v62, v246\n G1Q 1, 1, 2, 1, 0\n v_fma_f32 v247, v103, v62, v247\n v_mul_f32_e64 v240, v63, v240\n G1Q 1, 1, 2, 2, 0\n " \
  "v_mul_f32_e64 v241, v63, v241\n v_mul_f32_e64 v242, v63, v242\n G1Q 1, 1, 2, 3, 0\n v_mul_f32_e64 v243, v63, v243\n v_mul_f32_e64 v244, v63, v244\n " \
  "G1Q 1, 0, 3, 0, 0\n v_mul_f32_e64 v245, v63, v245\n v_mul_f32_e64 v246, v63, v246\n G1Q 1, 0, 3, 1, 0\n v_mul_f32_e64 v247, v63, v247\n " \
  "v_cvt_pk_bf16_f32 v240, v240, v241\n G1Q 1, 0, 3, 2, 0\n v_cvt_pk_bf16_f32 v241, v242, v243\n v_cvt_pk_bf16_f32 v242, v244, v245\n " \
  "G1Q 1, 0, 3, 3, 0\n v_cvt_pk_bf16_f32 v243, v246, v247\n s_waitcnt lgkmcnt(0)\n v_fmac_f32 v248, v136, v56\n G1Q 2, 2, 0, 0, 0\n " \
  "ds_read2_b64 v[144:147], v49 offset1:16\n v_fma_f32 v249, v137, v56, v249\n G1Q 2, 2, 0, 1, 0\n ds_read_b128 v[148:151], v48\n " \
  "v_fma_f32 v250, v138, v56, v250\n G1Q 2, 2, 0, 2, 0\n ds_read_b128 v[104:107], v48 offset:64\n v_fma_f32 v251, v139, v56, v251\n G1Q 2, 2, 0, 3, 0\n " \
  "ds_read_b128 v[108:111], v54 offset:128\n v_fma_f32 v252, v136, v57, v252\n G1Q 2, 3, 1, 0, 0\n v_fma_f32 v253, v137, v57, v253\n " \
  "v_fma_f32 v254, v138, v57, v254\n G1Q 2, 3, 1, 1, 0\n ds_read_b128 v[88:91], v54 offset:640\n v_fma_f32 v255, v139, v57, v255\n G1Q 2, 3, 1, 2, 0\n " \
  "ds_read_b128 v[128:131], v54\n v_mul_f32_e64 v248, v140, v248\n G1Q 2, 3, 1, 3, 0\n v_mul_f32_e64 v249, v141, v249\n v_mul_f32_e64 v250, v142, v250\n " \
  "G1Q 2, 0, 2, 0, 0\n v_mul_f32_e64 v251, v143, v251\n v_mul_f32_e64 v252, v140, v252\n G1Q 2, 0, 2, 1, 0\n ds_read_b128 v[92:95], v54 offset:512\n " \
  "v_mul_f32_e64 v253, v141, v253\n G1Q 2, 0, 2, 2, 0\n v_mul_f32_e64 v254, v142, v254\n v_mul_f32_e64 v255, v143, v255\n G1Q 2, 0, 2, 3, 0\n " \
  "v_cvt_pk_bf16_f32 v248, v248, v249\n v_cvt_pk_bf16_f32 v249, v250, v251\n G1Q 2, 1, 3, 0, 0\n v_cvt_pk_bf16_f32 v255, v254, v255\n " \
  "v_cvt_pk_bf16_f32 v254, v252, v253\n G1Q 2, 1, 3, 1, 0\n v_min_u32_e64 v136, -2, v240\n v_min_u32_e64 v137, -2, v241\n v_min_u32_e64 v138, -2, v242\n " \
  "G1Q 2, 1, 3, 2, 0\n v_min_u32_e64 v139, -2, v243\n v_min_u32_e64 v140, -2, v248\n v_min_u32_e64 v141, -2, v249\n G1Q 2, 1, 3, 3, 0\n " \
  "v_min_u32_e64 v142, -2, v254\n v_min_u32_e64 v143, -2, v255\n ds_read_b128 v[112:115], v54 offset:64\n ds_read_b128 v[116:119], v54 offset:576\n " \
  "s_cmp_eq_u32 %[wm], 0\n s_cbranch_scc1 .Lg1d%=\n buffer_store_dwordx4 v[136:139], v55, %[rs], 0 offen sc1\n " \
  "buffer_store_dwordx4 v[140:143], v55, %[rs], 0 offen offset:1024 sc1\n ds_read_b32 v53, v59\n ds_read_b32 v53, v59\n ds_read_b32 v53, v59\n " \
  "s_branch .Lg1e%=\n .Lg1d%=:\n v_mov_b32 v53, 1\n ds_write_b128 v58, v[136:139]\n ds_write_b128 v58, v[140:143] offset:1024\n ds_write_b32 v59, v53\n " \
  ".Lg1e%=:\n s_waitcnt lgkmcnt(8)\n v_pk_fma_f32 v[232:233], v[148:149], v[62:63], v[232:233] op_sel_hi:[1,0,1]\n " \
  "v_pk_fma_f32 v[234:235], v[150:151], v[62:63], v[234:235] op_sel_hi:[1,0,1]\n " \
  "v_pk_fma_f32 v[236:237], v[104:105], v[62:63], v[236:237] op_sel_hi:[1,0,1]\n " \
  "v_pk_fma_f32 v[238:239], v[106:107], v[62:63], v[238:239] op_sel_hi:[1,0,1]\n " \
  "v_pk_fma_f32 v[216:217], v[96:97], v[60:61], v[216:217] op_sel_hi:[1,0,1]\n " \
  "v_pk_fma_f32 v[218:219], v[98:99], v[60:61], v[218:219] op_sel_hi:[1,0,1]\n " \
  "v_pk_fma_f32 v[220:221], v[100:101], v[60:61], v[220:221] op_sel_hi:[1,0,1]\n " \
  "v_pk_fma_f32 v[222:223], v[102:103], v[60:61], v[222:223] op_sel_hi:[1,0,1]\n v_pk_mul_f32 v[216:217], v[60:61], v[216:217] op_sel:[1,0]\n " \
  "v_pk_mul_f32 v[218:219], v[60:61], v[218:219] op_sel:[1,0]\n v_pk_mul_f32 v[220:221], v[60:61], v[220:221] op_sel:[1,0]\n " \
  "v_pk_mul_f32 v[222:223], v[60:61], v[222:223] op_sel:[1,0]\n v_cvt_pk_bf16_f32 v232, v232, v233\n v_cvt_pk_bf16_f32 v233, v234, v235\n " \
  "v_cvt_pk_bf16_f32 v234, v236, v237\n v_cvt_pk_bf16_f32 v235, v238, v239\n v_cvt_pk_bf16_f32 v216, v216, v217\n v_cvt_pk_bf16_f32 v217, v218, v219\n " \
  "v_cvt_pk_bf16_f32 v218, v220, v221\n v_cvt_pk_bf16_f32 v219, v222, v223\n " \
  "v_pk_fma_f32 v[208:209], v[148:149], v[60:61], v[208:209] op_sel_hi:[1,0,1]\n " \
  "v_pk_fma_f32 v[210:211], v[150:151], v[60:61], v[210:211] op_sel_hi:[1,0,1]\n " \
  "v_pk_fma_f32 v[212:213], v[104:105], v[60:61], v[212:213] op_sel_hi:[1,0,1]\n " \
  "v_pk_fma_f32 v[214:215], v[106:107], v[60:61], v[214:215] op_sel_hi:[1,0,1]\n " \
  "v_pk_fma_f32 v[192:193], v[96:97], v[146:147], v[192:193] op_sel_hi:[1,0,1]\n " \
  "v_pk_fma_f32 v[194:195], v[98:99], v[146:147], v[194:195] op_sel_hi:[1,0,1]\n " \
  "v_pk_fma_f32 v[196:197], v[100:101], v[146:147], v[196:197] op_sel_hi:[1,0,1]\n " \
  "v_pk_fma_f32 v[198:199], v[102:103], v[146:147], v[198:199] op_sel_hi:[1,0,1]\n v_pk_mul_f32 v[192:193], v[146:147], v[192:193] op_sel:[1,0]\n " \
  "v_pk_mul_f32 v[194:195], v[146:147], v[194:195] op_sel:[1,0]\n v_pk_mul_f32 v[196:197], v[146:147], v[196:197] op_sel:[1,0]\n " \
  "v_pk_mul_f32 v[198:199], v[146:147], v[198:199] op_sel:[1,0]\n v_mfma_f32_16x16x32_bf16 v[120:123], v[240:243], v[232:235], v[40:43]\n " \
  "v_mfma_f32_16x16x32_bf16 v[236:239], v[216:219], v[232:235], v[36:39]\n v_cvt_pk_bf16_f32 v208, v208, v209\n v_cvt_pk_bf16_f32 v209, v210, v211\n " \
  "v_cvt_pk_bf16_f32 v210, v212, v213\n v_cvt_pk_bf16_f32 v211, v214, v215\n v_cvt_pk_bf16_f32 v192, v192, v193\n v_cvt_pk_bf16_f32 v193, v194, v195\n " \
  "v_cvt_pk_bf16_f32 v194, v196, v197\n v_cvt_pk_bf16_f32 v195, v198, v199\n " \
  "v_pk_fma_f32 v[184:185], v[148:149], v[146:147], v[184:185] op_sel_hi:[1,0,1]\n " \
  "v_pk_fma_f32 v[186:187], v[150:151], v[146:147], v[186:187] op_sel_hi:[1,0,1]\n " \
  "v_pk_fma_f32 v[188:189], v[104:105], v[146:147], v[188:189] op_sel_hi:[1,0,1]\n " \
  "v_pk_fma_f32 v[190:191], v[106:107], v[146:147], v[190:191] op_sel_hi:[1,0,1]\n " \
  "v_pk_fma_f32 v[168:169], v[96:97], v[144:145], v[168:169] op_sel_hi:[1,0,1]\n " \
  "v_pk_fma_f32 v[170:171], v[98:99], v[144:145], v[170:171] op_sel_hi:[1,0,1]\n " \
  "v_pk_fma_f32 v[172:173], v[100:101], v[144:145], v[172:173] op_sel_hi:[1,0,1]\n " \
  "v_pk_fma_f32 v[174:175], v[102:103], v[144:145], v[174:175] op_sel_hi:[1,0,1]\n v_pk_mul_f32 v[168:169], v[144:145], v[168:169] op_sel:[1,0]\n " \
  "v_pk_mul_f32 v[170:171], v[144:145], v[170:171] op_sel:[1,0]\n v_pk_mul_f32 v[172:173], v[144:145], v[172:173] op_sel:[1,0]\n " \
  "v_pk_mul_f32 v[174:175], v[144:145], v[174:175] op_sel:[1,0]\n v_mfma_f32_16x16x32_bf16 v[220:223], v[216:219], v[208:211], v[40:43]\n " \
  "v_mfma_f32_16x16x32_bf16 v[212:215], v[192:195], v[208:211], v[36:39]\n v_cvt_pk_bf16_f32 v184, v184, v185\n v_cvt_pk_bf16_f32 v185, v186, v187\n " \
  "v_cvt_pk_bf16_f32 v186, v188, v189\n v_cvt_pk_bf16_f32 v187, v190, v191\n v_cvt_pk_bf16_f32 v168, v168, v169\n v_cvt_pk_bf16_f32 v169, v170, v171\n " \
  "v_cvt_pk_bf16_f32 v170, v172, v173\n v_cvt_pk_bf16_f32 v171, v174, v175\n " \
  "v_pk_fma_f32 v[160:161], v[148:149], v[144:145], v[160:161] op_sel_hi:[1,0,1]\n " \
  "v_pk_fma_f32 v[162:163], v[150:151], v[144:145], v[162:163] op_sel_hi:[1,0,1]\n " \
  "v_pk_fma_f32 v[164:165], v[104:105], v[144:145], v[164:165] op_sel_hi:[1,0,1]\n " \
  "v_pk_fma_f32 v[166:167], v[106:107], v[144:145], v[166:167] op_sel_hi:[1,0,1]\n s_waitcnt lgkmcnt(3)\n " \
  "v_pk_fma_f32 v[176:177], v[128:129], v[56:57], v[176:177] op_sel_hi:[1,0,1]\n " \
  "v_pk_fma_f32 v[178:179], v[130:131], v[56:57], v[178:179] op_sel_hi:[1,0,1]\n " \
  "v_pk_fma_f32 v[180:181], v[128:129], v[56:57], v[180:181] op_sel:[0,1,0]\n v_pk_fma_f32 v[182:183], v[130:131], v[56:57], v[182:183] op_sel:[0,1,0]\n " \
  "v_pk_mul_f32 v[176:177], v[176:177], v[92:93]\n v_pk_mul_f32 v[178:179], v[178:179], v[94:95]\n v_pk_mul_f32 v[180:181], v[180:181], v[92:93]\n " \
  "v_pk_mul_f32 v[182:183], v[182:183], v[94:95]\n v_mfma_f32_16x16x32_bf16 v[196:199], v[192:195], v[184:187], v[40:43]\n " \
  "v_mfma_f32_16x16x32_bf16 v[188:191], v[168:171], v[184:187], v[36:39]\n v_cvt_pk_bf16_f32 v160, v160, v161\n v_cvt_pk_bf16_f32 v161, v162, v163\n " \
  "v_cvt_pk_bf16_f32 v162, v164, v165\n v_cvt_pk_bf16_f32 v163, v166, v167\n v_cvt_pk_bf16_f32 v174, v176, v177\n v_cvt_pk_bf16_f32 v175, v178, v179\n " \
  "v_cvt_pk_bf16_f32 v180, v180, v181\n v_cvt_pk_bf16_f32 v181, v182, v183\n " \
  "v_pk_fma_f32 v[224:225], v[108:109], v[56:57], v[224:225] op_sel_hi:[1,0,1]\n " \
  "v_pk_fma_f32 v[226:227], v[110:111], v[56:57], v[226:227] op_sel_hi:[1,0,1]\n " \
  "v_pk_fma_f32 v[228:229], v[108:109], v[56:57], v[228:229] op_sel:[0,1,0]\n v_pk_fma_f32 v[230:231], v[110:111], v[56:57], v[230:231] op_sel:[0,1,0]\n " \
  "v_pk_mul_f32 v[224:225], v[224:225], v[88:89]\n v_pk_mul_f32 v[226:227], v[226:227], v[90:91]\n v_pk_mul_f32 v[228:229], v[228:229], v[88:89]\n " \
  "v_pk_mul_f32 v[230:231], v[230:231], v[90:91]\n v_mfma_f32_16x16x32_bf16 v[124:127], v[168:171], v[160:163], v[40:43]\n " \
  "v_cvt_pk_bf16_f32 v246, v224, v225\n v_cvt_pk_bf16_f32 v247, v226, v227\n v_cvt_pk_bf16_f32 v252, v228, v229\n v_cvt_pk_bf16_f32 v253, v230, v231\n " \
  "v_max3_f32 v140, v236, v237, v238\n v_max3_f32 v138, v212, v213, v214\n v_max3_f32 v141, v239, v120, v121\n v_max3_f32 v139, v215, v220, v221\n " \
  "v_max3_f32 v140, v140, v122, v123\n v_max3_f32 v138, v138, v222, v223\n v_max_f32 v140, v140, v141\n v_max_f32 v138, v138, v139\n " \
  "v_max3_f32 v136, v188, v189, v190\n v_max3_f32 v137, v191, v196, v197\n v_max3_f32 v136, v136, v198, v199\n " \
  "v_pk_fma_f32 v[200:201], v[112:113], v[56:57], v[200:201] op_sel_hi:[1,0,1]\n " \
  "v_pk_fma_f32 v[202:203], v[114:115], v[56:57], v[202:203] op_sel_hi:[1,0,1]\n v_max_f32 v136, v136, v137\n v_mov_b32 v142, v140\n " \
  "v_pk_fma_f32 v[204:205], v[112:113], v[56:57], v[204:205] op_sel:[0,1,0]\n v_pk_fma_f32 v[206:207], v[114:115], v[56:57], v[206:207] op_sel:[0,1,0]\n " \
  "v_permlane16_swap_b32 v136, v138\n v_permlane16_swap_b32 v140, v142\n v_pk_mul_f32 v[200:201], v[200:201], v[116:117]\n " \
  "v_pk_mul_f32 v[202:203], v[202:203], v[118:119]\n v_max_f32 v136, v136, v138\n v_max_f32 v140, v140, v142\n " \
  "v_pk_mul_f32 v[204:205], v[204:205], v[116:117]\n v_pk_mul_f32 v[206:207], v[206:207], v[118:119]\n v_permlane32_swap_b32 v136, v140\n " \
  "v_mul_f32 v152, 0x3e8293ee, v63\n v_mul_f32 v154, 0x3e8293ee, v61\n v_max_f32 v136, v136, v140\n v_mov_b32 v140, v136\n " \
  "v_mul_f32 v156, 0x3e8293ee, v147\n v_mul_f32 v158, 0x3e8293ee, v145\n v_permlane32_swap_b32 v136, v140\n v_mov_b32 v132, 0x3f803f80\n " \
  "v_mov_b32 v133, 0x3f803f80\n v_mov_b32 v138, v136\n v_mov_b32 v134, 0x3f803f80\n v_mov_b32 v135, 0x3f803f80\n v_permlane16_swap_b32 v136, v138\n " \
  "v_cvt_pk_bf16_f32 v244, v200, v201\n v_cvt_pk_bf16_f32 v245, v202, v203\n v_cvt_pk_bf16_f32 v250, v204, v205\n v_cvt_pk_bf16_f32 v251, v206, v207\n " \
  "v_cvt_pk_bf16_f32 v176, v200, v201\n v_cvt_pk_bf16_f32 v177, v202, v203\n v_cvt_pk_bf16_f32 v182, v204, v205\n v_cvt_pk_bf16_f32 v183, v206, v207\n " \
  "v_mul_f32_e64 v153, v152, -v140\n v_mul_f32_e64 v155, v154, -v138\n v_mul_f32_e64 v157, v156, -v136\n " \
  "v_pk_fma_f32 v[236:237], v[236:237], v[152:153], v[152:153] op_sel:[0,0,1] op_sel_hi:[1,0,1]\n " \
  "v_pk_fma_f32 v[238:239], v[238:239], v[152:153], v[152:153] op_sel:[0,0,1] op_sel_hi:[1,0,1]\n " \
  "v_pk_fma_f32 v[120:121], v[120:121], v[152:153], v[152:153] op_sel:[0,0,1] op_sel_hi:[1,0,1]\n " \
  "v_pk_fma_f32 v[122:123], v[122:123], v[152:153], v[152:153] op_sel:[0,0,1] op_sel_hi:[1,0,1]\n v_exp_f32 v236, v236\n v_exp_f32 v237, v237\n " \
  "v_exp_f32 v238, v238\n v_exp_f32 v239, v239\n v_exp_f32 v120, v120\n v_exp_f32 v121, v121\n v_exp_f32 v122, v122\n v_exp_f32 v123, v123\n " \
  "v_pk_fma_f32 v[212:213], v[212:213], v[154:155], v[154:155] op_sel:[0,0,1] op_sel_hi:[1,0,1]\n " \
  "v_pk_fma_f32 v[214:215], v[214:215], v[154:155], v[154:155] op_sel:[0,0,1] op_sel_hi:[1,0,1]\n " \
  "v_pk_fma_f32 v[220:221], v[220:221], v[154:155], v[154:155] op_sel:[0,0,1] op_sel_hi:[1,0,1]\n " \
  "v_pk_fma_f32 v[222:223], v[222:223], v[154:155], v[154:155] op_sel:[0,0,1] op_sel_hi:[1,0,1]\n v_cvt_pk_bf16_f32 v236, v236, v237\n " \
  "v_cvt_pk_bf16_f32 v237, v238, v239\n v_cvt_pk_bf16_f32 v238, v120, v121\n v_cvt_pk_bf16_f32 v239, v122, v123\n v_exp_f32 v212, v212\n " \
  "v_exp_f32 v213, v213\n v_mfma_f32_16x16x32_bf16 v[120:123], v[132:135], v[236:239], 0\n " \
  "v_mfma_f32_16x16x32_bf16 v[232:235], v[246:249], v[236:239], 0\n v_mfma_f32_16x16x32_bf16 v[240:243], v[252:255], v[236:239], 0\n " \
  "v_exp_f32 v214, v214\n v_exp_f32 v215, v215\n v_exp_f32 v220, v220\n v_exp_f32 v221, v221\n v_exp_f32 v222, v222\n v_exp_f32 v223, v223\n " \
  "v_pk_fma_f32 v[188:189], v[188:189], v[156:157], v[156:157] op_sel:[0,0,1] op_sel_hi:[1,0,1]\n " \
  "v_pk_fma_f32 v[190:191], v[190:191], v[156:157], v[156:157] op_sel:[0,0,1] op_sel_hi:[1,0,1]\n " \
  "v_pk_fma_f32 v[196:197], v[196:197], v[156:157], v[156:157] op_sel:[0,0,1] op_sel_hi:[1,0,1]\n " \
  "v_pk_fma_f32 v[198:199], v[198:199], v[156:157], v[156:157] op_sel:[0,0,1] op_sel_hi:[1,0,1]\n v_cvt_pk_bf16_f32 v212, v212, v213\n " \
  "v_cvt_pk_bf16_f32 v213, v214, v215\n v_cvt_pk_bf16_f32 v214, v220, v221\n v_cvt_pk_bf16_f32 v215, v222, v223\n v_exp_f32 v188, v188\n " \
  "s_cmp_eq_u32 %[wm], 0\n s_cbranch_scc1 .Lg1f%=\n ds_read_b32 v53, v59\n ds_read_b128 v[80:83], v58\n ds_read_b64 v[172:173], v58 offset:1024\n " \
  "ds_read_b64 v[178:179], v58 offset:1032\n s_branch .Lg1g%=\n .Lg1f%=:\n buffer_load_dwordx4 v[80:83], v55, %[rs], 0 offen sc1\n " \
  "buffer_load_dwordx2 v[172:173], v55, %[rs], 0 offen offset:1024 sc1\n buffer_load_dwordx2 v[178:179], v55, %[rs], 0 offen offset:1032 sc1\n " \
  ".Lg1g%=:\n v_exp_f32 v189, v189\n v_mfma_f32_16x16x32_bf16 v[220:223], v[132:135], v[212:215], 0\n " \
  "v_mfma_f32_16x16x32_bf16 v[224:227], v[244:247], v[212:215], 0\n v_mfma_f32_16x16x32_bf16 v[228:231], v[250:253], v[212:215], 0\n " \
  "v_exp_f32 v190, v190\n v_exp_f32 v191, v191\n v_exp_f32 v196, v196\n v_exp_f32 v197, v197\n v_exp_f32 v198, v198\n v_exp_f32 v199, v199\n " \
  "v_rcp_f32 v120, v120\n v_cvt_pk_bf16_f32 v188, v188, v189\n v_pk_mul_f32 v[232:233], v[232:233], v[120:121] op_sel_hi:[1,0]\n " \
  "v_pk_mul_f32 v[234:235], v[234:235], v[120:121] op_sel_hi:[1,0]\n v_pk_mul_f32 v[240:241], v[240:241], v[120:121] op_sel_hi:[1,0]\n " \
  "v_pk_mul_f32 v[242:243], v[242:243], v[120:121] op_sel_hi:[1,0]\n v_cvt_pk_bf16_f32 v189, v190, v191\n v_cvt_pk_bf16_f32 v190, v196, v197\n " \
  "v_cvt_pk_bf16_f32 v191, v198, v199\n v_rcp_f32 v220, v220\n v_cvt_pk_bf16_f32 v232, v232, v233\n v_cvt_pk_bf16_f32 v233, v234, v235\n " \
  "v_mfma_f32_16x16x32_bf16 v[196:199], v[132:135], v[188:191], 0\n v_mfma_f32_16x16x32_bf16 v[200:203], v[174:177], v[188:191], 0\n " \
  "v_mfma_f32_16x16x32_bf16 v[204:207], v[180:183], v[188:191], 0\n v_cvt_pk_bf16_f32 v234, v240, v241\n v_cvt_pk_bf16_f32 v235, v242, v243\n " \
  "buffer_store_dwordx4 v[232:235], v51, %[rs], 0 offen offset:3072 sc1\n ds_write_b128 v50, v[232:235] offset:3072\n " \
  "v_pk_mul_f32 v[224:225], v[224:225], v[220:221] op_sel_hi:[1,0]\n v_pk_mul_f32 v[226:227], v[226:227], v[220:221] op_sel_hi:[1,0]\n " \
  "v_pk_mul_f32 v[228:229], v[228:229], v[220:221] op_sel_hi:[1,0]\n v_pk_mul_f32 v[230:231], v[230:231], v[220:221] op_sel_hi:[1,0]\n " \
  "v_cvt_pk_bf16_f32 v224, v224, v225\n v_cvt_pk_bf16_f32 v225, v226, v227\n v_cvt_pk_bf16_f32 v226, v228, v229\n v_cvt_pk_bf16_f32 v227, v230, v231\n " \
  "v_rcp_f32 v196, v196\n buffer_store_dwordx4 v[224:227], v51, %[rs], 0 offen offset:2048 sc1\n ds_write_b128 v50, v[224:227] offset:2048\n " \
  "v_pk_mul_f32 v[200:201], v[200:201], v[196:197] op_sel_hi:[1,0]\n v_pk_mul_f32 v[202:203], v[202:203], v[196:197] op_sel_hi:[1,0]\n " \
  "v_pk_mul_f32 v[204:205], v[204:205], v[196:197] op_sel_hi:[1,0]\n v_pk_mul_f32 v[206:207], v[206:207], v[196:197] op_sel_hi:[1,0]\n " \
  "v_cvt_pk_bf16_f32 v200, v200, v201\n v_cvt_pk_bf16_f32 v201, v202, v203\n v_cvt_pk_bf16_f32 v202, v204, v205\n v_cvt_pk_bf16_f32 v203, v206, v207\n " \
  "buffer_store_dwordx4 v[200:203], v51, %[rs], 0 offen offset:1024 sc1\n ds_write_b128 v50, v[200:203] offset:1024\n s_mov_b32 vcc_hi, 0x40000\n " \
  "s_cmp_eq_u32 %[wm], 0\n s_cbranch_scc1 .Lg1h%=\n s_waitcnt lgkmcnt(3)\n .Lg1i%=:\n v_readfirstlane_b32 vcc_lo, v53\n s_nop 1\n " \
  "s_cmp_lg_u32 vcc_lo, 0\n s_cbranch_scc1 .Lg1k%=\n ds_read_b32 v53, v59\n ds_read_b128 v[80:83], v58\n ds_read_b64 v[172:173], v58 offset:1024\n " \
  "ds_read_b64 v[178:179], v58 offset:1032\n s_waitcnt lgkmcnt(0)\n s_sub_u32 vcc_hi, vcc_hi, 1\n s_cbranch_scc0 .Lg1i%=\n s_branch .Lg1k%=\n .Lg1h%=:\n " \
  "s_mov_b32 m0, 0x40000\n s_waitcnt vmcnt(3)\n .Lg1j%=:\n v_max3_u32 v136, v80, v81, v82\n v_max3_u32 v137, v83, v172, v173\n " \
  "v_max3_u32 v136, v136, v178, v179\n v_max_u32 v136, v136, v137\n v_cmp_eq_u32 vcc, -1, v136\n s_cbranch_vccz .Lg1k%=\n " \
  "buffer_load_dwordx4 v[80:83], v55, %[rs], 0 offen sc1\n buffer_load_dwordx2 v[172:173], v55, %[rs], 0 offen offset:1024 sc1\n " \
  "buffer_load_dwordx2 v[178:179], v55, %[rs], 0 offen offset:1032 sc1\n s_waitcnt vmcnt(0)\n s_sub_u32 m0, m0, 1\n s_cbranch_scc0 .Lg1j%=\n .Lg1k%=:\n " \
  "v_mfma_f32_16x16x32_bf16 v[164:167], v[80:83], v[160:163], v[44:47]\n s_nop 7\n v_max3_f32 v136, v164, v165, v166\n " \
  "v_max3_f32 v137, v167, v124, v125\n v_max3_f32 v136, v136, v126, v127\n v_max_f32 v136, v136, v137\n v_mov_b32 v137, v136\n s_nop 1\n " \
  "v_permlane16_swap_b32 v136, v137\n v_max_f32 v136, v136, v137\n v_mov_b32 v137, v136\n s_nop 1\n v_permlane32_swap_b32 v136, v137\n " \
  "v_max_f32 v136, v136, v137\n v_mul_f32_e64 v159, v158, -v136\n " \
  "v_pk_fma_f32 v[164:165], v[164:165], v[158:159], v[158:159] op_sel:[0,0,1] op_sel_hi:[1,0,1]\n " \
  "v_pk_fma_f32 v[166:167], v[166:167], v[158:159], v[158:159] op_sel:[0,0,1] op_sel_hi:[1,0,1]\n " \
  "v_pk_fma_f32 v[124:125], v[124:125], v[158:159], v[158:159] op_sel:[0,0,1] op_sel_hi:[1,0,1]\n " \
  "v_pk_fma_f32 v[126:127], v[126:127], v[158:159], v[158:159] op_sel:[0,0,1] op_sel_hi:[1,0,1]\n v_exp_f32 v164, v164\n v_exp_f32 v165, v165\n " \
  "v_exp_f32 v166, v166\n v_exp_f32 v167, v167\n v_exp_f32 v124, v124\n v_exp_f32 v125, v125\n v_exp_f32 v126, v126\n v_exp_f32 v127, v127\n " \
  "v_cvt_pk_bf16_f32 v164, v164, v165\n v_cvt_pk_bf16_f32 v165, v166, v167\n v_cvt_pk_bf16_f32 v166, v124, v125\n v_cvt_pk_bf16_f32 v167, v126, v127\n " \
  "s_nop 1\n v_mfma_f32_16x16x32_bf16 v[124:127], v[132:135], v[164:167], 0\n v_mfma_f32_16x16x32_bf16 v[168:171], v[172:175], v[164:167], 0\n " \
  "v_mfma_f32_16x16x32_bf16 v[84:87], v[178:181], v[164:167], 0\n s_nop 5\n v_rcp_f32 v124, v124\n s_nop 0\n " \
  "v_pk_mul_f32 v[168:169], v[168:169], v[124:125] op_sel_hi:[1,0]\n v_pk_mul_f32 v[170:171], v[170:171], v[124:125] op_sel_hi:[1,0]\n " \
  "v_pk_mul_f32 v[84:85], v[84:85], v[124:125] op_sel_hi:[1,0]\n v_pk_mul_f32 v[86:87], v[86:87], v[124:125] op_sel_hi:[1,0]\n " \
  "v_cvt_pk_bf16_f32 v168, v168, v169\n v_cvt_pk_bf16_f32 v169, v170, v171\n v_cvt_pk_bf16_f32 v170, v84, v85\n v_cvt_pk_bf16_f32 v171, v86, v87\n " \
  "buffer_store_dwordx4 v[168:171], v51, %[rs], 0 offen offset:0 sc1\n ds_write_b128 v50, v[168:171] offset:0\n "
#define G1_HEDGE \
  "G1Q 1, 1, 0, 0, 0\n ds_read2_b64 v[140:143], v49 offset1:16\n ds_read_b128 v[96:99], v48 offset:256\n G1Q 1, 1, 0, 1, 0\n " \
  "ds_read_b128 v[100:103], v48 offset:320\n ds_read2_b64 v[136:139], v49 offset0:32 offset1:48\n G1Q 1, 1, 0, 2, 0\n ds_read_b64_tr_b16 v[36:37], v53\n " \
  "ds_read_b64_tr_b16 v[38:39], v53 offset:1024\n G1Q 1, 1, 0, 3, 0\n ds_read_b64_tr_b16 v[40:41], v53 offset:32\n " \
  "ds_read_b64_tr_b16 v[42:43], v53 offset:1056\n G1Q 1, 0, 1, 0, 0\n G1Q 1, 0, 1, 1, 0\n s_waitcnt lgkmcnt(4)\n v_fmac_f32 v168, v96, v140\n " \
  "G1Q 1, 0, 1, 2, 0\n v_fma_f32 v169, v97, v140, v169\n v_fma_f32 v170, v98, v140, v170\n G1Q 1, 0, 1, 3, 0\n v_fma_f32 v171, v99, v140, v171\n " \
  "v_fma_f32 v172, v100, v140, v172\n G1Q 1, 3, 2, 0, 0\n v_fma_f32 v173, v101, v140, v173\n v_fma_f32 v174, v102, v140, v174\n G1Q 1, 3, 2, 1, 0\n " \
  "v_fma_f32 v175, v103, v140, v175\n v_cvt_pk_bf16_f32 v168, v168, v169\n G1Q 1, 3, 2, 2, 0\n v_cvt_pk_bf16_f32 v169, v170, v171\n " \
  "v_cvt_pk_bf16_f32 v170, v172, v173\n G1Q 1, 3, 2, 3, 0\n v_cvt_pk_bf16_f32 v171, v174, v175\n v_fma_f32 v192, v96, v142, v192\n G1Q 1, 2, 3, 0, 0\n " \
  "v_fma_f32 v193, v97, v142, v193\n v_fma_f32 v194, v98, v142, v194\n G1Q 1, 2, 3, 1, 0\n v_fma_f32 v195, v99, v142, v195\n " \
  "v_fma_f32 v196, v100, v142, v196\n G1Q 1, 2, 3, 2, 0\n v_fma_f32 v197, v101, v142, v197\n v_fma_f32 v198, v102, v142, v198\n G1Q 1, 2, 3, 3, 0\n " \
  "v_fma_f32 v199, v103, v142, v199\n v_cvt_pk_bf16_f32 v192, v192, v193\n G1Q 2, 2, 0, 0, 0\n ds_read2_b32 v[144:145], v52 offset1:16\n " \
  "v_cvt_pk_bf16_f32 v193, v194, v195\n G1Q 2, 2, 0, 1, 0\n ds_read_b128 v[148:151], v48\n v_cvt_pk_bf16_f32 v194, v196, v197\n G1Q 2, 2, 0, 2, 0\n " \
  "v_cvt_pk_bf16_f32 v195, v198, v199\n v_fma_f32 v216, v96, v136, v216\n G1Q 2, 2, 0, 3, 0\n v_fma_f32 v217, v97, v136, v217\n " \
  "v_fma_f32 v218, v98, v136, v218\n G1Q 2, 3, 1, 0, 0\n v_fma_f32 v219, v99, v136, v219\n v_fma_f32 v220, v100, v136, v220\n G1Q 2, 3, 1, 1, 0\n " \
  "ds_read_b128 v[88:91], v48 offset:64\n v_fma_f32 v221, v101, v136, v221\n v_fma_f32 v222, v102, v136, v222\n G1Q 2, 3, 1, 2, 0\n " \
  "ds_read_b128 v[128:131], v54\n v_fma_f32 v223, v103, v136, v223\n v_cvt_pk_bf16_f32 v216, v216, v217\n G1Q 2, 3, 1, 3, 0\n " \
  "v_cvt_pk_bf16_f32 v217, v218, v219\n v_cvt_pk_bf16_f32 v218, v220, v221\n G1Q 2, 0, 2, 0, 0\n v_cvt_pk_bf16_f32 v219, v222, v223\n " \
  "v_fma_f32 v240, v96, v138, v240\n G1Q 2, 0, 2, 1, 0\n ds_read_b128 v[92:95], v54 offset:512\n v_fma_f32 v241, v97, v138, v241\n " \
  "v_fma_f32 v242, v98, v138, v242\n G1Q 2, 0, 2, 2, 0\n ds_read_b128 v[132:135], v54 offset:64\n v_fma_f32 v243, v99, v138, v243\n " \
  "v_fma_f32 v244, v100, v138, v244\n G1Q 2, 0, 2, 3, 0\n v_fma_f32 v245, v101, v138, v245\n v_fma_f32 v246, v102, v138, v246\n G1Q 2, 1, 3, 0, 0\n " \
  "v_fma_f32 v247, v103, v138, v247\n v_cvt_pk_bf16_f32 v240, v240, v241\n G1Q 2, 1, 3, 1, 0\n v_cvt_pk_bf16_f32 v241, v242, v243\n " \
  "v_cvt_pk_bf16_f32 v242, v244, v245\n G1Q 2, 1, 3, 2, 0\n v_cvt_pk_bf16_f32 v243, v246, v247\n G1Q 2, 1, 3, 3, 0\n s_waitcnt lgkmcnt(6)\n " \
  "v_mfma_f32_16x16x32_bf16 v[172:175], v[36:39], v[168:171], 0\n ds_read_b128 v[112:115], v54 offset:576\n ds_read_b128 v[116:119], v54 offset:128\n " \
  "v_mfma_f32_16x16x32_bf16 v[80:83], v[40:43], v[168:171], 0\n ds_read_b128 v[152:155], v54 offset:640\n ds_read_b128 v[156:159], v54 offset:192\n " \
  "v_mfma_f32_16x16x32_bf16 v[196:199], v[36:39], v[192:195], 0\n ds_read_b128 v[120:123], v54 offset:704\n v_mul_f32 v44, 0x3fb8aa3b, v141\n " \
  "v_mfma_f32_16x16x32_bf16 v[84:87], v[40:43], v[192:195], 0\n v_mul_f32 v46, 0x3fb8aa3b, v143\n v_mul_f32 v104, 0x3fb8aa3b, v137\n " \
  "v_mfma_f32_16x16x32_bf16 v[220:223], v[36:39], v[216:219], 0\n v_mul_f32 v106, 0x3fb8aa3b, v139\n " \
  "v_mfma_f32_16x16x32_bf16 v[124:127], v[40:43], v[216:219], 0\n v_mfma_f32_16x16x32_bf16 v[244:247], v[36:39], v[240:243], 0\n " \
  "v_mfma_f32_16x16x32_bf16 v[108:111], v[40:43], v[240:243], 0\n v_max3_f32 v168, v172, v173, v174\n v_max3_f32 v169, -v172, -v173, -v174\n " \
  "v_max3_f32 v168, v168, v175, v80\n v_max3_f32 v169, v169, -v175, -v80\n v_max3_f32 v168, v168, v81, v82\n v_max3_f32 v169, v169, -v81, -v82\n " \
  "v_max_f32_e64 v168, v168, v83\n v_max_f32_e64 v169, v169, -v83\n v_max3_f32 v170, v196, v197, v198\n v_max3_f32 v171, -v196, -v197, -v198\n " \
  "v_max3_f32 v170, v170, v199, v84\n v_max3_f32 v171, v171, -v199, -v84\n v_max3_f32 v170, v170, v85, v86\n v_max3_f32 v171, v171, -v85, -v86\n " \
  "v_max_f32_e64 v170, v170, v87\n v_max_f32_e64 v171, v171, -v87\n s_waitcnt lgkmcnt(0)\n " \
  "v_pk_fma_f32 v[176:177], v[128:129], v[144:145], v[176:177] op_sel_hi:[1,0,1]\n " \
  "v_pk_fma_f32 v[178:179], v[130:131], v[144:145], v[178:179] op_sel_hi:[1,0,1]\n " \
  "v_pk_fma_f32 v[180:181], v[128:129], v[144:145], v[180:181] op_sel:[0,1,0]\n " \
  "v_pk_fma_f32 v[182:183], v[130:131], v[144:145], v[182:183] op_sel:[0,1,0]\n v_pk_mul_f32 v[176:177], v[176:177], v[92:93]\n " \
  "v_pk_mul_f32 v[178:179], v[178:179], v[94:95]\n v_pk_mul_f32 v[180:181], v[180:181], v[92:93]\n v_pk_mul_f32 v[182:183], v[182:183], v[94:95]\n " \
  "v_pk_fma_f32 v[200:201], v[132:133], v[144:145], v[200:201] op_sel_hi:[1,0,1]\n " \
  "v_pk_fma_f32 v[202:203], v[134:135], v[144:145], v[202:203] op_sel_hi:[1,0,1]\n " \
  "v_pk_fma_f32 v[204:205], v[132:133], v[144:145], v[204:205] op_sel:[0,1,0]\n " \
  "v_pk_fma_f32 v[206:207], v[134:135], v[144:145], v[206:207] op_sel:[0,1,0]\n v_pk_mul_f32 v[200:201], v[200:201], v[112:113]\n " \
  "v_pk_mul_f32 v[202:203], v[202:203], v[114:115]\n v_pk_mul_f32 v[204:205], v[204:205], v[112:113]\n v_pk_mul_f32 v[206:207], v[206:207], v[114:115]\n " \
  "v_max3_f32 v192, v220, v221, v222\n v_max3_f32 v193, -v220, -v221, -v222\n v_max3_f32 v192, v192, v223, v124\n v_max3_f32 v193, v193, -v223, -v124\n " \
  "v_max3_f32 v192, v192, v125, v126\n v_max3_f32 v193, v193, -v125, -v126\n v_max_f32_e64 v192, v192, v127\n v_max_f32_e64 v193, v193, -v127\n " \
  "v_max3_f32 v194, v244, v245, v246\n v_max3_f32 v195, -v244, -v245, -v246\n v_max3_f32 v194, v194, v247, v108\n v_max3_f32 v195, v195, -v247, -v108\n " \
  "v_max3_f32 v194, v194, v109, v110\n v_max3_f32 v195, v195, -v109, -v110\n v_max_f32_e64 v194, v194, v111\n v_max_f32_e64 v195, v195, -v111\n " \
  "v_permlane16_swap_b32 v168, v169\n v_permlane16_swap_b32 v170, v171\n v_permlane16_swap_b32 v192, v193\n v_permlane16_swap_b32 v194, v195\n " \
  "v_pk_fma_f32 v[224:225], v[116:117], v[144:145], v[224:225] op_sel_hi:[1,0,1]\n " \
  "v_pk_fma_f32 v[226:227], v[118:119], v[144:145], v[226:227] op_sel_hi:[1,0,1]\n v_max_f32 v168, v168, v169\n v_max_f32 v170, v170, v171\n " \
  "v_max_f32 v192, v192, v193\n v_max_f32 v194, v194, v195\n v_pk_fma_f32 v[228:229], v[116:117], v[144:145], v[228:229] op_sel:[0,1,0]\n " \
  "v_pk_fma_f32 v[230:231], v[118:119], v[144:145], v[230:231] op_sel:[0,1,0]\n v_permlane32_swap_b32 v168, v170\n v_permlane32_swap_b32 v192, v194\n " \
  "v_pk_mul_f32 v[224:225], v[224:225], v[152:153]\n v_pk_mul_f32 v[226:227], v[226:227], v[154:155]\n v_max_f32 v168, v168, v170\n " \
  "v_max_f32 v192, v192, v194\n v_pk_mul_f32 v[228:229], v[228:229], v[152:153]\n v_pk_mul_f32 v[230:231], v[230:231], v[154:155]\n " \
  "v_mov_b32 v170, v168\n v_mov_b32 v194, v192\n v_pk_fma_f32 v[248:249], v[156:157], v[144:145], v[248:249] op_sel_hi:[1,0,1]\n " \
  "v_pk_fma_f32 v[250:251], v[158:159], v[144:145], v[250:251] op_sel_hi:[1,0,1]\n v_permlane32_swap_b32 v168, v170\n v_permlane32_swap_b32 v192, v194\n " \
  "v_pk_fma_f32 v[252:253], v[156:157], v[144:145], v[252:253] op_sel:[0,1,0]\n " \
  "v_pk_fma_f32 v[254:255], v[158:159], v[144:145], v[254:255] op_sel:[0,1,0]\n v_mov_b32 v169, v168\n v_mov_b32 v171, v170\n v_mov_b32 v193, v192\n " \
  "v_mov_b32 v195, v194\n v_pk_mul_f32 v[248:249], v[248:249], v[120:121]\n v_pk_mul_f32 v[250:251], v[250:251], v[122:123]\n " \
  "v_permlane16_swap_b32 v168, v169\n v_permlane16_swap_b32 v170, v171\n v_permlane16_swap_b32 v192, v193\n v_permlane16_swap_b32 v194, v195\n " \
  "v_pk_mul_f32 v[252:253], v[252:253], v[120:121]\n v_pk_mul_f32 v[254:255], v[254:255], v[122:123]\n v_cvt_pk_bf16_f32 v176, v176, v177\n " \
  "v_cvt_pk_bf16_f32 v177, v178, v179\n v_cvt_pk_bf16_f32 v178, v200, v201\n v_cvt_pk_bf16_f32 v179, v202, v203\n v_cvt_pk_bf16_f32 v180, v180, v181\n " \
  "v_cvt_pk_bf16_f32 v181, v182, v183\n v_cvt_pk_bf16_f32 v182, v204, v205\n v_cvt_pk_bf16_f32 v183, v206, v207\n v_cvt_pk_bf16_f32 v224, v224, v225\n " \
  "v_cvt_pk_bf16_f32 v225, v226, v227\n v_cvt_pk_bf16_f32 v226, v248, v249\n v_cvt_pk_bf16_f32 v227, v250, v251\n v_cvt_pk_bf16_f32 v228, v228, v229\n " \
  "v_cvt_pk_bf16_f32 v229, v230, v231\n v_cvt_pk_bf16_f32 v230, v252, v253\n v_cvt_pk_bf16_f32 v231, v254, v255\n v_mul_f32_e64 v45, v44, -v168\n " \
  "v_mul_f32_e64 v146, v44, -v169\n v_mul_f32_e64 v47, v46, -v170\n v_mul_f32_e64 v147, v46, -v171\n v_mul_f32_e64 v105, v104, -v192\n " \
  "v_mul_f32_e64 v144, v104, -v193\n v_mul_f32_e64 v107, v106, -v194\n v_mul_f32_e64 v145, v106, -v195\n " \
  "v_pk_fma_f32 v[200:201], v[172:173], v[44:45], v[146:147] op_sel_hi:[1,0,0] neg_lo:[1,0,0] neg_hi:[1,0,0]\n " \
  "v_pk_fma_f32 v[202:203], v[174:175], v[44:45], v[146:147] op_sel_hi:[1,0,0] neg_lo:[1,0,0] neg_hi:[1,0,0]\n " \
  "v_pk_fma_f32 v[204:205], v[80:81], v[44:45], v[146:147] op_sel_hi:[1,0,0] neg_lo:[1,0,0] neg_hi:[1,0,0]\n " \
  "v_pk_fma_f32 v[206:207], v[82:83], v[44:45], v[146:147] op_sel_hi:[1,0,0] neg_lo:[1,0,0] neg_hi:[1,0,0]\n " \
  "v_pk_fma_f32 v[172:173], v[172:173], v[44:45], v[44:45] op_sel:[0,0,1] op_sel_hi:[1,0,1]\n " \
  "v_pk_fma_f32 v[174:175], v[174:175], v[44:45], v[44:45] op_sel:[0,0,1] op_sel_hi:[1,0,1]\n " \
  "v_pk_fma_f32 v[80:81], v[80:81], v[44:45], v[44:45] op_sel:[0,0,1] op_sel_hi:[1,0,1]\n " \
  "v_pk_fma_f32 v[82:83], v[82:83], v[44:45], v[44:45] op_sel:[0,0,1] op_sel_hi:[1,0,1]\n ds_write_b128 v55, v[176:179] offset:0\n " \
  "v_exp_f32 v200, v200\n v_exp_f32 v201, v201\n v_exp_f32 v202, v202\n v_exp_f32 v203, v203\n v_exp_f32 v204, v204\n v_exp_f32 v205, v205\n " \
  "v_exp_f32 v206, v206\n v_exp_f32 v207, v207\n v_exp_f32 v172, v172\n v_exp_f32 v173, v173\n v_exp_f32 v174, v174\n v_exp_f32 v175, v175\n " \
  "v_exp_f32 v80, v80\n v_exp_f32 v81, v81\n v_exp_f32 v82, v82\n v_exp_f32 v83, v83\n " \
  "v_pk_fma_f32 v[248:249], v[196:197], v[46:47], v[146:147] op_sel:[0,0,1] op_sel_hi:[1,0,1] neg_lo:[1,0,0] neg_hi:[1,0,0]\n " \
  "v_pk_fma_f32 v[250:251], v[198:199], v[46:47], v[146:147] op_sel:[0,0,1] op_sel_hi:[1,0,1] neg_lo:[1,0,0] neg_hi:[1,0,0]\n " \
  "v_pk_fma_f32 v[252:253], v[84:85], v[46:47], v[146:147] op_sel:[0,0,1] op_sel_hi:[1,0,1] neg_lo:[1,0,0] neg_hi:[1,0,0]\n " \
  "v_pk_fma_f32 v[254:255], v[86:87], v[46:47], v[146:147] op_sel:[0,0,1] op_sel_hi:[1,0,1] neg_lo:[1,0,0] neg_hi:[1,0,0]\n " \
  "v_pk_fma_f32 v[196:197], v[196:197], v[46:47], v[46:47] op_sel:[0,0,1] op_sel_hi:[1,0,1]\n " \
  "v_pk_fma_f32 v[198:199], v[198:199], v[46:47], v[46:47] op_sel:[0,0,1] op_sel_hi:[1,0,1]\n " \
  "v_pk_fma_f32 v[84:85], v[84:85], v[46:47], v[46:47] op_sel:[0,0,1] op_sel_hi:[1,0,1]\n " \
  "v_pk_fma_f32 v[86:87], v[86:87], v[46:47], v[46:47] op_sel:[0,0,1] op_sel_hi:[1,0,1]\n ds_write_b128 v55, v[180:183] offset:1024\n " \
  "v_exp_f32 v248, v248\n v_exp_f32 v249, v249\n v_exp_f32 v250, v250\n v_exp_f32 v251, v251\n v_exp_f32 v252, v252\n v_exp_f32 v253, v253\n " \
  "v_exp_f32 v254, v254\n v_exp_f32 v255, v255\n v_exp_f32 v196, v196\n v_exp_f32 v197, v197\n v_exp_f32 v198, v198\n v_exp_f32 v199, v199\n " \
  "v_exp_f32 v84, v84\n v_exp_f32 v85, v85\n v_exp_f32 v86, v86\n v_exp_f32 v87, v87\n " \
  "v_pk_fma_f32 v[128:129], v[220:221], v[104:105], v[144:145] op_sel_hi:[1,0,0] neg_lo:[1,0,0] neg_hi:[1,0,0]\n " \
  "v_pk_fma_f32 v[130:131], v[222:223], v[104:105], v[144:145] op_sel_hi:[1,0,0] neg_lo:[1,0,0] neg_hi:[1,0,0]\n " \
  "v_pk_fma_f32 v[132:133], v[124:125], v[104:105], v[144:145] op_sel_hi:[1,0,0] neg_lo:[1,0,0] neg_hi:[1,0,0]\n " \
  "v_pk_fma_f32 v[134:135], v[126:127], v[104:105], v[144:145] op_sel_hi:[1,0,0] neg_lo:[1,0,0] neg_hi:[1,0,0]\n " \
  "v_pk_fma_f32 v[220:221], v[220:221], v[104:105], v[104:105] op_sel:[0,0,1] op_sel_hi:[1,0,1]\n " \
  "v_pk_fma_f32 v[222:223], v[222:223], v[104:105], v[104:105] op_sel:[0,0,1] op_sel_hi:[1,0,1]\n " \
  "v_pk_fma_f32 v[124:125], v[124:125], v[104:105], v[104:105] op_sel:[0,0,1] op_sel_hi:[1,0,1]\n " \
  "v_pk_fma_f32 v[126:127], v[126:127], v[104:105], v[104:105] op_sel:[0,0,1] op_sel_hi:[1,0,1]\n ds_write_b128 v55, v[224:227] offset:8192\n " \
  "v_pk_add_f32 v[216:217], v[172:173], v[174:175]\n v_pk_add_f32 v[218:219], v[80:81], v[82:83]\n v_pk_add_f32 v[216:217], v[216:217], v[218:219]\n " \
  "v_add_f32 v168, v216, v217\n v_pk_add_f32 v[216:217], v[200:201], v[202:203]\n v_pk_add_f32 v[218:219], v[204:205], v[206:207]\n " \
  "v_pk_add_f32 v[216:217], v[216:217], v[218:219]\n v_add_f32 v169, v216, v217\n v_pk_add_f32 v[216:217], v[196:197], v[198:199]\n " \
  "v_pk_add_f32 v[218:219], v[84:85], v[86:87]\n v_pk_add_f32 v[216:217], v[216:217], v[218:219]\n v_add_f32 v170, v216, v217\n " \
  "v_pk_add_f32 v[216:217], v[248:249], v[250:251]\n v_pk_add_f32 v[218:219], v[252:253], v[254:255]\n v_pk_add_f32 v[216:217], v[216:217], v[218:219]\n " \
  "v_add_f32 v171, v216, v217\n v_exp_f32 v128, v128\n v_exp_f32 v129, v129\n v_permlane16_swap_b32 v168, v169\n v_permlane16_swap_b32 v170, v171\n " \
  "v_exp_f32 v130, v130\n v_exp_f32 v131, v131\n v_add_f32 v168, v168, v169\n v_add_f32 v170, v170, v171\n v_exp_f32 v132, v132\n v_exp_f32 v133, v133\n " \
  "v_permlane32_swap_b32 v168, v170\n v_exp_f32 v134, v134\n v_exp_f32 v135, v135\n v_add_f32 v168, v168, v170\n v_exp_f32 v220, v220\n " \
  "v_exp_f32 v221, v221\n v_rcp_f32 v168, v168\n v_exp_f32 v222, v222\n v_exp_f32 v223, v223\n v_mov_b32 v170, v168\n v_exp_f32 v124, v124\n " \
  "v_exp_f32 v125, v125\n v_permlane32_swap_b32 v168, v170\n v_exp_f32 v126, v126\n v_exp_f32 v127, v127\n v_mov_b32 v169, v168\n v_mov_b32 v171, v170\n " \
  "v_pk_fma_f32 v[112:113], v[244:245], v[106:107], v[144:145] op_sel:[0,0,1] op_sel_hi:[1,0,1] neg_lo:[1,0,0] neg_hi:[1,0,0]\n " \
  "v_pk_fma_f32 v[114:115], v[246:247], v[106:107], v[144:145] op_sel:[0,0,1] op_sel_hi:[1,0,1] neg_lo:[1,0,0] neg_hi:[1,0,0]\n " \
  "v_permlane16_swap_b32 v168, v169\n v_permlane16_swap_b32 v170, v171\n " \
  "v_pk_fma_f32 v[116:117], v[108:109], v[106:107], v[144:145] op_sel:[0,0,1] op_sel_hi:[1,0,1] neg_lo:[1,0,0] neg_hi:[1,0,0]\n " \
  "v_pk_fma_f32 v[118:119], v[110:111], v[106:107], v[144:145] op_sel:[0,0,1] op_sel_hi:[1,0,1] neg_lo:[1,0,0] neg_hi:[1,0,0]\n " \
  "v_pk_fma_f32 v[244:245], v[244:245], v[106:107], v[106:107] op_sel:[0,0,1] op_sel_hi:[1,0,1]\n " \
  "v_pk_fma_f32 v[246:247], v[246:247], v[106:107], v[106:107] op_sel:[0,0,1] op_sel_hi:[1,0,1]\n " \
  "v_pk_fma_f32 v[108:109], v[108:109], v[106:107], v[106:107] op_sel:[0,0,1] op_sel_hi:[1,0,1]\n " \
  "v_pk_fma_f32 v[110:111], v[110:111], v[106:107], v[106:107] op_sel:[0,0,1] op_sel_hi:[1,0,1]\n ds_write_b128 v55, v[228:231] offset:9216\n " \
  "v_exp_f32 v112, v112\n v_pk_mul_f32 v[172:173], v[172:173], v[168:169] op_sel_hi:[1,0]\n v_exp_f32 v113, v113\n " \
  "v_pk_mul_f32 v[174:175], v[174:175], v[168:169] op_sel_hi:[1,0]\n v_exp_f32 v114, v114\n v_cvt_pk_bf16_f32 v172, v172, v173\n v_exp_f32 v115, v115\n " \
  "v_cvt_pk_bf16_f32 v173, v174, v175\n v_exp_f32 v116, v116\n ds_write_b64 v58, v[172:173] offset:0\n v_exp_f32 v117, v117\n " \
  "v_pk_mul_f32 v[80:81], v[80:81], v[168:169] op_sel_hi:[1,0]\n v_exp_f32 v118, v118\n v_pk_mul_f32 v[82:83], v[82:83], v[168:169] op_sel_hi:[1,0]\n " \
  "v_exp_f32 v119, v119\n v_cvt_pk_bf16_f32 v80, v80, v81\n v_exp_f32 v244, v244\n v_cvt_pk_bf16_f32 v81, v82, v83\n v_exp_f32 v245, v245\n " \
  "ds_write_b64 v58, v[80:81] offset:32\n v_exp_f32 v246, v246\n v_pk_mul_f32 v[200:201], v[200:201], v[168:169] op_sel:[0,1]\n v_exp_f32 v247, v247\n " \
  "v_pk_mul_f32 v[202:203], v[202:203], v[168:169] op_sel:[0,1]\n v_exp_f32 v108, v108\n v_cvt_pk_bf16_f32 v200, v200, v201\n v_exp_f32 v109, v109\n " \
  "v_cvt_pk_bf16_f32 v201, v202, v203\n v_exp_f32 v110, v110\n ds_write_b64 v58, v[200:201] offset:64\n v_exp_f32 v111, v111\n " \
  "v_pk_mul_f32 v[204:205], v[204:205], v[168:169] op_sel:[0,1]\n v_pk_mul_f32 v[206:207], v[206:207], v[168:169] op_sel:[0,1]\n " \
  "v_cvt_pk_bf16_f32 v204, v204, v205\n v_cvt_pk_bf16_f32 v205, v206, v207\n ds_write_b64 v58, v[204:205] offset:96\n " \
  "v_pk_add_f32 v[216:217], v[220:221], v[222:223]\n v_pk_add_f32 v[218:219], v[124:125], v[126:127]\n v_pk_add_f32 v[216:217], v[216:217], v[218:219]\n " \
  "v_add_f32 v192, v216, v217\n v_pk_add_f32 v[216:217], v[128:129], v[130:131]\n v_pk_add_f32 v[218:219], v[132:133], v[134:135]\n " \
  "v_pk_add_f32 v[216:217], v[216:217], v[218:219]\n v_add_f32 v193, v216, v217\n v_pk_add_f32 v[216:217], v[244:245], v[246:247]\n " \
  "v_pk_add_f32 v[218:219], v[108:109], v[110:111]\n v_pk_add_f32 v[216:217], v[216:217], v[218:219]\n v_add_f32 v194, v216, v217\n " \
  "v_pk_add_f32 v[216:217], v[112:113], v[114:115]\n v_pk_add_f32 v[218:219], v[116:117], v[118:119]\n v_pk_add_f32 v[216:217], v[216:217], v[218:219]\n " \
  "v_add_f32 v195, v216, v217\n v_pk_mul_f32 v[196:197], v[196:197], v[170:171] op_sel_hi:[1,0]\n " \
  "v_pk_mul_f32 v[198:199], v[198:199], v[170:171] op_sel_hi:[1,0]\n v_permlane16_swap_b32 v192, v193\n v_permlane16_swap_b32 v194, v195\n " \
  "v_cvt_pk_bf16_f32 v196, v196, v197\n v_cvt_pk_bf16_f32 v197, v198, v199\n v_add_f32 v192, v192, v193\n v_add_f32 v194, v194, v195\n " \
  "ds_write_b64 v58, v[196:197] offset:2304\n v_pk_mul_f32 v[84:85], v[84:85], v[170:171] op_sel_hi:[1,0]\n v_permlane32_swap_b32 v192, v194\n " \
  "v_pk_mul_f32 v[86:87], v[86:87], v[170:171] op_sel_hi:[1,0]\n v_cvt_pk_bf16_f32 v84, v84, v85\n v_add_f32 v192, v192, v194\n " \
  "v_cvt_pk_bf16_f32 v85, v86, v87\n ds_write_b64 v58, v[84:85] offset:2336\n v_rcp_f32 v192, v192\n " \
  "v_pk_mul_f32 v[248:249], v[248:249], v[170:171] op_sel:[0,1]\n v_pk_mul_f32 v[250:251], v[250:251], v[170:171] op_sel:[0,1]\n v_mov_b32 v194, v192\n " \
  "v_cvt_pk_bf16_f32 v248, v248, v249\n v_cvt_pk_bf16_f32 v249, v250, v251\n v_permlane32_swap_b32 v192, v194\n " \
  "ds_write_b64 v58, v[248:249] offset:2368\n v_pk_mul_f32 v[252:253], v[252:253], v[170:171] op_sel:[0,1]\n v_mov_b32 v193, v192\n " \
  "v_mov_b32 v195, v194\n v_pk_mul_f32 v[254:255], v[254:255], v[170:171] op_sel:[0,1]\n v_cvt_pk_bf16_f32 v252, v252, v253\n " \
  "v_permlane16_swap_b32 v192, v193\n v_permlane16_swap_b32 v194, v195\n v_cvt_pk_bf16_f32 v253, v254, v255\n ds_write_b64 v58, v[252:253] offset:2400\n " \
  "v_pk_mul_f32 v[220:221], v[220:221], v[192:193] op_sel_hi:[1,0]\n v_pk_mul_f32 v[222:223], v[222:223], v[192:193] op_sel_hi:[1,0]\n " \
  "v_cvt_pk_bf16_f32 v220, v220, v221\n v_cvt_pk_bf16_f32 v221, v222, v223\n ds_write_b64 v58, v[220:221] offset:18432\n " \
  "v_pk_mul_f32 v[124:125], v[124:125], v[192:193] op_sel_hi:[1,0]\n v_pk_mul_f32 v[126:127], v[126:127], v[192:193] op_sel_hi:[1,0]\n " \
  "v_cvt_pk_bf16_f32 v124, v124, v125\n v_cvt_pk_bf16_f32 v125, v126, v127\n ds_write_b64 v58, v[124:125] offset:18464\n " \
  "v_pk_mul_f32 v[128:129], v[128:129], v[192:193] op_sel:[0,1]\n v_pk_mul_f32 v[130:131], v[130:131], v[192:193] op_sel:[0,1]\n " \
  "v_cvt_pk_bf16_f32 v128, v128, v129\n v_cvt_pk_bf16_f32 v129, v130, v131\n ds_write_b64 v58, v[128:129] offset:18496\n " \
  "v_pk_mul_f32 v[132:133], v[132:133], v[192:193] op_sel:[0,1]\n v_pk_mul_f32 v[134:135], v[134:135], v[192:193] op_sel:[0,1]\n " \
  "v_cvt_pk_bf16_f32 v132, v132, v133\n v_cvt_pk_bf16_f32 v133, v134, v135\n ds_write_b64 v58, v[132:133] offset:18528\n " \
  "v_pk_mul_f32 v[244:245], v[244:245], v[194:195] op_sel_hi:[1,0]\n v_pk_mul_f32 v[246:247], v[246:247], v[194:195] op_sel_hi:[1,0]\n " \
  "v_cvt_pk_bf16_f32 v244, v244, v245\n v_cvt_pk_bf16_f32 v245, v246, v247\n ds_write_b64 v58, v[244:245] offset:20736\n " \
  "v_pk_mul_f32 v[108:109], v[108:109], v[194:195] op_sel_hi:[1,0]\n v_pk_mul_f32 v[110:111], v[110:111], v[194:195] op_sel_hi:[1,0]\n " \
  "v_cvt_pk_bf16_f32 v108, v108, v109\n v_cvt_pk_bf16_f32 v109, v110, v111\n ds_write_b64 v58, v[108:109] offset:20768\n " \
  "v_pk_mul_f32 v[112:113], v[112:113], v[194:195] op_sel:[0,1]\n v_pk_mul_f32 v[114:115], v[114:115], v[194:195] op_sel:[0,1]\n " \
  "v_cvt_pk_bf16_f32 v112, v112, v113\n v_cvt_pk_bf16_f32 v113, v114, v115\n ds_write_b64 v58, v[112:113] offset:20800\n " \
  "v_pk_mul_f32 v[116:117], v[116:117], v[194:195] op_sel:[0,1]\n v_pk_mul_f32 v[118:119], v[118:119], v[194:195] op_sel:[0,1]\n " \
  "v_cvt_pk_bf16_f32 v116, v116, v117\n v_cvt_pk_bf16_f32 v117, v118, v119\n ds_write_b64 v58, v[116:117] offset:20832\n "
#define G1_OUTPUT \
  "s_mov_b32 m0, 0x40000\n s_branch .Lg1p%=\n .Lg1q%=:\n s_sleep 1\n s_sub_u32 m0, m0, 1\n s_cbranch_scc1 .Lg1r%=\n .Lg1p%=:\n " \
  "buffer_load_dwordx4 v[216:219], %[fz], %[rs], 0 offen sc1\n buffer_load_dwordx4 v[220:223], %[fz], %[rs], 0 offen offset:64 sc1\n " \
  "buffer_load_dwordx4 v[200:203], %[fo], %[rs], 0 offen offset:0 sc1\n buffer_load_dwordx4 v[204:207], %[fo], %[rs], 0 offen offset:2048 sc1\n " \
  "buffer_load_dwordx4 v[208:211], %[fo], %[rs], 0 offen offset:1024 sc1\n buffer_load_dwordx4 v[212:215], %[fo], %[rs], 0 offen offset:3072 sc1\n " \
  "s_waitcnt vmcnt(5)\n v_max_u32 v152, v216, v217\n v_max3_u32 v152, v218, v219, v152\n s_waitcnt vmcnt(4)\n v_max3_u32 v152, v220, v221, v152\n " \
  "v_max3_u32 v152, v222, v223, v152\n s_waitcnt vmcnt(3)\n v_max3_u32 v152, v200, v201, v152\n v_max3_u32 v152, v202, v203, v152\n s_waitcnt vmcnt(2)\n " \
  "v_max3_u32 v152, v204, v205, v152\n v_max3_u32 v152, v206, v207, v152\n s_waitcnt vmcnt(1)\n v_max3_u32 v152, v208, v209, v152\n " \
  "v_max3_u32 v152, v210, v211, v152\n s_waitcnt vmcnt(0)\n v_max3_u32 v152, v212, v213, v152\n v_max3_u32 v152, v214, v215, v152\n " \
  "v_cmp_eq_u32 vcc, -1, v152\n s_cbranch_vccnz .Lg1q%=\n .Lg1r%=:\n v_mfma_f32_16x16x32_bf16 v[160:163], v[216:219], v[224:227], 0\n " \
  "v_mfma_f32_16x16x32_bf16 v[160:163], v[220:223], v[228:231], v[160:163]\n v_mfma_f32_16x16x32_bf16 v[152:155], v[200:203], v[224:227], 0\n " \
  "v_mfma_f32_16x16x32_bf16 v[152:155], v[204:207], v[228:231], v[152:155]\n v_mfma_f32_16x16x32_bf16 v[156:159], v[208:211], v[224:227], 0\n " \
  "v_mfma_f32_16x16x32_bf16 v[156:159], v[212:215], v[228:231], v[156:159]\n v_mfma_f32_16x16x32_bf16 v[172:175], v[216:219], v[232:235], 0\n " \
  "v_mfma_f32_16x16x32_bf16 v[172:175], v[220:223], v[236:239], v[172:175]\n v_mfma_f32_16x16x32_bf16 v[164:167], v[200:203], v[232:235], 0\n " \
  "v_mfma_f32_16x16x32_bf16 v[164:167], v[204:207], v[236:239], v[164:167]\n v_mfma_f32_16x16x32_bf16 v[168:171], v[208:211], v[232:235], 0\n " \
  "v_add_f32 v160, 0x358637bd, v160\n v_rcp_f32 v160, v160\n v_mfma_f32_16x16x32_bf16 v[168:171], v[212:215], v[236:239], v[168:171]\n " \
  "v_mul_f32 v152, v160, v152\n v_mfma_f32_16x16x32_bf16 v[184:187], v[216:219], v[240:243], 0\n v_mul_f32 v153, v160, v153\n " \
  "v_cvt_pk_bf16_f32 v152, v152, v153\n v_mul_f32 v154, v160, v154\n v_mfma_f32_16x16x32_bf16 v[184:187], v[220:223], v[244:247], v[184:187]\n " \
  "v_mul_f32 v155, v160, v155\n v_cvt_pk_bf16_f32 v153, v154, v155\n v_mul_f32 v156, v160, v156\n " \
  "v_mfma_f32_16x16x32_bf16 v[176:179], v[200:203], v[240:243], 0\n v_mul_f32 v157, v160, v157\n v_cvt_pk_bf16_f32 v154, v156, v157\n " \
  "v_mul_f32 v158, v160, v158\n v_mfma_f32_16x16x32_bf16 v[176:179], v[204:207], v[244:247], v[176:179]\n v_mul_f32 v159, v160, v159\n " \
  "v_cvt_pk_bf16_f32 v155, v158, v159\n buffer_store_dwordx4 v[152:155], %[go], %[rs], 0 offen offset:0 sc1\n " \
  "v_mfma_f32_16x16x32_bf16 v[180:183], v[208:211], v[240:243], 0\n ds_write_b128 %[lo], v[152:155] offset:0\n v_add_f32 v172, 0x358637bd, v172\n " \
  "v_mfma_f32_16x16x32_bf16 v[180:183], v[212:215], v[244:247], v[180:183]\n v_rcp_f32 v172, v172\n " \
  "v_mfma_f32_16x16x32_bf16 v[196:199], v[216:219], v[248:251], 0\n v_mul_f32 v164, v172, v164\n v_mul_f32 v165, v172, v165\n " \
  "v_cvt_pk_bf16_f32 v164, v164, v165\n v_mfma_f32_16x16x32_bf16 v[196:199], v[220:223], v[252:255], v[196:199]\n v_mul_f32 v166, v172, v166\n " \
  "v_mul_f32 v167, v172, v167\n v_cvt_pk_bf16_f32 v165, v166, v167\n v_mfma_f32_16x16x32_bf16 v[188:191], v[200:203], v[248:251], 0\n " \
  "v_mul_f32 v168, v172, v168\n v_mul_f32 v169, v172, v169\n v_cvt_pk_bf16_f32 v166, v168, v169\n " \
  "v_mfma_f32_16x16x32_bf16 v[188:191], v[204:207], v[252:255], v[188:191]\n v_mul_f32 v170, v172, v170\n v_mul_f32 v171, v172, v171\n " \
  "v_cvt_pk_bf16_f32 v167, v170, v171\n v_mfma_f32_16x16x32_bf16 v[192:195], v[208:211], v[248:251], 0\n " \
  "buffer_store_dwordx4 v[164:167], %[go], %[rs], 0 offen offset:1024 sc1\n ds_write_b128 %[lo], v[164:167] offset:1024\n " \
  "v_mfma_f32_16x16x32_bf16 v[192:195], v[212:215], v[252:255], v[192:195]\n v_add_f32 v184, 0x358637bd, v184\n v_rcp_f32 v184, v184\n s_nop 0\n " \
  "v_pk_mul_f32 v[176:177], v[176:177], v[184:185] op_sel_hi:[1,0]\n v_pk_mul_f32 v[178:179], v[178:179], v[184:185] op_sel_hi:[1,0]\n " \
  "v_pk_mul_f32 v[180:181], v[180:181], v[184:185] op_sel_hi:[1,0]\n v_pk_mul_f32 v[182:183], v[182:183], v[184:185] op_sel_hi:[1,0]\n " \
  "v_cvt_pk_bf16_f32 v176, v176, v177\n v_cvt_pk_bf16_f32 v177, v178, v179\n v_cvt_pk_bf16_f32 v178, v180, v181\n v_cvt_pk_bf16_f32 v179, v182, v183\n " \
  "buffer_store_dwordx4 v[176:179], %[go], %[rs], 0 offen offset:2048 sc1\n ds_write_b128 %[lo], v[176:179] offset:2048\n " \
  "v_add_f32 v196, 0x358637bd, v196\n v_rcp_f32 v196, v196\n s_nop 0\n v_pk_mul_f32 v[188:189], v[188:189], v[196:197] op_sel_hi:[1,0]\n " \
  "v_pk_mul_f32 v[190:191], v[190:191], v[196:197] op_sel_hi:[1,0]\n v_pk_mul_f32 v[192:193], v[192:193], v[196:197] op_sel_hi:[1,0]\n " \
  "v_pk_mul_f32 v[194:195], v[194:195], v[196:197] op_sel_hi:[1,0]\n v_cvt_pk_bf16_f32 v188, v188, v189\n v_cvt_pk_bf16_f32 v189, v190, v191\n " \
  "v_cvt_pk_bf16_f32 v190, v192, v193\n v_cvt_pk_bf16_f32 v191, v194, v195\n buffer_store_dwordx4 v[188:191], %[go], %[rs], 0 offen offset:3072 sc1\n " \
  "ds_write_b128 %[lo], v[188:191] offset:3072\n "
#define G1_PURGE \
  ".purgem G1M\n .purgem G1RA\n .purgem G1RB\n .purgem G1BK\n .purgem G1AK\n .purgem G1RDB\n .purgem G1RDA\n .purgem G1Q\n .purgem G1SG\n "
#define G1_CLOBBER \
  "v80", "v81", "v82", "v83", "v84", "v85", "v86", "v87", "v92", "v93", "v94", "v95", "v96", "v97", "v98", "v99", "v100", "v101", "v102", "v103", \
  "v104", "v105", "v106", "v107", "v108", "v109", "v110", "v111", \
  "v112", "v113", "v114", "v115", "v116", "v117", "v118", "v119", "v120", "v121", "v122", "v123", "v124", "v125", "v126", "v127", "v128", "v129", \
  "v130", "v131", "v132", "v133", "v134", "v135", "v144", "v145", "v146", "v147", "v152", "v153", "v154", "v155", \
  "v156", "v157", "v158", "v159"

template <int MODE>
__device__ __forceinline__ void g1_phase(char* smem, char* ws, u64& E, int w, f32x16& xq, Next& nx, int layer, const u16* __restrict__ x0, const u16* __restrict__ W,
                                         const u16* __restrict__ wo, const u16* __restrict__ wphi,
                                         const u16* __restrict__ wn1, const u16* __restrict__ wn2) {
  constexpr bool HH = (MODE == 1);
  constexpr int ABYTES = 128 * 128;
  constexpr int STAGE = ABYTES + 3 * 8192;
  constexpr int OFF_FX = 16384;
  constexpr int OFF_SP = 3 * STAGE;
  constexpr int OFF_RS = !HH ? 159744 : OFF_SP + 16384;
  constexpr int OFF_CS = OFF_RS + (HH ? 1152 : 1024);
  constexpr int OFF_WP = OFF_CS + 1536;
  constexpr int OFF_JK = LDS_TOTAL - 1024;
  constexpr int OFF_PK = 0;
  constexpr int OFF_VF = OFF_WP + 4096;
  constexpr int OFF_R2 = HH ? OFF_VF + 16384 : OFF_CS + 1024;
  constexpr int OFF_RD = OFF_CS + 768;
  constexpr int PSTR = 144;
  IDS
  W = uni(W); wphi = uni(wphi); wn1 = uni(wn1); wn2 = uni(wn2);
  const int kg = w >> 2, wm = (w >> 1) & 1, wn = w & 1;
  const bool first = !HH && layer == 0;
  f32x4 acc[2][2], qh[2][2], csq[2], st01, st23;
  bf16x8 wf[2];
  u64 kol;
  u32x4 pw[8];
  ST(layer * 24 + 0)
  const int R0 = wm * 64;
  u32x16 pa;
#define pcs pa[0]
#define prs pa[1]
#define opo pa[2]
#define ogo pa[3]
#define hof pa[7]
#define dm pa[8]
#define dm2 pa[9]
#define pcv pa[4]
#define pwf pa[5]
#define prv pa[6]
#define pvf pa[7]
#define ppk pa[10]
#define ppa pa[11]
#define pvr pa[12]
#define gkv pa[13]
#define gkz pa[14]
#define pwl pa[15]
#define pwm pa[4]
#define plb pa[10]
#define prd pa[11]
#define dm3 pa[12]
  f32x4 bias[3];
  const char* wos = wo_src(ws, wo, layer, hp);
  const unsigned tO = img(layer, hp) ? 8192u : 131072u;
  const unsigned sb = lds_addr(smem);
  auto prep = [&](auto ldr) {
    constexpr bool LDR = decltype(ldr)::value;
    if constexpr (!LDR) {
      pcs = sb + OFF_CS + wn * 128 + g * 16;
      prs = sb + OFF_RS + (R0 + fr) * 8;
      opo = (unsigned)(wn * 8192 + R0 * 64 + lane * 16);
      ogo = opo + WS_O + layer * (SEQ * DM * 2) + (unsigned)((rt * NH + 2 * hp) * 8192);
      opo += sb;
      pcv = sb + OFF_CS + (wn * 32 + 128 + fr) * 4;
      prv = sb + OFF_R2 + (R0 + 4 * g) * 4;
    } else {
      constexpr unsigned kvn = WS_KV + MODE * KVLB;
      const int t = tid & 255;
      dm = kvn + (unsigned)((rt * NH + 2 * hp) * KVPB) + t * 16;
      dm2 = t < 16 ? dm + 8192 : t >= 64 && t < 97 ? kvn + 16u * NH * KVPB + (unsigned)(hp * KVFB + rt * 528 + (t - 64) * 16) : 0x7fff0000u;
      if (!HH) dm3 = WS_HL + (unsigned)((256 + rt * 16 + hp) * 4096 + t * 16);
    }
    if constexpr (!HH) {
      prd = sb + OFF_RD + wn * 4;
      if constexpr (!LDR) {
        plb = sb + OFF_FX + wn * 2048 + lane * 16;
        hof = m0 + wm == 0 ? 0x7fff0000u : WS_HL + (unsigned)((layer * 128 + rt * 16 - 16 + wm * 16 + hp) * 4096 + wn * 2048 + lane * 16);
        const unsigned out = ~((0x5555u << 2 << fr) >> (4 * g));
#pragma unroll
        for (int i = 0; i < 8; ++i) {
          unsigned t;
          asm("v_bfe_i32 %0, %1, %2, 1" : "=v"(t) : "v"(out), "n"((i >> 2) * 16 + (i & 3)));
          bias[i >> 2][i & 3] = __uint_as_float(t & 0xff61b1e6u);
        }
        bias[2] = m0 + R0 == 0 ? f32x4{-3.0e38f, -3.0e38f, -3.0e38f, -3.0e38f} : bias[0];
        asm volatile("" : "+{v[36:39]}"(bias[0]), "+{v[40:43]}"(bias[1]), "+{v[44:47]}"(bias[2]));
      }
    } else {
      const int u = wm * 2 + kg;
      if constexpr (!LDR) {
        pwf = sb + OFF_WP + wn * 2048 + (4 * g + q4) * 64 + p4 * 8;
        pvf = sb + OFF_VF + w * 2048 + lane * 16;
        ppk = sb + OFF_PK + w * (32 * PSTR) + fr * PSTR + g * 8;
      }
      ppa = sb + OFF_PK + wn * (32 * PSTR) + (4 * g + q4) * PSTR + u * 32 + p4 * 8;
      pvr = sb + OFF_VF + wn * 2048 + lane * 16;
      const unsigned doff = WS_KV + (layer >> 1) * KVLB + (unsigned)((rt * NH + 2 * hp + wn) * KVPB);
      gkv = doff + u * 1024 + lane * 16;
      gkz = fr == 0 ? doff + 4096 + u * 32 + g * 8 : 0x7fff0000u;
    }
    asm volatile("" : "+{v[48:63]}"(pa));
  };

  if (kg == 1) {

    const int lw = w & 3;
    const int kB = 16 * lw + (lane >> 3);
    const int fB = (kB >> 1) & 1;
    const int cB0 = ((((lane & 7) >> 1) ^ fB) << 1) | (lane & 1), cB1 = ((((lane & 7) >> 1) ^ (fB | 2)) << 1) | (lane & 1);
    const bool im = img(layer, hp);

    unsigned vW0, vW1, vP, vX, vY;
    u64 ko2;
    auto ld_pw = [&](int i0) {
#pragma unroll
      for (int i = i0; i < i0 + 4; ++i) gld16(pw[i], vP, wn1 + i * 8 * 3072);
    };
    auto wo_tile = [&](int t) { glds2v(wos + knib(ko2, t) * tO, vW0, vW1, lds_addr(smem) + OFF_WO + t * 8192 + lw * 2048); };
    auto run = [&](auto fst) {
      constexpr bool FIRST = decltype(fst)::value;
      constexpr int N = 10;
      const unsigned vA = FIRST ? (unsigned)(((m0 + lw * 32 + (lane >> 3)) * DM + ((lane & 7) ^ (lane >> 3)) * 8) * 2)
                                : (unsigned)(rt * 262144 + lw * 4096 + lane * 16);
      unsigned imv = !FIRST && im ? ~0u : 0u;
      if (!FIRST) asm volatile("" : "+v"(imv));
      const unsigned vB0 = ((unsigned)(lw * 6144 + lane * 16) & imv) | ((unsigned)((kB * 3072 + hp * 64 + cB0 * 8) * 2) & ~imv);
      const unsigned vB1 = ((unsigned)(lw * 6144 + lane * 16) & imv) | (((unsigned)(((kB + 8) * 3072 + hp * 64 + cB1 * 8) * 2) - 1024u) & ~imv);
      const char* xa = FIRST ? (const char*)x0 : ws + WS_X + (layer - 1) * (SEQ * DM * 2);
      constexpr int KS = FIRST ? 128 : 16384;
      const u64 kord = FIRST ? korder0(rt) : nx.kord;
      auto ktile = [&](int ks) { return knib(kord, ks); };
      auto stageA = [&](int ks, int boff) {
        const int kt = ktile(ks);
        const unsigned sA = lds_addr(smem) + boff;
        if (FIRST) glds4v(xa + kt * KS, vA, vA + 15360u, vA + 2 * 15360u, vA + 3 * 15360u, sA + lw * 4096);
        else glds4c(xa + kt * KS, vA, sA + lw * 4096);
      };
      auto stageB = [&](const u16* Wp, int ks, int boff) {
        const int kt = ktile(ks);
        const unsigned sB = lds_addr(smem) + boff + ABYTES;
        const char* b = FIRST ? (const char*)(Wp + (size_t)kt * 64 * 3072) : nx.wq + kt * nx.tw;
        glds4v(b, vB0, vB1, vB0, vB1, sB + lw * 6144);
        glds2v(b + 4096, vB0, vB1, sB + lw * 6144 + 4096);
      };
      bf16x8 sel[3];
      unsigned cso[3], cst;
      if (FIRST) {
        stageA(1, STAGE); stageB(W, 1, STAGE);
        __builtin_amdgcn_sched_barrier(0);
        const int fl = (q4 >> 1) | ((g & 1) << 1), j = 3 * wm + (fr < 3 ? fr : 0);
#pragma unroll
        for (int i = 0; i < 3; ++i) {
          const unsigned o = fr == i ? 0x3F803F80u : 0u;
          sel[i] = __builtin_bit_cast(bf16x8, u32x4{o, o, o, o});
          cso[i] = (unsigned)(2 * STAGE + ABYTES + (g >> 1) * 6144 + (g & 1) * 1024 + q4 * 128 + p4 * 8 + (((wn * 2 + ((wm + i) & 1)) ^ fl) << 5) + ((3 * wm + i) >> 1) * 2048);
          asm volatile("" : "+v"(sel[i]), "+v"(cso[i]));
        }
        cst = fr < 3 ? WS_CPA + (unsigned)(((hp * 16 + rt) * 192 + (j >> 1) * 64 + wn * 32 + (j & 1) * 16 + 4 * g) * 4) : 0x7fff0000u;
        asm volatile("" : "+v"(cst));
        WAITVN(N);
        BAR;
        stageA(2, 2 * STAGE); stageB(W, 2, 2 * STAGE);
      }
      u64 fall, fal2;
      unsigned fao, fa2, vS, vC, dS, dC, dR;
      auto setup = [&]() {
        fao = (unsigned)((256 + rt * 16 + (lane & 15)) * FLS);
        fa2 = (unsigned)(((FIRST ? 512 : 256) + (lane & 15) * 16 + (FIRST ? hp : (hp - ((1 << (lane >> 4)) >> 1)) & 15)) * FLS);
        vP = (unsigned)((rt * 64 + (lane >> 3)) * 6144 + lw * 2048 + ((hp + (1 << lw)) & 15) * 128 + (lane & 7) * 16);
        vS = FIRST ? (unsigned)(WS_G0 + (m0 + lw * 32 + (lane & 31)) * 16)
                   : WS_ST + (unsigned)((layer - 1) * (SEQ * 128) + rt * 16384 + g * 1024 + lw * 256 + fr * 16);
        vC = (FIRST ? WS_CPA : WS_CSP) + (unsigned)(hp * 12288 + g * 768 + lw * 192 + (fr < 12 ? fr : 0) * 16);
        if (!FIRST) vX = HH ? (unsigned)(hp * 4096 + lw * 1024 + lane * 16) : (unsigned)((rt * 16 + hp) * 1024 + lane * 16);
        if (HH) { vY = (unsigned)(bid * 8192 + lw * 2048 + lane * 16); asm volatile("" : "+v"(vY)); }
        vW0 = ((unsigned)(lw * 2048 + lane * 16) & imv) | ((unsigned)((kB * DM + hp * 64 + ((lane & 7) ^ ((lane >> 4) << 1)) * 8) * 2) & ~imv); vW1 = vW0 + ((8 * DM * 2 - 1024u) & ~imv);
        dS = OFF_RS + lw * 256 + (FIRST ? (lane & 31) * 8 : fr * 16 + (g & 1) * 8); dC = OFF_CS + lw * 192 + (fr < 12 ? fr : 0) * 16 + g * 4;
        ko2 = korder(bid & 3, hp);
        dR = OFF_R2 + lw * 128 + (FIRST ? (lane & 31) * 4 : fr * 8 + (g & 1) * 4);
        asm volatile("" : "+v"(dR));
        asm volatile("" : "+v"(vS), "+v"(vX), "+v"(vW0), "+v"(vW1), "+v"(dS));
        asm volatile("" : "+s"(ko2));
        if (FIRST || HH) asm volatile("" : "+v"(vC), "+v"(dC), "+v"(fa2));
        if (FIRST) asm volatile("" : "+v"(vP)); else asm volatile("" : "+v"(fao));
      };
      if (!FIRST) setup();
      u32x4 sv[4], cv[4];
      auto ld_c = [&](u32x4& d, unsigned v, int so) { asm volatile("buffer_load_dwordx4 %0, %1, %2, %3 offen sc1" : "=v"(d) : "v"(v), "s"(rsW), "s"(so) : "memory"); };
      float tm, tr;
      unsigned tc;
      auto fin = [&]() {
        if (FIRST) {
          const unsigned tag = (unsigned)E + 1u;
          asm volatile("" : "+v"(sv[0]));
          _Pragma("unroll 1") for (unsigned spins = 0; spins < (1u << 18); ++spins) {
            if (__all(sv[0][1] == tag && sv[0][3] == tag)) break;
            __builtin_amdgcn_s_sleep(1);
            asm volatile("buffer_load_dwordx4 %0, %1, %2, 0 offen sc1\n\ts_waitcnt vmcnt(0)" : "=v"(sv[0]) : "v"(vS), "s"(rsW) : "memory");
          }
          tm = __uint_as_float(sv[0][0]); tr = __uint_as_float(sv[0][2]);
        } else {
          asm volatile("" : "+v"(sv[0]), "+v"(sv[1]), "+v"(sv[2]), "+v"(sv[3]));
          const f32x4 t = sum4(sv);
          const unsigned u = xs4(t[0], t[2], t[1], t[3]);
          auto h = __builtin_amdgcn_permlane32_swap(u, u, false, false);
          const float mean = __uint_as_float(h[0]) * (1.0f / 1024.0f);
          const float var = fmaxf(__uint_as_float(h[1]) * (1.0f / 1024.0f) - mean * mean, 0.f);
          tm = -mean; tr = __builtin_amdgcn_rsqf(var + 1e-5f);
        }
      };
      auto cfin = [&]() {
        if (FIRST || HH) {
          asm volatile("" : "+v"(cv[0]), "+v"(cv[1]), "+v"(cv[2]), "+v"(cv[3]));
          const f32x4 t = sum4(cv);
          tc = xs4(t[0], t[1], t[2], t[3]);
        }
      };
      bf16x8 cf[6];
      f32x4 csum = {0, 0, 0, 0};
      if (FIRST) { fall = E + 1u; vX = (unsigned)(bid * 8192 + lw * 2048 + lane * 16); asm volatile("" : "+v"(fall), "+v"(vX)); }
#pragma unroll
      for (int kt = 0; kt < 13; ++kt) {
        const int cur = (kt % 3) * STAGE;
        if (!FIRST && kt == 0) { WAITV(4); }
        else if (FIRST && kt == 4 && lw == 1) { WAITVN(N + 2); }
        else if (HH && (kt == 4 || kt == 6) && true) { WAITVN(N + 1); }
        else if (HH && kt == 5 && true) { WAITVN(N + 2); }
        else if ((!FIRST && kt == 1 && lw == 3) || ((FIRST || HH) && kt == 10 && lw == 3) || (FIRST && (kt == 2 || kt == 4 || kt == 5))) { WAITVN(N + 1); }
        else { WAITVN(N); }
        if (!FIRST && kt == 2 && lw == 3) {
          asm volatile("" : "+v"(fall));
          if (__any(fall != E + (unsigned)layer)) poll_flags(ws, E, 1, rt, hp, lane, layer, 0xffffull);
        }
        if (HH && kt == 11 && lw == 3) {
          asm volatile("" : "+v"(fal2));
          if (__any(fal2 != E + (unsigned)layer)) {
            _Pragma("unroll 1") for (int j = 0; j < 4; ++j) poll_flags(ws, E, 1, rt, (hp - ((1 << j) >> 1)) & 15, lane, layer, 0xffff00000000ull);
          }
        }
        if (FIRST && kt == 11 && lw == 3) {
          asm volatile("" : "+v"(fal2));
          if (__any(fal2 != E + 1u)) poll_flags(ws, E, 2, rt, hp, lane, 1u, 0xffff00000000ull);
        }
        BAR;
        if (FIRST && kt == 1) {
          asm volatile("" ::: "memory");
#pragma unroll
          for (int i = 0; i < 6; ++i) { const char* t = smem + cso[i >> 1] + (i & 1) * 12288; cf[i] = cat8(tr_read(t), tr_read(t + 512)); }
        }
        if (FIRST && kt >= 2 && kt < 6 && lw == kt - 2) setup();
        if (kt >= 6 && kt < 10 && lw == kt - 6) prep(IC<1>{});
        stageA(kt + 3, cur);
        if (FIRST && kt == 1) {
          asm volatile("" : "+v"(sel[0]));
#pragma unroll
          for (int i = 0; i < 6; ++i) csum = MFMA(cf[i], sel[i >> 1], csum);
          asm volatile("" : "+v"(csum));
        }
        stageB(W, kt + 3, cur);
        if (FIRST) {
          if (kt == 1) __builtin_amdgcn_raw_buffer_store_b128(__builtin_bit_cast(u32x4, csum), rsW, cst, 0, 16);
          if (kt == 3 && tid == 320) __builtin_amdgcn_raw_buffer_store_b64(__builtin_bit_cast(u32x2, fall), rsW, WS_FLAGS + (unsigned)(512 + rt * 16 + hp) * FLS, 0, 16);
          if (kt == 3 || kt == 4) glds16s(wo + (kt - 3) * 512, vX, lds_addr(smem) + OFF_JK);
        }
        if (HH && (kt == 3 || kt == 4) && true) glds16s(wo + (kt - 3) * 512, vY, lds_addr(smem) + OFF_JK);
        if (!FIRST && kt == 0 && lw == 3) asm volatile("global_load_dwordx2 %0, %1, %2 sc1" : "=v"(fall) : "v"(fao), "s"(ws + WS_FLAGS) : "memory");
        if ((FIRST || HH) && kt == 9 && lw == 3) asm volatile("global_load_dwordx2 %0, %1, %2 sc1" : "=v"(fal2) : "v"(fa2), "s"(ws + WS_FLAGS) : "memory");
      }
      WAITVN(N);
      BAR;
#pragma unroll
      for (int i = 0; i < (FIRST ? 1 : 4); ++i) ld_c(sv[i], vS, i * 4096);
      if (FIRST || HH) {
#pragma unroll
        for (int i = 0; i < 4; ++i) ld_c(cv[i], vC, i * 3072);
      }
      if (HH) glds16s(wphi, vX, lds_addr(smem) + OFF_WP + lw * 1024);
      else if (!FIRST) glds16c(ws + WS_CSA, vX, lds_addr(smem) + OFF_CS);
      wo_tile(0); wo_tile(1);
      WAITVN(HH ? 13 : 9);
      BAR;
      if (FIRST) {
        if (lw < 3) { ld_pw(0); ld_pw(4); WAITV(12); } else { WAITV(4); }
      } else {
        WAITV(4);
      }
      cfin();
      fin();
      asm volatile("" : "+v"(tm), "+v"(tr));
      if (FIRST || HH) asm volatile("" : "+v"(tc));
      if (FIRST && lw < 3) { WAITV(8); } else { WAITV(0); }
      lds<f32x2>(sb + dS) = f32x2{tm, tr};
      lds<float>(sb + dR) = tm; lds<float>(sb + dR + 512) = tr;
      if (FIRST || HH) lds<unsigned>(sb + dC) = tc;
      if (!HH) lds<unsigned>(prd) = 0u;
      WAITL0;
    };
    typedef std::integral_constant<bool, true> T_;
    typedef std::integral_constant<bool, false> F_;
    __builtin_amdgcn_s_setprio(3);
    if (first) run(T_{}); else run(F_{});
    __builtin_amdgcn_s_setprio(0);
    BAR;
    if constexpr (HH) {
      u32x4 c0, c1;
      if (layer == 1) {
        const int n = bid * 8 + lw * 2;
        const void* p;
        unsigned a0, a1;
        if (n < 1536) {
          const int r = n % 768, q = r % 384, e = q % 24;
          p = n < 768 ? wn1 : W;
          a0 = (unsigned)((((q / 24) * 64 + (e / 6) * 16 + (lane >> 3)) * 3072 + ((e % 6) >> 1) * 1024 + (r < 384 ? 192 : 704)) * 2);
          a1 = a0 + 8 * 6144 + cB1 * 16; a0 += cB0 * 16;
        } else {
          p = n < 1792 ? wn2 : wo;
          a0 = (unsigned)((((n & 127) * 8 + (lane >> 3)) * DM + ((n & 128) ? 704 : 192) + ((lane & 7) ^ ((lane >> 4) << 1)) * 8) * 2);
          a1 = a0 + 8 * DM * 2;
        }
        p = uni(p);
        gld16(c0, a0, p); gld16(c1, a1, p);
        const u32x4 pz = empty4();
        __builtin_amdgcn_raw_buffer_store_b128(pz, rsW, dm, 0, 16);
        __builtin_amdgcn_raw_buffer_store_b128(pz, rsW, dm, 4096, 16);
        __builtin_amdgcn_raw_buffer_store_b128(pz, rsW, dm2, 0, 16);
      }
#pragma unroll
      for (int t = 2; t < 6; ++t) wo_tile(t);
      if (layer == 1) {
        WAITV(11);
        __builtin_amdgcn_sched_barrier(0);
        asm volatile("" : "+v"(c0), "+v"(c1));
        __builtin_amdgcn_raw_buffer_store_b128(c0, rsW, WS_IMG + (unsigned)(bid * 8192 + lw * 2048 + lane * 16), 0, 16);
        __builtin_amdgcn_raw_buffer_store_b128(c1, rsW, WS_IMG + (unsigned)(bid * 8192 + lw * 2048 + 1024 + lane * 16), 0, 16);
        WAITV(0);
      }
      pwl = vW0; pwm = vW1; kol = ko2;
      asm volatile("" : "=v"(csq[0]), "=v"(csq[1]), "=v"(st01), "=v"(st23), "=v"(wf[0]), "=v"(wf[1]));
      asm volatile("" : "=v"(acc[0][0]), "=v"(acc[0][1]), "=v"(acc[1][0]), "=v"(acc[1][1]));
      asm volatile("" : "=v"(qh[0][0]), "=v"(qh[0][1]), "=v"(qh[1][0]), "=v"(qh[1][1]));
    } else {
      const int lane = lane_id();
#pragma unroll
      for (int t = 2; t < 12; ++t) {
        wo_tile(t);
        if (t == 2 && first) {
        const u32x4 pz = empty4();
        __builtin_amdgcn_raw_buffer_store_b128(pz, rsW, dm, 0, 16);
        __builtin_amdgcn_raw_buffer_store_b128(pz, rsW, dm, 4096, 16);
        __builtin_amdgcn_raw_buffer_store_b128(pz, rsW, dm3, 0, 16);
        __builtin_amdgcn_raw_buffer_store_b128(pz, rsW, dm2, 0, 16);
        if (lw == 3 && lane < 48) __builtin_amdgcn_raw_buffer_store_b128(lds<u32x4>(sb + OFF_CS + lane * 16), rsW, WS_CSA + (unsigned)((rt * 16 + hp) * 1024 + lane * 16), 0, 16);
        }
      }
      if (first) { BAR;   WAITV(0); }
    }
    asm volatile("" : "={v[64:79]}"(xq));
  } else {

    prep(IC<0>{});
    if (first) {
      const int row = m0 + hp * 8 + w * 2 + (lane >> 5);
      asm volatile("s_load_dwordx2 %0, %1, 0x0\n\ts_waitcnt lgkmcnt(0)" : "=s"(E) : "s"(ws) : "memory");
      const unsigned tag = (unsigned)E + 1u;
      static_assert(HH || (OFF_SP == 122880 && OFF_R2 + 1024 <= OFF_JK), "addresses used at the top of the kernel");
      WAITV(0);
      u32x4 xs[4];
#pragma unroll
      for (int i = 0; i < 4; ++i) xs[i] = *(const u32x4*)(smem + OFF_SP + w * 4096 + (lane >> 5) * 2048 + (lane & 31) * 16 + i * 512);
      typedef __attribute__((ext_vector_type(2))) __bf16 bf2;
      const bf2 one2 = __builtin_bit_cast(bf2, 0x3F803F80u);
      float sa[2] = {0.f, 0.f}, sq[2] = {0.f, 0.f};
#pragma unroll
      for (int i = 0; i < 16; ++i) {
        const unsigned u = xs[i >> 2][i & 3];
        const bf2 t = __builtin_bit_cast(bf2, u);
        sa[i & 1] = __builtin_amdgcn_fdot2_f32_bf16(t, one2, sa[i & 1], false);
        sq[i & 1] = __builtin_amdgcn_fdot2_f32_bf16(t, t, sq[i & 1], false);
      }
      float sm = r16_sum(sa[0] + sa[1]), m2 = r16_sum(sq[0] + sq[1]);
      {
        auto r = __builtin_amdgcn_permlane16_swap(__float_as_uint(sm), __float_as_uint(sm), false, false);
        auto q = __builtin_amdgcn_permlane16_swap(__float_as_uint(m2), __float_as_uint(m2), false, false);
        sm = __uint_as_float(r[0]) + __uint_as_float(r[1]); m2 = __uint_as_float(q[0]) + __uint_as_float(q[1]);
      }
      const float mean = sm * (1.0f / 1024.0f);
      m2 = fmaxf(m2 - sm * mean, 0.f);
      u32x4 gw = {__float_as_uint(-mean), tag, __float_as_uint(rsqrtf(m2 * (1.0f / 1024.0f) + 1e-5f)), tag};
      if ((lane & 31) == 0) __builtin_amdgcn_raw_buffer_store_b128(gw, rsW, (unsigned)(WS_G0 + row * 16), 0, 16);
      __builtin_amdgcn_raw_buffer_store_b128(empty4(), rsW, WS_HL + (unsigned)((rt * 16 + hp) * 4096 + tid * 16), 0, 16);
    }
    const int fl = (q4 >> 1) | ((g & 1) << 1);
    const unsigned bo = lds_addr(smem) + ABYTES + (g >> 1) * 6144 + (g & 1) * 1024 + q4 * 128 + p4 * 8;
    const unsigned bo0 = bo + (((wn * 2 + 0) ^ fl) << 5), bo1 = bo + (((wn * 2 + 1) ^ fl) << 5);
    const unsigned ar = lds_addr(smem) + (wm * 64 + fr) * 128;
    const unsigned aK0 = ar + ((g ^ (fr & 7)) << 4), aK1 = ar + (((4 + g) ^ (fr & 7)) << 4);
    f32x32 ac0, ac1, ac2;
    asm volatile("" :: "v"(aK0), "v"(aK1), "v"(bo0), "v"(bo1));
    if (first) BAR;
    else publish(rsW, tid, 256 + rt * 16 + hp, E + (unsigned)layer);
#define G1_OUT
#define G1_T(a)
#define G1_TEND
#define G1_TCL
    asm volatile(G1_MACROS
        "s_mov_b32 vcc_hi, %[lay]\n"
        ".if %[hh]\n s_branch .Lg1m\n .else\n"
        ".p2align 3\n .Lg1m:\n"
        "G1RDB 0, v16, v17, 0\n G1RDA v12, v13, 0\n G1RDB 1, v16, v17, 0\n"
        "ds_read_b128 v[64:67], v20\n ds_read_b128 v[68:71], v20 offset:2048\n"
        "ds_read_b128 v[72:75], v20 offset:4096\n ds_read_b128 v[76:79], v20 offset:6144\n"
        "s_cmp_eq_u32 vcc_hi, 0\n s_waitcnt lgkmcnt(12)\n"
        "G1SG 0, 1, 1, v12, v13, v16, v17, 0\n"
        "s_mov_b32 vcc_lo, 16\n"
        "G1SG 1, 1, 1, v12, v13, v16, v17, 40960\n"
        "G1SG 2, 1, 2, v12, v13, v16, v17, 40960\n"
        "s_cbranch_scc1 .Lg1a%=\n"
        "s_barrier\n"
        G1_T(88)
        "s_barrier\n"
        G1_T(92)
        "G1RDA v12, v13, 40960\n"
        "s_waitcnt lgkmcnt(4)\n"
        "s_branch .Lg1b%=\n"
        ".Lg1a%=:\n"
        G1_T(88)
        "s_barrier\n"
        G1_T(92)
        "G1RDB 0, v16, v17, 40960\n G1RDA v12, v13, 40960\n G1RDB 1, v16, v17, 40960\n"
        ".p2align 3\n s_nop 0\n"
        ".Lg1b%=:\n"
        "s_waitcnt lgkmcnt(8)\n"
        "G1SG 0, 0, 1, v12, v13, v16, v17, 40960\n"
        "s_barrier\n"
        "G1SG 1, 0, 1, v14, v15, v18, v19, 0\n"
        "G1SG 2, 0, 1, v14, v15, v18, v19, 0\n"
        "s_lshr_b32 vcc_lo, vcc_lo, 1\n s_waitcnt vmcnt(0) lgkmcnt(8)\n"
        "G1SG 0, 0, 1, v14, v15, v18, v19, 0\n"
        "s_barrier\n"
        "G1SG 1, 0, 1, v12, v13, v16, v17, 0\n"
        "G1SG 2, 0, 1, v12, v13, v16, v17, 0\n"
        "s_cbranch_scc0 .Lg1c%=\n s_waitcnt lgkmcnt(8)\n"
        "G1SG 0, 0, 1, v12, v13, v16, v17, 0\n"
        "s_barrier\n"
        "G1SG 1, 0, 1, v12, v13, v16, v17, 40960\n"
        "G1SG 2, 0, 1, v12, v13, v16, v17, 40960\n"
        "s_branch .Lg1b%=\n"
        ".Lg1c%=:\n"
        "s_waitcnt lgkmcnt(8)\n"
        "G1SG 0, 0, 1, v12, v13, v16, v17, 0\n s_bitcmp1_b32 vcc_hi, 0\n"
        G1_T(96)
        "s_cbranch_scc1 .Lg1n\n s_barrier\n"
        G1_SLIDE
        ".endif\n .if %[hh]\n .p2align 3\n s_nop 0\n .Lg1n:\n s_barrier\n"
        G1_HEDGE
        ".endif\n"
        G1_TEND G1_PURGE
        : "=&{v[160:191]}"(ac0), "=&{v[192:223]}"(ac1), "=&{v[224:255]}"(ac2), "=&{v[64:79]}"(xq),
          "=&{v[88:91]}"(csq[1]), "=&{v[148:151]}"(csq[0]), "=&{v[140:143]}"(st01), "=&{v[136:139]}"(st23),
          "+{v[48:63]}"(pa), "+{v[36:39]}"(bias[0]), "+{v[40:43]}"(bias[1]), "+{v[44:47]}"(bias[2]) G1_OUT
        : "{v12}"(aK0), "{v13}"(aK1), "{v14}"(aK0 + 2 * STAGE), "{v15}"(aK1 + 2 * STAGE), "{v16}"(bo0), "{v17}"(bo1),
          "{v18}"(bo0 + 2 * STAGE), "{v19}"(bo1 + 2 * STAGE), "{v20}"(wn ? aK1 : aK0),
          [lay] "s"(layer), [hh] "n"(MODE), [rs] "s"(rsW), [wm] "s"(wm)
        : "memory", "scc", "vcc", "m0", G1_TCL G1_CLOBBER);
    acc[0][0] = quad<0>(ac0); acc[0][1] = quad<1>(ac0); acc[1][0] = quad<6>(ac0); acc[1][1] = quad<7>(ac0);
    asm volatile("" : "=s"(kol));
    wf[0] = __builtin_bit_cast(bf16x8, bias[0]); wf[1] = __builtin_bit_cast(bf16x8, bias[1]);
    qh[0][0] = quad<4>(ac1); qh[0][1] = quad<5>(ac1); qh[1][0] = quad<2>(ac2); qh[1][1] = quad<3>(ac2);
  }
  ST(layer * 24 + 4)
  {
  const int lane = lane_id(), tid = w * 64 + lane, fr = lane & 15, g = lane >> 4;
  (void)fr; (void)g;
  if constexpr (HH) {
    const f32x4 z4 = {0, 0, 0, 0};
    constexpr float L2E = 1.4426950408889634f;
    const unsigned kvo = WS_KV + (layer >> 1) * KVLB, kfo = kvo + 16u * NH * KVPB + hp * KVFB;
    auto phi2 = [&](const f32x4 (&a)[2][2], f32x4 st, bf16x8 (&qp)[2][2]) {
      f32x4 pm[2][2];
      f32x2 f[2][4];
      asm volatile("" : "+v"(st));
#pragma unroll
      for (int i = 0; i < 8; ++i) {
        const int rb = i >> 2, c = (i >> 1) & 1, e = (i & 1) * 2;
        f[rb][i & 3] = __builtin_elementwise_fma(f32x2{csq[c][e], csq[c][e + 1]}, f32x2{st[2 * rb], st[2 * rb]}, f32x2{a[rb][c][e], a[rb][c][e + 1]});
      }
#pragma unroll
      for (int rb = 0; rb < 2; ++rb) asm volatile("" : "+v"(f[rb][0]), "+v"(f[rb][1]), "+v"(f[rb][2]), "+v"(f[rb][3]));
#pragma unroll
      for (int rb = 0; rb < 2; ++rb) {
        const bf16x8 xf = __builtin_bit_cast(bf16x8, u32x4{pk2(f[rb][0][0], f[rb][0][1]), pk2(f[rb][1][0], f[rb][1][1]), pk2(f[rb][2][0], f[rb][2][1]), pk2(f[rb][3][0], f[rb][3][1])});
        pm[rb][0] = MFMA(wf[0], xf, z4); pm[rb][1] = MFMA(wf[1], xf, z4);
      }
      asm volatile("s_nop 7" : "+v"(pm[0][0]), "+v"(pm[0][1]), "+v"(pm[1][0]), "+v"(pm[1][1]));
      float mx[2], mn[2];
#pragma unroll
      for (int u = 0; u < 2; ++u) max8pn(pm[u][0], pm[u][1], mx[u], mn[u]);
      xr4<true>(mx[0], mn[0], mx[1], mn[1]);
      float ep[2][8], en[2][8], sp[2], sn[2];
#pragma unroll
      for (int u = 0; u < 2; ++u) {
        const float c = st[2 * u + 1] * L2E, mxs = -mx[u] * c, mns = -mn[u] * c;
#pragma unroll
        for (int i = 0; i < 4; ++i) {
          const f32x2 p2 = {pm[u][i >> 1][(i & 1) * 2], pm[u][i >> 1][(i & 1) * 2 + 1]};
          const f32x2 tp = __builtin_elementwise_fma(p2, f32x2{c, c}, f32x2{mxs, mxs});
          const f32x2 tn = __builtin_elementwise_fma(-p2, f32x2{c, c}, f32x2{mns, mns});
          ep[u][2 * i] = __builtin_amdgcn_exp2f(tp[0]); ep[u][2 * i + 1] = __builtin_amdgcn_exp2f(tp[1]);
          en[u][2 * i] = __builtin_amdgcn_exp2f(tn[0]); en[u][2 * i + 1] = __builtin_amdgcn_exp2f(tn[1]);
        }
        const f32x2 a2 = (f32x2{ep[u][0], ep[u][1]} + f32x2{ep[u][2], ep[u][3]}) + (f32x2{ep[u][4], ep[u][5]} + f32x2{ep[u][6], ep[u][7]});
        const f32x2 b2 = (f32x2{en[u][0], en[u][1]} + f32x2{en[u][2], en[u][3]}) + (f32x2{en[u][4], en[u][5]} + f32x2{en[u][6], en[u][7]});
        sp[u] = a2[0] + a2[1]; sn[u] = b2[0] + b2[1];
      }
      xr4<false>(sp[0], sn[0], sp[1], sn[1]);
#pragma unroll
      for (int rb = 0; rb < 2; ++rb) {
        const float ip = __builtin_amdgcn_rcpf(sp[rb]), in = __builtin_amdgcn_rcpf(sn[rb]);
        qp[rb][0] = __builtin_bit_cast(bf16x8, u32x4{pk2(ep[rb][0] * ip, ep[rb][1] * ip), pk2(ep[rb][2] * ip, ep[rb][3] * ip), pk2(ep[rb][4] * ip, ep[rb][5] * ip), pk2(ep[rb][6] * ip, ep[rb][7] * ip)});
        qp[rb][1] = __builtin_bit_cast(bf16x8, u32x4{pk2(en[rb][0] * in, en[rb][1] * in), pk2(en[rb][2] * in, en[rb][3] * in), pk2(en[rb][4] * in, en[rb][5] * in), pk2(en[rb][6] * in, en[rb][7] * in)});
        asm volatile("" : "+v"(qp[rb][0]), "+v"(qp[rb][1]));
      }
    };
    bf16x8 one8 = ones8();
    asm volatile("" : "+v"(one8));
    WAITL0; BAR;
    ST(layer * 24 + 5)
    {
      bf16x8 af[4], v0[4], v1[4];
      asm volatile("" ::: "memory");
#pragma unroll
      for (int sw = 0; sw < 4; ++sw) {
        af[sw] = cat8(tr_read(ppa + sw * (64 * PSTR)), tr_read(ppa + sw * (64 * PSTR) + 16 * PSTR));
        v0[sw] = lds<bf16x8>(pvr + sw * 4096);
        v1[sw] = lds<bf16x8>(pvr + sw * 4096 + 1024);
      }
      __builtin_amdgcn_sched_barrier(0);
      f32x4 kv[3] = {z4, z4, z4};
#pragma unroll
      for (int sw = 0; sw < 4; ++sw) {
        kv[0] = MFMA(af[sw], v0[sw], kv[0]);
        kv[1] = MFMA(af[sw], v1[sw], kv[1]);
        kv[2] = MFMA(af[sw], one8, kv[2]);
      }
      auto pc = [](float x, float y) { const unsigned t = pk2(x, y); return t < EMPTY - 1u ? t : EMPTY - 1u; };
      __builtin_amdgcn_raw_buffer_store_b128(u32x4{pc(kv[0][0], kv[0][1]), pc(kv[0][2], kv[0][3]), pc(kv[1][0], kv[1][1]), pc(kv[1][2], kv[1][3])}, rsW, gkv, 0, 16);
      __builtin_amdgcn_raw_buffer_store_b64(u32x2{pc(kv[2][0], kv[2][1]), pc(kv[2][2], kv[2][3])}, rsW, gkz, 0, 16);
    }
    auto wo_late = [&](int t) { glds2v(wos + knib(kol, t) * tO, pwl, pwm, sb + OFF_WO + t * 8192 + (w & 3) * 2048); };
    ST(layer * 24 + 6)
    u32x4 l1[8];
    const bool red = w == 7 || (w == 6 && (rt & 7) == 0);
    unsigned lo = kvo + (unsigned)((2 * hp + (rt >> 3) + (lane >> 5) * 8 * NH) * KVPB) +
                  (w == 7 ? (unsigned)((rt & 4) * 512 + (g & 1) * 1024 + (rt & 3) * 256 + fr * 16) : (unsigned)(4096 + (lane & 7) * 16));
    unsigned fs = w == 7 ? kfo + (unsigned)((rt >> 2) * 2048 + (lane >> 5) * 1024 + (rt & 3) * 256 + fr * 16 + (g & 1) * 8)
                : (lane & 24) ? 0x7fff0000u : kfo + (unsigned)(8192 + (rt >> 3) * 128 + (lane & 4) * 16 + (lane & 1) * 32 + (lane >> 5) * 16 + (lane & 2) * 4);
    auto l1_load = [&]() {
      asm volatile("" ::: "memory");
#pragma unroll
      for (int p = 0; p < 8; ++p) asm volatile("buffer_load_dwordx4 %0, %1, %2, %3 offen sc1" : "=v"(l1[p]) : "v"(lo), "s"(rsW), "s"(p * (NH * KVPB)) : "memory");
      __builtin_amdgcn_sched_barrier(0);
    };
    if (red) asm volatile("" : "+v"(lo), "+v"(fs));
    if (red) { __builtin_amdgcn_s_sleep(13); l1_load(); }
    bf16x8 qp2[2][2];
    if (!kg) {
      phi2(qh, st23, qp2);
    } else if (red) {
      typedef __attribute__((ext_vector_type(2))) __bf16 bf2;
      unsigned c0 = 0x00003F80u, c1 = 0x3F800000u;
      asm volatile("" : "+v"(c0), "+v"(c1));
      float s4[4];
      _Pragma("unroll 1") for (unsigned spins = 0; spins < (1u << 18); ++spins) {
        float a[8] = {0.f, 0.f, 0.f, 0.f, 0.f, 0.f, 0.f, 0.f};
        ARRIVED("+v"(l1[0]), "+v"(l1[1]), "+v"(l1[2]), "+v"(l1[3]), "+v"(l1[4]), "+v"(l1[5]), "+v"(l1[6]), "+v"(l1[7]));
#pragma unroll
        for (int p = 0; p < 8; ++p)
#pragma unroll
          for (int e = 0; e < 4; ++e) {
            const unsigned u = l1[p][e];
            a[2 * e] = __builtin_amdgcn_fdot2_f32_bf16(__builtin_bit_cast(bf2, u), __builtin_bit_cast(bf2, c0), a[2 * e], false);
            a[2 * e + 1] = __builtin_amdgcn_fdot2_f32_bf16(__builtin_bit_cast(bf2, u), __builtin_bit_cast(bf2, c1), a[2 * e + 1], false);
          }
#pragma unroll
        for (int i = 0; i < 4; ++i) {
          auto r = __builtin_amdgcn_permlane32_swap(__float_as_uint(a[i]), __float_as_uint(a[i + 4]), false, false);
          s4[i] = __uint_as_float(r[0]) + __uint_as_float(r[1]);
        }
        const float t = s4[0] + s4[2];
        if (__all(t == t)) break;
        unsigned mxw = 0;
#pragma unroll
        for (int p = 0; p < 8; ++p)
#pragma unroll
          for (int e = 0; e < 4; ++e) mxw = l1[p][e] > mxw ? l1[p][e] : mxw;
        if (__all(mxw != EMPTY)) break;
        l1_load();
      }
      WAITV(0);
      unsigned o0 = pk2(s4[0], s4[1]), o1 = pk2(s4[2], s4[3]);
      o0 = o0 < EMPTY - 1u ? o0 : EMPTY - 1u; o1 = o1 < EMPTY - 1u ? o1 : EMPTY - 1u;
      __builtin_amdgcn_raw_buffer_store_b64(u32x2{o0, o1}, rsW, fs, 0, 16);
    }
    ST(layer * 24 + 8)
    WAITL0;
    BAR;
    ST(layer * 24 + 9)
    if (kg) {
#pragma unroll
      for (int i = 6; i < 12; ++i) wo_late(i);
    } else {
      unsigned fo = kfo + (unsigned)(wn * 4096 + lane * 16), fz = kfo + (unsigned)(8192 + wn * 128 + g * 16);
      asm volatile("" : "+v"(fo), "+v"(fz));
      bf16x8 qp[2][2];
      phi2(acc, st01, qp);
      asm volatile(G1_OUTPUT
          :: "{v[224:227]}"(qp[0][0]), "{v[228:231]}"(qp[0][1]), "{v[232:235]}"(qp[1][0]), "{v[236:239]}"(qp[1][1]),
             "{v[240:243]}"(qp2[0][0]), "{v[244:247]}"(qp2[0][1]), "{v[248:251]}"(qp2[1][0]), "{v[252:255]}"(qp2[1][1]),
             [fo] "v"(fo), [fz] "v"(fz), [go] "v"(ogo), [lo] "v"(opo), [rs] "s"(rsW)
          : "memory", "vcc", "scc", "m0", "v152", "v153", "v154", "v155", "v156", "v157", "v158", "v159", "v160", "v161", "v162", "v163", "v164", "v165", "v166", "v167", "v168", "v169", "v170", "v171", "v172", "v173", "v174", "v175", "v176", "v177", "v178", "v179", "v180", "v181", "v182", "v183", "v184", "v185", "v186", "v187", "v188", "v189", "v190", "v191", "v192", "v193", "v194", "v195", "v196", "v197", "v198", "v199", "v200", "v201", "v202", "v203", "v204", "v205", "v206", "v207", "v208", "v209", "v210", "v211", "v212", "v213", "v214", "v215", "v216", "v217", "v218", "v219", "v220", "v221", "v222", "v223");
    }
  }
  ST(layer * 24 + 12)
  if (first && w >= 4 && w < 7) {
    typedef __attribute__((ext_vector_type(2))) __bf16 bf2;
    unsigned c0 = 0x00003F80u, c1 = 0x3F800000u;
    asm volatile("" : "+v"(c0), "+v"(c1));
    const bf2 lo1 = __builtin_bit_cast(bf2, c0), hi1 = __builtin_bit_cast(bf2, c1);
    float a[8] = {0.f, 0.f, 0.f, 0.f, 0.f, 0.f, 0.f, 0.f};
#pragma unroll
    for (int i = 0; i < 8; ++i) {
      asm volatile("" : "+v"(pw[i]));
#pragma unroll
      for (int e = 0; e < 4; ++e) {
        const unsigned u = pw[i][e];
        a[2 * e] = __builtin_amdgcn_fdot2_f32_bf16(__builtin_bit_cast(bf2, u), lo1, a[2 * e], false);
        a[2 * e + 1] = __builtin_amdgcn_fdot2_f32_bf16(__builtin_bit_cast(bf2, u), hi1, a[2 * e + 1], false);
      }
    }
#pragma unroll
    for (int i = 0; i < 4; ++i) {
      auto r = __builtin_amdgcn_permlane32_swap(__float_as_uint(a[i]), __float_as_uint(a[i + 4]), false, false);
      a[i] = __uint_as_float(r[0]) + __uint_as_float(r[1]);
    }
#pragma unroll
    for (int i = 0; i < 2; ++i) {
      auto r = __builtin_amdgcn_permlane16_swap(__float_as_uint(a[i]), __float_as_uint(a[i + 2]), false, false);
      a[i] = __uint_as_float(r[0]) + __uint_as_float(r[1]);
      a[i] += dpp_ror<8>(a[i]);
    }
    if (!(lane & 8))
      __builtin_amdgcn_raw_buffer_store_b64(u32x2{__float_as_uint(a[0]), __float_as_uint(a[1])}, rsW,
          (unsigned)(WS_CSP + ((((hp + (1 << (w - 4))) & 15) * 16 + rt) * 192 + (w - 4) * 64 + (lane & 7) * 8 + (lane >> 5) * 4 + ((lane >> 4) & 1) * 2) * 4), 0, 16);
  }
  }
}

#undef pcs
#undef prs
#undef opo
#undef ogo
#undef hof
#undef dm
#undef dm2
#undef pcv
#undef pwf
#undef prv
#undef pvf
#undef ppk
#undef ppa
#undef pvr
#undef gkv
#undef gkz
#undef pwl
#undef pwm
#undef plb
#undef prd
#undef dm3

__device__ __forceinline__ void g2_phase(char* smem, char* ws, u64 E, int w, f32x16& xq, Next& nx, int layer, const u16* __restrict__ x0,
                                         const u16* __restrict__ wo, const u16* __restrict__ wnx, u16* __restrict__ out) {
  IDS
  wnx = uni(wnx);
  const unsigned sb = lds_addr(smem);
  const int lw = w & 3, k2 = 16 * lw + (lane >> 3);
  const int r0 = w * 16 + (lane >> 3);
  unsigned pxr = sb + r0 * 128 + (lane & 7) * 16;
  const unsigned rmo = (unsigned)(((m0 + r0) * DM + hp * 64 + ((lane & 7) ^ (lane >> 3)) * 8) * 2);
  const unsigned imo = (unsigned)((rt * 16 + hp) * 16384 + r0 * 128 + (lane & 7) * 16);
  constexpr int S0 = 0, S1 = 16384, S2 = 32768, S3 = 147456, S4 = OFF_WO, S5 = 98304, S6 = 114688;
  constexpr int AS[16] = {S0, S1, S2, S3, S0, S1, S4, S2, S3, S0, S1, S4, S5, S3, S0, S6};
  constexpr int OFF_C = 0;
  auto bs = [](int t) { return OFF_WO + (t < 12 ? t : t - 10) * 8192; };
  const char *xa1, *xa2, *wq2;
  u64 fm;
  unsigned vB0, vB1, vXA, po;
  if (w >= 4) {
    constexpr int WT[15] = {0, 8, 12, 14, 16, 18, 20, 18, 16, 14, 12, 12, 10, 10, 8};
    const char* o = ws + WS_O + layer * (SEQ * DM * 2) + rt * 262144;
    const unsigned vA = (unsigned)(lw * 4096 + lane * 16);
    u64 kord = korder(bid & 3, hp);
    auto ldA = [&](int s) { glds4c(o + knib(kord, s) * 16384, vA, sb + AS[s] + lw * 4096); };
    const bool im = img(layer, hp), in = img(layer + 1, hp);
    unsigned vW0 = im ? (unsigned)(lw * 2048 + lane * 16) : (unsigned)((k2 * DM + hp * 64 + ((lane & 7) ^ ((lane >> 4) << 1)) * 8) * 2), vW1 = vW0 + (im ? 0u : 8 * DM * 2 - 1024u);
    const char *wos = wo_src(ws, wo, layer, hp), *wqs = wq_src(ws, wnx, layer + 1, hp);
    unsigned tO = im ? 8192u : 131072u;
    asm volatile("" : "+v"(vW0), "+v"(vW1));
    asm volatile("" : "+s"(wos), "+s"(tO));
    auto ldW = [&](int t) { glds2v(wos + knib(kord, t) * tO, vW0, vW1, sb + bs(t) + lw * 2048); };
    const int fB = (k2 >> 1) & 1;
    vB0 = in ? (unsigned)(lw * 6144 + lane * 16) : (unsigned)((k2 * 3072 + hp * 64 + (((((lane & 7) >> 1) ^ fB) << 1) | (lane & 1)) * 8) * 2);
    vB1 = in ? vB0 : (unsigned)(((k2 + 8) * 3072 + hp * 64 + (((((lane & 7) >> 1) ^ (fB | 2)) << 1) | (lane & 1)) * 8) * 2) - 1024u;
    const u64 kn = nx.kord = korder(kv1(bid), hp);
    nx.wq = wqs; nx.tw = in ? 24576u : 393216u;
    const char *wq0 = wqs + hp * (in ? 24576u : 393216u), *wq1 = wqs + knib(kn, 1) * (in ? 24576u : 393216u);
    wq2 = wqs + knib(kn, 2) * (in ? 24576u : 393216u);
    xa1 = ws + WS_X + layer * (SEQ * DM * 2) + knib(kn, 1) * 16384; xa2 = ws + WS_X + layer * (SEQ * DM * 2) + knib(kn, 2) * 16384;
    vXA = (unsigned)(rt * 262144 + lw * 4096 + lane * 16);
    fm = kfirst(kn);
    po = ((fm >> lane) & 1) ? WS_FLAGS + (unsigned)(256 + rt * 16 + lane) * FLS : 0u;
    asm volatile("" : "+v"(vB0), "+v"(vB1), "+v"(vXA), "+v"(po));
    asm volatile("" : "+s"(xa1), "+s"(xa2), "+s"(wq2), "+s"(fm), "+s"(nx.kord), "+s"(nx.wq), "+s"(nx.tw));
    asm volatile("" : "+s"(wq0), "+s"(wq1));
    auto ldB = [&](int s, int part) {
      const char* b = s ? wq1 : wq0;
      if (part) glds4v(b, vB0, vB1, vB0, vB1, sb + s * 40960 + 16384 + lw * 6144);
      else glds2v(b + 4096, vB0, vB1, sb + s * 40960 + 16384 + lw * 6144 + 4096);
    };
    const char* o1 = o + knib(kord, 1) * 16384;
    asm volatile("" : "+s"(o1), "+s"(kord), "+s"(o));
    asm volatile("" :: "v"(vA));
    if (layer) { BAR;   }
    BAR;
    glds4c(o1, vA, sb + AS[1] + lw * 4096); __builtin_amdgcn_s_sleep(3); ldA(2); __builtin_amdgcn_s_sleep(3); ldA(3); __builtin_amdgcn_s_sleep(3); ldA(4);
    WAITV(12);
    BAR;
    auto step = [&](auto ktc) {
      constexpr int kt = decltype(ktc)::value;
      WAITVN(WT[kt]);
      BAR;
      if constexpr (kt == 1) { ldA(5); ldA(6); }
      if constexpr (kt >= 2 && kt <= 10) ldA(kt + 5);
      if constexpr (kt >= 2 && kt <= 5) ldW(kt + 10);
      if constexpr (kt >= 11) ldB((kt - 11) >> 1, (kt - 11) & 1);
      if constexpr (kt >= 6) { __builtin_amdgcn_s_sleep(kt >= 11 ? 3 : 2); if (layer == 1) __builtin_amdgcn_s_sleep(1); }
    };
    step(IC<1>{}); step(IC<2>{}); step(IC<3>{}); step(IC<4>{}); step(IC<5>{}); step(IC<6>{}); step(IC<7>{});
    step(IC<8>{}); step(IC<9>{}); step(IC<10>{}); step(IC<11>{}); step(IC<12>{}); step(IC<13>{}); step(IC<14>{});
  } else {
    asm volatile("" : "=v"(vB0), "=v"(vB1), "=v"(vXA), "=v"(po));
    asm volatile("" : "=s"(xa1), "=s"(xa2), "=s"(wq2), "=s"(fm), "=s"(nx.kord), "=s"(nx.wq), "=s"(nx.tw));
    const int wm = w >> 1, wn = w & 1;
    f32x4 acc[4][2];
    unsigned co[2];
#pragma unroll
    for (int cb = 0; cb < 2; ++cb) co[cb] = sb + OFF_C + (unsigned)((wm * 64 + fr) * 128 + (((wn * 4 + cb * 2 + (g >> 1)) ^ (fr & 7)) << 4) + (g & 1) * 8);
    const int fl = (q4 >> 1) | ((g & 1) << 1);
    const int bb0 = (4 * g + q4) * 128 + p4 * 8 + (((wn * 2) ^ fl) << 5), bb1 = (4 * g + q4) * 128 + p4 * 8 + (((wn * 2 + 1) ^ fl) << 5);
    const int abase = wm * 4096 + lane * 16;
    bf16x8 fa[2][2][4], fb[2][2][2];
    unsigned ra[3], rb0[2], rb1[2];
#pragma unroll
    for (int i = 0; i < 3; ++i) { ra[i] = abase + i * 65536; asm volatile("" : "+v"(ra[i])); }
#pragma unroll
    for (int i = 0; i < 2; ++i) { rb0[i] = bb0 + OFF_WO + i * 49152; rb1[i] = bb1 + OFF_WO + i * 49152; asm volatile("" : "+v"(rb0[i]), "+v"(rb1[i])); }
    auto loadf = [&](int kt, bf16x8 (&a)[2][4], bf16x8 (&b)[2][2]) {
#pragma unroll
      for (int s = 0; s < 2; ++s) {
        const char* sA = smem + ra[AS[kt] >> 16] + (AS[kt] & 65535) + s * 8192;
        const int hi = bs(kt) >= OFF_WO + 49152, ob = bs(kt) - OFF_WO - hi * 49152 + s * 4096;
        b[s][0] = cat8(tr_read(smem + rb0[hi] + ob), tr_read(smem + rb0[hi] + ob + 2048));
        b[s][1] = cat8(tr_read(smem + rb1[hi] + ob), tr_read(smem + rb1[hi] + ob + 2048));
#pragma unroll
        for (int rb = 0; rb < 4; ++rb) a[s][rb] = *(const bf16x8*)(sA + rb * 1024);
      }
    };
    auto mma = [&](bf16x8 (&a)[2][4], bf16x8 (&b)[2][2]) {
#pragma unroll
      for (int i = 0; i < 16; ++i) {
        const int rb = i >> 2, cb = ((i >> 1) ^ rb) & 1, s = ((i + 1) >> 1) & 1;
        acc[rb][cb] = MFMA(b[s][cb], a[s][rb], acc[rb][cb]);
      }
    };
    unsigned fo = lane < 16 && lane != hp ? WS_FLAGS + (unsigned)(rt * 16 + lane) * FLS : 0u;
    const u64 ko = korder(bid & 3, hp);
    unsigned kf = kfirst(ko), kf2 = kfirst(ko >> 16) | (1u << knib(ko, 9));
    unsigned jo = (unsigned)((((rt & 7) * 128 + (w - 1) * 64 + lane) * 3072 + (hp & 3) * 1024 + (hp | 3) * 64) * 2);
    unsigned fo1 = ((kf >> (lane & 31)) & 1) ? fo : 0u, fo2 = ((kf2 >> (lane & 31)) & 1) ? fo : 0u;
    fo = (((kf | kf2) >> (lane & 31)) & 1) ? 0u : fo;
    asm volatile("" : "+v"(fo), "+v"(fo1), "+v"(fo2));
    asm volatile("" : "+s"(kf), "+s"(kf2));
    publish(rsW, tid, rt * 16 + hp, E + (unsigned)(layer + 1));
    ST(layer * 24 + 13)
    u64 fl1 = 0;
    if (w == 0) asm volatile("global_load_dwordx2 %0, %1, %2 sc1" : "+v"(fl1) : "v"(fo1), "s"(ws) : "memory");
    if (layer == 0) {
      const unsigned xo = (unsigned)(((m0 + wm * 64 + fr) * DM + hp * 64 + wn * 32 + g * 8) * 2);
      asm volatile("global_load_dwordx4 v[64:67], %1, %3\n\tglobal_load_dwordx4 v[68:71], %1, %4\n\tglobal_load_dwordx4 v[72:75], %2, %3\n\tglobal_load_dwordx4 v[76:79], %2, %4"
                   : "+{v[64:79]}"(xq) : "v"(xo), "v"(xo + 32 * DM * 2), "s"(x0), "s"(x0 + 16 * DM) : "memory");
    }
    loadf(0, fa[0], fb[0]);
    unsigned junk = 0;
    if (layer < 2 && w == 3 && (bid >> 6) == 0) code_touch(junk, bid, lane);
    if (layer == 0 && (hp & 7) < 3 && (w == 1 || w == 2))
      asm volatile("global_load_dword %0, %1, %2" : "+v"(junk) : "v"(jo), "s"(wnx) : "memory");
    __builtin_amdgcn_sched_barrier(0);
    bf16x8 id[2];
#pragma unroll
    for (int cb = 0; cb < 2; ++cb) {
      const unsigned one = (g >> 1) == cb && (g & 1) == (fr >> 3) ? 0x3F80u << ((fr & 1) * 16) : 0u;
      u32x4 t;
#pragma unroll
      for (int j = 0; j < 4; ++j) t[j] = ((fr >> 1) & 3) == j ? one : 0u;
      id[cb] = __builtin_bit_cast(bf16x8, t);
      asm volatile("" : "+v"(id[cb]));
    }
    asm volatile("" : "+v"(co[0]), "+v"(co[1]));
#pragma unroll
    for (int rb = 0; rb < 4; ++rb) { acc[rb][0] = f32x4{0, 0, 0, 0}; acc[rb][1] = f32x4{0, 0, 0, 0}; }
    mma(fa[0], fb[0]);
    __builtin_amdgcn_sched_barrier(0);
    WAITL0;
    u64 miss = 0;
    if (w == 0) { ARRIVED("+v"(fl1)); if (__ballot(fl1 != E + (unsigned)(layer + 1)) & kf) poll_eq(ws, fo1, E + (unsigned)(layer + 1), kf, kf); miss = 0xffffu & ~(kf | kf2 | (1u << hp)); }
    ST(layer * 24 + 14)
    __builtin_amdgcn_sched_barrier(0);
    BAR;
    __builtin_amdgcn_sched_barrier(0);
    asm volatile("" ::: "memory");
    if (layer == 0) WAITV(0);
    __builtin_amdgcn_sched_barrier(0);
    asm volatile("" : "+{v[64:79]}"(xq));
    const bf16x8 xf[4] = {__builtin_bit_cast(bf16x8, quad<0>(xq)), __builtin_bit_cast(bf16x8, quad<1>(xq)), __builtin_bit_cast(bf16x8, quad<2>(xq)), __builtin_bit_cast(bf16x8, quad<3>(xq))};
#pragma unroll
    for (int rb = 0; rb < 4; ++rb) { acc[rb][0] = MFMA(id[0], xf[rb], acc[rb][0]); acc[rb][1] = MFMA(id[1], xf[rb], acc[rb][1]); }
    if (w == 0) poll_eq(ws, fo2, E + (unsigned)(layer + 1), kf2, kf2);
    u64 fl2 = 0;
    auto step = [&](auto ktc) {
      constexpr int kt = decltype(ktc)::value;
      __builtin_amdgcn_sched_barrier(0);
      if (kt == 5 && miss) {
        ARRIVED("+v"(fl2));
        if (__ballot(fl2 != E + (unsigned)(layer + 1)) & miss) poll_eq(ws, fo, E + (unsigned)(layer + 1), miss, ~0ull);
      }
      WAITL0;
      BAR;
      if (kt == 2 && miss) asm volatile("global_load_dwordx2 %0, %1, %2 sc1" : "+v"(fl2) : "v"(fo), "s"(ws) : "memory");
      if (kt == 0) { ST(layer * 24 + 15) }
      __builtin_amdgcn_sched_barrier(0);
      loadf(kt + 1, fa[(kt + 1) & 1], fb[(kt + 1) & 1]);
      if (kt == 0) return;
      mma(fa[kt & 1], fb[kt & 1]);
#pragma unroll
      for (int i = 0; i < 4; ++i) {
        __builtin_amdgcn_sched_group_barrier(0x008, 1, 0);
        __builtin_amdgcn_sched_group_barrier(0x100, 1, 0);
        __builtin_amdgcn_sched_group_barrier(0x008, 1, 0);
        __builtin_amdgcn_sched_group_barrier(0x100, 3, 0);
      }
#pragma unroll
      for (int i = 0; i < 8; ++i) __builtin_amdgcn_sched_group_barrier(0x008, 1, 0);
    };
    step(IC<0>{}); step(IC<1>{}); step(IC<2>{}); step(IC<3>{}); step(IC<4>{}); step(IC<5>{}); step(IC<6>{}); step(IC<7>{});
    step(IC<8>{}); step(IC<9>{}); step(IC<10>{}); step(IC<11>{}); step(IC<12>{}); step(IC<13>{}); step(IC<14>{});
    WAITV(0);
    asm volatile("" :: "v"(junk));
    __builtin_amdgcn_sched_barrier(0);
    auto put = [&](int rb) {
#pragma unroll
      for (int cb = 0; cb < 2; ++cb) lds<u32x2>(co[cb] + rb * 2048) = u32x2{pk2(acc[rb][cb][0], acc[rb][cb][1]), pk2(acc[rb][cb][2], acc[rb][cb][3])};
    };
#pragma unroll
    for (int rb = 0; rb < 4; ++rb)
#pragma unroll
      for (int cb = 0; cb < 2; ++cb) acc[rb][cb] = MFMA(fb[1][0][cb], fa[1][0][rb], acc[rb][cb]);
#pragma unroll
    for (int rb = 0; rb < 4; ++rb) {
      __builtin_amdgcn_sched_barrier(0);
#pragma unroll
      for (int cb = 0; cb < 2; ++cb) acc[rb][cb] = MFMA(fb[1][1][cb], fa[1][1][rb], acc[rb][cb]);
      __builtin_amdgcn_sched_barrier(0);
      if (rb >= 2) put(rb - 2);
    }
    __builtin_amdgcn_sched_barrier(0);
    ST(layer * 24 + 16)
    put(2); put(3);
  }
  WAITL0; BAR;
  asm volatile("" ::: "memory");
  u32x4 xn[2];
#pragma unroll
  for (int i = 0; i < 2; ++i) xn[i] = lds<u32x4>(pxr + OFF_C + i * 1024);
  if (layer == 3) {
    const __amdgpu_buffer_rsrc_t rsO = __builtin_amdgcn_make_buffer_rsrc((void*)uni(out), (short)0, SEQ * DM * 2, 0x00020000);
    __builtin_amdgcn_raw_buffer_store_b128(xn[0], rsO, rmo, 0, 16);
    __builtin_amdgcn_raw_buffer_store_b128(xn[1], rsO, rmo + 8 * DM * 2, 0, 16);
  } else {
    __builtin_amdgcn_raw_buffer_store_b128(xn[0], rsW, WS_X + layer * (SEQ * DM * 2) + imo, 0, 16);
    __builtin_amdgcn_raw_buffer_store_b128(xn[1], rsW, WS_X + layer * (SEQ * DM * 2) + imo + 1024, 0, 16);
    float mean[2], m2[2];
#pragma unroll
    for (int i = 0; i < 2; ++i) {
      typedef __attribute__((ext_vector_type(2))) __bf16 bf2;
      const bf2 one2 = __builtin_bit_cast(bf2, 0x3F803F80u);
      float sa = 0.f, sq = 0.f;
#pragma unroll
      for (int e = 0; e < 4; ++e) {
        const unsigned u = xn[i][e];
        const bf2 t = __builtin_bit_cast(bf2, u);
        sa = __builtin_amdgcn_fdot2_f32_bf16(t, one2, sa, false);
        sq = __builtin_amdgcn_fdot2_f32_bf16(t, t, sq, false);
      }
      mean[i] = sa;
      m2[i] = sq;
    }
#define DPA(c) "v_add_f32_dpp %0, %0, %0 " c "\n\tv_add_f32_dpp %1, %1, %1 " c "\n\tv_add_f32_dpp %2, %2, %2 " c "\n\tv_add_f32_dpp %3, %3, %3 " c "\n\t"
    asm volatile("s_nop 3\n\t" DPA("quad_perm:[1,0,3,2] row_mask:0xf bank_mask:0xf") DPA("quad_perm:[2,3,0,1] row_mask:0xf bank_mask:0xf")
                 DPA("row_half_mirror row_mask:0xf bank_mask:0xf") "s_nop 0" : "+v"(mean[0]), "+v"(m2[0]), "+v"(mean[1]), "+v"(m2[1]));
    if ((lane & 7) < 2) {
      const int i = lane & 1;
      const u32x2 sv = {__float_as_uint(i ? mean[1] : mean[0]), __float_as_uint(i ? m2[1] : m2[0])};
      __builtin_amdgcn_raw_buffer_store_b64(sv, rsW, WS_ST + layer * (SEQ * 128) + (unsigned)(((rt * 16 + hp) * 128 + r0 + i * 8) * 8), 0, 16);
    }
  }
  ST(layer * 24 + 17)
  if (layer < 3 && w >= 4) {
    WAITV(0); BAR;
    u64 fl = 0;
    if (lw == 0) { __builtin_amdgcn_s_sleep(7); asm volatile("global_load_dwordx2 %0, %1, %2 sc1" : "+v"(fl) : "v"(po), "s"(ws) : "memory"); }
    glds4v(wq2, vB0, vB1, vB0, vB1, sb + 2 * 40960 + 16384 + lw * 6144);
    glds2v(wq2 + 4096, vB0, vB1, sb + 2 * 40960 + 16384 + lw * 6144 + 4096);
    if (lw == 0) {
      WAITV(6);
      __builtin_amdgcn_sched_barrier(0);
      asm volatile("" : "+v"(fl));
      if (__ballot(fl != E + (unsigned)(layer + 1)) & fm) poll_eq(ws, po, E + (unsigned)(layer + 1), fm, ~0ull);
    }
    BAR;
    glds4c(xa1, vXA, sb + 40960 + lw * 4096); glds4c(xa2, vXA, sb + 2 * 40960 + lw * 4096);
  }
  ST(layer * 24 + 18)
}

__global__ __launch_bounds__(512, 2) void mega_kernel(const u16* __restrict__ x0, const u16* __restrict__ wqkvA,
                                                      const u16* __restrict__ woA, const u16* __restrict__ wqkvB,
                                                      const u16* __restrict__ wphi, const u16* __restrict__ woB,
                                                      u16* __restrict__ out, char* __restrict__ ws) {
  extern __shared__ __attribute__((aligned(16))) char smem[];
  const int w = __builtin_amdgcn_readfirstlane(threadIdx.x >> 6);
  u64 E = 0;
  {
    const int bid = blockIdx.x, lane = threadIdx.x & 63;
    const int rt = 2 * (bid & 4) + ((bid >> 3) & 7), hp = 4 * (bid & 3) + (bid >> 6), m0 = rt * 128;
    if (w < 4) {
      if (w == 3) {
        unsigned long long pc;
        unsigned len;
        asm volatile("s_getpc_b64 %0\n.Lmk_pc:\n\ts_mov_b32 %1, .Lmk_end-.Lmk_pc" : "=s"(pc), "=s"(len));
        const unsigned per = ((len >> 10) + 1) * 128, off = ((bid >> 3) & 7) * per + (lane * 128 < per ? lane * 128 : 0);
        asm volatile("s_mov_b32 m0, %2\n\ts_nop 0\n\tglobal_load_lds_dword %0, %1" :: "v"(off < len - 4 ? off : 0u), "s"(pc), "s"(lds_addr(smem) + LDS_TOTAL - 1024) : "memory", "m0");
      }
      const unsigned xo = (unsigned)((m0 + hp * 8 + w * 2) * (DM * 2) + lane * 16);
      glds4v(x0, xo, xo, xo, xo, lds_addr(smem) + 122880 + w * 4096);
    } else {
      const int lw = w & 3;
      const int kB = 16 * lw + (lane >> 3), fB = (kB >> 1) & 1;
      const unsigned vB0 = (unsigned)((kB * 3072 + hp * 64 + (((((lane & 7) >> 1) ^ fB) << 1) | (lane & 1)) * 8) * 2);
      const unsigned vB1 = (unsigned)(((kB + 8) * 3072 + hp * 64 + (((((lane & 7) >> 1) ^ (fB | 2)) << 1) | (lane & 1)) * 8) * 2) - 1024u;
      const unsigned vA = (unsigned)(((m0 + lw * 32 + (lane >> 3)) * DM + ((lane & 7) ^ (lane >> 3)) * 8) * 2);
#pragma unroll
      for (int s = 0; s < 1; ++s) {
        const int kt = (rt & 8) + s;
        const unsigned sA = lds_addr(smem) + s * 40960, sB = sA + 16384;
        glds4v(x0 + kt * 64, vA, vA + 15360u, vA + 2 * 15360u, vA + 3 * 15360u, sA + lw * 4096);
        const u16* b = wqkvA + (size_t)kt * 64 * 3072;
        glds4v(b, vB0, vB1, vB0, vB1, sB + lw * 6144);
        glds2v(b + 2048, vB0, vB1, sB + lw * 6144 + 4096);
      }
      asm volatile("s_load_dwordx2 %0, %1, 0x0\n\ts_waitcnt lgkmcnt(0)" : "=s"(E) : "s"(ws) : "memory");
    }
  }
  f32x16 xq;
  Next nx;
  asm volatile("" : "=s"(nx.kord), "=s"(nx.wq), "=s"(nx.tw));
#pragma unroll 1
  for (int layer = 0; layer < 4; ++layer) {
    if (layer & 1) g1_phase<1>(smem, ws, E, w, xq, nx, layer, x0, wqkvB, woB, wphi, wqkvA, woA);
    else g1_phase<0>(smem, ws, E, w, xq, nx, layer, x0, wqkvA, woA, nullptr, wqkvB, woB);
    g2_phase(smem, ws, E, w, xq, nx, layer, x0, (layer & 1) ? woB : woA, (layer & 1) ? wqkvA : wqkvB, out);
  }
  asm volatile(".Lmk_end:");
  WAITV(0);
  if (blockIdx.x == 0 && w == 0 && lane_id() == 0) __hip_atomic_store((gu64*)ws, E + 16u, __ATOMIC_RELAXED, __HIP_MEMORY_SCOPE_AGENT);
}

extern "C" void kernel_launch(void* const* d_in, const int* in_sizes, int n_in,
                              void* d_out, int out_size, void* d_ws, size_t ws_size,
                              hipStream_t stream) {
  hipFuncSetAttribute((const void*)mega_kernel, hipFuncAttributeMaxDynamicSharedMemorySize, LDS_TOTAL);
  mega_kernel<<<256, 512, LDS_TOTAL, stream>>>((const u16*)d_in[0], (const u16*)d_in[1], (const u16*)d_in[2], (const u16*)d_in[4],
                                               (const u16*)d_in[5], (const u16*)d_in[6], (u16*)d_out, (char*)d_ws);
}
```

```cpp
#include <hip/hip_runtime.h>
#include <hip/hip_bf16.h>
#include <stdint.h>
#include <type_traits>
#pragma clang diagnostic ignored "-Winline-asm"
#pragma clang diagnostic ignored "-Wunused-value"

typedef unsigned short u16;
typedef __attribute__((ext_vector_type(8))) short bf16x8;
typedef __attribute__((ext_vector_type(4))) short bf16x4;
typedef __attribute__((ext_vector_type(4))) float f32x4;
typedef __attribute__((ext_vector_type(4))) unsigned u32x4;
typedef __attribute__((ext_vector_type(2))) unsigned u32x2;
typedef __attribute__((ext_vector_type(8))) float f32x8;
typedef __attribute__((ext_vector_type(16))) unsigned u32x16;
typedef __attribute__((ext_vector_type(16))) float f32x16;
typedef __attribute__((ext_vector_type(32))) float f32x32;
template <int I, typename T> __device__ __forceinline__ f32x4 quad(const T& v) { return __builtin_shufflevector(v, v, 4 * I, 4 * I + 1, 4 * I + 2, 4 * I + 3); }

constexpr int SEQ = 2048, DM = 1024, NH = 32;
#define ST(i)
constexpr int KVP = 64 * 32 + 64;

typedef __attribute__((ext_vector_type(2))) float f32x2;
typedef __attribute__((ext_vector_type(2))) __bf16 bf16x2_t;
__device__ __forceinline__ unsigned pk2(float a, float b) {
  f32x2 v = {a, b};
  bf16x2_t r = __builtin_convertvector(v, bf16x2_t);
  return __builtin_bit_cast(unsigned, r);
}

__device__ __forceinline__ bf16x4 tr_read(const char* p) {
  return __builtin_amdgcn_ds_read_tr16_b64_v4i16((__attribute__((address_space(3))) bf16x4*)p);
}
template <typename T> __device__ __forceinline__ __attribute__((address_space(3))) T& lds(unsigned a) { return *(__attribute__((address_space(3))) T*)(size_t)a; }
__device__ __forceinline__ bf16x4 tr_read(unsigned a) { return __builtin_amdgcn_ds_read_tr16_b64_v4i16(&lds<bf16x4>(a)); }
__device__ __forceinline__ bf16x8 cat8(bf16x4 a, bf16x4 b) {
  bf16x8 f = {a[0], a[1], a[2], a[3], b[0], b[1], b[2], b[3]};
  return f;
}
__device__ __forceinline__ void glds16s(const void* sbase, unsigned voff, unsigned lds_base) {
  asm volatile("s_mov_b32 m0, %2\n\ts_nop 0\n\tglobal_load_lds_dwordx4 %0, %1" :: "v"(voff), "s"(sbase), "s"(lds_base) : "memory", "m0");
}
__device__ __forceinline__ void glds16c(const void* sbase, unsigned voff, unsigned lds_base) {
  asm volatile("s_mov_b32 m0, %2\n\ts_nop 0\n\tglobal_load_lds_dwordx4 %0, %1 sc1" :: "v"(voff), "s"(sbase), "s"(lds_base) : "memory", "m0");
}
#define GL_ "\n\tglobal_load_lds_dwordx4 "
__device__ __forceinline__ void glds4c(const void* sbase, unsigned v, unsigned lds_base) {
  asm volatile("s_mov_b32 m0, %2\n\ts_nop 0" GL_ "%0, %1 sc1" GL_ "%0, %1 offset:1024 sc1" GL_ "%0, %1 offset:2048 sc1" GL_ "%0, %1 offset:3072 sc1"
               :: "v"(v), "s"(sbase), "s"(lds_base) : "memory", "m0");
}
__device__ __forceinline__ void glds4v(const void* sbase, unsigned v0, unsigned v1, unsigned v2, unsigned v3, unsigned lds_base) {
  asm volatile("s_mov_b32 m0, %5\n\ts_nop 0" GL_ "%0, %4 sc1" GL_ "%1, %4 offset:1024 sc1" GL_ "%2, %4 offset:2048 sc1" GL_ "%3, %4 offset:3072 sc1"
               :: "v"(v0), "v"(v1), "v"(v2), "v"(v3), "s"(sbase), "s"(lds_base) : "memory", "m0");
}
__device__ __forceinline__ void glds2v(const void* sbase, unsigned v0, unsigned v1, unsigned lds_base) {
  asm volatile("s_mov_b32 m0, %3\n\ts_nop 0" GL_ "%0, %2 sc1" GL_ "%1, %2 offset:1024 sc1" :: "v"(v0), "v"(v1), "s"(sbase), "s"(lds_base) : "memory", "m0");
}
__device__ __forceinline__ void gld16(u32x4& d, unsigned voff, const void* sbase) {
  asm volatile("global_load_dwordx4 %0, %1, %2" : "=v"(d) : "v"(voff), "s"(sbase) : "memory");
}
__device__ __forceinline__ unsigned lds_addr(const void* p) { return (unsigned)(size_t)p; }
#define WAITV(n) asm volatile("s_waitcnt vmcnt(" #n ")" ::: "memory")
#define WAITVN(n) asm volatile("s_waitcnt vmcnt(%0)" :: "n"(n) : "memory")
#define WAITL0 do { asm volatile("" ::: "memory"); __builtin_amdgcn_s_waitcnt(0xC07F); asm volatile("" ::: "memory"); } while (0)
#define BAR __builtin_amdgcn_s_barrier()
#define ARRIVED(...) do { WAITV(0); __builtin_amdgcn_sched_barrier(0); asm volatile("" : __VA_ARGS__); } while (0)

template <int N> __device__ __forceinline__ float dpp_ror(float x) {
  return __uint_as_float(__builtin_amdgcn_update_dpp(0u, __float_as_uint(x), 0x120 + N, 0xf, 0xf, false));
}
__device__ __forceinline__ float r16_sum(float x) { x += dpp_ror<8>(x); x += dpp_ror<4>(x); x += dpp_ror<2>(x); return x + dpp_ror<1>(x); }
__device__ __forceinline__ void max8pn(const f32x4& a, const f32x4& b, float& mx, float& mn) {
  asm("v_max3_f32 %0, %2, %3, %4\n\tv_max3_f32 %1, -%2, -%3, -%4\n\tv_max3_f32 %0, %0, %5, %6\n\tv_max3_f32 %1, %1, -%5, -%6\n\t"
      "v_max3_f32 %0, %0, %7, %8\n\tv_max3_f32 %1, %1, -%7, -%8\n\tv_max_f32 %0, %0, %9\n\tv_max_f32 %1, %1, -%9\n\ts_nop 0"
      : "=&v"(mx), "=&v"(mn) : "v"(a[0]), "v"(a[1]), "v"(a[2]), "v"(a[3]), "v"(b[0]), "v"(b[1]), "v"(b[2]), "v"(b[3]));
}
__device__ __forceinline__ float vmaxf_n(float a, float b) { float r; asm("v_max_f32 %0, %1, %2\n\ts_nop 0" : "=v"(r) : "v"(a), "v"(b)); return r; }
template <bool MAXOP> __device__ __forceinline__ float rop(float a, float b) { return MAXOP ? vmaxf_n(a, b) : a + b; }
template <bool MAXOP> __device__ __forceinline__ void xr4(float& a, float& b, float& c, float& d) {
  auto r = __builtin_amdgcn_permlane16_swap(__float_as_uint(a), __float_as_uint(b), false, false);
  auto q = __builtin_amdgcn_permlane16_swap(__float_as_uint(c), __float_as_uint(d), false, false);
  const unsigned ab = __float_as_uint(rop<MAXOP>(__uint_as_float(r[0]), __uint_as_float(r[1])));
  const unsigned cd = __float_as_uint(rop<MAXOP>(__uint_as_float(q[0]), __uint_as_float(q[1])));
  auto t = __builtin_amdgcn_permlane32_swap(ab, cd, false, false);
  const unsigned u = __float_as_uint(rop<MAXOP>(__uint_as_float(t[0]), __uint_as_float(t[1])));
  auto h = __builtin_amdgcn_permlane32_swap(u, u, false, false);
  auto p0 = __builtin_amdgcn_permlane16_swap(h[0], h[0], false, false);
  auto p1 = __builtin_amdgcn_permlane16_swap(h[1], h[1], false, false);
  a = __uint_as_float(p0[0]); b = __uint_as_float(p0[1]); c = __uint_as_float(p1[0]); d = __uint_as_float(p1[1]);
}

__device__ __forceinline__ unsigned xs4(float a, float b, float c, float d) {
  auto r = __builtin_amdgcn_permlane16_swap(__float_as_uint(a), __float_as_uint(b), false, false);
  auto q = __builtin_amdgcn_permlane16_swap(__float_as_uint(c), __float_as_uint(d), false, false);
  auto t = __builtin_amdgcn_permlane32_swap(__float_as_uint(__uint_as_float(r[0]) + __uint_as_float(r[1])), __float_as_uint(__uint_as_float(q[0]) + __uint_as_float(q[1])), false, false);
  return __float_as_uint(__uint_as_float(t[0]) + __uint_as_float(t[1]));
}

__device__ __forceinline__ f32x4 sum4(const u32x4 (&v)[4]) {
  float r0, r1, r2, r3, t0, t1, t2, t3;
  asm("v_add_f32 %0, %8, %12\n\tv_add_f32 %1, %9, %13\n\tv_add_f32 %2, %10, %14\n\tv_add_f32 %3, %11, %15\n\t"
      "v_add_f32 %4, %16, %20\n\tv_add_f32 %5, %17, %21\n\tv_add_f32 %6, %18, %22\n\tv_add_f32 %7, %19, %23\n\t"
      "v_add_f32 %0, %0, %4\n\tv_add_f32 %1, %1, %5\n\tv_add_f32 %2, %2, %6\n\tv_add_f32 %3, %3, %7\n\ts_nop 0"
      : "=&v"(r0), "=&v"(r1), "=&v"(r2), "=&v"(r3), "=&v"(t0), "=&v"(t1), "=&v"(t2), "=&v"(t3)
      : "v"(v[0][0]), "v"(v[0][1]), "v"(v[0][2]), "v"(v[0][3]), "v"(v[1][0]), "v"(v[1][1]), "v"(v[1][2]), "v"(v[1][3]),
        "v"(v[2][0]), "v"(v[2][1]), "v"(v[2][2]), "v"(v[2][3]), "v"(v[3][0]), "v"(v[3][1]), "v"(v[3][2]), "v"(v[3][3]));
  return f32x4{r0, r1, r2, r3};
}
#define MFMA(a, b, c) __builtin_amdgcn_mfma_f32_16x16x32_bf16(a, b, c, 0, 0, 0)

typedef unsigned long long u64;
typedef __attribute__((address_space(1))) unsigned long long gu64;
constexpr unsigned WS_FLAGS = 4096;
constexpr int FLS = 64;
constexpr unsigned WS_G0 = 65536;
constexpr unsigned WS_CSP = WS_G0 + SEQ * 16;
constexpr unsigned WS_ST = WS_CSP + 16 * 16 * 768;
constexpr unsigned WS_X = 1u << 21;
constexpr unsigned WS_O = WS_X + 3u * SEQ * DM * 2;
constexpr unsigned KVPB = KVP * 2, KVFB = KVP * 4;
constexpr unsigned EMPTY = 0xffffffffu;
constexpr unsigned WS_KV = WS_O + 4u * SEQ * DM * 2;
constexpr unsigned KVLB = 16u * NH * KVPB + 16u * KVFB;
constexpr unsigned WS_HL = WS_KV + 2u * KVLB;
constexpr unsigned WS_CSA = WS_HL + 512u * 4096;
constexpr unsigned WS_CPA = WS_CSA + 256u * 1024;
constexpr unsigned WS_IMG = WS_CPA + 16 * 16 * 768;
constexpr unsigned WS_END = WS_IMG + 2048 * 1024;
constexpr int LDS_TOTAL = 163840;
constexpr int OFF_WO = 49152;
struct Next { u64 kord; const char* wq; unsigned tw; };
template <int V> using IC = std::integral_constant<int, V>;
__device__ __forceinline__ bf16x8 ones8() { unsigned e = 0x3F803F80u; asm volatile("" : "+v"(e)); return __builtin_bit_cast(bf16x8, u32x4{e, e, e, e}); }
__device__ __forceinline__ float zero1() { float z; asm volatile("v_mov_b32 %0, 0" : "=v"(z)); return z; }
__device__ __forceinline__ u32x4 empty4() { unsigned e = EMPTY; asm volatile("" : "+v"(e)); return u32x4{e, e, e, e}; }
__device__ __forceinline__ u64 korder(int v, int own) {
  const u64 t = v == 0 ? 0x3b210fedca987654ull : v == 1 ? 0xb37654210fedca98ull : v == 2 ? 0x3ba987654210fedcull : 0xb3fedca987654210ull;
  const u64 inv = v == 0 ? 0xa987e6543210fdcbull : v == 1 ? 0x6543f210dcbae987ull : v == 2 ? 0x3210edcba987f654ull : 0xdcbaf9876543e210ull;
  const u64 low = (1ull << (4 * ((inv >> (4 * own)) & 15))) - 1ull;
  return (u64)own | ((t & low) << 4) | (t & ~((low << 4) | 15ull));
}
__device__ __forceinline__ u64 korder0(int rt) {
  const int r = rt & 7;
  const u64 own = (0x765432765432ull >> (4 * (r < 6 ? r : r - 6))) & 0xffffffull, oth = 0x7654321076543210ull >> (4 * r);
  const u64 t = 0x10ull | ((oth & 15ull) << 8) | (own << 12) | ((oth >> 4) << 36);
  return t | (rt < 8 ? 0x8888888000000800ull : 0x0000000888888088ull);
}
__device__ __forceinline__ int knib(u64 t, int s) { return (int)(t >> (4 * s)) & 15; }
__device__ __forceinline__ unsigned kfirst(u64 t) { return (1u << knib(t, 1)) | (1u << knib(t, 2)) | (1u << knib(t, 3)) | (1u << knib(t, 4)); }
__device__ __forceinline__ int kv1(int bid) { return (bid + (0x4924 >> ((bid >> 2) & 14))) & 3; }

__device__ __forceinline__ bool img(int layer, int hp) { return layer >= 2 && (hp & 7) == 3; }
__device__ __forceinline__ const char* wq_src(const char* ws, const void* W, int layer, int hp) { return img(layer, hp) ? ws + WS_IMG + ((layer & 1) * 768 + (hp >> 3) * 384) * 1024 : (const char*)W; }
__device__ __forceinline__ const char* wo_src(const char* ws, const void* W, int layer, int hp) { return img(layer, hp) ? ws + WS_IMG + (1536 + (layer & 1) * 256 + (hp >> 3) * 128) * 1024 : (const char*)W; }

template <typename T> __device__ __forceinline__ T* uni(T* p) {
  const unsigned long long v = (unsigned long long)p;
  const unsigned lo = __builtin_amdgcn_readfirstlane((unsigned)v), hi = __builtin_amdgcn_readfirstlane((unsigned)(v >> 32));
  return (T*)(((unsigned long long)hi << 32) | lo);
}
__device__ __forceinline__ int lane_id() {
  int l;
  asm volatile("v_mbcnt_lo_u32_b32 %0, -1, 0\n\tv_mbcnt_hi_u32_b32 %0, -1, %0" : "=v"(l));
  __builtin_assume(l >= 0 && l < 64);
  return l;
}
#define IDS \
  const int lane = lane_id(); \
  asm volatile("" : "+s"(w)); \
  const int tid = w * 64 + lane; \
  int bid = blockIdx.x; asm volatile("" : "+s"(bid)); \
  ws = uni(ws); asm volatile("" : "+s"(ws)); \
  layer = __builtin_amdgcn_readfirstlane(layer); x0 = uni(x0); wo = uni(wo); \
    \
  const int rt = 2 * (bid & 4) + ((bid >> 3) & 7); \
  const int hp = 4 * (bid & 3) + (bid >> 6); \
  const int m0 = rt * 128; \
  const int fr = lane & 15, g = lane >> 4; \
  const int q4 = (lane & 15) >> 2, p4 = lane & 3; \
  const __amdgpu_buffer_rsrc_t rsW = __builtin_amdgcn_make_buffer_rsrc((void*)ws, (short)0, (int)WS_END, 0x00020000); \
  (void)m0; (void)fr; (void)q4; (void)p4; (void)rsW; (void)g;

__device__ __forceinline__ void code_touch(unsigned& junk, int bid, int lane) {
  unsigned long long pc;
  unsigned len, d;
  asm volatile("s_getpc_b64 %0\n.Lct%=:\n\ts_mov_b32 %2, .Lct%=-.Lmk_pc\n\ts_mov_b32 %1, .Lmk_end-.Lmk_pc" : "=s"(pc), "=s"(len), "=s"(d));
  pc -= d;
  const unsigned per = ((len >> 10) + 1) * 128, off = ((bid >> 3) & 7) * per + (lane * 128 < per ? lane * 128 : 0);
  asm volatile("global_load_dword %0, %1, %2" : "+v"(junk) : "v"(off < len - 4 ? off : 0u), "s"(pc) : "memory");
}

__device__ __forceinline__ void publish(__amdgpu_buffer_rsrc_t rsW, int tid, unsigned idx, u64 v) {
  unsigned off = WS_FLAGS + idx * FLS;
  asm volatile("" : "+v"(off), "+v"(v));
  WAITV(0);
  WAITL0; BAR;
  if (tid == 192) __builtin_amdgcn_raw_buffer_store_b64(__builtin_bit_cast(u32x2, v), rsW, off, 0, 16);
  asm volatile("" ::: "memory");
}
__device__ __forceinline__ void poll_flags(char* ws, u64 E, int set, int rt, int hp, int lane, unsigned n, bool col) {
  const int l = lane & 15;
  const unsigned fo = WS_FLAGS + (unsigned)(set * 256 + (col ? l * 16 + hp : rt * 16 + l)) * FLS;
  _Pragma("unroll 1") for (unsigned spins = 0; spins < (1u << 18); ++spins) {
    u64 d;
    asm volatile("global_load_dwordx2 %0, %1, %2 sc1\n\ts_waitcnt vmcnt(0)" : "=v"(d) : "v"(fo), "s"(ws) : "memory");
    if (__all(d - (E + n) <= (u64)(16u - n))) break;
    __builtin_amdgcn_s_sleep(1);
  }
}

__device__ __forceinline__ u64 poll_eq(const char* ws, unsigned off, u64 want, u64 mask, u64 now) {
  u64 miss;
  _Pragma("unroll 1") for (unsigned spins = 0; spins < (1u << 18); ++spins) {
    u64 d;
    asm volatile("global_load_dwordx2 %0, %1, %2 sc1\n\ts_waitcnt vmcnt(0)" : "=v"(d) : "v"(off), "s"(ws) : "memory");
    miss = __ballot(d != want) & mask;
    if (!(miss & now)) break;
    __builtin_amdgcn_s_sleep(1);
  }
  return miss;
}

#define G1_MACROS \
  ".macro G1M rb, j, s, z\n" \
  " .set g1d, 160+24*(\\rb)+4*(((\\j)+2)-6*((\\j)>>2))\n .set g1a, 80+40*(\\s)+4*(\\rb)\n .set g1b, 96+40*(\\s)+4*(\\j)\n" \
  " .if ((\\j)>>1) == 1\n  .set g1t, g1a\n  .set g1a, g1b\n  .set g1b, g1t\n .endif\n" \
  " .if \\z\n  v_mfma_f32_16x16x32_bf16 v[g1d:g1d+3], v[g1b:g1b+3], v[g1a:g1a+3], 0\n" \
  " .else\n  v_mfma_f32_16x16x32_bf16 v[g1d:g1d+3], v[g1b:g1b+3], v[g1a:g1a+3], v[g1d:g1d+3]\n .endif\n" \
  ".endm\n" \
  ".macro G1RA rb, s, ra, oa\n" \
  " ds_read_b128 v[80+40*(\\s)+4*(\\rb):83+40*(\\s)+4*(\\rb)], \\ra offset:(\\oa)+(\\rb)*2048\n" \
  ".endm\n" \
  ".macro G1RB k, s, r0, r1, ob\n" \
  " .if (\\k) & 2\n  ds_read_b64_tr_b16 v[96+40*(\\s)+2*(\\k):97+40*(\\s)+2*(\\k)], \\r1 offset:(\\ob)+(((\\k)>>2)+1-3*((\\k)>>3))*2048+((\\k)&1)*512\n" \
  " .else\n  ds_read_b64_tr_b16 v[96+40*(\\s)+2*(\\k):97+40*(\\s)+2*(\\k)], \\r0 offset:(\\ob)+(((\\k)>>2)+1-3*((\\k)>>3))*2048+((\\k)&1)*512\n .endif\n" \
  ".endm\n" \
    \
  ".macro G1BK i, g, r0, r1, ob\n G1RB 4*(\\g)+((\\i)&3), (\\i)>>2, \\r0, \\r1, (\\ob)+((\\i)>>2)*12288\n.endm\n" \
  ".macro G1AK rb, s, a0, a1, oa\n .if \\s\n  G1RA \\rb, 1, \\a1, \\oa\n .else\n  G1RA \\rb, 0, \\a0, \\oa\n .endif\n.endm\n" \
  ".macro G1RDB g, r0, r1, ob\n .set g1i, 0\n .rept 8\n  G1BK g1i, \\g, \\r0, \\r1, \\ob\n  .set g1i, g1i+1\n .endr\n.endm\n" \
  ".macro G1RDA a0, a1, oa\n .set g1i, 0\n .rept 8\n  G1AK (g1i>>1)^2, g1i&1, \\a0, \\a1, \\oa\n  .set g1i, g1i+1\n .endr\n.endm\n" \
    \
  ".macro G1Q g, rb, q, i, z\n G1M \\rb, 2*(\\g)+((((\\i)>>1)^(\\q))&1), (((\\i)+1)>>1)&1, (\\z)&&(((\\i)&1)==0)\n.endm\n" \
    \
  ".macro G1SG g, z, rd, a0, a1, r0, r1, o\n" \
  " .set g1i, 0\n .rept 16\n" \
  "  .if ((\\g) == 0) && (g1i == 8) && (\\rd)\n   s_waitcnt lgkmcnt(7)\n   s_nop 0\n  .endif\n" \
  "  .if ((\\g) == 0) && (g1i == 15)\n   s_waitcnt lgkmcnt(0)\n  .endif\n" \
  "  G1Q \\g, (g1i>>2)^(2-((\\g)&1)), g1i>>2, g1i&3, \\z\n" \
  "  .if \\rd\n" \
  "   .if (\\g) == 0\n    .if g1i < 7\n     G1BK g1i, 2, \\r0, \\r1, \\o\n    .elseif g1i == 8\n     G1BK 7, 2, \\r0, \\r1, \\o\n    .endif\n" \
  "   .elseif (\\g) == 1\n    .if g1i < 8\n     G1BK g1i, 0, \\r0, \\r1, \\o\n    .endif\n" \
  "   .elseif (g1i & 2) == 0\n    G1BK (g1i&1)+2*(g1i>>2), 1, \\r0, \\r1, \\o\n" \
  "   .elseif (\\rd) == 1\n    G1AK (g1i>>2)^2, 1-(g1i&1), \\a0, \\a1, \\o\n   .endif\n" \
  "  .endif\n" \
  "  .set g1i, g1i+1\n .endr\n" \
  ".endm\n" \
  ""
#define G1_SLIDE \
  "G1Q 1, 3, 0, 0, 0\n ds_read2_b64 v[60:63], v49 offset0:32 offset1:48\n ds_read_b128 v[96:99], v48 offset:256\n G1Q 1, 3, 0, 1, 0\n " \
  "ds_read_b128 v[100:103], v48 offset:320\n ds_read2_b32 v[56:57], v52 offset1:16\n G1Q 1, 3, 0, 2, 0\n ds_read_b128 v[136:139], v54 offset:192\n " \
  "ds_read_b128 v[140:143], v54 offset:704\n G1Q 1, 3, 0, 3, 0\n G1Q 1, 2, 1, 0, 0\n G1Q 1, 2, 1, 1, 0\n s_waitcnt lgkmcnt(3)\n " \
  "v_fmac_f32 v240, v96, v62\n G1Q 1, 2, 1, 2, 0\n v_fma_f32 v241, v97, v62, v241\n v_fma_f32 v242, v98, v62, v242\n G1Q 1, 2, 1, 3, 0\n " \
  "v_fma_f32 v243, v99, v62, v243\n v_fma_f32 v244, v100, v62, v244\n G1Q 1, 1, 2, 0, 0\n v_fma_f32 v245, v101, v62, v245\n " \
  "v_fma_f32 v246, v102, v62, v246\n G1Q 1, 1, 2, 1, 0\n v_fma_f32 v247, v103, v62, v247\n v_mul_f32_e64 v240, v63, v240\n G1Q 1, 1, 2, 2, 0\n " \
  "v_mul_f32_e64 v241, v63, v241\n v_mul_f32_e64 v242, v63, v242\n G1Q 1, 1, 2, 3, 0\n v_mul_f32_e64 v243, v63, v243\n v_mul_f32_e64 v244, v63, v244\n " \
  "G1Q 1, 0, 3, 0, 0\n v_mul_f32_e64 v245, v63, v245\n v_mul_f32_e64 v246, v63, v246\n G1Q 1, 0, 3, 1, 0\n v_mul_f32_e64 v247, v63, v247\n " \
  "v_cvt_pk_bf16_f32 v240, v240, v241\n G1Q 1, 0, 3, 2, 0\n v_cvt_pk_bf16_f32 v241, v242, v243\n v_cvt_pk_bf16_f32 v242, v244, v245\n " \
  "G1Q 1, 0, 3, 3, 0\n v_cvt_pk_bf16_f32 v243, v246, v247\n s_waitcnt lgkmcnt(0)\n v_fmac_f32 v248, v136, v56\n G1Q 2, 2, 0, 0, 0\n " \
  "ds_read2_b64 v[144:147], v49 offset1:16\n v_fma_f32 v249, v137, v56, v249\n G1Q 2, 2, 0, 1, 0\n ds_read_b128 v[148:151], v48\n " \
  "v_fma_f32 v250, v138, v56, v250\n G1Q 2, 2, 0, 2, 0\n ds_read_b128 v[104:107], v48 offset:64\n v_fma_f32 v251, v139, v56, v251\n G1Q 2, 2, 0, 3, 0\n " \
  "ds_read_b128 v[108:111], v54 offset:128\n v_fma_f32 v252, v136, v57, v252\n G1Q 2, 3, 1, 0, 0\n v_fma_f32 v253, v137, v57, v253\n " \
  "v_fma_f32 v254, v138, v57, v254\n G1Q 2, 3, 1, 1, 0\n ds_read_b128 v[88:91], v54 offset:640\n v_fma_f32 v255, v139, v57, v255\n G1Q 2, 3, 1, 2, 0\n " \
  "ds_read_b128 v[128:131], v54\n v_mul_f32_e64 v248, v140, v248\n G1Q 2, 3, 1, 3, 0\n v_mul_f32_e64 v249, v141, v249\n v_mul_f32_e64 v250, v142, v250\n " \
  "G1Q 2, 0, 2, 0, 0\n v_mul_f32_e64 v251, v143, v251\n v_mul_f32_e64 v252, v140, v252\n G1Q 2, 0, 2, 1, 0\n ds_read_b128 v[92:95], v54 offset:512\n " \
  "v_mul_f32_e64 v253, v141, v253\n G1Q 2, 0, 2, 2, 0\n v_mul_f32_e64 v254, v142, v254\n v_mul_f32_e64 v255, v143, v255\n G1Q 2, 0, 2, 3, 0\n " \
  "v_cvt_pk_bf16_f32 v248, v248, v249\n v_cvt_pk_bf16_f32 v249, v250, v251\n G1Q 2, 1, 3, 0, 0\n v_cvt_pk_bf16_f32 v255, v254, v255\n " \
  "v_cvt_pk_bf16_f32 v254, v252, v253\n G1Q 2, 1, 3, 1, 0\n v_min_u32_e64 v136, -2, v240\n v_min_u32_e64 v137, -2, v241\n v_min_u32_e64 v138, -2, v242\n " \
  "G1Q 2, 1, 3, 2, 0\n v_min_u32_e64 v139, -2, v243\n v_min_u32_e64 v140, -2, v248\n v_min_u32_e64 v141, -2, v249\n G1Q 2, 1, 3, 3, 0\n " \
  "v_min_u32_e64 v142, -2, v254\n v_min_u32_e64 v143, -2, v255\n ds_read_b128 v[112:115], v54 offset:64\n ds_read_b128 v[116:119], v54 offset:576\n " \
  "s_cmp_eq_u32 %[wm], 0\n s_cbranch_scc1 .Lg1d%=\n buffer_store_dwordx4 v[136:139], v55, %[rs], 0 offen sc1\n " \
  "buffer_store_dwordx4 v[140:143], v55, %[rs], 0 offen offset:1024 sc1\n ds_read_b32 v53, v59\n ds_read_b32 v53, v59\n ds_read_b32 v53, v59\n " \
  "s_branch .Lg1e%=\n .Lg1d%=:\n v_mov_b32 v53, 1\n ds_write_b128 v58, v[136:139]\n ds_write_b128 v58, v[140:143] offset:1024\n ds_write_b32 v59, v53\n " \
  ".Lg1e%=:\n s_waitcnt lgkmcnt(8)\n v_pk_fma_f32 v[232:233], v[148:149], v[62:63], v[232:233] op_sel_hi:[1,0,1]\n " \
  "v_pk_fma_f32 v[234:235], v[150:151], v[62:63], v[234:235] op_sel_hi:[1,0,1]\n " \
  "v_pk_fma_f32 v[236:237], v[104:105], v[62:63], v[236:237] op_sel_hi:[1,0,1]\n " \
  "v_pk_fma_f32 v[238:239], v[106:107], v[62:63], v[238:239] op_sel_hi:[1,0,1]\n " \
  "v_pk_fma_f32 v[216:217], v[96:97], v[60:61], v[216:217] op_sel_hi:[1,0,1]\n " \
  "v_pk_fma_f32 v[218:219], v[98:99], v[60:61], v[218:219] op_sel_hi:[1,0,1]\n " \
  "v_pk_fma_f32 v[220:221], v[100:101], v[60:61], v[220:221] op_sel_hi:[1,0,1]\n " \
  "v_pk_fma_f32 v[222:223], v[102:103], v[60:61], v[222:223] op_sel_hi:[1,0,1]\n v_pk_mul_f32 v[216:217], v[60:61], v[216:217] op_sel:[1,0]\n " \
  "v_pk_mul_f32 v[218:219], v[60:61], v[218:219] op_sel:[1,0]\n v_pk_mul_f32 v[220:221], v[60:61], v[220:221] op_sel:[1,0]\n " \
  "v_pk_mul_f32 v[222:223], v[60:61], v[222:223] op_sel:[1,0]\n v_cvt_pk_bf16_f32 v232, v232, v233\n v_cvt_pk_bf16_f32 v233, v234, v235\n " \
  "v_cvt_pk_bf16_f32 v234, v236, v237\n v_cvt_pk_bf16_f32 v235, v238, v239\n v_cvt_pk_bf16_f32 v216, v216, v217\n v_cvt_pk_bf16_f32 v217, v218, v219\n " \
  "v_cvt_pk_bf16_f32 v218, v220, v221\n v_cvt_pk_bf16_f32 v219, v222, v223\n " \
  "v_pk_fma_f32 v[208:209], v[148:149], v[60:61], v[208:209] op_sel_hi:[1,0,1]\n " \
  "v_pk_fma_f32 v[210:211], v[150:151], v[60:61], v[210:211] op_sel_hi:[1,0,1]\n " \
  "v_pk_fma_f32 v[212:213], v[104:105], v[60:61], v[212:213] op_sel_hi:[1,0,1]\n " \
  "v_pk_fma_f32 v[214:215], v[106:107], v[60:61], v[214:215] op_sel_hi:[1,0,1]\n " \
  "v_pk_fma_f32 v[192:193], v[96:97], v[146:147], v[192:193] op_sel_hi:[1,0,1]\n " \
  "v_pk_fma_f32 v[194:195], v[98:99], v[146:147], v[194:195] op_sel_hi:[1,0,1]\n " \
  "v_pk_fma_f32 v[196:197], v[100:101], v[146:147], v[196:197] op_sel_hi:[1,0,1]\n " \
  "v_pk_fma_f32 v[198:199], v[102:103], v[146:147], v[198:199] op_sel_hi:[1,0,1]\n v_pk_mul_f32 v[192:193], v[146:147], v[192:193] op_sel:[1,0]\n " \
  "v_pk_mul_f32 v[194:195], v[146:147], v[194:195] op_sel:[1,0]\n v_pk_mul_f32 v[196:197], v[146:147], v[196:197] op_sel:[1,0]\n " \
  "v_pk_mul_f32 v[198:199], v[146:147], v[198:199] op_sel:[1,0]\n v_mfma_f32_16x16x32_bf16 v[120:123], v[240:243], v[232:235], v[40:43]\n " \
  "v_mfma_f32_16x16x32_bf16 v[236:239], v[216:219], v[232:235], v[36:39]\n v_cvt_pk_bf16_f32 v208, v208, v209\n v_cvt_pk_bf16_f32 v209, v210, v211\n " \
  "v_cvt_pk_bf16_f32 v210, v212, v213\n v_cvt_pk_bf16_f32 v211, v214, v215\n v_cvt_pk_bf16_f32 v192, v192, v193\n v_cvt_pk_bf16_f32 v193, v194, v195\n " \
  "v_cvt_pk_bf16_f32 v194, v196, v197\n v_cvt_pk_bf16_f32 v195, v198, v199\n " \
  "v_pk_fma_f32 v[184:185], v[148:149], v[146:147], v[184:185] op_sel_hi:[1,0,1]\n " \
  "v_pk_fma_f32 v[186:187], v[150:151], v[146:147], v[186:187] op_sel_hi:[1,0,1]\n " \
  "v_pk_fma_f32 v[188:189], v[104:105], v[146:147], v[188:189] op_sel_hi:[1,0,1]\n " \
  "v_pk_fma_f32 v[190:191], v[106:107], v[146:147], v[190:191] op_sel_hi:[1,0,1]\n " \
  "v_pk_fma_f32 v[168:169], v[96:97], v[144:145], v[168:169] op_sel_hi:[1,0,1]\n " \
  "v_pk_fma_f32 v[170:171], v[98:99], v[144:145], v[170:171] op_sel_hi:[1,0,1]\n " \
  "v_pk_fma_f32 v[172:173], v[100:101], v[144:145], v[172:173] op_sel_hi:[1,0,1]\n " \
  "v_pk_fma_f32 v[174:175], v[102:103], v[144:145], v[174:175] op_sel_hi:[1,0,1]\n v_pk_mul_f32 v[168:169], v[144:145], v[168:169] op_sel:[1,0]\n " \
  "v_pk_mul_f32 v[170:171], v[144:145], v[170:171] op_sel:[1,0]\n v_pk_mul_f32 v[172:173], v[144:145], v[172:173] op_sel:[1,0]\n " \
  "v_pk_mul_f32 v[174:175], v[144:145], v[174:175] op_sel:[1,0]\n v_mfma_f32_16x16x32_bf16 v[220:223], v[216:219], v[208:211], v[40:43]\n " \
  "v_mfma_f32_16x16x32_bf16 v[212:215], v[192:195], v[208:211], v[36:39]\n v_cvt_pk_bf16_f32 v184, v184, v185\n v_cvt_pk_bf16_f32 v185, v186, v187\n " \
  "v_cvt_pk_bf16_f32 v186, v188, v189\n v_cvt_pk_bf16_f32 v187, v190, v191\n v_cvt_pk_bf16_f32 v168, v168, v169\n v_cvt_pk_bf16_f32 v169, v170, v171\n " \
  "v_cvt_pk_bf16_f32 v170, v172, v173\n v_cvt_pk_bf16_f32 v171, v174, v175\n " \
  "v_pk_fma_f32 v[160:161], v[148:149], v[144:145], v[160:161] op_sel_hi:[1,0,1]\n " \
  "v_pk_fma_f32 v[162:163], v[150:151], v[144:145], v[162:163] op_sel_hi:[1,0,1]\n " \
  "v_pk_fma_f32 v[164:165], v[104:105], v[144:145], v[164:165] op_sel_hi:[1,0,1]\n " \
  "v_pk_fma_f32 v[166:167], v[106:107], v[144:145], v[166:167] op_sel_hi:[1,0,1]\n s_waitcnt lgkmcnt(3)\n " \
  "v_pk_fma_f32 v[176:177], v[128:129], v[56:57], v[176:177] op_sel_hi:[1,0,1]\n " \
  "v_pk_fma_f32 v[178:179], v[130:131], v[56:57], v[178:179] op_sel_hi:[1,0,1]\n " \
  "v_pk_fma_f32 v[180:181], v[128:129], v[56:57], v[180:181] op_sel:[0,1,0]\n v_pk_fma_f32 v[182:183], v[130:131], v[56:57], v[182:183] op_sel:[0,1,0]\n " \
  "v_pk_mul_f32 v[176:177], v[176:177], v[92:93]\n v_pk_mul_f32 v[178:179], v[178:179], v[94:95]\n v_pk_mul_f32 v[180:181], v[180:181], v[92:93]\n " \
  "v_pk_mul_f32 v[182:183], v[182:183], v[94:95]\n v_mfma_f32_16x16x32_bf16 v[196:199], v[192:195], v[184:187], v[40:43]\n " \
  "v_mfma_f32_16x16x32_bf16 v[188:191], v[168:171], v[184:187], v[36:39]\n v_cvt_pk_bf16_f32 v160, v160, v161\n v_cvt_pk_bf16_f32 v161, v162, v163\n " \
  "v_cvt_pk_bf16_f32 v162, v164, v165\n v_cvt_pk_bf16_f32 v163, v166, v167\n v_cvt_pk_bf16_f32 v174, v176, v177\n v_cvt_pk_bf16_f32 v175, v178, v179\n " \
  "v_cvt_pk_bf16_f32 v180, v180, v181\n v_cvt_pk_bf16_f32 v181, v182, v183\n " \
  "v_pk_fma_f32 v[224:225], v[108:109], v[56:57], v[224:225] op_sel_hi:[1,0,1]\n " \
  "v_pk_fma_f32 v[226:227], v[110:111], v[56:57], v[226:227] op_sel_hi:[1,0,1]\n " \
  "v_pk_fma_f32 v[228:229], v[108:109], v[56:57], v[228:229] op_sel:[0,1,0]\n v_pk_fma_f32 v[230:231], v[110:111], v[56:57], v[230:231] op_sel:[0,1,0]\n " \
  "v_pk_mul_f32 v[224:225], v[224:225], v[88:89]\n v_pk_mul_f32 v[226:227], v[226:227], v[90:91]\n v_pk_mul_f32 v[228:229], v[228:229], v[88:89]\n " \
  "v_pk_mul_f32 v[230:231], v[230:231], v[90:91]\n v_mfma_f32_16x16x32_bf16 v[124:127], v[168:171], v[160:163], v[40:43]\n " \
  "v_cvt_pk_bf16_f32 v246, v224, v225\n v_cvt_pk_bf16_f32 v247, v226, v227\n v_cvt_pk_bf16_f32 v252, v228, v229\n v_cvt_pk_bf16_f32 v253, v230, v231\n " \
  "v_max3_f32 v140, v236, v237, v238\n v_max3_f32 v138, v212, v213, v214\n v_max3_f32 v141, v239, v120, v121\n v_max3_f32 v139, v215, v220, v221\n " \
  "v_max3_f32 v140, v140, v122, v123\n v_max3_f32 v138, v138, v222, v223\n v_max_f32 v140, v140, v141\n v_max_f32 v138, v138, v139\n " \
  "v_max3_f32 v136, v188, v189, v190\n v_max3_f32 v137, v191, v196, v197\n v_max3_f32 v136, v136, v198, v199\n " \
  "v_pk_fma_f32 v[200:201], v[112:113], v[56:57], v[200:201] op_sel_hi:[1,0,1]\n " \
  "v_pk_fma_f32 v[202:203], v[114:115], v[56:57], v[202:203] op_sel_hi:[1,0,1]\n v_max_f32 v136, v136, v137\n v_mov_b32 v142, v140\n " \
  "v_pk_fma_f32 v[204:205], v[112:113], v[56:57], v[204:205] op_sel:[0,1,0]\n v_pk_fma_f32 v[206:207], v[114:115], v[56:57], v[206:207] op_sel:[0,1,0]\n " \
  "v_permlane16_swap_b32 v136, v138\n v_permlane16_swap_b32 v140, v142\n v_pk_mul_f32 v[200:201], v[200:201], v[116:117]\n " \
  "v_pk_mul_f32 v[202:203], v[202:203], v[118:119]\n v_max_f32 v136, v136, v138\n v_max_f32 v140, v140, v142\n " \
  "v_pk_mul_f32 v[204:205], v[204:205], v[116:117]\n v_pk_mul_f32 v[206:207], v[206:207], v[118:119]\n v_permlane32_swap_b32 v136, v140\n " \
  "v_mul_f32 v152, 0x3e8293ee, v63\n v_mul_f32 v154, 0x3e8293ee, v61\n v_max_f32 v136, v136, v140\n v_mov_b32 v140, v136\n " \
  "v_mul_f32 v156, 0x3e8293ee, v147\n v_mul_f32 v158, 0x3e8293ee, v145\n v_permlane32_swap_b32 v136, v140\n v_mov_b32 v132, 0x3f803f80\n " \
  "v_mov_b32 v133, 0x3f803f80\n v_mov_b32 v138, v136\n v_mov_b32 v134, 0x3f803f80\n v_mov_b32 v135, 0x3f803f80\n v_permlane16_swap_b32 v136, v138\n " \
  "v_cvt_pk_bf16_f32 v244, v200, v201\n v_cvt_pk_bf16_f32 v245, v202, v203\n v_cvt_pk_bf16_f32 v250, v204, v205\n v_cvt_pk_bf16_f32 v251, v206, v207\n " \
  "v_cvt_pk_bf16_f32 v176, v200, v201\n v_cvt_pk_bf16_f32 v177, v202, v203\n v_cvt_pk_bf16_f32 v182, v204, v205\n v_cvt_pk_bf16_f32 v183, v206, v207\n " \
  "v_mul_f32_e64 v153, v152, -v140\n v_mul_f32_e64 v155, v154, -v138\n v_mul_f32_e64 v157, v156, -v136\n " \
  "v_pk_fma_f32 v[236:237], v[236:237], v[152:153], v[152:153] op_sel:[0,0,1] op_sel_hi:[1,0,1]\n " \
  "v_pk_fma_f32 v[238:239], v[238:239], v[152:153], v[152:153] op_sel:[0,0,1] op_sel_hi:[1,0,1]\n " \
  "v_pk_fma_f32 v[120:121], v[120:121], v[152:153], v[152:153] op_sel:[0,0,1] op_sel_hi:[1,0,1]\n " \
  "v_pk_fma_f32 v[122:123], v[122:123], v[152:153], v[152:153] op_sel:[0,0,1] op_sel_hi:[1,0,1]\n v_exp_f32 v236, v236\n v_exp_f32 v237, v237\n " \
  "v_exp_f32 v238, v238\n v_exp_f32 v239, v239\n v_exp_f32 v120, v120\n v_exp_f32 v121, v121\n v_exp_f32 v122, v122\n v_exp_f32 v123, v123\n " \
  "v_pk_fma_f32 v[212:213], v[212:213], v[154:155], v[154:155] op_sel:[0,0,1] op_sel_hi:[1,0,1]\n " \
  "v_pk_fma_f32 v[214:215], v[214:215], v[154:155], v[154:155] op_sel:[0,0,1] op_sel_hi:[1,0,1]\n " \
  "v_pk_fma_f32 v[220:221], v[220:221], v[154:155], v[154:155] op_sel:[0,0,1] op_sel_hi:[1,0,1]\n " \
  "v_pk_fma_f32 v[222:223], v[222:223], v[154:155], v[154:155] op_sel:[0,0,1] op_sel_hi:[1,0,1]\n v_cvt_pk_bf16_f32 v236, v236, v237\n " \
  "v_cvt_pk_bf16_f32 v237, v238, v239\n v_cvt_pk_bf16_f32 v238, v120, v121\n v_cvt_pk_bf16_f32 v239, v122, v123\n v_exp_f32 v212, v212\n " \
  "v_exp_f32 v213, v213\n v_mfma_f32_16x16x32_bf16 v[120:123], v[132:135], v[236:239], 0\n " \
  "v_mfma_f32_16x16x32_bf16 v[232:235], v[246:249], v[236:239], 0\n v_mfma_f32_16x16x32_bf16 v[240:243], v[252:255], v[236:239], 0\n " \
  "v_exp_f32 v214, v214\n v_exp_f32 v215, v215\n v_exp_f32 v220, v220\n v_exp_f32 v221, v221\n v_exp_f32 v222, v222\n v_exp_f32 v223, v223\n " \
  "v_pk_fma_f32 v[188:189], v[188:189], v[156:157], v[156:157] op_sel:[0,0,1] op_sel_hi:[1,0,1]\n " \
  "v_pk_fma_f32 v[190:191], v[190:191], v[156:157], v[156:157] op_sel:[0,0,1] op_sel_hi:[1,0,1]\n " \
  "v_pk_fma_f32 v[196:197], v[196:197], v[156:157], v[156:157] op_sel:[0,0,1] op_sel_hi:[1,0,1]\n " \
  "v_pk_fma_f32 v[198:199], v[198:199], v[156:157], v[156:157] op_sel:[0,0,1] op_sel_hi:[1,0,1]\n v_cvt_pk_bf16_f32 v212, v212, v213\n " \
  "v_cvt_pk_bf16_f32 v213, v214, v215\n v_cvt_pk_bf16_f32 v214, v220, v221\n v_cvt_pk_bf16_f32 v215, v222, v223\n v_exp_f32 v188, v188\n " \
  "s_cmp_eq_u32 %[wm], 0\n s_cbranch_scc1 .Lg1f%=\n ds_read_b32 v53, v59\n ds_read_b128 v[80:83], v58\n ds_read_b64 v[172:173], v58 offset:1024\n " \
  "ds_read_b64 v[178:179], v58 offset:1032\n s_branch .Lg1g%=\n .Lg1f%=:\n buffer_load_dwordx4 v[80:83], v55, %[rs], 0 offen sc1\n " \
  "buffer_load_dwordx2 v[172:173], v55, %[rs], 0 offen offset:1024 sc1\n buffer_load_dwordx2 v[178:179], v55, %[rs], 0 offen offset:1032 sc1\n " \
  ".Lg1g%=:\n v_exp_f32 v189, v189\n v_mfma_f32_16x16x32_bf16 v[220:223], v[132:135], v[212:215], 0\n " \
  "v_mfma_f32_16x16x32_bf16 v[224:227], v[244:247], v[212:215], 0\n v_mfma_f32_16x16x32_bf16 v[228:231], v[250:253], v[212:215], 0\n " \
  "v_exp_f32 v190, v190\n v_exp_f32 v191, v191\n v_exp_f32 v196, v196\n v_exp_f32 v197, v197\n v_exp_f32 v198, v198\n v_exp_f32 v199, v199\n " \
  "v_rcp_f32 v120, v120\n v_cvt_pk_bf16_f32 v188, v188, v189\n v_pk_mul_f32 v[232:233], v[232:233], v[120:121] op_sel_hi:[1,0]\n " \
  "v_pk_mul_f32 v[234:235], v[234:235], v[120:121] op_sel_hi:[1,0]\n v_pk_mul_f32 v[240:241], v[240:241], v[120:121] op_sel_hi:[1,0]\n " \
  "v_pk_mul_f32 v[242:243], v[242:243], v[120:121] op_sel_hi:[1,0]\n v_cvt_pk_bf16_f32 v189, v190, v191\n v_cvt_pk_bf16_f32 v190, v196, v197\n " \
  "v_cvt_pk_bf16_f32 v191, v198, v199\n v_rcp_f32 v220, v220\n v_cvt_pk_bf16_f32 v232, v232, v233\n v_cvt_pk_bf16_f32 v233, v234, v235\n " \
  "v_mfma_f32_16x16x32_bf16 v[196:199], v[132:135], v[188:191], 0\n v_mfma_f32_16x16x32_bf16 v[200:203], v[174:177], v[188:191], 0\n " \
  "v_mfma_f32_16x16x32_bf16 v[204:207], v[180:183], v[188:191], 0\n v_cvt_pk_bf16_f32 v234, v240, v241\n v_cvt_pk_bf16_f32 v235, v242, v243\n " \
  "buffer_store_dwordx4 v[232:235], v51, %[rs], 0 offen offset:3072 sc1\n ds_write_b128 v50, v[232:235] offset:3072\n " \
  "v_pk_mul_f32 v[224:225], v[224:225], v[220:221] op_sel_hi:[1,0]\n v_pk_mul_f32 v[226:227], v[226:227], v[220:221] op_sel_hi:[1,0]\n " \
  "v_pk_mul_f32 v[228:229], v[228:229], v[220:221] op_sel_hi:[1,0]\n v_pk_mul_f32 v[230:231], v[230:231], v[220:221] op_sel_hi:[1,0]\n " \
  "v_cvt_pk_bf16_f32 v224, v224, v225\n v_cvt_pk_bf16_f32 v225, v226, v227\n v_cvt_pk_bf16_f32 v226, v228, v229\n v_cvt_pk_bf16_f32 v227, v230, v231\n " \
  "v_rcp_f32 v196, v196\n buffer_store_dwordx4 v[224:227], v51, %[rs], 0 offen offset:2048 sc1\n ds_write_b128 v50, v[224:227] offset:2048\n " \
  "v_pk_mul_f32 v[200:201], v[200:201], v[196:197] op_sel_hi:[1,0]\n v_pk_mul_f32 v[202:203], v[202:203], v[196:197] op_sel_hi:[1,0]\n " \
  "v_pk_mul_f32 v[204:205], v[204:205], v[196:197] op_sel_hi:[1,0]\n v_pk_mul_f32 v[206:207], v[206:207], v[196:197] op_sel_hi:[1,0]\n " \
  "v_cvt_pk_bf16_f32 v200, v200, v201\n v_cvt_pk_bf16_f32 v201, v202, v203\n v_cvt_pk_bf16_f32 v202, v204, v205\n v_cvt_pk_bf16_f32 v203, v206, v207\n " \
  "buffer_store_dwordx4 v[200:203], v51, %[rs], 0 offen offset:1024 sc1\n ds_write_b128 v50, v[200:203] offset:1024\n s_mov_b32 vcc_hi, 0x40000\n " \
  "s_cmp_eq_u32 %[wm], 0\n s_cbranch_scc1 .Lg1h%=\n s_waitcnt lgkmcnt(3)\n .Lg1i%=:\n v_readfirstlane_b32 vcc_lo, v53\n s_nop 1\n " \
  "s_cmp_lg_u32 vcc_lo, 0\n s_cbranch_scc1 .Lg1k%=\n ds_read_b32 v53, v59\n ds_read_b128 v[80:83], v58\n ds_read_b64 v[172:173], v58 offset:1024\n " \
  "ds_read_b64 v[178:179], v58 offset:1032\n s_waitcnt lgkmcnt(0)\n s_sub_u32 vcc_hi, vcc_hi, 1\n s_cbranch_scc0 .Lg1i%=\n s_branch .Lg1k%=\n .Lg1h%=:\n " \
  "s_mov_b32 m0, 0x40000\n s_waitcnt vmcnt(3)\n .Lg1j%=:\n v_max3_u32 v136, v80, v81, v82\n v_max3_u32 v137, v83, v172, v173\n " \
  "v_max3_u32 v136, v136, v178, v179\n v_max_u32 v136, v136, v137\n v_cmp_eq_u32 vcc, -1, v136\n s_cbranch_vccz .Lg1k%=\n " \
  "buffer_load_dwordx4 v[80:83], v55, %[rs], 0 offen sc1\n buffer_load_dwordx2 v[172:173], v55, %[rs], 0 offen offset:1024 sc1\n " \
  "buffer_load_dwordx2 v[178:179], v55, %[rs], 0 offen offset:1032 sc1\n s_waitcnt vmcnt(0)\n s_sub_u32 m0, m0, 1\n s_cbranch_scc0 .Lg1j%=\n .Lg1k%=:\n " \
  "v_mfma_f32_16x16x32_bf16 v[164:167], v[80:83], v[160:163], v[44:47]\n s_nop 7\n v_max3_f32 v136, v164, v165, v166\n " \
  "v_max3_f32 v137, v167, v124, v125\n v_max3_f32 v136, v136, v126, v127\n v_max_f32 v136, v136, v137\n v_mov_b32 v137, v136\n s_nop 1\n " \
  "v_permlane16_swap_b32 v136, v137\n v_max_f32 v136, v136, v137\n v_mov_b32 v137, v136\n s_nop 1\n v_permlane32_swap_b32 v136, v137\n " \
  "v_max_f32 v136, v136, v137\n v_mul_f32_e64 v159, v158, -v136\n " \
  "v_pk_fma_f32 v[164:165], v[164:165], v[158:159], v[158:159] op_sel:[0,0,1] op_sel_hi:[1,0,1]\n " \
  "v_pk_fma_f32 v[166:167], v[166:167], v[158:159], v[158:159] op_sel:[0,0,1] op_sel_hi:[1,0,1]\n " \
  "v_pk_fma_f32 v[124:125], v[124:125], v[158:159], v[158:159] op_sel:[0,0,1] op_sel_hi:[1,0,1]\n " \
  "v_pk_fma_f32 v[126:127], v[126:127], v[158:159], v[158:159] op_sel:[0,0,1] op_sel_hi:[1,0,1]\n v_exp_f32 v164, v164\n v_exp_f32 v165, v165\n " \
  "v_exp_f32 v166, v166\n v_exp_f32 v167, v167\n v_exp_f32 v124, v124\n v_exp_f32 v125, v125\n v_exp_f32 v126, v126\n v_exp_f32 v127, v127\n " \
  "v_cvt_pk_bf16_f32 v164, v164, v165\n v_cvt_pk_bf16_f32 v165, v166, v167\n v_cvt_pk_bf16_f32 v166, v124, v125\n v_cvt_pk_bf16_f32 v167, v126, v127\n " \
  "s_nop 1\n v_mfma_f32_16x16x32_bf16 v[124:127], v[132:135], v[164:167], 0\n v_mfma_f32_16x16x32_bf16 v[168:171], v[172:175], v[164:167], 0\n " \
  "v_mfma_f32_16x16x32_bf16 v[84:87], v[178:181], v[164:167], 0\n s_nop 5\n v_rcp_f32 v124, v124\n s_nop 0\n " \
  "v_pk_mul_f32 v[168:169], v[168:169], v[124:125] op_sel_hi:[1,0]\n v_pk_mul_f32 v[170:171], v[170:171], v[124:125] op_sel_hi:[1,0]\n " \
  "v_pk_mul_f32 v[84:85], v[84:85], v[124:125] op_sel_hi:[1,0]\n v_pk_mul_f32 v[86:87], v[86:87], v[124:125] op_sel_hi:[1,0]\n " \
  "v_cvt_pk_bf16_f32 v168, v168, v169\n v_cvt_pk_bf16_f32 v169, v170, v171\n v_cvt_pk_bf16_f32 v170, v84, v85\n v_cvt_pk_bf16_f32 v171, v86, v87\n " \
  "buffer_store_dwordx4 v[168:171], v51, %[rs], 0 offen offset:0 sc1\n ds_write_b128 v50, v[168:171] offset:0\n "
#define G1_HEDGE \
  "G1Q 1, 1, 0, 0, 0\n ds_read2_b64 v[140:143], v49 offset1:16\n ds_read_b128 v[96:99], v48 offset:256\n G1Q 1, 1, 0, 1, 0\n " \
  "ds_read_b128 v[100:103], v48 offset:320\n ds_read2_b64 v[136:139], v49 offset0:32 offset1:48\n G1Q 1, 1, 0, 2, 0\n ds_read_b64_tr_b16 v[36:37], v53\n " \
  "ds_read_b64_tr_b16 v[38:39], v53 offset:1024\n G1Q 1, 1, 0, 3, 0\n ds_read_b64_tr_b16 v[40:41], v53 offset:32\n " \
  "ds_read_b64_tr_b16 v[42:43], v53 offset:1056\n G1Q 1, 0, 1, 0, 0\n G1Q 1, 0, 1, 1, 0\n s_waitcnt lgkmcnt(4)\n v_fmac_f32 v168, v96, v140\n " \
  "G1Q 1, 0, 1, 2, 0\n v_fma_f32 v169, v97, v140, v169\n v_fma_f32 v170, v98, v140, v170\n G1Q 1, 0, 1, 3, 0\n v_fma_f32 v171, v99, v140, v171\n " \
  "v_fma_f32 v172, v100, v140, v172\n G1Q 1, 3, 2, 0, 0\n v_fma_f32 v173, v101, v140, v173\n v_fma_f32 v174, v102, v140, v174\n G1Q 1, 3, 2, 1, 0\n " \
  "v_fma_f32 v175, v103, v140, v175\n v_cvt_pk_bf16_f32 v168, v168, v169\n G1Q 1, 3, 2, 2, 0\n v_cvt_pk_bf16_f32 v169, v170, v171\n " \
  "v_cvt_pk_bf16_f32 v170, v172, v173\n G1Q 1, 3, 2, 3, 0\n v_cvt_pk_bf16_f32 v171, v174, v175\n v_fma_f32 v192, v96, v142, v192\n G1Q 1, 2, 3, 0, 0\n " \
  "v_fma_f32 v193, v97, v142, v193\n v_fma_f32 v194, v98, v142, v194\n G1Q 1, 2, 3, 1, 0\n v_fma_f32 v195, v99, v142, v195\n " \
  "v_fma_f32 v196, v100, v142, v196\n G1Q 1, 2, 3, 2, 0\n v_fma_f32 v197, v101, v142, v197\n v_fma_f32 v198, v102, v142, v198\n G1Q 1, 2, 3, 3, 0\n " \
  "v_fma_f32 v199, v103, v142, v199\n v_cvt_pk_bf16_f32 v192, v192, v193\n G1Q 2, 2, 0, 0, 0\n ds_read2_b32 v[144:145], v52 offset1:16\n " \
  "v_cvt_pk_bf16_f32 v193, v194, v195\n G1Q 2, 2, 0, 1, 0\n ds_read_b128 v[148:151], v48\n v_cvt_pk_bf16_f32 v194, v196, v197\n G1Q 2, 2, 0, 2, 0\n " \
  "v_cvt_pk_bf16_f32 v195, v198, v199\n v_fma_f32 v216, v96, v136, v216\n G1Q 2, 2, 0, 3, 0\n v_fma_f32 v217, v97, v136, v217\n " \
  "v_fma_f32 v218, v98, v136, v218\n G1Q 2, 3, 1, 0, 0\n v_fma_f32 v219, v99, v136, v219\n v_fma_f32 v220, v100, v136, v220\n G1Q 2, 3, 1, 1, 0\n " \
  "ds_read_b128 v[88:91], v48 offset:64\n v_fma_f32 v221, v101, v136, v221\n v_fma_f32 v222, v102, v136, v222\n G1Q 2, 3, 1, 2, 0\n " \
  "ds_read_b128 v[128:131], v54\n v_fma_f32 v223, v103, v136, v223\n v_cvt_pk_bf16_f32 v216, v216, v217\n G1Q 2, 3, 1, 3, 0\n " \
  "v_cvt_pk_bf16_f32 v217, v218, v219\n v_cvt_pk_bf16_f32 v218, v220, v221\n G1Q 2, 0, 2, 0, 0\n v_cvt_pk_bf16_f32 v219, v222, v223\n " \
  "v_fma_f32 v240, v96, v138, v240\n G1Q 2, 0, 2, 1, 0\n ds_read_b128 v[92:95], v54 offset:512\n v_fma_f32 v241, v97, v138, v241\n " \
  "v_fma_f32 v242, v98, v138, v242\n G1Q 2, 0, 2, 2, 0\n ds_read_b128 v[132:135], v54 offset:64\n v_fma_f32 v243, v99, v138, v243\n " \
  "v_fma_f32 v244, v100, v138, v244\n G1Q 2, 0, 2, 3, 0\n v_fma_f32 v245, v101, v138, v245\n v_fma_f32 v246, v102, v138, v246\n G1Q 2, 1, 3, 0, 0\n " \
  "v_fma_f32 v247, v103, v138, v247\n v_cvt_pk_bf16_f32 v240, v240, v241\n G1Q 2, 1, 3, 1, 0\n v_cvt_pk_bf16_f32 v241, v242, v243\n " \
  "v_cvt_pk_bf16_f32 v242, v244, v245\n G1Q 2, 1, 3, 2, 0\n v_cvt_pk_bf16_f32 v243, v246, v247\n G1Q 2, 1, 3, 3, 0\n s_waitcnt lgkmcnt(6)\n " \
  "v_mfma_f32_16x16x32_bf16 v[172:175], v[36:39], v[168:171], 0\n ds_read_b128 v[112:115], v54 offset:576\n ds_read_b128 v[116:119], v54 offset:128\n " \
  "v_mfma_f32_16x16x32_bf16 v[80:83], v[40:43], v[168:171], 0\n ds_read_b128 v[152:155], v54 offset:640\n ds_read_b128 v[156:159], v54 offset:192\n " \
  "v_mfma_f32_16x16x32_bf16 v[196:199], v[36:39], v[192:195], 0\n ds_read_b128 v[120:123], v54 offset:704\n v_mul_f32 v44, 0x3fb8aa3b, v141\n " \
  "v_mfma_f32_16x16x32_bf16 v[84:87], v[40:43], v[192:195], 0\n v_mul_f32 v46, 0x3fb8aa3b, v143\n v_mul_f32 v104, 0x3fb8aa3b, v137\n " \
  "v_mfma_f32_16x16x32_bf16 v[220:223], v[36:39], v[216:219], 0\n v_mul_f32 v106, 0x3fb8aa3b, v139\n " \
  "v_mfma_f32_16x16x32_bf16 v[124:127], v[40:43], v[216:219], 0\n v_mfma_f32_16x16x32_bf16 v[244:247], v[36:39], v[240:243], 0\n " \
  "v_mfma_f32_16x16x32_bf16 v[108:111], v[40:43], v[240:243], 0\n v_max3_f32 v168, v172, v173, v174\n v_max3_f32 v169, -v172, -v173, -v174\n " \
  "v_max3_f32 v168, v168, v175, v80\n v_max3_f32 v169, v169, -v175, -v80\n v_max3_f32 v168, v168, v81, v82\n v_max3_f32 v169, v169, -v81, -v82\n " \
  "v_max_f32_e64 v168, v168, v83\n v_max_f32_e64 v169, v169, -v83\n v_max3_f32 v170, v196, v197, v198\n v_max3_f32 v171, -v196, -v197, -v198\n " \
  "v_max3_f32 v170, v170, v199, v84\n v_max3_f32 v171, v171, -v199, -v84\n v_max3_f32 v170, v170, v85, v86\n v_max3_f32 v171, v171, -v85, -v86\n " \
  "v_max_f32_e64 v170, v170, v87\n v_max_f32_e64 v171, v171, -v87\n s_waitcnt lgkmcnt(0)\n " \
  "v_pk_fma_f32 v[176:177], v[128:129], v[144:145], v[176:177] op_sel_hi:[1,0,1]\n " \
  "v_pk_fma_f32 v[178:179], v[130:131], v[144:145], v[178:179] op_sel_hi:[1,0,1]\n " \
  "v_pk_fma_f32 v[180:181], v[128:129], v[144:145], v[180:181] op_sel:[0,1,0]\n " \
  "v_pk_fma_f32 v[182:183], v[130:131], v[144:145], v[182:183] op_sel:[0,1,0]\n v_pk_mul_f32 v[176:177], v[176:177], v[92:93]\n " \
  "v_pk_mul_f32 v[178:179], v[178:179], v[94:95]\n v_pk_mul_f32 v[180:181], v[180:181], v[92:93]\n v_pk_mul_f32 v[182:183], v[182:183], v[94:95]\n " \
  "v_pk_fma_f32 v[200:201], v[132:133], v[144:145], v[200:201] op_sel_hi:[1,0,1]\n " \
  "v_pk_fma_f32 v[202:203], v[134:135], v[144:145], v[202:203] op_sel_hi:[1,0,1]\n " \
  "v_pk_fma_f32 v[204:205], v[132:133], v[144:145], v[204:205] op_sel:[0,1,0]\n " \
  "v_pk_fma_f32 v[206:207], v[134:135], v[144:145], v[206:207] op_sel:[0,1,0]\n v_pk_mul_f32 v[200:201], v[200:201], v[112:113]\n " \
  "v_pk_mul_f32 v[202:203], v[202:203], v[114:115]\n v_pk_mul_f32 v[204:205], v[204:205], v[112:113]\n v_pk_mul_f32 v[206:207], v[206:207], v[114:115]\n " \
  "v_max3_f32 v192, v220, v221, v222\n v_max3_f32 v193, -v220, -v221, -v222\n v_max3_f32 v192, v192, v223, v124\n v_max3_f32 v193, v193, -v223, -v124\n " \
  "v_max3_f32 v192, v192, v125, v126\n v_max3_f32 v193, v193, -v125, -v126\n v_max_f32_e64 v192, v192, v127\n v_max_f32_e64 v193, v193, -v127\n " \
  "v_max3_f32 v194, v244, v245, v246\n v_max3_f32 v195, -v244, -v245, -v246\n v_max3_f32 v194, v194, v247, v108\n v_max3_f32 v195, v195, -v247, -v108\n " \
  "v_max3_f32 v194, v194, v109, v110\n v_max3_f32 v195, v195, -v109, -v110\n v_max_f32_e64 v194, v194, v111\n v_max_f32_e64 v195, v195, -v111\n " \
  "v_permlane16_swap_b32 v168, v169\n v_permlane16_swap_b32 v170, v171\n v_permlane16_swap_b32 v192, v193\n v_permlane16_swap_b32 v194, v195\n " \
  "v_pk_fma_f32 v[224:225], v[116:117], v[144:145], v[224:225] op_sel_hi:[1,0,1]\n " \
  "v_pk_fma_f32 v[226:227], v[118:119], v[144:145], v[226:227] op_sel_hi:[1,0,1]\n v_max_f32 v168, v168, v169\n v_max_f32 v170, v170, v171\n " \
  "v_max_f32 v192, v192, v193\n v_max_f32 v194, v194, v195\n v_pk_fma_f32 v[228:229], v[116:117], v[144:145], v[228:229] op_sel:[0,1,0]\n " \
  "v_pk_fma_f32 v[230:231], v[118:119], v[144:145], v[230:231] op_sel:[0,1,0]\n v_permlane32_swap_b32 v168, v170\n v_permlane32_swap_b32 v192, v194\n " \
  "v_pk_mul_f32 v[224:225], v[224:225], v[152:153]\n v_pk_mul_f32 v[226:227], v[226:227], v[154:155]\n v_max_f32 v168, v168, v170\n " \
  "v_max_f32 v192, v192, v194\n v_pk_mul_f32 v[228:229], v[228:229], v[152:153]\n v_pk_mul_f32 v[230:231], v[230:231], v[154:155]\n " \
  "v_mov_b32 v170, v168\n v_mov_b32 v194, v192\n v_pk_fma_f32 v[248:249], v[156:157], v[144:145], v[248:249] op_sel_hi:[1,0,1]\n " \
  "v_pk_fma_f32 v[250:251], v[158:159], v[144:145], v[250:251] op_sel_hi:[1,0,1]\n v_permlane32_swap_b32 v168, v170\n v_permlane32_swap_b32 v192, v194\n " \
  "v_pk_fma_f32 v[252:253], v[156:157], v[144:145], v[252:253] op_sel:[0,1,0]\n " \
  "v_pk_fma_f32 v[254:255], v[158:159], v[144:145], v[254:255] op_sel:[0,1,0]\n v_mov_b32 v169, v168\n v_mov_b32 v171, v170\n v_mov_b32 v193, v192\n " \
  "v_mov_b32 v195, v194\n v_pk_mul_f32 v[248:249], v[248:249], v[120:121]\n v_pk_mul_f32 v[250:251], v[250:251], v[122:123]\n " \
  "v_permlane16_swap_b32 v168, v169\n v_permlane16_swap_b32 v170, v171\n v_permlane16_swap_b32 v192, v193\n v_permlane16_swap_b32 v194, v195\n " \
  "v_pk_mul_f32 v[252:253], v[252:253], v[120:121]\n v_pk_mul_f32 v[254:255], v[254:255], v[122:123]\n v_cvt_pk_bf16_f32 v176, v176, v177\n " \
  "v_cvt_pk_bf16_f32 v177, v178, v179\n v_cvt_pk_bf16_f32 v178, v200, v201\n v_cvt_pk_bf16_f32 v179, v202, v203\n v_cvt_pk_bf16_f32 v180, v180, v181\n " \
  "v_cvt_pk_bf16_f32 v181, v182, v183\n v_cvt_pk_bf16_f32 v182, v204, v205\n v_cvt_pk_bf16_f32 v183, v206, v207\n v_cvt_pk_bf16_f32 v224, v224, v225\n " \
  "v_cvt_pk_bf16_f32 v225, v226, v227\n v_cvt_pk_bf16_f32 v226, v248, v249\n v_cvt_pk_bf16_f32 v227, v250, v251\n v_cvt_pk_bf16_f32 v228, v228, v229\n " \
  "v_cvt_pk_bf16_f32 v229, v230, v231\n v_cvt_pk_bf16_f32 v230, v252, v253\n v_cvt_pk_bf16_f32 v231, v254, v255\n v_mul_f32_e64 v45, v44, -v168\n " \
  "v_mul_f32_e64 v146, v44, -v169\n v_mul_f32_e64 v47, v46, -v170\n v_mul_f32_e64 v147, v46, -v171\n v_mul_f32_e64 v105, v104, -v192\n " \
  "v_mul_f32_e64 v144, v104, -v193\n v_mul_f32_e64 v107, v106, -v194\n v_mul_f32_e64 v145, v106, -v195\n " \
  "v_pk_fma_f32 v[200:201], v[172:173], v[44:45], v[146:147] op_sel_hi:[1,0,0] neg_lo:[1,0,0] neg_hi:[1,0,0]\n " \
  "v_pk_fma_f32 v[202:203], v[174:175], v[44:45], v[146:147] op_sel_hi:[1,0,0] neg_lo:[1,0,0] neg_hi:[1,0,0]\n " \
  "v_pk_fma_f32 v[204:205], v[80:81], v[44:45], v[146:147] op_sel_hi:[1,0,0] neg_lo:[1,0,0] neg_hi:[1,0,0]\n " \
  "v_pk_fma_f32 v[206:207], v[82:83], v[44:45], v[146:147] op_sel_hi:[1,0,0] neg_lo:[1,0,0] neg_hi:[1,0,0]\n " \
  "v_pk_fma_f32 v[172:173], v[172:173], v[44:45], v[44:45] op_sel:[0,0,1] op_sel_hi:[1,0,1]\n " \
  "v_pk_fma_f32 v[174:175], v[174:175], v[44:45], v[44:45] op_sel:[0,0,1] op_sel_hi:[1,0,1]\n " \
  "v_pk_fma_f32 v[80:81], v[80:81], v[44:45], v[44:45] op_sel:[0,0,1] op_sel_hi:[1,0,1]\n " \
  "v_pk_fma_f32 v[82:83], v[82:83], v[44:45], v[44:45] op_sel:[0,0,1] op_sel_hi:[1,0,1]\n ds_write_b128 v55, v[176:179] offset:0\n " \
  "v_exp_f32 v200, v200\n v_exp_f32 v201, v201\n v_exp_f32 v202, v202\n v_exp_f32 v203, v203\n v_exp_f32 v204, v204\n v_exp_f32 v205, v205\n " \
  "v_exp_f32 v206, v206\n v_exp_f32 v207, v207\n v_exp_f32 v172, v172\n v_exp_f32 v173, v173\n v_exp_f32 v174, v174\n v_exp_f32 v175, v175\n " \
  "v_exp_f32 v80, v80\n v_exp_f32 v81, v81\n v_exp_f32 v82, v82\n v_exp_f32 v83, v83\n " \
  "v_pk_fma_f32 v[248:249], v[196:197], v[46:47], v[146:147] op_sel:[0,0,1] op_sel_hi:[1,0,1] neg_lo:[1,0,0] neg_hi:[1,0,0]\n " \
  "v_pk_fma_f32 v[250:251], v[198:199], v[46:47], v[146:147] op_sel:[0,0,1] op_sel_hi:[1,0,1] neg_lo:[1,0,0] neg_hi:[1,0,0]\n " \
  "v_pk_fma_f32 v[252:253], v[84:85], v[46:47], v[146:147] op_sel:[0,0,1] op_sel_hi:[1,0,1] neg_lo:[1,0,0] neg_hi:[1,0,0]\n " \
  "v_pk_fma_f32 v[254:255], v[86:87], v[46:47], v[146:147] op_sel:[0,0,1] op_sel_hi:[1,0,1] neg_lo:[1,0,0] neg_hi:[1,0,0]\n " \
  "v_pk_fma_f32 v[196:197], v[196:197], v[46:47], v[46:47] op_sel:[0,0,1] op_sel_hi:[1,0,1]\n " \
  "v_pk_fma_f32 v[198:199], v[198:199], v[46:47], v[46:47] op_sel:[0,0,1] op_sel_hi:[1,0,1]\n " \
  "v_pk_fma_f32 v[84:85], v[84:85], v[46:47], v[46:47] op_sel:[0,0,1] op_sel_hi:[1,0,1]\n " \
  "v_pk_fma_f32 v[86:87], v[86:87], v[46:47], v[46:47] op_sel:[0,0,1] op_sel_hi:[1,0,1]\n ds_write_b128 v55, v[180:183] offset:1024\n " \
  "v_exp_f32 v248, v248\n v_exp_f32 v249, v249\n v_exp_f32 v250, v250\n v_exp_f32 v251, v251\n v_exp_f32 v252, v252\n v_exp_f32 v253, v253\n " \
  "v_exp_f32 v254, v254\n v_exp_f32 v255, v255\n v_exp_f32 v196, v196\n v_exp_f32 v197, v197\n v_exp_f32 v198, v198\n v_exp_f32 v199, v199\n " \
  "v_exp_f32 v84, v84\n v_exp_f32 v85, v85\n v_exp_f32 v86, v86\n v_exp_f32 v87, v87\n " \
  "v_pk_fma_f32 v[128:129], v[220:221], v[104:105], v[144:145] op_sel_hi:[1,0,0] neg_lo:[1,0,0] neg_hi:[1,0,0]\n " \
  "v_pk_fma_f32 v[130:131], v[222:223], v[104:105], v[144:145] op_sel_hi:[1,0,0] neg_lo:[1,0,0] neg_hi:[1,0,0]\n " \
  "v_pk_fma_f32 v[132:133], v[124:125], v[104:105], v[144:145] op_sel_hi:[1,0,0] neg_lo:[1,0,0] neg_hi:[1,0,0]\n " \
  "v_pk_fma_f32 v[134:135], v[126:127], v[104:105], v[144:145] op_sel_hi:[1,0,0] neg_lo:[1,0,0] neg_hi:[1,0,0]\n " \
  "v_pk_fma_f32 v[220:221], v[220:221], v[104:105], v[104:105] op_sel:[0,0,1] op_sel_hi:[1,0,1]\n " \
  "v_pk_fma_f32 v[222:223], v[222:223], v[104:105], v[104:105] op_sel:[0,0,1] op_sel_hi:[1,0,1]\n " \
  "v_pk_fma_f32 v[124:125], v[124:125], v[104:105], v[104:105] op_sel:[0,0,1] op_sel_hi:[1,0,1]\n " \
  "v_pk_fma_f32 v[126:127], v[126:127], v[104:105], v[104:105] op_sel:[0,0,1] op_sel_hi:[1,0,1]\n ds_write_b128 v55, v[224:227] offset:8192\n " \
  "v_pk_add_f32 v[216:217], v[172:173], v[174:175]\n v_pk_add_f32 v[218:219], v[80:81], v[82:83]\n v_pk_add_f32 v[216:217], v[216:217], v[218:219]\n " \
  "v_add_f32 v168, v216, v217\n v_pk_add_f32 v[216:217], v[200:201], v[202:203]\n v_pk_add_f32 v[218:219], v[204:205], v[206:207]\n " \
  "v_pk_add_f32 v[216:217], v[216:217], v[218:219]\n v_add_f32 v169, v216, v217\n v_pk_add_f32 v[216:217], v[196:197], v[198:199]\n " \
  "v_pk_add_f32 v[218:219], v[84:85], v[86:87]\n v_pk_add_f32 v[216:217], v[216:217], v[218:219]\n v_add_f32 v170, v216, v217\n " \
  "v_pk_add_f32 v[216:217], v[248:249], v[250:251]\n v_pk_add_f32 v[218:219], v[252:253], v[254:255]\n v_pk_add_f32 v[216:217], v[216:217], v[218:219]\n " \
  "v_add_f32 v171, v216, v217\n v_exp_f32 v128, v128\n v_exp_f32 v129, v129\n v_permlane16_swap_b32 v168, v169\n v_permlane16_swap_b32 v170, v171\n " \
  "v_exp_f32 v130, v130\n v_exp_f32 v131, v131\n v_add_f32 v168, v168, v169\n v_add_f32 v170, v170, v171\n v_exp_f32 v132, v132\n v_exp_f32 v133, v133\n " \
  "v_permlane32_swap_b32 v168, v170\n v_exp_f32 v134, v134\n v_exp_f32 v135, v135\n v_add_f32 v168, v168, v170\n v_exp_f32 v220, v220\n " \
  "v_exp_f32 v221, v221\n v_rcp_f32 v168, v168\n v_exp_f32 v222, v222\n v_exp_f32 v223, v223\n v_mov_b32 v170, v168\n v_exp_f32 v124, v124\n " \
  "v_exp_f32 v125, v125\n v_permlane32_swap_b32 v168, v170\n v_exp_f32 v126, v126\n v_exp_f32 v127, v127\n v_mov_b32 v169, v168\n v_mov_b32 v171, v170\n " \
  "v_pk_fma_f32 v[112:113], v[244:245], v[106:107], v[144:145] op_sel:[0,0,1] op_sel_hi:[1,0,1] neg_lo:[1,0,0] neg_hi:[1,0,0]\n " \
  "v_pk_fma_f32 v[114:115], v[246:247], v[106:107], v[144:145] op_sel:[0,0,1] op_sel_hi:[1,0,1] neg_lo:[1,0,0] neg_hi:[1,0,0]\n " \
  "v_permlane16_swap_b32 v168, v169\n v_permlane16_swap_b32 v170, v171\n " \
  "v_pk_fma_f32 v[116:117], v[108:109], v[106:107], v[144:145] op_sel:[0,0,1] op_sel_hi:[1,0,1] neg_lo:[1,0,0] neg_hi:[1,0,0]\n " \
  "v_pk_fma_f32 v[118:119], v[110:111], v[106:107], v[144:145] op_sel:[0,0,1] op_sel_hi:[1,0,1] neg_lo:[1,0,0] neg_hi:[1,0,0]\n " \
  "v_pk_fma_f32 v[244:245], v[244:245], v[106:107], v[106:107] op_sel:[0,0,1] op_sel_hi:[1,0,1]\n " \
  "v_pk_fma_f32 v[246:247], v[246:247], v[106:107], v[106:107] op_sel:[0,0,1] op_sel_hi:[1,0,1]\n " \
  "v_pk_fma_f32 v[108:109], v[108:109], v[106:107], v[106:107] op_sel:[0,0,1] op_sel_hi:[1,0,1]\n " \
  "v_pk_fma_f32 v[110:111], v[110:111], v[106:107], v[106:107] op_sel:[0,0,1] op_sel_hi:[1,0,1]\n ds_write_b128 v55, v[228:231] offset:9216\n " \
  "v_exp_f32 v112, v112\n v_pk_mul_f32 v[172:173], v[172:173], v[168:169] op_sel_hi:[1,0]\n v_exp_f32 v113, v113\n " \
  "v_pk_mul_f32 v[174:175], v[174:175], v[168:169] op_sel_hi:[1,0]\n v_exp_f32 v114, v114\n v_cvt_pk_bf16_f32 v172, v172, v173\n v_exp_f32 v115, v115\n " \
  "v_cvt_pk_bf16_f32 v173, v174, v175\n v_exp_f32 v116, v116\n ds_write_b64 v58, v[172:173] offset:0\n v_exp_f32 v117, v117\n " \
  "v_pk_mul_f32 v[80:81], v[80:81], v[168:169] op_sel_hi:[1,0]\n v_exp_f32 v118, v118\n v_pk_mul_f32 v[82:83], v[82:83], v[168:169] op_sel_hi:[1,0]\n " \
  "v_exp_f32 v119, v119\n v_cvt_pk_bf16_f32 v80, v80, v81\n v_exp_f32 v244, v244\n v_cvt_pk_bf16_f32 v81, v82, v83\n v_exp_f32 v245, v245\n " \
  "ds_write_b64 v58, v[80:81] offset:32\n v_exp_f32 v246, v246\n v_pk_mul_f32 v[200:201], v[200:201], v[168:169] op_sel:[0,1]\n v_exp_f32 v247, v247\n " \
  "v_pk_mul_f32 v[202:203], v[202:203], v[168:169] op_sel:[0,1]\n v_exp_f32 v108, v108\n v_cvt_pk_bf16_f32 v200, v200, v201\n v_exp_f32 v109, v109\n " \
  "v_cvt_pk_bf16_f32 v201, v202, v203\n v_exp_f32 v110, v110\n ds_write_b64 v58, v[200:201] offset:64\n v_exp_f32 v111, v111\n " \
  "v_pk_mul_f32 v[204:205], v[204:205], v[168:169] op_sel:[0,1]\n v_pk_mul_f32 v[206:207], v[206:207], v[168:169] op_sel:[0,1]\n " \
  "v_cvt_pk_bf16_f32 v204, v204, v205\n v_cvt_pk_bf16_f32 v205, v206, v207\n ds_write_b64 v58, v[204:205] offset:96\n " \
  "v_pk_add_f32 v[216:217], v[220:221], v[222:223]\n v_pk_add_f32 v[218:219], v[124:125], v[126:127]\n v_pk_add_f32 v[216:217], v[216:217], v[218:219]\n " \
  "v_add_f32 v192, v216, v217\n v_pk_add_f32 v[216:217], v[128:129], v[130:131]\n v_pk_add_f32 v[218:219], v[132:133], v[134:135]\n " \
  "v_pk_add_f32 v[216:217], v[216:217], v[218:219]\n v_add_f32 v193, v216, v217\n v_pk_add_f32 v[216:217], v[244:245], v[246:247]\n " \
  "v_pk_add_f32 v[218:219], v[108:109], v[110:111]\n v_pk_add_f32 v[216:217], v[216:217], v[218:219]\n v_add_f32 v194, v216, v217\n " \
  "v_pk_add_f32 v[216:217], v[112:113], v[114:115]\n v_pk_add_f32 v[218:219], v[116:117], v[118:119]\n v_pk_add_f32 v[216:217], v[216:217], v[218:219]\n " \
  "v_add_f32 v195, v216, v217\n v_pk_mul_f32 v[196:197], v[196:197], v[170:171] op_sel_hi:[1,0]\n " \
  "v_pk_mul_f32 v[198:199], v[198:199], v[170:171] op_sel_hi:[1,0]\n v_permlane16_swap_b32 v192, v193\n v_permlane16_swap_b32 v194, v195\n " \
  "v_cvt_pk_bf16_f32 v196, v196, v197\n v_cvt_pk_bf16_f32 v197, v198, v199\n v_add_f32 v192, v192, v193\n v_add_f32 v194, v194, v195\n " \
  "ds_write_b64 v58, v[196:197] offset:2304\n v_pk_mul_f32 v[84:85], v[84:85], v[170:171] op_sel_hi:[1,0]\n v_permlane32_swap_b32 v192, v194\n " \
  "v_pk_mul_f32 v[86:87], v[86:87], v[170:171] op_sel_hi:[1,0]\n v_cvt_pk_bf16_f32 v84, v84, v85\n v_add_f32 v192, v192, v194\n " \
  "v_cvt_pk_bf16_f32 v85, v86, v87\n ds_write_b64 v58, v[84:85] offset:2336\n v_rcp_f32 v192, v192\n " \
  "v_pk_mul_f32 v[248:249], v[248:249], v[170:171] op_sel:[0,1]\n v_pk_mul_f32 v[250:251], v[250:251], v[170:171] op_sel:[0,1]\n v_mov_b32 v194, v192\n " \
  "v_cvt_pk_bf16_f32 v248, v248, v249\n v_cvt_pk_bf16_f32 v249, v250, v251\n v_permlane32_swap_b32 v192, v194\n " \
  "ds_write_b64 v58, v[248:249] offset:2368\n v_pk_mul_f32 v[252:253], v[252:253], v[170:171] op_sel:[0,1]\n v_mov_b32 v193, v192\n " \
  "v_mov_b32 v195, v194\n v_pk_mul_f32 v[254:255], v[254:255], v[170:171] op_sel:[0,1]\n v_cvt_pk_bf16_f32 v252, v252, v253\n " \
  "v_permlane16_swap_b32 v192, v193\n v_permlane16_swap_b32 v194, v195\n v_cvt_pk_bf16_f32 v253, v254, v255\n ds_write_b64 v58, v[252:253] offset:2400\n " \
  "v_pk_mul_f32 v[220:221], v[220:221], v[192:193] op_sel_hi:[1,0]\n v_pk_mul_f32 v[222:223], v[222:223], v[192:193] op_sel_hi:[1,0]\n " \
  "v_cvt_pk_bf16_f32 v220, v220, v221\n v_cvt_pk_bf16_f32 v221, v222, v223\n ds_write_b64 v58, v[220:221] offset:18432\n " \
  "v_pk_mul_f32 v[124:125], v[124:125], v[192:193] op_sel_hi:[1,0]\n v_pk_mul_f32 v[126:127], v[126:127], v[192:193] op_sel_hi:[1,0]\n " \
  "v_cvt_pk_bf16_f32 v124, v124, v125\n v_cvt_pk_bf16_f32 v125, v126, v127\n ds_write_b64 v58, v[124:125] offset:18464\n " \
  "v_pk_mul_f32 v[128:129], v[128:129], v[192:193] op_sel:[0,1]\n v_pk_mul_f32 v[130:131], v[130:131], v[192:193] op_sel:[0,1]\n " \
  "v_cvt_pk_bf16_f32 v128, v128, v129\n v_cvt_pk_bf16_f32 v129, v130, v131\n ds_write_b64 v58, v[128:129] offset:18496\n " \
  "v_pk_mul_f32 v[132:133], v[132:133], v[192:193] op_sel:[0,1]\n v_pk_mul_f32 v[134:135], v[134:135], v[192:193] op_sel:[0,1]\n " \
  "v_cvt_pk_bf16_f32 v132, v132, v133\n v_cvt_pk_bf16_f32 v133, v134, v135\n ds_write_b64 v58, v[132:133] offset:18528\n " \
  "v_pk_mul_f32 v[244:245], v[244:245], v[194:195] op_sel_hi:[1,0]\n v_pk_mul_f32 v[246:247], v[246:247], v[194:195] op_sel_hi:[1,0]\n " \
  "v_cvt_pk_bf16_f32 v244, v244, v245\n v_cvt_pk_bf16_f32 v245, v246, v247\n ds_write_b64 v58, v[244:245] offset:20736\n " \
  "v_pk_mul_f32 v[108:109], v[108:109], v[194:195] op_sel_hi:[1,0]\n v_pk_mul_f32 v[110:111], v[110:111], v[194:195] op_sel_hi:[1,0]\n " \
  "v_cvt_pk_bf16_f32 v108, v108, v109\n v_cvt_pk_bf16_f32 v109, v110, v111\n ds_write_b64 v58, v[108:109] offset:20768\n " \
  "v_pk_mul_f32 v[112:113], v[112:113], v[194:195] op_sel:[0,1]\n v_pk_mul_f32 v[114:115], v[114:115], v[194:195] op_sel:[0,1]\n " \
  "v_cvt_pk_bf16_f32 v112, v112, v113\n v_cvt_pk_bf16_f32 v113, v114, v115\n ds_write_b64 v58, v[112:113] offset:20800\n " \
  "v_pk_mul_f32 v[116:117], v[116:117], v[194:195] op_sel:[0,1]\n v_pk_mul_f32 v[118:119], v[118:119], v[194:195] op_sel:[0,1]\n " \
  "v_cvt_pk_bf16_f32 v116, v116, v117\n v_cvt_pk_bf16_f32 v117, v118, v119\n ds_write_b64 v58, v[116:117] offset:20832\n "
#define G1_OUTPUT \
  "s_mov_b32 m0, 0x40000\n s_branch .Lg1p%=\n .Lg1q%=:\n s_sleep 1\n s_sub_u32 m0, m0, 1\n s_cbranch_scc1 .Lg1r%=\n .Lg1p%=:\n " \
  "buffer_load_dwordx4 v[216:219], %[fz], %[rs], 0 offen sc1\n buffer_load_dwordx4 v[220:223], %[fz], %[rs], 0 offen offset:64 sc1\n " \
  "buffer_load_dwordx4 v[200:203], %[fo], %[rs], 0 offen offset:0 sc1\n buffer_load_dwordx4 v[204:207], %[fo], %[rs], 0 offen offset:2048 sc1\n " \
  "buffer_load_dwordx4 v[208:211], %[fo], %[rs], 0 offen offset:1024 sc1\n buffer_load_dwordx4 v[212:215], %[fo], %[rs], 0 offen offset:3072 sc1\n " \
  "s_waitcnt vmcnt(5)\n v_max_u32 v152, v216, v217\n v_max3_u32 v152, v218, v219, v152\n s_waitcnt vmcnt(4)\n v_max3_u32 v152, v220, v221, v152\n " \
  "v_max3_u32 v152, v222, v223, v152\n s_waitcnt vmcnt(3)\n v_max3_u32 v152, v200, v201, v152\n v_max3_u32 v152, v202, v203, v152\n s_waitcnt vmcnt(2)\n " \
  "v_max3_u32 v152, v204, v205, v152\n v_max3_u32 v152, v206, v207, v152\n s_waitcnt vmcnt(1)\n v_max3_u32 v152, v208, v209, v152\n " \
  "v_max3_u32 v152, v210, v211, v152\n s_waitcnt vmcnt(0)\n v_max3_u32 v152, v212, v213, v152\n v_max3_u32 v152, v214, v215, v152\n " \
  "v_cmp_eq_u32 vcc, -1, v152\n s_cbranch_vccnz .Lg1q%=\n .Lg1r%=:\n v_mfma_f32_16x16x32_bf16 v[160:163], v[216:219], v[224:227], 0\n " \
  "v_mfma_f32_16x16x32_bf16 v[160:163], v[220:223], v[228:231], v[160:163]\n v_mfma_f32_16x16x32_bf16 v[152:155], v[200:203], v[224:227], 0\n " \
  "v_mfma_f32_16x16x32_bf16 v[152:155], v[204:207], v[228:231], v[152:155]\n v_mfma_f32_16x16x32_bf16 v[156:159], v[208:211], v[224:227], 0\n " \
  "v_mfma_f32_16x16x32_bf16 v[156:159], v[212:215], v[228:231], v[156:159]\n v_mfma_f32_16x16x32_bf16 v[172:175], v[216:219], v[232:235], 0\n " \
  "v_mfma_f32_16x16x32_bf16 v[172:175], v[220:223], v[236:239], v[172:175]\n v_mfma_f32_16x16x32_bf16 v[164:167], v[200:203], v[232:235], 0\n " \
  "v_mfma_f32_16x16x32_bf16 v[164:167], v[204:207], v[236:239], v[164:167]\n v_mfma_f32_16x16x32_bf16 v[168:171], v[208:211], v[232:235], 0\n " \
  "v_add_f32 v160, 0x358637bd, v160\n v_rcp_f32 v160, v160\n v_mfma_f32_16x16x32_bf16 v[168:171], v[212:215], v[236:239], v[168:171]\n " \
  "v_mul_f32 v152, v160, v152\n v_mfma_f32_16x16x32_bf16 v[184:187], v[216:219], v[240:243], 0\n v_mul_f32 v153, v160, v153\n " \
  "v_cvt_pk_bf16_f32 v152, v152, v153\n v_mul_f32 v154, v160, v154\n v_mfma_f32_16x16x32_bf16 v[184:187], v[220:223], v[244:247], v[184:187]\n " \
  "v_mul_f32 v155, v160, v155\n v_cvt_pk_bf16_f32 v153, v154, v155\n v_mul_f32 v156, v160, v156\n " \
  "v_mfma_f32_16x16x32_bf16 v[176:179], v[200:203], v[240:243], 0\n v_mul_f32 v157, v160, v157\n v_cvt_pk_bf16_f32 v154, v156, v157\n " \
  "v_mul_f32 v158, v160, v158\n v_mfma_f32_16x16x32_bf16 v[176:179], v[204:207], v[244:247], v[176:179]\n v_mul_f32 v159, v160, v159\n " \
  "v_cvt_pk_bf16_f32 v155, v158, v159\n buffer_store_dwordx4 v[152:155], %[go], %[rs], 0 offen offset:0 sc1\n " \
  "v_mfma_f32_16x16x32_bf16 v[180:183], v[208:211], v[240:243], 0\n ds_write_b128 %[lo], v[152:155] offset:0\n v_add_f32 v172, 0x358637bd, v172\n " \
  "v_mfma_f32_16x16x32_bf16 v[180:183], v[212:215], v[244:247], v[180:183]\n v_rcp_f32 v172, v172\n " \
  "v_mfma_f32_16x16x32_bf16 v[196:199], v[216:219], v[248:251], 0\n v_mul_f32 v164, v172, v164\n v_mul_f32 v165, v172, v165\n " \
  "v_cvt_pk_bf16_f32 v164, v164, v165\n v_mfma_f32_16x16x32_bf16 v[196:199], v[220:223], v[252:255], v[196:199]\n v_mul_f32 v166, v172, v166\n " \
  "v_mul_f32 v167, v172, v167\n v_cvt_pk_bf16_f32 v165, v166, v167\n v_mfma_f32_16x16x32_bf16 v[188:191], v[200:203], v[248:251], 0\n " \
  "v_mul_f32 v168, v172, v168\n v_mul_f32 v169, v172, v169\n v_cvt_pk_bf16_f32 v166, v168, v169\n " \
  "v_mfma_f32_16x16x32_bf16 v[188:191], v[204:207], v[252:255], v[188:191]\n v_mul_f32 v170, v172, v170\n v_mul_f32 v171, v172, v171\n " \
  "v_cvt_pk_bf16_f32 v167, v170, v171\n v_mfma_f32_16x16x32_bf16 v[192:195], v[208:211], v[248:251], 0\n " \
  "buffer_store_dwordx4 v[164:167], %[go], %[rs], 0 offen offset:1024 sc1\n ds_write_b128 %[lo], v[164:167] offset:1024\n " \
  "v_mfma_f32_16x16x32_bf16 v[192:195], v[212:215], v[252:255], v[192:195]\n v_add_f32 v184, 0x358637bd, v184\n v_rcp_f32 v184, v184\n s_nop 0\n " \
  "v_pk_mul_f32 v[176:177], v[176:177], v[184:185] op_sel_hi:[1,0]\n v_pk_mul_f32 v[178:179], v[178:179], v[184:185] op_sel_hi:[1,0]\n " \
  "v_pk_mul_f32 v[180:181], v[180:181], v[184:185] op_sel_hi:[1,0]\n v_pk_mul_f32 v[182:183], v[182:183], v[184:185] op_sel_hi:[1,0]\n " \
  "v_cvt_pk_bf16_f32 v176, v176, v177\n v_cvt_pk_bf16_f32 v177, v178, v179\n v_cvt_pk_bf16_f32 v178, v180, v181\n v_cvt_pk_bf16_f32 v179, v182, v183\n " \
  "buffer_store_dwordx4 v[176:179], %[go], %[rs], 0 offen offset:2048 sc1\n ds_write_b128 %[lo], v[176:179] offset:2048\n " \
  "v_add_f32 v196, 0x358637bd, v196\n v_rcp_f32 v196, v196\n s_nop 0\n v_pk_mul_f32 v[188:189], v[188:189], v[196:197] op_sel_hi:[1,0]\n " \
  "v_pk_mul_f32 v[190:191], v[190:191], v[196:197] op_sel_hi:[1,0]\n v_pk_mul_f32 v[192:193], v[192:193], v[196:197] op_sel_hi:[1,0]\n " \
  "v_pk_mul_f32 v[194:195], v[194:195], v[196:197] op_sel_hi:[1,0]\n v_cvt_pk_bf16_f32 v188, v188, v189\n v_cvt_pk_bf16_f32 v189, v190, v191\n " \
  "v_cvt_pk_bf16_f32 v190, v192, v193\n v_cvt_pk_bf16_f32 v191, v194, v195\n buffer_store_dwordx4 v[188:191], %[go], %[rs], 0 offen offset:3072 sc1\n " \
  "ds_write_b128 %[lo], v[188:191] offset:3072\n "
#define G1_PURGE \
  ".purgem G1M\n .purgem G1RA\n .purgem G1RB\n .purgem G1BK\n .purgem G1AK\n .purgem G1RDB\n .purgem G1RDA\n .purgem G1Q\n .purgem G1SG\n "
#define G1_CLOBBER \
  "v80", "v81", "v82", "v83", "v84", "v85", "v86", "v87", "v92", "v93", "v94", "v95", "v96", "v97", "v98", "v99", "v100", "v101", "v102", "v103", \
  "v104", "v105", "v106", "v107", "v108", "v109", "v110", "v111", \
  "v112", "v113", "v114", "v115", "v116", "v117", "v118", "v119", "v120", "v121", "v122", "v123", "v124", "v125", "v126", "v127", "v128", "v129", \
  "v130", "v131", "v132", "v133", "v134", "v135", "v144", "v145", "v146", "v147", "v152", "v153", "v154", "v155", \
  "v156", "v157", "v158", "v159"

template <int MODE>
__device__ __forceinline__ void g1_phase(char* smem, char* ws, u64& E, int w, f32x16& xq, Next& nx, int layer, const u16* __restrict__ x0, const u16* __restrict__ W,
                                         const u16* __restrict__ wo, const u16* __restrict__ wphi,
                                         const u16* __restrict__ wn1, const u16* __restrict__ wn2) {
  constexpr bool HH = (MODE == 1);
  constexpr int ABYTES = 128 * 128;
  constexpr int STAGE = ABYTES + 3 * 8192;
  constexpr int OFF_FX = 16384;
  constexpr int OFF_SP = 3 * STAGE;
  constexpr int OFF_RS = !HH ? 159744 : OFF_SP + 16384;
  constexpr int OFF_CS = OFF_RS + (HH ? 1152 : 1024);
  constexpr int OFF_WP = OFF_CS + 1536;
  constexpr int OFF_JK = LDS_TOTAL - 1024;
  constexpr int OFF_PK = 0;
  constexpr int OFF_VF = OFF_WP + 4096;
  constexpr int OFF_R2 = HH ? OFF_VF + 16384 : OFF_CS + 1024;
  constexpr int OFF_RD = OFF_CS + 768;
  constexpr int PSTR = 144;
  IDS
  W = uni(W); wphi = uni(wphi); wn1 = uni(wn1); wn2 = uni(wn2);
  const int kg = w >> 2, wm = (w >> 1) & 1, wn = w & 1;
  const bool first = !HH && layer == 0;
  f32x4 acc[2][2], qh[2][2], csq[2], st01, st23;
  bf16x8 wf[2];
  u64 kol;
  u32x4 pw[8];
  ST(layer * 24 + 0)
  const int R0 = wm * 64;
  u32x16 pa;
#define pcs pa[0]
#define prs pa[1]
#define opo pa[2]
#define ogo pa[3]
#define hof pa[7]
#define dm pa[8]
#define dm2 pa[9]
#define pcv pa[4]
#define pwf pa[5]
#define prv pa[6]
#define pvf pa[7]
#define ppk pa[10]
#define ppa pa[11]
#define pvr pa[12]
#define gkv pa[13]
#define gkz pa[14]
#define pwl pa[15]
#define pwm pa[4]
#define plb pa[10]
#define prd pa[11]
#define dm3 pa[12]
  f32x4 bias[3];
  const char* wos = wo_src(ws, wo, layer, hp);
  const unsigned tO = img(layer, hp) ? 8192u : 131072u;
  const unsigned sb = lds_addr(smem);
  auto prep = [&](auto ldr) {
    constexpr bool LDR = decltype(ldr)::value;
    if constexpr (!LDR) {
      pcs = sb + OFF_CS + wn * 128 + g * 16;
      prs = sb + OFF_RS + (R0 + fr) * 8;
      opo = (unsigned)(wn * 8192 + R0 * 64 + lane * 16);
      ogo = opo + WS_O + layer * (SEQ * DM * 2) + (unsigned)((rt * NH + 2 * hp) * 8192);
      opo += sb;
      pcv = sb + OFF_CS + (wn * 32 + 128 + fr) * 4;
      prv = sb + OFF_R2 + (R0 + 4 * g) * 4;
    } else {
      constexpr unsigned kvn = WS_KV + MODE * KVLB;
      const int t = tid & 255;
      dm = kvn + (unsigned)((rt * NH + 2 * hp) * KVPB) + t * 16;
      dm2 = t < 16 ? dm + 8192 : t >= 64 && t < 97 ? kvn + 16u * NH * KVPB + (unsigned)(hp * KVFB + rt * 528 + (t - 64) * 16) : 0x7fff0000u;
      if (!HH) dm3 = WS_HL + (unsigned)((256 + rt * 16 + hp) * 4096 + t * 16);
    }
    if constexpr (!HH) {
      prd = sb + OFF_RD + wn * 4;
      if constexpr (!LDR) {
        plb = sb + OFF_FX + wn * 2048 + lane * 16;
        hof = m0 + wm == 0 ? 0x7fff0000u : WS_HL + (unsigned)((layer * 128 + rt * 16 - 16 + wm * 16 + hp) * 4096 + wn * 2048 + lane * 16);
        const unsigned out = ~((0x5555u << 2 << fr) >> (4 * g));
#pragma unroll
        for (int i = 0; i < 8; ++i) {
          unsigned t;
          asm("v_bfe_i32 %0, %1, %2, 1" : "=v"(t) : "v"(out), "n"((i >> 2) * 16 + (i & 3)));
          bias[i >> 2][i & 3] = __uint_as_float(t & 0xff61b1e6u);
        }
        bias[2] = m0 + R0 == 0 ? f32x4{-3.0e38f, -3.0e38f, -3.0e38f, -3.0e38f} : bias[0];
        asm volatile("" : "+{v[36:39]}"(bias[0]), "+{v[40:43]}"(bias[1]), "+{v[44:47]}"(bias[2]));
      }
    } else {
      const int u = wm * 2 + kg;
      if constexpr (!LDR) {
        pwf = sb + OFF_WP + wn * 2048 + (4 * g + q4) * 64 + p4 * 8;
        pvf = sb + OFF_VF + w * 2048 + lane * 16;
        ppk = sb + OFF_PK + w * (32 * PSTR) + fr * PSTR + g * 8;
      }
      ppa = sb + OFF_PK + wn * (32 * PSTR) + (4 * g + q4) * PSTR + u * 32 + p4 * 8;
      pvr = sb + OFF_VF + wn * 2048 + lane * 16;
      const unsigned doff = WS_KV + (layer >> 1) * KVLB + (unsigned)((rt * NH + 2 * hp + wn) * KVPB);
      gkv = doff + u * 1024 + lane * 16;
      gkz = fr == 0 ? doff + 4096 + u * 32 + g * 8 : 0x7fff0000u;
    }
    asm volatile("" : "+{v[48:63]}"(pa));
  };

  if (kg == 1) {

    const int lw = w & 3;
    const int kB = 16 * lw + (lane >> 3);
    const int fB = (kB >> 1) & 1;
    const int cB0 = ((((lane & 7) >> 1) ^ fB) << 1) | (lane & 1), cB1 = ((((lane & 7) >> 1) ^ (fB | 2)) << 1) | (lane & 1);
    const bool im = img(layer, hp);

    unsigned vW0, vW1, vP, vX, vY;
    u64 ko2;
    auto ld_pw = [&](int i0) {
#pragma unroll
      for (int i = i0; i < i0 + 4; ++i) gld16(pw[i], vP, wn1 + i * 8 * 3072);
    };
    auto wo_tile = [&](int t) { glds2v(wos + knib(ko2, t) * tO, vW0, vW1, lds_addr(smem) + OFF_WO + t * 8192 + lw * 2048); };
    auto run = [&](auto fst) {
      constexpr bool FIRST = decltype(fst)::value;
      constexpr int N = 10;
      const unsigned vA = FIRST ? (unsigned)(((m0 + lw * 32 + (lane >> 3)) * DM + ((lane & 7) ^ (lane >> 3)) * 8) * 2)
                                : (unsigned)(rt * 262144 + lw * 4096 + lane * 16);
      unsigned imv = !FIRST && im ? ~0u : 0u;
      if (!FIRST) asm volatile("" : "+v"(imv));
      const unsigned vB0 = ((unsigned)(lw * 6144 + lane * 16) & imv) | ((unsigned)((kB * 3072 + hp * 64 + cB0 * 8) * 2) & ~imv);
      const unsigned vB1 = ((unsigned)(lw * 6144 + lane * 16) & imv) | (((unsigned)(((kB + 8) * 3072 + hp * 64 + cB1 * 8) * 2) - 1024u) & ~imv);
      const char* xa = FIRST ? (const char*)x0 : ws + WS_X + (layer - 1) * (SEQ * DM * 2);
      constexpr int KS = FIRST ? 128 : 16384;
      const u64 kord = FIRST ? korder0(rt) : nx.kord;
      auto ktile = [&](int ks) { return knib(kord, ks); };
      auto stageA = [&](int ks, int boff) {
        const int kt = ktile(ks);
        const unsigned sA = lds_addr(smem) + boff;
        if (FIRST) glds4v(xa + kt * KS, vA, vA + 15360u, vA + 2 * 15360u, vA + 3 * 15360u, sA + lw * 4096);
        else glds4c(xa + kt * KS, vA, sA + lw * 4096);
      };
      auto stageB = [&](const u16* Wp, int ks, int boff) {
        const int kt = ktile(ks);
        const unsigned sB = lds_addr(smem) + boff + ABYTES;
        const char* b = FIRST ? (const char*)(Wp + (size_t)kt * 64 * 3072) : nx.wq + kt * nx.tw;
        glds4v(b, vB0, vB1, vB0, vB1, sB + lw * 6144);
        glds2v(b + 4096, vB0, vB1, sB + lw * 6144 + 4096);
      };
      bf16x8 sel[3];
      unsigned cso[3], cst;
      if (FIRST) {
        WAITVN(N);
        BAR;
        stageA(2, 2 * STAGE); stageB(W, 2, 2 * STAGE);
        __builtin_amdgcn_sched_barrier(0);
        const int fl = (q4 >> 1) | ((g & 1) << 1), j = 3 * wm + (fr < 3 ? fr : 0);
#pragma unroll
        for (int i = 0; i < 3; ++i) {
          const unsigned o = fr == i ? 0x3F803F80u : 0u;
          sel[i] = __builtin_bit_cast(bf16x8, u32x4{o, o, o, o});
          cso[i] = (unsigned)(2 * STAGE + ABYTES + (g >> 1) * 6144 + (g & 1) * 1024 + q4 * 128 + p4 * 8 + (((wn * 2 + ((wm + i) & 1)) ^ fl) << 5) + ((3 * wm + i) >> 1) * 2048);
          asm volatile("" : "+v"(sel[i]), "+v"(cso[i]));
        }
        cst = fr < 3 ? WS_CPA + (unsigned)(((hp * 16 + rt) * 192 + (j >> 1) * 64 + wn * 32 + (j & 1) * 16 + 4 * g) * 4) : 0x7fff0000u;
        asm volatile("" : "+v"(cst));
      }
      u64 fall, fal2;
      unsigned fao, fa2, vS, vC, dS, dC, dR;
      auto setup = [&]() {
        if (FIRST) { asm volatile("s_load_dwordx2 %0, %1, 0x0\n\ts_waitcnt lgkmcnt(0)" : "=s"(E) : "s"(ws) : "memory"); fall = E + 1u; asm volatile("" : "+v"(fall)); }
        fao = (unsigned)((256 + rt * 16 + (lane & 15)) * FLS);
        fa2 = (unsigned)(((FIRST ? 512 : 256) + (lane & 15) * 16 + (FIRST ? hp : (hp - ((1 << (lane >> 4)) >> 1)) & 15)) * FLS);
        vP = (unsigned)((rt * 64 + (lane >> 3)) * 6144 + lw * 2048 + ((hp + (1 << lw)) & 15) * 128 + (lane & 7) * 16);
        vS = FIRST ? (unsigned)(WS_G0 + (m0 + lw * 32 + (lane & 31)) * 16)
                   : WS_ST + (unsigned)((layer - 1) * (SEQ * 128) + rt * 16384 + g * 1024 + lw * 256 + fr * 16);
        vC = (FIRST ? WS_CPA : WS_CSP) + (unsigned)(hp * 12288 + g * 768 + lw * 192 + (fr < 12 ? fr : 0) * 16);
        if (!FIRST) vX = HH ? (unsigned)(hp * 4096 + lw * 1024 + lane * 16) : (unsigned)((rt * 16 + hp) * 1024 + lane * 16);
        if (HH) { vY = (unsigned)(bid * 8192 + lw * 2048 + lane * 16); asm volatile("" : "+v"(vY)); }
        vW0 = ((unsigned)(lw * 2048 + lane * 16) & imv) | ((unsigned)((kB * DM + hp * 64 + ((lane & 7) ^ ((lane >> 4) << 1)) * 8) * 2) & ~imv); vW1 = vW0 + ((8 * DM * 2 - 1024u) & ~imv);
        dS = OFF_RS + lw * 256 + (FIRST ? (lane & 31) * 8 : fr * 16 + (g & 1) * 8); dC = OFF_CS + lw * 192 + (fr < 12 ? fr : 0) * 16 + g * 4;
        ko2 = korder(bid & 3, hp);
        dR = OFF_R2 + lw * 128 + (FIRST ? (lane & 31) * 4 : fr * 8 + (g & 1) * 4);
        asm volatile("" : "+v"(dR));
        asm volatile("" : "+v"(vS), "+v"(vX), "+v"(vW0), "+v"(vW1), "+v"(dS));
        asm volatile("" : "+s"(ko2));
        if (FIRST || HH) asm volatile("" : "+v"(vC), "+v"(dC), "+v"(fa2));
        if (FIRST) asm volatile("" : "+v"(vP)); else asm volatile("" : "+v"(fao));
      };
      if (!FIRST) setup();
      u32x4 sv[4], cv[4];
      auto ld_c = [&](u32x4& d, unsigned v, int so) { asm volatile("buffer_load_dwordx4 %0, %1, %2, %3 offen sc1" : "=v"(d) : "v"(v), "s"(rsW), "s"(so) : "memory"); };
      float tm, tr;
      unsigned tc;
      auto fin = [&]() {
        if (FIRST) {
          const unsigned tag = (unsigned)E + 1u;
          asm volatile("" : "+v"(sv[0]));
          _Pragma("unroll 1") for (unsigned spins = 0; spins < (1u << 18); ++spins) {
            if (__all(sv[0][1] == tag && sv[0][3] == tag)) break;
            __builtin_amdgcn_s_sleep(1);
            asm volatile("buffer_load_dwordx4 %0, %1, %2, 0 offen sc1\n\ts_waitcnt vmcnt(0)" : "=v"(sv[0]) : "v"(vS), "s"(rsW) : "memory");
          }
          tm = __uint_as_float(sv[0][0]); tr = __uint_as_float(sv[0][2]);
        } else {
          asm volatile("" : "+v"(sv[0]), "+v"(sv[1]), "+v"(sv[2]), "+v"(sv[3]));
          const f32x4 t = sum4(sv);
          const unsigned u = xs4(t[0], t[2], t[1], t[3]);
          auto h = __builtin_amdgcn_permlane32_swap(u, u, false, false);
          const float mean = __uint_as_float(h[0]) * (1.0f / 1024.0f);
          const float var = fmaxf(__uint_as_float(h[1]) * (1.0f / 1024.0f) - mean * mean, 0.f);
          tm = -mean; tr = __builtin_amdgcn_rsqf(var + 1e-5f);
        }
      };
      auto cfin = [&]() {
        if (FIRST || HH) {
          asm volatile("" : "+v"(cv[0]), "+v"(cv[1]), "+v"(cv[2]), "+v"(cv[3]));
          const f32x4 t = sum4(cv);
          tc = xs4(t[0], t[1], t[2], t[3]);
        }
      };
      bf16x8 cf[6];
      f32x4 csum = {0, 0, 0, 0};
      if (FIRST) { vX = (unsigned)(bid * 8192 + lw * 2048 + lane * 16); asm volatile("" : "+v"(vX)); }
#pragma unroll
      for (int kt = 0; kt < 13; ++kt) {
        const int cur = (kt % 3) * STAGE;
        if (!FIRST && kt == 0) { WAITV(4); }
        else if (FIRST && kt == 4 && lw == 1) { WAITVN(N + 2); }
        else if (HH && (kt == 4 || kt == 6) && true) { WAITVN(N + 1); }
        else if (HH && kt == 5 && true) { WAITVN(N + 2); }
        else if ((!FIRST && kt == 1 && lw == 3) || ((FIRST || HH) && kt == 10 && lw == 3) || (FIRST && (kt == 2 || kt == 4 || kt == 5))) { WAITVN(N + 1); }
        else { WAITVN(N); }
        if (!FIRST && kt == 2 && lw == 3) {
          asm volatile("" : "+v"(fall));
          if (__any(fall != E + (unsigned)layer)) poll_flags(ws, E, 1, rt, hp, lane, layer, false);
        }
        if (HH && kt == 11 && lw == 3) {
          asm volatile("" : "+v"(fal2));
          if (__any(fal2 != E + (unsigned)layer)) {
            _Pragma("unroll 1") for (int j = 0; j < 4; ++j) poll_flags(ws, E, 1, rt, (hp - ((1 << j) >> 1)) & 15, lane, layer, true);
          }
        }
        if (FIRST && kt == 11 && lw == 3) {
          asm volatile("" : "+v"(fal2));
          if (__any(fal2 != E + 1u)) poll_flags(ws, E, 2, rt, hp, lane, 1u, true);
        }
        BAR;
        if (FIRST && kt == 1) {
          asm volatile("" ::: "memory");
#pragma unroll
          for (int i = 0; i < 6; ++i) { const char* t = smem + cso[i >> 1] + (i & 1) * 12288; cf[i] = cat8(tr_read(t), tr_read(t + 512)); }
        }
        if (FIRST && kt >= 2 && kt < 6 && lw == kt - 2) setup();
        if (kt >= 6 && kt < 10 && lw == kt - 6) prep(IC<1>{});
        stageA(kt + 3, cur);
        if (FIRST && kt == 1) {
          asm volatile("" : "+v"(sel[0]));
#pragma unroll
          for (int i = 0; i < 6; ++i) csum = MFMA(cf[i], sel[i >> 1], csum);
          asm volatile("" : "+v"(csum));
        }
        stageB(W, kt + 3, cur);
        if (FIRST) {
          if (kt == 1) __builtin_amdgcn_raw_buffer_store_b128(__builtin_bit_cast(u32x4, csum), rsW, cst, 0, 16);
          if (kt == 3 && tid == 320) __builtin_amdgcn_raw_buffer_store_b64(__builtin_bit_cast(u32x2, fall), rsW, WS_FLAGS + (unsigned)(512 + rt * 16 + hp) * FLS, 0, 16);
          if (kt == 3 || kt == 4) glds16s(wo + (kt - 3) * 512, vX, lds_addr(smem) + OFF_JK);
        }
        if (HH && (kt == 3 || kt == 4) && true) glds16s(wo + (kt - 3) * 512, vY, lds_addr(smem) + OFF_JK);
        if (!FIRST && kt == 0 && lw == 3) asm volatile("global_load_dwordx2 %0, %1, %2 sc1" : "=v"(fall) : "v"(fao), "s"(ws + WS_FLAGS) : "memory");
        if ((FIRST || HH) && kt == 9 && lw == 3) asm volatile("global_load_dwordx2 %0, %1, %2 sc1" : "=v"(fal2) : "v"(fa2), "s"(ws + WS_FLAGS) : "memory");
      }
      WAITVN(N);
      BAR;
#pragma unroll
      for (int i = 0; i < (FIRST ? 1 : 4); ++i) ld_c(sv[i], vS, i * 4096);
      if (FIRST || HH) {
#pragma unroll
        for (int i = 0; i < 4; ++i) ld_c(cv[i], vC, i * 3072);
      }
      if (HH) glds16s(wphi, vX, lds_addr(smem) + OFF_WP + lw * 1024);
      else if (!FIRST) glds16c(ws + WS_CSA, vX, lds_addr(smem) + OFF_CS);
      wo_tile(0); wo_tile(1);
      WAITVN(HH ? 13 : 9);
      BAR;
      if (FIRST) {
        if (lw < 3) { ld_pw(0); ld_pw(4); WAITV(12); } else { WAITV(4); }
      } else {
        WAITV(4);
      }
      cfin();
      fin();
      asm volatile("" : "+v"(tm), "+v"(tr));
      if (FIRST || HH) asm volatile("" : "+v"(tc));
      if (FIRST && lw < 3) { WAITV(8); } else { WAITV(0); }
      lds<f32x2>(sb + dS) = f32x2{tm, tr};
      lds<float>(sb + dR) = tm; lds<float>(sb + dR + 512) = tr;
      if (FIRST || HH) lds<unsigned>(sb + dC) = tc;
      if (!HH) lds<float>(prd) = zero1();
      WAITL0;
    };
    typedef std::integral_constant<bool, true> T_;
    typedef std::integral_constant<bool, false> F_;
    __builtin_amdgcn_s_setprio(3);
    if (first) run(T_{}); else run(F_{});
    __builtin_amdgcn_s_setprio(0);
    BAR;
    if constexpr (HH) {
      u32x4 c0, c1;
      if (layer == 1) {
        const int n = bid * 8 + lw * 2;
        const void* p;
        unsigned a0, a1;
        if (n < 1536) {
          const int r = n % 768, q = r % 384, e = q % 24;
          p = n < 768 ? wn1 : W;
          a0 = (unsigned)((((q / 24) * 64 + (e / 6) * 16 + (lane >> 3)) * 3072 + ((e % 6) >> 1) * 1024 + (r < 384 ? 192 : 704)) * 2);
          a1 = a0 + 8 * 6144 + cB1 * 16; a0 += cB0 * 16;
        } else {
          p = n < 1792 ? wn2 : wo;
          a0 = (unsigned)((((n & 127) * 8 + (lane >> 3)) * DM + ((n & 128) ? 704 : 192) + ((lane & 7) ^ ((lane >> 4) << 1)) * 8) * 2);
          a1 = a0 + 8 * DM * 2;
        }
        p = uni(p);
        gld16(c0, a0, p); gld16(c1, a1, p);
        const u32x4 pz = empty4();
        __builtin_amdgcn_raw_buffer_store_b128(pz, rsW, dm, 0, 16);
        __builtin_amdgcn_raw_buffer_store_b128(pz, rsW, dm, 4096, 16);
        __builtin_amdgcn_raw_buffer_store_b128(pz, rsW, dm2, 0, 16);
      }
#pragma unroll
      for (int t = 2; t < 6; ++t) wo_tile(t);
      if (layer == 1) {
        WAITV(11);
        __builtin_amdgcn_sched_barrier(0);
        asm volatile("" : "+v"(c0), "+v"(c1));
        __builtin_amdgcn_raw_buffer_store_b128(c0, rsW, WS_IMG + (unsigned)(bid * 8192 + lw * 2048 + lane * 16), 0, 16);
        __builtin_amdgcn_raw_buffer_store_b128(c1, rsW, WS_IMG + (unsigned)(bid * 8192 + lw * 2048 + 1024 + lane * 16), 0, 16);
        WAITV(0);
      }
      pwl = vW0; pwm = vW1; kol = ko2;
      asm volatile("" : "=v"(csq[0]), "=v"(csq[1]), "=v"(st01), "=v"(st23), "=v"(wf[0]), "=v"(wf[1]));
      asm volatile("" : "=v"(acc[0][0]), "=v"(acc[0][1]), "=v"(acc[1][0]), "=v"(acc[1][1]));
      asm volatile("" : "=v"(qh[0][0]), "=v"(qh[0][1]), "=v"(qh[1][0]), "=v"(qh[1][1]));
    } else {
      const int lane = lane_id();
#pragma unroll
      for (int t = 2; t < 12; ++t) {
        wo_tile(t);
        if (t == 2 && first) {
        const u32x4 pz = empty4();
        __builtin_amdgcn_raw_buffer_store_b128(pz, rsW, dm, 0, 16);
        __builtin_amdgcn_raw_buffer_store_b128(pz, rsW, dm, 4096, 16);
        __builtin_amdgcn_raw_buffer_store_b128(pz, rsW, dm3, 0, 16);
        __builtin_amdgcn_raw_buffer_store_b128(pz, rsW, dm2, 0, 16);
        if (lw == 3 && lane < 48) __builtin_amdgcn_raw_buffer_store_b128(lds<u32x4>(sb + OFF_CS + lane * 16), rsW, WS_CSA + (unsigned)((rt * 16 + hp) * 1024 + lane * 16), 0, 16);
        }
      }
      if (first) { BAR;   WAITV(0); }
    }
    asm volatile("" : "={v[64:79]}"(xq));
  } else {

    prep(IC<0>{});
    if (first) {
      const int row = m0 + hp * 8 + w * 2 + (lane >> 5);
      asm volatile("s_load_dwordx2 %0, %1, 0x0\n\ts_waitcnt lgkmcnt(0)" : "=s"(E) : "s"(ws) : "memory");
      const unsigned tag = (unsigned)E + 1u;
      static_assert(HH || (OFF_SP == 122880 && OFF_R2 + 1024 <= OFF_JK), "addresses used at the top of the kernel");
      WAITV(0);
      u32x4 xs[4];
#pragma unroll
      for (int i = 0; i < 4; ++i) xs[i] = *(const u32x4*)(smem + OFF_SP + w * 4096 + (lane >> 5) * 2048 + (lane & 31) * 16 + i * 512);
      typedef __attribute__((ext_vector_type(2))) __bf16 bf2;
      const bf2 one2 = __builtin_bit_cast(bf2, 0x3F803F80u);
      float sa[2] = {zero1(), zero1()}, sq[2] = {zero1(), zero1()};
#pragma unroll
      for (int i = 0; i < 16; ++i) {
        const unsigned u = xs[i >> 2][i & 3];
        const bf2 t = __builtin_bit_cast(bf2, u);
        sa[i & 1] = __builtin_amdgcn_fdot2_f32_bf16(t, one2, sa[i & 1], false);
        sq[i & 1] = __builtin_amdgcn_fdot2_f32_bf16(t, t, sq[i & 1], false);
      }
      float sm = r16_sum(sa[0] + sa[1]), m2 = r16_sum(sq[0] + sq[1]);
      {
        auto r = __builtin_amdgcn_permlane16_swap(__float_as_uint(sm), __float_as_uint(sm), false, false);
        auto q = __builtin_amdgcn_permlane16_swap(__float_as_uint(m2), __float_as_uint(m2), false, false);
        sm = __uint_as_float(r[0]) + __uint_as_float(r[1]); m2 = __uint_as_float(q[0]) + __uint_as_float(q[1]);
      }
      const float mean = sm * (1.0f / 1024.0f);
      m2 = fmaxf(m2 - sm * mean, 0.f);
      u32x4 gw = {__float_as_uint(-mean), tag, __float_as_uint(rsqrtf(m2 * (1.0f / 1024.0f) + 1e-5f)), tag};
      if ((lane & 31) == 0) __builtin_amdgcn_raw_buffer_store_b128(gw, rsW, (unsigned)(WS_G0 + row * 16), 0, 16);
      __builtin_amdgcn_raw_buffer_store_b128(empty4(), rsW, WS_HL + (unsigned)((rt * 16 + hp) * 4096 + tid * 16), 0, 16);
    }
    const int fl = (q4 >> 1) | ((g & 1) << 1);
    const unsigned bo = lds_addr(smem) + ABYTES + (g >> 1) * 6144 + (g & 1) * 1024 + q4 * 128 + p4 * 8;
    const unsigned bo0 = bo + (((wn * 2 + 0) ^ fl) << 5), bo1 = bo + (((wn * 2 + 1) ^ fl) << 5);
    const unsigned ar = lds_addr(smem) + (wm * 64 + fr) * 128;
    const unsigned aK0 = ar + ((g ^ (fr & 7)) << 4), aK1 = ar + (((4 + g) ^ (fr & 7)) << 4);
    f32x32 ac0, ac1, ac2;
    asm volatile("" :: "v"(aK0), "v"(aK1), "v"(bo0), "v"(bo1));
    if (first) BAR;
    else publish(rsW, tid, 256 + rt * 16 + hp, E + (unsigned)layer);
#define G1_OUT
#define G1_T(a)
#define G1_TEND
#define G1_TCL
    asm volatile(G1_MACROS
        "s_mov_b32 vcc_hi, %[lay]\n"
        ".if %[hh]\n s_branch .Lg1m\n .else\n"
        ".p2align 3\n .Lg1m:\n"
        "G1RDB 0, v16, v17, 0\n G1RDA v12, v13, 0\n G1RDB 1, v16, v17, 0\n"
        "ds_read_b128 v[64:67], v20\n ds_read_b128 v[68:71], v20 offset:2048\n"
        "ds_read_b128 v[72:75], v20 offset:4096\n ds_read_b128 v[76:79], v20 offset:6144\n"
        "s_cmp_eq_u32 vcc_hi, 0\n s_waitcnt lgkmcnt(12)\n"
        "G1SG 0, 1, 1, v12, v13, v16, v17, 0\n"
        "s_mov_b32 vcc_lo, 16\n"
        "G1SG 1, 1, 1, v12, v13, v16, v17, 40960\n"
        "G1SG 2, 1, 2, v12, v13, v16, v17, 40960\n"
        "s_cbranch_scc1 .Lg1a%=\n"
        "s_barrier\n"
        G1_T(88)
        "s_barrier\n"
        G1_T(92)
        "G1RDA v12, v13, 40960\n"
        "s_waitcnt lgkmcnt(4)\n"
        "s_branch .Lg1b%=\n"
        ".Lg1a%=:\n"
        G1_T(88)
        "s_barrier\n"
        G1_T(92)
        "G1RDB 0, v16, v17, 40960\n G1RDA v12, v13, 40960\n G1RDB 1, v16, v17, 40960\n"
        ".p2align 3\n s_nop 0\n"
        ".Lg1b%=:\n"
        "s_waitcnt lgkmcnt(8)\n"
        "G1SG 0, 0, 1, v12, v13, v16, v17, 40960\n"
        "s_barrier\n"
        "G1SG 1, 0, 1, v14, v15, v18, v19, 0\n"
        "G1SG 2, 0, 1, v14, v15, v18, v19, 0\n"
        "s_lshr_b32 vcc_lo, vcc_lo, 1\n s_waitcnt vmcnt(0) lgkmcnt(8)\n"
        "G1SG 0, 0, 1, v14, v15, v18, v19, 0\n"
        "s_barrier\n"
        "G1SG 1, 0, 1, v12, v13, v16, v17, 0\n"
        "G1SG 2, 0, 1, v12, v13, v16, v17, 0\n"
        "s_cbranch_scc0 .Lg1c%=\n s_waitcnt lgkmcnt(8)\n"
        "G1SG 0, 0, 1, v12, v13, v16, v17, 0\n"
        "s_barrier\n"
        "G1SG 1, 0, 1, v12, v13, v16, v17, 40960\n"
        "G1SG 2, 0, 1, v12, v13, v16, v17, 40960\n"
        "s_branch .Lg1b%=\n"
        ".Lg1c%=:\n"
        "s_waitcnt lgkmcnt(8)\n"
        "G1SG 0, 0, 1, v12, v13, v16, v17, 0\n s_bitcmp1_b32 vcc_hi, 0\n"
        G1_T(96)
        "s_cbranch_scc1 .Lg1n\n s_barrier\n"
        G1_SLIDE
        ".endif\n .if %[hh]\n .p2align 3\n s_nop 0\n .Lg1n:\n s_barrier\n"
        G1_HEDGE
        ".endif\n"
        G1_TEND G1_PURGE
        : "=&{v[160:191]}"(ac0), "=&{v[192:223]}"(ac1), "=&{v[224:255]}"(ac2), "=&{v[64:79]}"(xq),
          "=&{v[88:91]}"(csq[1]), "=&{v[148:151]}"(csq[0]), "=&{v[140:143]}"(st01), "=&{v[136:139]}"(st23),
          "+{v[48:63]}"(pa), "+{v[36:39]}"(bias[0]), "+{v[40:43]}"(bias[1]), "+{v[44:47]}"(bias[2]) G1_OUT
        : "{v12}"(aK0), "{v13}"(aK1), "{v14}"(aK0 + 2 * STAGE), "{v15}"(aK1 + 2 * STAGE), "{v16}"(bo0), "{v17}"(bo1),
          "{v18}"(bo0 + 2 * STAGE), "{v19}"(bo1 + 2 * STAGE), "{v20}"(wn ? aK1 : aK0),
          [lay] "s"(layer), [hh] "n"(MODE), [rs] "s"(rsW), [wm] "s"(wm)
        : "memory", "scc", "vcc", "m0", G1_TCL G1_CLOBBER);
    acc[0][0] = quad<0>(ac0); acc[0][1] = quad<1>(ac0); acc[1][0] = quad<6>(ac0); acc[1][1] = quad<7>(ac0);
    asm volatile("" : "=s"(kol));
    wf[0] = __builtin_bit_cast(bf16x8, bias[0]); wf[1] = __builtin_bit_cast(bf16x8, bias[1]);
    qh[0][0] = quad<4>(ac1); qh[0][1] = quad<5>(ac1); qh[1][0] = quad<2>(ac2); qh[1][1] = quad<3>(ac2);
  }
  ST(layer * 24 + 4)
  {
  const int lane = lane_id(), tid = w * 64 + lane, fr = lane & 15, g = lane >> 4;
  (void)fr; (void)g;
  if constexpr (HH) {
    const f32x4 z4 = {0, 0, 0, 0};
    constexpr float L2E = 1.4426950408889634f;
    const unsigned kvo = WS_KV + (layer >> 1) * KVLB, kfo = kvo + 16u * NH * KVPB + hp * KVFB;
    auto phi2 = [&](const f32x4 (&a)[2][2], f32x4 st, bf16x8 (&qp)[2][2]) {
      f32x4 pm[2][2];
      f32x2 f[2][4];
      asm volatile("" : "+v"(st));
#pragma unroll
      for (int i = 0; i < 8; ++i) {
        const int rb = i >> 2, c = (i >> 1) & 1, e = (i & 1) * 2;
        f[rb][i & 3] = __builtin_elementwise_fma(f32x2{csq[c][e], csq[c][e + 1]}, f32x2{st[2 * rb], st[2 * rb]}, f32x2{a[rb][c][e], a[rb][c][e + 1]});
      }
#pragma unroll
      for (int rb = 0; rb < 2; ++rb) asm volatile("" : "+v"(f[rb][0]), "+v"(f[rb][1]), "+v"(f[rb][2]), "+v"(f[rb][3]));
#pragma unroll
      for (int rb = 0; rb < 2; ++rb) {
        const bf16x8 xf = __builtin_bit_cast(bf16x8, u32x4{pk2(f[rb][0][0], f[rb][0][1]), pk2(f[rb][1][0], f[rb][1][1]), pk2(f[rb][2][0], f[rb][2][1]), pk2(f[rb][3][0], f[rb][3][1])});
        pm[rb][0] = MFMA(wf[0], xf, z4); pm[rb][1] = MFMA(wf[1], xf, z4);
      }
      asm volatile("s_nop 7" : "+v"(pm[0][0]), "+v"(pm[0][1]), "+v"(pm[1][0]), "+v"(pm[1][1]));
      float mx[2], mn[2];
#pragma unroll
      for (int u = 0; u < 2; ++u) max8pn(pm[u][0], pm[u][1], mx[u], mn[u]);
      xr4<true>(mx[0], mn[0], mx[1], mn[1]);
      float ep[2][8], en[2][8], sp[2], sn[2];
#pragma unroll
      for (int u = 0; u < 2; ++u) {
        const float c = st[2 * u + 1] * L2E, mxs = -mx[u] * c, mns = -mn[u] * c;
#pragma unroll
        for (int i = 0; i < 4; ++i) {
          const f32x2 p2 = {pm[u][i >> 1][(i & 1) * 2], pm[u][i >> 1][(i & 1) * 2 + 1]};
          const f32x2 tp = __builtin_elementwise_fma(p2, f32x2{c, c}, f32x2{mxs, mxs});
          const f32x2 tn = __builtin_elementwise_fma(-p2, f32x2{c, c}, f32x2{mns, mns});
          ep[u][2 * i] = __builtin_amdgcn_exp2f(tp[0]); ep[u][2 * i + 1] = __builtin_amdgcn_exp2f(tp[1]);
          en[u][2 * i] = __builtin_amdgcn_exp2f(tn[0]); en[u][2 * i + 1] = __builtin_amdgcn_exp2f(tn[1]);
        }
        const f32x2 a2 = (f32x2{ep[u][0], ep[u][1]} + f32x2{ep[u][2], ep[u][3]}) + (f32x2{ep[u][4], ep[u][5]} + f32x2{ep[u][6], ep[u][7]});
        const f32x2 b2 = (f32x2{en[u][0], en[u][1]} + f32x2{en[u][2], en[u][3]}) + (f32x2{en[u][4], en[u][5]} + f32x2{en[u][6], en[u][7]});
        sp[u] = a2[0] + a2[1]; sn[u] = b2[0] + b2[1];
      }
      xr4<false>(sp[0], sn[0], sp[1], sn[1]);
#pragma unroll
      for (int rb = 0; rb < 2; ++rb) {
        const float ip = __builtin_amdgcn_rcpf(sp[rb]), in = __builtin_amdgcn_rcpf(sn[rb]);
        qp[rb][0] = __builtin_bit_cast(bf16x8, u32x4{pk2(ep[rb][0] * ip, ep[rb][1] * ip), pk2(ep[rb][2] * ip, ep[rb][3] * ip), pk2(ep[rb][4] * ip, ep[rb][5] * ip), pk2(ep[rb][6] * ip, ep[rb][7] * ip)});
        qp[rb][1] = __builtin_bit_cast(bf16x8, u32x4{pk2(en[rb][0] * in, en[rb][1] * in), pk2(en[rb][2] * in, en[rb][3] * in), pk2(en[rb][4] * in, en[rb][5] * in), pk2(en[rb][6] * in, en[rb][7] * in)});
        asm volatile("" : "+v"(qp[rb][0]), "+v"(qp[rb][1]));
      }
    };
    bf16x8 one8 = ones8();
    asm volatile("" : "+v"(one8));
    WAITL0; BAR;
    ST(layer * 24 + 5)
    {
      bf16x8 af[4], v0[4], v1[4];
      asm volatile("" ::: "memory");
#pragma unroll
      for (int sw = 0; sw < 4; ++sw) {
        af[sw] = cat8(tr_read(ppa + sw * (64 * PSTR)), tr_read(ppa + sw * (64 * PSTR) + 16 * PSTR));
        v0[sw] = lds<bf16x8>(pvr + sw * 4096);
        v1[sw] = lds<bf16x8>(pvr + sw * 4096 + 1024);
      }
      __builtin_amdgcn_sched_barrier(0);
      f32x4 kv[3] = {z4, z4, z4};
#pragma unroll
      for (int sw = 0; sw < 4; ++sw) {
        kv[0] = MFMA(af[sw], v0[sw], kv[0]);
        kv[1] = MFMA(af[sw], v1[sw], kv[1]);
        kv[2] = MFMA(af[sw], one8, kv[2]);
      }
      auto pc = [](float x, float y) { const unsigned t = pk2(x, y); return t < EMPTY - 1u ? t : EMPTY - 1u; };
      __builtin_amdgcn_raw_buffer_store_b128(u32x4{pc(kv[0][0], kv[0][1]), pc(kv[0][2], kv[0][3]), pc(kv[1][0], kv[1][1]), pc(kv[1][2], kv[1][3])}, rsW, gkv, 0, 16);
      __builtin_amdgcn_raw_buffer_store_b64(u32x2{pc(kv[2][0], kv[2][1]), pc(kv[2][2], kv[2][3])}, rsW, gkz, 0, 16);
    }
    auto wo_late = [&](int t) { glds2v(wos + knib(kol, t) * tO, pwl, pwm, sb + OFF_WO + t * 8192 + (w & 3) * 2048); };
    ST(layer * 24 + 6)
    u32x4 l1[8];
    const bool red = w == 7 || (w == 6 && (rt & 7) == 0);
    unsigned lo = kvo + (unsigned)((2 * hp + (rt >> 3) + (lane >> 5) * 8 * NH) * KVPB) +
                  (w == 7 ? (unsigned)((rt & 4) * 512 + (g & 1) * 1024 + (rt & 3) * 256 + fr * 16) : (unsigned)(4096 + (lane & 7) * 16));
    unsigned fs = w == 7 ? kfo + (unsigned)((rt >> 2) * 2048 + (lane >> 5) * 1024 + (rt & 3) * 256 + fr * 16 + (g & 1) * 8)
                : (lane & 24) ? 0x7fff0000u : kfo + (unsigned)(8192 + (rt >> 3) * 128 + (lane & 4) * 16 + (lane & 1) * 32 + (lane >> 5) * 16 + (lane & 2) * 4);
    auto l1_load = [&]() {
      asm volatile("" ::: "memory");
#pragma unroll
      for (int p = 0; p < 8; ++p) asm volatile("buffer_load_dwordx4 %0, %1, %2, %3 offen sc1" : "=v"(l1[p]) : "v"(lo), "s"(rsW), "s"(p * (NH * KVPB)) : "memory");
      __builtin_amdgcn_sched_barrier(0);
    };
    if (red) asm volatile("" : "+v"(lo), "+v"(fs));
    if (red) { __builtin_amdgcn_s_sleep(13); l1_load(); }
    bf16x8 qp2[2][2];
    if (!kg) {
      phi2(qh, st23, qp2);
    } else if (red) {
      typedef __attribute__((ext_vector_type(2))) __bf16 bf2;
      unsigned c0 = 0x00003F80u, c1 = 0x3F800000u;
      asm volatile("" : "+v"(c0), "+v"(c1));
      float s4[4];
      _Pragma("unroll 1") for (unsigned spins = 0; spins < (1u << 18); ++spins) {
        float a[8] = {zero1(), zero1(), zero1(), zero1(), zero1(), zero1(), zero1(), zero1()};
        ARRIVED("+v"(l1[0]), "+v"(l1[1]), "+v"(l1[2]), "+v"(l1[3]), "+v"(l1[4]), "+v"(l1[5]), "+v"(l1[6]), "+v"(l1[7]));
#pragma unroll
        for (int p = 0; p < 8; ++p)
#pragma unroll
          for (int e = 0; e < 4; ++e) {
            const unsigned u = l1[p][e];
            a[2 * e] = __builtin_amdgcn_fdot2_f32_bf16(__builtin_bit_cast(bf2, u), __builtin_bit_cast(bf2, c0), a[2 * e], false);
            a[2 * e + 1] = __builtin_amdgcn_fdot2_f32_bf16(__builtin_bit_cast(bf2, u), __builtin_bit_cast(bf2, c1), a[2 * e + 1], false);
          }
#pragma unroll
        for (int i = 0; i < 4; ++i) {
          auto r = __builtin_amdgcn_permlane32_swap(__float_as_uint(a[i]), __float_as_uint(a[i + 4]), false, false);
          s4[i] = __uint_as_float(r[0]) + __uint_as_float(r[1]);
        }
        const float t = s4[0] + s4[2];
        if (__all(t == t)) break;
        unsigned mxw = 0;
#pragma unroll
        for (int p = 0; p < 8; ++p)
#pragma unroll
          for (int e = 0; e < 4; ++e) mxw = l1[p][e] > mxw ? l1[p][e] : mxw;
        if (__all(mxw != EMPTY)) break;
        l1_load();
      }
      WAITV(0);
      unsigned o0 = pk2(s4[0], s4[1]), o1 = pk2(s4[2], s4[3]);
      o0 = o0 < EMPTY - 1u ? o0 : EMPTY - 1u; o1 = o1 < EMPTY - 1u ? o1 : EMPTY - 1u;
      __builtin_amdgcn_raw_buffer_store_b64(u32x2{o0, o1}, rsW, fs, 0, 16);
    }
    ST(layer * 24 + 8)
    WAITL0;
    BAR;
    ST(layer * 24 + 9)
    if (kg) {
#pragma unroll
      for (int i = 6; i < 12; ++i) wo_late(i);
    } else {
      unsigned fo = kfo + (unsigned)(wn * 4096 + lane * 16), fz = kfo + (unsigned)(8192 + wn * 128 + g * 16);
      asm volatile("" : "+v"(fo), "+v"(fz));
      bf16x8 qp[2][2];
      phi2(acc, st01, qp);
      asm volatile(G1_OUTPUT
          :: "{v[224:227]}"(qp[0][0]), "{v[228:231]}"(qp[0][1]), "{v[232:235]}"(qp[1][0]), "{v[236:239]}"(qp[1][1]),
             "{v[240:243]}"(qp2[0][0]), "{v[244:247]}"(qp2[0][1]), "{v[248:251]}"(qp2[1][0]), "{v[252:255]}"(qp2[1][1]),
             [fo] "v"(fo), [fz] "v"(fz), [go] "v"(ogo), [lo] "v"(opo), [rs] "s"(rsW)
          : "memory", "vcc", "scc", "m0", "v152", "v153", "v154", "v155", "v156", "v157", "v158", "v159", "v160", "v161", "v162", "v163", "v164", "v165", "v166", "v167", "v168", "v169", "v170", "v171", "v172", "v173", "v174", "v175", "v176", "v177", "v178", "v179", "v180", "v181", "v182", "v183", "v184", "v185", "v186", "v187", "v188", "v189", "v190", "v191", "v192", "v193", "v194", "v195", "v196", "v197", "v198", "v199", "v200", "v201", "v202", "v203", "v204", "v205", "v206", "v207", "v208", "v209", "v210", "v211", "v212", "v213", "v214", "v215", "v216", "v217", "v218", "v219", "v220", "v221", "v222", "v223");
    }
  }
  ST(layer * 24 + 12)
  if (first && w >= 4 && w < 7) {
    typedef __attribute__((ext_vector_type(2))) __bf16 bf2;
    unsigned c0 = 0x00003F80u, c1 = 0x3F800000u;
    asm volatile("" : "+v"(c0), "+v"(c1));
    const bf2 lo1 = __builtin_bit_cast(bf2, c0), hi1 = __builtin_bit_cast(bf2, c1);
    float a[8] = {zero1(), zero1(), zero1(), zero1(), zero1(), zero1(), zero1(), zero1()};
#pragma unroll
    for (int i = 0; i < 8; ++i) {
      asm volatile("" : "+v"(pw[i]));
#pragma unroll
      for (int e = 0; e < 4; ++e) {
        const unsigned u = pw[i][e];
        a[2 * e] = __builtin_amdgcn_fdot2_f32_bf16(__builtin_bit_cast(bf2, u), lo1, a[2 * e], false);
        a[2 * e + 1] = __builtin_amdgcn_fdot2_f32_bf16(__builtin_bit_cast(bf2, u), hi1, a[2 * e + 1], false);
      }
    }
#pragma unroll
    for (int i = 0; i < 4; ++i) {
      auto r = __builtin_amdgcn_permlane32_swap(__float_as_uint(a[i]), __float_as_uint(a[i + 4]), false, false);
      a[i] = __uint_as_float(r[0]) + __uint_as_float(r[1]);
    }
#pragma unroll
    for (int i = 0; i < 2; ++i) {
      auto r = __builtin_amdgcn_permlane16_swap(__float_as_uint(a[i]), __float_as_uint(a[i + 2]), false, false);
      a[i] = __uint_as_float(r[0]) + __uint_as_float(r[1]);
      a[i] += dpp_ror<8>(a[i]);
    }
    if (!(lane & 8))
      __builtin_amdgcn_raw_buffer_store_b64(u32x2{__float_as_uint(a[0]), __float_as_uint(a[1])}, rsW,
          (unsigned)(WS_CSP + ((((hp + (1 << (w - 4))) & 15) * 16 + rt) * 192 + (w - 4) * 64 + (lane & 7) * 8 + (lane >> 5) * 4 + ((lane >> 4) & 1) * 2) * 4), 0, 16);
  }
  }
}

#undef pcs
#undef prs
#undef opo
#undef ogo
#undef hof
#undef dm
#undef dm2
#undef pcv
#undef pwf
#undef prv
#undef pvf
#undef ppk
#undef ppa
#undef pvr
#undef gkv
#undef gkz
#undef pwl
#undef pwm
#undef plb
#undef prd
#undef dm3

__device__ __forceinline__ void g2_phase(char* smem, char* ws, u64 E, int w, f32x16& xq, Next& nx, int layer, const u16* __restrict__ x0,
                                         const u16* __restrict__ wo, const u16* __restrict__ wnx, u16* __restrict__ out) {
  IDS
  wnx = uni(wnx);
  const unsigned sb = lds_addr(smem);
  const int lw = w & 3, k2 = 16 * lw + (lane >> 3);
  const int r0 = w * 16 + (lane >> 3);
  unsigned pxr = sb + r0 * 128 + (lane & 7) * 16;
  const unsigned rmo = (unsigned)(((m0 + r0) * DM + hp * 64 + ((lane & 7) ^ (lane >> 3)) * 8) * 2);
  const unsigned imo = (unsigned)((rt * 16 + hp) * 16384 + r0 * 128 + (lane & 7) * 16);
  constexpr int S0 = 0, S1 = 16384, S2 = 32768, S3 = 147456, S4 = OFF_WO, S5 = 98304, S6 = 114688;
  constexpr int AS[16] = {S0, S1, S2, S3, S0, S1, S4, S2, S3, S0, S1, S4, S5, S3, S0, S6};
  constexpr int OFF_C = 0;
  auto bs = [](int t) { return OFF_WO + (t < 12 ? t : t - 10) * 8192; };
  const char *xa1, *xa2, *wq2;
  u64 fm;
  unsigned vB0, vB1, vXA, po;
  if (w >= 4) {
    constexpr int WT[15] = {0, 8, 12, 14, 16, 18, 20, 18, 16, 14, 12, 12, 10, 10, 8};
    const char* o = ws + WS_O + layer * (SEQ * DM * 2) + rt * 262144;
    const unsigned vA = (unsigned)(lw * 4096 + lane * 16);
    u64 kord = korder(bid & 3, hp);
    auto ldA = [&](int s) { glds4c(o + knib(kord, s) * 16384, vA, sb + AS[s] + lw * 4096); };
    const bool im = img(layer, hp), in = img(layer + 1, hp);
    unsigned vW0 = im ? (unsigned)(lw * 2048 + lane * 16) : (unsigned)((k2 * DM + hp * 64 + ((lane & 7) ^ ((lane >> 4) << 1)) * 8) * 2), vW1 = vW0 + (im ? 0u : 8 * DM * 2 - 1024u);
    const char *wos = wo_src(ws, wo, layer, hp), *wqs = wq_src(ws, wnx, layer + 1, hp);
    unsigned tO = im ? 8192u : 131072u;
    asm volatile("" : "+v"(vW0), "+v"(vW1));
    asm volatile("" : "+s"(wos), "+s"(tO));
    auto ldW = [&](int t) { glds2v(wos + knib(kord, t) * tO, vW0, vW1, sb + bs(t) + lw * 2048); };
    const int fB = (k2 >> 1) & 1;
    vB0 = in ? (unsigned)(lw * 6144 + lane * 16) : (unsigned)((k2 * 3072 + hp * 64 + (((((lane & 7) >> 1) ^ fB) << 1) | (lane & 1)) * 8) * 2);
    vB1 = in ? vB0 : (unsigned)(((k2 + 8) * 3072 + hp * 64 + (((((lane & 7) >> 1) ^ (fB | 2)) << 1) | (lane & 1)) * 8) * 2) - 1024u;
    const u64 kn = nx.kord = korder(kv1(bid), hp);
    nx.wq = wqs; nx.tw = in ? 24576u : 393216u;
    const char *wq0 = wqs + hp * (in ? 24576u : 393216u), *wq1 = wqs + knib(kn, 1) * (in ? 24576u : 393216u);
    wq2 = wqs + knib(kn, 2) * (in ? 24576u : 393216u);
    xa1 = ws + WS_X + layer * (SEQ * DM * 2) + knib(kn, 1) * 16384; xa2 = ws + WS_X + layer * (SEQ * DM * 2) + knib(kn, 2) * 16384;
    vXA = (unsigned)(rt * 262144 + lw * 4096 + lane * 16);
    fm = kfirst(kn);
    po = ((fm >> lane) & 1) ? WS_FLAGS + (unsigned)(256 + rt * 16 + lane) * FLS : 0u;
    asm volatile("" : "+v"(vB0), "+v"(vB1), "+v"(vXA), "+v"(po));
    asm volatile("" : "+s"(xa1), "+s"(xa2), "+s"(wq2), "+s"(fm), "+s"(nx.kord), "+s"(nx.wq), "+s"(nx.tw));
    asm volatile("" : "+s"(wq0), "+s"(wq1));
    auto ldB = [&](int s, int part) {
      const char* b = s ? wq1 : wq0;
      if (part) glds4v(b, vB0, vB1, vB0, vB1, sb + s * 40960 + 16384 + lw * 6144);
      else glds2v(b + 4096, vB0, vB1, sb + s * 40960 + 16384 + lw * 6144 + 4096);
    };
    const char* o1 = o + knib(kord, 1) * 16384;
    asm volatile("" : "+s"(o1), "+s"(kord), "+s"(o));
    asm volatile("" :: "v"(vA));
    if (layer) { BAR;   }
    BAR;
    glds4c(o1, vA, sb + AS[1] + lw * 4096); __builtin_amdgcn_s_sleep(3); ldA(2); __builtin_amdgcn_s_sleep(3); ldA(3); __builtin_amdgcn_s_sleep(3); ldA(4);
    WAITV(12);
    BAR;
    auto step = [&](auto ktc) {
      constexpr int kt = decltype(ktc)::value;
      WAITVN(WT[kt]);
      BAR;
      if constexpr (kt == 1) { ldA(5); ldA(6); }
      if constexpr (kt >= 2 && kt <= 10) ldA(kt + 5);
      if constexpr (kt >= 2 && kt <= 5) ldW(kt + 10);
      if constexpr (kt >= 11) ldB((kt - 11) >> 1, (kt - 11) & 1);
      if constexpr (kt >= 6) { __builtin_amdgcn_s_sleep(kt >= 11 ? 3 : 2); if (layer == 1) __builtin_amdgcn_s_sleep(1); }
    };
    step(IC<1>{}); step(IC<2>{}); step(IC<3>{}); step(IC<4>{}); step(IC<5>{}); step(IC<6>{}); step(IC<7>{});
    step(IC<8>{}); step(IC<9>{}); step(IC<10>{}); step(IC<11>{}); step(IC<12>{}); step(IC<13>{}); step(IC<14>{});
  } else {
    asm volatile("" : "=v"(vB0), "=v"(vB1), "=v"(vXA), "=v"(po));
    asm volatile("" : "=s"(xa1), "=s"(xa2), "=s"(wq2), "=s"(fm), "=s"(nx.kord), "=s"(nx.wq), "=s"(nx.tw));
    const int wm = w >> 1, wn = w & 1;
    f32x4 acc[4][2];
    unsigned co[2];
#pragma unroll
    for (int cb = 0; cb < 2; ++cb) co[cb] = sb + OFF_C + (unsigned)((wm * 64 + fr) * 128 + (((wn * 4 + cb * 2 + (g >> 1)) ^ (fr & 7)) << 4) + (g & 1) * 8);
    const int fl = (q4 >> 1) | ((g & 1) << 1);
    const int bb0 = (4 * g + q4) * 128 + p4 * 8 + (((wn * 2) ^ fl) << 5), bb1 = (4 * g + q4) * 128 + p4 * 8 + (((wn * 2 + 1) ^ fl) << 5);
    const int abase = wm * 4096 + lane * 16;
    bf16x8 fa[2][2][4], fb[2][2][2];
    unsigned ra[3], rb0[2], rb1[2];
#pragma unroll
    for (int i = 0; i < 3; ++i) { ra[i] = abase + i * 65536; asm volatile("" : "+v"(ra[i])); }
#pragma unroll
    for (int i = 0; i < 2; ++i) { rb0[i] = bb0 + OFF_WO + i * 49152; rb1[i] = bb1 + OFF_WO + i * 49152; asm volatile("" : "+v"(rb0[i]), "+v"(rb1[i])); }
    auto loadf = [&](int kt, bf16x8 (&a)[2][4], bf16x8 (&b)[2][2]) {
#pragma unroll
      for (int s = 0; s < 2; ++s) {
        const char* sA = smem + ra[AS[kt] >> 16] + (AS[kt] & 65535) + s * 8192;
        const int hi = bs(kt) >= OFF_WO + 49152, ob = bs(kt) - OFF_WO - hi * 49152 + s * 4096;
        b[s][0] = cat8(tr_read(smem + rb0[hi] + ob), tr_read(smem + rb0[hi] + ob + 2048));
        b[s][1] = cat8(tr_read(smem + rb1[hi] + ob), tr_read(smem + rb1[hi] + ob + 2048));
#pragma unroll
        for (int rb = 0; rb < 4; ++rb) a[s][rb] = *(const bf16x8*)(sA + rb * 1024);
      }
    };
    auto mma = [&](bf16x8 (&a)[2][4], bf16x8 (&b)[2][2]) {
#pragma unroll
      for (int i = 0; i < 16; ++i) {
        const int rb = i >> 2, cb = ((i >> 1) ^ rb) & 1, s = ((i + 1) >> 1) & 1;
        acc[rb][cb] = MFMA(b[s][cb], a[s][rb], acc[rb][cb]);
      }
    };
    unsigned fo = lane < 16 && lane != hp ? WS_FLAGS + (unsigned)(rt * 16 + lane) * FLS : 0u;
    const u64 ko = korder(bid & 3, hp);
    unsigned kf = kfirst(ko), kf2 = kfirst(ko >> 16) | (1u << knib(ko, 9));
    unsigned jo = (unsigned)((((rt & 7) * 128 + (w - 1) * 64 + lane) * 3072 + (hp & 3) * 1024 + (hp | 3) * 64) * 2);
    unsigned fo1 = ((kf >> (lane & 31)) & 1) ? fo : 0u, fo2 = ((kf2 >> (lane & 31)) & 1) ? fo : 0u;
    fo = (((kf | kf2) >> (lane & 31)) & 1) ? 0u : fo;
    asm volatile("" : "+v"(fo), "+v"(fo1), "+v"(fo2));
    asm volatile("" : "+s"(kf), "+s"(kf2));
    publish(rsW, tid, rt * 16 + hp, E + (unsigned)(layer + 1));
    ST(layer * 24 + 13)
    u64 fl1;
    asm volatile("" : "=v"(fl1));
    if (w == 0) asm volatile("global_load_dwordx2 %0, %1, %2 sc1" : "+v"(fl1) : "v"(fo1), "s"(ws) : "memory");
    if (layer == 0) {
      const unsigned xo = (unsigned)(((m0 + wm * 64 + fr) * DM + hp * 64 + wn * 32 + g * 8) * 2);
      asm volatile("global_load_dwordx4 v[64:67], %1, %3\n\tglobal_load_dwordx4 v[68:71], %1, %4\n\tglobal_load_dwordx4 v[72:75], %2, %3\n\tglobal_load_dwordx4 v[76:79], %2, %4"
                   : "+{v[64:79]}"(xq) : "v"(xo), "v"(xo + 32 * DM * 2), "s"(x0), "s"(x0 + 16 * DM) : "memory");
    }
    loadf(0, fa[0], fb[0]);
    unsigned junk;
    asm volatile("" : "=v"(junk));
    if (layer < 2 && w == 3 && (bid >> 6) == 0) code_touch(junk, bid, lane);
    if (layer == 0 && (hp & 7) < 3 && (w == 1 || w == 2))
      asm volatile("global_load_dword %0, %1, %2" : "+v"(junk) : "v"(jo), "s"(wnx) : "memory");
    __builtin_amdgcn_sched_barrier(0);
    bf16x8 id[2];
#pragma unroll
    for (int cb = 0; cb < 2; ++cb) {
      const unsigned one = (g >> 1) == cb && (g & 1) == (fr >> 3) ? 0x3F80u << ((fr & 1) * 16) : 0u;
      u32x4 t;
#pragma unroll
      for (int j = 0; j < 4; ++j) t[j] = ((fr >> 1) & 3) == j ? one : 0u;
      id[cb] = __builtin_bit_cast(bf16x8, t);
      asm volatile("" : "+v"(id[cb]));
    }
    asm volatile("" : "+v"(co[0]), "+v"(co[1]));
#pragma unroll
    for (int rb = 0; rb < 4; ++rb) { acc[rb][0] = f32x4{0, 0, 0, 0}; acc[rb][1] = f32x4{0, 0, 0, 0}; }
    mma(fa[0], fb[0]);
    __builtin_amdgcn_sched_barrier(0);
    WAITL0;
    u64 miss = 0;
    if (w == 0) { ARRIVED("+v"(fl1)); if (__ballot(fl1 != E + (unsigned)(layer + 1)) & kf) poll_eq(ws, fo1, E + (unsigned)(layer + 1), kf, kf); miss = 0xffffu & ~(kf | kf2 | (1u << hp)); }
    ST(layer * 24 + 14)
    __builtin_amdgcn_sched_barrier(0);
    BAR;
    __builtin_amdgcn_sched_barrier(0);
    asm volatile("" ::: "memory");
    if (layer == 0) WAITV(0);
    __builtin_amdgcn_sched_barrier(0);
    asm volatile("" : "+{v[64:79]}"(xq));
    const bf16x8 xf[4] = {__builtin_bit_cast(bf16x8, quad<0>(xq)), __builtin_bit_cast(bf16x8, quad<1>(xq)), __builtin_bit_cast(bf16x8, quad<2>(xq)), __builtin_bit_cast(bf16x8, quad<3>(xq))};
#pragma unroll
    for (int rb = 0; rb < 4; ++rb) { acc[rb][0] = MFMA(id[0], xf[rb], acc[rb][0]); acc[rb][1] = MFMA(id[1], xf[rb], acc[rb][1]); }
    if (w == 0) poll_eq(ws, fo2, E + (unsigned)(layer + 1), kf2, kf2);
    u64 fl2;
    asm volatile("" : "=v"(fl2));
    auto step = [&](auto ktc) {
      constexpr int kt = decltype(ktc)::value;
      __builtin_amdgcn_sched_barrier(0);
      if (kt == 5 && miss) {
        ARRIVED("+v"(fl2));
        if (__ballot(fl2 != E + (unsigned)(layer + 1)) & miss) poll_eq(ws, fo, E + (unsigned)(layer + 1), miss, ~0ull);
      }
      WAITL0;
      BAR;
      if (kt == 2 && miss) asm volatile("global_load_dwordx2 %0, %1, %2 sc1" : "+v"(fl2) : "v"(fo), "s"(ws) : "memory");
      if (kt == 0) { ST(layer * 24 + 15) }
      __builtin_amdgcn_sched_barrier(0);
      loadf(kt + 1, fa[(kt + 1) & 1], fb[(kt + 1) & 1]);
      if (kt == 0) return;
      mma(fa[kt & 1], fb[kt & 1]);
#pragma unroll
      for (int i = 0; i < 4; ++i) {
        __builtin_amdgcn_sched_group_barrier(0x008, 1, 0);
        __builtin_amdgcn_sched_group_barrier(0x100, 1, 0);
        __builtin_amdgcn_sched_group_barrier(0x008, 1, 0);
        __builtin_amdgcn_sched_group_barrier(0x100, 3, 0);
      }
#pragma unroll
      for (int i = 0; i < 8; ++i) __builtin_amdgcn_sched_group_barrier(0x008, 1, 0);
    };
    step(IC<0>{}); step(IC<1>{}); step(IC<2>{}); step(IC<3>{}); step(IC<4>{}); step(IC<5>{}); step(IC<6>{}); step(IC<7>{});
    step(IC<8>{}); step(IC<9>{}); step(IC<10>{}); step(IC<11>{}); step(IC<12>{}); step(IC<13>{}); step(IC<14>{});
    WAITV(0);
    asm volatile("" :: "v"(junk));
    __builtin_amdgcn_sched_barrier(0);
    auto put = [&](int rb) {
#pragma unroll
      for (int cb = 0; cb < 2; ++cb) lds<u32x2>(co[cb] + rb * 2048) = u32x2{pk2(acc[rb][cb][0], acc[rb][cb][1]), pk2(acc[rb][cb][2], acc[rb][cb][3])};
    };
#pragma unroll
    for (int rb = 0; rb < 4; ++rb)
#pragma unroll
      for (int cb = 0; cb < 2; ++cb) acc[rb][cb] = MFMA(fb[1][0][cb], fa[1][0][rb], acc[rb][cb]);
#pragma unroll
    for (int rb = 0; rb < 4; ++rb) {
      __builtin_amdgcn_sched_barrier(0);
#pragma unroll
      for (int cb = 0; cb < 2; ++cb) acc[rb][cb] = MFMA(fb[1][1][cb], fa[1][1][rb], acc[rb][cb]);
      __builtin_amdgcn_sched_barrier(0);
      if (rb >= 2) put(rb - 2);
    }
    __builtin_amdgcn_sched_barrier(0);
    ST(layer * 24 + 16)
    put(2); put(3);
  }
  WAITL0; BAR;
  asm volatile("" ::: "memory");
  u32x4 xn[2];
#pragma unroll
  for (int i = 0; i < 2; ++i) xn[i] = lds<u32x4>(pxr + OFF_C + i * 1024);
  if (layer == 3) {
    const __amdgpu_buffer_rsrc_t rsO = __builtin_amdgcn_make_buffer_rsrc((void*)uni(out), (short)0, SEQ * DM * 2, 0x00020000);
    __builtin_amdgcn_raw_buffer_store_b128(xn[0], rsO, rmo, 0, 16);
    __builtin_amdgcn_raw_buffer_store_b128(xn[1], rsO, rmo + 8 * DM * 2, 0, 16);
  } else {
    __builtin_amdgcn_raw_buffer_store_b128(xn[0], rsW, WS_X + layer * (SEQ * DM * 2) + imo, 0, 16);
    __builtin_amdgcn_raw_buffer_store_b128(xn[1], rsW, WS_X + layer * (SEQ * DM * 2) + imo + 1024, 0, 16);
    float mean[2], m2[2];
#pragma unroll
    for (int i = 0; i < 2; ++i) {
      typedef __attribute__((ext_vector_type(2))) __bf16 bf2;
      const bf2 one2 = __builtin_bit_cast(bf2, 0x3F803F80u);
      float sa = zero1(), sq = zero1();
#pragma unroll
      for (int e = 0; e < 4; ++e) {
        const unsigned u = xn[i][e];
        const bf2 t = __builtin_bit_cast(bf2, u);
        sa = __builtin_amdgcn_fdot2_f32_bf16(t, one2, sa, false);
        sq = __builtin_amdgcn_fdot2_f32_bf16(t, t, sq, false);
      }
      mean[i] = sa;
      m2[i] = sq;
    }
#define DPA(c) "v_add_f32_dpp %0, %0, %0 " c "\n\tv_add_f32_dpp %1, %1, %1 " c "\n\tv_add_f32_dpp %2, %2, %2 " c "\n\tv_add_f32_dpp %3, %3, %3 " c "\n\t"
    asm volatile("s_nop 3\n\t" DPA("quad_perm:[1,0,3,2] row_mask:0xf bank_mask:0xf") DPA("quad_perm:[2,3,0,1] row_mask:0xf bank_mask:0xf")
                 DPA("row_half_mirror row_mask:0xf bank_mask:0xf") "s_nop 0" : "+v"(mean[0]), "+v"(m2[0]), "+v"(mean[1]), "+v"(m2[1]));
    if ((lane & 7) < 2) {
      const int i = lane & 1;
      const u32x2 sv = {__float_as_uint(i ? mean[1] : mean[0]), __float_as_uint(i ? m2[1] : m2[0])};
      __builtin_amdgcn_raw_buffer_store_b64(sv, rsW, WS_ST + layer * (SEQ * 128) + (unsigned)(((rt * 16 + hp) * 128 + r0 + i * 8) * 8), 0, 16);
    }
  }
  ST(layer * 24 + 17)
  if (layer < 3 && w >= 4) {
    WAITV(0); BAR;
    u64 fl;
    asm volatile("" : "=v"(fl));
    if (lw == 0) { __builtin_amdgcn_s_sleep(7); asm volatile("global_load_dwordx2 %0, %1, %2 sc1" : "+v"(fl) : "v"(po), "s"(ws) : "memory"); }
    glds4v(wq2, vB0, vB1, vB0, vB1, sb + 2 * 40960 + 16384 + lw * 6144);
    glds2v(wq2 + 4096, vB0, vB1, sb + 2 * 40960 + 16384 + lw * 6144 + 4096);
    if (lw == 0) {
      WAITV(6);
      __builtin_amdgcn_sched_barrier(0);
      asm volatile("" : "+v"(fl));
      if (__ballot(fl != E + (unsigned)(layer + 1)) & fm) poll_eq(ws, po, E + (unsigned)(layer + 1), fm, ~0ull);
    }
    BAR;
    glds4c(xa1, vXA, sb + 40960 + lw * 4096); glds4c(xa2, vXA, sb + 2 * 40960 + lw * 4096);
  }
  ST(layer * 24 + 18)
}

__global__ __launch_bounds__(512, 2) void mega_kernel(const u16* __restrict__ x0, const u16* __restrict__ wqkvA,
                                                      const u16* __restrict__ woA, const u16* __restrict__ wqkvB,
                                                      const u16* __restrict__ wphi, const u16* __restrict__ woB,
                                                      u16* __restrict__ out, char* __restrict__ ws) {
  extern __shared__ __attribute__((aligned(16))) char smem[];
  {
    unsigned long long pc;
    unsigned t;
    asm volatile("s_getpc_b64 %0\n.Lmk_pc:\n\tv_lshl_add_u32 %1, %2, 9, %3\n\tv_lshlrev_b32 %1, 2, %1\n\tv_and_b32 %1, 0xffffff80, %1\n\ts_mov_b32 m0, %4\n\t"
                 "v_min_u32 %1, .Lmk_end-.Lmk_pc-4, %1\n\tglobal_load_lds_dword %1, %0" : "=&s"(pc), "=&v"(t) : "s"(blockIdx.x >> 3), "v"(threadIdx.x), "n"(LDS_TOTAL - 1024) : "memory", "m0");
  }
  const int w = __builtin_amdgcn_readfirstlane(threadIdx.x >> 6);
  u64 E = 0;
  {
    const int bid = blockIdx.x, lane = threadIdx.x & 63;
    const int rt = 2 * (bid & 4) + ((bid >> 3) & 7), hp = 4 * (bid & 3) + (bid >> 6), m0 = rt * 128;
    if (w < 4) {
      const unsigned xo = (unsigned)((m0 + hp * 8 + w * 2) * (DM * 2) + lane * 16);
      glds4v(x0, xo, xo, xo, xo, lds_addr(smem) + 122880 + w * 4096);
    } else {
      const int lw = w & 3;
      const int kB = 16 * lw + (lane >> 3), fB = (kB >> 1) & 1;
      const unsigned vB0 = (unsigned)((kB * 3072 + hp * 64 + (((((lane & 7) >> 1) ^ fB) << 1) | (lane & 1)) * 8) * 2);
      const unsigned vB1 = (unsigned)(((kB + 8) * 3072 + hp * 64 + (((((lane & 7) >> 1) ^ (fB | 2)) << 1) | (lane & 1)) * 8) * 2) - 1024u;
      const unsigned vA = (unsigned)(((m0 + lw * 32 + (lane >> 3)) * DM + ((lane & 7) ^ (lane >> 3)) * 8) * 2);
#pragma unroll
      for (int s = 0; s < 2; ++s) {
        const int kt = (rt & 8) + s;
        const unsigned sA = lds_addr(smem) + s * 40960, sB = sA + 16384;
        glds4v(x0 + kt * 64, vA, vA + 15360u, vA + 2 * 15360u, vA + 3 * 15360u, sA + lw * 4096);
        const u16* b = wqkvA + (size_t)kt * 64 * 3072;
        glds4v(b, vB0, vB1, vB0, vB1, sB + lw * 6144);
        glds2v(b + 2048, vB0, vB1, sB + lw * 6144 + 4096);
      }
    }
  }
  f32x16 xq;
  Next nx;
  asm volatile("" : "=s"(nx.kord), "=s"(nx.wq), "=s"(nx.tw));
#pragma unroll 1
  for (int layer = 0; layer < 4; ++layer) {
    if (layer & 1) g1_phase<1>(smem, ws, E, w, xq, nx, layer, x0, wqkvB, woB, wphi, wqkvA, woA);
    else g1_phase<0>(smem, ws, E, w, xq, nx, layer, x0, wqkvA, woA, nullptr, wqkvB, woB);
    g2_phase(smem, ws, E, w, xq, nx, layer, x0, (layer & 1) ? woB : woA, (layer & 1) ? wqkvA : wqkvB, out);
  }
  asm volatile(".Lmk_end:");
  WAITV(0);
  if (blockIdx.x == 0 && w == 0 && lane_id() == 0) __hip_atomic_store((gu64*)ws, E + 16u, __ATOMIC_RELAXED, __HIP_MEMORY_SCOPE_AGENT);
}

extern "C" void kernel_launch(void* const* d_in, const int* in_sizes, int n_in,
                              void* d_out, int out_size, void* d_ws, size_t ws_size,
                              hipStream_t stream) {
  hipFuncSetAttribute((const void*)mega_kernel, hipFuncAttributeMaxDynamicSharedMemorySize, LDS_TOTAL);
  mega_kernel<<<256, 512, LDS_TOTAL, stream>>>((const u16*)d_in[0], (const u16*)d_in[1], (const u16*)d_in[2], (const u16*)d_in[4],
                                               (const u16*)d_in[5], (const u16*)d_in[6], (u16*)d_out, (char*)d_ws);
}
```

```cpp
#include <hip/hip_runtime.h>
#include <hip/hip_bf16.h>
#include <stdint.h>
#include <type_traits>
#pragma clang diagnostic ignored "-Winline-asm"
#pragma clang diagnostic ignored "-Wunused-value"

typedef unsigned short u16;
typedef __attribute__((ext_vector_type(8))) short bf16x8;
typedef __attribute__((ext_vector_type(4))) short bf16x4;
typedef __attribute__((ext_vector_type(4))) float f32x4;
typedef __attribute__((ext_vector_type(4))) unsigned u32x4;
typedef __attribute__((ext_vector_type(2))) unsigned u32x2;
typedef __attribute__((ext_vector_type(8))) float f32x8;
typedef __attribute__((ext_vector_type(16))) unsigned u32x16;
typedef __attribute__((ext_vector_type(16))) float f32x16;
typedef __attribute__((ext_vector_type(32))) float f32x32;
template <int I, typename T> __device__ __forceinline__ f32x4 quad(const T& v) { return __builtin_shufflevector(v, v, 4 * I, 4 * I + 1, 4 * I + 2, 4 * I + 3); }

constexpr int SEQ = 2048, DM = 1024, NH = 32;
#define ST(i)
constexpr int KVP = 64 * 32 + 64;

typedef __attribute__((ext_vector_type(2))) float f32x2;
typedef __attribute__((ext_vector_type(2))) __bf16 bf16x2_t;
__device__ __forceinline__ unsigned pk2(float a, float b) {
  f32x2 v = {a, b};
  bf16x2_t r = __builtin_convertvector(v, bf16x2_t);
  return __builtin_bit_cast(unsigned, r);
}

__device__ __forceinline__ bf16x4 tr_read(const char* p) {
  return __builtin_amdgcn_ds_read_tr16_b64_v4i16((__attribute__((address_space(3))) bf16x4*)p);
}
template <typename T> __device__ __forceinline__ __attribute__((address_space(3))) T& lds(unsigned a) { return *(__attribute__((address_space(3))) T*)(size_t)a; }
__device__ __forceinline__ bf16x4 tr_read(unsigned a) { return __builtin_amdgcn_ds_read_tr16_b64_v4i16(&lds<bf16x4>(a)); }
__device__ __forceinline__ bf16x8 cat8(bf16x4 a, bf16x4 b) {
  bf16x8 f = {a[0], a[1], a[2], a[3], b[0], b[1], b[2], b[3]};
  return f;
}
__device__ __forceinline__ void glds16s(const void* sbase, unsigned voff, unsigned lds_base) {
  asm volatile("s_mov_b32 m0, %2\n\ts_nop 0\n\tglobal_load_lds_dwordx4 %0, %1" :: "v"(voff), "s"(sbase), "s"(lds_base) : "memory", "m0");
}
__device__ __forceinline__ void glds16c(const void* sbase, unsigned voff, unsigned lds_base) {
  asm volatile("s_mov_b32 m0, %2\n\ts_nop 0\n\tglobal_load_lds_dwordx4 %0, %1 sc1" :: "v"(voff), "s"(sbase), "s"(lds_base) : "memory", "m0");
}
#define GL_ "\n\tglobal_load_lds_dwordx4 "
__device__ __forceinline__ void glds4c(const void* sbase, unsigned v, unsigned lds_base) {
  asm volatile("s_mov_b32 m0, %2\n\ts_nop 0" GL_ "%0, %1 sc1" GL_ "%0, %1 offset:1024 sc1" GL_ "%0, %1 offset:2048 sc1" GL_ "%0, %1 offset:3072 sc1"
               :: "v"(v), "s"(sbase), "s"(lds_base) : "memory", "m0");
}
__device__ __forceinline__ void glds4v(const void* sbase, unsigned v0, unsigned v1, unsigned v2, unsigned v3, unsigned lds_base) {
  asm volatile("s_mov_b32 m0, %5\n\ts_nop 0" GL_ "%0, %4 sc1" GL_ "%1, %4 offset:1024 sc1" GL_ "%2, %4 offset:2048 sc1" GL_ "%3, %4 offset:3072 sc1"
               :: "v"(v0), "v"(v1), "v"(v2), "v"(v3), "s"(sbase), "s"(lds_base) : "memory", "m0");
}
__device__ __forceinline__ void glds2v(const void* sbase, unsigned v0, unsigned v1, unsigned lds_base) {
  asm volatile("s_mov_b32 m0, %3\n\ts_nop 0" GL_ "%0, %2 sc1" GL_ "%1, %2 offset:1024 sc1" :: "v"(v0), "v"(v1), "s"(sbase), "s"(lds_base) : "memory", "m0");
}
__device__ __forceinline__ void gld16(u32x4& d, unsigned voff, const void* sbase) {
  asm volatile("global_load_dwordx4 %0, %1, %2" : "=v"(d) : "v"(voff), "s"(sbase) : "memory");
}
__device__ __forceinline__ unsigned lds_addr(const void* p) { return (unsigned)(size_t)p; }
#define WAITV(n) asm volatile("s_waitcnt vmcnt(" #n ")" ::: "memory")
#define WAITVN(n) asm volatile("s_waitcnt vmcnt(%0)" :: "n"(n) : "memory")
#define WAITL0 do { asm volatile("" ::: "memory"); __builtin_amdgcn_s_waitcnt(0xC07F); asm volatile("" ::: "memory"); } while (0)
#define BAR __builtin_amdgcn_s_barrier()
#define ARRIVED(...) do { WAITV(0); __builtin_amdgcn_sched_barrier(0); asm volatile("" : __VA_ARGS__); } while (0)

template <int N> __device__ __forceinline__ float dpp_ror(float x) {
  return __uint_as_float(__builtin_amdgcn_update_dpp(0u, __float_as_uint(x), 0x120 + N, 0xf, 0xf, false));
}
__device__ __forceinline__ float r16_sum(float x) { x += dpp_ror<8>(x); x += dpp_ror<4>(x); x += dpp_ror<2>(x); return x + dpp_ror<1>(x); }
__device__ __forceinline__ void max8pn(const f32x4& a, const f32x4& b, float& mx, float& mn) {
  asm("v_max3_f32 %0, %2, %3, %4\n\tv_max3_f32 %1, -%2, -%3, -%4\n\tv_max3_f32 %0, %0, %5, %6\n\tv_max3_f32 %1, %1, -%5, -%6\n\t"
      "v_max3_f32 %0, %0, %7, %8\n\tv_max3_f32 %1, %1, -%7, -%8\n\tv_max_f32 %0, %0, %9\n\tv_max_f32 %1, %1, -%9\n\ts_nop 0"
      : "=&v"(mx), "=&v"(mn) : "v"(a[0]), "v"(a[1]), "v"(a[2]), "v"(a[3]), "v"(b[0]), "v"(b[1]), "v"(b[2]), "v"(b[3]));
}
__device__ __forceinline__ float vmaxf_n(float a, float b) { float r; asm("v_max_f32 %0, %1, %2\n\ts_nop 0" : "=v"(r) : "v"(a), "v"(b)); return r; }
template <bool MAXOP> __device__ __forceinline__ float rop(float a, float b) { return MAXOP ? vmaxf_n(a, b) : a + b; }
template <bool MAXOP> __device__ __forceinline__ void xr4(float& a, float& b, float& c, float& d) {
  auto r = __builtin_amdgcn_permlane16_swap(__float_as_uint(a), __float_as_uint(b), false, false);
  auto q = __builtin_amdgcn_permlane16_swap(__float_as_uint(c), __float_as_uint(d), false, false);
  const unsigned ab = __float_as_uint(rop<MAXOP>(__uint_as_float(r[0]), __uint_as_float(r[1])));
  const unsigned cd = __float_as_uint(rop<MAXOP>(__uint_as_float(q[0]), __uint_as_float(q[1])));
  auto t = __builtin_amdgcn_permlane32_swap(ab, cd, false, false);
  const unsigned u = __float_as_uint(rop<MAXOP>(__uint_as_float(t[0]), __uint_as_float(t[1])));
  auto h = __builtin_amdgcn_permlane32_swap(u, u, false, false);
  auto p0 = __builtin_amdgcn_permlane16_swap(h[0], h[0], false, false);
  auto p1 = __builtin_amdgcn_permlane16_swap(h[1], h[1], false, false);
  a = __uint_as_float(p0[0]); b = __uint_as_float(p0[1]); c = __uint_as_float(p1[0]); d = __uint_as_float(p1[1]);
}

__device__ __forceinline__ unsigned xs4(float a, float b, float c, float d) {
  auto r = __builtin_amdgcn_permlane16_swap(__float_as_uint(a), __float_as_uint(b), false, false);
  auto q = __builtin_amdgcn_permlane16_swap(__float_as_uint(c), __float_as_uint(d), false, false);
  auto t = __builtin_amdgcn_permlane32_swap(__float_as_uint(__uint_as_float(r[0]) + __uint_as_float(r[1])), __float_as_uint(__uint_as_float(q[0]) + __uint_as_float(q[1])), false, false);
  return __float_as_uint(__uint_as_float(t[0]) + __uint_as_float(t[1]));
}

__device__ __forceinline__ f32x4 sum4(const u32x4 (&v)[4]) {
  float r0, r1, r2, r3, t0, t1, t2, t3;
  asm("v_add_f32 %0, %8, %12\n\tv_add_f32 %1, %9, %13\n\tv_add_f32 %2, %10, %14\n\tv_add_f32 %3, %11, %15\n\t"
      "v_add_f32 %4, %16, %20\n\tv_add_f32 %5, %17, %21\n\tv_add_f32 %6, %18, %22\n\tv_add_f32 %7, %19, %23\n\t"
      "v_add_f32 %0, %0, %4\n\tv_add_f32 %1, %1, %5\n\tv_add_f32 %2, %2, %6\n\tv_add_f32 %3, %3, %7\n\ts_nop 0"
      : "=&v"(r0), "=&v"(r1), "=&v"(r2), "=&v"(r3), "=&v"(t0), "=&v"(t1), "=&v"(t2), "=&v"(t3)
      : "v"(v[0][0]), "v"(v[0][1]), "v"(v[0][2]), "v"(v[0][3]), "v"(v[1][0]), "v"(v[1][1]), "v"(v[1][2]), "v"(v[1][3]),
        "v"(v[2][0]), "v"(v[2][1]), "v"(v[2][2]), "v"(v[2][3]), "v"(v[3][0]), "v"(v[3][1]), "v"(v[3][2]), "v"(v[3][3]));
  return f32x4{r0, r1, r2, r3};
}
#define MFMA(a, b, c) __builtin_amdgcn_mfma_f32_16x16x32_bf16(a, b, c, 0, 0, 0)

typedef unsigned long long u64;
typedef __attribute__((address_space(1))) unsigned long long gu64;
constexpr unsigned WS_FLAGS = 4096;
constexpr int FLS = 64;
constexpr unsigned WS_G0 = 65536;
constexpr unsigned WS_CSP = WS_G0 + SEQ * 16;
constexpr unsigned WS_ST = WS_CSP + 16 * 16 * 768;
constexpr unsigned WS_X = 1u << 21;
constexpr unsigned WS_O = WS_X + 3u * SEQ * DM * 2;
constexpr unsigned KVPB = KVP * 2, KVFB = KVP * 4;
constexpr unsigned EMPTY = 0xffffffffu;
constexpr unsigned WS_KV = WS_O + 4u * SEQ * DM * 2;
constexpr unsigned KVLB = 16u * NH * KVPB + 16u * KVFB;
constexpr unsigned WS_HL = WS_KV + 2u * KVLB;
constexpr unsigned WS_CSA = WS_HL + 512u * 4096;
constexpr unsigned WS_CPA = WS_CSA + 256u * 1024;
constexpr unsigned WS_IMG = WS_CPA + 16 * 16 * 768;
constexpr unsigned WS_END = WS_IMG + 2048 * 1024;
constexpr int LDS_TOTAL = 163840;
constexpr int OFF_WO = 49152;
struct Next { u64 kord; const char* wq; unsigned tw; };
template <int V> using IC = std::integral_constant<int, V>;
__device__ __forceinline__ bf16x8 ones8() { unsigned e = 0x3F803F80u; asm volatile("" : "+v"(e)); return __builtin_bit_cast(bf16x8, u32x4{e, e, e, e}); }
__device__ __forceinline__ float zero1() { float z; asm volatile("v_mov_b32 %0, 0" : "=v"(z)); return z; }
__device__ __forceinline__ u32x4 empty4() { unsigned e = EMPTY; asm volatile("" : "+v"(e)); return u32x4{e, e, e, e}; }
__device__ __forceinline__ u64 korder(int v, int own) {
  const u64 t = v == 0 ? 0x3b210fedca987654ull : v == 1 ? 0xb37654210fedca98ull : v == 2 ? 0x3ba987654210fedcull : 0xb3fedca987654210ull;
  const u64 inv = v == 0 ? 0xa987e6543210fdcbull : v == 1 ? 0x6543f210dcbae987ull : v == 2 ? 0x3210edcba987f654ull : 0xdcbaf9876543e210ull;
  const u64 low = (1ull << (4 * ((inv >> (4 * own)) & 15))) - 1ull;
  return (u64)own | ((t & low) << 4) | (t & ~((low << 4) | 15ull));
}
__device__ __forceinline__ u64 korder0(int rt) {
  const int r = rt & 7;
  const u64 own = (0x765432765432ull >> (4 * (r < 6 ? r : r - 6))) & 0xffffffull, oth = 0x7654321076543210ull >> (4 * r);
  const u64 t = 0x10ull | ((oth & 15ull) << 8) | (own << 12) | ((oth >> 4) << 36);
  return t | (rt < 8 ? 0x8888888000000800ull : 0x0000000888888088ull);
}
__device__ __forceinline__ int knib(u64 t, int s) { return (int)(t >> (4 * s)) & 15; }
__device__ __forceinline__ unsigned kfirst(u64 t) { return (1u << knib(t, 1)) | (1u << knib(t, 2)) | (1u << knib(t, 3)) | (1u << knib(t, 4)); }
__device__ __forceinline__ int kv1(int bid) { return (bid + (0x4924 >> ((bid >> 2) & 14))) & 3; }

__device__ __forceinline__ bool img(int layer, int hp) { return layer >= 2 && (hp & 7) == 3; }
__device__ __forceinline__ const char* wq_src(const char* ws, const void* W, int layer, int hp) { return img(layer, hp) ? ws + WS_IMG + ((layer & 1) * 768 + (hp >> 3) * 384) * 1024 : (const char*)W; }
__device__ __forceinline__ const char* wo_src(const char* ws, const void* W, int layer, int hp) { return img(layer, hp) ? ws + WS_IMG + (1536 + (layer & 1) * 256 + (hp >> 3) * 128) * 1024 : (const char*)W; }

template <typename T> __device__ __forceinline__ T* uni(T* p) {
  const unsigned long long v = (unsigned long long)p;
  const unsigned lo = __builtin_amdgcn_readfirstlane((unsigned)v), hi = __builtin_amdgcn_readfirstlane((unsigned)(v >> 32));
  return (T*)(((unsigned long long)hi << 32) | lo);
}
__device__ __forceinline__ int lane_id() {
  int l;
  asm volatile("v_mbcnt_lo_u32_b32 %0, -1, 0\n\tv_mbcnt_hi_u32_b32 %0, -1, %0" : "=v"(l));
  __builtin_assume(l >= 0 && l < 64);
  return l;
}
#define IDS \
  const int lane = lane_id(); \
  asm volatile("" : "+s"(w)); \
  const int tid = w * 64 + lane; \
  int bid = blockIdx.x; asm volatile("" : "+s"(bid)); \
  ws = uni(ws); asm volatile("" : "+s"(ws)); \
  layer = __builtin_amdgcn_readfirstlane(layer); x0 = uni(x0); wo = uni(wo); \
    \
  const int rt = 2 * (bid & 4) + ((bid >> 3) & 7); \
  const int hp = 4 * (bid & 3) + (bid >> 6); \
  const int m0 = rt * 128; \
  const int fr = lane & 15, g = lane >> 4; \
  const int q4 = (lane & 15) >> 2, p4 = lane & 3; \
  const __amdgpu_buffer_rsrc_t rsW = __builtin_amdgcn_make_buffer_rsrc((void*)ws, (short)0, (int)WS_END, 0x00020000); \
  (void)m0; (void)fr; (void)q4; (void)p4; (void)rsW; (void)g;

__device__ __forceinline__ void code_touch(unsigned& junk, int bid, int lane) {
  unsigned long long pc;
  unsigned len, d;
  asm volatile("s_getpc_b64 %0\n.Lct%=:\n\ts_mov_b32 %2, .Lct%=-.Lmk_pc\n\ts_mov_b32 %1, .Lmk_end-.Lmk_pc" : "=s"(pc), "=s"(len), "=s"(d));
  pc -= d;
  const unsigned per = ((len >> 10) + 1) * 128, off = ((bid >> 3) & 7) * per + (lane * 128 < per ? lane * 128 : 0);
  asm volatile("global_load_dword %0, %1, %2" : "+v"(junk) : "v"(off < len - 4 ? off : 0u), "s"(pc) : "memory");
}

__device__ __forceinline__ void publish(__amdgpu_buffer_rsrc_t rsW, int tid, unsigned idx, u64 v) {
  unsigned off = WS_FLAGS + idx * FLS;
  asm volatile("" : "+v"(off), "+v"(v));
  WAITV(0);
  WAITL0; BAR;
  if (tid == 192) __builtin_amdgcn_raw_buffer_store_b64(__builtin_bit_cast(u32x2, v), rsW, off, 0, 16);
  asm volatile("" ::: "memory");
}
__device__ __forceinline__ void poll_flags(char* ws, u64 E, int set, int rt, int hp, int lane, unsigned n, bool col) {
  const int l = lane & 15;
  const unsigned fo = WS_FLAGS + (unsigned)(set * 256 + (col ? l * 16 + hp : rt * 16 + l)) * FLS;
  _Pragma("unroll 1") for (unsigned spins = 0; spins < (1u << 18); ++spins) {
    u64 d;
    asm volatile("global_load_dwordx2 %0, %1, %2 sc1\n\ts_waitcnt vmcnt(0)" : "=v"(d) : "v"(fo), "s"(ws) : "memory");
    if (__all(d - (E + n) <= (u64)(16u - n))) break;
    __builtin_amdgcn_s_sleep(1);
  }
}

__device__ __forceinline__ u64 poll_eq(const char* ws, unsigned off, u64 want, u64 mask, u64 now) {
  u64 miss;
  _Pragma("unroll 1") for (unsigned spins = 0; spins < (1u << 18); ++spins) {
    u64 d;
    asm volatile("global_load_dwordx2 %0, %1, %2 sc1\n\ts_waitcnt vmcnt(0)" : "=v"(d) : "v"(off), "s"(ws) : "memory");
    miss = __ballot(d != want) & mask;
    if (!(miss & now)) break;
    __builtin_amdgcn_s_sleep(1);
  }
  return miss;
}

#define G1_MACROS \
  ".macro G1M rb, j, s, z\n" \
  " .set g1d, 160+24*(\\rb)+4*(((\\j)+2)-6*((\\j)>>2))\n .set g1a, 80+40*(\\s)+4*(\\rb)\n .set g1b, 96+40*(\\s)+4*(\\j)\n" \
  " .if ((\\j)>>1) == 1\n  .set g1t, g1a\n  .set g1a, g1b\n  .set g1b, g1t\n .endif\n" \
  " .if \\z\n  v_mfma_f32_16x16x32_bf16 v[g1d:g1d+3], v[g1b:g1b+3], v[g1a:g1a+3], 0\n" \
  " .else\n  v_mfma_f32_16x16x32_bf16 v[g1d:g1d+3], v[g1b:g1b+3], v[g1a:g1a+3], v[g1d:g1d+3]\n .endif\n" \
  ".endm\n" \
  ".macro G1RA rb, s, ra, oa\n" \
  " ds_read_b128 v[80+40*(\\s)+4*(\\rb):83+40*(\\s)+4*(\\rb)], \\ra offset:(\\oa)+(\\rb)*2048\n" \
  ".endm\n" \
  ".macro G1RB k, s, r0, r1, ob\n" \
  " .if (\\k) & 2\n  ds_read_b64_tr_b16 v[96+40*(\\s)+2*(\\k):97+40*(\\s)+2*(\\k)], \\r1 offset:(\\ob)+(((\\k)>>2)+1-3*((\\k)>>3))*2048+((\\k)&1)*512\n" \
  " .else\n  ds_read_b64_tr_b16 v[96+40*(\\s)+2*(\\k):97+40*(\\s)+2*(\\k)], \\r0 offset:(\\ob)+(((\\k)>>2)+1-3*((\\k)>>3))*2048+((\\k)&1)*512\n .endif\n" \
  ".endm\n" \
    \
  ".macro G1BK i, g, r0, r1, ob\n G1RB 4*(\\g)+((\\i)&3), (\\i)>>2, \\r0, \\r1, (\\ob)+((\\i)>>2)*12288\n.endm\n" \
  ".macro G1AK rb, s, a0, a1, oa\n .if \\s\n  G1RA \\rb, 1, \\a1, \\oa\n .else\n  G1RA \\rb, 0, \\a0, \\oa\n .endif\n.endm\n" \
  ".macro G1RDB g, r0, r1, ob\n .set g1i, 0\n .rept 8\n  G1BK g1i, \\g, \\r0, \\r1, \\ob\n  .set g1i, g1i+1\n .endr\n.endm\n" \
  ".macro G1RDA a0, a1, oa\n .set g1i, 0\n .rept 8\n  G1AK (g1i>>1)^2, g1i&1, \\a0, \\a1, \\oa\n  .set g1i, g1i+1\n .endr\n.endm\n" \
    \
  ".macro G1Q g, rb, q, i, z\n G1M \\rb, 2*(\\g)+((((\\i)>>1)^(\\q))&1), (((\\i)+1)>>1)&1, (\\z)&&(((\\i)&1)==0)\n.endm\n" \
    \
  ".macro G1SG g, z, rd, a0, a1, r0, r1, o\n" \
  " .set g1i, 0\n .rept 16\n" \
  "  .if ((\\g) == 0) && (g1i == 8) && (\\rd)\n   s_waitcnt lgkmcnt(7)\n   s_nop 0\n  .endif\n" \
  "  .if ((\\g) == 0) && (g1i == 15)\n   s_waitcnt lgkmcnt(0)\n  .endif\n" \
  "  G1Q \\g, (g1i>>2)^(2-((\\g)&1)), g1i>>2, g1i&3, \\z\n" \
  "  .if \\rd\n" \
  "   .if (\\g) == 0\n    .if g1i < 7\n     G1BK g1i, 2, \\r0, \\r1, \\o\n    .elseif g1i == 8\n     G1BK 7, 2, \\r0, \\r1, \\o\n    .endif\n" \
  "   .elseif (\\g) == 1\n    .if g1i < 8\n     G1BK g1i, 0, \\r0, \\r1, \\o\n    .endif\n" \
  "   .elseif (g1i & 2) == 0\n    G1BK (g1i&1)+2*(g1i>>2), 1, \\r0, \\r1, \\o\n" \
  "   .elseif (\\rd) == 1\n    G1AK (g1i>>2)^2, 1-(g1i&1), \\a0, \\a1, \\o\n   .endif\n" \
  "  .endif\n" \
  "  .set g1i, g1i+1\n .endr\n" \
  ".endm\n" \
  ""
#define G1_SLIDE \
  "G1Q 1, 3, 0, 0, 0\n ds_read2_b64 v[60:63], v49 offset0:32 offset1:48\n ds_read_b128 v[96:99], v48 offset:256\n G1Q 1, 3, 0, 1, 0\n " \
  "ds_read_b128 v[100:103], v48 offset:320\n ds_read2_b32 v[56:57], v52 offset1:16\n G1Q 1, 3, 0, 2, 0\n ds_read_b128 v[136:139], v54 offset:192\n " \
  "ds_read_b128 v[140:143], v54 offset:704\n G1Q 1, 3, 0, 3, 0\n G1Q 1, 2, 1, 0, 0\n G1Q 1, 2, 1, 1, 0\n s_waitcnt lgkmcnt(3)\n " \
  "v_fmac_f32 v240, v96, v62\n G1Q 1, 2, 1, 2, 0\n v_fma_f32 v241, v97, v62, v241\n v_fma_f32 v242, v98, v62, v242\n G1Q 1, 2, 1, 3, 0\n " \
  "v_fma_f32 v243, v99, v62, v243\n v_fma_f32 v244, v100, v62, v244\n G1Q 1, 1, 2, 0, 0\n v_fma_f32 v245, v101, v62, v245\n " \
  "v_fma_f32 v246, v102, v62, v246\n G1Q 1, 1, 2, 1, 0\n v_fma_f32 v247, v103, v62, v247\n v_mul_f32_e64 v240, v63, v240\n G1Q 1, 1, 2, 2, 0\n " \
  "v_mul_f32_e64 v241, v63, v241\n v_mul_f32_e64 v242, v63, v242\n G1Q 1, 1, 2, 3, 0\n v_mul_f32_e64 v243, v63, v243\n v_mul_f32_e64 v244, v63, v244\n " \
  "G1Q 1, 0, 3, 0, 0\n v_mul_f32_e64 v245, v63, v245\n v_mul_f32_e64 v246, v63, v246\n G1Q 1, 0, 3, 1, 0\n v_mul_f32_e64 v247, v63, v247\n " \
  "v_cvt_pk_bf16_f32 v240, v240, v241\n G1Q 1, 0, 3, 2, 0\n v_cvt_pk_bf16_f32 v241, v242, v243\n v_cvt_pk_bf16_f32 v242, v244, v245\n " \
  "G1Q 1, 0, 3, 3, 0\n v_cvt_pk_bf16_f32 v243, v246, v247\n s_waitcnt lgkmcnt(0)\n v_fmac_f32 v248, v136, v56\n G1Q 2, 2, 0, 0, 0\n " \
  "ds_read2_b64 v[144:147], v49 offset1:16\n v_fma_f32 v249, v137, v56, v249\n G1Q 2, 2, 0, 1, 0\n ds_read_b128 v[148:151], v48\n " \
  "v_fma_f32 v250, v138, v56, v250\n G1Q 2, 2, 0, 2, 0\n ds_read_b128 v[104:107], v48 offset:64\n v_fma_f32 v251, v139, v56, v251\n G1Q 2, 2, 0, 3, 0\n " \
  "ds_read_b128 v[108:111], v54 offset:128\n v_fma_f32 v252, v136, v57, v252\n G1Q 2, 3, 1, 0, 0\n v_fma_f32 v253, v137, v57, v253\n " \
  "v_fma_f32 v254, v138, v57, v254\n G1Q 2, 3, 1, 1, 0\n ds_read_b128 v[88:91], v54 offset:640\n v_fma_f32 v255, v139, v57, v255\n G1Q 2, 3, 1, 2, 0\n " \
  "ds_read_b128 v[128:131], v54\n v_mul_f32_e64 v248, v140, v248\n G1Q 2, 3, 1, 3, 0\n v_mul_f32_e64 v249, v141, v249\n v_mul_f32_e64 v250, v142, v250\n " \
  "G1Q 2, 0, 2, 0, 0\n v_mul_f32_e64 v251, v143, v251\n v_mul_f32_e64 v252, v140, v252\n G1Q 2, 0, 2, 1, 0\n ds_read_b128 v[92:95], v54 offset:512\n " \
  "v_mul_f32_e64 v253, v141, v253\n G1Q 2, 0, 2, 2, 0\n v_mul_f32_e64 v254, v142, v254\n v_mul_f32_e64 v255, v143, v255\n G1Q 2, 0, 2, 3, 0\n " \
  "v_cvt_pk_bf16_f32 v248, v248, v249\n v_cvt_pk_bf16_f32 v249, v250, v251\n G1Q 2, 1, 3, 0, 0\n v_cvt_pk_bf16_f32 v255, v254, v255\n " \
  "v_cvt_pk_bf16_f32 v254, v252, v253\n G1Q 2, 1, 3, 1, 0\n v_min_u32_e64 v136, -2, v240\n v_min_u32_e64 v137, -2, v241\n v_min_u32_e64 v138, -2, v242\n " \
  "G1Q 2, 1, 3, 2, 0\n v_min_u32_e64 v139, -2, v243\n v_min_u32_e64 v140, -2, v248\n v_min_u32_e64 v141, -2, v249\n G1Q 2, 1, 3, 3, 0\n " \
  "v_min_u32_e64 v142, -2, v254\n v_min_u32_e64 v143, -2, v255\n ds_read_b128 v[112:115], v54 offset:64\n ds_read_b128 v[116:119], v54 offset:576\n " \
  "s_cmp_eq_u32 %[wm], 0\n s_cbranch_scc1 .Lg1d%=\n buffer_store_dwordx4 v[136:139], v55, %[rs], 0 offen sc1\n " \
  "buffer_store_dwordx4 v[140:143], v55, %[rs], 0 offen offset:1024 sc1\n ds_read_b32 v53, v59\n ds_read_b32 v53, v59\n ds_read_b32 v53, v59\n " \
  "s_branch .Lg1e%=\n .Lg1d%=:\n v_mov_b32 v53, 1\n ds_write_b128 v58, v[136:139]\n ds_write_b128 v58, v[140:143] offset:1024\n ds_write_b32 v59, v53\n " \
  ".Lg1e%=:\n s_waitcnt lgkmcnt(8)\n v_pk_fma_f32 v[232:233], v[148:149], v[62:63], v[232:233] op_sel_hi:[1,0,1]\n " \
  "v_pk_fma_f32 v[234:235], v[150:151], v[62:63], v[234:235] op_sel_hi:[1,0,1]\n " \
  "v_pk_fma_f32 v[236:237], v[104:105], v[62:63], v[236:237] op_sel_hi:[1,0,1]\n " \
  "v_pk_fma_f32 v[238:239], v[106:107], v[62:63], v[238:239] op_sel_hi:[1,0,1]\n " \
  "v_pk_fma_f32 v[216:217], v[96:97], v[60:61], v[216:217] op_sel_hi:[1,0,1]\n " \
  "v_pk_fma_f32 v[218:219], v[98:99], v[60:61], v[218:219] op_sel_hi:[1,0,1]\n " \
  "v_pk_fma_f32 v[220:221], v[100:101], v[60:61], v[220:221] op_sel_hi:[1,0,1]\n " \
  "v_pk_fma_f32 v[222:223], v[102:103], v[60:61], v[222:223] op_sel_hi:[1,0,1]\n v_pk_mul_f32 v[216:217], v[60:61], v[216:217] op_sel:[1,0]\n " \
  "v_pk_mul_f32 v[218:219], v[60:61], v[218:219] op_sel:[1,0]\n v_pk_mul_f32 v[220:221], v[60:61], v[220:221] op_sel:[1,0]\n " \
  "v_pk_mul_f32 v[222:223], v[60:61], v[222:223] op_sel:[1,0]\n v_cvt_pk_bf16_f32 v232, v232, v233\n v_cvt_pk_bf16_f32 v233, v234, v235\n " \
  "v_cvt_pk_bf16_f32 v234, v236, v237\n v_cvt_pk_bf16_f32 v235, v238, v239\n v_cvt_pk_bf16_f32 v216, v216, v217\n v_cvt_pk_bf16_f32 v217, v218, v219\n " \
  "v_cvt_pk_bf16_f32 v218, v220, v221\n v_cvt_pk_bf16_f32 v219, v222, v223\n " \
  "v_pk_fma_f32 v[208:209], v[148:149], v[60:61], v[208:209] op_sel_hi:[1,0,1]\n " \
  "v_pk_fma_f32 v[210:211], v[150:151], v[60:61], v[210:211] op_sel_hi:[1,0,1]\n " \
  "v_pk_fma_f32 v[212:213], v[104:105], v[60:61], v[212:213] op_sel_hi:[1,0,1]\n " \
  "v_pk_fma_f32 v[214:215], v[106:107], v[60:61], v[214:215] op_sel_hi:[1,0,1]\n " \
  "v_pk_fma_f32 v[192:193], v[96:97], v[146:147], v[192:193] op_sel_hi:[1,0,1]\n " \
  "v_pk_fma_f32 v[194:195], v[98:99], v[146:147], v[194:195] op_sel_hi:[1,0,1]\n " \
  "v_pk_fma_f32 v[196:197], v[100:101], v[146:147], v[196:197] op_sel_hi:[1,0,1]\n " \
  "v_pk_fma_f32 v[198:199], v[102:103], v[146:147], v[198:199] op_sel_hi:[1,0,1]\n v_pk_mul_f32 v[192:193], v[146:147], v[192:193] op_sel:[1,0]\n " \
  "v_pk_mul_f32 v[194:195], v[146:147], v[194:195] op_sel:[1,0]\n v_pk_mul_f32 v[196:197], v[146:147], v[196:197] op_sel:[1,0]\n " \
  "v_pk_mul_f32 v[198:199], v[146:147], v[198:199] op_sel:[1,0]\n v_mfma_f32_16x16x32_bf16 v[120:123], v[240:243], v[232:235], v[40:43]\n " \
  "v_mfma_f32_16x16x32_bf16 v[236:239], v[216:219], v[232:235], v[36:39]\n v_cvt_pk_bf16_f32 v208, v208, v209\n v_cvt_pk_bf16_f32 v209, v210, v211\n " \
  "v_cvt_pk_bf16_f32 v210, v212, v213\n v_cvt_pk_bf16_f32 v211, v214, v215\n v_cvt_pk_bf16_f32 v192, v192, v193\n v_cvt_pk_bf16_f32 v193, v194, v195\n " \
  "v_cvt_pk_bf16_f32 v194, v196, v197\n v_cvt_pk_bf16_f32 v195, v198, v199\n " \
  "v_pk_fma_f32 v[184:185], v[148:149], v[146:147], v[184:185] op_sel_hi:[1,0,1]\n " \
  "v_pk_fma_f32 v[186:187], v[150:151], v[146:147], v[186:187] op_sel_hi:[1,0,1]\n " \
  "v_pk_fma_f32 v[188:189], v[104:105], v[146:147], v[188:189] op_sel_hi:[1,0,1]\n " \
  "v_pk_fma_f32 v[190:191], v[106:107], v[146:147], v[190:191] op_sel_hi:[1,0,1]\n " \
  "v_pk_fma_f32 v[168:169], v[96:97], v[144:145], v[168:169] op_sel_hi:[1,0,1]\n " \
  "v_pk_fma_f32 v[170:171], v[98:99], v[144:145], v[170:171] op_sel_hi:[1,0,1]\n " \
  "v_pk_fma_f32 v[172:173], v[100:101], v[144:145], v[172:173] op_sel_hi:[1,0,1]\n " \
  "v_pk_fma_f32 v[174:175], v[102:103], v[144:145], v[174:175] op_sel_hi:[1,0,1]\n v_pk_mul_f32 v[168:169], v[144:145], v[168:169] op_sel:[1,0]\n " \
  "v_pk_mul_f32 v[170:171], v[144:145], v[170:171] op_sel:[1,0]\n v_pk_mul_f32 v[172:173], v[144:145], v[172:173] op_sel:[1,0]\n " \
  "v_pk_mul_f32 v[174:175], v[144:145], v[174:175] op_sel:[1,0]\n v_mfma_f32_16x16x32_bf16 v[220:223], v[216:219], v[208:211], v[40:43]\n " \
  "v_mfma_f32_16x16x32_bf16 v[212:215], v[192:195], v[208:211], v[36:39]\n v_cvt_pk_bf16_f32 v184, v184, v185\n v_cvt_pk_bf16_f32 v185, v186, v187\n " \
  "v_cvt_pk_bf16_f32 v186, v188, v189\n v_cvt_pk_bf16_f32 v187, v190, v191\n v_cvt_pk_bf16_f32 v168, v168, v169\n v_cvt_pk_bf16_f32 v169, v170, v171\n " \
  "v_cvt_pk_bf16_f32 v170, v172, v173\n v_cvt_pk_bf16_f32 v171, v174, v175\n " \
  "v_pk_fma_f32 v[160:161], v[148:149], v[144:145], v[160:161] op_sel_hi:[1,0,1]\n " \
  "v_pk_fma_f32 v[162:163], v[150:151], v[144:145], v[162:163] op_sel_hi:[1,0,1]\n " \
  "v_pk_fma_f32 v[164:165], v[104:105], v[144:145], v[164:165] op_sel_hi:[1,0,1]\n " \
  "v_pk_fma_f32 v[166:167], v[106:107], v[144:145], v[166:167] op_sel_hi:[1,0,1]\n s_waitcnt lgkmcnt(3)\n " \
  "v_pk_fma_f32 v[176:177], v[128:129], v[56:57], v[176:177] op_sel_hi:[1,0,1]\n " \
  "v_pk_fma_f32 v[178:179], v[130:131], v[56:57], v[178:179] op_sel_hi:[1,0,1]\n " \
  "v_pk_fma_f32 v[180:181], v[128:129], v[56:57], v[180:181] op_sel:[0,1,0]\n v_pk_fma_f32 v[182:183], v[130:131], v[56:57], v[182:183] op_sel:[0,1,0]\n " \
  "v_pk_mul_f32 v[176:177], v[176:177], v[92:93]\n v_pk_mul_f32 v[178:179], v[178:179], v[94:95]\n v_pk_mul_f32 v[180:181], v[180:181], v[92:93]\n " \
  "v_pk_mul_f32 v[182:183], v[182:183], v[94:95]\n v_mfma_f32_16x16x32_bf16 v[196:199], v[192:195], v[184:187], v[40:43]\n " \
  "v_mfma_f32_16x16x32_bf16 v[188:191], v[168:171], v[184:187], v[36:39]\n v_cvt_pk_bf16_f32 v160, v160, v161\n v_cvt_pk_bf16_f32 v161, v162, v163\n " \
  "v_cvt_pk_bf16_f32 v162, v164, v165\n v_cvt_pk_bf16_f32 v163, v166, v167\n v_cvt_pk_bf16_f32 v174, v176, v177\n v_cvt_pk_bf16_f32 v175, v178, v179\n " \
  "v_cvt_pk_bf16_f32 v180, v180, v181\n v_cvt_pk_bf16_f32 v181, v182, v183\n " \
  "v_pk_fma_f32 v[224:225], v[108:109], v[56:57], v[224:225] op_sel_hi:[1,0,1]\n " \
  "v_pk_fma_f32 v[226:227], v[110:111], v[56:57], v[226:227] op_sel_hi:[1,0,1]\n " \
  "v_pk_fma_f32 v[228:229], v[108:109], v[56:57], v[228:229] op_sel:[0,1,0]\n v_pk_fma_f32 v[230:231], v[110:111], v[56:57], v[230:231] op_sel:[0,1,0]\n " \
  "v_pk_mul_f32 v[224:225], v[224:225], v[88:89]\n v_pk_mul_f32 v[226:227], v[226:227], v[90:91]\n v_pk_mul_f32 v[228:229], v[228:229], v[88:89]\n " \
  "v_pk_mul_f32 v[230:231], v[230:231], v[90:91]\n v_mfma_f32_16x16x32_bf16 v[124:127], v[168:171], v[160:163], v[40:43]\n " \
  "v_cvt_pk_bf16_f32 v246, v224, v225\n v_cvt_pk_bf16_f32 v247, v226, v227\n v_cvt_pk_bf16_f32 v252, v228, v229\n v_cvt_pk_bf16_f32 v253, v230, v231\n " \
  "v_max3_f32 v140, v236, v237, v238\n v_max3_f32 v138, v212, v213, v214\n v_max3_f32 v141, v239, v120, v121\n v_max3_f32 v139, v215, v220, v221\n " \
  "v_max3_f32 v140, v140, v122, v123\n v_max3_f32 v138, v138, v222, v223\n v_max_f32 v140, v140, v141\n v_max_f32 v138, v138, v139\n " \
  "v_max3_f32 v136, v188, v189, v190\n v_max3_f32 v137, v191, v196, v197\n v_max3_f32 v136, v136, v198, v199\n " \
  "v_pk_fma_f32 v[200:201], v[112:113], v[56:57], v[200:201] op_sel_hi:[1,0,1]\n " \
  "v_pk_fma_f32 v[202:203], v[114:115], v[56:57], v[202:203] op_sel_hi:[1,0,1]\n v_max_f32 v136, v136, v137\n v_mov_b32 v142, v140\n " \
  "v_pk_fma_f32 v[204:205], v[112:113], v[56:57], v[204:205] op_sel:[0,1,0]\n v_pk_fma_f32 v[206:207], v[114:115], v[56:57], v[206:207] op_sel:[0,1,0]\n " \
  "v_permlane16_swap_b32 v136, v138\n v_permlane16_swap_b32 v140, v142\n v_pk_mul_f32 v[200:201], v[200:201], v[116:117]\n " \
  "v_pk_mul_f32 v[202:203], v[202:203], v[118:119]\n v_max_f32 v136, v136, v138\n v_max_f32 v140, v140, v142\n " \
  "v_pk_mul_f32 v[204:205], v[204:205], v[116:117]\n v_pk_mul_f32 v[206:207], v[206:207], v[118:119]\n v_permlane32_swap_b32 v136, v140\n " \
  "v_mul_f32 v152, 0x3e8293ee, v63\n v_mul_f32 v154, 0x3e8293ee, v61\n v_max_f32 v136, v136, v140\n v_mov_b32 v140, v136\n " \
  "v_mul_f32 v156, 0x3e8293ee, v147\n v_mul_f32 v158, 0x3e8293ee, v145\n v_permlane32_swap_b32 v136, v140\n v_mov_b32 v132, 0x3f803f80\n " \
  "v_mov_b32 v133, 0x3f803f80\n v_mov_b32 v138, v136\n v_mov_b32 v134, 0x3f803f80\n v_mov_b32 v135, 0x3f803f80\n v_permlane16_swap_b32 v136, v138\n " \
  "v_cvt_pk_bf16_f32 v244, v200, v201\n v_cvt_pk_bf16_f32 v245, v202, v203\n v_cvt_pk_bf16_f32 v250, v204, v205\n v_cvt_pk_bf16_f32 v251, v206, v207\n " \
  "v_cvt_pk_bf16_f32 v176, v200, v201\n v_cvt_pk_bf16_f32 v177, v202, v203\n v_cvt_pk_bf16_f32 v182, v204, v205\n v_cvt_pk_bf16_f32 v183, v206, v207\n " \
  "v_mul_f32_e64 v153, v152, -v140\n v_mul_f32_e64 v155, v154, -v138\n v_mul_f32_e64 v157, v156, -v136\n " \
  "v_pk_fma_f32 v[236:237], v[236:237], v[152:153], v[152:153] op_sel:[0,0,1] op_sel_hi:[1,0,1]\n " \
  "v_pk_fma_f32 v[238:239], v[238:239], v[152:153], v[152:153] op_sel:[0,0,1] op_sel_hi:[1,0,1]\n " \
  "v_pk_fma_f32 v[120:121], v[120:121], v[152:153], v[152:153] op_sel:[0,0,1] op_sel_hi:[1,0,1]\n " \
  "v_pk_fma_f32 v[122:123], v[122:123], v[152:153], v[152:153] op_sel:[0,0,1] op_sel_hi:[1,0,1]\n v_exp_f32 v236, v236\n v_exp_f32 v237, v237\n " \
  "v_exp_f32 v238, v238\n v_exp_f32 v239, v239\n v_exp_f32 v120, v120\n v_exp_f32 v121, v121\n v_exp_f32 v122, v122\n v_exp_f32 v123, v123\n " \
  "v_pk_fma_f32 v[212:213], v[212:213], v[154:155], v[154:155] op_sel:[0,0,1] op_sel_hi:[1,0,1]\n " \
  "v_pk_fma_f32 v[214:215], v[214:215], v[154:155], v[154:155] op_sel:[0,0,1] op_sel_hi:[1,0,1]\n " \
  "v_pk_fma_f32 v[220:221], v[220:221], v[154:155], v[154:155] op_sel:[0,0,1] op_sel_hi:[1,0,1]\n " \
  "v_pk_fma_f32 v[222:223], v[222:223], v[154:155], v[154:155] op_sel:[0,0,1] op_sel_hi:[1,0,1]\n v_cvt_pk_bf16_f32 v236, v236, v237\n " \
  "v_cvt_pk_bf16_f32 v237, v238, v239\n v_cvt_pk_bf16_f32 v238, v120, v121\n v_cvt_pk_bf16_f32 v239, v122, v123\n v_exp_f32 v212, v212\n " \
  "v_exp_f32 v213, v213\n v_mfma_f32_16x16x32_bf16 v[120:123], v[132:135], v[236:239], 0\n " \
  "v_mfma_f32_16x16x32_bf16 v[232:235], v[246:249], v[236:239], 0\n v_mfma_f32_16x16x32_bf16 v[240:243], v[252:255], v[236:239], 0\n " \
  "v_exp_f32 v214, v214\n v_exp_f32 v215, v215\n v_exp_f32 v220, v220\n v_exp_f32 v221, v221\n v_exp_f32 v222, v222\n v_exp_f32 v223, v223\n " \
  "v_pk_fma_f32 v[188:189], v[188:189], v[156:157], v[156:157] op_sel:[0,0,1] op_sel_hi:[1,0,1]\n " \
  "v_pk_fma_f32 v[190:191], v[190:191], v[156:157], v[156:157] op_sel:[0,0,1] op_sel_hi:[1,0,1]\n " \
  "v_pk_fma_f32 v[196:197], v[196:197], v[156:157], v[156:157] op_sel:[0,0,1] op_sel_hi:[1,0,1]\n " \
  "v_pk_fma_f32 v[198:199], v[198:199], v[156:157], v[156:157] op_sel:[0,0,1] op_sel_hi:[1,0,1]\n v_cvt_pk_bf16_f32 v212, v212, v213\n " \
  "v_cvt_pk_bf16_f32 v213, v214, v215\n v_cvt_pk_bf16_f32 v214, v220, v221\n v_cvt_pk_bf16_f32 v215, v222, v223\n v_exp_f32 v188, v188\n " \
  "s_cmp_eq_u32 %[wm], 0\n s_cbranch_scc1 .Lg1f%=\n ds_read_b32 v53, v59\n ds_read_b128 v[80:83], v58\n ds_read_b64 v[172:173], v58 offset:1024\n " \
  "ds_read_b64 v[178:179], v58 offset:1032\n s_branch .Lg1g%=\n .Lg1f%=:\n buffer_load_dwordx4 v[80:83], v55, %[rs], 0 offen sc1\n " \
  "buffer_load_dwordx2 v[172:173], v55, %[rs], 0 offen offset:1024 sc1\n buffer_load_dwordx2 v[178:179], v55, %[rs], 0 offen offset:1032 sc1\n " \
  ".Lg1g%=:\n v_exp_f32 v189, v189\n v_mfma_f32_16x16x32_bf16 v[220:223], v[132:135], v[212:215], 0\n " \
  "v_mfma_f32_16x16x32_bf16 v[224:227], v[244:247], v[212:215], 0\n v_mfma_f32_16x16x32_bf16 v[228:231], v[250:253], v[212:215], 0\n " \
  "v_exp_f32 v190, v190\n v_exp_f32 v191, v191\n v_exp_f32 v196, v196\n v_exp_f32 v197, v197\n v_exp_f32 v198, v198\n v_exp_f32 v199, v199\n " \
  "v_rcp_f32 v120, v120\n v_cvt_pk_bf16_f32 v188, v188, v189\n v_pk_mul_f32 v[232:233], v[232:233], v[120:121] op_sel_hi:[1,0]\n " \
  "v_pk_mul_f32 v[234:235], v[234:235], v[120:121] op_sel_hi:[1,0]\n v_pk_mul_f32 v[240:241], v[240:241], v[120:121] op_sel_hi:[1,0]\n " \
  "v_pk_mul_f32 v[242:243], v[242:243], v[120:121] op_sel_hi:[1,0]\n v_cvt_pk_bf16_f32 v189, v190, v191\n v_cvt_pk_bf16_f32 v190, v196, v197\n " \
  "v_cvt_pk_bf16_f32 v191, v198, v199\n v_rcp_f32 v220, v220\n v_cvt_pk_bf16_f32 v232, v232, v233\n v_cvt_pk_bf16_f32 v233, v234, v235\n " \
  "v_mfma_f32_16x16x32_bf16 v[196:199], v[132:135], v[188:191], 0\n v_mfma_f32_16x16x32_bf16 v[200:203], v[174:177], v[188:191], 0\n " \
  "v_mfma_f32_16x16x32_bf16 v[204:207], v[180:183], v[188:191], 0\n v_cvt_pk_bf16_f32 v234, v240, v241\n v_cvt_pk_bf16_f32 v235, v242, v243\n " \
  "buffer_store_dwordx4 v[232:235], v51, %[rs], 0 offen offset:3072 sc1\n ds_write_b128 v50, v[232:235] offset:3072\n " \
  "v_pk_mul_f32 v[224:225], v[224:225], v[220:221] op_sel_hi:[1,0]\n v_pk_mul_f32 v[226:227], v[226:227], v[220:221] op_sel_hi:[1,0]\n " \
  "v_pk_mul_f32 v[228:229], v[228:229], v[220:221] op_sel_hi:[1,0]\n v_pk_mul_f32 v[230:231], v[230:231], v[220:221] op_sel_hi:[1,0]\n " \
  "v_cvt_pk_bf16_f32 v224, v224, v225\n v_cvt_pk_bf16_f32 v225, v226, v227\n v_cvt_pk_bf16_f32 v226, v228, v229\n v_cvt_pk_bf16_f32 v227, v230, v231\n " \
  "v_rcp_f32 v196, v196\n buffer_store_dwordx4 v[224:227], v51, %[rs], 0 offen offset:2048 sc1\n ds_write_b128 v50, v[224:227] offset:2048\n " \
  "v_pk_mul_f32 v[200:201], v[200:201], v[196:197] op_sel_hi:[1,0]\n v_pk_mul_f32 v[202:203], v[202:203], v[196:197] op_sel_hi:[1,0]\n " \
  "v_pk_mul_f32 v[204:205], v[204:205], v[196:197] op_sel_hi:[1,0]\n v_pk_mul_f32 v[206:207], v[206:207], v[196:197] op_sel_hi:[1,0]\n " \
  "v_cvt_pk_bf16_f32 v200, v200, v201\n v_cvt_pk_bf16_f32 v201, v202, v203\n v_cvt_pk_bf16_f32 v202, v204, v205\n v_cvt_pk_bf16_f32 v203, v206, v207\n " \
  "buffer_store_dwordx4 v[200:203], v51, %[rs], 0 offen offset:1024 sc1\n ds_write_b128 v50, v[200:203] offset:1024\n s_mov_b32 vcc_hi, 0x40000\n " \
  "s_cmp_eq_u32 %[wm], 0\n s_cbranch_scc1 .Lg1h%=\n s_waitcnt lgkmcnt(3)\n .Lg1i%=:\n v_readfirstlane_b32 vcc_lo, v53\n s_nop 1\n " \
  "s_cmp_lg_u32 vcc_lo, 0\n s_cbranch_scc1 .Lg1k%=\n ds_read_b32 v53, v59\n ds_read_b128 v[80:83], v58\n ds_read_b64 v[172:173], v58 offset:1024\n " \
  "ds_read_b64 v[178:179], v58 offset:1032\n s_waitcnt lgkmcnt(0)\n s_sub_u32 vcc_hi, vcc_hi, 1\n s_cbranch_scc0 .Lg1i%=\n s_branch .Lg1k%=\n .Lg1h%=:\n " \
  "s_mov_b32 m0, 0x40000\n s_waitcnt vmcnt(3)\n .Lg1j%=:\n v_max3_u32 v136, v80, v81, v82\n v_max3_u32 v137, v83, v172, v173\n " \
  "v_max3_u32 v136, v136, v178, v179\n v_max_u32 v136, v136, v137\n v_cmp_eq_u32 vcc, -1, v136\n s_cbranch_vccz .Lg1k%=\n " \
  "buffer_load_dwordx4 v[80:83], v55, %[rs], 0 offen sc1\n buffer_load_dwordx2 v[172:173], v55, %[rs], 0 offen offset:1024 sc1\n " \
  "buffer_load_dwordx2 v[178:179], v55, %[rs], 0 offen offset:1032 sc1\n s_waitcnt vmcnt(0)\n s_sub_u32 m0, m0, 1\n s_cbranch_scc0 .Lg1j%=\n .Lg1k%=:\n " \
  "v_mfma_f32_16x16x32_bf16 v[164:167], v[80:83], v[160:163], v[44:47]\n s_nop 7\n v_max3_f32 v136, v164, v165, v166\n " \
  "v_max3_f32 v137, v167, v124, v125\n v_max3_f32 v136, v136, v126, v127\n v_max_f32 v136, v136, v137\n v_mov_b32 v137, v136\n s_nop 1\n " \
  "v_permlane16_swap_b32 v136, v137\n v_max_f32 v136, v136, v137\n v_mov_b32 v137, v136\n s_nop 1\n v_permlane32_swap_b32 v136, v137\n " \
  "v_max_f32 v136, v136, v137\n v_mul_f32_e64 v159, v158, -v136\n " \
  "v_pk_fma_f32 v[164:165], v[164:165], v[158:159], v[158:159] op_sel:[0,0,1] op_sel_hi:[1,0,1]\n " \
  "v_pk_fma_f32 v[166:167], v[166:167], v[158:159], v[158:159] op_sel:[0,0,1] op_sel_hi:[1,0,1]\n " \
  "v_pk_fma_f32 v[124:125], v[124:125], v[158:159], v[158:159] op_sel:[0,0,1] op_sel_hi:[1,0,1]\n " \
  "v_pk_fma_f32 v[126:127], v[126:127], v[158:159], v[158:159] op_sel:[0,0,1] op_sel_hi:[1,0,1]\n v_exp_f32 v164, v164\n v_exp_f32 v165, v165\n " \
  "v_exp_f32 v166, v166\n v_exp_f32 v167, v167\n v_exp_f32 v124, v124\n v_exp_f32 v125, v125\n v_exp_f32 v126, v126\n v_exp_f32 v127, v127\n " \
  "v_cvt_pk_bf16_f32 v164, v164, v165\n v_cvt_pk_bf16_f32 v165, v166, v167\n v_cvt_pk_bf16_f32 v166, v124, v125\n v_cvt_pk_bf16_f32 v167, v126, v127\n " \
  "s_nop 1\n v_mfma_f32_16x16x32_bf16 v[124:127], v[132:135], v[164:167], 0\n v_mfma_f32_16x16x32_bf16 v[168:171], v[172:175], v[164:167], 0\n " \
  "v_mfma_f32_16x16x32_bf16 v[84:87], v[178:181], v[164:167], 0\n s_nop 5\n v_rcp_f32 v124, v124\n s_nop 0\n " \
  "v_pk_mul_f32 v[168:169], v[168:169], v[124:125] op_sel_hi:[1,0]\n v_pk_mul_f32 v[170:171], v[170:171], v[124:125] op_sel_hi:[1,0]\n " \
  "v_pk_mul_f32 v[84:85], v[84:85], v[124:125] op_sel_hi:[1,0]\n v_pk_mul_f32 v[86:87], v[86:87], v[124:125] op_sel_hi:[1,0]\n " \
  "v_cvt_pk_bf16_f32 v168, v168, v169\n v_cvt_pk_bf16_f32 v169, v170, v171\n v_cvt_pk_bf16_f32 v170, v84, v85\n v_cvt_pk_bf16_f32 v171, v86, v87\n " \
  "buffer_store_dwordx4 v[168:171], v51, %[rs], 0 offen offset:0 sc1\n ds_write_b128 v50, v[168:171] offset:0\n "
#define G1_HEDGE \
  "G1Q 1, 1, 0, 0, 0\n ds_read2_b64 v[140:143], v49 offset1:16\n ds_read_b128 v[96:99], v48 offset:256\n G1Q 1, 1, 0, 1, 0\n " \
  "ds_read_b128 v[100:103], v48 offset:320\n ds_read2_b64 v[136:139], v49 offset0:32 offset1:48\n G1Q 1, 1, 0, 2, 0\n ds_read_b64_tr_b16 v[36:37], v53\n " \
  "ds_read_b64_tr_b16 v[38:39], v53 offset:1024\n G1Q 1, 1, 0, 3, 0\n ds_read_b64_tr_b16 v[40:41], v53 offset:32\n " \
  "ds_read_b64_tr_b16 v[42:43], v53 offset:1056\n G1Q 1, 0, 1, 0, 0\n G1Q 1, 0, 1, 1, 0\n s_waitcnt lgkmcnt(4)\n v_fmac_f32 v168, v96, v140\n " \
  "G1Q 1, 0, 1, 2, 0\n v_fma_f32 v169, v97, v140, v169\n v_fma_f32 v170, v98, v140, v170\n G1Q 1, 0, 1, 3, 0\n v_fma_f32 v171, v99, v140, v171\n " \
  "v_fma_f32 v172, v100, v140, v172\n G1Q 1, 3, 2, 0, 0\n v_fma_f32 v173, v101, v140, v173\n v_fma_f32 v174, v102, v140, v174\n G1Q 1, 3, 2, 1, 0\n " \
  "v_fma_f32 v175, v103, v140, v175\n v_cvt_pk_bf16_f32 v168, v168, v169\n G1Q 1, 3, 2, 2, 0\n v_cvt_pk_bf16_f32 v169, v170, v171\n " \
  "v_cvt_pk_bf16_f32 v170, v172, v173\n G1Q 1, 3, 2, 3, 0\n v_cvt_pk_bf16_f32 v171, v174, v175\n v_fma_f32 v192, v96, v142, v192\n G1Q 1, 2, 3, 0, 0\n " \
  "v_fma_f32 v193, v97, v142, v193\n v_fma_f32 v194, v98, v142, v194\n G1Q 1, 2, 3, 1, 0\n v_fma_f32 v195, v99, v142, v195\n " \
  "v_fma_f32 v196, v100, v142, v196\n G1Q 1, 2, 3, 2, 0\n v_fma_f32 v197, v101, v142, v197\n v_fma_f32 v198, v102, v142, v198\n G1Q 1, 2, 3, 3, 0\n " \
  "v_fma_f32 v199, v103, v142, v199\n v_cvt_pk_bf16_f32 v192, v192, v193\n G1Q 2, 2, 0, 0, 0\n ds_read2_b32 v[144:145], v52 offset1:16\n " \
  "v_cvt_pk_bf16_f32 v193, v194, v195\n G1Q 2, 2, 0, 1, 0\n ds_read_b128 v[148:151], v48\n v_cvt_pk_bf16_f32 v194, v196, v197\n G1Q 2, 2, 0, 2, 0\n " \
  "v_cvt_pk_bf16_f32 v195, v198, v199\n v_fma_f32 v216, v96, v136, v216\n G1Q 2, 2, 0, 3, 0\n v_fma_f32 v217, v97, v136, v217\n " \
  "v_fma_f32 v218, v98, v136, v218\n G1Q 2, 3, 1, 0, 0\n v_fma_f32 v219, v99, v136, v219\n v_fma_f32 v220, v100, v136, v220\n G1Q 2, 3, 1, 1, 0\n " \
  "ds_read_b128 v[88:91], v48 offset:64\n v_fma_f32 v221, v101, v136, v221\n v_fma_f32 v222, v102, v136, v222\n G1Q 2, 3, 1, 2, 0\n " \
  "ds_read_b128 v[128:131], v54\n v_fma_f32 v223, v103, v136, v223\n v_cvt_pk_bf16_f32 v216, v216, v217\n G1Q 2, 3, 1, 3, 0\n " \
  "v_cvt_pk_bf16_f32 v217, v218, v219\n v_cvt_pk_bf16_f32 v218, v220, v221\n G1Q 2, 0, 2, 0, 0\n v_cvt_pk_bf16_f32 v219, v222, v223\n " \
  "v_fma_f32 v240, v96, v138, v240\n G1Q 2, 0, 2, 1, 0\n ds_read_b128 v[92:95], v54 offset:512\n v_fma_f32 v241, v97, v138, v241\n " \
  "v_fma_f32 v242, v98, v138, v242\n G1Q 2, 0, 2, 2, 0\n ds_read_b128 v[132:135], v54 offset:64\n v_fma_f32 v243, v99, v138, v243\n " \
  "v_fma_f32 v244, v100, v138, v244\n G1Q 2, 0, 2, 3, 0\n v_fma_f32 v245, v101, v138, v245\n v_fma_f32 v246, v102, v138, v246\n G1Q 2, 1, 3, 0, 0\n " \
  "v_fma_f32 v247, v103, v138, v247\n v_cvt_pk_bf16_f32 v240, v240, v241\n G1Q 2, 1, 3, 1, 0\n v_cvt_pk_bf16_f32 v241, v242, v243\n " \
  "v_cvt_pk_bf16_f32 v242, v244, v245\n G1Q 2, 1, 3, 2, 0\n v_cvt_pk_bf16_f32 v243, v246, v247\n G1Q 2, 1, 3, 3, 0\n s_waitcnt lgkmcnt(6)\n " \
  "v_mfma_f32_16x16x32_bf16 v[172:175], v[36:39], v[168:171], 0\n ds_read_b128 v[112:115], v54 offset:576\n ds_read_b128 v[116:119], v54 offset:128\n " \
  "v_mfma_f32_16x16x32_bf16 v[80:83], v[40:43], v[168:171], 0\n ds_read_b128 v[152:155], v54 offset:640\n ds_read_b128 v[156:159], v54 offset:192\n " \
  "v_mfma_f32_16x16x32_bf16 v[196:199], v[36:39], v[192:195], 0\n ds_read_b128 v[120:123], v54 offset:704\n v_mul_f32 v44, 0x3fb8aa3b, v141\n " \
  "v_mfma_f32_16x16x32_bf16 v[84:87], v[40:43], v[192:195], 0\n v_mul_f32 v46, 0x3fb8aa3b, v143\n v_mul_f32 v104, 0x3fb8aa3b, v137\n " \
  "v_mfma_f32_16x16x32_bf16 v[220:223], v[36:39], v[216:219], 0\n v_mul_f32 v106, 0x3fb8aa3b, v139\n " \
  "v_mfma_f32_16x16x32_bf16 v[124:127], v[40:43], v[216:219], 0\n v_mfma_f32_16x16x32_bf16 v[244:247], v[36:39], v[240:243], 0\n " \
  "v_mfma_f32_16x16x32_bf16 v[108:111], v[40:43], v[240:243], 0\n v_max3_f32 v168, v172, v173, v174\n v_max3_f32 v169, -v172, -v173, -v174\n " \
  "v_max3_f32 v168, v168, v175, v80\n v_max3_f32 v169, v169, -v175, -v80\n v_max3_f32 v168, v168, v81, v82\n v_max3_f32 v169, v169, -v81, -v82\n " \
  "v_max_f32_e64 v168, v168, v83\n v_max_f32_e64 v169, v169, -v83\n v_max3_f32 v170, v196, v197, v198\n v_max3_f32 v171, -v196, -v197, -v198\n " \
  "v_max3_f32 v170, v170, v199, v84\n v_max3_f32 v171, v171, -v199, -v84\n v_max3_f32 v170, v170, v85, v86\n v_max3_f32 v171, v171, -v85, -v86\n " \
  "v_max_f32_e64 v170, v170, v87\n v_max_f32_e64 v171, v171, -v87\n s_waitcnt lgkmcnt(0)\n " \
  "v_pk_fma_f32 v[176:177], v[128:129], v[144:145], v[176:177] op_sel_hi:[1,0,1]\n " \
  "v_pk_fma_f32 v[178:179], v[130:131], v[144:145], v[178:179] op_sel_hi:[1,0,1]\n " \
  "v_pk_fma_f32 v[180:181], v[128:129], v[144:145], v[180:181] op_sel:[0,1,0]\n " \
  "v_pk_fma_f32 v[182:183], v[130:131], v[144:145], v[182:183] op_sel:[0,1,0]\n v_pk_mul_f32 v[176:177], v[176:177], v[92:93]\n " \
  "v_pk_mul_f32 v[178:179], v[178:179], v[94:95]\n v_pk_mul_f32 v[180:181], v[180:181], v[92:93]\n v_pk_mul_f32 v[182:183], v[182:183], v[94:95]\n " \
  "v_pk_fma_f32 v[200:201], v[132:133], v[144:145], v[200:201] op_sel_hi:[1,0,1]\n " \
  "v_pk_fma_f32 v[202:203], v[134:135], v[144:145], v[202:203] op_sel_hi:[1,0,1]\n " \
  "v_pk_fma_f32 v[204:205], v[132:133], v[144:145], v[204:205] op_sel:[0,1,0]\n " \
  "v_pk_fma_f32 v[206:207], v[134:135], v[144:145], v[206:207] op_sel:[0,1,0]\n v_pk_mul_f32 v[200:201], v[200:201], v[112:113]\n " \
  "v_pk_mul_f32 v[202:203], v[202:203], v[114:115]\n v_pk_mul_f32 v[204:205], v[204:205], v[112:113]\n v_pk_mul_f32 v[206:207], v[206:207], v[114:115]\n " \
  "v_max3_f32 v192, v220, v221, v222\n v_max3_f32 v193, -v220, -v221, -v222\n v_max3_f32 v192, v192, v223, v124\n v_max3_f32 v193, v193, -v223, -v124\n " \
  "v_max3_f32 v192, v192, v125, v126\n v_max3_f32 v193, v193, -v125, -v126\n v_max_f32_e64 v192, v192, v127\n v_max_f32_e64 v193, v193, -v127\n " \
  "v_max3_f32 v194, v244, v245, v246\n v_max3_f32 v195, -v244, -v245, -v246\n v_max3_f32 v194, v194, v247, v108\n v_max3_f32 v195, v195, -v247, -v108\n " \
  "v_max3_f32 v194, v194, v109, v110\n v_max3_f32 v195, v195, -v109, -v110\n v_max_f32_e64 v194, v194, v111\n v_max_f32_e64 v195, v195, -v111\n " \
  "v_permlane16_swap_b32 v168, v169\n v_permlane16_swap_b32 v170, v171\n v_permlane16_swap_b32 v192, v193\n v_permlane16_swap_b32 v194, v195\n " \
  "v_pk_fma_f32 v[224:225], v[116:117], v[144:145], v[224:225] op_sel_hi:[1,0,1]\n " \
  "v_pk_fma_f32 v[226:227], v[118:119], v[144:145], v[226:227] op_sel_hi:[1,0,1]\n v_max_f32 v168, v168, v169\n v_max_f32 v170, v170, v171\n " \
  "v_max_f32 v192, v192, v193\n v_max_f32 v194, v194, v195\n v_pk_fma_f32 v[228:229], v[116:117], v[144:145], v[228:229] op_sel:[0,1,0]\n " \
  "v_pk_fma_f32 v[230:231], v[118:119], v[144:145], v[230:231] op_sel:[0,1,0]\n v_permlane32_swap_b32 v168, v170\n v_permlane32_swap_b32 v192, v194\n " \
  "v_pk_mul_f32 v[224:225], v[224:225], v[152:153]\n v_pk_mul_f32 v[226:227], v[226:227], v[154:155]\n v_max_f32 v168, v168, v170\n " \
  "v_max_f32 v192, v192, v194\n v_pk_mul_f32 v[228:229], v[228:229], v[152:153]\n v_pk_mul_f32 v[230:231], v[230:231], v[154:155]\n " \
  "v_mov_b32 v170, v168\n v_mov_b32 v194, v192\n v_pk_fma_f32 v[248:249], v[156:157], v[144:145], v[248:249] op_sel_hi:[1,0,1]\n " \
  "v_pk_fma_f32 v[250:251], v[158:159], v[144:145], v[250:251] op_sel_hi:[1,0,1]\n v_permlane32_swap_b32 v168, v170\n v_permlane32_swap_b32 v192, v194\n " \
  "v_pk_fma_f32 v[252:253], v[156:157], v[144:145], v[252:253] op_sel:[0,1,0]\n " \
  "v_pk_fma_f32 v[254:255], v[158:159], v[144:145], v[254:255] op_sel:[0,1,0]\n v_mov_b32 v169, v168\n v_mov_b32 v171, v170\n v_mov_b32 v193, v192\n " \
  "v_mov_b32 v195, v194\n v_pk_mul_f32 v[248:249], v[248:249], v[120:121]\n v_pk_mul_f32 v[250:251], v[250:251], v[122:123]\n " \
  "v_permlane16_swap_b32 v168, v169\n v_permlane16_swap_b32 v170, v171\n v_permlane16_swap_b32 v192, v193\n v_permlane16_swap_b32 v194, v195\n " \
  "v_pk_mul_f32 v[252:253], v[252:253], v[120:121]\n v_pk_mul_f32 v[254:255], v[254:255], v[122:123]\n v_cvt_pk_bf16_f32 v176, v176, v177\n " \
  "v_cvt_pk_bf16_f32 v177, v178, v179\n v_cvt_pk_bf16_f32 v178, v200, v201\n v_cvt_pk_bf16_f32 v179, v202, v203\n v_cvt_pk_bf16_f32 v180, v180, v181\n " \
  "v_cvt_pk_bf16_f32 v181, v182, v183\n v_cvt_pk_bf16_f32 v182, v204, v205\n v_cvt_pk_bf16_f32 v183, v206, v207\n v_cvt_pk_bf16_f32 v224, v224, v225\n " \
  "v_cvt_pk_bf16_f32 v225, v226, v227\n v_cvt_pk_bf16_f32 v226, v248, v249\n v_cvt_pk_bf16_f32 v227, v250, v251\n v_cvt_pk_bf16_f32 v228, v228, v229\n " \
  "v_cvt_pk_bf16_f32 v229, v230, v231\n v_cvt_pk_bf16_f32 v230, v252, v253\n v_cvt_pk_bf16_f32 v231, v254, v255\n v_mul_f32_e64 v45, v44, -v168\n " \
  "v_mul_f32_e64 v146, v44, -v169\n v_mul_f32_e64 v47, v46, -v170\n v_mul_f32_e64 v147, v46, -v171\n v_mul_f32_e64 v105, v104, -v192\n " \
  "v_mul_f32_e64 v144, v104, -v193\n v_mul_f32_e64 v107, v106, -v194\n v_mul_f32_e64 v145, v106, -v195\n " \
  "v_pk_fma_f32 v[200:201], v[172:173], v[44:45], v[146:147] op_sel_hi:[1,0,0] neg_lo:[1,0,0] neg_hi:[1,0,0]\n " \
  "v_pk_fma_f32 v[202:203], v[174:175], v[44:45], v[146:147] op_sel_hi:[1,0,0] neg_lo:[1,0,0] neg_hi:[1,0,0]\n " \
  "v_pk_fma_f32 v[204:205], v[80:81], v[44:45], v[146:147] op_sel_hi:[1,0,0] neg_lo:[1,0,0] neg_hi:[1,0,0]\n " \
  "v_pk_fma_f32 v[206:207], v[82:83], v[44:45], v[146:147] op_sel_hi:[1,0,0] neg_lo:[1,0,0] neg_hi:[1,0,0]\n " \
  "v_pk_fma_f32 v[172:173], v[172:173], v[44:45], v[44:45] op_sel:[0,0,1] op_sel_hi:[1,0,1]\n " \
  "v_pk_fma_f32 v[174:175], v[174:175], v[44:45], v[44:45] op_sel:[0,0,1] op_sel_hi:[1,0,1]\n " \
  "v_pk_fma_f32 v[80:81], v[80:81], v[44:45], v[44:45] op_sel:[0,0,1] op_sel_hi:[1,0,1]\n " \
  "v_pk_fma_f32 v[82:83], v[82:83], v[44:45], v[44:45] op_sel:[0,0,1] op_sel_hi:[1,0,1]\n ds_write_b128 v55, v[176:179] offset:0\n " \
  "v_exp_f32 v200, v200\n v_exp_f32 v201, v201\n v_exp_f32 v202, v202\n v_exp_f32 v203, v203\n v_exp_f32 v204, v204\n v_exp_f32 v205, v205\n " \
  "v_exp_f32 v206, v206\n v_exp_f32 v207, v207\n v_exp_f32 v172, v172\n v_exp_f32 v173, v173\n v_exp_f32 v174, v174\n v_exp_f32 v175, v175\n " \
  "v_exp_f32 v80, v80\n v_exp_f32 v81, v81\n v_exp_f32 v82, v82\n v_exp_f32 v83, v83\n " \
  "v_pk_fma_f32 v[248:249], v[196:197], v[46:47], v[146:147] op_sel:[0,0,1] op_sel_hi:[1,0,1] neg_lo:[1,0,0] neg_hi:[1,0,0]\n " \
  "v_pk_fma_f32 v[250:251], v[198:199], v[46:47], v[146:147] op_sel:[0,0,1] op_sel_hi:[1,0,1] neg_lo:[1,0,0] neg_hi:[1,0,0]\n " \
  "v_pk_fma_f32 v[252:253], v[84:85], v[46:47], v[146:147] op_sel:[0,0,1] op_sel_hi:[1,0,1] neg_lo:[1,0,0] neg_hi:[1,0,0]\n " \
  "v_pk_fma_f32 v[254:255], v[86:87], v[46:47], v[146:147] op_sel:[0,0,1] op_sel_hi:[1,0,1] neg_lo:[1,0,0] neg_hi:[1,0,0]\n " \
  "v_pk_fma_f32 v[196:197], v[196:197], v[46:47], v[46:47] op_sel:[0,0,1] op_sel_hi:[1,0,1]\n " \
  "v_pk_fma_f32 v[198:199], v[198:199], v[46:47], v[46:47] op_sel:[0,0,1] op_sel_hi:[1,0,1]\n " \
  "v_pk_fma_f32 v[84:85], v[84:85], v[46:47], v[46:47] op_sel:[0,0,1] op_sel_hi:[1,0,1]\n " \
  "v_pk_fma_f32 v[86:87], v[86:87], v[46:47], v[46:47] op_sel:[0,0,1] op_sel_hi:[1,0,1]\n ds_write_b128 v55, v[180:183] offset:1024\n " \
  "v_exp_f32 v248, v248\n v_exp_f32 v249, v249\n v_exp_f32 v250, v250\n v_exp_f32 v251, v251\n v_exp_f32 v252, v252\n v_exp_f32 v253, v253\n " \
  "v_exp_f32 v254, v254\n v_exp_f32 v255, v255\n v_exp_f32 v196, v196\n v_exp_f32 v197, v197\n v_exp_f32 v198, v198\n v_exp_f32 v199, v199\n " \
  "v_exp_f32 v84, v84\n v_exp_f32 v85, v85\n v_exp_f32 v86, v86\n v_exp_f32 v87, v87\n " \
  "v_pk_fma_f32 v[128:129], v[220:221], v[104:105], v[144:145] op_sel_hi:[1,0,0] neg_lo:[1,0,0] neg_hi:[1,0,0]\n " \
  "v_pk_fma_f32 v[130:131], v[222:223], v[104:105], v[144:145] op_sel_hi:[1,0,0] neg_lo:[1,0,0] neg_hi:[1,0,0]\n " \
  "v_pk_fma_f32 v[132:133], v[124:125], v[104:105], v[144:145] op_sel_hi:[1,0,0] neg_lo:[1,0,0] neg_hi:[1,0,0]\n " \
  "v_pk_fma_f32 v[134:135], v[126:127], v[104:105], v[144:145] op_sel_hi:[1,0,0] neg_lo:[1,0,0] neg_hi:[1,0,0]\n " \
  "v_pk_fma_f32 v[220:221], v[220:221], v[104:105], v[104:105] op_sel:[0,0,1] op_sel_hi:[1,0,1]\n " \
  "v_pk_fma_f32 v[222:223], v[222:223], v[104:105], v[104:105] op_sel:[0,0,1] op_sel_hi:[1,0,1]\n " \
  "v_pk_fma_f32 v[124:125], v[124:125], v[104:105], v[104:105] op_sel:[0,0,1] op_sel_hi:[1,0,1]\n " \
  "v_pk_fma_f32 v[126:127], v[126:127], v[104:105], v[104:105] op_sel:[0,0,1] op_sel_hi:[1,0,1]\n ds_write_b128 v55, v[224:227] offset:8192\n " \
  "v_pk_add_f32 v[216:217], v[172:173], v[174:175]\n v_pk_add_f32 v[218:219], v[80:81], v[82:83]\n v_pk_add_f32 v[216:217], v[216:217], v[218:219]\n " \
  "v_add_f32 v168, v216, v217\n v_pk_add_f32 v[216:217], v[200:201], v[202:203]\n v_pk_add_f32 v[218:219], v[204:205], v[206:207]\n " \
  "v_pk_add_f32 v[216:217], v[216:217], v[218:219]\n v_add_f32 v169, v216, v217\n v_pk_add_f32 v[216:217], v[196:197], v[198:199]\n " \
  "v_pk_add_f32 v[218:219], v[84:85], v[86:87]\n v_pk_add_f32 v[216:217], v[216:217], v[218:219]\n v_add_f32 v170, v216, v217\n " \
  "v_pk_add_f32 v[216:217], v[248:249], v[250:251]\n v_pk_add_f32 v[218:219], v[252:253], v[254:255]\n v_pk_add_f32 v[216:217], v[216:217], v[218:219]\n " \
  "v_add_f32 v171, v216, v217\n v_exp_f32 v128, v128\n v_exp_f32 v129, v129\n v_permlane16_swap_b32 v168, v169\n v_permlane16_swap_b32 v170, v171\n " \
  "v_exp_f32 v130, v130\n v_exp_f32 v131, v131\n v_add_f32 v168, v168, v169\n v_add_f32 v170, v170, v171\n v_exp_f32 v132, v132\n v_exp_f32 v133, v133\n " \
  "v_permlane32_swap_b32 v168, v170\n v_exp_f32 v134, v134\n v_exp_f32 v135, v135\n v_add_f32 v168, v168, v170\n v_exp_f32 v220, v220\n " \
  "v_exp_f32 v221, v221\n v_rcp_f32 v168, v168\n v_exp_f32 v222, v222\n v_exp_f32 v223, v223\n v_mov_b32 v170, v168\n v_exp_f32 v124, v124\n " \
  "v_exp_f32 v125, v125\n v_permlane32_swap_b32 v168, v170\n v_exp_f32 v126, v126\n v_exp_f32 v127, v127\n v_mov_b32 v169, v168\n v_mov_b32 v171, v170\n " \
  "v_pk_fma_f32 v[112:113], v[244:245], v[106:107], v[144:145] op_sel:[0,0,1] op_sel_hi:[1,0,1] neg_lo:[1,0,0] neg_hi:[1,0,0]\n " \
  "v_pk_fma_f32 v[114:115], v[246:247], v[106:107], v[144:145] op_sel:[0,0,1] op_sel_hi:[1,0,1] neg_lo:[1,0,0] neg_hi:[1,0,0]\n " \
  "v_permlane16_swap_b32 v168, v169\n v_permlane16_swap_b32 v170, v171\n " \
  "v_pk_fma_f32 v[116:117], v[108:109], v[106:107], v[144:145] op_sel:[0,0,1] op_sel_hi:[1,0,1] neg_lo:[1,0,0] neg_hi:[1,0,0]\n " \
  "v_pk_fma_f32 v[118:119], v[110:111], v[106:107], v[144:145] op_sel:[0,0,1] op_sel_hi:[1,0,1] neg_lo:[1,0,0] neg_hi:[1,0,0]\n " \
  "v_pk_fma_f32 v[244:245], v[244:245], v[106:107], v[106:107] op_sel:[0,0,1] op_sel_hi:[1,0,1]\n " \
  "v_pk_fma_f32 v[246:247], v[246:247], v[106:107], v[106:107] op_sel:[0,0,1] op_sel_hi:[1,0,1]\n " \
  "v_pk_fma_f32 v[108:109], v[108:109], v[106:107], v[106:107] op_sel:[0,0,1] op_sel_hi:[1,0,1]\n " \
  "v_pk_fma_f32 v[110:111], v[110:111], v[106:107], v[106:107] op_sel:[0,0,1] op_sel_hi:[1,0,1]\n ds_write_b128 v55, v[228:231] offset:9216\n " \
  "v_exp_f32 v112, v112\n v_pk_mul_f32 v[172:173], v[172:173], v[168:169] op_sel_hi:[1,0]\n v_exp_f32 v113, v113\n " \
  "v_pk_mul_f32 v[174:175], v[174:175], v[168:169] op_sel_hi:[1,0]\n v_exp_f32 v114, v114\n v_cvt_pk_bf16_f32 v172, v172, v173\n v_exp_f32 v115, v115\n " \
  "v_cvt_pk_bf16_f32 v173, v174, v175\n v_exp_f32 v116, v116\n ds_write_b64 v58, v[172:173] offset:0\n v_exp_f32 v117, v117\n " \
  "v_pk_mul_f32 v[80:81], v[80:81], v[168:169] op_sel_hi:[1,0]\n v_exp_f32 v118, v118\n v_pk_mul_f32 v[82:83], v[82:83], v[168:169] op_sel_hi:[1,0]\n " \
  "v_exp_f32 v119, v119\n v_cvt_pk_bf16_f32 v80, v80, v81\n v_exp_f32 v244, v244\n v_cvt_pk_bf16_f32 v81, v82, v83\n v_exp_f32 v245, v245\n " \
  "ds_write_b64 v58, v[80:81] offset:32\n v_exp_f32 v246, v246\n v_pk_mul_f32 v[200:201], v[200:201], v[168:169] op_sel:[0,1]\n v_exp_f32 v247, v247\n " \
  "v_pk_mul_f32 v[202:203], v[202:203], v[168:169] op_sel:[0,1]\n v_exp_f32 v108, v108\n v_cvt_pk_bf16_f32 v200, v200, v201\n v_exp_f32 v109, v109\n " \
  "v_cvt_pk_bf16_f32 v201, v202, v203\n v_exp_f32 v110, v110\n ds_write_b64 v58, v[200:201] offset:64\n v_exp_f32 v111, v111\n " \
  "v_pk_mul_f32 v[204:205], v[204:205], v[168:169] op_sel:[0,1]\n v_pk_mul_f32 v[206:207], v[206:207], v[168:169] op_sel:[0,1]\n " \
  "v_cvt_pk_bf16_f32 v204, v204, v205\n v_cvt_pk_bf16_f32 v205, v206, v207\n ds_write_b64 v58, v[204:205] offset:96\n " \
  "v_pk_add_f32 v[216:217], v[220:221], v[222:223]\n v_pk_add_f32 v[218:219], v[124:125], v[126:127]\n v_pk_add_f32 v[216:217], v[216:217], v[218:219]\n " \
  "v_add_f32 v192, v216, v217\n v_pk_add_f32 v[216:217], v[128:129], v[130:131]\n v_pk_add_f32 v[218:219], v[132:133], v[134:135]\n " \
  "v_pk_add_f32 v[216:217], v[216:217], v[218:219]\n v_add_f32 v193, v216, v217\n v_pk_add_f32 v[216:217], v[244:245], v[246:247]\n " \
  "v_pk_add_f32 v[218:219], v[108:109], v[110:111]\n v_pk_add_f32 v[216:217], v[216:217], v[218:219]\n v_add_f32 v194, v216, v217\n " \
  "v_pk_add_f32 v[216:217], v[112:113], v[114:115]\n v_pk_add_f32 v[218:219], v[116:117], v[118:119]\n v_pk_add_f32 v[216:217], v[216:217], v[218:219]\n " \
  "v_add_f32 v195, v216, v217\n v_pk_mul_f32 v[196:197], v[196:197], v[170:171] op_sel_hi:[1,0]\n " \
  "v_pk_mul_f32 v[198:199], v[198:199], v[170:171] op_sel_hi:[1,0]\n v_permlane16_swap_b32 v192, v193\n v_permlane16_swap_b32 v194, v195\n " \
  "v_cvt_pk_bf16_f32 v196, v196, v197\n v_cvt_pk_bf16_f32 v197, v198, v199\n v_add_f32 v192, v192, v193\n v_add_f32 v194, v194, v195\n " \
  "ds_write_b64 v58, v[196:197] offset:2304\n v_pk_mul_f32 v[84:85], v[84:85], v[170:171] op_sel_hi:[1,0]\n v_permlane32_swap_b32 v192, v194\n " \
  "v_pk_mul_f32 v[86:87], v[86:87], v[170:171] op_sel_hi:[1,0]\n v_cvt_pk_bf16_f32 v84, v84, v85\n v_add_f32 v192, v192, v194\n " \
  "v_cvt_pk_bf16_f32 v85, v86, v87\n ds_write_b64 v58, v[84:85] offset:2336\n v_rcp_f32 v192, v192\n " \
  "v_pk_mul_f32 v[248:249], v[248:249], v[170:171] op_sel:[0,1]\n v_pk_mul_f32 v[250:251], v[250:251], v[170:171] op_sel:[0,1]\n v_mov_b32 v194, v192\n " \
  "v_cvt_pk_bf16_f32 v248, v248, v249\n v_cvt_pk_bf16_f32 v249, v250, v251\n v_permlane32_swap_b32 v192, v194\n " \
  "ds_write_b64 v58, v[248:249] offset:2368\n v_pk_mul_f32 v[252:253], v[252:253], v[170:171] op_sel:[0,1]\n v_mov_b32 v193, v192\n " \
  "v_mov_b32 v195, v194\n v_pk_mul_f32 v[254:255], v[254:255], v[170:171] op_sel:[0,1]\n v_cvt_pk_bf16_f32 v252, v252, v253\n " \
  "v_permlane16_swap_b32 v192, v193\n v_permlane16_swap_b32 v194, v195\n v_cvt_pk_bf16_f32 v253, v254, v255\n ds_write_b64 v58, v[252:253] offset:2400\n " \
  "v_pk_mul_f32 v[220:221], v[220:221], v[192:193] op_sel_hi:[1,0]\n v_pk_mul_f32 v[222:223], v[222:223], v[192:193] op_sel_hi:[1,0]\n " \
  "v_cvt_pk_bf16_f32 v220, v220, v221\n v_cvt_pk_bf16_f32 v221, v222, v223\n ds_write_b64 v58, v[220:221] offset:18432\n " \
  "v_pk_mul_f32 v[124:125], v[124:125], v[192:193] op_sel_hi:[1,0]\n v_pk_mul_f32 v[126:127], v[126:127], v[192:193] op_sel_hi:[1,0]\n " \
  "v_cvt_pk_bf16_f32 v124, v124, v125\n v_cvt_pk_bf16_f32 v125, v126, v127\n ds_write_b64 v58, v[124:125] offset:18464\n " \
  "v_pk_mul_f32 v[128:129], v[128:129], v[192:193] op_sel:[0,1]\n v_pk_mul_f32 v[130:131], v[130:131], v[192:193] op_sel:[0,1]\n " \
  "v_cvt_pk_bf16_f32 v128, v128, v129\n v_cvt_pk_bf16_f32 v129, v130, v131\n ds_write_b64 v58, v[128:129] offset:18496\n " \
  "v_pk_mul_f32 v[132:133], v[132:133], v[192:193] op_sel:[0,1]\n v_pk_mul_f32 v[134:135], v[134:135], v[192:193] op_sel:[0,1]\n " \
  "v_cvt_pk_bf16_f32 v132, v132, v133\n v_cvt_pk_bf16_f32 v133, v134, v135\n ds_write_b64 v58, v[132:133] offset:18528\n " \
  "v_pk_mul_f32 v[244:245], v[244:245], v[194:195] op_sel_hi:[1,0]\n v_pk_mul_f32 v[246:247], v[246:247], v[194:195] op_sel_hi:[1,0]\n " \
  "v_cvt_pk_bf16_f32 v244, v244, v245\n v_cvt_pk_bf16_f32 v245, v246, v247\n ds_write_b64 v58, v[244:245] offset:20736\n " \
  "v_pk_mul_f32 v[108:109], v[108:109], v[194:195] op_sel_hi:[1,0]\n v_pk_mul_f32 v[110:111], v[110:111], v[194:195] op_sel_hi:[1,0]\n " \
  "v_cvt_pk_bf16_f32 v108, v108, v109\n v_cvt_pk_bf16_f32 v109, v110, v111\n ds_write_b64 v58, v[108:109] offset:20768\n " \
  "v_pk_mul_f32 v[112:113], v[112:113], v[194:195] op_sel:[0,1]\n v_pk_mul_f32 v[114:115], v[114:115], v[194:195] op_sel:[0,1]\n " \
  "v_cvt_pk_bf16_f32 v112, v112, v113\n v_cvt_pk_bf16_f32 v113, v114, v115\n ds_write_b64 v58, v[112:113] offset:20800\n " \
  "v_pk_mul_f32 v[116:117], v[116:117], v[194:195] op_sel:[0,1]\n v_pk_mul_f32 v[118:119], v[118:119], v[194:195] op_sel:[0,1]\n " \
  "v_cvt_pk_bf16_f32 v116, v116, v117\n v_cvt_pk_bf16_f32 v117, v118, v119\n ds_write_b64 v58, v[116:117] offset:20832\n "
#define G1_OUTPUT \
  "s_mov_b32 m0, 0x40000\n s_branch .Lg1p%=\n .Lg1q%=:\n s_sleep 1\n s_sub_u32 m0, m0, 1\n s_cbranch_scc1 .Lg1r%=\n .Lg1p%=:\n " \
  "buffer_load_dwordx4 v[216:219], %[fz], %[rs], 0 offen sc1\n buffer_load_dwordx4 v[220:223], %[fz], %[rs], 0 offen offset:64 sc1\n " \
  "buffer_load_dwordx4 v[200:203], %[fo], %[rs], 0 offen offset:0 sc1\n buffer_load_dwordx4 v[204:207], %[fo], %[rs], 0 offen offset:2048 sc1\n " \
  "buffer_load_dwordx4 v[208:211], %[fo], %[rs], 0 offen offset:1024 sc1\n buffer_load_dwordx4 v[212:215], %[fo], %[rs], 0 offen offset:3072 sc1\n " \
  "s_waitcnt vmcnt(5)\n v_max_u32 v152, v216, v217\n v_max3_u32 v152, v218, v219, v152\n s_waitcnt vmcnt(4)\n v_max3_u32 v152, v220, v221, v152\n " \
  "v_max3_u32 v152, v222, v223, v152\n s_waitcnt vmcnt(3)\n v_max3_u32 v152, v200, v201, v152\n v_max3_u32 v152, v202, v203, v152\n s_waitcnt vmcnt(2)\n " \
  "v_max3_u32 v152, v204, v205, v152\n v_max3_u32 v152, v206, v207, v152\n s_waitcnt vmcnt(1)\n v_max3_u32 v152, v208, v209, v152\n " \
  "v_max3_u32 v152, v210, v211, v152\n s_waitcnt vmcnt(0)\n v_max3_u32 v152, v212, v213, v152\n v_max3_u32 v152, v214, v215, v152\n " \
  "v_cmp_eq_u32 vcc, -1, v152\n s_cbranch_vccnz .Lg1q%=\n .Lg1r%=:\n v_mfma_f32_16x16x32_bf16 v[160:163], v[216:219], v[224:227], 0\n " \
  "v_mfma_f32_16x16x32_bf16 v[160:163], v[220:223], v[228:231], v[160:163]\n v_mfma_f32_16x16x32_bf16 v[152:155], v[200:203], v[224:227], 0\n " \
  "v_mfma_f32_16x16x32_bf16 v[152:155], v[204:207], v[228:231], v[152:155]\n v_mfma_f32_16x16x32_bf16 v[156:159], v[208:211], v[224:227], 0\n " \
  "v_mfma_f32_16x16x32_bf16 v[156:159], v[212:215], v[228:231], v[156:159]\n v_mfma_f32_16x16x32_bf16 v[172:175], v[216:219], v[232:235], 0\n " \
  "v_mfma_f32_16x16x32_bf16 v[172:175], v[220:223], v[236:239], v[172:175]\n v_mfma_f32_16x16x32_bf16 v[164:167], v[200:203], v[232:235], 0\n " \
  "v_mfma_f32_16x16x32_bf16 v[164:167], v[204:207], v[236:239], v[164:167]\n v_mfma_f32_16x16x32_bf16 v[168:171], v[208:211], v[232:235], 0\n " \
  "v_add_f32 v160, 0x358637bd, v160\n v_rcp_f32 v160, v160\n v_mfma_f32_16x16x32_bf16 v[168:171], v[212:215], v[236:239], v[168:171]\n " \
  "v_mul_f32 v152, v160, v152\n v_mfma_f32_16x16x32_bf16 v[184:187], v[216:219], v[240:243], 0\n v_mul_f32 v153, v160, v153\n " \
  "v_cvt_pk_bf16_f32 v152, v152, v153\n v_mul_f32 v154, v160, v154\n v_mfma_f32_16x16x32_bf16 v[184:187], v[220:223], v[244:247], v[184:187]\n " \
  "v_mul_f32 v155, v160, v155\n v_cvt_pk_bf16_f32 v153, v154, v155\n v_mul_f32 v156, v160, v156\n " \
  "v_mfma_f32_16x16x32_bf16 v[176:179], v[200:203], v[240:243], 0\n v_mul_f32 v157, v160, v157\n v_cvt_pk_bf16_f32 v154, v156, v157\n " \
  "v_mul_f32 v158, v160, v158\n v_mfma_f32_16x16x32_bf16 v[176:179], v[204:207], v[244:247], v[176:179]\n v_mul_f32 v159, v160, v159\n " \
  "v_cvt_pk_bf16_f32 v155, v158, v159\n buffer_store_dwordx4 v[152:155], %[go], %[rs], 0 offen offset:0 sc1\n " \
  "v_mfma_f32_16x16x32_bf16 v[180:183], v[208:211], v[240:243], 0\n ds_write_b128 %[lo], v[152:155] offset:0\n v_add_f32 v172, 0x358637bd, v172\n " \
  "v_mfma_f32_16x16x32_bf16 v[180:183], v[212:215], v[244:247], v[180:183]\n v_rcp_f32 v172, v172\n " \
  "v_mfma_f32_16x16x32_bf16 v[196:199], v[216:219], v[248:251], 0\n v_mul_f32 v164, v172, v164\n v_mul_f32 v165, v172, v165\n " \
  "v_cvt_pk_bf16_f32 v164, v164, v165\n v_mfma_f32_16x16x32_bf16 v[196:199], v[220:223], v[252:255], v[196:199]\n v_mul_f32 v166, v172, v166\n " \
  "v_mul_f32 v167, v172, v167\n v_cvt_pk_bf16_f32 v165, v166, v167\n v_mfma_f32_16x16x32_bf16 v[188:191], v[200:203], v[248:251], 0\n " \
  "v_mul_f32 v168, v172, v168\n v_mul_f32 v169, v172, v169\n v_cvt_pk_bf16_f32 v166, v168, v169\n " \
  "v_mfma_f32_16x16x32_bf16 v[188:191], v[204:207], v[252:255], v[188:191]\n v_mul_f32 v170, v172, v170\n v_mul_f32 v171, v172, v171\n " \
  "v_cvt_pk_bf16_f32 v167, v170, v171\n v_mfma_f32_16x16x32_bf16 v[192:195], v[208:211], v[248:251], 0\n " \
  "buffer_store_dwordx4 v[164:167], %[go], %[rs], 0 offen offset:1024 sc1\n ds_write_b128 %[lo], v[164:167] offset:1024\n " \
  "v_mfma_f32_16x16x32_bf16 v[192:195], v[212:215], v[252:255], v[192:195]\n v_add_f32 v184, 0x358637bd, v184\n v_rcp_f32 v184, v184\n s_nop 0\n " \
  "v_pk_mul_f32 v[176:177], v[176:177], v[184:185] op_sel_hi:[1,0]\n v_pk_mul_f32 v[178:179], v[178:179], v[184:185] op_sel_hi:[1,0]\n " \
  "v_pk_mul_f32 v[180:181], v[180:181], v[184:185] op_sel_hi:[1,0]\n v_pk_mul_f32 v[182:183], v[182:183], v[184:185] op_sel_hi:[1,0]\n " \
  "v_cvt_pk_bf16_f32 v176, v176, v177\n v_cvt_pk_bf16_f32 v177, v178, v179\n v_cvt_pk_bf16_f32 v178, v180, v181\n v_cvt_pk_bf16_f32 v179, v182, v183\n " \
  "buffer_store_dwordx4 v[176:179], %[go], %[rs], 0 offen offset:2048 sc1\n ds_write_b128 %[lo], v[176:179] offset:2048\n " \
  "v_add_f32 v196, 0x358637bd, v196\n v_rcp_f32 v196, v196\n s_nop 0\n v_pk_mul_f32 v[188:189], v[188:189], v[196:197] op_sel_hi:[1,0]\n " \
  "v_pk_mul_f32 v[190:191], v[190:191], v[196:197] op_sel_hi:[1,0]\n v_pk_mul_f32 v[192:193], v[192:193], v[196:197] op_sel_hi:[1,0]\n " \
  "v_pk_mul_f32 v[194:195], v[194:195], v[196:197] op_sel_hi:[1,0]\n v_cvt_pk_bf16_f32 v188, v188, v189\n v_cvt_pk_bf16_f32 v189, v190, v191\n " \
  "v_cvt_pk_bf16_f32 v190, v192, v193\n v_cvt_pk_bf16_f32 v191, v194, v195\n buffer_store_dwordx4 v[188:191], %[go], %[rs], 0 offen offset:3072 sc1\n " \
  "ds_write_b128 %[lo], v[188:191] offset:3072\n "
#define G1_PURGE \
  ".purgem G1M\n .purgem G1RA\n .purgem G1RB\n .purgem G1BK\n .purgem G1AK\n .purgem G1RDB\n .purgem G1RDA\n .purgem G1Q\n .purgem G1SG\n "
#define G1_CLOBBER \
  "v80", "v81", "v82", "v83", "v84", "v85", "v86", "v87", "v92", "v93", "v94", "v95", "v96", "v97", "v98", "v99", "v100", "v101", "v102", "v103", \
  "v104", "v105", "v106", "v107", "v108", "v109", "v110", "v111", \
  "v112", "v113", "v114", "v115", "v116", "v117", "v118", "v119", "v120", "v121", "v122", "v123", "v124", "v125", "v126", "v127", "v128", "v129", \
  "v130", "v131", "v132", "v133", "v134", "v135", "v144", "v145", "v146", "v147", "v152", "v153", "v154", "v155", \
  "v156", "v157", "v158", "v159"

template <int MODE>
__device__ __forceinline__ void g1_phase(char* smem, char* ws, u64& E, int w, f32x16& xq, Next& nx, int layer, const u16* __restrict__ x0, const u16* __restrict__ W,
                                         const u16* __restrict__ wo, const u16* __restrict__ wphi,
                                         const u16* __restrict__ wn1, const u16* __restrict__ wn2) {
  constexpr bool HH = (MODE == 1);
  constexpr int ABYTES = 128 * 128;
  constexpr int STAGE = ABYTES + 3 * 8192;
  constexpr int OFF_FX = 16384;
  constexpr int OFF_SP = 3 * STAGE;
  constexpr int OFF_RS = !HH ? 159744 : OFF_SP + 16384;
  constexpr int OFF_CS = OFF_RS + (HH ? 1152 : 1024);
  constexpr int OFF_WP = OFF_CS + 1536;
  constexpr int OFF_JK = LDS_TOTAL - 1024;
  constexpr int OFF_PK = 0;
  constexpr int OFF_VF = OFF_WP + 4096;
  constexpr int OFF_R2 = HH ? OFF_VF + 16384 : OFF_CS + 1024;
  constexpr int OFF_RD = OFF_CS + 768;
  constexpr int PSTR = 144;
  IDS
  W = uni(W); wphi = uni(wphi); wn1 = uni(wn1); wn2 = uni(wn2);
  const int kg = w >> 2, wm = (w >> 1) & 1, wn = w & 1;
  const bool first = !HH && layer == 0;
  f32x4 acc[2][2], qh[2][2], csq[2], st01, st23;
  bf16x8 wf[2];
  u64 kol;
  u32x4 pw[8];
  ST(layer * 24 + 0)
  const int R0 = wm * 64;
  u32x16 pa;
#define pcs pa[0]
#define prs pa[1]
#define opo pa[2]
#define ogo pa[3]
#define hof pa[7]
#define dm pa[8]
#define dm2 pa[9]
#define pcv pa[4]
#define pwf pa[5]
#define prv pa[6]
#define pvf pa[7]
#define ppk pa[10]
#define ppa pa[11]
#define pvr pa[12]
#define gkv pa[13]
#define gkz pa[14]
#define pwl pa[15]
#define pwm pa[4]
#define plb pa[10]
#define prd pa[11]
#define dm3 pa[12]
  f32x4 bias[3];
  const char* wos = wo_src(ws, wo, layer, hp);
  const unsigned tO = img(layer, hp) ? 8192u : 131072u;
  const unsigned sb = lds_addr(smem);
  auto prep = [&](auto ldr) {
    constexpr bool LDR = decltype(ldr)::value;
    if constexpr (!LDR) {
      pcs = sb + OFF_CS + wn * 128 + g * 16;
      prs = sb + OFF_RS + (R0 + fr) * 8;
      opo = (unsigned)(wn * 8192 + R0 * 64 + lane * 16);
      ogo = opo + WS_O + layer * (SEQ * DM * 2) + (unsigned)((rt * NH + 2 * hp) * 8192);
      opo += sb;
      pcv = sb + OFF_CS + (wn * 32 + 128 + fr) * 4;
      prv = sb + OFF_R2 + (R0 + 4 * g) * 4;
    } else {
      constexpr unsigned kvn = WS_KV + MODE * KVLB;
      const int t = tid & 255;
      dm = kvn + (unsigned)((rt * NH + 2 * hp) * KVPB) + t * 16;
      dm2 = t < 16 ? dm + 8192 : t >= 64 && t < 97 ? kvn + 16u * NH * KVPB + (unsigned)(hp * KVFB + rt * 528 + (t - 64) * 16) : 0x7fff0000u;
      if (!HH) dm3 = WS_HL + (unsigned)((256 + rt * 16 + hp) * 4096 + t * 16);
    }
    if constexpr (!HH) {
      prd = sb + OFF_RD + wn * 4;
      if constexpr (!LDR) {
        plb = sb + OFF_FX + wn * 2048 + lane * 16;
        hof = m0 + wm == 0 ? 0x7fff0000u : WS_HL + (unsigned)((layer * 128 + rt * 16 - 16 + wm * 16 + hp) * 4096 + wn * 2048 + lane * 16);
        const unsigned out = ~((0x5555u << 2 << fr) >> (4 * g));
#pragma unroll
        for (int i = 0; i < 8; ++i) {
          unsigned t;
          asm("v_bfe_i32 %0, %1, %2, 1" : "=v"(t) : "v"(out), "n"((i >> 2) * 16 + (i & 3)));
          bias[i >> 2][i & 3] = __uint_as_float(t & 0xff61b1e6u);
        }
        bias[2] = m0 + R0 == 0 ? f32x4{-3.0e38f, -3.0e38f, -3.0e38f, -3.0e38f} : bias[0];
        asm volatile("" : "+{v[36:39]}"(bias[0]), "+{v[40:43]}"(bias[1]), "+{v[44:47]}"(bias[2]));
      }
    } else {
      const int u = wm * 2 + kg;
      if constexpr (!LDR) {
        pwf = sb + OFF_WP + wn * 2048 + (4 * g + q4) * 64 + p4 * 8;
        pvf = sb + OFF_VF + w * 2048 + lane * 16;
        ppk = sb + OFF_PK + w * (32 * PSTR) + fr * PSTR + g * 8;
      }
      ppa = sb + OFF_PK + wn * (32 * PSTR) + (4 * g + q4) * PSTR + u * 32 + p4 * 8;
      pvr = sb + OFF_VF + wn * 2048 + lane * 16;
      const unsigned doff = WS_KV + (layer >> 1) * KVLB + (unsigned)((rt * NH + 2 * hp + wn) * KVPB);
      gkv = doff + u * 1024 + lane * 16;
      gkz = fr == 0 ? doff + 4096 + u * 32 + g * 8 : 0x7fff0000u;
    }
    asm volatile("" : "+{v[48:63]}"(pa));
  };

  if (kg == 1) {

    const int lw = w & 3;
    const int kB = 16 * lw + (lane >> 3);
    const int fB = (kB >> 1) & 1;
    const int cB0 = ((((lane & 7) >> 1) ^ fB) << 1) | (lane & 1), cB1 = ((((lane & 7) >> 1) ^ (fB | 2)) << 1) | (lane & 1);
    const bool im = img(layer, hp);

    unsigned vW0, vW1, vP, vX, vY;
    u64 ko2;
    auto ld_pw = [&](int i0) {
#pragma unroll
      for (int i = i0; i < i0 + 4; ++i) gld16(pw[i], vP, wn1 + i * 8 * 3072);
    };
    auto wo_tile = [&](int t) { glds2v(wos + knib(ko2, t) * tO, vW0, vW1, lds_addr(smem) + OFF_WO + t * 8192 + lw * 2048); };
    auto run = [&](auto fst) {
      constexpr bool FIRST = decltype(fst)::value;
      constexpr int N = 10;
      const unsigned vA = FIRST ? (unsigned)(((m0 + lw * 32 + (lane >> 3)) * DM + ((lane & 7) ^ (lane >> 3)) * 8) * 2)
                                : (unsigned)(rt * 262144 + lw * 4096 + lane * 16);
      unsigned imv = !FIRST && im ? ~0u : 0u;
      if (!FIRST) asm volatile("" : "+v"(imv));
      const unsigned vB0 = ((unsigned)(lw * 6144 + lane * 16) & imv) | ((unsigned)((kB * 3072 + hp * 64 + cB0 * 8) * 2) & ~imv);
      const unsigned vB1 = ((unsigned)(lw * 6144 + lane * 16) & imv) | (((unsigned)(((kB + 8) * 3072 + hp * 64 + cB1 * 8) * 2) - 1024u) & ~imv);
      const char* xa = FIRST ? (const char*)x0 : ws + WS_X + (layer - 1) * (SEQ * DM * 2);
      constexpr int KS = FIRST ? 128 : 16384;
      const u64 kord = FIRST ? korder0(rt) : nx.kord;
      auto ktile = [&](int ks) { return knib(kord, ks); };
      auto stageA = [&](int ks, int boff) {
        const int kt = ktile(ks);
        const unsigned sA = lds_addr(smem) + boff;
        if (FIRST) glds4v(xa + kt * KS, vA, vA + 15360u, vA + 2 * 15360u, vA + 3 * 15360u, sA + lw * 4096);
        else glds4c(xa + kt * KS, vA, sA + lw * 4096);
      };
      auto stageB = [&](const u16* Wp, int ks, int boff) {
        const int kt = ktile(ks);
        const unsigned sB = lds_addr(smem) + boff + ABYTES;
        const char* b = FIRST ? (const char*)(Wp + (size_t)kt * 64 * 3072) : nx.wq + kt * nx.tw;
        glds4v(b, vB0, vB1, vB0, vB1, sB + lw * 6144);
        glds2v(b + 4096, vB0, vB1, sB + lw * 6144 + 4096);
      };
      bf16x8 sel[3];
      unsigned cso[3], cst;
      if (FIRST) {
        WAITVN(N);
        BAR;
        stageA(2, 2 * STAGE); stageB(W, 2, 2 * STAGE);
        __builtin_amdgcn_sched_barrier(0);
        const int fl = (q4 >> 1) | ((g & 1) << 1), j = 3 * wm + (fr < 3 ? fr : 0);
#pragma unroll
        for (int i = 0; i < 3; ++i) {
          const unsigned o = fr == i ? 0x3F803F80u : 0u;
          sel[i] = __builtin_bit_cast(bf16x8, u32x4{o, o, o, o});
          cso[i] = (unsigned)(2 * STAGE + ABYTES + (g >> 1) * 6144 + (g & 1) * 1024 + q4 * 128 + p4 * 8 + (((wn * 2 + ((wm + i) & 1)) ^ fl) << 5) + ((3 * wm + i) >> 1) * 2048);
          asm volatile("" : "+v"(sel[i]), "+v"(cso[i]));
        }
        cst = fr < 3 ? WS_CPA + (unsigned)(((hp * 16 + rt) * 192 + (j >> 1) * 64 + wn * 32 + (j & 1) * 16 + 4 * g) * 4) : 0x7fff0000u;
        asm volatile("" : "+v"(cst));
      }
      u64 fall, fal2, fal3, fsv = E + (unsigned)(layer + 1);
      unsigned fao, fa2, vS, vC, dS, dC, dR;
      auto setup = [&]() {
        if (FIRST) { asm volatile("s_load_dwordx2 %0, %1, 0x0\n\ts_waitcnt lgkmcnt(0)" : "=s"(E) : "s"(ws) : "memory"); fall = E + 1u; asm volatile("" : "+v"(fall)); }
        fao = (unsigned)((256 + rt * 16 + (lane & 15)) * FLS);
        fa2 = (unsigned)(((FIRST ? 512 : 256) + (lane & 15) * 16 + (FIRST ? hp : (hp - ((1 << (lane >> 4)) >> 1)) & 15)) * FLS);
        vP = (unsigned)((rt * 64 + (lane >> 3)) * 6144 + lw * 2048 + ((hp + (1 << lw)) & 15) * 128 + (lane & 7) * 16);
        vS = FIRST ? (unsigned)(WS_G0 + (m0 + lw * 32 + (lane & 31)) * 16)
                   : WS_ST + (unsigned)((layer - 1) * (SEQ * 128) + rt * 16384 + g * 1024 + lw * 256 + fr * 16);
        vC = (FIRST ? WS_CPA : WS_CSP) + (unsigned)(hp * 12288 + g * 768 + lw * 192 + (fr < 12 ? fr : 0) * 16);
        if (!FIRST) vX = HH ? (unsigned)(hp * 4096 + lw * 1024 + lane * 16) : (unsigned)((rt * 16 + hp) * 1024 + lane * 16);
        if (HH) { vY = (unsigned)(bid * 8192 + lw * 2048 + lane * 16); asm volatile("" : "+v"(vY)); }
        vW0 = ((unsigned)(lw * 2048 + lane * 16) & imv) | ((unsigned)((kB * DM + hp * 64 + ((lane & 7) ^ ((lane >> 4) << 1)) * 8) * 2) & ~imv); vW1 = vW0 + ((8 * DM * 2 - 1024u) & ~imv);
        dS = OFF_RS + lw * 256 + (FIRST ? (lane & 31) * 8 : fr * 16 + (g & 1) * 8); dC = OFF_CS + lw * 192 + (fr < 12 ? fr : 0) * 16 + g * 4;
        ko2 = korder(bid & 3, hp);
        dR = OFF_R2 + lw * 128 + (FIRST ? (lane & 31) * 4 : fr * 8 + (g & 1) * 4);
        asm volatile("" : "+v"(dR));
        asm volatile("" : "+v"(vS), "+v"(vX), "+v"(vW0), "+v"(vW1), "+v"(dS));
        asm volatile("" : "+s"(ko2));
        if (FIRST || HH) asm volatile("" : "+v"(vC), "+v"(dC), "+v"(fa2));
        if (FIRST) asm volatile("" : "+v"(vP)); else asm volatile("" : "+v"(fao));
      };
      if (!FIRST) { setup(); asm volatile("" : "+v"(fsv)); }
      u32x4 sv[4], cv[4];
      auto ld_c = [&](u32x4& d, unsigned v, int so) { asm volatile("buffer_load_dwordx4 %0, %1, %2, %3 offen sc1" : "=v"(d) : "v"(v), "s"(rsW), "s"(so) : "memory"); };
      float tm, tr;
      unsigned tc;
      auto fin = [&]() {
        if (FIRST) {
          const unsigned tag = (unsigned)E + 1u;
          asm volatile("" : "+v"(sv[0]));
          _Pragma("unroll 1") for (unsigned spins = 0; spins < (1u << 18); ++spins) {
            if (__all(sv[0][1] == tag && sv[0][3] == tag)) break;
            __builtin_amdgcn_s_sleep(1);
            asm volatile("buffer_load_dwordx4 %0, %1, %2, 0 offen sc1\n\ts_waitcnt vmcnt(0)" : "=v"(sv[0]) : "v"(vS), "s"(rsW) : "memory");
          }
          tm = __uint_as_float(sv[0][0]); tr = __uint_as_float(sv[0][2]);
        } else {
          asm volatile("" : "+v"(sv[0]), "+v"(sv[1]), "+v"(sv[2]), "+v"(sv[3]));
          const f32x4 t = sum4(sv);
          const unsigned u = xs4(t[0], t[2], t[1], t[3]);
          auto h = __builtin_amdgcn_permlane32_swap(u, u, false, false);
          const float mean = __uint_as_float(h[0]) * (1.0f / 1024.0f);
          const float var = fmaxf(__uint_as_float(h[1]) * (1.0f / 1024.0f) - mean * mean, 0.f);
          tm = -mean; tr = __builtin_amdgcn_rsqf(var + 1e-5f);
        }
      };
      auto cfin = [&]() {
        if (FIRST || HH) {
          asm volatile("" : "+v"(cv[0]), "+v"(cv[1]), "+v"(cv[2]), "+v"(cv[3]));
          const f32x4 t = sum4(cv);
          tc = xs4(t[0], t[1], t[2], t[3]);
        }
      };
      bf16x8 cf[6];
      f32x4 csum = {0, 0, 0, 0};
      if (FIRST) { vX = (unsigned)(bid * 8192 + lw * 2048 + lane * 16); asm volatile("" : "+v"(vX)); }
#pragma unroll
      for (int kt = 0; kt < 13; ++kt) {
        const int cur = (kt % 3) * STAGE;
        if (!FIRST && kt == 0) { WAITV(4); }
        else if (FIRST && kt == 4 && lw == 1) { WAITVN(N + 2); }
        else if (HH && (kt == 4 || kt == 6)) { WAITVN(N + 1); }
        else if (HH && kt == 5) { WAITVN(N + 2); }
        else if ((!FIRST && kt == 1 && lw == 3) || (!FIRST && kt == 9 && lw == 2) || ((FIRST || HH) && kt == 10 && lw == 3) || (FIRST && (kt == 2 || kt == 4 || kt == 5))) { WAITVN(N + 1); }
        else { WAITVN(N); }
        if (!FIRST && kt == 2 && lw == 3) {
          asm volatile("" : "+v"(fall));
          if (__any(fall != E + (unsigned)layer)) poll_flags(ws, E, 1, rt, hp, lane, layer, false);
        }
        if (!FIRST && kt == 10 && lw == 2) {
          asm volatile("" : "+v"(fal3));
          if (__any(fal3 != E + (unsigned)(layer + 1))) poll_flags(ws, E, 2, rt, hp, lane, layer + 1, false);
        }
        if (HH && kt == 11 && lw == 3) {
          asm volatile("" : "+v"(fal2));
          if (__any(fal2 != E + (unsigned)layer)) {
            _Pragma("unroll 1") for (int j = 0; j < 4; ++j) poll_flags(ws, E, 1, rt, (hp - ((1 << j) >> 1)) & 15, lane, layer, true);
          }
        }
        if (FIRST && kt == 11 && lw == 3) {
          asm volatile("" : "+v"(fal2));
          if (__any(fal2 != E + 1u)) poll_flags(ws, E, 2, rt, hp, lane, 1u, true);
        }
        BAR;
        if (FIRST && kt == 1) {
          asm volatile("" ::: "memory");
#pragma unroll
          for (int i = 0; i < 6; ++i) { const char* t = smem + cso[i >> 1] + (i & 1) * 12288; cf[i] = cat8(tr_read(t), tr_read(t + 512)); }
        }
        if (FIRST && kt >= 2 && kt < 6 && lw == kt - 2) setup();
        if (kt >= 6 && kt < 10 && lw == kt - 6) prep(IC<1>{});
        stageA(kt + 3, cur);
        if (FIRST && kt == 1) {
          asm volatile("" : "+v"(sel[0]));
#pragma unroll
          for (int i = 0; i < 6; ++i) csum = MFMA(cf[i], sel[i >> 1], csum);
          asm volatile("" : "+v"(csum));
        }
        stageB(W, kt + 3, cur);
        if (FIRST) {
          if (kt == 1) __builtin_amdgcn_raw_buffer_store_b128(__builtin_bit_cast(u32x4, csum), rsW, cst, 0, 16);
          if (kt == 3 && tid == 320) __builtin_amdgcn_raw_buffer_store_b64(__builtin_bit_cast(u32x2, fall), rsW, WS_FLAGS + (unsigned)(512 + rt * 16 + hp) * FLS, 0, 16);
          if (kt == 3 || kt == 4) glds16s(wo + (kt - 3) * 512, vX, lds_addr(smem) + OFF_JK);
        }
        if (HH && (kt == 3 || kt == 4)) glds16s(wo + (kt - 3) * 512, vY, lds_addr(smem) + OFF_JK);
        if (!FIRST && kt == 3 && tid == 320) __builtin_amdgcn_raw_buffer_store_b64(__builtin_bit_cast(u32x2, fsv), rsW, WS_FLAGS + (unsigned)(512 + rt * 16 + hp) * FLS, 0, 16);
        if (!FIRST && kt == 8 && lw == 2) asm volatile("global_load_dwordx2 %0, %1, %2 sc1" : "=v"(fal3) : "v"(fao), "s"(ws + WS_FLAGS + 256 * FLS) : "memory");
        if (!FIRST && kt == 0 && lw == 3) asm volatile("global_load_dwordx2 %0, %1, %2 sc1" : "=v"(fall) : "v"(fao), "s"(ws + WS_FLAGS) : "memory");
        if ((FIRST || HH) && kt == 9 && lw == 3) asm volatile("global_load_dwordx2 %0, %1, %2 sc1" : "=v"(fal2) : "v"(fa2), "s"(ws + WS_FLAGS) : "memory");
      }
      WAITVN(N);
      BAR;
#pragma unroll
      for (int i = 0; i < (FIRST ? 1 : 4); ++i) ld_c(sv[i], vS, i * 4096);
      if (FIRST || HH) {
#pragma unroll
        for (int i = 0; i < 4; ++i) ld_c(cv[i], vC, i * 3072);
      }
      if (HH) glds16s(wphi, vX, lds_addr(smem) + OFF_WP + lw * 1024);
      else if (!FIRST) glds16c(ws + WS_CSA, vX, lds_addr(smem) + OFF_CS);
      wo_tile(0); wo_tile(1);
      WAITVN(HH ? 13 : 9);
      BAR;
      if (FIRST) {
        if (lw < 3) { ld_pw(0); ld_pw(4); WAITV(12); } else { WAITV(4); }
      } else {
        WAITV(4);
      }
      cfin();
      fin();
      asm volatile("" : "+v"(tm), "+v"(tr));
      if (FIRST || HH) asm volatile("" : "+v"(tc));
      if (FIRST && lw < 3) { WAITV(8); } else { WAITV(0); }
      lds<f32x2>(sb + dS) = f32x2{tm, tr};
      lds<float>(sb + dR) = tm; lds<float>(sb + dR + 512) = tr;
      if (FIRST || HH) lds<unsigned>(sb + dC) = tc;
      if (!HH) lds<float>(prd) = zero1();
      WAITL0;
    };
    typedef std::integral_constant<bool, true> T_;
    typedef std::integral_constant<bool, false> F_;
    __builtin_amdgcn_s_setprio(3);
    if (first) run(T_{}); else run(F_{});
    __builtin_amdgcn_s_setprio(0);
    BAR;
    if constexpr (HH) {
      u32x4 c0, c1;
      if (layer == 1) {
        const int n = bid * 8 + lw * 2;
        const void* p;
        unsigned a0, a1;
        if (n < 1536) {
          const int r = n % 768, q = r % 384, e = q % 24;
          p = n < 768 ? wn1 : W;
          a0 = (unsigned)((((q / 24) * 64 + (e / 6) * 16 + (lane >> 3)) * 3072 + ((e % 6) >> 1) * 1024 + (r < 384 ? 192 : 704)) * 2);
          a1 = a0 + 8 * 6144 + cB1 * 16; a0 += cB0 * 16;
        } else {
          p = n < 1792 ? wn2 : wo;
          a0 = (unsigned)((((n & 127) * 8 + (lane >> 3)) * DM + ((n & 128) ? 704 : 192) + ((lane & 7) ^ ((lane >> 4) << 1)) * 8) * 2);
          a1 = a0 + 8 * DM * 2;
        }
        p = uni(p);
        gld16(c0, a0, p); gld16(c1, a1, p);
        const u32x4 pz = empty4();
        __builtin_amdgcn_raw_buffer_store_b128(pz, rsW, dm, 0, 16);
        __builtin_amdgcn_raw_buffer_store_b128(pz, rsW, dm, 4096, 16);
        __builtin_amdgcn_raw_buffer_store_b128(pz, rsW, dm2, 0, 16);
      }
#pragma unroll
      for (int t = 2; t < 6; ++t) wo_tile(t);
      if (layer == 1) {
        WAITV(11);
        __builtin_amdgcn_sched_barrier(0);
        asm volatile("" : "+v"(c0), "+v"(c1));
        __builtin_amdgcn_raw_buffer_store_b128(c0, rsW, WS_IMG + (unsigned)(bid * 8192 + lw * 2048 + lane * 16), 0, 16);
        __builtin_amdgcn_raw_buffer_store_b128(c1, rsW, WS_IMG + (unsigned)(bid * 8192 + lw * 2048 + 1024 + lane * 16), 0, 16);
        WAITV(0);
      }
      pwl = vW0; pwm = vW1; kol = ko2;
      asm volatile("" : "=v"(csq[0]), "=v"(csq[1]), "=v"(st01), "=v"(st23), "=v"(wf[0]), "=v"(wf[1]));
      asm volatile("" : "=v"(acc[0][0]), "=v"(acc[0][1]), "=v"(acc[1][0]), "=v"(acc[1][1]));
      asm volatile("" : "=v"(qh[0][0]), "=v"(qh[0][1]), "=v"(qh[1][0]), "=v"(qh[1][1]));
    } else {
      const int lane = lane_id();
#pragma unroll
      for (int t = 2; t < 12; ++t) {
        wo_tile(t);
        if (t == 2 && first) {
        const u32x4 pz = empty4();
        __builtin_amdgcn_raw_buffer_store_b128(pz, rsW, dm, 0, 16);
        __builtin_amdgcn_raw_buffer_store_b128(pz, rsW, dm, 4096, 16);
        __builtin_amdgcn_raw_buffer_store_b128(pz, rsW, dm3, 0, 16);
        __builtin_amdgcn_raw_buffer_store_b128(pz, rsW, dm2, 0, 16);
        if (lw == 3 && lane < 48) __builtin_amdgcn_raw_buffer_store_b128(lds<u32x4>(sb + OFF_CS + lane * 16), rsW, WS_CSA + (unsigned)((rt * 16 + hp) * 1024 + lane * 16), 0, 16);
        }
      }
      if (first) { BAR;   WAITV(0); }
    }
    asm volatile("" : "={v[64:79]}"(xq));
  } else {

    prep(IC<0>{});
    if (first) {
      const int row = m0 + hp * 8 + w * 2 + (lane >> 5);
      asm volatile("s_load_dwordx2 %0, %1, 0x0\n\ts_waitcnt lgkmcnt(0)" : "=s"(E) : "s"(ws) : "memory");
      const unsigned tag = (unsigned)E + 1u;
      static_assert(HH || (OFF_SP == 122880 && OFF_R2 + 1024 <= OFF_JK), "addresses used at the top of the kernel");
      WAITV(0);
      u32x4 xs[4];
#pragma unroll
      for (int i = 0; i < 4; ++i) xs[i] = *(const u32x4*)(smem + OFF_SP + w * 4096 + (lane >> 5) * 2048 + (lane & 31) * 16 + i * 512);
      typedef __attribute__((ext_vector_type(2))) __bf16 bf2;
      const bf2 one2 = __builtin_bit_cast(bf2, 0x3F803F80u);
      float sa[2] = {zero1(), zero1()}, sq[2] = {zero1(), zero1()};
#pragma unroll
      for (int i = 0; i < 16; ++i) {
        const unsigned u = xs[i >> 2][i & 3];
        const bf2 t = __builtin_bit_cast(bf2, u);
        sa[i & 1] = __builtin_amdgcn_fdot2_f32_bf16(t, one2, sa[i & 1], false);
        sq[i & 1] = __builtin_amdgcn_fdot2_f32_bf16(t, t, sq[i & 1], false);
      }
      float sm = r16_sum(sa[0] + sa[1]), m2 = r16_sum(sq[0] + sq[1]);
      {
        auto r = __builtin_amdgcn_permlane16_swap(__float_as_uint(sm), __float_as_uint(sm), false, false);
        auto q = __builtin_amdgcn_permlane16_swap(__float_as_uint(m2), __float_as_uint(m2), false, false);
        sm = __uint_as_float(r[0]) + __uint_as_float(r[1]); m2 = __uint_as_float(q[0]) + __uint_as_float(q[1]);
      }
      const float mean = sm * (1.0f / 1024.0f);
      m2 = fmaxf(m2 - sm * mean, 0.f);
      u32x4 gw = {__float_as_uint(-mean), tag, __float_as_uint(rsqrtf(m2 * (1.0f / 1024.0f) + 1e-5f)), tag};
      if ((lane & 31) == 0) __builtin_amdgcn_raw_buffer_store_b128(gw, rsW, (unsigned)(WS_G0 + row * 16), 0, 16);
      __builtin_amdgcn_raw_buffer_store_b128(empty4(), rsW, WS_HL + (unsigned)((rt * 16 + hp) * 4096 + tid * 16), 0, 16);
    }
    const int fl = (q4 >> 1) | ((g & 1) << 1);
    const unsigned bo = lds_addr(smem) + ABYTES + (g >> 1) * 6144 + (g & 1) * 1024 + q4 * 128 + p4 * 8;
    const unsigned bo0 = bo + (((wn * 2 + 0) ^ fl) << 5), bo1 = bo + (((wn * 2 + 1) ^ fl) << 5);
    const unsigned ar = lds_addr(smem) + (wm * 64 + fr) * 128;
    const unsigned aK0 = first ? ar + ((g ^ (fr & 7)) << 4) : sb + wm * 8192 + lane * 16, aK1 = first ? ar + (((4 + g) ^ (fr & 7)) << 4) : aK0 + 1024;
    f32x32 ac0, ac1, ac2;
    asm volatile("" :: "v"(aK0), "v"(aK1), "v"(bo0), "v"(bo1));
    if (first) BAR;
    else publish(rsW, tid, 256 + rt * 16 + hp, E + (unsigned)layer);
#define G1_OUT
#define G1_T(a)
#define G1_TEND
#define G1_TCL
    asm volatile(G1_MACROS
        "s_mov_b32 vcc_hi, %[lay]\n"
        ".if %[hh]\n s_branch .Lg1m\n .else\n"
        ".p2align 3\n .Lg1m:\n"
        "G1RDB 0, v16, v17, 0\n G1RDA v12, v13, 0\n G1RDB 1, v16, v17, 0\n"
        "ds_read_b128 v[64:67], v20\n ds_read_b128 v[68:71], v20 offset:2048\n"
        "ds_read_b128 v[72:75], v20 offset:4096\n ds_read_b128 v[76:79], v20 offset:6144\n"
        "s_cmp_eq_u32 vcc_hi, 0\n s_waitcnt lgkmcnt(12)\n"
        "G1SG 0, 1, 1, v12, v13, v16, v17, 0\n"
        "s_mov_b32 vcc_lo, 16\n"
        "G1SG 1, 1, 1, v12, v13, v16, v17, 40960\n"
        "G1SG 2, 1, 2, v12, v13, v16, v17, 40960\n"
        "s_cbranch_scc1 .Lg1a%=\n"
        "s_barrier\n"
        G1_T(88)
        "s_barrier\n"
        G1_T(92)
        "G1RDA v12, v13, 40960\n"
        "s_waitcnt lgkmcnt(4)\n"
        "s_branch .Lg1b%=\n"
        ".Lg1a%=:\n"
        G1_T(88)
        "s_barrier\n"
        G1_T(92)
        "G1RDB 0, v16, v17, 40960\n G1RDA v12, v13, 40960\n G1RDB 1, v16, v17, 40960\n"
        ".p2align 3\n s_nop 0\n"
        ".Lg1b%=:\n"
        "s_waitcnt lgkmcnt(8)\n"
        "G1SG 0, 0, 1, v12, v13, v16, v17, 40960\n"
        "s_barrier\n"
        "G1SG 1, 0, 1, v14, v15, v18, v19, 0\n"
        "G1SG 2, 0, 1, v14, v15, v18, v19, 0\n"
        "s_lshr_b32 vcc_lo, vcc_lo, 1\n s_waitcnt vmcnt(0) lgkmcnt(8)\n"
        "G1SG 0, 0, 1, v14, v15, v18, v19, 0\n"
        "s_barrier\n"
        "G1SG 1, 0, 1, v12, v13, v16, v17, 0\n"
        "G1SG 2, 0, 1, v12, v13, v16, v17, 0\n"
        "s_cbranch_scc0 .Lg1c%=\n s_waitcnt lgkmcnt(8)\n"
        "G1SG 0, 0, 1, v12, v13, v16, v17, 0\n"
        "s_barrier\n"
        "G1SG 1, 0, 1, v12, v13, v16, v17, 40960\n"
        "G1SG 2, 0, 1, v12, v13, v16, v17, 40960\n"
        "s_branch .Lg1b%=\n"
        ".Lg1c%=:\n"
        "s_waitcnt lgkmcnt(8)\n"
        "G1SG 0, 0, 1, v12, v13, v16, v17, 0\n s_bitcmp1_b32 vcc_hi, 0\n"
        G1_T(96)
        "s_cbranch_scc1 .Lg1n\n s_barrier\n"
        G1_SLIDE
        ".endif\n .if %[hh]\n .p2align 3\n s_nop 0\n .Lg1n:\n s_barrier\n"
        G1_HEDGE
        ".endif\n"
        G1_TEND G1_PURGE
        : "=&{v[160:191]}"(ac0), "=&{v[192:223]}"(ac1), "=&{v[224:255]}"(ac2), "=&{v[64:79]}"(xq),
          "=&{v[88:91]}"(csq[1]), "=&{v[148:151]}"(csq[0]), "=&{v[140:143]}"(st01), "=&{v[136:139]}"(st23),
          "+{v[48:63]}"(pa), "+{v[36:39]}"(bias[0]), "+{v[40:43]}"(bias[1]), "+{v[44:47]}"(bias[2]) G1_OUT
        : "{v12}"(aK0), "{v13}"(aK1), "{v14}"(aK0 + 2 * STAGE), "{v15}"(aK1 + 2 * STAGE), "{v16}"(bo0), "{v17}"(bo1),
          "{v18}"(bo0 + 2 * STAGE), "{v19}"(bo1 + 2 * STAGE), "{v20}"(wn ? aK1 : aK0),
          [lay] "s"(layer), [hh] "n"(MODE), [rs] "s"(rsW), [wm] "s"(wm)
        : "memory", "scc", "vcc", "m0", G1_TCL G1_CLOBBER);
    acc[0][0] = quad<0>(ac0); acc[0][1] = quad<1>(ac0); acc[1][0] = quad<6>(ac0); acc[1][1] = quad<7>(ac0);
    asm volatile("" : "=s"(kol));
    wf[0] = __builtin_bit_cast(bf16x8, bias[0]); wf[1] = __builtin_bit_cast(bf16x8, bias[1]);
    qh[0][0] = quad<4>(ac1); qh[0][1] = quad<5>(ac1); qh[1][0] = quad<2>(ac2); qh[1][1] = quad<3>(ac2);
  }
  ST(layer * 24 + 4)
  {
  const int lane = lane_id(), tid = w * 64 + lane, fr = lane & 15, g = lane >> 4;
  (void)fr; (void)g;
  if constexpr (HH) {
    const f32x4 z4 = {0, 0, 0, 0};
    constexpr float L2E = 1.4426950408889634f;
    const unsigned kvo = WS_KV + (layer >> 1) * KVLB, kfo = kvo + 16u * NH * KVPB + hp * KVFB;
    auto phi2 = [&](const f32x4 (&a)[2][2], f32x4 st, bf16x8 (&qp)[2][2]) {
      f32x4 pm[2][2];
      f32x2 f[2][4];
      asm volatile("" : "+v"(st));
#pragma unroll
      for (int i = 0; i < 8; ++i) {
        const int rb = i >> 2, c = (i >> 1) & 1, e = (i & 1) * 2;
        f[rb][i & 3] = __builtin_elementwise_fma(f32x2{csq[c][e], csq[c][e + 1]}, f32x2{st[2 * rb], st[2 * rb]}, f32x2{a[rb][c][e], a[rb][c][e + 1]});
      }
#pragma unroll
      for (int rb = 0; rb < 2; ++rb) asm volatile("" : "+v"(f[rb][0]), "+v"(f[rb][1]), "+v"(f[rb][2]), "+v"(f[rb][3]));
#pragma unroll
      for (int rb = 0; rb < 2; ++rb) {
        const bf16x8 xf = __builtin_bit_cast(bf16x8, u32x4{pk2(f[rb][0][0], f[rb][0][1]), pk2(f[rb][1][0], f[rb][1][1]), pk2(f[rb][2][0], f[rb][2][1]), pk2(f[rb][3][0], f[rb][3][1])});
        pm[rb][0] = MFMA(wf[0], xf, z4); pm[rb][1] = MFMA(wf[1], xf, z4);
      }
      asm volatile("s_nop 7" : "+v"(pm[0][0]), "+v"(pm[0][1]), "+v"(pm[1][0]), "+v"(pm[1][1]));
      float mx[2], mn[2];
#pragma unroll
      for (int u = 0; u < 2; ++u) max8pn(pm[u][0], pm[u][1], mx[u], mn[u]);
      xr4<true>(mx[0], mn[0], mx[1], mn[1]);
      float ep[2][8], en[2][8], sp[2], sn[2];
#pragma unroll
      for (int u = 0; u < 2; ++u) {
        const float c = st[2 * u + 1] * L2E, mxs = -mx[u] * c, mns = -mn[u] * c;
#pragma unroll
        for (int i = 0; i < 4; ++i) {
          const f32x2 p2 = {pm[u][i >> 1][(i & 1) * 2], pm[u][i >> 1][(i & 1) * 2 + 1]};
          const f32x2 tp = __builtin_elementwise_fma(p2, f32x2{c, c}, f32x2{mxs, mxs});
          const f32x2 tn = __builtin_elementwise_fma(-p2, f32x2{c, c}, f32x2{mns, mns});
          ep[u][2 * i] = __builtin_amdgcn_exp2f(tp[0]); ep[u][2 * i + 1] = __builtin_amdgcn_exp2f(tp[1]);
          en[u][2 * i] = __builtin_amdgcn_exp2f(tn[0]); en[u][2 * i + 1] = __builtin_amdgcn_exp2f(tn[1]);
        }
        const f32x2 a2 = (f32x2{ep[u][0], ep[u][1]} + f32x2{ep[u][2], ep[u][3]}) + (f32x2{ep[u][4], ep[u][5]} + f32x2{ep[u][6], ep[u][7]});
        const f32x2 b2 = (f32x2{en[u][0], en[u][1]} + f32x2{en[u][2], en[u][3]}) + (f32x2{en[u][4], en[u][5]} + f32x2{en[u][6], en[u][7]});
        sp[u] = a2[0] + a2[1]; sn[u] = b2[0] + b2[1];
      }
      xr4<false>(sp[0], sn[0], sp[1], sn[1]);
#pragma unroll
      for (int rb = 0; rb < 2; ++rb) {
        const float ip = __builtin_amdgcn_rcpf(sp[rb]), in = __builtin_amdgcn_rcpf(sn[rb]);
        qp[rb][0] = __builtin_bit_cast(bf16x8, u32x4{pk2(ep[rb][0] * ip, ep[rb][1] * ip), pk2(ep[rb][2] * ip, ep[rb][3] * ip), pk2(ep[rb][4] * ip, ep[rb][5] * ip), pk2(ep[rb][6] * ip, ep[rb][7] * ip)});
        qp[rb][1] = __builtin_bit_cast(bf16x8, u32x4{pk2(en[rb][0] * in, en[rb][1] * in), pk2(en[rb][2] * in, en[rb][3] * in), pk2(en[rb][4] * in, en[rb][5] * in), pk2(en[rb][6] * in, en[rb][7] * in)});
        asm volatile("" : "+v"(qp[rb][0]), "+v"(qp[rb][1]));
      }
    };
    bf16x8 one8 = ones8();
    asm volatile("" : "+v"(one8));
    WAITL0; BAR;
    ST(layer * 24 + 5)
    {
      bf16x8 af[4], v0[4], v1[4];
      asm volatile("" ::: "memory");
#pragma unroll
      for (int sw = 0; sw < 4; ++sw) {
        af[sw] = cat8(tr_read(ppa + sw * (64 * PSTR)), tr_read(ppa + sw * (64 * PSTR) + 16 * PSTR));
        v0[sw] = lds<bf16x8>(pvr + sw * 4096);
        v1[sw] = lds<bf16x8>(pvr + sw * 4096 + 1024);
      }
      __builtin_amdgcn_sched_barrier(0);
      f32x4 kv[3] = {z4, z4, z4};
#pragma unroll
      for (int sw = 0; sw < 4; ++sw) {
        kv[0] = MFMA(af[sw], v0[sw], kv[0]);
        kv[1] = MFMA(af[sw], v1[sw], kv[1]);
        kv[2] = MFMA(af[sw], one8, kv[2]);
      }
      auto pc = [](float x, float y) { const unsigned t = pk2(x, y); return t < EMPTY - 1u ? t : EMPTY - 1u; };
      __builtin_amdgcn_raw_buffer_store_b128(u32x4{pc(kv[0][0], kv[0][1]), pc(kv[0][2], kv[0][3]), pc(kv[1][0], kv[1][1]), pc(kv[1][2], kv[1][3])}, rsW, gkv, 0, 16);
      __builtin_amdgcn_raw_buffer_store_b64(u32x2{pc(kv[2][0], kv[2][1]), pc(kv[2][2], kv[2][3])}, rsW, gkz, 0, 16);
    }
    auto wo_late = [&](int t) { glds2v(wos + knib(kol, t) * tO, pwl, pwm, sb + OFF_WO + t * 8192 + (w & 3) * 2048); };
    ST(layer * 24 + 6)
    u32x4 l1[8];
    const bool red = w == 7 || (w == 6 && (rt & 7) == 0);
    unsigned lo = kvo + (unsigned)((2 * hp + (rt >> 3) + (lane >> 5) * 8 * NH) * KVPB) +
                  (w == 7 ? (unsigned)((rt & 4) * 512 + (g & 1) * 1024 + (rt & 3) * 256 + fr * 16) : (unsigned)(4096 + (lane & 7) * 16));
    unsigned fs = w == 7 ? kfo + (unsigned)((rt >> 2) * 2048 + (lane >> 5) * 1024 + (rt & 3) * 256 + fr * 16 + (g & 1) * 8)
                : (lane & 24) ? 0x7fff0000u : kfo + (unsigned)(8192 + (rt >> 3) * 128 + (lane & 4) * 16 + (lane & 1) * 32 + (lane >> 5) * 16 + (lane & 2) * 4);
    auto l1_load = [&]() {
      asm volatile("" ::: "memory");
#pragma unroll
      for (int p = 0; p < 8; ++p) asm volatile("buffer_load_dwordx4 %0, %1, %2, %3 offen sc1" : "=v"(l1[p]) : "v"(lo), "s"(rsW), "s"(p * (NH * KVPB)) : "memory");
      __builtin_amdgcn_sched_barrier(0);
    };
    if (red) asm volatile("" : "+v"(lo), "+v"(fs));
    if (red) { __builtin_amdgcn_s_sleep(13); l1_load(); }
    bf16x8 qp2[2][2];
    if (!kg) {
      phi2(qh, st23, qp2);
    } else if (red) {
      typedef __attribute__((ext_vector_type(2))) __bf16 bf2;
      unsigned c0 = 0x00003F80u, c1 = 0x3F800000u;
      asm volatile("" : "+v"(c0), "+v"(c1));
      float s4[4];
      _Pragma("unroll 1") for (unsigned spins = 0; spins < (1u << 18); ++spins) {
        float a[8] = {zero1(), zero1(), zero1(), zero1(), zero1(), zero1(), zero1(), zero1()};
        ARRIVED("+v"(l1[0]), "+v"(l1[1]), "+v"(l1[2]), "+v"(l1[3]), "+v"(l1[4]), "+v"(l1[5]), "+v"(l1[6]), "+v"(l1[7]));
#pragma unroll
        for (int p = 0; p < 8; ++p)
#pragma unroll
          for (int e = 0; e < 4; ++e) {
            const unsigned u = l1[p][e];
            a[2 * e] = __builtin_amdgcn_fdot2_f32_bf16(__builtin_bit_cast(bf2, u), __builtin_bit_cast(bf2, c0), a[2 * e], false);
            a[2 * e + 1] = __builtin_amdgcn_fdot2_f32_bf16(__builtin_bit_cast(bf2, u), __builtin_bit_cast(bf2, c1), a[2 * e + 1], false);
          }
#pragma unroll
        for (int i = 0; i < 4; ++i) {
          auto r = __builtin_amdgcn_permlane32_swap(__float_as_uint(a[i]), __float_as_uint(a[i + 4]), false, false);
          s4[i] = __uint_as_float(r[0]) + __uint_as_float(r[1]);
        }
        const float t = s4[0] + s4[2];
        if (__all(t == t)) break;
        unsigned mxw = 0;
#pragma unroll
        for (int p = 0; p < 8; ++p)
#pragma unroll
          for (int e = 0; e < 4; ++e) mxw = l1[p][e] > mxw ? l1[p][e] : mxw;
        if (__all(mxw != EMPTY)) break;
        l1_load();
      }
      WAITV(0);
      unsigned o0 = pk2(s4[0], s4[1]), o1 = pk2(s4[2], s4[3]);
      o0 = o0 < EMPTY - 1u ? o0 : EMPTY - 1u; o1 = o1 < EMPTY - 1u ? o1 : EMPTY - 1u;
      __builtin_amdgcn_raw_buffer_store_b64(u32x2{o0, o1}, rsW, fs, 0, 16);
    }
    ST(layer * 24 + 8)
    WAITL0;
    BAR;
    ST(layer * 24 + 9)
    if (kg) {
#pragma unroll
      for (int i = 6; i < 12; ++i) wo_late(i);
    } else {
      unsigned fo = kfo + (unsigned)(wn * 4096 + lane * 16), fz = kfo + (unsigned)(8192 + wn * 128 + g * 16);
      asm volatile("" : "+v"(fo), "+v"(fz));
      bf16x8 qp[2][2];
      phi2(acc, st01, qp);
      asm volatile(G1_OUTPUT
          :: "{v[224:227]}"(qp[0][0]), "{v[228:231]}"(qp[0][1]), "{v[232:235]}"(qp[1][0]), "{v[236:239]}"(qp[1][1]),
             "{v[240:243]}"(qp2[0][0]), "{v[244:247]}"(qp2[0][1]), "{v[248:251]}"(qp2[1][0]), "{v[252:255]}"(qp2[1][1]),
             [fo] "v"(fo), [fz] "v"(fz), [go] "v"(ogo), [lo] "v"(opo), [rs] "s"(rsW)
          : "memory", "vcc", "scc", "m0", "v152", "v153", "v154", "v155", "v156", "v157", "v158", "v159", "v160", "v161", "v162", "v163", "v164", "v165", "v166", "v167", "v168", "v169", "v170", "v171", "v172", "v173", "v174", "v175", "v176", "v177", "v178", "v179", "v180", "v181", "v182", "v183", "v184", "v185", "v186", "v187", "v188", "v189", "v190", "v191", "v192", "v193", "v194", "v195", "v196", "v197", "v198", "v199", "v200", "v201", "v202", "v203", "v204", "v205", "v206", "v207", "v208", "v209", "v210", "v211", "v212", "v213", "v214", "v215", "v216", "v217", "v218", "v219", "v220", "v221", "v222", "v223");
    }
  }
  ST(layer * 24 + 12)
  if (first && w >= 4 && w < 7) {
    typedef __attribute__((ext_vector_type(2))) __bf16 bf2;
    unsigned c0 = 0x00003F80u, c1 = 0x3F800000u;
    asm volatile("" : "+v"(c0), "+v"(c1));
    const bf2 lo1 = __builtin_bit_cast(bf2, c0), hi1 = __builtin_bit_cast(bf2, c1);
    float a[8] = {zero1(), zero1(), zero1(), zero1(), zero1(), zero1(), zero1(), zero1()};
#pragma unroll
    for (int i = 0; i < 8; ++i) {
      asm volatile("" : "+v"(pw[i]));
#pragma unroll
      for (int e = 0; e < 4; ++e) {
        const unsigned u = pw[i][e];
        a[2 * e] = __builtin_amdgcn_fdot2_f32_bf16(__builtin_bit_cast(bf2, u), lo1, a[2 * e], false);
        a[2 * e + 1] = __builtin_amdgcn_fdot2_f32_bf16(__builtin_bit_cast(bf2, u), hi1, a[2 * e + 1], false);
      }
    }
#pragma unroll
    for (int i = 0; i < 4; ++i) {
      auto r = __builtin_amdgcn_permlane32_swap(__float_as_uint(a[i]), __float_as_uint(a[i + 4]), false, false);
      a[i] = __uint_as_float(r[0]) + __uint_as_float(r[1]);
    }
#pragma unroll
    for (int i = 0; i < 2; ++i) {
      auto r = __builtin_amdgcn_permlane16_swap(__float_as_uint(a[i]), __float_as_uint(a[i + 2]), false, false);
      a[i] = __uint_as_float(r[0]) + __uint_as_float(r[1]);
      a[i] += dpp_ror<8>(a[i]);
    }
    if (!(lane & 8))
      __builtin_amdgcn_raw_buffer_store_b64(u32x2{__float_as_uint(a[0]), __float_as_uint(a[1])}, rsW,
          (unsigned)(WS_CSP + ((((hp + (1 << (w - 4))) & 15) * 16 + rt) * 192 + (w - 4) * 64 + (lane & 7) * 8 + (lane >> 5) * 4 + ((lane >> 4) & 1) * 2) * 4), 0, 16);
  }
  }
}

#undef pcs
#undef prs
#undef opo
#undef ogo
#undef hof
#undef dm
#undef dm2
#undef pcv
#undef pwf
#undef prv
#undef pvf
#undef ppk
#undef ppa
#undef pvr
#undef gkv
#undef gkz
#undef pwl
#undef pwm
#undef plb
#undef prd
#undef dm3

__device__ __forceinline__ void g2_phase(char* smem, char* ws, u64 E, int w, f32x16& xq, Next& nx, int layer, const u16* __restrict__ x0,
                                         const u16* __restrict__ wo, const u16* __restrict__ wnx, u16* __restrict__ out) {
  IDS
  wnx = uni(wnx);
  const unsigned sb = lds_addr(smem);
  const int lw = w & 3, k2 = 16 * lw + (lane >> 3);
  unsigned pxr, rmo;
  auto ends = [&]() {
    const int lane = lane_id(), r0 = w * 16 + (lane >> 3);
    pxr = sb + r0 * 128 + (lane & 7) * 16;
    rmo = (unsigned)(((m0 + r0) * DM + hp * 64 + ((lane & 7) ^ (lane >> 3)) * 8) * 2);
    asm volatile("" : "+v"(pxr), "+v"(rmo));
  };
  constexpr int S0 = 0, S1 = 16384, S2 = 32768, S3 = 147456, S4 = OFF_WO, S5 = 98304, S6 = 114688;
  constexpr int AS[16] = {S0, S1, S2, S3, S0, S1, S4, S2, S3, S0, S1, S4, S5, S3, S0, S6};
  constexpr int OFF_C = 0;
  auto bs = [](int t) { return OFF_WO + (t < 12 ? t : t - 10) * 8192; };
  const char *xa1, *xa2, *wq2;
  u64 fm;
  unsigned vB0, vB1, vXA, po, pst;
  if (w >= 4) {
    constexpr int WT[15] = {0, 8, 12, 14, 16, 18, 20, 18, 16, 14, 12, 12, 10, 10, 8};
    const char* o = ws + WS_O + layer * (SEQ * DM * 2) + rt * 262144;
    const unsigned vA = (unsigned)(lw * 4096 + lane * 16);
    u64 kord = korder(bid & 3, hp);
    auto ldA = [&](int s) { glds4c(o + knib(kord, s) * 16384, vA, sb + AS[s] + lw * 4096); };
    const bool im = img(layer, hp), in = img(layer + 1, hp);
    unsigned vW0 = im ? (unsigned)(lw * 2048 + lane * 16) : (unsigned)((k2 * DM + hp * 64 + ((lane & 7) ^ ((lane >> 4) << 1)) * 8) * 2), vW1 = vW0 + (im ? 0u : 8 * DM * 2 - 1024u);
    const char *wos = wo_src(ws, wo, layer, hp), *wqs = wq_src(ws, wnx, layer + 1, hp);
    unsigned tO = im ? 8192u : 131072u;
    asm volatile("" : "+v"(vW0), "+v"(vW1));
    asm volatile("" : "+s"(wos), "+s"(tO));
    auto ldW = [&](int t) { glds2v(wos + knib(kord, t) * tO, vW0, vW1, sb + bs(t) + lw * 2048); };
    const int fB = (k2 >> 1) & 1;
    vB0 = in ? (unsigned)(lw * 6144 + lane * 16) : (unsigned)((k2 * 3072 + hp * 64 + (((((lane & 7) >> 1) ^ fB) << 1) | (lane & 1)) * 8) * 2);
    vB1 = in ? vB0 : (unsigned)(((k2 + 8) * 3072 + hp * 64 + (((((lane & 7) >> 1) ^ (fB | 2)) << 1) | (lane & 1)) * 8) * 2) - 1024u;
    const u64 kn = nx.kord = korder(kv1(bid), hp);
    nx.wq = wqs; nx.tw = in ? 24576u : 393216u;
    const char *wq0 = wqs + hp * (in ? 24576u : 393216u), *wq1 = wqs + knib(kn, 1) * (in ? 24576u : 393216u);
    wq2 = wqs + knib(kn, 2) * (in ? 24576u : 393216u);
    xa1 = ws + WS_X + layer * (SEQ * DM * 2) + knib(kn, 1) * 16384; xa2 = ws + WS_X + layer * (SEQ * DM * 2) + knib(kn, 2) * 16384;
    vXA = (unsigned)(rt * 262144 + lw * 4096 + lane * 16);
    fm = kfirst(kn);
    po = ((fm >> lane) & 1) ? WS_FLAGS + (unsigned)(256 + rt * 16 + lane) * FLS : 0u;
    pst = g != q4 ? 0x7fff0000u : WS_ST + layer * (SEQ * 128) + (unsigned)(((rt * 16 + hp) * 128 + lw * 32 + fr) * 8);
    asm volatile("" : "+v"(vB0), "+v"(vB1), "+v"(vXA), "+v"(po), "+v"(pst));
    asm volatile("" : "+s"(xa1), "+s"(xa2), "+s"(wq2), "+s"(fm), "+s"(nx.kord), "+s"(nx.wq), "+s"(nx.tw));
    asm volatile("" : "+s"(wq0), "+s"(wq1));
    auto ldB = [&](int s, int part) {
      const char* b = s ? wq1 : wq0;
      if (part) glds4v(b, vB0, vB1, vB0, vB1, sb + s * 40960 + 16384 + lw * 6144);
      else glds2v(b + 4096, vB0, vB1, sb + s * 40960 + 16384 + lw * 6144 + 4096);
    };
    ends();
    const char* o1 = o + knib(kord, 1) * 16384;
    asm volatile("" : "+s"(o1), "+s"(kord), "+s"(o));
    asm volatile("" :: "v"(vA));
    if (layer) { BAR;   }
    BAR;
    glds4c(o1, vA, sb + AS[1] + lw * 4096); __builtin_amdgcn_s_sleep(3); ldA(2); __builtin_amdgcn_s_sleep(3); ldA(3); __builtin_amdgcn_s_sleep(3); ldA(4);
    WAITV(12);
    BAR;
    auto step = [&](auto ktc) {
      constexpr int kt = decltype(ktc)::value;
      WAITVN(WT[kt]);
      BAR;
      if constexpr (kt == 1) { ldA(5); ldA(6); }
      if constexpr (kt >= 2 && kt <= 10) ldA(kt + 5);
      if constexpr (kt >= 2 && kt <= 5) ldW(kt + 10);
      if constexpr (kt >= 11) ldB((kt - 11) >> 1, (kt - 11) & 1);
      if constexpr (kt >= 6) { __builtin_amdgcn_s_sleep(kt >= 11 ? 3 : 2); if (layer == 1) __builtin_amdgcn_s_sleep(1); }
    };
    step(IC<1>{}); step(IC<2>{}); step(IC<3>{}); step(IC<4>{}); step(IC<5>{}); step(IC<6>{}); step(IC<7>{});
    step(IC<8>{}); step(IC<9>{}); step(IC<10>{}); step(IC<11>{}); step(IC<12>{}); step(IC<13>{}); step(IC<14>{});
  } else {
    asm volatile("" : "=v"(vB0), "=v"(vB1), "=v"(vXA), "=v"(po), "=v"(pst));
    asm volatile("" : "=s"(xa1), "=s"(xa2), "=s"(wq2), "=s"(fm), "=s"(nx.kord), "=s"(nx.wq), "=s"(nx.tw));
    const int wm = w >> 1, wn = w & 1;
    f32x4 acc[4][2];
    const int fl = (q4 >> 1) | ((g & 1) << 1);
    const int bb0 = (4 * g + q4) * 128 + p4 * 8 + (((wn * 2) ^ fl) << 5), bb1 = (4 * g + q4) * 128 + p4 * 8 + (((wn * 2 + 1) ^ fl) << 5);
    const int abase = wm * 4096 + lane * 16;
    bf16x8 fa[2][2][4], fb[2][2][2];
    unsigned ra[3], rb0[2], rb1[2];
#pragma unroll
    for (int i = 0; i < 3; ++i) { ra[i] = abase + i * 65536; asm volatile("" : "+v"(ra[i])); }
#pragma unroll
    for (int i = 0; i < 2; ++i) { rb0[i] = bb0 + OFF_WO + i * 49152; rb1[i] = bb1 + OFF_WO + i * 49152; asm volatile("" : "+v"(rb0[i]), "+v"(rb1[i])); }
    auto loadf = [&](int kt, bf16x8 (&a)[2][4], bf16x8 (&b)[2][2]) {
#pragma unroll
      for (int s = 0; s < 2; ++s) {
        const char* sA = smem + ra[AS[kt] >> 16] + (AS[kt] & 65535) + s * 8192;
        const int hi = bs(kt) >= OFF_WO + 49152, ob = bs(kt) - OFF_WO - hi * 49152 + s * 4096;
        b[s][0] = cat8(tr_read(smem + rb0[hi] + ob), tr_read(smem + rb0[hi] + ob + 2048));
        b[s][1] = cat8(tr_read(smem + rb1[hi] + ob), tr_read(smem + rb1[hi] + ob + 2048));
#pragma unroll
        for (int rb = 0; rb < 4; ++rb) a[s][rb] = *(const bf16x8*)(sA + rb * 1024);
      }
    };
    auto mma = [&](bf16x8 (&a)[2][4], bf16x8 (&b)[2][2]) {
#pragma unroll
      for (int i = 0; i < 16; ++i) {
        const int rb = i >> 2, cb = ((i >> 1) ^ rb) & 1, s = ((i + 1) >> 1) & 1;
        acc[rb][cb] = MFMA(b[s][cb], a[s][rb], acc[rb][cb]);
      }
    };
    unsigned fo = lane < 16 && lane != hp ? WS_FLAGS + (unsigned)(rt * 16 + lane) * FLS : 0u;
    const u64 ko = korder(bid & 3, hp);
    unsigned kf = kfirst(ko), kf2 = kfirst(ko >> 16) | (1u << knib(ko, 9));
    unsigned jo = (unsigned)((((rt & 7) * 128 + (w - 1) * 64 + lane) * 3072 + (hp & 3) * 1024 + (hp | 3) * 64) * 2);
    unsigned fo1 = ((kf >> (lane & 31)) & 1) ? fo : 0u, fo2 = ((kf2 >> (lane & 31)) & 1) ? fo : 0u;
    fo = (((kf | kf2) >> (lane & 31)) & 1) ? 0u : fo;
    asm volatile("" : "+v"(fo), "+v"(fo1), "+v"(fo2));
    asm volatile("" : "+s"(kf), "+s"(kf2));
    publish(rsW, tid, rt * 16 + hp, E + (unsigned)(layer + 1));
    ST(layer * 24 + 13)
    u64 fl1;
    asm volatile("" : "=v"(fl1));
    if (w == 0) asm volatile("global_load_dwordx2 %0, %1, %2 sc1" : "+v"(fl1) : "v"(fo1), "s"(ws) : "memory");
    if (layer == 0) {
      const unsigned xo = (unsigned)(((m0 + wm * 64 + fr) * DM + hp * 64 + wn * 32 + g * 8) * 2);
      asm volatile("global_load_dwordx4 v[64:67], %1, %3\n\tglobal_load_dwordx4 v[68:71], %1, %4\n\tglobal_load_dwordx4 v[72:75], %2, %3\n\tglobal_load_dwordx4 v[76:79], %2, %4"
                   : "+{v[64:79]}"(xq) : "v"(xo), "v"(xo + 32 * DM * 2), "s"(x0), "s"(x0 + 16 * DM) : "memory");
    }
    loadf(0, fa[0], fb[0]);
    unsigned junk;
    asm volatile("" : "=v"(junk));
    if (layer < 2 && w == 3 && (bid >> 6) == 0) code_touch(junk, bid, lane);
    if (layer == 0 && (hp & 7) < 3 && (w == 1 || w == 2))
      asm volatile("global_load_dword %0, %1, %2" : "+v"(junk) : "v"(jo), "s"(wnx) : "memory");
    __builtin_amdgcn_sched_barrier(0);
    bf16x8 id[2];
#pragma unroll
    for (int cb = 0; cb < 2; ++cb) {
      const unsigned one = (g >> 1) == cb && (g & 1) == (fr >> 3) ? 0x3F80u << ((fr & 1) * 16) : 0u;
      u32x4 t;
#pragma unroll
      for (int j = 0; j < 4; ++j) t[j] = ((fr >> 1) & 3) == j ? one : 0u;
      id[cb] = __builtin_bit_cast(bf16x8, t);
      asm volatile("" : "+v"(id[cb]));
    }
    const int l2 = lane_id(), ch = (l2 >> 5) | ((l2 >> 3) & 2), f2 = l2 & 15;
    unsigned pl = (unsigned)(wm * 8192 + wn * 1024 + ch * 256 + f2 * 16);
    unsigned pg = layer < 3 ? WS_X + layer * (SEQ * DM * 2) + (unsigned)((rt * 16 + hp) * 16384) + pl : 0x7fff0000u;
    pl = sb + OFF_C + (layer < 3 ? pl : (unsigned)((wm * 64 + f2) * 128 + (((wn * 4 + ch) ^ (f2 & 7)) << 4)));
    asm volatile("" : "+v"(pl), "+v"(pg));
    ends();
#pragma unroll
    for (int rb = 0; rb < 4; ++rb) { acc[rb][0] = f32x4{0, 0, 0, 0}; acc[rb][1] = f32x4{0, 0, 0, 0}; }
    mma(fa[0], fb[0]);
    __builtin_amdgcn_sched_barrier(0);
    WAITL0;
    u64 miss = 0;
    if (w == 0) { ARRIVED("+v"(fl1)); if (__ballot(fl1 != E + (unsigned)(layer + 1)) & kf) poll_eq(ws, fo1, E + (unsigned)(layer + 1), kf, kf); miss = 0xffffu & ~(kf | kf2 | (1u << hp)); }
    ST(layer * 24 + 14)
    __builtin_amdgcn_sched_barrier(0);
    BAR;
    __builtin_amdgcn_sched_barrier(0);
    asm volatile("" ::: "memory");
    if (layer == 0) WAITV(0);
    __builtin_amdgcn_sched_barrier(0);
    asm volatile("" : "+{v[64:79]}"(xq));
    const bf16x8 xf[4] = {__builtin_bit_cast(bf16x8, quad<0>(xq)), __builtin_bit_cast(bf16x8, quad<1>(xq)), __builtin_bit_cast(bf16x8, quad<2>(xq)), __builtin_bit_cast(bf16x8, quad<3>(xq))};
#pragma unroll
    for (int rb = 0; rb < 4; ++rb) { acc[rb][0] = MFMA(id[0], xf[rb], acc[rb][0]); acc[rb][1] = MFMA(id[1], xf[rb], acc[rb][1]); }
    if (w == 0) poll_eq(ws, fo2, E + (unsigned)(layer + 1), kf2, kf2);
    u64 fl2;
    asm volatile("" : "=v"(fl2));
    auto step = [&](auto ktc) {
      constexpr int kt = decltype(ktc)::value;
      __builtin_amdgcn_sched_barrier(0);
      if (kt == 5 && miss) {
        ARRIVED("+v"(fl2));
        if (__ballot(fl2 != E + (unsigned)(layer + 1)) & miss) poll_eq(ws, fo, E + (unsigned)(layer + 1), miss, ~0ull);
      }
      WAITL0;
      BAR;
      if (kt == 2 && miss) asm volatile("global_load_dwordx2 %0, %1, %2 sc1" : "+v"(fl2) : "v"(fo), "s"(ws) : "memory");
      if (kt == 0) { ST(layer * 24 + 15) }
      __builtin_amdgcn_sched_barrier(0);
      loadf(kt + 1, fa[(kt + 1) & 1], fb[(kt + 1) & 1]);
      if (kt == 0) return;
      mma(fa[kt & 1], fb[kt & 1]);
#pragma unroll
      for (int i = 0; i < 4; ++i) {
        __builtin_amdgcn_sched_group_barrier(0x008, 1, 0);
        __builtin_amdgcn_sched_group_barrier(0x100, 1, 0);
        __builtin_amdgcn_sched_group_barrier(0x008, 1, 0);
        __builtin_amdgcn_sched_group_barrier(0x100, 3, 0);
      }
#pragma unroll
      for (int i = 0; i < 8; ++i) __builtin_amdgcn_sched_group_barrier(0x008, 1, 0);
    };
    step(IC<0>{}); step(IC<1>{}); step(IC<2>{}); step(IC<3>{}); step(IC<4>{}); step(IC<5>{}); step(IC<6>{}); step(IC<7>{});
    step(IC<8>{}); step(IC<9>{}); step(IC<10>{}); step(IC<11>{}); step(IC<12>{}); step(IC<13>{}); step(IC<14>{});
    WAITV(0);
    asm volatile("" :: "v"(junk));
    __builtin_amdgcn_sched_barrier(0);
    u32x4 xv[4];
    auto put = [&](int rb) {
      auto a = __builtin_amdgcn_permlane16_swap(pk2(acc[rb][0][0], acc[rb][0][1]), pk2(acc[rb][1][0], acc[rb][1][1]), false, false);
      auto b = __builtin_amdgcn_permlane16_swap(pk2(acc[rb][0][2], acc[rb][0][3]), pk2(acc[rb][1][2], acc[rb][1][3]), false, false);
      xv[rb] = u32x4{a[0], b[0], a[1], b[1]};
      __builtin_amdgcn_raw_buffer_store_b128(xv[rb], rsW, pg + (rb & 1) * 2048, (rb >> 1) * 4096, 16);
    };
#pragma unroll
    for (int rb = 0; rb < 4; ++rb)
#pragma unroll
      for (int cb = 0; cb < 2; ++cb) acc[rb][cb] = MFMA(fb[1][0][cb], fa[1][0][rb], acc[rb][cb]);
#pragma unroll
    for (int rb = 0; rb < 4; ++rb) {
      __builtin_amdgcn_sched_barrier(0);
#pragma unroll
      for (int cb = 0; cb < 2; ++cb) acc[rb][cb] = MFMA(fb[1][1][cb], fa[1][1][rb], acc[rb][cb]);
      __builtin_amdgcn_sched_barrier(0);
      if (rb >= 2) put(rb - 2);
    }
    __builtin_amdgcn_sched_barrier(0);
    ST(layer * 24 + 16)
    put(2); put(3);
    __builtin_amdgcn_sched_barrier(0);
#pragma unroll
    for (int rb = 0; rb < 4; ++rb) lds<u32x4>(pl + rb * 2048) = xv[rb];
  }
  if (layer == 3) {
    WAITL0; BAR;
    asm volatile("" ::: "memory");
    const __amdgpu_buffer_rsrc_t rsO = __builtin_amdgcn_make_buffer_rsrc((void*)uni(out), (short)0, SEQ * DM * 2, 0x00020000);
    __builtin_amdgcn_raw_buffer_store_b128(lds<u32x4>(pxr + OFF_C), rsO, rmo, 0, 16);
    __builtin_amdgcn_raw_buffer_store_b128(lds<u32x4>(pxr + OFF_C + 1024), rsO, rmo, 8 * DM * 2, 16);
  }
  ST(layer * 24 + 17)
  if (layer < 3 && w >= 4) {
    WAITV(0); BAR;
    asm volatile("" ::: "memory");
    glds4v(wq2, vB0, vB1, vB0, vB1, sb + 2 * 40960 + 16384 + lw * 6144);
    glds2v(wq2 + 4096, vB0, vB1, sb + 2 * 40960 + 16384 + lw * 6144 + 4096);
    __builtin_amdgcn_s_setprio(3);
    bf16x8 xn[4];
#pragma unroll
    for (int i = 0; i < 4; ++i) xn[i] = lds<bf16x8>(sb + OFF_C + lw * 4096 + lane * 16 + i * 1024);
    const bf16x8 ones = ones8();
    u32x2 h[2];
#pragma unroll
    for (int i = 0; i < 2; ++i) {
      const f32x4 z4 = {0, 0, 0, 0};
      const f32x4 sa = MFMA(ones, xn[2 * i + 1], MFMA(ones, xn[2 * i], z4)), sq = MFMA(xn[2 * i + 1], xn[2 * i + 1], MFMA(xn[2 * i], xn[2 * i], z4));
      const float a = (lane & 1) ? sq[1] : sq[0], b = (lane & 1) ? sq[3] : sq[2];
      h[i] = u32x2{__float_as_uint(sa[0]), __float_as_uint((lane & 2) ? b : a)};
    }
    __builtin_amdgcn_s_setprio(0);
    u64 fl;
    asm volatile("" : "=v"(fl), "+v"(pst) : "v"(h[0]), "v"(h[1]));
    if (lw == 0) asm volatile("global_load_dwordx2 %0, %1, %2 sc1" : "+v"(fl) : "v"(po), "s"(ws) : "memory");
    __builtin_amdgcn_raw_buffer_store_b64(h[0], rsW, pst, 0, 16);
    __builtin_amdgcn_raw_buffer_store_b64(h[1], rsW, pst + 128, 0, 16);
    if (lw == 0) {
      WAITV(2);
      __builtin_amdgcn_sched_barrier(0);
      asm volatile("" : "+v"(fl));
      if (__ballot(fl != E + (unsigned)(layer + 1)) & fm) poll_eq(ws, po, E + (unsigned)(layer + 1), fm, ~0ull);
    }
    BAR;
    glds4c(xa1, vXA, sb + 40960 + lw * 4096); glds4c(xa2, vXA, sb + 2 * 40960 + lw * 4096);
  }
  ST(layer * 24 + 18)
}

__global__ __launch_bounds__(512, 2) void mega_kernel(const u16* __restrict__ x0, const u16* __restrict__ wqkvA,
                                                      const u16* __restrict__ woA, const u16* __restrict__ wqkvB,
                                                      const u16* __restrict__ wphi, const u16* __restrict__ woB,
                                                      u16* __restrict__ out, char* __restrict__ ws) {
  extern __shared__ __attribute__((aligned(16))) char smem[];
  {
    unsigned long long pc;
    unsigned t;
    asm volatile("s_getpc_b64 %0\n.Lmk_pc:\n\tv_lshl_add_u32 %1, %2, 9, %3\n\tv_lshlrev_b32 %1, 2, %1\n\tv_and_b32 %1, 0xffffff80, %1\n\ts_mov_b32 m0, %4\n\t"
                 "v_min_u32 %1, .Lmk_end-.Lmk_pc-4, %1\n\tglobal_load_lds_dword %1, %0" : "=&s"(pc), "=&v"(t) : "s"(blockIdx.x >> 3), "v"(threadIdx.x), "n"(LDS_TOTAL - 1024) : "memory", "m0");
  }
  const int w = __builtin_amdgcn_readfirstlane(threadIdx.x >> 6);
  u64 E = 0;
  {
    const int bid = blockIdx.x, lane = threadIdx.x & 63;
    const int rt = 2 * (bid & 4) + ((bid >> 3) & 7), hp = 4 * (bid & 3) + (bid >> 6), m0 = rt * 128;
    if (w < 4) {
      const unsigned xo = (unsigned)((m0 + hp * 8 + w * 2) * (DM * 2) + lane * 16);
      glds4v(x0, xo, xo, xo, xo, lds_addr(smem) + 122880 + w * 4096);
    } else {
      const int lw = w & 3;
      const int kB = 16 * lw + (lane >> 3), fB = (kB >> 1) & 1;
      const unsigned vB0 = (unsigned)((kB * 3072 + hp * 64 + (((((lane & 7) >> 1) ^ fB) << 1) | (lane & 1)) * 8) * 2);
      const unsigned vB1 = (unsigned)(((kB + 8) * 3072 + hp * 64 + (((((lane & 7) >> 1) ^ (fB | 2)) << 1) | (lane & 1)) * 8) * 2) - 1024u;
      const unsigned vA = (unsigned)(((m0 + lw * 32 + (lane >> 3)) * DM + ((lane & 7) ^ (lane >> 3)) * 8) * 2);
#pragma unroll
      for (int s = 0; s < 2; ++s) {
        const int kt = (rt & 8) + s;
        const unsigned sA = lds_addr(smem) + s * 40960, sB = sA + 16384;
        glds4v(x0 + kt * 64, vA, vA + 15360u, vA + 2 * 15360u, vA + 3 * 15360u, sA + lw * 4096);
        const u16* b = wqkvA + (size_t)kt * 64 * 3072;
        glds4v(b, vB0, vB1, vB0, vB1, sB + lw * 6144);
        glds2v(b + 2048, vB0, vB1, sB + lw * 6144 + 4096);
      }
    }
  }
  f32x16 xq;
  Next nx;
  asm volatile("" : "=s"(nx.kord), "=s"(nx.wq), "=s"(nx.tw));
#pragma unroll 1
  for (int layer = 0; layer < 4; ++layer) {
    if (layer & 1) g1_phase<1>(smem, ws, E, w, xq, nx, layer, x0, wqkvB, woB, wphi, wqkvA, woA);
    else g1_phase<0>(smem, ws, E, w, xq, nx, layer, x0, wqkvA, woA, nullptr, wqkvB, woB);
    g2_phase(smem, ws, E, w, xq, nx, layer, x0, (layer & 1) ? woB : woA, (layer & 1) ? wqkvA : wqkvB, out);
  }
  asm volatile(".Lmk_end:");
  WAITV(0);
  if (blockIdx.x == 0 && w == 0 && lane_id() == 0) __hip_atomic_store((gu64*)ws, E + 16u, __ATOMIC_RELAXED, __HIP_MEMORY_SCOPE_AGENT);
}

extern "C" void kernel_launch(void* const* d_in, const int* in_sizes, int n_in,
                              void* d_out, int out_size, void* d_ws, size_t ws_size,
                              hipStream_t stream) {
  hipFuncSetAttribute((const void*)mega_kernel, hipFuncAttributeMaxDynamicSharedMemorySize, LDS_TOTAL);
  mega_kernel<<<256, 512, LDS_TOTAL, stream>>>((const u16*)d_in[0], (const u16*)d_in[1], (const u16*)d_in[2], (const u16*)d_in[4],
                                               (const u16*)d_in[5], (const u16*)d_in[6], (u16*)d_out, (char*)d_ws);
}
```

```cpp
#include <hip/hip_runtime.h>
#include <hip/hip_bf16.h>
#include <stdint.h>
#include <type_traits>
#pragma clang diagnostic ignored "-Winline-asm"
#pragma clang diagnostic ignored "-Wunused-value"

typedef unsigned short u16;
typedef __attribute__((ext_vector_type(8))) short bf16x8;
typedef __attribute__((ext_vector_type(4))) short bf16x4;
typedef __attribute__((ext_vector_type(4))) float f32x4;
typedef __attribute__((ext_vector_type(4))) unsigned u32x4;
typedef __attribute__((ext_vector_type(2))) unsigned u32x2;
typedef __attribute__((ext_vector_type(8))) float f32x8;
typedef __attribute__((ext_vector_type(16))) unsigned u32x16;
typedef __attribute__((ext_vector_type(16))) float f32x16;
typedef __attribute__((ext_vector_type(32))) float f32x32;
template <int I, typename T> __device__ __forceinline__ f32x4 quad(const T& v) { return __builtin_shufflevector(v, v, 4 * I, 4 * I + 1, 4 * I + 2, 4 * I + 3); }

constexpr int SEQ = 2048, DM = 1024, NH = 32;
#define ST(i)
constexpr int KVP = 64 * 32 + 64;

typedef __attribute__((ext_vector_type(2))) float f32x2;
typedef __attribute__((ext_vector_type(2))) __bf16 bf16x2_t;
__device__ __forceinline__ unsigned pk2(float a, float b) {
  f32x2 v = {a, b};
  bf16x2_t r = __builtin_convertvector(v, bf16x2_t);
  return __builtin_bit_cast(unsigned, r);
}

__device__ __forceinline__ bf16x4 tr_read(const char* p) {
  return __builtin_amdgcn_ds_read_tr16_b64_v4i16((__attribute__((address_space(3))) bf16x4*)p);
}
template <typename T> __device__ __forceinline__ __attribute__((address_space(3))) T& lds(unsigned a) { return *(__attribute__((address_space(3))) T*)(size_t)a; }
__device__ __forceinline__ bf16x4 tr_read(unsigned a) { return __builtin_amdgcn_ds_read_tr16_b64_v4i16(&lds<bf16x4>(a)); }
__device__ __forceinline__ bf16x8 cat8(bf16x4 a, bf16x4 b) {
  bf16x8 f = {a[0], a[1], a[2], a[3], b[0], b[1], b[2], b[3]};
  return f;
}
__device__ __forceinline__ void glds16s(const void* sbase, unsigned voff, unsigned lds_base) {
  asm volatile("s_mov_b32 m0, %2\n\ts_nop 0\n\tglobal_load_lds_dwordx4 %0, %1" :: "v"(voff), "s"(sbase), "s"(lds_base) : "memory", "m0");
}
__device__ __forceinline__ void glds16c(const void* sbase, unsigned voff, unsigned lds_base) {
  asm volatile("s_mov_b32 m0, %2\n\ts_nop 0\n\tglobal_load_lds_dwordx4 %0, %1 sc1" :: "v"(voff), "s"(sbase), "s"(lds_base) : "memory", "m0");
}
#define GL_ "\n\tglobal_load_lds_dwordx4 "
__device__ __forceinline__ void glds4c(const void* sbase, unsigned v, unsigned lds_base) {
  asm volatile("s_mov_b32 m0, %2\n\ts_nop 0" GL_ "%0, %1 sc1" GL_ "%0, %1 offset:1024 sc1" GL_ "%0, %1 offset:2048 sc1" GL_ "%0, %1 offset:3072 sc1"
               :: "v"(v), "s"(sbase), "s"(lds_base) : "memory", "m0");
}
__device__ __forceinline__ void glds4v(const void* sbase, unsigned v0, unsigned v1, unsigned v2, unsigned v3, unsigned lds_base) {
  asm volatile("s_mov_b32 m0, %5\n\ts_nop 0" GL_ "%0, %4 sc1" GL_ "%1, %4 offset:1024 sc1" GL_ "%2, %4 offset:2048 sc1" GL_ "%3, %4 offset:3072 sc1"
               :: "v"(v0), "v"(v1), "v"(v2), "v"(v3), "s"(sbase), "s"(lds_base) : "memory", "m0");
}
__device__ __forceinline__ void glds2v(const void* sbase, unsigned v0, unsigned v1, unsigned lds_base) {
  asm volatile("s_mov_b32 m0, %3\n\ts_nop 0" GL_ "%0, %2 sc1" GL_ "%1, %2 offset:1024 sc1" :: "v"(v0), "v"(v1), "s"(sbase), "s"(lds_base) : "memory", "m0");
}
__device__ __forceinline__ void gld16(u32x4& d, unsigned voff, const void* sbase) {
  asm volatile("global_load_dwordx4 %0, %1, %2" : "=v"(d) : "v"(voff), "s"(sbase) : "memory");
}
__device__ __forceinline__ unsigned lds_addr(const void* p) { return (unsigned)(size_t)p; }
#define WAITV(n) asm volatile("s_waitcnt vmcnt(" #n ")" ::: "memory")
#define WAITVN(n) asm volatile("s_waitcnt vmcnt(%0)" :: "n"(n) : "memory")
#define WAITL0 do { asm volatile("" ::: "memory"); __builtin_amdgcn_s_waitcnt(0xC07F); asm volatile("" ::: "memory"); } while (0)
#define BAR __builtin_amdgcn_s_barrier()
#define ARRIVED(...) do { WAITV(0); __builtin_amdgcn_sched_barrier(0); asm volatile("" : __VA_ARGS__); } while (0)

template <int N> __device__ __forceinline__ float dpp_ror(float x) {
  return __uint_as_float(__builtin_amdgcn_update_dpp(0u, __float_as_uint(x), 0x120 + N, 0xf, 0xf, false));
}
__device__ __forceinline__ float r16_sum(float x) { x += dpp_ror<8>(x); x += dpp_ror<4>(x); x += dpp_ror<2>(x); return x + dpp_ror<1>(x); }
__device__ __forceinline__ void max8pn(const f32x4& a, const f32x4& b, float& mx, float& mn) {
  asm("v_max3_f32 %0, %2, %3, %4\n\tv_max3_f32 %1, -%2, -%3, -%4\n\tv_max3_f32 %0, %0, %5, %6\n\tv_max3_f32 %1, %1, -%5, -%6\n\t"
      "v_max3_f32 %0, %0, %7, %8\n\tv_max3_f32 %1, %1, -%7, -%8\n\tv_max_f32 %0, %0, %9\n\tv_max_f32 %1, %1, -%9\n\ts_nop 0"
      : "=&v"(mx), "=&v"(mn) : "v"(a[0]), "v"(a[1]), "v"(a[2]), "v"(a[3]), "v"(b[0]), "v"(b[1]), "v"(b[2]), "v"(b[3]));
}
__device__ __forceinline__ float vmaxf_n(float a, float b) { float r; asm("v_max_f32 %0, %1, %2\n\ts_nop 0" : "=v"(r) : "v"(a), "v"(b)); return r; }
template <bool MAXOP> __device__ __forceinline__ float rop(float a, float b) { return MAXOP ? vmaxf_n(a, b) : a + b; }
template <bool MAXOP> __device__ __forceinline__ void xr4(float& a, float& b, float& c, float& d) {
  auto r = __builtin_amdgcn_permlane16_swap(__float_as_uint(a), __float_as_uint(b), false, false);
  auto q = __builtin_amdgcn_permlane16_swap(__float_as_uint(c), __float_as_uint(d), false, false);
  const unsigned ab = __float_as_uint(rop<MAXOP>(__uint_as_float(r[0]), __uint_as_float(r[1])));
  const unsigned cd = __float_as_uint(rop<MAXOP>(__uint_as_float(q[0]), __uint_as_float(q[1])));
  auto t = __builtin_amdgcn_permlane32_swap(ab, cd, false, false);
  const unsigned u = __float_as_uint(rop<MAXOP>(__uint_as_float(t[0]), __uint_as_float(t[1])));
  auto h = __builtin_amdgcn_permlane32_swap(u, u, false, false);
  auto p0 = __builtin_amdgcn_permlane16_swap(h[0], h[0], false, false);
  auto p1 = __builtin_amdgcn_permlane16_swap(h[1], h[1], false, false);
  a = __uint_as_float(p0[0]); b = __uint_as_float(p0[1]); c = __uint_as_float(p1[0]); d = __uint_as_float(p1[1]);
}

__device__ __forceinline__ unsigned xs4(float a, float b, float c, float d) {
  auto r = __builtin_amdgcn_permlane16_swap(__float_as_uint(a), __float_as_uint(b), false, false);
  auto q = __builtin_amdgcn_permlane16_swap(__float_as_uint(c), __float_as_uint(d), false, false);
  auto t = __builtin_amdgcn_permlane32_swap(__float_as_uint(__uint_as_float(r[0]) + __uint_as_float(r[1])), __float_as_uint(__uint_as_float(q[0]) + __uint_as_float(q[1])), false, false);
  return __float_as_uint(__uint_as_float(t[0]) + __uint_as_float(t[1]));
}

__device__ __forceinline__ f32x4 sum4(const u32x4 (&v)[4]) {
  float r0, r1, r2, r3, t0, t1, t2, t3;
  asm("v_add_f32 %0, %8, %12\n\tv_add_f32 %1, %9, %13\n\tv_add_f32 %2, %10, %14\n\tv_add_f32 %3, %11, %15\n\t"
      "v_add_f32 %4, %16, %20\n\tv_add_f32 %5, %17, %21\n\tv_add_f32 %6, %18, %22\n\tv_add_f32 %7, %19, %23\n\t"
      "v_add_f32 %0, %0, %4\n\tv_add_f32 %1, %1, %5\n\tv_add_f32 %2, %2, %6\n\tv_add_f32 %3, %3, %7\n\ts_nop 0"
      : "=&v"(r0), "=&v"(r1), "=&v"(r2), "=&v"(r3), "=&v"(t0), "=&v"(t1), "=&v"(t2), "=&v"(t3)
      : "v"(v[0][0]), "v"(v[0][1]), "v"(v[0][2]), "v"(v[0][3]), "v"(v[1][0]), "v"(v[1][1]), "v"(v[1][2]), "v"(v[1][3]),
        "v"(v[2][0]), "v"(v[2][1]), "v"(v[2][2]), "v"(v[2][3]), "v"(v[3][0]), "v"(v[3][1]), "v"(v[3][2]), "v"(v[3][3]));
  return f32x4{r0, r1, r2, r3};
}
#define MFMA(a, b, c) __builtin_amdgcn_mfma_f32_16x16x32_bf16(a, b, c, 0, 0, 0)

typedef unsigned long long u64;
typedef __attribute__((address_space(1))) unsigned long long gu64;
constexpr unsigned WS_FLAGS = 4096;
constexpr int FLS = 64;
constexpr unsigned WS_G0 = 65536;
constexpr unsigned WS_CSP = WS_G0 + SEQ * 16;
constexpr unsigned WS_ST = WS_CSP + 16 * 16 * 768;
constexpr unsigned WS_X = 1u << 21;
constexpr unsigned WS_O = WS_X + 3u * SEQ * DM * 2;
constexpr unsigned KVPB = KVP * 2, KVFB = KVP * 4;
constexpr unsigned EMPTY = 0xffffffffu;
constexpr unsigned WS_KV = WS_O + 4u * SEQ * DM * 2;
constexpr unsigned KVLB = 16u * NH * KVPB + 16u * KVFB;
constexpr unsigned WS_HL = WS_KV + 2u * KVLB;
constexpr unsigned WS_CSA = WS_HL + 512u * 4096;
constexpr unsigned WS_CPA = WS_CSA + 256u * 1024;
constexpr unsigned WS_IMG = WS_CPA + 16 * 16 * 768;
constexpr unsigned WS_END = WS_IMG + 2048 * 1024;
constexpr int LDS_TOTAL = 163840;
constexpr int OFF_WO = 49152;
struct Next { u64 kord, ko2; const char* wq; unsigned tw; };
template <int V> using IC = std::integral_constant<int, V>;
__device__ __forceinline__ bf16x8 ones8() { unsigned e = 0x3F803F80u; asm volatile("" : "+v"(e)); return __builtin_bit_cast(bf16x8, u32x4{e, e, e, e}); }
__device__ __forceinline__ float zero1() { float z; asm volatile("v_mov_b32 %0, 0" : "=v"(z)); return z; }
__device__ __forceinline__ u32x4 empty4() { unsigned e = EMPTY; asm volatile("" : "+v"(e)); return u32x4{e, e, e, e}; }
__device__ __forceinline__ u64 korder(int v, int own) {
  const u64 t = v == 0 ? 0x3b210fedca987654ull : v == 1 ? 0xb37654210fedca98ull : v == 2 ? 0x3ba987654210fedcull : 0xb3fedca987654210ull;
  const u64 inv = v == 0 ? 0xa987e6543210fdcbull : v == 1 ? 0x6543f210dcbae987ull : v == 2 ? 0x3210edcba987f654ull : 0xdcbaf9876543e210ull;
  const u64 low = (1ull << (4 * ((inv >> (4 * own)) & 15))) - 1ull;
  return (u64)own | ((t & low) << 4) | (t & ~((low << 4) | 15ull));
}
__device__ __forceinline__ u64 korder0(int rt) {
  const int r = rt & 7;
  const u64 own = (0x765432765432ull >> (4 * (r < 6 ? r : r - 6))) & 0xffffffull, oth = 0x7654321076543210ull >> (4 * r);
  const u64 t = 0x10ull | ((oth & 15ull) << 8) | (own << 12) | ((oth >> 4) << 36);
  return t | (rt < 8 ? 0x8888888000000800ull : 0x0000000888888088ull);
}
__device__ __forceinline__ int knib(u64 t, int s) { return (int)(t >> (4 * s)) & 15; }
__device__ __forceinline__ unsigned kfirst(u64 t) { return (1u << knib(t, 1)) | (1u << knib(t, 2)) | (1u << knib(t, 3)) | (1u << knib(t, 4)); }
__device__ __forceinline__ int kv1(int bid) { return (bid + (0x4924 >> ((bid >> 2) & 14))) & 3; }

__device__ __forceinline__ bool img(int layer, int hp) { return layer >= 2 && (hp & 7) == 3; }
__device__ __forceinline__ const char* wq_src(const char* ws, const void* W, int layer, int hp) { return img(layer, hp) ? ws + WS_IMG + ((layer & 1) * 768 + (hp >> 3) * 384) * 1024 : (const char*)W; }
__device__ __forceinline__ const char* wo_src(const char* ws, const void* W, int layer, int hp) { return img(layer, hp) ? ws + WS_IMG + (1536 + (layer & 1) * 256 + (hp >> 3) * 128) * 1024 : (const char*)W; }

template <typename T> __device__ __forceinline__ T* uni(T* p) {
  const unsigned long long v = (unsigned long long)p;
  const unsigned lo = __builtin_amdgcn_readfirstlane((unsigned)v), hi = __builtin_amdgcn_readfirstlane((unsigned)(v >> 32));
  return (T*)(((unsigned long long)hi << 32) | lo);
}
__device__ __forceinline__ int lane_id() {
  int l;
  asm volatile("v_mbcnt_lo_u32_b32 %0, -1, 0\n\tv_mbcnt_hi_u32_b32 %0, -1, %0" : "=v"(l));
  __builtin_assume(l >= 0 && l < 64);
  return l;
}
#define IDS \
  const int lane = lane_id(); \
  asm volatile("" : "+s"(w)); \
  const int tid = w * 64 + lane; \
  int bid = blockIdx.x; asm volatile("" : "+s"(bid)); \
  ws = uni(ws); asm volatile("" : "+s"(ws)); \
  layer = __builtin_amdgcn_readfirstlane(layer); x0 = uni(x0); wo = uni(wo); \
    \
  const int rt = 2 * (bid & 4) + ((bid >> 3) & 7); \
  const int hp = 4 * (bid & 3) + (bid >> 6); \
  const int m0 = rt * 128; \
  const int fr = lane & 15, g = lane >> 4; \
  const int q4 = (lane & 15) >> 2, p4 = lane & 3; \
  const __amdgpu_buffer_rsrc_t rsW = __builtin_amdgcn_make_buffer_rsrc((void*)ws, (short)0, (int)WS_END, 0x00020000); \
  (void)m0; (void)fr; (void)q4; (void)p4; (void)rsW; (void)g;

__device__ __forceinline__ void code_touch(unsigned& junk, int bid, int lane) {
  unsigned long long pc;
  unsigned len, d;
  asm volatile("s_getpc_b64 %0\n.Lct%=:\n\ts_mov_b32 %2, .Lct%=-.Lmk_pc\n\ts_mov_b32 %1, .Lmk_end-.Lmk_pc" : "=s"(pc), "=s"(len), "=s"(d));
  pc -= d;
  const unsigned per = ((len >> 10) + 1) * 128, off = ((bid >> 3) & 7) * per + (lane * 128 < per ? lane * 128 : 0);
  asm volatile("global_load_dword %0, %1, %2" : "+v"(junk) : "v"(off < len - 4 ? off : 0u), "s"(pc) : "memory");
}

__device__ __forceinline__ void publish(__amdgpu_buffer_rsrc_t rsW, int tid, unsigned idx, u64 v) {
  unsigned off = WS_FLAGS + idx * FLS;
  asm volatile("" : "+v"(off), "+v"(v));
  WAITV(0);
  WAITL0; BAR;
  if (tid == 192) __builtin_amdgcn_raw_buffer_store_b64(__builtin_bit_cast(u32x2, v), rsW, off, 0, 16);
  asm volatile("" ::: "memory");
}
__device__ __forceinline__ void poll_flags(char* ws, u64 E, int set, int rt, int hp, int lane, unsigned n, bool col) {
  const int l = lane & 15;
  const unsigned fo = WS_FLAGS + (unsigned)(set * 256 + (col ? l * 16 + hp : rt * 16 + l)) * FLS;
  _Pragma("unroll 1") for (unsigned spins = 0; spins < (1u << 18); ++spins) {
    u64 d;
    asm volatile("global_load_dwordx2 %0, %1, %2 sc1\n\ts_waitcnt vmcnt(0)" : "=v"(d) : "v"(fo), "s"(ws) : "memory");
    if (__all(d - (E + n) <= (u64)(16u - n))) break;
    __builtin_amdgcn_s_sleep(1);
  }
}

__device__ __forceinline__ u64 poll_eq(const char* ws, unsigned off, u64 want, u64 mask, u64 now) {
  u64 miss;
  _Pragma("unroll 1") for (unsigned spins = 0; spins < (1u << 18); ++spins) {
    u64 d;
    asm volatile("global_load_dwordx2 %0, %1, %2 sc1\n\ts_waitcnt vmcnt(0)" : "=v"(d) : "v"(off), "s"(ws) : "memory");
    miss = __ballot(d != want) & mask;
    if (!(miss & now)) break;
    __builtin_amdgcn_s_sleep(1);
  }
  return miss;
}

#define G1_MACROS \
  ".macro G1M rb, j, s, z\n" \
  " .set g1d, 160+24*(\\rb)+4*(((\\j)+2)-6*((\\j)>>2))\n .set g1a, 80+40*(\\s)+4*(\\rb)\n .set g1b, 96+40*(\\s)+4*(\\j)\n" \
  " .if ((\\j)>>1) == 1\n  .set g1t, g1a\n  .set g1a, g1b\n  .set g1b, g1t\n .endif\n" \
  " .if \\z\n  v_mfma_f32_16x16x32_bf16 v[g1d:g1d+3], v[g1b:g1b+3], v[g1a:g1a+3], 0\n" \
  " .else\n  v_mfma_f32_16x16x32_bf16 v[g1d:g1d+3], v[g1b:g1b+3], v[g1a:g1a+3], v[g1d:g1d+3]\n .endif\n" \
  ".endm\n" \
  ".macro G1RA rb, s, ra, oa\n" \
  " ds_read_b128 v[80+40*(\\s)+4*(\\rb):83+40*(\\s)+4*(\\rb)], \\ra offset:(\\oa)+(\\rb)*2048\n" \
  ".endm\n" \
  ".macro G1RB k, s, r0, r1, ob\n" \
  " .if (\\k) & 2\n  ds_read_b64_tr_b16 v[96+40*(\\s)+2*(\\k):97+40*(\\s)+2*(\\k)], \\r1 offset:(\\ob)+(((\\k)>>2)+1-3*((\\k)>>3))*2048+((\\k)&1)*512\n" \
  " .else\n  ds_read_b64_tr_b16 v[96+40*(\\s)+2*(\\k):97+40*(\\s)+2*(\\k)], \\r0 offset:(\\ob)+(((\\k)>>2)+1-3*((\\k)>>3))*2048+((\\k)&1)*512\n .endif\n" \
  ".endm\n" \
    \
  ".macro G1BK i, g, r0, r1, ob\n G1RB 4*(\\g)+((\\i)&3), (\\i)>>2, \\r0, \\r1, (\\ob)+((\\i)>>2)*12288\n.endm\n" \
  ".macro G1AK rb, s, a0, a1, oa\n .if \\s\n  G1RA \\rb, 1, \\a1, \\oa\n .else\n  G1RA \\rb, 0, \\a0, \\oa\n .endif\n.endm\n" \
  ".macro G1RDB g, r0, r1, ob\n .set g1i, 0\n .rept 8\n  G1BK g1i, \\g, \\r0, \\r1, \\ob\n  .set g1i, g1i+1\n .endr\n.endm\n" \
  ".macro G1RDA a0, a1, oa\n .set g1i, 0\n .rept 8\n  G1AK (g1i>>1)^2, g1i&1, \\a0, \\a1, \\oa\n  .set g1i, g1i+1\n .endr\n.endm\n" \
    \
  ".macro G1Q g, rb, q, i, z\n G1M \\rb, 2*(\\g)+((((\\i)>>1)^(\\q))&1), (((\\i)+1)>>1)&1, (\\z)&&(((\\i)&1)==0)\n.endm\n" \
    \
  ".macro G1SG g, z, rd, a0, a1, r0, r1, o\n" \
  " .set g1i, 0\n .rept 16\n" \
  "  .if ((\\g) == 0) && (g1i == 8) && (\\rd)\n   s_waitcnt lgkmcnt(7)\n   s_nop 0\n  .endif\n" \
  "  .if ((\\g) == 0) && (g1i == 15)\n   s_waitcnt lgkmcnt(0)\n  .endif\n" \
  "  G1Q \\g, (g1i>>2)^(2-((\\g)&1)), g1i>>2, g1i&3, \\z\n" \
  "  .if \\rd\n" \
  "   .if (\\g) == 0\n    .if g1i < 7\n     G1BK g1i, 2, \\r0, \\r1, \\o\n    .elseif g1i == 8\n     G1BK 7, 2, \\r0, \\r1, \\o\n    .endif\n" \
  "   .elseif (\\g) == 1\n    .if g1i < 8\n     G1BK g1i, 0, \\r0, \\r1, \\o\n    .endif\n" \
  "   .elseif (g1i & 2) == 0\n    G1BK (g1i&1)+2*(g1i>>2), 1, \\r0, \\r1, \\o\n" \
  "   .elseif (\\rd) == 1\n    G1AK (g1i>>2)^2, 1-(g1i&1), \\a0, \\a1, \\o\n   .endif\n" \
  "  .endif\n" \
  "  .set g1i, g1i+1\n .endr\n" \
  ".endm\n" \
  ""
#define G1_SLIDE \
  "G1Q 1, 3, 0, 0, 0\n ds_read2_b64 v[60:63], v49 offset0:32 offset1:48\n ds_read_b128 v[96:99], v48 offset:256\n G1Q 1, 3, 0, 1, 0\n " \
  "ds_read_b128 v[100:103], v48 offset:320\n ds_read2_b32 v[56:57], v52 offset1:16\n G1Q 1, 3, 0, 2, 0\n ds_read_b128 v[136:139], v54 offset:192\n " \
  "ds_read_b128 v[140:143], v54 offset:704\n G1Q 1, 3, 0, 3, 0\n G1Q 1, 2, 1, 0, 0\n G1Q 1, 2, 1, 1, 0\n s_waitcnt lgkmcnt(3)\n " \
  "v_fmac_f32 v240, v96, v62\n G1Q 1, 2, 1, 2, 0\n v_fma_f32 v241, v97, v62, v241\n v_fma_f32 v242, v98, v62, v242\n G1Q 1, 2, 1, 3, 0\n " \
  "v_fma_f32 v243, v99, v62, v243\n v_fma_f32 v244, v100, v62, v244\n G1Q 1, 1, 2, 0, 0\n v_fma_f32 v245, v101, v62, v245\n " \
  "v_fma_f32 v246, v102, v62, v246\n G1Q 1, 1, 2, 1, 0\n v_fma_f32 v247, v103, v62, v247\n v_mul_f32_e64 v240, v63, v240\n G1Q 1, 1, 2, 2, 0\n " \
  "v_mul_f32_e64 v241, v63, v241\n v_mul_f32_e64 v242, v63, v242\n G1Q 1, 1, 2, 3, 0\n v_mul_f32_e64 v243, v63, v243\n v_mul_f32_e64 v244, v63, v244\n " \
  "G1Q 1, 0, 3, 0, 0\n v_mul_f32_e64 v245, v63, v245\n v_mul_f32_e64 v246, v63, v246\n G1Q 1, 0, 3, 1, 0\n v_mul_f32_e64 v247, v63, v247\n " \
  "v_cvt_pk_bf16_f32 v240, v240, v241\n G1Q 1, 0, 3, 2, 0\n v_cvt_pk_bf16_f32 v241, v242, v243\n v_cvt_pk_bf16_f32 v242, v244, v245\n " \
  "G1Q 1, 0, 3, 3, 0\n v_cvt_pk_bf16_f32 v243, v246, v247\n s_waitcnt lgkmcnt(0)\n v_fmac_f32 v248, v136, v56\n G1Q 2, 2, 0, 0, 0\n " \
  "ds_read2_b64 v[144:147], v49 offset1:16\n v_fma_f32 v249, v137, v56, v249\n G1Q 2, 2, 0, 1, 0\n ds_read_b128 v[148:151], v48\n " \
  "v_fma_f32 v250, v138, v56, v250\n G1Q 2, 2, 0, 2, 0\n ds_read_b128 v[104:107], v48 offset:64\n v_fma_f32 v251, v139, v56, v251\n G1Q 2, 2, 0, 3, 0\n " \
  "ds_read_b128 v[108:111], v54 offset:128\n v_fma_f32 v252, v136, v57, v252\n G1Q 2, 3, 1, 0, 0\n v_fma_f32 v253, v137, v57, v253\n " \
  "v_fma_f32 v254, v138, v57, v254\n G1Q 2, 3, 1, 1, 0\n ds_read_b128 v[88:91], v54 offset:640\n v_fma_f32 v255, v139, v57, v255\n G1Q 2, 3, 1, 2, 0\n " \
  "ds_read_b128 v[128:131], v54\n v_mul_f32_e64 v248, v140, v248\n G1Q 2, 3, 1, 3, 0\n v_mul_f32_e64 v249, v141, v249\n v_mul_f32_e64 v250, v142, v250\n " \
  "G1Q 2, 0, 2, 0, 0\n v_mul_f32_e64 v251, v143, v251\n v_mul_f32_e64 v252, v140, v252\n G1Q 2, 0, 2, 1, 0\n ds_read_b128 v[92:95], v54 offset:512\n " \
  "v_mul_f32_e64 v253, v141, v253\n G1Q 2, 0, 2, 2, 0\n v_mul_f32_e64 v254, v142, v254\n v_mul_f32_e64 v255, v143, v255\n G1Q 2, 0, 2, 3, 0\n " \
  "v_cvt_pk_bf16_f32 v248, v248, v249\n v_cvt_pk_bf16_f32 v249, v250, v251\n G1Q 2, 1, 3, 0, 0\n v_cvt_pk_bf16_f32 v255, v254, v255\n " \
  "v_cvt_pk_bf16_f32 v254, v252, v253\n G1Q 2, 1, 3, 1, 0\n v_min_u32_e64 v136, -2, v240\n v_min_u32_e64 v137, -2, v241\n v_min_u32_e64 v138, -2, v242\n " \
  "G1Q 2, 1, 3, 2, 0\n v_min_u32_e64 v139, -2, v243\n v_min_u32_e64 v140, -2, v248\n v_min_u32_e64 v141, -2, v249\n G1Q 2, 1, 3, 3, 0\n " \
  "v_min_u32_e64 v142, -2, v254\n v_min_u32_e64 v143, -2, v255\n ds_read_b128 v[112:115], v54 offset:64\n ds_read_b128 v[116:119], v54 offset:576\n " \
  "s_cmp_eq_u32 %[wm], 0\n s_cbranch_scc1 .Lg1d%=\n buffer_store_dwordx4 v[136:139], v55, %[rs], 0 offen sc1\n " \
  "buffer_store_dwordx4 v[140:143], v55, %[rs], 0 offen offset:1024 sc1\n ds_read_b32 v53, v59\n ds_read_b32 v53, v59\n ds_read_b32 v53, v59\n " \
  "s_branch .Lg1e%=\n .Lg1d%=:\n v_mov_b32 v53, 1\n ds_write_b128 v58, v[136:139]\n ds_write_b128 v58, v[140:143] offset:1024\n ds_write_b32 v59, v53\n " \
  ".Lg1e%=:\n s_waitcnt lgkmcnt(8)\n v_pk_fma_f32 v[232:233], v[148:149], v[62:63], v[232:233] op_sel_hi:[1,0,1]\n " \
  "v_pk_fma_f32 v[234:235], v[150:151], v[62:63], v[234:235] op_sel_hi:[1,0,1]\n " \
  "v_pk_fma_f32 v[236:237], v[104:105], v[62:63], v[236:237] op_sel_hi:[1,0,1]\n " \
  "v_pk_fma_f32 v[238:239], v[106:107], v[62:63], v[238:239] op_sel_hi:[1,0,1]\n " \
  "v_pk_fma_f32 v[216:217], v[96:97], v[60:61], v[216:217] op_sel_hi:[1,0,1]\n " \
  "v_pk_fma_f32 v[218:219], v[98:99], v[60:61], v[218:219] op_sel_hi:[1,0,1]\n " \
  "v_pk_fma_f32 v[220:221], v[100:101], v[60:61], v[220:221] op_sel_hi:[1,0,1]\n " \
  "v_pk_fma_f32 v[222:223], v[102:103], v[60:61], v[222:223] op_sel_hi:[1,0,1]\n v_pk_mul_f32 v[216:217], v[60:61], v[216:217] op_sel:[1,0]\n " \
  "v_pk_mul_f32 v[218:219], v[60:61], v[218:219] op_sel:[1,0]\n v_pk_mul_f32 v[220:221], v[60:61], v[220:221] op_sel:[1,0]\n " \
  "v_pk_mul_f32 v[222:223], v[60:61], v[222:223] op_sel:[1,0]\n v_cvt_pk_bf16_f32 v232, v232, v233\n v_cvt_pk_bf16_f32 v233, v234, v235\n " \
  "v_cvt_pk_bf16_f32 v234, v236, v237\n v_cvt_pk_bf16_f32 v235, v238, v239\n v_cvt_pk_bf16_f32 v216, v216, v217\n v_cvt_pk_bf16_f32 v217, v218, v219\n " \
  "v_cvt_pk_bf16_f32 v218, v220, v221\n v_cvt_pk_bf16_f32 v219, v222, v223\n " \
  "v_pk_fma_f32 v[208:209], v[148:149], v[60:61], v[208:209] op_sel_hi:[1,0,1]\n " \
  "v_pk_fma_f32 v[210:211], v[150:151], v[60:61], v[210:211] op_sel_hi:[1,0,1]\n " \
  "v_pk_fma_f32 v[212:213], v[104:105], v[60:61], v[212:213] op_sel_hi:[1,0,1]\n " \
  "v_pk_fma_f32 v[214:215], v[106:107], v[60:61], v[214:215] op_sel_hi:[1,0,1]\n " \
  "v_pk_fma_f32 v[192:193], v[96:97], v[146:147], v[192:193] op_sel_hi:[1,0,1]\n " \
  "v_pk_fma_f32 v[194:195], v[98:99], v[146:147], v[194:195] op_sel_hi:[1,0,1]\n " \
  "v_pk_fma_f32 v[196:197], v[100:101], v[146:147], v[196:197] op_sel_hi:[1,0,1]\n " \
  "v_pk_fma_f32 v[198:199], v[102:103], v[146:147], v[198:199] op_sel_hi:[1,0,1]\n v_pk_mul_f32 v[192:193], v[146:147], v[192:193] op_sel:[1,0]\n " \
  "v_pk_mul_f32 v[194:195], v[146:147], v[194:195] op_sel:[1,0]\n v_pk_mul_f32 v[196:197], v[146:147], v[196:197] op_sel:[1,0]\n " \
  "v_pk_mul_f32 v[198:199], v[146:147], v[198:199] op_sel:[1,0]\n v_mfma_f32_16x16x32_bf16 v[120:123], v[240:243], v[232:235], v[40:43]\n " \
  "v_mfma_f32_16x16x32_bf16 v[236:239], v[216:219], v[232:235], v[36:39]\n v_cvt_pk_bf16_f32 v208, v208, v209\n v_cvt_pk_bf16_f32 v209, v210, v211\n " \
  "v_cvt_pk_bf16_f32 v210, v212, v213\n v_cvt_pk_bf16_f32 v211, v214, v215\n v_cvt_pk_bf16_f32 v192, v192, v193\n v_cvt_pk_bf16_f32 v193, v194, v195\n " \
  "v_cvt_pk_bf16_f32 v194, v196, v197\n v_cvt_pk_bf16_f32 v195, v198, v199\n " \
  "v_pk_fma_f32 v[184:185], v[148:149], v[146:147], v[184:185] op_sel_hi:[1,0,1]\n " \
  "v_pk_fma_f32 v[186:187], v[150:151], v[146:147], v[186:187] op_sel_hi:[1,0,1]\n " \
  "v_pk_fma_f32 v[188:189], v[104:105], v[146:147], v[188:189] op_sel_hi:[1,0,1]\n " \
  "v_pk_fma_f32 v[190:191], v[106:107], v[146:147], v[190:191] op_sel_hi:[1,0,1]\n " \
  "v_pk_fma_f32 v[168:169], v[96:97], v[144:145], v[168:169] op_sel_hi:[1,0,1]\n " \
  "v_pk_fma_f32 v[170:171], v[98:99], v[144:145], v[170:171] op_sel_hi:[1,0,1]\n " \
  "v_pk_fma_f32 v[172:173], v[100:101], v[144:145], v[172:173] op_sel_hi:[1,0,1]\n " \
  "v_pk_fma_f32 v[174:175], v[102:103], v[144:145], v[174:175] op_sel_hi:[1,0,1]\n v_pk_mul_f32 v[168:169], v[144:145], v[168:169] op_sel:[1,0]\n " \
  "v_pk_mul_f32 v[170:171], v[144:145], v[170:171] op_sel:[1,0]\n v_pk_mul_f32 v[172:173], v[144:145], v[172:173] op_sel:[1,0]\n " \
  "v_pk_mul_f32 v[174:175], v[144:145], v[174:175] op_sel:[1,0]\n v_mfma_f32_16x16x32_bf16 v[220:223], v[216:219], v[208:211], v[40:43]\n " \
  "v_mfma_f32_16x16x32_bf16 v[212:215], v[192:195], v[208:211], v[36:39]\n v_cvt_pk_bf16_f32 v184, v184, v185\n v_cvt_pk_bf16_f32 v185, v186, v187\n " \
  "v_cvt_pk_bf16_f32 v186, v188, v189\n v_cvt_pk_bf16_f32 v187, v190, v191\n v_cvt_pk_bf16_f32 v168, v168, v169\n v_cvt_pk_bf16_f32 v169, v170, v171\n " \
  "v_cvt_pk_bf16_f32 v170, v172, v173\n v_cvt_pk_bf16_f32 v171, v174, v175\n " \
  "v_pk_fma_f32 v[160:161], v[148:149], v[144:145], v[160:161] op_sel_hi:[1,0,1]\n " \
  "v_pk_fma_f32 v[162:163], v[150:151], v[144:145], v[162:163] op_sel_hi:[1,0,1]\n " \
  "v_pk_fma_f32 v[164:165], v[104:105], v[144:145], v[164:165] op_sel_hi:[1,0,1]\n " \
  "v_pk_fma_f32 v[166:167], v[106:107], v[144:145], v[166:167] op_sel_hi:[1,0,1]\n s_waitcnt lgkmcnt(3)\n " \
  "v_pk_fma_f32 v[176:177], v[128:129], v[56:57], v[176:177] op_sel_hi:[1,0,1]\n " \
  "v_pk_fma_f32 v[178:179], v[130:131], v[56:57], v[178:179] op_sel_hi:[1,0,1]\n " \
  "v_pk_fma_f32 v[180:181], v[128:129], v[56:57], v[180:181] op_sel:[0,1,0]\n v_pk_fma_f32 v[182:183], v[130:131], v[56:57], v[182:183] op_sel:[0,1,0]\n " \
  "v_pk_mul_f32 v[176:177], v[176:177], v[92:93]\n v_pk_mul_f32 v[178:179], v[178:179], v[94:95]\n v_pk_mul_f32 v[180:181], v[180:181], v[92:93]\n " \
  "v_pk_mul_f32 v[182:183], v[182:183], v[94:95]\n v_mfma_f32_16x16x32_bf16 v[196:199], v[192:195], v[184:187], v[40:43]\n " \
  "v_mfma_f32_16x16x32_bf16 v[188:191], v[168:171], v[184:187], v[36:39]\n v_cvt_pk_bf16_f32 v160, v160, v161\n v_cvt_pk_bf16_f32 v161, v162, v163\n " \
  "v_cvt_pk_bf16_f32 v162, v164, v165\n v_cvt_pk_bf16_f32 v163, v166, v167\n v_cvt_pk_bf16_f32 v174, v176, v177\n v_cvt_pk_bf16_f32 v175, v178, v179\n " \
  "v_cvt_pk_bf16_f32 v180, v180, v181\n v_cvt_pk_bf16_f32 v181, v182, v183\n " \
  "v_pk_fma_f32 v[224:225], v[108:109], v[56:57], v[224:225] op_sel_hi:[1,0,1]\n " \
  "v_pk_fma_f32 v[226:227], v[110:111], v[56:57], v[226:227] op_sel_hi:[1,0,1]\n " \
  "v_pk_fma_f32 v[228:229], v[108:109], v[56:57], v[228:229] op_sel:[0,1,0]\n v_pk_fma_f32 v[230:231], v[110:111], v[56:57], v[230:231] op_sel:[0,1,0]\n " \
  "v_pk_mul_f32 v[224:225], v[224:225], v[88:89]\n v_pk_mul_f32 v[226:227], v[226:227], v[90:91]\n v_pk_mul_f32 v[228:229], v[228:229], v[88:89]\n " \
  "v_pk_mul_f32 v[230:231], v[230:231], v[90:91]\n v_mfma_f32_16x16x32_bf16 v[124:127], v[168:171], v[160:163], v[40:43]\n " \
  "v_cvt_pk_bf16_f32 v246, v224, v225\n v_cvt_pk_bf16_f32 v247, v226, v227\n v_cvt_pk_bf16_f32 v252, v228, v229\n v_cvt_pk_bf16_f32 v253, v230, v231\n " \
  "v_max3_f32 v140, v236, v237, v238\n v_max3_f32 v138, v212, v213, v214\n v_max3_f32 v141, v239, v120, v121\n v_max3_f32 v139, v215, v220, v221\n " \
  "v_max3_f32 v140, v140, v122, v123\n v_max3_f32 v138, v138, v222, v223\n v_max_f32 v140, v140, v141\n v_max_f32 v138, v138, v139\n " \
  "v_max3_f32 v136, v188, v189, v190\n v_max3_f32 v137, v191, v196, v197\n v_max3_f32 v136, v136, v198, v199\n " \
  "v_pk_fma_f32 v[200:201], v[112:113], v[56:57], v[200:201] op_sel_hi:[1,0,1]\n " \
  "v_pk_fma_f32 v[202:203], v[114:115], v[56:57], v[202:203] op_sel_hi:[1,0,1]\n v_max_f32 v136, v136, v137\n v_mov_b32 v142, v140\n " \
  "v_pk_fma_f32 v[204:205], v[112:113], v[56:57], v[204:205] op_sel:[0,1,0]\n v_pk_fma_f32 v[206:207], v[114:115], v[56:57], v[206:207] op_sel:[0,1,0]\n " \
  "v_permlane16_swap_b32 v136, v138\n v_permlane16_swap_b32 v140, v142\n v_pk_mul_f32 v[200:201], v[200:201], v[116:117]\n " \
  "v_pk_mul_f32 v[202:203], v[202:203], v[118:119]\n v_max_f32 v136, v136, v138\n v_max_f32 v140, v140, v142\n " \
  "v_pk_mul_f32 v[204:205], v[204:205], v[116:117]\n v_pk_mul_f32 v[206:207], v[206:207], v[118:119]\n v_permlane32_swap_b32 v136, v140\n " \
  "v_mul_f32 v152, 0x3e8293ee, v63\n v_mul_f32 v154, 0x3e8293ee, v61\n v_max_f32 v136, v136, v140\n v_mov_b32 v140, v136\n " \
  "v_mul_f32 v156, 0x3e8293ee, v147\n v_mul_f32 v158, 0x3e8293ee, v145\n v_permlane32_swap_b32 v136, v140\n v_mov_b32 v132, 0x3f803f80\n " \
  "v_mov_b32 v133, 0x3f803f80\n v_mov_b32 v138, v136\n v_mov_b32 v134, 0x3f803f80\n v_mov_b32 v135, 0x3f803f80\n v_permlane16_swap_b32 v136, v138\n " \
  "v_cvt_pk_bf16_f32 v244, v200, v201\n v_cvt_pk_bf16_f32 v245, v202, v203\n v_cvt_pk_bf16_f32 v250, v204, v205\n v_cvt_pk_bf16_f32 v251, v206, v207\n " \
  "v_cvt_pk_bf16_f32 v176, v200, v201\n v_cvt_pk_bf16_f32 v177, v202, v203\n v_cvt_pk_bf16_f32 v182, v204, v205\n v_cvt_pk_bf16_f32 v183, v206, v207\n " \
  "v_mul_f32_e64 v153, v152, -v140\n v_mul_f32_e64 v155, v154, -v138\n v_mul_f32_e64 v157, v156, -v136\n " \
  "v_pk_fma_f32 v[236:237], v[236:237], v[152:153], v[152:153] op_sel:[0,0,1] op_sel_hi:[1,0,1]\n " \
  "v_pk_fma_f32 v[238:239], v[238:239], v[152:153], v[152:153] op_sel:[0,0,1] op_sel_hi:[1,0,1]\n " \
  "v_pk_fma_f32 v[120:121], v[120:121], v[152:153], v[152:153] op_sel:[0,0,1] op_sel_hi:[1,0,1]\n " \
  "v_pk_fma_f32 v[122:123], v[122:123], v[152:153], v[152:153] op_sel:[0,0,1] op_sel_hi:[1,0,1]\n v_exp_f32 v236, v236\n v_exp_f32 v237, v237\n " \
  "v_exp_f32 v238, v238\n v_exp_f32 v239, v239\n v_exp_f32 v120, v120\n v_exp_f32 v121, v121\n v_exp_f32 v122, v122\n v_exp_f32 v123, v123\n " \
  "v_pk_fma_f32 v[212:213], v[212:213], v[154:155], v[154:155] op_sel:[0,0,1] op_sel_hi:[1,0,1]\n " \
  "v_pk_fma_f32 v[214:215], v[214:215], v[154:155], v[154:155] op_sel:[0,0,1] op_sel_hi:[1,0,1]\n " \
  "v_pk_fma_f32 v[220:221], v[220:221], v[154:155], v[154:155] op_sel:[0,0,1] op_sel_hi:[1,0,1]\n " \
  "v_pk_fma_f32 v[222:223], v[222:223], v[154:155], v[154:155] op_sel:[0,0,1] op_sel_hi:[1,0,1]\n v_cvt_pk_bf16_f32 v236, v236, v237\n " \
  "v_cvt_pk_bf16_f32 v237, v238, v239\n v_cvt_pk_bf16_f32 v238, v120, v121\n v_cvt_pk_bf16_f32 v239, v122, v123\n v_exp_f32 v212, v212\n " \
  "v_exp_f32 v213, v213\n v_mfma_f32_16x16x32_bf16 v[120:123], v[132:135], v[236:239], 0\n " \
  "v_mfma_f32_16x16x32_bf16 v[232:235], v[246:249], v[236:239], 0\n v_mfma_f32_16x16x32_bf16 v[240:243], v[252:255], v[236:239], 0\n " \
  "v_exp_f32 v214, v214\n v_exp_f32 v215, v215\n v_exp_f32 v220, v220\n v_exp_f32 v221, v221\n v_exp_f32 v222, v222\n v_exp_f32 v223, v223\n " \
  "v_pk_fma_f32 v[188:189], v[188:189], v[156:157], v[156:157] op_sel:[0,0,1] op_sel_hi:[1,0,1]\n " \
  "v_pk_fma_f32 v[190:191], v[190:191], v[156:157], v[156:157] op_sel:[0,0,1] op_sel_hi:[1,0,1]\n " \
  "v_pk_fma_f32 v[196:197], v[196:197], v[156:157], v[156:157] op_sel:[0,0,1] op_sel_hi:[1,0,1]\n " \
  "v_pk_fma_f32 v[198:199], v[198:199], v[156:157], v[156:157] op_sel:[0,0,1] op_sel_hi:[1,0,1]\n v_cvt_pk_bf16_f32 v212, v212, v213\n " \
  "v_cvt_pk_bf16_f32 v213, v214, v215\n v_cvt_pk_bf16_f32 v214, v220, v221\n v_cvt_pk_bf16_f32 v215, v222, v223\n v_exp_f32 v188, v188\n " \
  "s_cmp_eq_u32 %[wm], 0\n s_cbranch_scc1 .Lg1f%=\n ds_read_b32 v53, v59\n ds_read_b128 v[80:83], v58\n ds_read_b64 v[172:173], v58 offset:1024\n " \
  "ds_read_b64 v[178:179], v58 offset:1032\n s_branch .Lg1g%=\n .Lg1f%=:\n buffer_load_dwordx4 v[80:83], v55, %[rs], 0 offen sc1\n " \
  "buffer_load_dwordx2 v[172:173], v55, %[rs], 0 offen offset:1024 sc1\n buffer_load_dwordx2 v[178:179], v55, %[rs], 0 offen offset:1032 sc1\n " \
  ".Lg1g%=:\n v_exp_f32 v189, v189\n v_mfma_f32_16x16x32_bf16 v[220:223], v[132:135], v[212:215], 0\n " \
  "v_mfma_f32_16x16x32_bf16 v[224:227], v[244:247], v[212:215], 0\n v_mfma_f32_16x16x32_bf16 v[228:231], v[250:253], v[212:215], 0\n " \
  "v_exp_f32 v190, v190\n v_exp_f32 v191, v191\n v_exp_f32 v196, v196\n v_exp_f32 v197, v197\n v_exp_f32 v198, v198\n v_exp_f32 v199, v199\n " \
  "v_rcp_f32 v120, v120\n v_cvt_pk_bf16_f32 v188, v188, v189\n v_pk_mul_f32 v[232:233], v[232:233], v[120:121] op_sel_hi:[1,0]\n " \
  "v_pk_mul_f32 v[234:235], v[234:235], v[120:121] op_sel_hi:[1,0]\n v_pk_mul_f32 v[240:241], v[240:241], v[120:121] op_sel_hi:[1,0]\n " \
  "v_pk_mul_f32 v[242:243], v[242:243], v[120:121] op_sel_hi:[1,0]\n v_cvt_pk_bf16_f32 v189, v190, v191\n v_cvt_pk_bf16_f32 v190, v196, v197\n " \
  "v_cvt_pk_bf16_f32 v191, v198, v199\n v_rcp_f32 v220, v220\n v_cvt_pk_bf16_f32 v232, v232, v233\n v_cvt_pk_bf16_f32 v233, v234, v235\n " \
  "v_mfma_f32_16x16x32_bf16 v[196:199], v[132:135], v[188:191], 0\n v_mfma_f32_16x16x32_bf16 v[200:203], v[174:177], v[188:191], 0\n " \
  "v_mfma_f32_16x16x32_bf16 v[204:207], v[180:183], v[188:191], 0\n v_cvt_pk_bf16_f32 v234, v240, v241\n v_cvt_pk_bf16_f32 v235, v242, v243\n " \
  "buffer_store_dwordx4 v[232:235], v51, %[rs], 0 offen offset:3072 sc1\n ds_write_b128 v50, v[232:235] offset:3072\n " \
  "v_pk_mul_f32 v[224:225], v[224:225], v[220:221] op_sel_hi:[1,0]\n v_pk_mul_f32 v[226:227], v[226:227], v[220:221] op_sel_hi:[1,0]\n " \
  "v_pk_mul_f32 v[228:229], v[228:229], v[220:221] op_sel_hi:[1,0]\n v_pk_mul_f32 v[230:231], v[230:231], v[220:221] op_sel_hi:[1,0]\n " \
  "v_cvt_pk_bf16_f32 v224, v224, v225\n v_cvt_pk_bf16_f32 v225, v226, v227\n v_cvt_pk_bf16_f32 v226, v228, v229\n v_cvt_pk_bf16_f32 v227, v230, v231\n " \
  "v_rcp_f32 v196, v196\n buffer_store_dwordx4 v[224:227], v51, %[rs], 0 offen offset:2048 sc1\n ds_write_b128 v50, v[224:227] offset:2048\n " \
  "v_pk_mul_f32 v[200:201], v[200:201], v[196:197] op_sel_hi:[1,0]\n v_pk_mul_f32 v[202:203], v[202:203], v[196:197] op_sel_hi:[1,0]\n " \
  "v_pk_mul_f32 v[204:205], v[204:205], v[196:197] op_sel_hi:[1,0]\n v_pk_mul_f32 v[206:207], v[206:207], v[196:197] op_sel_hi:[1,0]\n " \
  "v_cvt_pk_bf16_f32 v200, v200, v201\n v_cvt_pk_bf16_f32 v201, v202, v203\n v_cvt_pk_bf16_f32 v202, v204, v205\n v_cvt_pk_bf16_f32 v203, v206, v207\n " \
  "buffer_store_dwordx4 v[200:203], v51, %[rs], 0 offen offset:1024 sc1\n ds_write_b128 v50, v[200:203] offset:1024\n s_mov_b32 vcc_hi, 0x40000\n " \
  "s_cmp_eq_u32 %[wm], 0\n s_cbranch_scc1 .Lg1h%=\n s_waitcnt lgkmcnt(3)\n .Lg1i%=:\n v_readfirstlane_b32 vcc_lo, v53\n s_nop 1\n " \
  "s_cmp_lg_u32 vcc_lo, 0\n s_cbranch_scc1 .Lg1k%=\n ds_read_b32 v53, v59\n ds_read_b128 v[80:83], v58\n ds_read_b64 v[172:173], v58 offset:1024\n " \
  "ds_read_b64 v[178:179], v58 offset:1032\n s_waitcnt lgkmcnt(0)\n s_sub_u32 vcc_hi, vcc_hi, 1\n s_cbranch_scc0 .Lg1i%=\n s_branch .Lg1k%=\n .Lg1h%=:\n " \
  "s_mov_b32 m0, 0x40000\n s_waitcnt vmcnt(3)\n .Lg1j%=:\n v_max3_u32 v136, v80, v81, v82\n v_max3_u32 v137, v83, v172, v173\n " \
  "v_max3_u32 v136, v136, v178, v179\n v_max_u32 v136, v136, v137\n v_cmp_eq_u32 vcc, -1, v136\n s_cbranch_vccz .Lg1k%=\n " \
  "buffer_load_dwordx4 v[80:83], v55, %[rs], 0 offen sc1\n buffer_load_dwordx2 v[172:173], v55, %[rs], 0 offen offset:1024 sc1\n " \
  "buffer_load_dwordx2 v[178:179], v55, %[rs], 0 offen offset:1032 sc1\n s_waitcnt vmcnt(0)\n s_sub_u32 m0, m0, 1\n s_cbranch_scc0 .Lg1j%=\n .Lg1k%=:\n " \
  "v_mfma_f32_16x16x32_bf16 v[164:167], v[80:83], v[160:163], v[44:47]\n s_nop 7\n v_max3_f32 v136, v164, v165, v166\n " \
  "v_max3_f32 v137, v167, v124, v125\n v_max3_f32 v136, v136, v126, v127\n v_max_f32 v136, v136, v137\n v_mov_b32 v137, v136\n s_nop 1\n " \
  "v_permlane16_swap_b32 v136, v137\n v_max_f32 v136, v136, v137\n v_mov_b32 v137, v136\n s_nop 1\n v_permlane32_swap_b32 v136, v137\n " \
  "v_max_f32 v136, v136, v137\n v_mul_f32_e64 v159, v158, -v136\n " \
  "v_pk_fma_f32 v[164:165], v[164:165], v[158:159], v[158:159] op_sel:[0,0,1] op_sel_hi:[1,0,1]\n " \
  "v_pk_fma_f32 v[166:167], v[166:167], v[158:159], v[158:159] op_sel:[0,0,1] op_sel_hi:[1,0,1]\n " \
  "v_pk_fma_f32 v[124:125], v[124:125], v[158:159], v[158:159] op_sel:[0,0,1] op_sel_hi:[1,0,1]\n " \
  "v_pk_fma_f32 v[126:127], v[126:127], v[158:159], v[158:159] op_sel:[0,0,1] op_sel_hi:[1,0,1]\n v_exp_f32 v164, v164\n v_exp_f32 v165, v165\n " \
  "v_exp_f32 v166, v166\n v_exp_f32 v167, v167\n v_exp_f32 v124, v124\n v_exp_f32 v125, v125\n v_exp_f32 v126, v126\n v_exp_f32 v127, v127\n " \
  "v_cvt_pk_bf16_f32 v164, v164, v165\n v_cvt_pk_bf16_f32 v165, v166, v167\n v_cvt_pk_bf16_f32 v166, v124, v125\n v_cvt_pk_bf16_f32 v167, v126, v127\n " \
  "s_nop 1\n v_mfma_f32_16x16x32_bf16 v[124:127], v[132:135], v[164:167], 0\n v_mfma_f32_16x16x32_bf16 v[168:171], v[172:175], v[164:167], 0\n " \
  "v_mfma_f32_16x16x32_bf16 v[84:87], v[178:181], v[164:167], 0\n s_nop 5\n v_rcp_f32 v124, v124\n s_nop 0\n " \
  "v_pk_mul_f32 v[168:169], v[168:169], v[124:125] op_sel_hi:[1,0]\n v_pk_mul_f32 v[170:171], v[170:171], v[124:125] op_sel_hi:[1,0]\n " \
  "v_pk_mul_f32 v[84:85], v[84:85], v[124:125] op_sel_hi:[1,0]\n v_pk_mul_f32 v[86:87], v[86:87], v[124:125] op_sel_hi:[1,0]\n " \
  "v_cvt_pk_bf16_f32 v168, v168, v169\n v_cvt_pk_bf16_f32 v169, v170, v171\n v_cvt_pk_bf16_f32 v170, v84, v85\n v_cvt_pk_bf16_f32 v171, v86, v87\n " \
  "buffer_store_dwordx4 v[168:171], v51, %[rs], 0 offen offset:0 sc1\n ds_write_b128 v50, v[168:171] offset:0\n "
#define G1_HEDGE \
  "G1Q 1, 1, 0, 0, 0\n ds_read2_b64 v[140:143], v49 offset1:16\n ds_read_b128 v[96:99], v48 offset:256\n G1Q 1, 1, 0, 1, 0\n " \
  "ds_read_b128 v[100:103], v48 offset:320\n ds_read2_b64 v[136:139], v49 offset0:32 offset1:48\n G1Q 1, 1, 0, 2, 0\n ds_read_b64_tr_b16 v[36:37], v53\n " \
  "ds_read_b64_tr_b16 v[38:39], v53 offset:1024\n G1Q 1, 1, 0, 3, 0\n ds_read_b64_tr_b16 v[40:41], v53 offset:32\n " \
  "ds_read_b64_tr_b16 v[42:43], v53 offset:1056\n G1Q 1, 0, 1, 0, 0\n G1Q 1, 0, 1, 1, 0\n s_waitcnt lgkmcnt(4)\n v_fmac_f32 v168, v96, v140\n " \
  "G1Q 1, 0, 1, 2, 0\n v_fma_f32 v169, v97, v140, v169\n v_fma_f32 v170, v98, v140, v170\n G1Q 1, 0, 1, 3, 0\n v_fma_f32 v171, v99, v140, v171\n " \
  "v_fma_f32 v172, v100, v140, v172\n G1Q 1, 3, 2, 0, 0\n v_fma_f32 v173, v101, v140, v173\n v_fma_f32 v174, v102, v140, v174\n G1Q 1, 3, 2, 1, 0\n " \
  "v_fma_f32 v175, v103, v140, v175\n v_cvt_pk_bf16_f32 v168, v168, v169\n G1Q 1, 3, 2, 2, 0\n v_cvt_pk_bf16_f32 v169, v170, v171\n " \
  "v_cvt_pk_bf16_f32 v170, v172, v173\n G1Q 1, 3, 2, 3, 0\n v_cvt_pk_bf16_f32 v171, v174, v175\n v_fma_f32 v192, v96, v142, v192\n G1Q 1, 2, 3, 0, 0\n " \
  "v_fma_f32 v193, v97, v142, v193\n v_fma_f32 v194, v98, v142, v194\n G1Q 1, 2, 3, 1, 0\n v_fma_f32 v195, v99, v142, v195\n " \
  "v_fma_f32 v196, v100, v142, v196\n G1Q 1, 2, 3, 2, 0\n v_fma_f32 v197, v101, v142, v197\n v_fma_f32 v198, v102, v142, v198\n G1Q 1, 2, 3, 3, 0\n " \
  "v_fma_f32 v199, v103, v142, v199\n v_cvt_pk_bf16_f32 v192, v192, v193\n G1Q 2, 2, 0, 0, 0\n ds_read2_b32 v[144:145], v52 offset1:16\n " \
  "v_cvt_pk_bf16_f32 v193, v194, v195\n G1Q 2, 2, 0, 1, 0\n ds_read_b128 v[148:151], v48\n v_cvt_pk_bf16_f32 v194, v196, v197\n G1Q 2, 2, 0, 2, 0\n " \
  "v_cvt_pk_bf16_f32 v195, v198, v199\n v_fma_f32 v216, v96, v136, v216\n G1Q 2, 2, 0, 3, 0\n v_fma_f32 v217, v97, v136, v217\n " \
  "v_fma_f32 v218, v98, v136, v218\n G1Q 2, 3, 1, 0, 0\n v_fma_f32 v219, v99, v136, v219\n v_fma_f32 v220, v100, v136, v220\n G1Q 2, 3, 1, 1, 0\n " \
  "ds_read_b128 v[88:91], v48 offset:64\n v_fma_f32 v221, v101, v136, v221\n v_fma_f32 v222, v102, v136, v222\n G1Q 2, 3, 1, 2, 0\n " \
  "ds_read_b128 v[128:131], v54\n v_fma_f32 v223, v103, v136, v223\n v_cvt_pk_bf16_f32 v216, v216, v217\n G1Q 2, 3, 1, 3, 0\n " \
  "v_cvt_pk_bf16_f32 v217, v218, v219\n v_cvt_pk_bf16_f32 v218, v220, v221\n G1Q 2, 0, 2, 0, 0\n v_cvt_pk_bf16_f32 v219, v222, v223\n " \
  "v_fma_f32 v240, v96, v138, v240\n G1Q 2, 0, 2, 1, 0\n ds_read_b128 v[92:95], v54 offset:512\n v_fma_f32 v241, v97, v138, v241\n " \
  "v_fma_f32 v242, v98, v138, v242\n G1Q 2, 0, 2, 2, 0\n ds_read_b128 v[132:135], v54 offset:64\n v_fma_f32 v243, v99, v138, v243\n " \
  "v_fma_f32 v244, v100, v138, v244\n G1Q 2, 0, 2, 3, 0\n v_fma_f32 v245, v101, v138, v245\n v_fma_f32 v246, v102, v138, v246\n G1Q 2, 1, 3, 0, 0\n " \
  "v_fma_f32 v247, v103, v138, v247\n v_cvt_pk_bf16_f32 v240, v240, v241\n G1Q 2, 1, 3, 1, 0\n v_cvt_pk_bf16_f32 v241, v242, v243\n " \
  "v_cvt_pk_bf16_f32 v242, v244, v245\n G1Q 2, 1, 3, 2, 0\n v_cvt_pk_bf16_f32 v243, v246, v247\n G1Q 2, 1, 3, 3, 0\n s_waitcnt lgkmcnt(6)\n " \
  "v_mfma_f32_16x16x32_bf16 v[172:175], v[36:39], v[168:171], 0\n ds_read_b128 v[112:115], v54 offset:576\n ds_read_b128 v[116:119], v54 offset:128\n " \
  "v_mfma_f32_16x16x32_bf16 v[80:83], v[40:43], v[168:171], 0\n ds_read_b128 v[152:155], v54 offset:640\n ds_read_b128 v[156:159], v54 offset:192\n " \
  "v_mfma_f32_16x16x32_bf16 v[196:199], v[36:39], v[192:195], 0\n ds_read_b128 v[120:123], v54 offset:704\n v_mul_f32 v44, 0x3fb8aa3b, v141\n " \
  "v_mfma_f32_16x16x32_bf16 v[84:87], v[40:43], v[192:195], 0\n v_mul_f32 v46, 0x3fb8aa3b, v143\n v_mul_f32 v104, 0x3fb8aa3b, v137\n " \
  "v_mfma_f32_16x16x32_bf16 v[220:223], v[36:39], v[216:219], 0\n v_mul_f32 v106, 0x3fb8aa3b, v139\n " \
  "v_mfma_f32_16x16x32_bf16 v[124:127], v[40:43], v[216:219], 0\n v_mfma_f32_16x16x32_bf16 v[244:247], v[36:39], v[240:243], 0\n " \
  "v_mfma_f32_16x16x32_bf16 v[108:111], v[40:43], v[240:243], 0\n v_max3_f32 v168, v172, v173, v174\n v_max3_f32 v169, -v172, -v173, -v174\n " \
  "v_max3_f32 v168, v168, v175, v80\n v_max3_f32 v169, v169, -v175, -v80\n v_max3_f32 v168, v168, v81, v82\n v_max3_f32 v169, v169, -v81, -v82\n " \
  "v_max_f32_e64 v168, v168, v83\n v_max_f32_e64 v169, v169, -v83\n v_max3_f32 v170, v196, v197, v198\n v_max3_f32 v171, -v196, -v197, -v198\n " \
  "v_max3_f32 v170, v170, v199, v84\n v_max3_f32 v171, v171, -v199, -v84\n v_max3_f32 v170, v170, v85, v86\n v_max3_f32 v171, v171, -v85, -v86\n " \
  "v_max_f32_e64 v170, v170, v87\n v_max_f32_e64 v171, v171, -v87\n s_waitcnt lgkmcnt(0)\n " \
  "v_pk_fma_f32 v[176:177], v[128:129], v[144:145], v[176:177] op_sel_hi:[1,0,1]\n " \
  "v_pk_fma_f32 v[178:179], v[130:131], v[144:145], v[178:179] op_sel_hi:[1,0,1]\n " \
  "v_pk_fma_f32 v[180:181], v[128:129], v[144:145], v[180:181] op_sel:[0,1,0]\n " \
  "v_pk_fma_f32 v[182:183], v[130:131], v[144:145], v[182:183] op_sel:[0,1,0]\n v_pk_mul_f32 v[176:177], v[176:177], v[92:93]\n " \
  "v_pk_mul_f32 v[178:179], v[178:179], v[94:95]\n v_pk_mul_f32 v[180:181], v[180:181], v[92:93]\n v_pk_mul_f32 v[182:183], v[182:183], v[94:95]\n " \
  "v_pk_fma_f32 v[200:201], v[132:133], v[144:145], v[200:201] op_sel_hi:[1,0,1]\n " \
  "v_pk_fma_f32 v[202:203], v[134:135], v[144:145], v[202:203] op_sel_hi:[1,0,1]\n " \
  "v_pk_fma_f32 v[204:205], v[132:133], v[144:145], v[204:205] op_sel:[0,1,0]\n " \
  "v_pk_fma_f32 v[206:207], v[134:135], v[144:145], v[206:207] op_sel:[0,1,0]\n v_pk_mul_f32 v[200:201], v[200:201], v[112:113]\n " \
  "v_pk_mul_f32 v[202:203], v[202:203], v[114:115]\n v_pk_mul_f32 v[204:205], v[204:205], v[112:113]\n v_pk_mul_f32 v[206:207], v[206:207], v[114:115]\n " \
  "v_max3_f32 v192, v220, v221, v222\n v_max3_f32 v193, -v220, -v221, -v222\n v_max3_f32 v192, v192, v223, v124\n v_max3_f32 v193, v193, -v223, -v124\n " \
  "v_max3_f32 v192, v192, v125, v126\n v_max3_f32 v193, v193, -v125, -v126\n v_max_f32_e64 v192, v192, v127\n v_max_f32_e64 v193, v193, -v127\n " \
  "v_max3_f32 v194, v244, v245, v246\n v_max3_f32 v195, -v244, -v245, -v246\n v_max3_f32 v194, v194, v247, v108\n v_max3_f32 v195, v195, -v247, -v108\n " \
  "v_max3_f32 v194, v194, v109, v110\n v_max3_f32 v195, v195, -v109, -v110\n v_max_f32_e64 v194, v194, v111\n v_max_f32_e64 v195, v195, -v111\n " \
  "v_permlane16_swap_b32 v168, v169\n v_permlane16_swap_b32 v170, v171\n v_permlane16_swap_b32 v192, v193\n v_permlane16_swap_b32 v194, v195\n " \
  "v_pk_fma_f32 v[224:225], v[116:117], v[144:145], v[224:225] op_sel_hi:[1,0,1]\n " \
  "v_pk_fma_f32 v[226:227], v[118:119], v[144:145], v[226:227] op_sel_hi:[1,0,1]\n v_max_f32 v168, v168, v169\n v_max_f32 v170, v170, v171\n " \
  "v_max_f32 v192, v192, v193\n v_max_f32 v194, v194, v195\n v_pk_fma_f32 v[228:229], v[116:117], v[144:145], v[228:229] op_sel:[0,1,0]\n " \
  "v_pk_fma_f32 v[230:231], v[118:119], v[144:145], v[230:231] op_sel:[0,1,0]\n v_permlane32_swap_b32 v168, v170\n v_permlane32_swap_b32 v192, v194\n " \
  "v_pk_mul_f32 v[224:225], v[224:225], v[152:153]\n v_pk_mul_f32 v[226:227], v[226:227], v[154:155]\n v_max_f32 v168, v168, v170\n " \
  "v_max_f32 v192, v192, v194\n v_pk_mul_f32 v[228:229], v[228:229], v[152:153]\n v_pk_mul_f32 v[230:231], v[230:231], v[154:155]\n " \
  "v_mov_b32 v170, v168\n v_mov_b32 v194, v192\n v_pk_fma_f32 v[248:249], v[156:157], v[144:145], v[248:249] op_sel_hi:[1,0,1]\n " \
  "v_pk_fma_f32 v[250:251], v[158:159], v[144:145], v[250:251] op_sel_hi:[1,0,1]\n v_permlane32_swap_b32 v168, v170\n v_permlane32_swap_b32 v192, v194\n " \
  "v_pk_fma_f32 v[252:253], v[156:157], v[144:145], v[252:253] op_sel:[0,1,0]\n " \
  "v_pk_fma_f32 v[254:255], v[158:159], v[144:145], v[254:255] op_sel:[0,1,0]\n v_mov_b32 v169, v168\n v_mov_b32 v171, v170\n v_mov_b32 v193, v192\n " \
  "v_mov_b32 v195, v194\n v_pk_mul_f32 v[248:249], v[248:249], v[120:121]\n v_pk_mul_f32 v[250:251], v[250:251], v[122:123]\n " \
  "v_permlane16_swap_b32 v168, v169\n v_permlane16_swap_b32 v170, v171\n v_permlane16_swap_b32 v192, v193\n v_permlane16_swap_b32 v194, v195\n " \
  "v_pk_mul_f32 v[252:253], v[252:253], v[120:121]\n v_pk_mul_f32 v[254:255], v[254:255], v[122:123]\n v_cvt_pk_bf16_f32 v176, v176, v177\n " \
  "v_cvt_pk_bf16_f32 v177, v178, v179\n v_cvt_pk_bf16_f32 v178, v200, v201\n v_cvt_pk_bf16_f32 v179, v202, v203\n v_cvt_pk_bf16_f32 v180, v180, v181\n " \
  "v_cvt_pk_bf16_f32 v181, v182, v183\n v_cvt_pk_bf16_f32 v182, v204, v205\n v_cvt_pk_bf16_f32 v183, v206, v207\n v_cvt_pk_bf16_f32 v224, v224, v225\n " \
  "v_cvt_pk_bf16_f32 v225, v226, v227\n v_cvt_pk_bf16_f32 v226, v248, v249\n v_cvt_pk_bf16_f32 v227, v250, v251\n v_cvt_pk_bf16_f32 v228, v228, v229\n " \
  "v_cvt_pk_bf16_f32 v229, v230, v231\n v_cvt_pk_bf16_f32 v230, v252, v253\n v_cvt_pk_bf16_f32 v231, v254, v255\n v_mul_f32_e64 v45, v44, -v168\n " \
  "v_mul_f32_e64 v146, v44, -v169\n v_mul_f32_e64 v47, v46, -v170\n v_mul_f32_e64 v147, v46, -v171\n v_mul_f32_e64 v105, v104, -v192\n " \
  "v_mul_f32_e64 v144, v104, -v193\n v_mul_f32_e64 v107, v106, -v194\n v_mul_f32_e64 v145, v106, -v195\n " \
  "v_pk_fma_f32 v[200:201], v[172:173], v[44:45], v[146:147] op_sel_hi:[1,0,0] neg_lo:[1,0,0] neg_hi:[1,0,0]\n " \
  "v_pk_fma_f32 v[202:203], v[174:175], v[44:45], v[146:147] op_sel_hi:[1,0,0] neg_lo:[1,0,0] neg_hi:[1,0,0]\n " \
  "v_pk_fma_f32 v[204:205], v[80:81], v[44:45], v[146:147] op_sel_hi:[1,0,0] neg_lo:[1,0,0] neg_hi:[1,0,0]\n " \
  "v_pk_fma_f32 v[206:207], v[82:83], v[44:45], v[146:147] op_sel_hi:[1,0,0] neg_lo:[1,0,0] neg_hi:[1,0,0]\n " \
  "v_pk_fma_f32 v[172:173], v[172:173], v[44:45], v[44:45] op_sel:[0,0,1] op_sel_hi:[1,0,1]\n " \
  "v_pk_fma_f32 v[174:175], v[174:175], v[44:45], v[44:45] op_sel:[0,0,1] op_sel_hi:[1,0,1]\n " \
  "v_pk_fma_f32 v[80:81], v[80:81], v[44:45], v[44:45] op_sel:[0,0,1] op_sel_hi:[1,0,1]\n " \
  "v_pk_fma_f32 v[82:83], v[82:83], v[44:45], v[44:45] op_sel:[0,0,1] op_sel_hi:[1,0,1]\n ds_write_b128 v55, v[176:179] offset:0\n " \
  "v_exp_f32 v200, v200\n v_exp_f32 v201, v201\n v_exp_f32 v202, v202\n v_exp_f32 v203, v203\n v_exp_f32 v204, v204\n v_exp_f32 v205, v205\n " \
  "v_exp_f32 v206, v206\n v_exp_f32 v207, v207\n v_exp_f32 v172, v172\n v_exp_f32 v173, v173\n v_exp_f32 v174, v174\n v_exp_f32 v175, v175\n " \
  "v_exp_f32 v80, v80\n v_exp_f32 v81, v81\n v_exp_f32 v82, v82\n v_exp_f32 v83, v83\n " \
  "v_pk_fma_f32 v[248:249], v[196:197], v[46:47], v[146:147] op_sel:[0,0,1] op_sel_hi:[1,0,1] neg_lo:[1,0,0] neg_hi:[1,0,0]\n " \
  "v_pk_fma_f32 v[250:251], v[198:199], v[46:47], v[146:147] op_sel:[0,0,1] op_sel_hi:[1,0,1] neg_lo:[1,0,0] neg_hi:[1,0,0]\n " \
  "v_pk_fma_f32 v[252:253], v[84:85], v[46:47], v[146:147] op_sel:[0,0,1] op_sel_hi:[1,0,1] neg_lo:[1,0,0] neg_hi:[1,0,0]\n " \
  "v_pk_fma_f32 v[254:255], v[86:87], v[46:47], v[146:147] op_sel:[0,0,1] op_sel_hi:[1,0,1] neg_lo:[1,0,0] neg_hi:[1,0,0]\n " \
  "v_pk_fma_f32 v[196:197], v[196:197], v[46:47], v[46:47] op_sel:[0,0,1] op_sel_hi:[1,0,1]\n " \
  "v_pk_fma_f32 v[198:199], v[198:199], v[46:47], v[46:47] op_sel:[0,0,1] op_sel_hi:[1,0,1]\n " \
  "v_pk_fma_f32 v[84:85], v[84:85], v[46:47], v[46:47] op_sel:[0,0,1] op_sel_hi:[1,0,1]\n " \
  "v_pk_fma_f32 v[86:87], v[86:87], v[46:47], v[46:47] op_sel:[0,0,1] op_sel_hi:[1,0,1]\n ds_write_b128 v55, v[180:183] offset:1024\n " \
  "v_exp_f32 v248, v248\n v_exp_f32 v249, v249\n v_exp_f32 v250, v250\n v_exp_f32 v251, v251\n v_exp_f32 v252, v252\n v_exp_f32 v253, v253\n " \
  "v_exp_f32 v254, v254\n v_exp_f32 v255, v255\n v_exp_f32 v196, v196\n v_exp_f32 v197, v197\n v_exp_f32 v198, v198\n v_exp_f32 v199, v199\n " \
  "v_exp_f32 v84, v84\n v_exp_f32 v85, v85\n v_exp_f32 v86, v86\n v_exp_f32 v87, v87\n " \
  "v_pk_fma_f32 v[128:129], v[220:221], v[104:105], v[144:145] op_sel_hi:[1,0,0] neg_lo:[1,0,0] neg_hi:[1,0,0]\n " \
  "v_pk_fma_f32 v[130:131], v[222:223], v[104:105], v[144:145] op_sel_hi:[1,0,0] neg_lo:[1,0,0] neg_hi:[1,0,0]\n " \
  "v_pk_fma_f32 v[132:133], v[124:125], v[104:105], v[144:145] op_sel_hi:[1,0,0] neg_lo:[1,0,0] neg_hi:[1,0,0]\n " \
  "v_pk_fma_f32 v[134:135], v[126:127], v[104:105], v[144:145] op_sel_hi:[1,0,0] neg_lo:[1,0,0] neg_hi:[1,0,0]\n " \
  "v_pk_fma_f32 v[220:221], v[220:221], v[104:105], v[104:105] op_sel:[0,0,1] op_sel_hi:[1,0,1]\n " \
  "v_pk_fma_f32 v[222:223], v[222:223], v[104:105], v[104:105] op_sel:[0,0,1] op_sel_hi:[1,0,1]\n " \
  "v_pk_fma_f32 v[124:125], v[124:125], v[104:105], v[104:105] op_sel:[0,0,1] op_sel_hi:[1,0,1]\n " \
  "v_pk_fma_f32 v[126:127], v[126:127], v[104:105], v[104:105] op_sel:[0,0,1] op_sel_hi:[1,0,1]\n ds_write_b128 v55, v[224:227] offset:8192\n " \
  "v_pk_add_f32 v[216:217], v[172:173], v[174:175]\n v_pk_add_f32 v[218:219], v[80:81], v[82:83]\n v_pk_add_f32 v[216:217], v[216:217], v[218:219]\n " \
  "v_add_f32 v168, v216, v217\n v_pk_add_f32 v[216:217], v[200:201], v[202:203]\n v_pk_add_f32 v[218:219], v[204:205], v[206:207]\n " \
  "v_pk_add_f32 v[216:217], v[216:217], v[218:219]\n v_add_f32 v169, v216, v217\n v_pk_add_f32 v[216:217], v[196:197], v[198:199]\n " \
  "v_pk_add_f32 v[218:219], v[84:85], v[86:87]\n v_pk_add_f32 v[216:217], v[216:217], v[218:219]\n v_add_f32 v170, v216, v217\n " \
  "v_pk_add_f32 v[216:217], v[248:249], v[250:251]\n v_pk_add_f32 v[218:219], v[252:253], v[254:255]\n v_pk_add_f32 v[216:217], v[216:217], v[218:219]\n " \
  "v_add_f32 v171, v216, v217\n v_exp_f32 v128, v128\n v_exp_f32 v129, v129\n v_permlane16_swap_b32 v168, v169\n v_permlane16_swap_b32 v170, v171\n " \
  "v_exp_f32 v130, v130\n v_exp_f32 v131, v131\n v_add_f32 v168, v168, v169\n v_add_f32 v170, v170, v171\n v_exp_f32 v132, v132\n v_exp_f32 v133, v133\n " \
  "v_permlane32_swap_b32 v168, v170\n v_exp_f32 v134, v134\n v_exp_f32 v135, v135\n v_add_f32 v168, v168, v170\n v_exp_f32 v220, v220\n " \
  "v_exp_f32 v221, v221\n v_rcp_f32 v168, v168\n v_exp_f32 v222, v222\n v_exp_f32 v223, v223\n v_mov_b32 v170, v168\n v_exp_f32 v124, v124\n " \
  "v_exp_f32 v125, v125\n v_permlane32_swap_b32 v168, v170\n v_exp_f32 v126, v126\n v_exp_f32 v127, v127\n v_mov_b32 v169, v168\n v_mov_b32 v171, v170\n " \
  "v_pk_fma_f32 v[112:113], v[244:245], v[106:107], v[144:145] op_sel:[0,0,1] op_sel_hi:[1,0,1] neg_lo:[1,0,0] neg_hi:[1,0,0]\n " \
  "v_pk_fma_f32 v[114:115], v[246:247], v[106:107], v[144:145] op_sel:[0,0,1] op_sel_hi:[1,0,1] neg_lo:[1,0,0] neg_hi:[1,0,0]\n " \
  "v_permlane16_swap_b32 v168, v169\n v_permlane16_swap_b32 v170, v171\n " \
  "v_pk_fma_f32 v[116:117], v[108:109], v[106:107], v[144:145] op_sel:[0,0,1] op_sel_hi:[1,0,1] neg_lo:[1,0,0] neg_hi:[1,0,0]\n " \
  "v_pk_fma_f32 v[118:119], v[110:111], v[106:107], v[144:145] op_sel:[0,0,1] op_sel_hi:[1,0,1] neg_lo:[1,0,0] neg_hi:[1,0,0]\n " \
  "v_pk_fma_f32 v[244:245], v[244:245], v[106:107], v[106:107] op_sel:[0,0,1] op_sel_hi:[1,0,1]\n " \
  "v_pk_fma_f32 v[246:247], v[246:247], v[106:107], v[106:107] op_sel:[0,0,1] op_sel_hi:[1,0,1]\n " \
  "v_pk_fma_f32 v[108:109], v[108:109], v[106:107], v[106:107] op_sel:[0,0,1] op_sel_hi:[1,0,1]\n " \
  "v_pk_fma_f32 v[110:111], v[110:111], v[106:107], v[106:107] op_sel:[0,0,1] op_sel_hi:[1,0,1]\n ds_write_b128 v55, v[228:231] offset:9216\n " \
  "v_exp_f32 v112, v112\n v_pk_mul_f32 v[172:173], v[172:173], v[168:169] op_sel_hi:[1,0]\n v_exp_f32 v113, v113\n " \
  "v_pk_mul_f32 v[174:175], v[174:175], v[168:169] op_sel_hi:[1,0]\n v_exp_f32 v114, v114\n v_cvt_pk_bf16_f32 v172, v172, v173\n v_exp_f32 v115, v115\n " \
  "v_cvt_pk_bf16_f32 v173, v174, v175\n v_exp_f32 v116, v116\n ds_write_b64 v58, v[172:173] offset:0\n v_exp_f32 v117, v117\n " \
  "v_pk_mul_f32 v[80:81], v[80:81], v[168:169] op_sel_hi:[1,0]\n v_exp_f32 v118, v118\n v_pk_mul_f32 v[82:83], v[82:83], v[168:169] op_sel_hi:[1,0]\n " \
  "v_exp_f32 v119, v119\n v_cvt_pk_bf16_f32 v80, v80, v81\n v_exp_f32 v244, v244\n v_cvt_pk_bf16_f32 v81, v82, v83\n v_exp_f32 v245, v245\n " \
  "ds_write_b64 v58, v[80:81] offset:32\n v_exp_f32 v246, v246\n v_pk_mul_f32 v[200:201], v[200:201], v[168:169] op_sel:[0,1]\n v_exp_f32 v247, v247\n " \
  "v_pk_mul_f32 v[202:203], v[202:203], v[168:169] op_sel:[0,1]\n v_exp_f32 v108, v108\n v_cvt_pk_bf16_f32 v200, v200, v201\n v_exp_f32 v109, v109\n " \
  "v_cvt_pk_bf16_f32 v201, v202, v203\n v_exp_f32 v110, v110\n ds_write_b64 v58, v[200:201] offset:64\n v_exp_f32 v111, v111\n " \
  "v_pk_mul_f32 v[204:205], v[204:205], v[168:169] op_sel:[0,1]\n v_pk_mul_f32 v[206:207], v[206:207], v[168:169] op_sel:[0,1]\n " \
  "v_cvt_pk_bf16_f32 v204, v204, v205\n v_cvt_pk_bf16_f32 v205, v206, v207\n ds_write_b64 v58, v[204:205] offset:96\n " \
  "v_pk_add_f32 v[216:217], v[220:221], v[222:223]\n v_pk_add_f32 v[218:219], v[124:125], v[126:127]\n v_pk_add_f32 v[216:217], v[216:217], v[218:219]\n " \
  "v_add_f32 v192, v216, v217\n v_pk_add_f32 v[216:217], v[128:129], v[130:131]\n v_pk_add_f32 v[218:219], v[132:133], v[134:135]\n " \
  "v_pk_add_f32 v[216:217], v[216:217], v[218:219]\n v_add_f32 v193, v216, v217\n v_pk_add_f32 v[216:217], v[244:245], v[246:247]\n " \
  "v_pk_add_f32 v[218:219], v[108:109], v[110:111]\n v_pk_add_f32 v[216:217], v[216:217], v[218:219]\n v_add_f32 v194, v216, v217\n " \
  "v_pk_add_f32 v[216:217], v[112:113], v[114:115]\n v_pk_add_f32 v[218:219], v[116:117], v[118:119]\n v_pk_add_f32 v[216:217], v[216:217], v[218:219]\n " \
  "v_add_f32 v195, v216, v217\n v_pk_mul_f32 v[196:197], v[196:197], v[170:171] op_sel_hi:[1,0]\n " \
  "v_pk_mul_f32 v[198:199], v[198:199], v[170:171] op_sel_hi:[1,0]\n v_permlane16_swap_b32 v192, v193\n v_permlane16_swap_b32 v194, v195\n " \
  "v_cvt_pk_bf16_f32 v196, v196, v197\n v_cvt_pk_bf16_f32 v197, v198, v199\n v_add_f32 v192, v192, v193\n v_add_f32 v194, v194, v195\n " \
  "ds_write_b64 v58, v[196:197] offset:2304\n v_pk_mul_f32 v[84:85], v[84:85], v[170:171] op_sel_hi:[1,0]\n v_permlane32_swap_b32 v192, v194\n " \
  "v_pk_mul_f32 v[86:87], v[86:87], v[170:171] op_sel_hi:[1,0]\n v_cvt_pk_bf16_f32 v84, v84, v85\n v_add_f32 v192, v192, v194\n " \
  "v_cvt_pk_bf16_f32 v85, v86, v87\n ds_write_b64 v58, v[84:85] offset:2336\n v_rcp_f32 v192, v192\n " \
  "v_pk_mul_f32 v[248:249], v[248:249], v[170:171] op_sel:[0,1]\n v_pk_mul_f32 v[250:251], v[250:251], v[170:171] op_sel:[0,1]\n v_mov_b32 v194, v192\n " \
  "v_cvt_pk_bf16_f32 v248, v248, v249\n v_cvt_pk_bf16_f32 v249, v250, v251\n v_permlane32_swap_b32 v192, v194\n " \
  "ds_write_b64 v58, v[248:249] offset:2368\n v_pk_mul_f32 v[252:253], v[252:253], v[170:171] op_sel:[0,1]\n v_mov_b32 v193, v192\n " \
  "v_mov_b32 v195, v194\n v_pk_mul_f32 v[254:255], v[254:255], v[170:171] op_sel:[0,1]\n v_cvt_pk_bf16_f32 v252, v252, v253\n " \
  "v_permlane16_swap_b32 v192, v193\n v_permlane16_swap_b32 v194, v195\n v_cvt_pk_bf16_f32 v253, v254, v255\n ds_write_b64 v58, v[252:253] offset:2400\n " \
  "v_pk_mul_f32 v[220:221], v[220:221], v[192:193] op_sel_hi:[1,0]\n v_pk_mul_f32 v[222:223], v[222:223], v[192:193] op_sel_hi:[1,0]\n " \
  "v_cvt_pk_bf16_f32 v220, v220, v221\n v_cvt_pk_bf16_f32 v221, v222, v223\n ds_write_b64 v58, v[220:221] offset:18432\n " \
  "v_pk_mul_f32 v[124:125], v[124:125], v[192:193] op_sel_hi:[1,0]\n v_pk_mul_f32 v[126:127], v[126:127], v[192:193] op_sel_hi:[1,0]\n " \
  "v_cvt_pk_bf16_f32 v124, v124, v125\n v_cvt_pk_bf16_f32 v125, v126, v127\n ds_write_b64 v58, v[124:125] offset:18464\n " \
  "v_pk_mul_f32 v[128:129], v[128:129], v[192:193] op_sel:[0,1]\n v_pk_mul_f32 v[130:131], v[130:131], v[192:193] op_sel:[0,1]\n " \
  "v_cvt_pk_bf16_f32 v128, v128, v129\n v_cvt_pk_bf16_f32 v129, v130, v131\n ds_write_b64 v58, v[128:129] offset:18496\n " \
  "v_pk_mul_f32 v[132:133], v[132:133], v[192:193] op_sel:[0,1]\n v_pk_mul_f32 v[134:135], v[134:135], v[192:193] op_sel:[0,1]\n " \
  "v_cvt_pk_bf16_f32 v132, v132, v133\n v_cvt_pk_bf16_f32 v133, v134, v135\n ds_write_b64 v58, v[132:133] offset:18528\n " \
  "v_pk_mul_f32 v[244:245], v[244:245], v[194:195] op_sel_hi:[1,0]\n v_pk_mul_f32 v[246:247], v[246:247], v[194:195] op_sel_hi:[1,0]\n " \
  "v_cvt_pk_bf16_f32 v244, v244, v245\n v_cvt_pk_bf16_f32 v245, v246, v247\n ds_write_b64 v58, v[244:245] offset:20736\n " \
  "v_pk_mul_f32 v[108:109], v[108:109], v[194:195] op_sel_hi:[1,0]\n v_pk_mul_f32 v[110:111], v[110:111], v[194:195] op_sel_hi:[1,0]\n " \
  "v_cvt_pk_bf16_f32 v108, v108, v109\n v_cvt_pk_bf16_f32 v109, v110, v111\n ds_write_b64 v58, v[108:109] offset:20768\n " \
  "v_pk_mul_f32 v[112:113], v[112:113], v[194:195] op_sel:[0,1]\n v_pk_mul_f32 v[114:115], v[114:115], v[194:195] op_sel:[0,1]\n " \
  "v_cvt_pk_bf16_f32 v112, v112, v113\n v_cvt_pk_bf16_f32 v113, v114, v115\n ds_write_b64 v58, v[112:113] offset:20800\n " \
  "v_pk_mul_f32 v[116:117], v[116:117], v[194:195] op_sel:[0,1]\n v_pk_mul_f32 v[118:119], v[118:119], v[194:195] op_sel:[0,1]\n " \
  "v_cvt_pk_bf16_f32 v116, v116, v117\n v_cvt_pk_bf16_f32 v117, v118, v119\n ds_write_b64 v58, v[116:117] offset:20832\n "
#define G1_OUTPUT \
  "s_mov_b32 m0, 0x40000\n s_branch .Lg1p%=\n .Lg1q%=:\n s_sleep 1\n s_sub_u32 m0, m0, 1\n s_cbranch_scc1 .Lg1r%=\n .Lg1p%=:\n " \
  "buffer_load_dwordx4 v[216:219], %[fz], %[rs], 0 offen sc1\n buffer_load_dwordx4 v[220:223], %[fz], %[rs], 0 offen offset:64 sc1\n " \
  "buffer_load_dwordx4 v[200:203], %[fo], %[rs], 0 offen offset:0 sc1\n buffer_load_dwordx4 v[204:207], %[fo], %[rs], 0 offen offset:2048 sc1\n " \
  "buffer_load_dwordx4 v[208:211], %[fo], %[rs], 0 offen offset:1024 sc1\n buffer_load_dwordx4 v[212:215], %[fo], %[rs], 0 offen offset:3072 sc1\n " \
  "s_waitcnt vmcnt(5)\n v_max_u32 v152, v216, v217\n v_max3_u32 v152, v218, v219, v152\n s_waitcnt vmcnt(4)\n v_max3_u32 v152, v220, v221, v152\n " \
  "v_max3_u32 v152, v222, v223, v152\n s_waitcnt vmcnt(3)\n v_max3_u32 v152, v200, v201, v152\n v_max3_u32 v152, v202, v203, v152\n s_waitcnt vmcnt(2)\n " \
  "v_max3_u32 v152, v204, v205, v152\n v_max3_u32 v152, v206, v207, v152\n s_waitcnt vmcnt(1)\n v_max3_u32 v152, v208, v209, v152\n " \
  "v_max3_u32 v152, v210, v211, v152\n s_waitcnt vmcnt(0)\n v_max3_u32 v152, v212, v213, v152\n v_max3_u32 v152, v214, v215, v152\n " \
  "v_cmp_eq_u32 vcc, -1, v152\n s_cbranch_vccnz .Lg1q%=\n .Lg1r%=:\n v_mfma_f32_16x16x32_bf16 v[160:163], v[216:219], v[224:227], 0\n " \
  "v_mfma_f32_16x16x32_bf16 v[160:163], v[220:223], v[228:231], v[160:163]\n v_mfma_f32_16x16x32_bf16 v[152:155], v[200:203], v[224:227], 0\n " \
  "v_mfma_f32_16x16x32_bf16 v[152:155], v[204:207], v[228:231], v[152:155]\n v_mfma_f32_16x16x32_bf16 v[156:159], v[208:211], v[224:227], 0\n " \
  "v_mfma_f32_16x16x32_bf16 v[156:159], v[212:215], v[228:231], v[156:159]\n v_mfma_f32_16x16x32_bf16 v[172:175], v[216:219], v[232:235], 0\n " \
  "v_mfma_f32_16x16x32_bf16 v[172:175], v[220:223], v[236:239], v[172:175]\n v_mfma_f32_16x16x32_bf16 v[164:167], v[200:203], v[232:235], 0\n " \
  "v_mfma_f32_16x16x32_bf16 v[164:167], v[204:207], v[236:239], v[164:167]\n v_mfma_f32_16x16x32_bf16 v[168:171], v[208:211], v[232:235], 0\n " \
  "v_add_f32 v160, 0x358637bd, v160\n v_rcp_f32 v160, v160\n v_mfma_f32_16x16x32_bf16 v[168:171], v[212:215], v[236:239], v[168:171]\n " \
  "v_mul_f32 v152, v160, v152\n v_mfma_f32_16x16x32_bf16 v[184:187], v[216:219], v[240:243], 0\n v_mul_f32 v153, v160, v153\n " \
  "v_cvt_pk_bf16_f32 v152, v152, v153\n v_mul_f32 v154, v160, v154\n v_mfma_f32_16x16x32_bf16 v[184:187], v[220:223], v[244:247], v[184:187]\n " \
  "v_mul_f32 v155, v160, v155\n v_cvt_pk_bf16_f32 v153, v154, v155\n v_mul_f32 v156, v160, v156\n " \
  "v_mfma_f32_16x16x32_bf16 v[176:179], v[200:203], v[240:243], 0\n v_mul_f32 v157, v160, v157\n v_cvt_pk_bf16_f32 v154, v156, v157\n " \
  "v_mul_f32 v158, v160, v158\n v_mfma_f32_16x16x32_bf16 v[176:179], v[204:207], v[244:247], v[176:179]\n v_mul_f32 v159, v160, v159\n " \
  "v_cvt_pk_bf16_f32 v155, v158, v159\n buffer_store_dwordx4 v[152:155], %[go], %[rs], 0 offen offset:0 sc1\n " \
  "v_mfma_f32_16x16x32_bf16 v[180:183], v[208:211], v[240:243], 0\n ds_write_b128 %[lo], v[152:155] offset:0\n v_add_f32 v172, 0x358637bd, v172\n " \
  "v_mfma_f32_16x16x32_bf16 v[180:183], v[212:215], v[244:247], v[180:183]\n v_rcp_f32 v172, v172\n " \
  "v_mfma_f32_16x16x32_bf16 v[196:199], v[216:219], v[248:251], 0\n v_mul_f32 v164, v172, v164\n v_mul_f32 v165, v172, v165\n " \
  "v_cvt_pk_bf16_f32 v164, v164, v165\n v_mfma_f32_16x16x32_bf16 v[196:199], v[220:223], v[252:255], v[196:199]\n v_mul_f32 v166, v172, v166\n " \
  "v_mul_f32 v167, v172, v167\n v_cvt_pk_bf16_f32 v165, v166, v167\n v_mfma_f32_16x16x32_bf16 v[188:191], v[200:203], v[248:251], 0\n " \
  "v_mul_f32 v168, v172, v168\n v_mul_f32 v169, v172, v169\n v_cvt_pk_bf16_f32 v166, v168, v169\n " \
  "v_mfma_f32_16x16x32_bf16 v[188:191], v[204:207], v[252:255], v[188:191]\n v_mul_f32 v170, v172, v170\n v_mul_f32 v171, v172, v171\n " \
  "v_cvt_pk_bf16_f32 v167, v170, v171\n v_mfma_f32_16x16x32_bf16 v[192:195], v[208:211], v[248:251], 0\n " \
  "buffer_store_dwordx4 v[164:167], %[go], %[rs], 0 offen offset:1024 sc1\n ds_write_b128 %[lo], v[164:167] offset:1024\n " \
  "v_mfma_f32_16x16x32_bf16 v[192:195], v[212:215], v[252:255], v[192:195]\n v_add_f32 v184, 0x358637bd, v184\n v_rcp_f32 v184, v184\n s_nop 0\n " \
  "v_pk_mul_f32 v[176:177], v[176:177], v[184:185] op_sel_hi:[1,0]\n v_pk_mul_f32 v[178:179], v[178:179], v[184:185] op_sel_hi:[1,0]\n " \
  "v_pk_mul_f32 v[180:181], v[180:181], v[184:185] op_sel_hi:[1,0]\n v_pk_mul_f32 v[182:183], v[182:183], v[184:185] op_sel_hi:[1,0]\n " \
  "v_cvt_pk_bf16_f32 v176, v176, v177\n v_cvt_pk_bf16_f32 v177, v178, v179\n v_cvt_pk_bf16_f32 v178, v180, v181\n v_cvt_pk_bf16_f32 v179, v182, v183\n " \
  "buffer_store_dwordx4 v[176:179], %[go], %[rs], 0 offen offset:2048 sc1\n ds_write_b128 %[lo], v[176:179] offset:2048\n " \
  "v_add_f32 v196, 0x358637bd, v196\n v_rcp_f32 v196, v196\n s_nop 0\n v_pk_mul_f32 v[188:189], v[188:189], v[196:197] op_sel_hi:[1,0]\n " \
  "v_pk_mul_f32 v[190:191], v[190:191], v[196:197] op_sel_hi:[1,0]\n v_pk_mul_f32 v[192:193], v[192:193], v[196:197] op_sel_hi:[1,0]\n " \
  "v_pk_mul_f32 v[194:195], v[194:195], v[196:197] op_sel_hi:[1,0]\n v_cvt_pk_bf16_f32 v188, v188, v189\n v_cvt_pk_bf16_f32 v189, v190, v191\n " \
  "v_cvt_pk_bf16_f32 v190, v192, v193\n v_cvt_pk_bf16_f32 v191, v194, v195\n buffer_store_dwordx4 v[188:191], %[go], %[rs], 0 offen offset:3072 sc1\n " \
  "ds_write_b128 %[lo], v[188:191] offset:3072\n "
#define G1_PURGE \
  ".purgem G1M\n .purgem G1RA\n .purgem G1RB\n .purgem G1BK\n .purgem G1AK\n .purgem G1RDB\n .purgem G1RDA\n .purgem G1Q\n .purgem G1SG\n "
#define G1_CLOBBER \
  "v80", "v81", "v82", "v83", "v84", "v85", "v86", "v87", "v92", "v93", "v94", "v95", "v96", "v97", "v98", "v99", "v100", "v101", "v102", "v103", \
  "v104", "v105", "v106", "v107", "v108", "v109", "v110", "v111", \
  "v112", "v113", "v114", "v115", "v116", "v117", "v118", "v119", "v120", "v121", "v122", "v123", "v124", "v125", "v126", "v127", "v128", "v129", \
  "v130", "v131", "v132", "v133", "v134", "v135", "v144", "v145", "v146", "v147", "v152", "v153", "v154", "v155", \
  "v156", "v157", "v158", "v159"

template <int MODE>
__device__ __forceinline__ void g1_phase(char* smem, char* ws, u64& E, int w, f32x16& xq, Next& nx, int layer, const u16* __restrict__ x0, const u16* __restrict__ W,
                                         const u16* __restrict__ wo, const u16* __restrict__ wphi,
                                         const u16* __restrict__ wn1, const u16* __restrict__ wn2) {
  constexpr bool HH = (MODE == 1);
  constexpr int ABYTES = 128 * 128;
  constexpr int STAGE = ABYTES + 3 * 8192;
  constexpr int OFF_FX = 16384;
  constexpr int OFF_SP = 3 * STAGE;
  constexpr int OFF_RS = !HH ? 159744 : OFF_SP + 16384;
  constexpr int OFF_CS = OFF_RS + (HH ? 1152 : 1024);
  constexpr int OFF_WP = OFF_CS + 1536;
  constexpr int OFF_JK = LDS_TOTAL - 1024;
  constexpr int OFF_PK = 0;
  constexpr int OFF_VF = OFF_WP + 4096;
  constexpr int OFF_R2 = HH ? OFF_VF + 16384 : OFF_CS + 1024;
  constexpr int OFF_RD = OFF_CS + 768;
  constexpr int PSTR = 144;
  IDS
  W = uni(W); wphi = uni(wphi); wn1 = uni(wn1); wn2 = uni(wn2);
  const int kg = w >> 2, wm = (w >> 1) & 1, wn = w & 1;
  const bool first = !HH && layer == 0;
  f32x4 acc[2][2], qh[2][2], csq[2], st01, st23;
  bf16x8 wf[2];
  u64 kol;
  u32x4 pw[8];
  ST(layer * 24 + 0)
  const int R0 = wm * 64;
  u32x16 pa;
#define pcs pa[0]
#define prs pa[1]
#define opo pa[2]
#define ogo pa[3]
#define hof pa[7]
#define dm pa[8]
#define dm2 pa[9]
#define pcv pa[4]
#define pwf pa[5]
#define prv pa[6]
#define pvf pa[7]
#define ppk pa[10]
#define ppa pa[11]
#define pvr pa[12]
#define gkv pa[13]
#define gkz pa[14]
#define pwl pa[15]
#define pwm pa[4]
#define plb pa[10]
#define prd pa[11]
#define dm3 pa[12]
  f32x4 bias[3];
  const char* wos = wo_src(ws, wo, layer, hp);
  const unsigned tO = img(layer, hp) ? 8192u : 131072u;
  const unsigned sb = lds_addr(smem);
  auto prep = [&](auto ldr) {
    constexpr bool LDR = decltype(ldr)::value;
    if constexpr (!LDR) {
      pcs = sb + OFF_CS + wn * 128 + g * 16;
      prs = sb + OFF_RS + (R0 + fr) * 8;
      opo = (unsigned)(wn * 8192 + R0 * 64 + lane * 16);
      ogo = opo + WS_O + layer * (SEQ * DM * 2) + (unsigned)((rt * NH + 2 * hp) * 8192);
      opo += sb;
      pcv = sb + OFF_CS + (wn * 32 + 128 + fr) * 4;
      prv = sb + OFF_R2 + (R0 + 4 * g) * 4;
    } else {
      constexpr unsigned kvn = WS_KV + MODE * KVLB;
      const int t = tid & 255;
      dm = kvn + (unsigned)((rt * NH + 2 * hp) * KVPB) + t * 16;
      dm2 = t < 16 ? dm + 8192 : t >= 64 && t < 97 ? kvn + 16u * NH * KVPB + (unsigned)(hp * KVFB + rt * 528 + (t - 64) * 16) : 0x7fff0000u;
      if (!HH) dm3 = WS_HL + (unsigned)((256 + rt * 16 + hp) * 4096 + t * 16);
    }
    if constexpr (!HH) {
      prd = sb + OFF_RD + wn * 4;
      if constexpr (!LDR) {
        plb = sb + OFF_FX + wn * 2048 + lane * 16;
        hof = m0 + wm == 0 ? 0x7fff0000u : WS_HL + (unsigned)((layer * 128 + rt * 16 - 16 + wm * 16 + hp) * 4096 + wn * 2048 + lane * 16);
        const unsigned out = ~((0x5555u << 2 << fr) >> (4 * g));
#pragma unroll
        for (int i = 0; i < 8; ++i) {
          unsigned t;
          asm("v_bfe_i32 %0, %1, %2, 1" : "=v"(t) : "v"(out), "n"((i >> 2) * 16 + (i & 3)));
          bias[i >> 2][i & 3] = __uint_as_float(t & 0xff61b1e6u);
        }
        bias[2] = m0 + R0 == 0 ? f32x4{-3.0e38f, -3.0e38f, -3.0e38f, -3.0e38f} : bias[0];
        asm volatile("" : "+{v[36:39]}"(bias[0]), "+{v[40:43]}"(bias[1]), "+{v[44:47]}"(bias[2]));
      }
    } else {
      const int u = wm * 2 + kg;
      if constexpr (!LDR) {
        pwf = sb + OFF_WP + wn * 2048 + (4 * g + q4) * 64 + p4 * 8;
        pvf = sb + OFF_VF + w * 2048 + lane * 16;
        ppk = sb + OFF_PK + w * (32 * PSTR) + fr * PSTR + g * 8;
      }
      ppa = sb + OFF_PK + wn * (32 * PSTR) + (4 * g + q4) * PSTR + u * 32 + p4 * 8;
      pvr = sb + OFF_VF + wn * 2048 + lane * 16;
      const unsigned doff = WS_KV + (layer >> 1) * KVLB + (unsigned)((rt * NH + 2 * hp + wn) * KVPB);
      gkv = doff + u * 1024 + lane * 16;
      gkz = fr == 0 ? doff + 4096 + u * 32 + g * 8 : 0x7fff0000u;
    }
    asm volatile("" : "+{v[48:63]}"(pa));
  };

  if (kg == 1) {

    const int lw = w & 3;
    const int kB = 16 * lw + (lane >> 3);
    const int fB = (kB >> 1) & 1;
    const int cB0 = ((((lane & 7) >> 1) ^ fB) << 1) | (lane & 1), cB1 = ((((lane & 7) >> 1) ^ (fB | 2)) << 1) | (lane & 1);
    const bool im = img(layer, hp);

    unsigned vW0, vW1, vP, vX, vY;
    u64 ko2;
    auto ld_pw = [&](int i0) {
#pragma unroll
      for (int i = i0; i < i0 + 4; ++i) gld16(pw[i], vP, wn1 + i * 8 * 3072);
    };
    auto wo_tile = [&](int t) { glds2v(wos + knib(ko2, t) * tO, vW0, vW1, lds_addr(smem) + OFF_WO + t * 8192 + lw * 2048); };
    auto run = [&](auto fst) {
      constexpr bool FIRST = decltype(fst)::value;
      constexpr int N = 10;
      const unsigned vA = FIRST ? (unsigned)(((m0 + lw * 32 + (lane >> 3)) * DM + ((lane & 7) ^ (lane >> 3)) * 8) * 2)
                                : (unsigned)(rt * 262144 + lw * 4096 + lane * 16);
      unsigned imv = !FIRST && im ? ~0u : 0u;
      if (!FIRST) asm volatile("" : "+v"(imv));
      const unsigned vB0 = ((unsigned)(lw * 6144 + lane * 16) & imv) | ((unsigned)((kB * 3072 + hp * 64 + cB0 * 8) * 2) & ~imv);
      const unsigned vB1 = ((unsigned)(lw * 6144 + lane * 16) & imv) | (((unsigned)(((kB + 8) * 3072 + hp * 64 + cB1 * 8) * 2) - 1024u) & ~imv);
      const char* xa = FIRST ? (const char*)x0 : ws + WS_X + (layer - 1) * (SEQ * DM * 2);
      constexpr int KS = FIRST ? 128 : 16384;
      const u64 kord = FIRST ? korder0(rt) : nx.kord;
      auto ktile = [&](int ks) { return knib(kord, ks); };
      auto stageA = [&](int ks, int boff) {
        const int kt = ktile(ks);
        const unsigned sA = lds_addr(smem) + boff;
        if (FIRST) glds4v(xa + kt * KS, vA, vA + 15360u, vA + 2 * 15360u, vA + 3 * 15360u, sA + lw * 4096);
        else glds4c(xa + kt * KS, vA, sA + lw * 4096);
      };
      auto stageB = [&](const u16* Wp, int ks, int boff) {
        const int kt = ktile(ks);
        const unsigned sB = lds_addr(smem) + boff + ABYTES;
        const char* b = FIRST ? (const char*)(Wp + (size_t)kt * 64 * 3072) : nx.wq + kt * nx.tw;
        glds4v(b, vB0, vB1, vB0, vB1, sB + lw * 6144);
        glds2v(b + 4096, vB0, vB1, sB + lw * 6144 + 4096);
      };
      bf16x8 sel[3];
      unsigned cso[3], cst;
      if (FIRST) {
        WAITVN(N);
        BAR;
        stageA(2, 2 * STAGE); stageB(W, 2, 2 * STAGE);
        __builtin_amdgcn_sched_barrier(0);
        const int fl = (q4 >> 1) | ((g & 1) << 1), j = 3 * wm + (fr < 3 ? fr : 0);
#pragma unroll
        for (int i = 0; i < 3; ++i) {
          const unsigned o = fr == i ? 0x3F803F80u : 0u;
          sel[i] = __builtin_bit_cast(bf16x8, u32x4{o, o, o, o});
          cso[i] = (unsigned)(2 * STAGE + ABYTES + (g >> 1) * 6144 + (g & 1) * 1024 + q4 * 128 + p4 * 8 + (((wn * 2 + ((wm + i) & 1)) ^ fl) << 5) + ((3 * wm + i) >> 1) * 2048);
          asm volatile("" : "+v"(sel[i]), "+v"(cso[i]));
        }
        cst = fr < 3 ? WS_CPA + (unsigned)(((hp * 16 + rt) * 192 + (j >> 1) * 64 + wn * 32 + (j & 1) * 16 + 4 * g) * 4) : 0x7fff0000u;
        asm volatile("" : "+v"(cst));
      }
      u64 fall, fal2, fal3, fsv = E + (unsigned)(layer + 1);
      unsigned fao, fa2, vS, vC, dS, dC, dR;
      auto setup = [&]() {
        if (FIRST) { asm volatile("s_load_dwordx2 %0, %1, 0x0\n\ts_waitcnt lgkmcnt(0)" : "=s"(E) : "s"(ws) : "memory"); fall = E + 1u; asm volatile("" : "+v"(fall)); }
        if (FIRST) fa2 = (unsigned)((512 + (lane & 15) * 16 + hp) * FLS);
        vP = (unsigned)((rt * 64 + (lane >> 3)) * 6144 + lw * 2048 + ((hp + (1 << lw)) & 15) * 128 + (lane & 7) * 16);
        vS = FIRST ? (unsigned)(WS_G0 + (m0 + lw * 32 + (lane & 31)) * 16)
                   : WS_ST + (unsigned)((layer - 1) * (SEQ * 128) + rt * 16384 + g * 1024 + lw * 256 + fr * 16);
        vC = (FIRST ? WS_CPA : WS_CSP) + (unsigned)(hp * 12288 + g * 768 + lw * 192 + (fr < 12 ? fr : 0) * 16);
        if (!FIRST) vX = HH ? (unsigned)(hp * 4096 + lw * 1024 + lane * 16) : (unsigned)((rt * 16 + hp) * 1024 + lane * 16);
        vW0 = ((unsigned)(lw * 2048 + lane * 16) & imv) | ((unsigned)((kB * DM + hp * 64 + ((lane & 7) ^ ((lane >> 4) << 1)) * 8) * 2) & ~imv); vW1 = vW0 + ((8 * DM * 2 - 1024u) & ~imv);
        dS = OFF_RS + lw * 256 + (FIRST ? (lane & 31) * 8 : fr * 16 + (g & 1) * 8); dC = OFF_CS + lw * 192 + (fr < 12 ? fr : 0) * 16 + g * 4;
        ko2 = FIRST ? korder(bid & 3, hp) : nx.ko2;
        dR = OFF_R2 + lw * 128 + (FIRST ? (lane & 31) * 4 : fr * 8 + (g & 1) * 4);
        asm volatile("" : "+v"(dR));
        asm volatile("" : "+v"(vS), "+v"(vX), "+v"(vW0), "+v"(vW1), "+v"(dS));
        asm volatile("" : "+s"(ko2));
        if (FIRST || HH) asm volatile("" : "+v"(vC), "+v"(dC), "+v"(fa2));
        if (FIRST) asm volatile("" : "+v"(vP));
      };
      if (!FIRST) {
        fao = (unsigned)((256 + rt * 16 + (lane & 15)) * FLS);
        if (HH) { vY = (unsigned)(bid * 8192 + lw * 2048 + lane * 16); fa2 = (unsigned)((256 + (lane & 15) * 16 + ((hp - ((1 << (lane >> 4)) >> 1)) & 15)) * FLS); asm volatile("" : "+v"(vY), "+v"(fa2)); }
        asm volatile("" : "+v"(fsv), "+v"(fao));
      }
      u32x4 sv[4], cv[4];
      auto ld_c = [&](u32x4& d, unsigned v, int so) { asm volatile("buffer_load_dwordx4 %0, %1, %2, %3 offen sc1" : "=v"(d) : "v"(v), "s"(rsW), "s"(so) : "memory"); };
      float tm, tr;
      unsigned tc;
      auto fin = [&]() {
        if (FIRST) {
          const unsigned tag = (unsigned)E + 1u;
          asm volatile("" : "+v"(sv[0]));
          _Pragma("unroll 1") for (unsigned spins = 0; spins < (1u << 18); ++spins) {
            if (__all(sv[0][1] == tag && sv[0][3] == tag)) break;
            __builtin_amdgcn_s_sleep(1);
            asm volatile("buffer_load_dwordx4 %0, %1, %2, 0 offen sc1\n\ts_waitcnt vmcnt(0)" : "=v"(sv[0]) : "v"(vS), "s"(rsW) : "memory");
          }
          tm = __uint_as_float(sv[0][0]); tr = __uint_as_float(sv[0][2]);
        } else {
          asm volatile("" : "+v"(sv[0]), "+v"(sv[1]), "+v"(sv[2]), "+v"(sv[3]));
          const f32x4 t = sum4(sv);
          const unsigned u = xs4(t[0], t[2], t[1], t[3]);
          auto h = __builtin_amdgcn_permlane32_swap(u, u, false, false);
          const float mean = __uint_as_float(h[0]) * (1.0f / 1024.0f);
          const float var = fmaxf(__uint_as_float(h[1]) * (1.0f / 1024.0f) - mean * mean, 0.f);
          tm = -mean; tr = __builtin_amdgcn_rsqf(var + 1e-5f);
        }
      };
      auto cfin = [&]() {
        if (FIRST || HH) {
          asm volatile("" : "+v"(cv[0]), "+v"(cv[1]), "+v"(cv[2]), "+v"(cv[3]));
          const f32x4 t = sum4(cv);
          tc = xs4(t[0], t[1], t[2], t[3]);
        }
      };
      bf16x8 cf[6];
      f32x4 csum = {0, 0, 0, 0};
      if (FIRST) { vX = (unsigned)(bid * 8192 + lw * 2048 + lane * 16); asm volatile("" : "+v"(vX)); }
#pragma unroll
      for (int kt = 0; kt < 13; ++kt) {
        const int cur = (kt % 3) * STAGE;
        if (!FIRST && kt == 0) { WAITV(4); }
        else if (FIRST && kt == 4 && lw == 1) { WAITVN(N + 2); }
        else if (HH && (kt == 4 || kt == 6)) { WAITVN(N + 1); }
        else if (HH && kt == 5) { WAITVN(N + 2); }
        else if ((!FIRST && kt == 1 && lw == 3) || (!FIRST && kt == 9 && lw == 2) || ((FIRST || HH) && kt == 10 && lw == 3) || (FIRST && (kt == 2 || kt == 4 || kt == 5))) { WAITVN(N + 1); }
        else { WAITVN(N); }
        if (!FIRST && kt == 2 && lw == 3) {
          asm volatile("" : "+v"(fall));
          if (__any(fall != E + (unsigned)layer)) poll_flags(ws, E, 1, rt, hp, lane, layer, false);
        }
        if (!FIRST && kt == 10 && lw == 2) {
          asm volatile("" : "+v"(fal3));
          if (__any(fal3 != E + (unsigned)(layer + 1))) poll_flags(ws, E, 2, rt, hp, lane, layer + 1, false);
        }
        if (HH && kt == 11 && lw == 3) {
          asm volatile("" : "+v"(fal2));
          if (__any(fal2 != E + (unsigned)layer)) {
            _Pragma("unroll 1") for (int j = 0; j < 4; ++j) poll_flags(ws, E, 1, rt, (hp - ((1 << j) >> 1)) & 15, lane, layer, true);
          }
        }
        if (FIRST && kt == 11 && lw == 3) {
          asm volatile("" : "+v"(fal2));
          if (__any(fal2 != E + 1u)) poll_flags(ws, E, 2, rt, hp, lane, 1u, true);
        }
        BAR;
        if (FIRST && kt == 1) {
          asm volatile("" ::: "memory");
#pragma unroll
          for (int i = 0; i < 6; ++i) { const char* t = smem + cso[i >> 1] + (i & 1) * 12288; cf[i] = cat8(tr_read(t), tr_read(t + 512)); }
        }
        if (FIRST && kt >= 2 && kt < 6 && lw == kt - 2) setup();
        if (FIRST && kt >= 6 && kt < 10 && lw == kt - 6) prep(IC<1>{});
        stageA(kt + 3, cur);
        if (FIRST && kt == 1) {
          asm volatile("" : "+v"(sel[0]));
#pragma unroll
          for (int i = 0; i < 6; ++i) csum = MFMA(cf[i], sel[i >> 1], csum);
          asm volatile("" : "+v"(csum));
        }
        stageB(W, kt + 3, cur);
        if (FIRST) {
          if (kt == 1) __builtin_amdgcn_raw_buffer_store_b128(__builtin_bit_cast(u32x4, csum), rsW, cst, 0, 16);
          if (kt == 3 && tid == 320) __builtin_amdgcn_raw_buffer_store_b64(__builtin_bit_cast(u32x2, fall), rsW, WS_FLAGS + (unsigned)(512 + rt * 16 + hp) * FLS, 0, 16);
          if (kt == 3 || kt == 4) glds16s(wo + (kt - 3) * 512, vX, lds_addr(smem) + OFF_JK);
        }
        if (HH && (kt == 3 || kt == 4)) glds16s(wo + (kt - 3) * 512, vY, lds_addr(smem) + OFF_JK);
        if (!FIRST && kt == 3 && tid == 320) __builtin_amdgcn_raw_buffer_store_b64(__builtin_bit_cast(u32x2, fsv), rsW, WS_FLAGS + (unsigned)(512 + rt * 16 + hp) * FLS, 0, 16);
        if (!FIRST && kt == 8 && lw == 2) asm volatile("global_load_dwordx2 %0, %1, %2 sc1" : "=v"(fal3) : "v"(fao), "s"(ws + WS_FLAGS + 256 * FLS) : "memory");
        if (!FIRST && kt == 0 && lw == 3) asm volatile("global_load_dwordx2 %0, %1, %2 sc1" : "=v"(fall) : "v"(fao), "s"(ws + WS_FLAGS) : "memory");
        if ((FIRST || HH) && kt == 9 && lw == 3) asm volatile("global_load_dwordx2 %0, %1, %2 sc1" : "=v"(fal2) : "v"(fa2), "s"(ws + WS_FLAGS) : "memory");
      }
      if (!FIRST) setup();
      WAITVN(N);
      BAR;
#pragma unroll
      for (int i = 0; i < (FIRST ? 1 : 4); ++i) ld_c(sv[i], vS, i * 4096);
      if (FIRST || HH) {
#pragma unroll
        for (int i = 0; i < 4; ++i) ld_c(cv[i], vC, i * 3072);
      }
      if (HH) glds16s(wphi, vX, lds_addr(smem) + OFF_WP + lw * 1024);
      else if (!FIRST) glds16c(ws + WS_CSA, vX, lds_addr(smem) + OFF_CS);
      wo_tile(0); wo_tile(1);
      WAITVN(HH ? 13 : 9);
      BAR;
      if (FIRST) {
        if (lw < 3) { ld_pw(0); ld_pw(4); WAITV(12); } else { WAITV(4); }
      } else {
        prep(IC<1>{});
        WAITV(4);
      }
      cfin();
      fin();
      asm volatile("" : "+v"(tm), "+v"(tr));
      if (FIRST || HH) asm volatile("" : "+v"(tc));
      if (FIRST && lw < 3) { WAITV(8); } else { WAITV(0); }
      lds<f32x2>(sb + dS) = f32x2{tm, tr};
      lds<float>(sb + dR) = tm; lds<float>(sb + dR + 512) = tr;
      if (FIRST || HH) lds<unsigned>(sb + dC) = tc;
      if (!HH) lds<float>(prd) = zero1();
      WAITL0;
    };
    typedef std::integral_constant<bool, true> T_;
    typedef std::integral_constant<bool, false> F_;
    __builtin_amdgcn_s_setprio(3);
    if (first) run(T_{}); else run(F_{});
    __builtin_amdgcn_s_setprio(0);
    BAR;
    if constexpr (HH) {
      u32x4 c0, c1;
      if (layer == 1) {
        const int n = bid * 8 + lw * 2;
        const void* p;
        unsigned a0, a1;
        if (n < 1536) {
          const int r = n % 768, q = r % 384, e = q % 24;
          p = n < 768 ? wn1 : W;
          a0 = (unsigned)((((q / 24) * 64 + (e / 6) * 16 + (lane >> 3)) * 3072 + ((e % 6) >> 1) * 1024 + (r < 384 ? 192 : 704)) * 2);
          a1 = a0 + 8 * 6144 + cB1 * 16; a0 += cB0 * 16;
        } else {
          p = n < 1792 ? wn2 : wo;
          a0 = (unsigned)((((n & 127) * 8 + (lane >> 3)) * DM + ((n & 128) ? 704 : 192) + ((lane & 7) ^ ((lane >> 4) << 1)) * 8) * 2);
          a1 = a0 + 8 * DM * 2;
        }
        p = uni(p);
        gld16(c0, a0, p); gld16(c1, a1, p);
        const u32x4 pz = empty4();
        __builtin_amdgcn_raw_buffer_store_b128(pz, rsW, dm, 0, 16);
        __builtin_amdgcn_raw_buffer_store_b128(pz, rsW, dm, 4096, 16);
        __builtin_amdgcn_raw_buffer_store_b128(pz, rsW, dm2, 0, 16);
      }
#pragma unroll
      for (int t = 2; t < 6; ++t) wo_tile(t);
      if (layer == 1) {
        WAITV(11);
        __builtin_amdgcn_sched_barrier(0);
        asm volatile("" : "+v"(c0), "+v"(c1));
        __builtin_amdgcn_raw_buffer_store_b128(c0, rsW, WS_IMG + (unsigned)(bid * 8192 + lw * 2048 + lane * 16), 0, 16);
        __builtin_amdgcn_raw_buffer_store_b128(c1, rsW, WS_IMG + (unsigned)(bid * 8192 + lw * 2048 + 1024 + lane * 16), 0, 16);
        WAITV(0);
      }
      pwl = vW0; pwm = vW1; kol = ko2;
      asm volatile("" : "=v"(csq[0]), "=v"(csq[1]), "=v"(st01), "=v"(st23), "=v"(wf[0]), "=v"(wf[1]));
      asm volatile("" : "=v"(acc[0][0]), "=v"(acc[0][1]), "=v"(acc[1][0]), "=v"(acc[1][1]));
      asm volatile("" : "=v"(qh[0][0]), "=v"(qh[0][1]), "=v"(qh[1][0]), "=v"(qh[1][1]));
    } else {
      const int lane = lane_id();
#pragma unroll
      for (int t = 2; t < 12; ++t) {
        wo_tile(t);
        if (t == 2 && first) {
        const u32x4 pz = empty4();
        __builtin_amdgcn_raw_buffer_store_b128(pz, rsW, dm, 0, 16);
        __builtin_amdgcn_raw_buffer_store_b128(pz, rsW, dm, 4096, 16);
        __builtin_amdgcn_raw_buffer_store_b128(pz, rsW, dm3, 0, 16);
        __builtin_amdgcn_raw_buffer_store_b128(pz, rsW, dm2, 0, 16);
        if (lw == 3 && lane < 48) __builtin_amdgcn_raw_buffer_store_b128(lds<u32x4>(sb + OFF_CS + lane * 16), rsW, WS_CSA + (unsigned)((rt * 16 + hp) * 1024 + lane * 16), 0, 16);
        }
      }
      if (first) { BAR;   WAITV(0); }
    }
    asm volatile("" : "={v[64:79]}"(xq));
  } else {

    prep(IC<0>{});
    if (first) {
      const int row = m0 + hp * 8 + w * 2 + (lane >> 5);
      asm volatile("s_load_dwordx2 %0, %1, 0x0\n\ts_waitcnt lgkmcnt(0)" : "=s"(E) : "s"(ws) : "memory");
      const unsigned tag = (unsigned)E + 1u;
      static_assert(HH || (OFF_SP == 122880 && OFF_R2 + 1024 <= OFF_JK), "addresses used at the top of the kernel");
      WAITV(0);
      u32x4 xs[4];
#pragma unroll
      for (int i = 0; i < 4; ++i) xs[i] = *(const u32x4*)(smem + OFF_SP + w * 4096 + (lane >> 5) * 2048 + (lane & 31) * 16 + i * 512);
      typedef __attribute__((ext_vector_type(2))) __bf16 bf2;
      const bf2 one2 = __builtin_bit_cast(bf2, 0x3F803F80u);
      float sa[2] = {zero1(), zero1()}, sq[2] = {zero1(), zero1()};
#pragma unroll
      for (int i = 0; i < 16; ++i) {
        const unsigned u = xs[i >> 2][i & 3];
        const bf2 t = __builtin_bit_cast(bf2, u);
        sa[i & 1] = __builtin_amdgcn_fdot2_f32_bf16(t, one2, sa[i & 1], false);
        sq[i & 1] = __builtin_amdgcn_fdot2_f32_bf16(t, t, sq[i & 1], false);
      }
      float sm = r16_sum(sa[0] + sa[1]), m2 = r16_sum(sq[0] + sq[1]);
      {
        auto r = __builtin_amdgcn_permlane16_swap(__float_as_uint(sm), __float_as_uint(sm), false, false);
        auto q = __builtin_amdgcn_permlane16_swap(__float_as_uint(m2), __float_as_uint(m2), false, false);
        sm = __uint_as_float(r[0]) + __uint_as_float(r[1]); m2 = __uint_as_float(q[0]) + __uint_as_float(q[1]);
      }
      const float mean = sm * (1.0f / 1024.0f);
      m2 = fmaxf(m2 - sm * mean, 0.f);
      u32x4 gw = {__float_as_uint(-mean), tag, __float_as_uint(rsqrtf(m2 * (1.0f / 1024.0f) + 1e-5f)), tag};
      if ((lane & 31) == 0) __builtin_amdgcn_raw_buffer_store_b128(gw, rsW, (unsigned)(WS_G0 + row * 16), 0, 16);
      __builtin_amdgcn_raw_buffer_store_b128(empty4(), rsW, WS_HL + (unsigned)((rt * 16 + hp) * 4096 + tid * 16), 0, 16);
    }
    const int fl = (q4 >> 1) | ((g & 1) << 1);
    const unsigned bo = lds_addr(smem) + ABYTES + (g >> 1) * 6144 + (g & 1) * 1024 + q4 * 128 + p4 * 8;
    const unsigned bo0 = bo + (((wn * 2 + 0) ^ fl) << 5), bo1 = bo + (((wn * 2 + 1) ^ fl) << 5);
    const unsigned ar = lds_addr(smem) + (wm * 64 + fr) * 128;
    const unsigned aK0 = first ? ar + ((g ^ (fr & 7)) << 4) : sb + wm * 8192 + lane * 16, aK1 = first ? ar + (((4 + g) ^ (fr & 7)) << 4) : aK0 + 1024;
    f32x32 ac0, ac1, ac2;
    asm volatile("" :: "v"(aK0), "v"(aK1), "v"(bo0), "v"(bo1));
    if (first) BAR;
    else publish(rsW, tid, 256 + rt * 16 + hp, E + (unsigned)layer);
#define G1_OUT
#define G1_T(a)
#define G1_TEND
#define G1_TCL
    asm volatile(G1_MACROS
        "s_mov_b32 vcc_hi, %[lay]\n"
        ".if %[hh]\n s_branch .Lg1m\n .else\n"
        ".p2align 3\n .Lg1m:\n"
        "G1RDB 0, v16, v17, 0\n G1RDA v12, v13, 0\n G1RDB 1, v16, v17, 0\n"
        "ds_read_b128 v[64:67], v20\n ds_read_b128 v[68:71], v20 offset:2048\n"
        "ds_read_b128 v[72:75], v20 offset:4096\n ds_read_b128 v[76:79], v20 offset:6144\n"
        "s_cmp_eq_u32 vcc_hi, 0\n s_waitcnt lgkmcnt(12)\n"
        "G1SG 0, 1, 1, v12, v13, v16, v17, 0\n"
        "s_mov_b32 vcc_lo, 16\n"
        "G1SG 1, 1, 1, v12, v13, v16, v17, 40960\n"
        "G1SG 2, 1, 2, v12, v13, v16, v17, 40960\n"
        "s_cbranch_scc1 .Lg1a%=\n"
        "s_barrier\n"
        G1_T(88)
        "s_barrier\n"
        G1_T(92)
        "G1RDA v12, v13, 40960\n"
        "s_waitcnt lgkmcnt(4)\n"
        "s_branch .Lg1b%=\n"
        ".Lg1a%=:\n"
        G1_T(88)
        "s_barrier\n"
        G1_T(92)
        "G1RDB 0, v16, v17, 40960\n G1RDA v12, v13, 40960\n G1RDB 1, v16, v17, 40960\n"
        ".p2align 3\n s_nop 0\n"
        ".Lg1b%=:\n"
        "s_waitcnt lgkmcnt(8)\n"
        "G1SG 0, 0, 1, v12, v13, v16, v17, 40960\n"
        "s_barrier\n"
        "G1SG 1, 0, 1, v14, v15, v18, v19, 0\n"
        "G1SG 2, 0, 1, v14, v15, v18, v19, 0\n"
        "s_lshr_b32 vcc_lo, vcc_lo, 1\n s_waitcnt vmcnt(0) lgkmcnt(8)\n"
        "G1SG 0, 0, 1, v14, v15, v18, v19, 0\n"
        "s_barrier\n"
        "G1SG 1, 0, 1, v12, v13, v16, v17, 0\n"
        "G1SG 2, 0, 1, v12, v13, v16, v17, 0\n"
        "s_cbranch_scc0 .Lg1c%=\n s_waitcnt lgkmcnt(8)\n"
        "G1SG 0, 0, 1, v12, v13, v16, v17, 0\n"
        "s_barrier\n"
        "G1SG 1, 0, 1, v12, v13, v16, v17, 40960\n"
        "G1SG 2, 0, 1, v12, v13, v16, v17, 40960\n"
        "s_branch .Lg1b%=\n"
        ".Lg1c%=:\n"
        "s_waitcnt lgkmcnt(8)\n"
        "G1SG 0, 0, 1, v12, v13, v16, v17, 0\n s_bitcmp1_b32 vcc_hi, 0\n"
        G1_T(96)
        "s_cbranch_scc1 .Lg1n\n s_barrier\n"
        G1_SLIDE
        ".endif\n .if %[hh]\n .p2align 3\n s_nop 0\n .Lg1n:\n s_barrier\n"
        G1_HEDGE
        ".endif\n"
        G1_TEND G1_PURGE
        : "=&{v[160:191]}"(ac0), "=&{v[192:223]}"(ac1), "=&{v[224:255]}"(ac2), "=&{v[64:79]}"(xq),
          "=&{v[88:91]}"(csq[1]), "=&{v[148:151]}"(csq[0]), "=&{v[140:143]}"(st01), "=&{v[136:139]}"(st23),
          "+{v[48:63]}"(pa), "+{v[36:39]}"(bias[0]), "+{v[40:43]}"(bias[1]), "+{v[44:47]}"(bias[2]) G1_OUT
        : "{v12}"(aK0), "{v13}"(aK1), "{v14}"(aK0 + 2 * STAGE), "{v15}"(aK1 + 2 * STAGE), "{v16}"(bo0), "{v17}"(bo1),
          "{v18}"(bo0 + 2 * STAGE), "{v19}"(bo1 + 2 * STAGE), "{v20}"(wn ? aK1 : aK0),
          [lay] "s"(layer), [hh] "n"(MODE), [rs] "s"(rsW), [wm] "s"(wm)
        : "memory", "scc", "vcc", "m0", G1_TCL G1_CLOBBER);
    acc[0][0] = quad<0>(ac0); acc[0][1] = quad<1>(ac0); acc[1][0] = quad<6>(ac0); acc[1][1] = quad<7>(ac0);
    asm volatile("" : "=s"(kol));
    wf[0] = __builtin_bit_cast(bf16x8, bias[0]); wf[1] = __builtin_bit_cast(bf16x8, bias[1]);
    qh[0][0] = quad<4>(ac1); qh[0][1] = quad<5>(ac1); qh[1][0] = quad<2>(ac2); qh[1][1] = quad<3>(ac2);
  }
  ST(layer * 24 + 4)
  {
  const int lane = lane_id(), tid = w * 64 + lane, fr = lane & 15, g = lane >> 4;
  (void)fr; (void)g;
  if constexpr (HH) {
    const f32x4 z4 = {0, 0, 0, 0};
    constexpr float L2E = 1.4426950408889634f;
    const unsigned kvo = WS_KV + (layer >> 1) * KVLB, kfo = kvo + 16u * NH * KVPB + hp * KVFB;
    auto phi2 = [&](const f32x4 (&a)[2][2], f32x4 st, bf16x8 (&qp)[2][2]) {
      f32x4 pm[2][2];
      f32x2 f[2][4];
      asm volatile("" : "+v"(st));
#pragma unroll
      for (int i = 0; i < 8; ++i) {
        const int rb = i >> 2, c = (i >> 1) & 1, e = (i & 1) * 2;
        f[rb][i & 3] = __builtin_elementwise_fma(f32x2{csq[c][e], csq[c][e + 1]}, f32x2{st[2 * rb], st[2 * rb]}, f32x2{a[rb][c][e], a[rb][c][e + 1]});
      }
#pragma unroll
      for (int rb = 0; rb < 2; ++rb) asm volatile("" : "+v"(f[rb][0]), "+v"(f[rb][1]), "+v"(f[rb][2]), "+v"(f[rb][3]));
#pragma unroll
      for (int rb = 0; rb < 2; ++rb) {
        const bf16x8 xf = __builtin_bit_cast(bf16x8, u32x4{pk2(f[rb][0][0], f[rb][0][1]), pk2(f[rb][1][0], f[rb][1][1]), pk2(f[rb][2][0], f[rb][2][1]), pk2(f[rb][3][0], f[rb][3][1])});
        pm[rb][0] = MFMA(wf[0], xf, z4); pm[rb][1] = MFMA(wf[1], xf, z4);
      }
      asm volatile("s_nop 7" : "+v"(pm[0][0]), "+v"(pm[0][1]), "+v"(pm[1][0]), "+v"(pm[1][1]));
      float mx[2], mn[2];
#pragma unroll
      for (int u = 0; u < 2; ++u) max8pn(pm[u][0], pm[u][1], mx[u], mn[u]);
      xr4<true>(mx[0], mn[0], mx[1], mn[1]);
      float ep[2][8], en[2][8], sp[2], sn[2];
#pragma unroll
      for (int u = 0; u < 2; ++u) {
        const float c = st[2 * u + 1] * L2E, mxs = -mx[u] * c, mns = -mn[u] * c;
#pragma unroll
        for (int i = 0; i < 4; ++i) {
          const f32x2 p2 = {pm[u][i >> 1][(i & 1) * 2], pm[u][i >> 1][(i & 1) * 2 + 1]};
          const f32x2 tp = __builtin_elementwise_fma(p2, f32x2{c, c}, f32x2{mxs, mxs});
          const f32x2 tn = __builtin_elementwise_fma(-p2, f32x2{c, c}, f32x2{mns, mns});
          ep[u][2 * i] = __builtin_amdgcn_exp2f(tp[0]); ep[u][2 * i + 1] = __builtin_amdgcn_exp2f(tp[1]);
          en[u][2 * i] = __builtin_amdgcn_exp2f(tn[0]); en[u][2 * i + 1] = __builtin_amdgcn_exp2f(tn[1]);
        }
        const f32x2 a2 = (f32x2{ep[u][0], ep[u][1]} + f32x2{ep[u][2], ep[u][3]}) + (f32x2{ep[u][4], ep[u][5]} + f32x2{ep[u][6], ep[u][7]});
        const f32x2 b2 = (f32x2{en[u][0], en[u][1]} + f32x2{en[u][2], en[u][3]}) + (f32x2{en[u][4], en[u][5]} + f32x2{en[u][6], en[u][7]});
        sp[u] = a2[0] + a2[1]; sn[u] = b2[0] + b2[1];
      }
      xr4<false>(sp[0], sn[0], sp[1], sn[1]);
#pragma unroll
      for (int rb = 0; rb < 2; ++rb) {
        const float ip = __builtin_amdgcn_rcpf(sp[rb]), in = __builtin_amdgcn_rcpf(sn[rb]);
        qp[rb][0] = __builtin_bit_cast(bf16x8, u32x4{pk2(ep[rb][0] * ip, ep[rb][1] * ip), pk2(ep[rb][2] * ip, ep[rb][3] * ip), pk2(ep[rb][4] * ip, ep[rb][5] * ip), pk2(ep[rb][6] * ip, ep[rb][7] * ip)});
        qp[rb][1] = __builtin_bit_cast(bf16x8, u32x4{pk2(en[rb][0] * in, en[rb][1] * in), pk2(en[rb][2] * in, en[rb][3] * in), pk2(en[rb][4] * in, en[rb][5] * in), pk2(en[rb][6] * in, en[rb][7] * in)});
        asm volatile("" : "+v"(qp[rb][0]), "+v"(qp[rb][1]));
      }
    };
    bf16x8 one8 = ones8();
    asm volatile("" : "+v"(one8));
    WAITL0; BAR;
    ST(layer * 24 + 5)
    {
      bf16x8 af[4], v0[4], v1[4];
      asm volatile("" ::: "memory");
#pragma unroll
      for (int sw = 0; sw < 4; ++sw) {
        af[sw] = cat8(tr_read(ppa + sw * (64 * PSTR)), tr_read(ppa + sw * (64 * PSTR) + 16 * PSTR));
        v0[sw] = lds<bf16x8>(pvr + sw * 4096);
        v1[sw] = lds<bf16x8>(pvr + sw * 4096 + 1024);
      }
      __builtin_amdgcn_sched_barrier(0);
      f32x4 kv[3] = {z4, z4, z4};
#pragma unroll
      for (int sw = 0; sw < 4; ++sw) {
        kv[0] = MFMA(af[sw], v0[sw], kv[0]);
        kv[1] = MFMA(af[sw], v1[sw], kv[1]);
        kv[2] = MFMA(af[sw], one8, kv[2]);
      }
      auto pc = [](float x, float y) { const unsigned t = pk2(x, y); return t < EMPTY - 1u ? t : EMPTY - 1u; };
      __builtin_amdgcn_raw_buffer_store_b128(u32x4{pc(kv[0][0], kv[0][1]), pc(kv[0][2], kv[0][3]), pc(kv[1][0], kv[1][1]), pc(kv[1][2], kv[1][3])}, rsW, gkv, 0, 16);
      __builtin_amdgcn_raw_buffer_store_b64(u32x2{pc(kv[2][0], kv[2][1]), pc(kv[2][2], kv[2][3])}, rsW, gkz, 0, 16);
    }
    auto wo_late = [&](int t) { glds2v(wos + knib(kol, t) * tO, pwl, pwm, sb + OFF_WO + t * 8192 + (w & 3) * 2048); };
    ST(layer * 24 + 6)
    u32x4 l1[8];
    const bool red = w == 7 || (w == 6 && (rt & 7) == 0);
    unsigned lo = kvo + (unsigned)((2 * hp + (rt >> 3) + (lane >> 5) * 8 * NH) * KVPB) +
                  (w == 7 ? (unsigned)((rt & 4) * 512 + (g & 1) * 1024 + (rt & 3) * 256 + fr * 16) : (unsigned)(4096 + (lane & 7) * 16));
    unsigned fs = w == 7 ? kfo + (unsigned)((rt >> 2) * 2048 + (lane >> 5) * 1024 + (rt & 3) * 256 + fr * 16 + (g & 1) * 8)
                : (lane & 24) ? 0x7fff0000u : kfo + (unsigned)(8192 + (rt >> 3) * 128 + (lane & 4) * 16 + (lane & 1) * 32 + (lane >> 5) * 16 + (lane & 2) * 4);
    auto l1_load = [&]() {
      asm volatile("" ::: "memory");
#pragma unroll
      for (int p = 0; p < 8; ++p) asm volatile("buffer_load_dwordx4 %0, %1, %2, %3 offen sc1" : "=v"(l1[p]) : "v"(lo), "s"(rsW), "s"(p * (NH * KVPB)) : "memory");
      __builtin_amdgcn_sched_barrier(0);
    };
    if (red) asm volatile("" : "+v"(lo), "+v"(fs));
    if (red) { __builtin_amdgcn_s_sleep(13); l1_load(); }
    bf16x8 qp2[2][2];
    if (!kg) {
      phi2(qh, st23, qp2);
    } else if (red) {
      typedef __attribute__((ext_vector_type(2))) __bf16 bf2;
      unsigned c0 = 0x00003F80u, c1 = 0x3F800000u;
      asm volatile("" : "+v"(c0), "+v"(c1));
      float s4[4];
      _Pragma("unroll 1") for (unsigned spins = 0; spins < (1u << 18); ++spins) {
        float a[8] = {zero1(), zero1(), zero1(), zero1(), zero1(), zero1(), zero1(), zero1()};
        ARRIVED("+v"(l1[0]), "+v"(l1[1]), "+v"(l1[2]), "+v"(l1[3]), "+v"(l1[4]), "+v"(l1[5]), "+v"(l1[6]), "+v"(l1[7]));
#pragma unroll
        for (int p = 0; p < 8; ++p)
#pragma unroll
          for (int e = 0; e < 4; ++e) {
            const unsigned u = l1[p][e];
            a[2 * e] = __builtin_amdgcn_fdot2_f32_bf16(__builtin_bit_cast(bf2, u), __builtin_bit_cast(bf2, c0), a[2 * e], false);
            a[2 * e + 1] = __builtin_amdgcn_fdot2_f32_bf16(__builtin_bit_cast(bf2, u), __builtin_bit_cast(bf2, c1), a[2 * e + 1], false);
          }
#pragma unroll
        for (int i = 0; i < 4; ++i) {
          auto r = __builtin_amdgcn_permlane32_swap(__float_as_uint(a[i]), __float_as_uint(a[i + 4]), false, false);
          s4[i] = __uint_as_float(r[0]) + __uint_as_float(r[1]);
        }
        const float t = s4[0] + s4[2];
        if (__all(t == t)) break;
        unsigned mxw = 0;
#pragma unroll
        for (int p = 0; p < 8; ++p)
#pragma unroll
          for (int e = 0; e < 4; ++e) mxw = l1[p][e] > mxw ? l1[p][e] : mxw;
        if (__all(mxw != EMPTY)) break;
        l1_load();
      }
      WAITV(0);
      unsigned o0 = pk2(s4[0], s4[1]), o1 = pk2(s4[2], s4[3]);
      o0 = o0 < EMPTY - 1u ? o0 : EMPTY - 1u; o1 = o1 < EMPTY - 1u ? o1 : EMPTY - 1u;
      __builtin_amdgcn_raw_buffer_store_b64(u32x2{o0, o1}, rsW, fs, 0, 16);
    }
    ST(layer * 24 + 8)
    WAITL0;
    BAR;
    ST(layer * 24 + 9)
    if (kg) {
#pragma unroll
      for (int i = 6; i < 12; ++i) wo_late(i);
    } else {
      unsigned fo = kfo + (unsigned)(wn * 4096 + lane * 16), fz = kfo + (unsigned)(8192 + wn * 128 + g * 16);
      asm volatile("" : "+v"(fo), "+v"(fz));
      bf16x8 qp[2][2];
      phi2(acc, st01, qp);
      asm volatile(G1_OUTPUT
          :: "{v[224:227]}"(qp[0][0]), "{v[228:231]}"(qp[0][1]), "{v[232:235]}"(qp[1][0]), "{v[236:239]}"(qp[1][1]),
             "{v[240:243]}"(qp2[0][0]), "{v[244:247]}"(qp2[0][1]), "{v[248:251]}"(qp2[1][0]), "{v[252:255]}"(qp2[1][1]),
             [fo] "v"(fo), [fz] "v"(fz), [go] "v"(ogo), [lo] "v"(opo), [rs] "s"(rsW)
          : "memory", "vcc", "scc", "m0", "v152", "v153", "v154", "v155", "v156", "v157", "v158", "v159", "v160", "v161", "v162", "v163", "v164", "v165", "v166", "v167", "v168", "v169", "v170", "v171", "v172", "v173", "v174", "v175", "v176", "v177", "v178", "v179", "v180", "v181", "v182", "v183", "v184", "v185", "v186", "v187", "v188", "v189", "v190", "v191", "v192", "v193", "v194", "v195", "v196", "v197", "v198", "v199", "v200", "v201", "v202", "v203", "v204", "v205", "v206", "v207", "v208", "v209", "v210", "v211", "v212", "v213", "v214", "v215", "v216", "v217", "v218", "v219", "v220", "v221", "v222", "v223");
    }
  }
  ST(layer * 24 + 12)
  if (first && w >= 4 && w < 7) {
    typedef __attribute__((ext_vector_type(2))) __bf16 bf2;
    unsigned c0 = 0x00003F80u, c1 = 0x3F800000u;
    asm volatile("" : "+v"(c0), "+v"(c1));
    const bf2 lo1 = __builtin_bit_cast(bf2, c0), hi1 = __builtin_bit_cast(bf2, c1);
    float a[8] = {zero1(), zero1(), zero1(), zero1(), zero1(), zero1(), zero1(), zero1()};
#pragma unroll
    for (int i = 0; i < 8; ++i) {
      asm volatile("" : "+v"(pw[i]));
#pragma unroll
      for (int e = 0; e < 4; ++e) {
        const unsigned u = pw[i][e];
        a[2 * e] = __builtin_amdgcn_fdot2_f32_bf16(__builtin_bit_cast(bf2, u), lo1, a[2 * e], false);
        a[2 * e + 1] = __builtin_amdgcn_fdot2_f32_bf16(__builtin_bit_cast(bf2, u), hi1, a[2 * e + 1], false);
      }
    }
#pragma unroll
    for (int i = 0; i < 4; ++i) {
      auto r = __builtin_amdgcn_permlane32_swap(__float_as_uint(a[i]), __float_as_uint(a[i + 4]), false, false);
      a[i] = __uint_as_float(r[0]) + __uint_as_float(r[1]);
    }
#pragma unroll
    for (int i = 0; i < 2; ++i) {
      auto r = __builtin_amdgcn_permlane16_swap(__float_as_uint(a[i]), __float_as_uint(a[i + 2]), false, false);
      a[i] = __uint_as_float(r[0]) + __uint_as_float(r[1]);
      a[i] += dpp_ror<8>(a[i]);
    }
    if (!(lane & 8))
      __builtin_amdgcn_raw_buffer_store_b64(u32x2{__float_as_uint(a[0]), __float_as_uint(a[1])}, rsW,
          (unsigned)(WS_CSP + ((((hp + (1 << (w - 4))) & 15) * 16 + rt) * 192 + (w - 4) * 64 + (lane & 7) * 8 + (lane >> 5) * 4 + ((lane >> 4) & 1) * 2) * 4), 0, 16);
  }
  }
}

#undef pcs
#undef prs
#undef opo
#undef ogo
#undef hof
#undef dm
#undef dm2
#undef pcv
#undef pwf
#undef prv
#undef pvf
#undef ppk
#undef ppa
#undef pvr
#undef gkv
#undef gkz
#undef pwl
#undef pwm
#undef plb
#undef prd
#undef dm3

__device__ __forceinline__ void g2_phase(char* smem, char* ws, u64 E, int w, f32x16& xq, Next& nx, int layer, const u16* __restrict__ x0,
                                         const u16* __restrict__ wo, const u16* __restrict__ wnx, u16* __restrict__ out) {
  IDS
  wnx = uni(wnx);
  const unsigned sb = lds_addr(smem);
  const int lw = w & 3, k2 = 16 * lw + (lane >> 3);
  unsigned pxr, rmo;
  auto ends = [&]() {
    const int lane = lane_id(), r0 = w * 16 + (lane >> 3);
    pxr = sb + r0 * 128 + (lane & 7) * 16;
    rmo = (unsigned)(((m0 + r0) * DM + hp * 64 + ((lane & 7) ^ (lane >> 3)) * 8) * 2);
    asm volatile("" : "+v"(pxr), "+v"(rmo));
  };
  constexpr int S0 = 0, S1 = 16384, S2 = 32768, S3 = 147456, S4 = OFF_WO, S5 = 98304, S6 = 114688;
  constexpr int AS[16] = {S0, S1, S2, S3, S0, S1, S4, S2, S3, S0, S1, S4, S5, S3, S0, S6};
  constexpr int OFF_C = 0;
  auto bs = [](int t) { return OFF_WO + (t < 12 ? t : t - 10) * 8192; };
  const char *xa1, *xa2, *wq2;
  u64 fm;
  unsigned vB0, vB1, vXA, po, pst;
  if (w >= 4) {
    constexpr int WT[15] = {0, 8, 12, 14, 16, 18, 20, 18, 16, 14, 12, 12, 10, 10, 8};
    const char* o = ws + WS_O + layer * (SEQ * DM * 2) + rt * 262144;
    const unsigned vA = (unsigned)(lw * 4096 + lane * 16);
    u64 kord = korder(bid & 3, hp);
    auto ldA = [&](int s) { glds4c(o + knib(kord, s) * 16384, vA, sb + AS[s] + lw * 4096); };
    const bool im = img(layer, hp), in = img(layer + 1, hp);
    unsigned vW0 = im ? (unsigned)(lw * 2048 + lane * 16) : (unsigned)((k2 * DM + hp * 64 + ((lane & 7) ^ ((lane >> 4) << 1)) * 8) * 2), vW1 = vW0 + (im ? 0u : 8 * DM * 2 - 1024u);
    const char *wos = wo_src(ws, wo, layer, hp), *wqs = wq_src(ws, wnx, layer + 1, hp);
    unsigned tO = im ? 8192u : 131072u;
    asm volatile("" : "+v"(vW0), "+v"(vW1));
    asm volatile("" : "+s"(wos), "+s"(tO));
    auto ldW = [&](int t) { glds2v(wos + knib(kord, t) * tO, vW0, vW1, sb + bs(t) + lw * 2048); };
    const int fB = (k2 >> 1) & 1;
    vB0 = in ? (unsigned)(lw * 6144 + lane * 16) : (unsigned)((k2 * 3072 + hp * 64 + (((((lane & 7) >> 1) ^ fB) << 1) | (lane & 1)) * 8) * 2);
    vB1 = in ? vB0 : (unsigned)(((k2 + 8) * 3072 + hp * 64 + (((((lane & 7) >> 1) ^ (fB | 2)) << 1) | (lane & 1)) * 8) * 2) - 1024u;
    const u64 kn = nx.kord = korder(kv1(bid), hp);
    nx.ko2 = kord; nx.wq = wqs; nx.tw = in ? 24576u : 393216u;
    const char *wq0 = wqs + hp * (in ? 24576u : 393216u), *wq1 = wqs + knib(kn, 1) * (in ? 24576u : 393216u);
    wq2 = wqs + knib(kn, 2) * (in ? 24576u : 393216u);
    xa1 = ws + WS_X + layer * (SEQ * DM * 2) + knib(kn, 1) * 16384; xa2 = ws + WS_X + layer * (SEQ * DM * 2) + knib(kn, 2) * 16384;
    vXA = (unsigned)(rt * 262144 + lw * 4096 + lane * 16);
    fm = kfirst(kn);
    po = ((fm >> lane) & 1) ? WS_FLAGS + (unsigned)(256 + rt * 16 + lane) * FLS : 0u;
    pst = g != q4 ? 0x7fff0000u : WS_ST + layer * (SEQ * 128) + (unsigned)(((rt * 16 + hp) * 128 + lw * 32 + fr) * 8);
    asm volatile("" : "+v"(vB0), "+v"(vB1), "+v"(vXA), "+v"(po), "+v"(pst));
    asm volatile("" : "+s"(xa1), "+s"(xa2), "+s"(wq2), "+s"(fm), "+s"(nx.kord), "+s"(nx.ko2), "+s"(nx.wq), "+s"(nx.tw));
    asm volatile("" : "+s"(wq0), "+s"(wq1));
    auto ldB = [&](int s, int part) {
      const char* b = s ? wq1 : wq0;
      if (part) glds4v(b, vB0, vB1, vB0, vB1, sb + s * 40960 + 16384 + lw * 6144);
      else glds2v(b + 4096, vB0, vB1, sb + s * 40960 + 16384 + lw * 6144 + 4096);
    };
    ends();
    const char* o1 = o + knib(kord, 1) * 16384;
    asm volatile("" : "+s"(o1), "+s"(kord), "+s"(o));
    asm volatile("" :: "v"(vA));
    if (layer) { BAR;   }
    BAR;
    glds4c(o1, vA, sb + AS[1] + lw * 4096); __builtin_amdgcn_s_sleep(3); ldA(2); __builtin_amdgcn_s_sleep(3); ldA(3); __builtin_amdgcn_s_sleep(3); ldA(4);
    WAITV(12);
    BAR;
    auto step = [&](auto ktc) {
      constexpr int kt = decltype(ktc)::value;
      WAITVN(WT[kt]);
      BAR;
      if constexpr (kt == 1) { ldA(5); ldA(6); }
      if constexpr (kt >= 2 && kt <= 10) ldA(kt + 5);
      if constexpr (kt >= 2 && kt <= 5) ldW(kt + 10);
      if constexpr (kt >= 11) ldB((kt - 11) >> 1, (kt - 11) & 1);
      if constexpr (kt >= 6) { __builtin_amdgcn_s_sleep(kt >= 11 ? 3 : 2); if (layer == 1) __builtin_amdgcn_s_sleep(1); }
    };
    step(IC<1>{}); step(IC<2>{}); step(IC<3>{}); step(IC<4>{}); step(IC<5>{}); step(IC<6>{}); step(IC<7>{});
    step(IC<8>{}); step(IC<9>{}); step(IC<10>{}); step(IC<11>{}); step(IC<12>{}); step(IC<13>{}); step(IC<14>{});
  } else {
    asm volatile("" : "=v"(vB0), "=v"(vB1), "=v"(vXA), "=v"(po), "=v"(pst));
    asm volatile("" : "=s"(xa1), "=s"(xa2), "=s"(wq2), "=s"(fm), "=s"(nx.kord), "=s"(nx.ko2), "=s"(nx.wq), "=s"(nx.tw));
    const int wm = w >> 1, wn = w & 1;
    f32x4 acc[4][2];
    const int fl = (q4 >> 1) | ((g & 1) << 1);
    const int bb0 = (4 * g + q4) * 128 + p4 * 8 + (((wn * 2) ^ fl) << 5), bb1 = (4 * g + q4) * 128 + p4 * 8 + (((wn * 2 + 1) ^ fl) << 5);
    const int abase = wm * 4096 + lane * 16;
    bf16x8 fa[2][2][4], fb[2][2][2];
    unsigned ra[3], rb0[2], rb1[2];
#pragma unroll
    for (int i = 0; i < 3; ++i) { ra[i] = abase + i * 65536; asm volatile("" : "+v"(ra[i])); }
#pragma unroll
    for (int i = 0; i < 2; ++i) { rb0[i] = bb0 + OFF_WO + i * 49152; rb1[i] = bb1 + OFF_WO + i * 49152; asm volatile("" : "+v"(rb0[i]), "+v"(rb1[i])); }
    auto loadf = [&](int kt, bf16x8 (&a)[2][4], bf16x8 (&b)[2][2]) {
#pragma unroll
      for (int s = 0; s < 2; ++s) {
        const char* sA = smem + ra[AS[kt] >> 16] + (AS[kt] & 65535) + s * 8192;
        const int hi = bs(kt) >= OFF_WO + 49152, ob = bs(kt) - OFF_WO - hi * 49152 + s * 4096;
        b[s][0] = cat8(tr_read(smem + rb0[hi] + ob), tr_read(smem + rb0[hi] + ob + 2048));
        b[s][1] = cat8(tr_read(smem + rb1[hi] + ob), tr_read(smem + rb1[hi] + ob + 2048));
#pragma unroll
        for (int rb = 0; rb < 4; ++rb) a[s][rb] = *(const bf16x8*)(sA + rb * 1024);
      }
    };
    auto mma = [&](bf16x8 (&a)[2][4], bf16x8 (&b)[2][2]) {
#pragma unroll
      for (int i = 0; i < 16; ++i) {
        const int rb = i >> 2, cb = ((i >> 1) ^ rb) & 1, s = ((i + 1) >> 1) & 1;
        acc[rb][cb] = MFMA(b[s][cb], a[s][rb], acc[rb][cb]);
      }
    };
    unsigned fo = lane < 16 && lane != hp ? WS_FLAGS + (unsigned)(rt * 16 + lane) * FLS : 0u;
    const u64 ko = korder(bid & 3, hp);
    unsigned kf = kfirst(ko), kf2 = kfirst(ko >> 16) | (1u << knib(ko, 9));
    unsigned jo = (unsigned)((((rt & 7) * 128 + (w - 1) * 64 + lane) * 3072 + (hp & 3) * 1024 + (hp | 3) * 64) * 2);
    unsigned fo1 = ((kf >> (lane & 31)) & 1) ? fo : 0u, fo2 = ((kf2 >> (lane & 31)) & 1) ? fo : 0u;
    fo = (((kf | kf2) >> (lane & 31)) & 1) ? 0u : fo;
    asm volatile("" : "+v"(fo), "+v"(fo1), "+v"(fo2));
    asm volatile("" : "+s"(kf), "+s"(kf2));
    publish(rsW, tid, rt * 16 + hp, E + (unsigned)(layer + 1));
    ST(layer * 24 + 13)
    u64 fl1;
    asm volatile("" : "=v"(fl1));
    if (w == 0) asm volatile("global_load_dwordx2 %0, %1, %2 sc1" : "+v"(fl1) : "v"(fo1), "s"(ws) : "memory");
    if (layer == 0) {
      const unsigned xo = (unsigned)(((m0 + wm * 64 + fr) * DM + hp * 64 + wn * 32 + g * 8) * 2);
      asm volatile("global_load_dwordx4 v[64:67], %1, %3\n\tglobal_load_dwordx4 v[68:71], %1, %4\n\tglobal_load_dwordx4 v[72:75], %2, %3\n\tglobal_load_dwordx4 v[76:79], %2, %4"
                   : "+{v[64:79]}"(xq) : "v"(xo), "v"(xo + 32 * DM * 2), "s"(x0), "s"(x0 + 16 * DM) : "memory");
    }
    loadf(0, fa[0], fb[0]);
    unsigned junk;
    asm volatile("" : "=v"(junk));
    if (layer < 2 && w == 3 && (bid >> 6) == 0) code_touch(junk, bid, lane);
    if (layer == 0 && (hp & 7) < 3 && (w == 1 || w == 2))
      asm volatile("global_load_dword %0, %1, %2" : "+v"(junk) : "v"(jo), "s"(wnx) : "memory");
    __builtin_amdgcn_sched_barrier(0);
    bf16x8 id[2];
#pragma unroll
    for (int cb = 0; cb < 2; ++cb) {
      const unsigned one = (g >> 1) == cb && (g & 1) == (fr >> 3) ? 0x3F80u << ((fr & 1) * 16) : 0u;
      u32x4 t;
#pragma unroll
      for (int j = 0; j < 4; ++j) t[j] = ((fr >> 1) & 3) == j ? one : 0u;
      id[cb] = __builtin_bit_cast(bf16x8, t);
      asm volatile("" : "+v"(id[cb]));
    }
    const int l2 = lane_id(), ch = (l2 >> 5) | ((l2 >> 3) & 2), f2 = l2 & 15;
    unsigned pl = (unsigned)(wm * 8192 + wn * 1024 + ch * 256 + f2 * 16);
    unsigned pg = layer < 3 ? WS_X + layer * (SEQ * DM * 2) + (unsigned)((rt * 16 + hp) * 16384) + pl : 0x7fff0000u;
    pl = sb + OFF_C + (layer < 3 ? pl : (unsigned)((wm * 64 + f2) * 128 + (((wn * 4 + ch) ^ (f2 & 7)) << 4)));
    asm volatile("" : "+v"(pl), "+v"(pg));
    ends();
#pragma unroll
    for (int rb = 0; rb < 4; ++rb) { acc[rb][0] = f32x4{0, 0, 0, 0}; acc[rb][1] = f32x4{0, 0, 0, 0}; }
    mma(fa[0], fb[0]);
    __builtin_amdgcn_sched_barrier(0);
    WAITL0;
    u64 miss = 0;
    if (w == 0) { ARRIVED("+v"(fl1)); if (__ballot(fl1 != E + (unsigned)(layer + 1)) & kf) poll_eq(ws, fo1, E + (unsigned)(layer + 1), kf, kf); miss = 0xffffu & ~(kf | kf2 | (1u << hp)); }
    ST(layer * 24 + 14)
    __builtin_amdgcn_sched_barrier(0);
    BAR;
    __builtin_amdgcn_sched_barrier(0);
    asm volatile("" ::: "memory");
    if (layer == 0) WAITV(0);
    __builtin_amdgcn_sched_barrier(0);
    asm volatile("" : "+{v[64:79]}"(xq));
    const bf16x8 xf[4] = {__builtin_bit_cast(bf16x8, quad<0>(xq)), __builtin_bit_cast(bf16x8, quad<1>(xq)), __builtin_bit_cast(bf16x8, quad<2>(xq)), __builtin_bit_cast(bf16x8, quad<3>(xq))};
#pragma unroll
    for (int rb = 0; rb < 4; ++rb) { acc[rb][0] = MFMA(id[0], xf[rb], acc[rb][0]); acc[rb][1] = MFMA(id[1], xf[rb], acc[rb][1]); }
    if (w == 0) poll_eq(ws, fo2, E + (unsigned)(layer + 1), kf2, kf2);
    u64 fl2;
    asm volatile("" : "=v"(fl2));
    auto step = [&](auto ktc) {
      constexpr int kt = decltype(ktc)::value;
      __builtin_amdgcn_sched_barrier(0);
      if (kt == 5 && miss) {
        ARRIVED("+v"(fl2));
        if (__ballot(fl2 != E + (unsigned)(layer + 1)) & miss) poll_eq(ws, fo, E + (unsigned)(layer + 1), miss, ~0ull);
      }
      WAITL0;
      BAR;
      if (kt == 2 && miss) asm volatile("global_load_dwordx2 %0, %1, %2 sc1" : "+v"(fl2) : "v"(fo), "s"(ws) : "memory");
      if (kt == 0) { ST(layer * 24 + 15) }
      __builtin_amdgcn_sched_barrier(0);
      loadf(kt + 1, fa[(kt + 1) & 1], fb[(kt + 1) & 1]);
      if (kt == 0) return;
      mma(fa[kt & 1], fb[kt & 1]);
#pragma unroll
      for (int i = 0; i < 4; ++i) {
        __builtin_amdgcn_sched_group_barrier(0x008, 1, 0);
        __builtin_amdgcn_sched_group_barrier(0x100, 1, 0);
        __builtin_amdgcn_sched_group_barrier(0x008, 1, 0);
        __builtin_amdgcn_sched_group_barrier(0x100, 3, 0);
      }
#pragma unroll
      for (int i = 0; i < 8; ++i) __builtin_amdgcn_sched_group_barrier(0x008, 1, 0);
    };
    step(IC<0>{}); step(IC<1>{}); step(IC<2>{}); step(IC<3>{}); step(IC<4>{}); step(IC<5>{}); step(IC<6>{}); step(IC<7>{});
    step(IC<8>{}); step(IC<9>{}); step(IC<10>{}); step(IC<11>{}); step(IC<12>{}); step(IC<13>{}); step(IC<14>{});
    WAITV(0);
    asm volatile("" :: "v"(junk));
    __builtin_amdgcn_sched_barrier(0);
    u32x4 xv[4];
    auto put = [&](int rb) {
      auto a = __builtin_amdgcn_permlane16_swap(pk2(acc[rb][0][0], acc[rb][0][1]), pk2(acc[rb][1][0], acc[rb][1][1]), false, false);
      auto b = __builtin_amdgcn_permlane16_swap(pk2(acc[rb][0][2], acc[rb][0][3]), pk2(acc[rb][1][2], acc[rb][1][3]), false, false);
      xv[rb] = u32x4{a[0], b[0], a[1], b[1]};
      __builtin_amdgcn_raw_buffer_store_b128(xv[rb], rsW, pg + (rb & 1) * 2048, (rb >> 1) * 4096, 16);
    };
#pragma unroll
    for (int rb = 0; rb < 4; ++rb)
#pragma unroll
      for (int cb = 0; cb < 2; ++cb) acc[rb][cb] = MFMA(fb[1][0][cb], fa[1][0][rb], acc[rb][cb]);
#pragma unroll
    for (int rb = 0; rb < 4; ++rb) {
      __builtin_amdgcn_sched_barrier(0);
#pragma unroll
      for (int cb = 0; cb < 2; ++cb) acc[rb][cb] = MFMA(fb[1][1][cb], fa[1][1][rb], acc[rb][cb]);
      __builtin_amdgcn_sched_barrier(0);
      if (rb >= 2) put(rb - 2);
    }
    __builtin_amdgcn_sched_barrier(0);
    ST(layer * 24 + 16)
    put(2); put(3);
    __builtin_amdgcn_sched_barrier(0);
#pragma unroll
    for (int rb = 0; rb < 4; ++rb) lds<u32x4>(pl + rb * 2048) = xv[rb];
  }
  if (layer == 3) {
    WAITL0; BAR;
    asm volatile("" ::: "memory");
    const __amdgpu_buffer_rsrc_t rsO = __builtin_amdgcn_make_buffer_rsrc((void*)uni(out), (short)0, SEQ * DM * 2, 0x00020000);
    __builtin_amdgcn_raw_buffer_store_b128(lds<u32x4>(pxr + OFF_C), rsO, rmo, 0, 16);
    __builtin_amdgcn_raw_buffer_store_b128(lds<u32x4>(pxr + OFF_C + 1024), rsO, rmo, 8 * DM * 2, 16);
  }
  ST(layer * 24 + 17)
  if (layer < 3 && w >= 4) {
    WAITV(0); BAR;
    asm volatile("" ::: "memory");
    glds4v(wq2, vB0, vB1, vB0, vB1, sb + 2 * 40960 + 16384 + lw * 6144);
    glds2v(wq2 + 4096, vB0, vB1, sb + 2 * 40960 + 16384 + lw * 6144 + 4096);
    __builtin_amdgcn_s_setprio(3);
    bf16x8 xn[4];
#pragma unroll
    for (int i = 0; i < 4; ++i) xn[i] = lds<bf16x8>(sb + OFF_C + lw * 4096 + lane * 16 + i * 1024);
    const bf16x8 ones = ones8();
    u32x2 h[2];
#pragma unroll
    for (int i = 0; i < 2; ++i) {
      const f32x4 z4 = {0, 0, 0, 0};
      const f32x4 sa = MFMA(ones, xn[2 * i + 1], MFMA(ones, xn[2 * i], z4)), sq = MFMA(xn[2 * i + 1], xn[2 * i + 1], MFMA(xn[2 * i], xn[2 * i], z4));
      const float a = (lane & 1) ? sq[1] : sq[0], b = (lane & 1) ? sq[3] : sq[2];
      h[i] = u32x2{__float_as_uint(sa[0]), __float_as_uint((lane & 2) ? b : a)};
    }
    __builtin_amdgcn_s_setprio(0);
    u64 fl;
    asm volatile("" : "=v"(fl), "+v"(pst) : "v"(h[0]), "v"(h[1]));
    if (lw == 0) asm volatile("global_load_dwordx2 %0, %1, %2 sc1" : "+v"(fl) : "v"(po), "s"(ws) : "memory");
    __builtin_amdgcn_raw_buffer_store_b64(h[0], rsW, pst, 0, 16);
    __builtin_amdgcn_raw_buffer_store_b64(h[1], rsW, pst + 128, 0, 16);
    if (lw == 0) {
      WAITV(2);
      __builtin_amdgcn_sched_barrier(0);
      asm volatile("" : "+v"(fl));
      if (__ballot(fl != E + (unsigned)(layer + 1)) & fm) poll_eq(ws, po, E + (unsigned)(layer + 1), fm, ~0ull);
    }
    BAR;
    glds4c(xa1, vXA, sb + 40960 + lw * 4096); glds4c(xa2, vXA, sb + 2 * 40960 + lw * 4096);
  }
  ST(layer * 24 + 18)
}

__global__ __launch_bounds__(512, 2) void mega_kernel(const u16* __restrict__ x0, const u16* __restrict__ wqkvA,
                                                      const u16* __restrict__ woA, const u16* __restrict__ wqkvB,
                                                      const u16* __restrict__ wphi, const u16* __restrict__ woB,
                                                      u16* __restrict__ out, char* __restrict__ ws) {
  extern __shared__ __attribute__((aligned(16))) char smem[];
  {
    unsigned long long pc;
    unsigned t;
    asm volatile("s_getpc_b64 %0\n.Lmk_pc:\n\tv_lshl_add_u32 %1, %2, 9, %3\n\tv_lshlrev_b32 %1, 2, %1\n\tv_and_b32 %1, 0xffffff80, %1\n\ts_mov_b32 m0, %4\n\t"
                 "v_min_u32 %1, .Lmk_end-.Lmk_pc-4, %1\n\tglobal_load_lds_dword %1, %0" : "=&s"(pc), "=&v"(t) : "s"(blockIdx.x >> 3), "v"(threadIdx.x), "n"(LDS_TOTAL - 1024) : "memory", "m0");
  }
  const int w = __builtin_amdgcn_readfirstlane(threadIdx.x >> 6);
  u64 E = 0;
  {
    const int bid = blockIdx.x, lane = threadIdx.x & 63;
    const int rt = 2 * (bid & 4) + ((bid >> 3) & 7), hp = 4 * (bid & 3) + (bid >> 6), m0 = rt * 128;
    if (w < 4) {
      const unsigned xo = (unsigned)((m0 + hp * 8 + w * 2) * (DM * 2) + lane * 16);
      glds4v(x0, xo, xo, xo, xo, lds_addr(smem) + 122880 + w * 4096);
    } else {
      const int lw = w & 3;
      const int kB = 16 * lw + (lane >> 3), fB = (kB >> 1) & 1;
      const unsigned vB0 = (unsigned)((kB * 3072 + hp * 64 + (((((lane & 7) >> 1) ^ fB) << 1) | (lane & 1)) * 8) * 2);
      const unsigned vB1 = (unsigned)(((kB + 8) * 3072 + hp * 64 + (((((lane & 7) >> 1) ^ (fB | 2)) << 1) | (lane & 1)) * 8) * 2) - 1024u;
      const unsigned vA = (unsigned)(((m0 + lw * 32 + (lane >> 3)) * DM + ((lane & 7) ^ (lane >> 3)) * 8) * 2);
#pragma unroll
      for (int s = 0; s < 2; ++s) {
        const int kt = (rt & 8) + s;
        const unsigned sA = lds_addr(smem) + s * 40960, sB = sA + 16384;
        glds4v(x0 + kt * 64, vA, vA + 15360u, vA + 2 * 15360u, vA + 3 * 15360u, sA + lw * 4096);
        const u16* b = wqkvA + (size_t)kt * 64 * 3072;
        glds4v(b, vB0, vB1, vB0, vB1, sB + lw * 6144);
        glds2v(b + 2048, vB0, vB1, sB + lw * 6144 + 4096);
      }
    }
  }
  f32x16 xq;
  Next nx;
  asm volatile("" : "=s"(nx.kord), "=s"(nx.ko2), "=s"(nx.wq), "=s"(nx.tw));
#pragma unroll 1
  for (int layer = 0; layer < 4; ++layer) {
    if (layer & 1) g1_phase<1>(smem, ws, E, w, xq, nx, layer, x0, wqkvB, woB, wphi, wqkvA, woA);
    else g1_phase<0>(smem, ws, E, w, xq, nx, layer, x0, wqkvA, woA, nullptr, wqkvB, woB);
    g2_phase(smem, ws, E, w, xq, nx, layer, x0, (layer & 1) ? woB : woA, (layer & 1) ? wqkvA : wqkvB, out);
  }
  asm volatile(".Lmk_end:");
  WAITV(0);
  if (blockIdx.x == 0 && w == 0 && lane_id() == 0) __hip_atomic_store((gu64*)ws, E + 16u, __ATOMIC_RELAXED, __HIP_MEMORY_SCOPE_AGENT);
}

extern "C" void kernel_launch(void* const* d_in, const int* in_sizes, int n_in,
                              void* d_out, int out_size, void* d_ws, size_t ws_size,
                              hipStream_t stream) {
  hipFuncSetAttribute((const void*)mega_kernel, hipFuncAttributeMaxDynamicSharedMemorySize, LDS_TOTAL);
  mega_kernel<<<256, 512, LDS_TOTAL, stream>>>((const u16*)d_in[0], (const u16*)d_in[1], (const u16*)d_in[2], (const u16*)d_in[4],
                                               (const u16*)d_in[5], (const u16*)d_in[6], (u16*)d_out, (char*)d_ws);
}
```

```cpp
#include <hip/hip_runtime.h>
#include <hip/hip_bf16.h>
#include <stdint.h>
#include <type_traits>
#pragma clang diagnostic ignored "-Winline-asm"
#pragma clang diagnostic ignored "-Wunused-value"

typedef unsigned short u16;
typedef __attribute__((ext_vector_type(8))) short bf16x8;
typedef __attribute__((ext_vector_type(4))) short bf16x4;
typedef __attribute__((ext_vector_type(4))) float f32x4;
typedef __attribute__((ext_vector_type(4))) unsigned u32x4;
typedef __attribute__((ext_vector_type(2))) unsigned u32x2;
typedef __attribute__((ext_vector_type(8))) float f32x8;
typedef __attribute__((ext_vector_type(16))) unsigned u32x16;
typedef __attribute__((ext_vector_type(16))) float f32x16;
typedef __attribute__((ext_vector_type(32))) float f32x32;
template <int I, typename T> __device__ __forceinline__ f32x4 quad(const T& v) { return __builtin_shufflevector(v, v, 4 * I, 4 * I + 1, 4 * I + 2, 4 * I + 3); }

constexpr int SEQ = 2048, DM = 1024, NH = 32;
#define ST(i)
constexpr int KVP = 64 * 32 + 64;

typedef __attribute__((ext_vector_type(2))) float f32x2;
typedef __attribute__((ext_vector_type(2))) __bf16 bf16x2_t;
__device__ __forceinline__ unsigned pk2(float a, float b) {
  f32x2 v = {a, b};
  bf16x2_t r = __builtin_convertvector(v, bf16x2_t);
  return __builtin_bit_cast(unsigned, r);
}

__device__ __forceinline__ bf16x4 tr_read(const char* p) {
  return __builtin_amdgcn_ds_read_tr16_b64_v4i16((__attribute__((address_space(3))) bf16x4*)p);
}
template <typename T> __device__ __forceinline__ __attribute__((address_space(3))) T& lds(unsigned a) { return *(__attribute__((address_space(3))) T*)(size_t)a; }
__device__ __forceinline__ bf16x4 tr_read(unsigned a) { return __builtin_amdgcn_ds_read_tr16_b64_v4i16(&lds<bf16x4>(a)); }
__device__ __forceinline__ bf16x8 cat8(bf16x4 a, bf16x4 b) {
  bf16x8 f = {a[0], a[1], a[2], a[3], b[0], b[1], b[2], b[3]};
  return f;
}
__device__ __forceinline__ void glds16s(const void* sbase, unsigned voff, unsigned lds_base) {
  asm volatile("s_mov_b32 m0, %2\n\ts_nop 0\n\tglobal_load_lds_dwordx4 %0, %1" :: "v"(voff), "s"(sbase), "s"(lds_base) : "memory", "m0");
}
__device__ __forceinline__ void glds16c(const void* sbase, unsigned voff, unsigned lds_base) {
  asm volatile("s_mov_b32 m0, %2\n\ts_nop 0\n\tglobal_load_lds_dwordx4 %0, %1 sc1" :: "v"(voff), "s"(sbase), "s"(lds_base) : "memory", "m0");
}
#define GL_ "\n\tglobal_load_lds_dwordx4 "
__device__ __forceinline__ void glds4c(const void* sbase, unsigned v, unsigned lds_base) {
  asm volatile("s_mov_b32 m0, %2\n\ts_nop 0" GL_ "%0, %1 sc1" GL_ "%0, %1 offset:1024 sc1" GL_ "%0, %1 offset:2048 sc1" GL_ "%0, %1 offset:3072 sc1"
               :: "v"(v), "s"(sbase), "s"(lds_base) : "memory", "m0");
}
__device__ __forceinline__ void glds4v(const void* sbase, unsigned v0, unsigned v1, unsigned v2, unsigned v3, unsigned lds_base) {
  asm volatile("s_mov_b32 m0, %5\n\ts_nop 0" GL_ "%0, %4 sc1" GL_ "%1, %4 offset:1024 sc1" GL_ "%2, %4 offset:2048 sc1" GL_ "%3, %4 offset:3072 sc1"
               :: "v"(v0), "v"(v1), "v"(v2), "v"(v3), "s"(sbase), "s"(lds_base) : "memory", "m0");
}
__device__ __forceinline__ void glds2v(const void* sbase, unsigned v0, unsigned v1, unsigned lds_base) {
  asm volatile("s_mov_b32 m0, %3\n\ts_nop 0" GL_ "%0, %2 sc1" GL_ "%1, %2 offset:1024 sc1" :: "v"(v0), "v"(v1), "s"(sbase), "s"(lds_base) : "memory", "m0");
}
__device__ __forceinline__ void gld16(u32x4& d, unsigned voff, const void* sbase) {
  asm volatile("global_load_dwordx4 %0, %1, %2" : "=v"(d) : "v"(voff), "s"(sbase) : "memory");
}
__device__ __forceinline__ unsigned lds_addr(const void* p) { return (unsigned)(size_t)p; }
#define WAITV(n) asm volatile("s_waitcnt vmcnt(" #n ")" ::: "memory")
#define WAITVN(n) asm volatile("s_waitcnt vmcnt(%0)" :: "n"(n) : "memory")
#define WAITL0 do { asm volatile("" ::: "memory"); __builtin_amdgcn_s_waitcnt(0xC07F); asm volatile("" ::: "memory"); } while (0)
#define BAR __builtin_amdgcn_s_barrier()
#define ARRIVED(...) do { WAITV(0); __builtin_amdgcn_sched_barrier(0); asm volatile("" : __VA_ARGS__); } while (0)

template <int N> __device__ __forceinline__ float dpp_ror(float x) {
  return __uint_as_float(__builtin_amdgcn_update_dpp(0u, __float_as_uint(x), 0x120 + N, 0xf, 0xf, false));
}
__device__ __forceinline__ float r16_sum(float x) { x += dpp_ror<8>(x); x += dpp_ror<4>(x); x += dpp_ror<2>(x); return x + dpp_ror<1>(x); }
__device__ __forceinline__ void max8pn(const f32x4& a, const f32x4& b, float& mx, float& mn) {
  asm("v_max3_f32 %0, %2, %3, %4\n\tv_max3_f32 %1, -%2, -%3, -%4\n\tv_max3_f32 %0, %0, %5, %6\n\tv_max3_f32 %1, %1, -%5, -%6\n\t"
      "v_max3_f32 %0, %0, %7, %8\n\tv_max3_f32 %1, %1, -%7, -%8\n\tv_max_f32 %0, %0, %9\n\tv_max_f32 %1, %1, -%9\n\ts_nop 0"
      : "=&v"(mx), "=&v"(mn) : "v"(a[0]), "v"(a[1]), "v"(a[2]), "v"(a[3]), "v"(b[0]), "v"(b[1]), "v"(b[2]), "v"(b[3]));
}
__device__ __forceinline__ float vmaxf_n(float a, float b) { float r; asm("v_max_f32 %0, %1, %2\n\ts_nop 0" : "=v"(r) : "v"(a), "v"(b)); return r; }
template <bool MAXOP> __device__ __forceinline__ float rop(float a, float b) { return MAXOP ? vmaxf_n(a, b) : a + b; }
template <bool MAXOP> __device__ __forceinline__ void xr4(float& a, float& b, float& c, float& d) {
  auto r = __builtin_amdgcn_permlane16_swap(__float_as_uint(a), __float_as_uint(b), false, false);
  auto q = __builtin_amdgcn_permlane16_swap(__float_as_uint(c), __float_as_uint(d), false, false);
  const unsigned ab = __float_as_uint(rop<MAXOP>(__uint_as_float(r[0]), __uint_as_float(r[1])));
  const unsigned cd = __float_as_uint(rop<MAXOP>(__uint_as_float(q[0]), __uint_as_float(q[1])));
  auto t = __builtin_amdgcn_permlane32_swap(ab, cd, false, false);
  const unsigned u = __float_as_uint(rop<MAXOP>(__uint_as_float(t[0]), __uint_as_float(t[1])));
  auto h = __builtin_amdgcn_permlane32_swap(u, u, false, false);
  auto p0 = __builtin_amdgcn_permlane16_swap(h[0], h[0], false, false);
  auto p1 = __builtin_amdgcn_permlane16_swap(h[1], h[1], false, false);
  a = __uint_as_float(p0[0]); b = __uint_as_float(p0[1]); c = __uint_as_float(p1[0]); d = __uint_as_float(p1[1]);
}

__device__ __forceinline__ unsigned xs4(float a, float b, float c, float d) {
  auto r = __builtin_amdgcn_permlane16_swap(__float_as_uint(a), __float_as_uint(b), false, false);
  auto q = __builtin_amdgcn_permlane16_swap(__float_as_uint(c), __float_as_uint(d), false, false);
  auto t = __builtin_amdgcn_permlane32_swap(__float_as_uint(__uint_as_float(r[0]) + __uint_as_float(r[1])), __float_as_uint(__uint_as_float(q[0]) + __uint_as_float(q[1])), false, false);
  return __float_as_uint(__uint_as_float(t[0]) + __uint_as_float(t[1]));
}

__device__ __forceinline__ f32x4 sum4(const u32x4 (&v)[4]) {
  float r0, r1, r2, r3, t0, t1, t2, t3;
  asm("v_add_f32 %0, %8, %12\n\tv_add_f32 %1, %9, %13\n\tv_add_f32 %2, %10, %14\n\tv_add_f32 %3, %11, %15\n\t"
      "v_add_f32 %4, %16, %20\n\tv_add_f32 %5, %17, %21\n\tv_add_f32 %6, %18, %22\n\tv_add_f32 %7, %19, %23\n\t"
      "v_add_f32 %0, %0, %4\n\tv_add_f32 %1, %1, %5\n\tv_add_f32 %2, %2, %6\n\tv_add_f32 %3, %3, %7\n\ts_nop 0"
      : "=&v"(r0), "=&v"(r1), "=&v"(r2), "=&v"(r3), "=&v"(t0), "=&v"(t1), "=&v"(t2), "=&v"(t3)
      : "v"(v[0][0]), "v"(v[0][1]), "v"(v[0][2]), "v"(v[0][3]), "v"(v[1][0]), "v"(v[1][1]), "v"(v[1][2]), "v"(v[1][3]),
        "v"(v[2][0]), "v"(v[2][1]), "v"(v[2][2]), "v"(v[2][3]), "v"(v[3][0]), "v"(v[3][1]), "v"(v[3][2]), "v"(v[3][3]));
  return f32x4{r0, r1, r2, r3};
}
#define MFMA(a, b, c) __builtin_amdgcn_mfma_f32_16x16x32_bf16(a, b, c, 0, 0, 0)

typedef unsigned long long u64;
typedef __attribute__((address_space(1))) unsigned long long gu64;
constexpr unsigned WS_FLAGS = 4096;
constexpr int FLS = 64;
constexpr unsigned WS_G0 = 65536;
constexpr unsigned WS_CSP = WS_G0 + SEQ * 16;
constexpr unsigned WS_ST = WS_CSP + 16 * 16 * 768;
constexpr unsigned WS_X = 1u << 21;
constexpr unsigned WS_O = WS_X + 3u * SEQ * DM * 2;
constexpr unsigned KVPB = KVP * 2, KVFB = KVP * 4;
constexpr unsigned EMPTY = 0xffffffffu;
constexpr unsigned WS_KV = WS_O + 4u * SEQ * DM * 2;
constexpr unsigned KVLB = 16u * NH * KVPB + 16u * KVFB;
constexpr unsigned WS_HL = WS_KV + 2u * KVLB;
constexpr unsigned WS_CSA = WS_HL + 512u * 4096;
constexpr unsigned WS_CPA = WS_CSA + 256u * 1024;
constexpr unsigned WS_IMG = WS_CPA + 16 * 16 * 768;
constexpr unsigned WS_END = WS_IMG + 2048 * 1024;
constexpr int LDS_TOTAL = 163840;
constexpr int OFF_WO = 49152;
struct Next { u64 kord, ko2; const char* wq; unsigned tw, kf; };
template <int V> using IC = std::integral_constant<int, V>;
__device__ __forceinline__ bf16x8 ones8() { unsigned e = 0x3F803F80u; asm volatile("" : "+v"(e)); return __builtin_bit_cast(bf16x8, u32x4{e, e, e, e}); }
__device__ __forceinline__ float zero1() { float z; asm volatile("v_mov_b32 %0, 0" : "=v"(z)); return z; }
__device__ __forceinline__ u32x4 empty4() { unsigned e = EMPTY; asm volatile("" : "+v"(e)); return u32x4{e, e, e, e}; }
__device__ __forceinline__ u64 korder(int v, int own) {
  const u64 t = v == 0 ? 0x3b210fedca987654ull : v == 1 ? 0xb37654210fedca98ull : v == 2 ? 0x3ba987654210fedcull : 0xb3fedca987654210ull;
  const u64 inv = v == 0 ? 0xa987e6543210fdcbull : v == 1 ? 0x6543f210dcbae987ull : v == 2 ? 0x3210edcba987f654ull : 0xdcbaf9876543e210ull;
  const u64 low = (1ull << (4 * ((inv >> (4 * own)) & 15))) - 1ull;
  return (u64)own | ((t & low) << 4) | (t & ~((low << 4) | 15ull));
}
__device__ __forceinline__ u64 korder0(int rt) {
  const int r = rt & 7;
  const u64 own = (0x765432765432ull >> (4 * (r < 6 ? r : r - 6))) & 0xffffffull, oth = 0x7654321076543210ull >> (4 * r);
  const u64 t = 0x10ull | ((oth & 15ull) << 8) | (own << 12) | ((oth >> 4) << 36);
  return t | (rt < 8 ? 0x8888888000000800ull : 0x0000000888888088ull);
}
__device__ __forceinline__ int knib(u64 t, int s) { return (int)(t >> (4 * s)) & 15; }
__device__ __forceinline__ unsigned kfirst(u64 t) { return (1u << knib(t, 1)) | (1u << knib(t, 2)) | (1u << knib(t, 3)) | (1u << knib(t, 4)); }
__device__ __forceinline__ int kv1(int bid) { return (bid + (0x4924 >> ((bid >> 2) & 14))) & 3; }

__device__ __forceinline__ bool img(int layer, int hp) { return layer >= 2 && (hp & 7) == 3; }
__device__ __forceinline__ const char* wq_src(const char* ws, const void* W, int layer, int hp) { return img(layer, hp) ? ws + WS_IMG + ((layer & 1) * 768 + (hp >> 3) * 384) * 1024 : (const char*)W; }
__device__ __forceinline__ const char* wo_src(const char* ws, const void* W, int layer, int hp) { return img(layer, hp) ? ws + WS_IMG + (1536 + (layer & 1) * 256 + (hp >> 3) * 128) * 1024 : (const char*)W; }

template <typename T> __device__ __forceinline__ T* uni(T* p) {
  const unsigned long long v = (unsigned long long)p;
  const unsigned lo = __builtin_amdgcn_readfirstlane((unsigned)v), hi = __builtin_amdgcn_readfirstlane((unsigned)(v >> 32));
  return (T*)(((unsigned long long)hi << 32) | lo);
}
__device__ __forceinline__ int lane_id() {
  int l;
  asm volatile("v_mbcnt_lo_u32_b32 %0, -1, 0\n\tv_mbcnt_hi_u32_b32 %0, -1, %0" : "=v"(l));
  __builtin_assume(l >= 0 && l < 64);
  return l;
}
#define IDS \
  const int lane = lane_id(); \
  asm volatile("" : "+s"(w)); \
  const int tid = w * 64 + lane; \
  int bid = blockIdx.x; asm volatile("" : "+s"(bid)); \
  ws = uni(ws); asm volatile("" : "+s"(ws)); \
  layer = __builtin_amdgcn_readfirstlane(layer); x0 = uni(x0); wo = uni(wo); \
    \
  const int rt = 2 * (bid & 4) + ((bid >> 3) & 7); \
  const int hp = 4 * (bid & 3) + (bid >> 6); \
  const int m0 = rt * 128; \
  const int fr = lane & 15, g = lane >> 4; \
  const int q4 = (lane & 15) >> 2, p4 = lane & 3; \
  const __amdgpu_buffer_rsrc_t rsW = __builtin_amdgcn_make_buffer_rsrc((void*)ws, (short)0, (int)WS_END, 0x00020000); \
  (void)m0; (void)fr; (void)q4; (void)p4; (void)rsW; (void)g;

__device__ __forceinline__ void code_touch(unsigned& junk, int bid, int lane) {
  unsigned long long pc;
  unsigned len, d;
  asm volatile("s_getpc_b64 %0\n.Lct%=:\n\ts_mov_b32 %2, .Lct%=-.Lmk_pc\n\ts_mov_b32 %1, .Lmk_end-.Lmk_pc" : "=s"(pc), "=s"(len), "=s"(d));
  pc -= d;
  const unsigned per = ((len >> 10) + 1) * 128, off = ((bid >> 3) & 7) * per + (lane * 128 < per ? lane * 128 : 0);
  asm volatile("global_load_dword %0, %1, %2" : "+v"(junk) : "v"(off < len - 4 ? off : 0u), "s"(pc) : "memory");
}

__device__ __forceinline__ void publish(__amdgpu_buffer_rsrc_t rsW, int tid, unsigned idx, u64 v) {
  unsigned off = WS_FLAGS + idx * FLS;
  asm volatile("" : "+v"(off), "+v"(v));
  WAITV(0);
  WAITL0; BAR;
  if (tid == 192) __builtin_amdgcn_raw_buffer_store_b64(__builtin_bit_cast(u32x2, v), rsW, off, 0, 16);
  asm volatile("" ::: "memory");
}
__device__ __forceinline__ void poll_flags(char* ws, u64 E, int set, int rt, int hp, int lane, unsigned n, bool col) {
  const int l = lane & 15;
  const unsigned fo = WS_FLAGS + (unsigned)(set * 256 + (col ? l * 16 + hp : rt * 16 + l)) * FLS;
  _Pragma("unroll 1") for (unsigned spins = 0; spins < (1u << 18); ++spins) {
    u64 d;
    asm volatile("global_load_dwordx2 %0, %1, %2 sc1\n\ts_waitcnt vmcnt(0)" : "=v"(d) : "v"(fo), "s"(ws) : "memory");
    if (__all(d - (E + n) <= (u64)(16u - n))) break;
    __builtin_amdgcn_s_sleep(1);
  }
}

__device__ __forceinline__ u64 poll_eq(const char* ws, unsigned off, u64 want, u64 mask, u64 now) {
  u64 miss;
  _Pragma("unroll 1") for (unsigned spins = 0; spins < (1u << 18); ++spins) {
    u64 d;
    asm volatile("global_load_dwordx2 %0, %1, %2 sc1\n\ts_waitcnt vmcnt(0)" : "=v"(d) : "v"(off), "s"(ws) : "memory");
    miss = __ballot(d != want) & mask;
    if (!(miss & now)) break;
    __builtin_amdgcn_s_sleep(1);
  }
  return miss;
}

#define G1_MACROS \
  ".macro G1M rb, j, s, z\n" \
  " .set g1d, 160+24*(\\rb)+4*(((\\j)+2)-6*((\\j)>>2))\n .set g1a, 80+40*(\\s)+4*(\\rb)\n .set g1b, 96+40*(\\s)+4*(\\j)\n" \
  " .if ((\\j)>>1) == 1\n  .set g1t, g1a\n  .set g1a, g1b\n  .set g1b, g1t\n .endif\n" \
  " .if \\z\n  v_mfma_f32_16x16x32_bf16 v[g1d:g1d+3], v[g1b:g1b+3], v[g1a:g1a+3], 0\n" \
  " .else\n  v_mfma_f32_16x16x32_bf16 v[g1d:g1d+3], v[g1b:g1b+3], v[g1a:g1a+3], v[g1d:g1d+3]\n .endif\n" \
  ".endm\n" \
  ".macro G1RA rb, s, ra, oa\n" \
  " ds_read_b128 v[80+40*(\\s)+4*(\\rb):83+40*(\\s)+4*(\\rb)], \\ra offset:(\\oa)+(\\rb)*2048\n" \
  ".endm\n" \
  ".macro G1RB k, s, r0, r1, ob\n" \
  " .if (\\k) & 2\n  ds_read_b64_tr_b16 v[96+40*(\\s)+2*(\\k):97+40*(\\s)+2*(\\k)], \\r1 offset:(\\ob)+(((\\k)>>2)+1-3*((\\k)>>3))*2048+((\\k)&1)*512\n" \
  " .else\n  ds_read_b64_tr_b16 v[96+40*(\\s)+2*(\\k):97+40*(\\s)+2*(\\k)], \\r0 offset:(\\ob)+(((\\k)>>2)+1-3*((\\k)>>3))*2048+((\\k)&1)*512\n .endif\n" \
  ".endm\n" \
    \
  ".macro G1BK i, g, r0, r1, ob\n G1RB 4*(\\g)+((\\i)&3), (\\i)>>2, \\r0, \\r1, (\\ob)+((\\i)>>2)*12288\n.endm\n" \
  ".macro G1AK rb, s, a0, a1, oa\n .if \\s\n  G1RA \\rb, 1, \\a1, \\oa\n .else\n  G1RA \\rb, 0, \\a0, \\oa\n .endif\n.endm\n" \
  ".macro G1RDB g, r0, r1, ob\n .set g1i, 0\n .rept 8\n  G1BK g1i, \\g, \\r0, \\r1, \\ob\n  .set g1i, g1i+1\n .endr\n.endm\n" \
  ".macro G1RDA a0, a1, oa\n .set g1i, 0\n .rept 8\n  G1AK (g1i>>1)^2, g1i&1, \\a0, \\a1, \\oa\n  .set g1i, g1i+1\n .endr\n.endm\n" \
    \
  ".macro G1Q g, rb, q, i, z\n G1M \\rb, 2*(\\g)+((((\\i)>>1)^(\\q))&1), (((\\i)+1)>>1)&1, (\\z)&&(((\\i)&1)==0)\n.endm\n" \
    \
  ".macro G1SG g, z, rd, a0, a1, r0, r1, o\n" \
  " .set g1i, 0\n .rept 16\n" \
  "  .if ((\\g) == 0) && (g1i == 8) && (\\rd)\n   s_waitcnt lgkmcnt(7)\n   s_nop 0\n  .endif\n" \
  "  .if ((\\g) == 0) && (g1i == 15)\n   s_waitcnt lgkmcnt(0)\n  .endif\n" \
  "  G1Q \\g, (g1i>>2)^(2-((\\g)&1)), g1i>>2, g1i&3, \\z\n" \
  "  .if \\rd\n" \
  "   .if (\\g) == 0\n    .if g1i < 7\n     G1BK g1i, 2, \\r0, \\r1, \\o\n    .elseif g1i == 8\n     G1BK 7, 2, \\r0, \\r1, \\o\n    .endif\n" \
  "   .elseif (\\g) == 1\n    .if g1i < 8\n     G1BK g1i, 0, \\r0, \\r1, \\o\n    .endif\n" \
  "   .elseif (g1i & 2) == 0\n    G1BK (g1i&1)+2*(g1i>>2), 1, \\r0, \\r1, \\o\n" \
  "   .elseif (\\rd) == 1\n    G1AK (g1i>>2)^2, 1-(g1i&1), \\a0, \\a1, \\o\n   .endif\n" \
  "  .endif\n" \
  "  .set g1i, g1i+1\n .endr\n" \
  ".endm\n" \
  ""
#define G1_SLIDE \
  "G1Q 1, 3, 0, 0, 0\n ds_read2_b64 v[60:63], v49 offset0:32 offset1:48\n ds_read_b128 v[96:99], v48 offset:256\n G1Q 1, 3, 0, 1, 0\n " \
  "ds_read_b128 v[100:103], v48 offset:320\n ds_read2_b32 v[56:57], v52 offset1:16\n G1Q 1, 3, 0, 2, 0\n ds_read_b128 v[136:139], v54 offset:192\n " \
  "ds_read_b128 v[140:143], v54 offset:704\n G1Q 1, 3, 0, 3, 0\n G1Q 1, 2, 1, 0, 0\n G1Q 1, 2, 1, 1, 0\n s_waitcnt lgkmcnt(3)\n " \
  "v_fmac_f32 v240, v96, v62\n G1Q 1, 2, 1, 2, 0\n v_fma_f32 v241, v97, v62, v241\n v_fma_f32 v242, v98, v62, v242\n G1Q 1, 2, 1, 3, 0\n " \
  "v_fma_f32 v243, v99, v62, v243\n v_fma_f32 v244, v100, v62, v244\n G1Q 1, 1, 2, 0, 0\n v_fma_f32 v245, v101, v62, v245\n " \
  "v_fma_f32 v246, v102, v62, v246\n G1Q 1, 1, 2, 1, 0\n v_fma_f32 v247, v103, v62, v247\n v_mul_f32_e64 v240, v63, v240\n G1Q 1, 1, 2, 2, 0\n " \
  "v_mul_f32_e64 v241, v63, v241\n v_mul_f32_e64 v242, v63, v242\n G1Q 1, 1, 2, 3, 0\n v_mul_f32_e64 v243, v63, v243\n v_mul_f32_e64 v244, v63, v244\n " \
  "G1Q 1, 0, 3, 0, 0\n v_mul_f32_e64 v245, v63, v245\n v_mul_f32_e64 v246, v63, v246\n G1Q 1, 0, 3, 1, 0\n v_mul_f32_e64 v247, v63, v247\n " \
  "v_cvt_pk_bf16_f32 v240, v240, v241\n G1Q 1, 0, 3, 2, 0\n v_cvt_pk_bf16_f32 v241, v242, v243\n v_cvt_pk_bf16_f32 v242, v244, v245\n " \
  "G1Q 1, 0, 3, 3, 0\n v_cvt_pk_bf16_f32 v243, v246, v247\n s_waitcnt lgkmcnt(0)\n v_fmac_f32 v248, v136, v56\n G1Q 2, 2, 0, 0, 0\n " \
  "ds_read2_b64 v[144:147], v49 offset1:16\n v_fma_f32 v249, v137, v56, v249\n G1Q 2, 2, 0, 1, 0\n ds_read_b128 v[148:151], v48\n " \
  "v_fma_f32 v250, v138, v56, v250\n G1Q 2, 2, 0, 2, 0\n ds_read_b128 v[104:107], v48 offset:64\n v_fma_f32 v251, v139, v56, v251\n G1Q 2, 2, 0, 3, 0\n " \
  "ds_read_b128 v[108:111], v54 offset:128\n v_fma_f32 v252, v136, v57, v252\n G1Q 2, 3, 1, 0, 0\n v_fma_f32 v253, v137, v57, v253\n " \
  "v_fma_f32 v254, v138, v57, v254\n G1Q 2, 3, 1, 1, 0\n ds_read_b128 v[88:91], v54 offset:640\n v_fma_f32 v255, v139, v57, v255\n G1Q 2, 3, 1, 2, 0\n " \
  "ds_read_b128 v[128:131], v54\n v_mul_f32_e64 v248, v140, v248\n G1Q 2, 3, 1, 3, 0\n v_mul_f32_e64 v249, v141, v249\n v_mul_f32_e64 v250, v142, v250\n " \
  "G1Q 2, 0, 2, 0, 0\n v_mul_f32_e64 v251, v143, v251\n v_mul_f32_e64 v252, v140, v252\n G1Q 2, 0, 2, 1, 0\n ds_read_b128 v[92:95], v54 offset:512\n " \
  "v_mul_f32_e64 v253, v141, v253\n G1Q 2, 0, 2, 2, 0\n v_mul_f32_e64 v254, v142, v254\n v_mul_f32_e64 v255, v143, v255\n G1Q 2, 0, 2, 3, 0\n " \
  "v_cvt_pk_bf16_f32 v248, v248, v249\n v_cvt_pk_bf16_f32 v249, v250, v251\n G1Q 2, 1, 3, 0, 0\n v_cvt_pk_bf16_f32 v255, v254, v255\n " \
  "v_cvt_pk_bf16_f32 v254, v252, v253\n G1Q 2, 1, 3, 1, 0\n v_min_u32_e64 v136, -2, v240\n v_min_u32_e64 v137, -2, v241\n v_min_u32_e64 v138, -2, v242\n " \
  "G1Q 2, 1, 3, 2, 0\n v_min_u32_e64 v139, -2, v243\n v_min_u32_e64 v140, -2, v248\n v_min_u32_e64 v141, -2, v249\n G1Q 2, 1, 3, 3, 0\n " \
  "v_min_u32_e64 v142, -2, v254\n v_min_u32_e64 v143, -2, v255\n ds_read_b128 v[112:115], v54 offset:64\n ds_read_b128 v[116:119], v54 offset:576\n " \
  "s_cmp_eq_u32 %[wm], 0\n s_cbranch_scc1 .Lg1d%=\n buffer_store_dwordx4 v[136:139], v55, %[rs], 0 offen sc1\n " \
  "buffer_store_dwordx4 v[140:143], v55, %[rs], 0 offen offset:1024 sc1\n ds_read_b32 v53, v59\n ds_read_b32 v53, v59\n ds_read_b32 v53, v59\n " \
  "s_branch .Lg1e%=\n .Lg1d%=:\n v_mov_b32 v53, 1\n ds_write_b128 v58, v[136:139]\n ds_write_b128 v58, v[140:143] offset:1024\n ds_write_b32 v59, v53\n " \
  ".Lg1e%=:\n s_waitcnt lgkmcnt(8)\n v_pk_fma_f32 v[232:233], v[148:149], v[62:63], v[232:233] op_sel_hi:[1,0,1]\n " \
  "v_pk_fma_f32 v[234:235], v[150:151], v[62:63], v[234:235] op_sel_hi:[1,0,1]\n " \
  "v_pk_fma_f32 v[236:237], v[104:105], v[62:63], v[236:237] op_sel_hi:[1,0,1]\n " \
  "v_pk_fma_f32 v[238:239], v[106:107], v[62:63], v[238:239] op_sel_hi:[1,0,1]\n " \
  "v_pk_fma_f32 v[216:217], v[96:97], v[60:61], v[216:217] op_sel_hi:[1,0,1]\n " \
  "v_pk_fma_f32 v[218:219], v[98:99], v[60:61], v[218:219] op_sel_hi:[1,0,1]\n " \
  "v_pk_fma_f32 v[220:221], v[100:101], v[60:61], v[220:221] op_sel_hi:[1,0,1]\n " \
  "v_pk_fma_f32 v[222:223], v[102:103], v[60:61], v[222:223] op_sel_hi:[1,0,1]\n v_pk_mul_f32 v[216:217], v[60:61], v[216:217] op_sel:[1,0]\n " \
  "v_pk_mul_f32 v[218:219], v[60:61], v[218:219] op_sel:[1,0]\n v_pk_mul_f32 v[220:221], v[60:61], v[220:221] op_sel:[1,0]\n " \
  "v_pk_mul_f32 v[222:223], v[60:61], v[222:223] op_sel:[1,0]\n v_cvt_pk_bf16_f32 v232, v232, v233\n v_cvt_pk_bf16_f32 v233, v234, v235\n " \
  "v_cvt_pk_bf16_f32 v234, v236, v237\n v_cvt_pk_bf16_f32 v235, v238, v239\n v_cvt_pk_bf16_f32 v216, v216, v217\n v_cvt_pk_bf16_f32 v217, v218, v219\n " \
  "v_cvt_pk_bf16_f32 v218, v220, v221\n v_cvt_pk_bf16_f32 v219, v222, v223\n " \
  "v_pk_fma_f32 v[208:209], v[148:149], v[60:61], v[208:209] op_sel_hi:[1,0,1]\n " \
  "v_pk_fma_f32 v[210:211], v[150:151], v[60:61], v[210:211] op_sel_hi:[1,0,1]\n " \
  "v_pk_fma_f32 v[212:213], v[104:105], v[60:61], v[212:213] op_sel_hi:[1,0,1]\n " \
  "v_pk_fma_f32 v[214:215], v[106:107], v[60:61], v[214:215] op_sel_hi:[1,0,1]\n " \
  "v_pk_fma_f32 v[192:193], v[96:97], v[146:147], v[192:193] op_sel_hi:[1,0,1]\n " \
  "v_pk_fma_f32 v[194:195], v[98:99], v[146:147], v[194:195] op_sel_hi:[1,0,1]\n " \
  "v_pk_fma_f32 v[196:197], v[100:101], v[146:147], v[196:197] op_sel_hi:[1,0,1]\n " \
  "v_pk_fma_f32 v[198:199], v[102:103], v[146:147], v[198:199] op_sel_hi:[1,0,1]\n v_pk_mul_f32 v[192:193], v[146:147], v[192:193] op_sel:[1,0]\n " \
  "v_pk_mul_f32 v[194:195], v[146:147], v[194:195] op_sel:[1,0]\n v_pk_mul_f32 v[196:197], v[146:147], v[196:197] op_sel:[1,0]\n " \
  "v_pk_mul_f32 v[198:199], v[146:147], v[198:199] op_sel:[1,0]\n v_mfma_f32_16x16x32_bf16 v[120:123], v[240:243], v[232:235], v[40:43]\n " \
  "v_mfma_f32_16x16x32_bf16 v[236:239], v[216:219], v[232:235], v[36:39]\n v_cvt_pk_bf16_f32 v208, v208, v209\n v_cvt_pk_bf16_f32 v209, v210, v211\n " \
  "v_cvt_pk_bf16_f32 v210, v212, v213\n v_cvt_pk_bf16_f32 v211, v214, v215\n v_cvt_pk_bf16_f32 v192, v192, v193\n v_cvt_pk_bf16_f32 v193, v194, v195\n " \
  "v_cvt_pk_bf16_f32 v194, v196, v197\n v_cvt_pk_bf16_f32 v195, v198, v199\n " \
  "v_pk_fma_f32 v[184:185], v[148:149], v[146:147], v[184:185] op_sel_hi:[1,0,1]\n " \
  "v_pk_fma_f32 v[186:187], v[150:151], v[146:147], v[186:187] op_sel_hi:[1,0,1]\n " \
  "v_pk_fma_f32 v[188:189], v[104:105], v[146:147], v[188:189] op_sel_hi:[1,0,1]\n " \
  "v_pk_fma_f32 v[190:191], v[106:107], v[146:147], v[190:191] op_sel_hi:[1,0,1]\n " \
  "v_pk_fma_f32 v[168:169], v[96:97], v[144:145], v[168:169] op_sel_hi:[1,0,1]\n " \
  "v_pk_fma_f32 v[170:171], v[98:99], v[144:145], v[170:171] op_sel_hi:[1,0,1]\n " \
  "v_pk_fma_f32 v[172:173], v[100:101], v[144:145], v[172:173] op_sel_hi:[1,0,1]\n " \
  "v_pk_fma_f32 v[174:175], v[102:103], v[144:145], v[174:175] op_sel_hi:[1,0,1]\n v_pk_mul_f32 v[168:169], v[144:145], v[168:169] op_sel:[1,0]\n " \
  "v_pk_mul_f32 v[170:171], v[144:145], v[170:171] op_sel:[1,0]\n v_pk_mul_f32 v[172:173], v[144:145], v[172:173] op_sel:[1,0]\n " \
  "v_pk_mul_f32 v[174:175], v[144:145], v[174:175] op_sel:[1,0]\n v_mfma_f32_16x16x32_bf16 v[220:223], v[216:219], v[208:211], v[40:43]\n " \
  "v_mfma_f32_16x16x32_bf16 v[212:215], v[192:195], v[208:211], v[36:39]\n v_cvt_pk_bf16_f32 v184, v184, v185\n v_cvt_pk_bf16_f32 v185, v186, v187\n " \
  "v_cvt_pk_bf16_f32 v186, v188, v189\n v_cvt_pk_bf16_f32 v187, v190, v191\n v_cvt_pk_bf16_f32 v168, v168, v169\n v_cvt_pk_bf16_f32 v169, v170, v171\n " \
  "v_cvt_pk_bf16_f32 v170, v172, v173\n v_cvt_pk_bf16_f32 v171, v174, v175\n " \
  "v_pk_fma_f32 v[160:161], v[148:149], v[144:145], v[160:161] op_sel_hi:[1,0,1]\n " \
  "v_pk_fma_f32 v[162:163], v[150:151], v[144:145], v[162:163] op_sel_hi:[1,0,1]\n " \
  "v_pk_fma_f32 v[164:165], v[104:105], v[144:145], v[164:165] op_sel_hi:[1,0,1]\n " \
  "v_pk_fma_f32 v[166:167], v[106:107], v[144:145], v[166:167] op_sel_hi:[1,0,1]\n s_waitcnt lgkmcnt(3)\n " \
  "v_pk_fma_f32 v[176:177], v[128:129], v[56:57], v[176:177] op_sel_hi:[1,0,1]\n " \
  "v_pk_fma_f32 v[178:179], v[130:131], v[56:57], v[178:179] op_sel_hi:[1,0,1]\n " \
  "v_pk_fma_f32 v[180:181], v[128:129], v[56:57], v[180:181] op_sel:[0,1,0]\n v_pk_fma_f32 v[182:183], v[130:131], v[56:57], v[182:183] op_sel:[0,1,0]\n " \
  "v_pk_mul_f32 v[176:177], v[176:177], v[92:93]\n v_pk_mul_f32 v[178:179], v[178:179], v[94:95]\n v_pk_mul_f32 v[180:181], v[180:181], v[92:93]\n " \
  "v_pk_mul_f32 v[182:183], v[182:183], v[94:95]\n v_mfma_f32_16x16x32_bf16 v[196:199], v[192:195], v[184:187], v[40:43]\n " \
  "v_mfma_f32_16x16x32_bf16 v[188:191], v[168:171], v[184:187], v[36:39]\n v_cvt_pk_bf16_f32 v160, v160, v161\n v_cvt_pk_bf16_f32 v161, v162, v163\n " \
  "v_cvt_pk_bf16_f32 v162, v164, v165\n v_cvt_pk_bf16_f32 v163, v166, v167\n v_cvt_pk_bf16_f32 v174, v176, v177\n v_cvt_pk_bf16_f32 v175, v178, v179\n " \
  "v_cvt_pk_bf16_f32 v180, v180, v181\n v_cvt_pk_bf16_f32 v181, v182, v183\n " \
  "v_pk_fma_f32 v[224:225], v[108:109], v[56:57], v[224:225] op_sel_hi:[1,0,1]\n " \
  "v_pk_fma_f32 v[226:227], v[110:111], v[56:57], v[226:227] op_sel_hi:[1,0,1]\n " \
  "v_pk_fma_f32 v[228:229], v[108:109], v[56:57], v[228:229] op_sel:[0,1,0]\n v_pk_fma_f32 v[230:231], v[110:111], v[56:57], v[230:231] op_sel:[0,1,0]\n " \
  "v_pk_mul_f32 v[224:225], v[224:225], v[88:89]\n v_pk_mul_f32 v[226:227], v[226:227], v[90:91]\n v_pk_mul_f32 v[228:229], v[228:229], v[88:89]\n " \
  "v_pk_mul_f32 v[230:231], v[230:231], v[90:91]\n v_mfma_f32_16x16x32_bf16 v[124:127], v[168:171], v[160:163], v[40:43]\n " \
  "v_cvt_pk_bf16_f32 v246, v224, v225\n v_cvt_pk_bf16_f32 v247, v226, v227\n v_cvt_pk_bf16_f32 v252, v228, v229\n v_cvt_pk_bf16_f32 v253, v230, v231\n " \
  "v_max3_f32 v140, v236, v237, v238\n v_max3_f32 v138, v212, v213, v214\n v_max3_f32 v141, v239, v120, v121\n v_max3_f32 v139, v215, v220, v221\n " \
  "v_max3_f32 v140, v140, v122, v123\n v_max3_f32 v138, v138, v222, v223\n v_max_f32 v140, v140, v141\n v_max_f32 v138, v138, v139\n " \
  "v_max3_f32 v136, v188, v189, v190\n v_max3_f32 v137, v191, v196, v197\n v_max3_f32 v136, v136, v198, v199\n " \
  "v_pk_fma_f32 v[200:201], v[112:113], v[56:57], v[200:201] op_sel_hi:[1,0,1]\n " \
  "v_pk_fma_f32 v[202:203], v[114:115], v[56:57], v[202:203] op_sel_hi:[1,0,1]\n v_max_f32 v136, v136, v137\n v_mov_b32 v142, v140\n " \
  "v_pk_fma_f32 v[204:205], v[112:113], v[56:57], v[204:205] op_sel:[0,1,0]\n v_pk_fma_f32 v[206:207], v[114:115], v[56:57], v[206:207] op_sel:[0,1,0]\n " \
  "v_permlane16_swap_b32 v136, v138\n v_permlane16_swap_b32 v140, v142\n v_pk_mul_f32 v[200:201], v[200:201], v[116:117]\n " \
  "v_pk_mul_f32 v[202:203], v[202:203], v[118:119]\n v_max_f32 v136, v136, v138\n v_max_f32 v140, v140, v142\n " \
  "v_pk_mul_f32 v[204:205], v[204:205], v[116:117]\n v_pk_mul_f32 v[206:207], v[206:207], v[118:119]\n v_permlane32_swap_b32 v136, v140\n " \
  "v_mul_f32 v152, 0x3e8293ee, v63\n v_mul_f32 v154, 0x3e8293ee, v61\n v_max_f32 v136, v136, v140\n v_mov_b32 v140, v136\n " \
  "v_mul_f32 v156, 0x3e8293ee, v147\n v_mul_f32 v158, 0x3e8293ee, v145\n v_permlane32_swap_b32 v136, v140\n v_mov_b32 v132, 0x3f803f80\n " \
  "v_mov_b32 v133, 0x3f803f80\n v_mov_b32 v138, v136\n v_mov_b32 v134, 0x3f803f80\n v_mov_b32 v135, 0x3f803f80\n v_permlane16_swap_b32 v136, v138\n " \
  "v_cvt_pk_bf16_f32 v244, v200, v201\n v_cvt_pk_bf16_f32 v245, v202, v203\n v_cvt_pk_bf16_f32 v250, v204, v205\n v_cvt_pk_bf16_f32 v251, v206, v207\n " \
  "v_cvt_pk_bf16_f32 v176, v200, v201\n v_cvt_pk_bf16_f32 v177, v202, v203\n v_cvt_pk_bf16_f32 v182, v204, v205\n v_cvt_pk_bf16_f32 v183, v206, v207\n " \
  "v_mul_f32_e64 v153, v152, -v140\n v_mul_f32_e64 v155, v154, -v138\n v_mul_f32_e64 v157, v156, -v136\n " \
  "v_pk_fma_f32 v[236:237], v[236:237], v[152:153], v[152:153] op_sel:[0,0,1] op_sel_hi:[1,0,1]\n " \
  "v_pk_fma_f32 v[238:239], v[238:239], v[152:153], v[152:153] op_sel:[0,0,1] op_sel_hi:[1,0,1]\n " \
  "v_pk_fma_f32 v[120:121], v[120:121], v[152:153], v[152:153] op_sel:[0,0,1] op_sel_hi:[1,0,1]\n " \
  "v_pk_fma_f32 v[122:123], v[122:123], v[152:153], v[152:153] op_sel:[0,0,1] op_sel_hi:[1,0,1]\n v_exp_f32 v236, v236\n v_exp_f32 v237, v237\n " \
  "v_exp_f32 v238, v238\n v_exp_f32 v239, v239\n v_exp_f32 v120, v120\n v_exp_f32 v121, v121\n v_exp_f32 v122, v122\n v_exp_f32 v123, v123\n " \
  "v_pk_fma_f32 v[212:213], v[212:213], v[154:155], v[154:155] op_sel:[0,0,1] op_sel_hi:[1,0,1]\n " \
  "v_pk_fma_f32 v[214:215], v[214:215], v[154:155], v[154:155] op_sel:[0,0,1] op_sel_hi:[1,0,1]\n " \
  "v_pk_fma_f32 v[220:221], v[220:221], v[154:155], v[154:155] op_sel:[0,0,1] op_sel_hi:[1,0,1]\n " \
  "v_pk_fma_f32 v[222:223], v[222:223], v[154:155], v[154:155] op_sel:[0,0,1] op_sel_hi:[1,0,1]\n v_cvt_pk_bf16_f32 v236, v236, v237\n " \
  "v_cvt_pk_bf16_f32 v237, v238, v239\n v_cvt_pk_bf16_f32 v238, v120, v121\n v_cvt_pk_bf16_f32 v239, v122, v123\n v_exp_f32 v212, v212\n " \
  "v_exp_f32 v213, v213\n v_mfma_f32_16x16x32_bf16 v[120:123], v[132:135], v[236:239], 0\n " \
  "v_mfma_f32_16x16x32_bf16 v[232:235], v[246:249], v[236:239], 0\n v_mfma_f32_16x16x32_bf16 v[240:243], v[252:255], v[236:239], 0\n " \
  "v_exp_f32 v214, v214\n v_exp_f32 v215, v215\n v_exp_f32 v220, v220\n v_exp_f32 v221, v221\n v_exp_f32 v222, v222\n v_exp_f32 v223, v223\n " \
  "v_pk_fma_f32 v[188:189], v[188:189], v[156:157], v[156:157] op_sel:[0,0,1] op_sel_hi:[1,0,1]\n " \
  "v_pk_fma_f32 v[190:191], v[190:191], v[156:157], v[156:157] op_sel:[0,0,1] op_sel_hi:[1,0,1]\n " \
  "v_pk_fma_f32 v[196:197], v[196:197], v[156:157], v[156:157] op_sel:[0,0,1] op_sel_hi:[1,0,1]\n " \
  "v_pk_fma_f32 v[198:199], v[198:199], v[156:157], v[156:157] op_sel:[0,0,1] op_sel_hi:[1,0,1]\n v_cvt_pk_bf16_f32 v212, v212, v213\n " \
  "v_cvt_pk_bf16_f32 v213, v214, v215\n v_cvt_pk_bf16_f32 v214, v220, v221\n v_cvt_pk_bf16_f32 v215, v222, v223\n v_exp_f32 v188, v188\n " \
  "s_cmp_eq_u32 %[wm], 0\n s_cbranch_scc1 .Lg1f%=\n ds_read_b32 v53, v59\n ds_read_b128 v[80:83], v58\n ds_read_b64 v[172:173], v58 offset:1024\n " \
  "ds_read_b64 v[178:179], v58 offset:1032\n s_branch .Lg1g%=\n .Lg1f%=:\n buffer_load_dwordx4 v[80:83], v55, %[rs], 0 offen sc1\n " \
  "buffer_load_dwordx2 v[172:173], v55, %[rs], 0 offen offset:1024 sc1\n buffer_load_dwordx2 v[178:179], v55, %[rs], 0 offen offset:1032 sc1\n " \
  ".Lg1g%=:\n v_exp_f32 v189, v189\n v_mfma_f32_16x16x32_bf16 v[220:223], v[132:135], v[212:215], 0\n " \
  "v_mfma_f32_16x16x32_bf16 v[224:227], v[244:247], v[212:215], 0\n v_mfma_f32_16x16x32_bf16 v[228:231], v[250:253], v[212:215], 0\n " \
  "v_exp_f32 v190, v190\n v_exp_f32 v191, v191\n v_exp_f32 v196, v196\n v_exp_f32 v197, v197\n v_exp_f32 v198, v198\n v_exp_f32 v199, v199\n " \
  "v_rcp_f32 v120, v120\n v_cvt_pk_bf16_f32 v188, v188, v189\n v_pk_mul_f32 v[232:233], v[232:233], v[120:121] op_sel_hi:[1,0]\n " \
  "v_pk_mul_f32 v[234:235], v[234:235], v[120:121] op_sel_hi:[1,0]\n v_pk_mul_f32 v[240:241], v[240:241], v[120:121] op_sel_hi:[1,0]\n " \
  "v_pk_mul_f32 v[242:243], v[242:243], v[120:121] op_sel_hi:[1,0]\n v_cvt_pk_bf16_f32 v189, v190, v191\n v_cvt_pk_bf16_f32 v190, v196, v197\n " \
  "v_cvt_pk_bf16_f32 v191, v198, v199\n v_rcp_f32 v220, v220\n v_cvt_pk_bf16_f32 v232, v232, v233\n v_cvt_pk_bf16_f32 v233, v234, v235\n " \
  "v_mfma_f32_16x16x32_bf16 v[196:199], v[132:135], v[188:191], 0\n v_mfma_f32_16x16x32_bf16 v[200:203], v[174:177], v[188:191], 0\n " \
  "v_mfma_f32_16x16x32_bf16 v[204:207], v[180:183], v[188:191], 0\n v_cvt_pk_bf16_f32 v234, v240, v241\n v_cvt_pk_bf16_f32 v235, v242, v243\n " \
  "buffer_store_dwordx4 v[232:235], v51, %[rs], 0 offen offset:3072 sc1\n ds_write_b128 v50, v[232:235] offset:3072\n " \
  "v_pk_mul_f32 v[224:225], v[224:225], v[220:221] op_sel_hi:[1,0]\n v_pk_mul_f32 v[226:227], v[226:227], v[220:221] op_sel_hi:[1,0]\n " \
  "v_pk_mul_f32 v[228:229], v[228:229], v[220:221] op_sel_hi:[1,0]\n v_pk_mul_f32 v[230:231], v[230:231], v[220:221] op_sel_hi:[1,0]\n " \
  "v_cvt_pk_bf16_f32 v224, v224, v225\n v_cvt_pk_bf16_f32 v225, v226, v227\n v_cvt_pk_bf16_f32 v226, v228, v229\n v_cvt_pk_bf16_f32 v227, v230, v231\n " \
  "v_rcp_f32 v196, v196\n buffer_store_dwordx4 v[224:227], v51, %[rs], 0 offen offset:2048 sc1\n ds_write_b128 v50, v[224:227] offset:2048\n " \
  "v_pk_mul_f32 v[200:201], v[200:201], v[196:197] op_sel_hi:[1,0]\n v_pk_mul_f32 v[202:203], v[202:203], v[196:197] op_sel_hi:[1,0]\n " \
  "v_pk_mul_f32 v[204:205], v[204:205], v[196:197] op_sel_hi:[1,0]\n v_pk_mul_f32 v[206:207], v[206:207], v[196:197] op_sel_hi:[1,0]\n " \
  "v_cvt_pk_bf16_f32 v200, v200, v201\n v_cvt_pk_bf16_f32 v201, v202, v203\n v_cvt_pk_bf16_f32 v202, v204, v205\n v_cvt_pk_bf16_f32 v203, v206, v207\n " \
  "buffer_store_dwordx4 v[200:203], v51, %[rs], 0 offen offset:1024 sc1\n ds_write_b128 v50, v[200:203] offset:1024\n s_mov_b32 vcc_hi, 0x40000\n " \
  "s_cmp_eq_u32 %[wm], 0\n s_cbranch_scc1 .Lg1h%=\n s_waitcnt lgkmcnt(3)\n .Lg1i%=:\n v_readfirstlane_b32 vcc_lo, v53\n s_nop 1\n " \
  "s_cmp_lg_u32 vcc_lo, 0\n s_cbranch_scc1 .Lg1k%=\n ds_read_b32 v53, v59\n ds_read_b128 v[80:83], v58\n ds_read_b64 v[172:173], v58 offset:1024\n " \
  "ds_read_b64 v[178:179], v58 offset:1032\n s_waitcnt lgkmcnt(0)\n s_sub_u32 vcc_hi, vcc_hi, 1\n s_cbranch_scc0 .Lg1i%=\n s_branch .Lg1k%=\n .Lg1h%=:\n " \
  "s_mov_b32 m0, 0x40000\n s_waitcnt vmcnt(3)\n .Lg1j%=:\n v_max3_u32 v136, v80, v81, v82\n v_max3_u32 v137, v83, v172, v173\n " \
  "v_max3_u32 v136, v136, v178, v179\n v_max_u32 v136, v136, v137\n v_cmp_eq_u32 vcc, -1, v136\n s_cbranch_vccz .Lg1k%=\n " \
  "buffer_load_dwordx4 v[80:83], v55, %[rs], 0 offen sc1\n buffer_load_dwordx2 v[172:173], v55, %[rs], 0 offen offset:1024 sc1\n " \
  "buffer_load_dwordx2 v[178:179], v55, %[rs], 0 offen offset:1032 sc1\n s_waitcnt vmcnt(0)\n s_sub_u32 m0, m0, 1\n s_cbranch_scc0 .Lg1j%=\n .Lg1k%=:\n " \
  "v_mfma_f32_16x16x32_bf16 v[164:167], v[80:83], v[160:163], v[44:47]\n s_nop 7\n v_max3_f32 v136, v164, v165, v166\n " \
  "v_max3_f32 v137, v167, v124, v125\n v_max3_f32 v136, v136, v126, v127\n v_max_f32 v136, v136, v137\n v_mov_b32 v137, v136\n s_nop 1\n " \
  "v_permlane16_swap_b32 v136, v137\n v_max_f32 v136, v136, v137\n v_mov_b32 v137, v136\n s_nop 1\n v_permlane32_swap_b32 v136, v137\n " \
  "v_max_f32 v136, v136, v137\n v_mul_f32_e64 v159, v158, -v136\n " \
  "v_pk_fma_f32 v[164:165], v[164:165], v[158:159], v[158:159] op_sel:[0,0,1] op_sel_hi:[1,0,1]\n " \
  "v_pk_fma_f32 v[166:167], v[166:167], v[158:159], v[158:159] op_sel:[0,0,1] op_sel_hi:[1,0,1]\n " \
  "v_pk_fma_f32 v[124:125], v[124:125], v[158:159], v[158:159] op_sel:[0,0,1] op_sel_hi:[1,0,1]\n " \
  "v_pk_fma_f32 v[126:127], v[126:127], v[158:159], v[158:159] op_sel:[0,0,1] op_sel_hi:[1,0,1]\n v_exp_f32 v164, v164\n v_exp_f32 v165, v165\n " \
  "v_exp_f32 v166, v166\n v_exp_f32 v167, v167\n v_exp_f32 v124, v124\n v_exp_f32 v125, v125\n v_exp_f32 v126, v126\n v_exp_f32 v127, v127\n " \
  "v_cvt_pk_bf16_f32 v164, v164, v165\n v_cvt_pk_bf16_f32 v165, v166, v167\n v_cvt_pk_bf16_f32 v166, v124, v125\n v_cvt_pk_bf16_f32 v167, v126, v127\n " \
  "s_nop 1\n v_mfma_f32_16x16x32_bf16 v[124:127], v[132:135], v[164:167], 0\n v_mfma_f32_16x16x32_bf16 v[168:171], v[172:175], v[164:167], 0\n " \
  "v_mfma_f32_16x16x32_bf16 v[84:87], v[178:181], v[164:167], 0\n s_nop 5\n v_rcp_f32 v124, v124\n s_nop 0\n " \
  "v_pk_mul_f32 v[168:169], v[168:169], v[124:125] op_sel_hi:[1,0]\n v_pk_mul_f32 v[170:171], v[170:171], v[124:125] op_sel_hi:[1,0]\n " \
  "v_pk_mul_f32 v[84:85], v[84:85], v[124:125] op_sel_hi:[1,0]\n v_pk_mul_f32 v[86:87], v[86:87], v[124:125] op_sel_hi:[1,0]\n " \
  "v_cvt_pk_bf16_f32 v168, v168, v169\n v_cvt_pk_bf16_f32 v169, v170, v171\n v_cvt_pk_bf16_f32 v170, v84, v85\n v_cvt_pk_bf16_f32 v171, v86, v87\n " \
  "buffer_store_dwordx4 v[168:171], v51, %[rs], 0 offen offset:0 sc1\n ds_write_b128 v50, v[168:171] offset:0\n "
#define G1_HEDGE \
  "G1Q 1, 1, 0, 0, 0\n ds_read2_b64 v[140:143], v49 offset1:16\n ds_read_b128 v[96:99], v48 offset:256\n G1Q 1, 1, 0, 1, 0\n " \
  "ds_read_b128 v[100:103], v48 offset:320\n ds_read2_b64 v[136:139], v49 offset0:32 offset1:48\n G1Q 1, 1, 0, 2, 0\n ds_read_b64_tr_b16 v[36:37], v53\n " \
  "ds_read_b64_tr_b16 v[38:39], v53 offset:1024\n G1Q 1, 1, 0, 3, 0\n ds_read_b64_tr_b16 v[40:41], v53 offset:32\n " \
  "ds_read_b64_tr_b16 v[42:43], v53 offset:1056\n G1Q 1, 0, 1, 0, 0\n G1Q 1, 0, 1, 1, 0\n s_waitcnt lgkmcnt(4)\n v_fmac_f32 v168, v96, v140\n " \
  "G1Q 1, 0, 1, 2, 0\n v_fma_f32 v169, v97, v140, v169\n v_fma_f32 v170, v98, v140, v170\n G1Q 1, 0, 1, 3, 0\n v_fma_f32 v171, v99, v140, v171\n " \
  "v_fma_f32 v172, v100, v140, v172\n G1Q 1, 3, 2, 0, 0\n v_fma_f32 v173, v101, v140, v173\n v_fma_f32 v174, v102, v140, v174\n G1Q 1, 3, 2, 1, 0\n " \
  "v_fma_f32 v175, v103, v140, v175\n v_cvt_pk_bf16_f32 v168, v168, v169\n G1Q 1, 3, 2, 2, 0\n v_cvt_pk_bf16_f32 v169, v170, v171\n " \
  "v_cvt_pk_bf16_f32 v170, v172, v173\n G1Q 1, 3, 2, 3, 0\n v_cvt_pk_bf16_f32 v171, v174, v175\n v_fma_f32 v192, v96, v142, v192\n G1Q 1, 2, 3, 0, 0\n " \
  "v_fma_f32 v193, v97, v142, v193\n v_fma_f32 v194, v98, v142, v194\n G1Q 1, 2, 3, 1, 0\n v_fma_f32 v195, v99, v142, v195\n " \
  "v_fma_f32 v196, v100, v142, v196\n G1Q 1, 2, 3, 2, 0\n v_fma_f32 v197, v101, v142, v197\n v_fma_f32 v198, v102, v142, v198\n G1Q 1, 2, 3, 3, 0\n " \
  "v_fma_f32 v199, v103, v142, v199\n v_cvt_pk_bf16_f32 v192, v192, v193\n G1Q 2, 2, 0, 0, 0\n ds_read2_b32 v[144:145], v52 offset1:16\n " \
  "v_cvt_pk_bf16_f32 v193, v194, v195\n G1Q 2, 2, 0, 1, 0\n ds_read_b128 v[148:151], v48\n v_cvt_pk_bf16_f32 v194, v196, v197\n G1Q 2, 2, 0, 2, 0\n " \
  "v_cvt_pk_bf16_f32 v195, v198, v199\n v_fma_f32 v216, v96, v136, v216\n G1Q 2, 2, 0, 3, 0\n v_fma_f32 v217, v97, v136, v217\n " \
  "v_fma_f32 v218, v98, v136, v218\n G1Q 2, 3, 1, 0, 0\n v_fma_f32 v219, v99, v136, v219\n v_fma_f32 v220, v100, v136, v220\n G1Q 2, 3, 1, 1, 0\n " \
  "ds_read_b128 v[88:91], v48 offset:64\n v_fma_f32 v221, v101, v136, v221\n v_fma_f32 v222, v102, v136, v222\n G1Q 2, 3, 1, 2, 0\n " \
  "ds_read_b128 v[128:131], v54\n v_fma_f32 v223, v103, v136, v223\n v_cvt_pk_bf16_f32 v216, v216, v217\n G1Q 2, 3, 1, 3, 0\n " \
  "v_cvt_pk_bf16_f32 v217, v218, v219\n v_cvt_pk_bf16_f32 v218, v220, v221\n G1Q 2, 0, 2, 0, 0\n v_cvt_pk_bf16_f32 v219, v222, v223\n " \
  "v_fma_f32 v240, v96, v138, v240\n G1Q 2, 0, 2, 1, 0\n ds_read_b128 v[92:95], v54 offset:512\n v_fma_f32 v241, v97, v138, v241\n " \
  "v_fma_f32 v242, v98, v138, v242\n G1Q 2, 0, 2, 2, 0\n ds_read_b128 v[132:135], v54 offset:64\n v_fma_f32 v243, v99, v138, v243\n " \
  "v_fma_f32 v244, v100, v138, v244\n G1Q 2, 0, 2, 3, 0\n v_fma_f32 v245, v101, v138, v245\n v_fma_f32 v246, v102, v138, v246\n G1Q 2, 1, 3, 0, 0\n " \
  "v_fma_f32 v247, v103, v138, v247\n v_cvt_pk_bf16_f32 v240, v240, v241\n G1Q 2, 1, 3, 1, 0\n v_cvt_pk_bf16_f32 v241, v242, v243\n " \
  "v_cvt_pk_bf16_f32 v242, v244, v245\n G1Q 2, 1, 3, 2, 0\n v_cvt_pk_bf16_f32 v243, v246, v247\n G1Q 2, 1, 3, 3, 0\n s_waitcnt lgkmcnt(6)\n " \
  "v_mfma_f32_16x16x32_bf16 v[172:175], v[36:39], v[168:171], 0\n ds_read_b128 v[112:115], v54 offset:576\n ds_read_b128 v[116:119], v54 offset:128\n " \
  "v_mfma_f32_16x16x32_bf16 v[80:83], v[40:43], v[168:171], 0\n ds_read_b128 v[152:155], v54 offset:640\n ds_read_b128 v[156:159], v54 offset:192\n " \
  "v_mfma_f32_16x16x32_bf16 v[196:199], v[36:39], v[192:195], 0\n ds_read_b128 v[120:123], v54 offset:704\n v_mul_f32 v44, 0x3fb8aa3b, v141\n " \
  "v_mfma_f32_16x16x32_bf16 v[84:87], v[40:43], v[192:195], 0\n v_mul_f32 v46, 0x3fb8aa3b, v143\n v_mul_f32 v104, 0x3fb8aa3b, v137\n " \
  "v_mfma_f32_16x16x32_bf16 v[220:223], v[36:39], v[216:219], 0\n v_mul_f32 v106, 0x3fb8aa3b, v139\n " \
  "v_mfma_f32_16x16x32_bf16 v[124:127], v[40:43], v[216:219], 0\n v_mfma_f32_16x16x32_bf16 v[244:247], v[36:39], v[240:243], 0\n " \
  "v_mfma_f32_16x16x32_bf16 v[108:111], v[40:43], v[240:243], 0\n v_max3_f32 v168, v172, v173, v174\n v_max3_f32 v169, -v172, -v173, -v174\n " \
  "v_max3_f32 v168, v168, v175, v80\n v_max3_f32 v169, v169, -v175, -v80\n v_max3_f32 v168, v168, v81, v82\n v_max3_f32 v169, v169, -v81, -v82\n " \
  "v_max_f32_e64 v168, v168, v83\n v_max_f32_e64 v169, v169, -v83\n v_max3_f32 v170, v196, v197, v198\n v_max3_f32 v171, -v196, -v197, -v198\n " \
  "v_max3_f32 v170, v170, v199, v84\n v_max3_f32 v171, v171, -v199, -v84\n v_max3_f32 v170, v170, v85, v86\n v_max3_f32 v171, v171, -v85, -v86\n " \
  "v_max_f32_e64 v170, v170, v87\n v_max_f32_e64 v171, v171, -v87\n s_waitcnt lgkmcnt(0)\n " \
  "v_pk_fma_f32 v[176:177], v[128:129], v[144:145], v[176:177] op_sel_hi:[1,0,1]\n " \
  "v_pk_fma_f32 v[178:179], v[130:131], v[144:145], v[178:179] op_sel_hi:[1,0,1]\n " \
  "v_pk_fma_f32 v[180:181], v[128:129], v[144:145], v[180:181] op_sel:[0,1,0]\n " \
  "v_pk_fma_f32 v[182:183], v[130:131], v[144:145], v[182:183] op_sel:[0,1,0]\n v_pk_mul_f32 v[176:177], v[176:177], v[92:93]\n " \
  "v_pk_mul_f32 v[178:179], v[178:179], v[94:95]\n v_pk_mul_f32 v[180:181], v[180:181], v[92:93]\n v_pk_mul_f32 v[182:183], v[182:183], v[94:95]\n " \
  "v_pk_fma_f32 v[200:201], v[132:133], v[144:145], v[200:201] op_sel_hi:[1,0,1]\n " \
  "v_pk_fma_f32 v[202:203], v[134:135], v[144:145], v[202:203] op_sel_hi:[1,0,1]\n " \
  "v_pk_fma_f32 v[204:205], v[132:133], v[144:145], v[204:205] op_sel:[0,1,0]\n " \
  "v_pk_fma_f32 v[206:207], v[134:135], v[144:145], v[206:207] op_sel:[0,1,0]\n v_pk_mul_f32 v[200:201], v[200:201], v[112:113]\n " \
  "v_pk_mul_f32 v[202:203], v[202:203], v[114:115]\n v_pk_mul_f32 v[204:205], v[204:205], v[112:113]\n v_pk_mul_f32 v[206:207], v[206:207], v[114:115]\n " \
  "v_max3_f32 v192, v220, v221, v222\n v_max3_f32 v193, -v220, -v221, -v222\n v_max3_f32 v192, v192, v223, v124\n v_max3_f32 v193, v193, -v223, -v124\n " \
  "v_max3_f32 v192, v192, v125, v126\n v_max3_f32 v193, v193, -v125, -v126\n v_max_f32_e64 v192, v192, v127\n v_max_f32_e64 v193, v193, -v127\n " \
  "v_max3_f32 v194, v244, v245, v246\n v_max3_f32 v195, -v244, -v245, -v246\n v_max3_f32 v194, v194, v247, v108\n v_max3_f32 v195, v195, -v247, -v108\n " \
  "v_max3_f32 v194, v194, v109, v110\n v_max3_f32 v195, v195, -v109, -v110\n v_max_f32_e64 v194, v194, v111\n v_max_f32_e64 v195, v195, -v111\n " \
  "v_permlane16_swap_b32 v168, v169\n v_permlane16_swap_b32 v170, v171\n v_permlane16_swap_b32 v192, v193\n v_permlane16_swap_b32 v194, v195\n " \
  "v_pk_fma_f32 v[224:225], v[116:117], v[144:145], v[224:225] op_sel_hi:[1,0,1]\n " \
  "v_pk_fma_f32 v[226:227], v[118:119], v[144:145], v[226:227] op_sel_hi:[1,0,1]\n v_max_f32 v168, v168, v169\n v_max_f32 v170, v170, v171\n " \
  "v_max_f32 v192, v192, v193\n v_max_f32 v194, v194, v195\n v_pk_fma_f32 v[228:229], v[116:117], v[144:145], v[228:229] op_sel:[0,1,0]\n " \
  "v_pk_fma_f32 v[230:231], v[118:119], v[144:145], v[230:231] op_sel:[0,1,0]\n v_permlane32_swap_b32 v168, v170\n v_permlane32_swap_b32 v192, v194\n " \
  "v_pk_mul_f32 v[224:225], v[224:225], v[152:153]\n v_pk_mul_f32 v[226:227], v[226:227], v[154:155]\n v_max_f32 v168, v168, v170\n " \
  "v_max_f32 v192, v192, v194\n v_pk_mul_f32 v[228:229], v[228:229], v[152:153]\n v_pk_mul_f32 v[230:231], v[230:231], v[154:155]\n " \
  "v_mov_b32 v170, v168\n v_mov_b32 v194, v192\n v_pk_fma_f32 v[248:249], v[156:157], v[144:145], v[248:249] op_sel_hi:[1,0,1]\n " \
  "v_pk_fma_f32 v[250:251], v[158:159], v[144:145], v[250:251] op_sel_hi:[1,0,1]\n v_permlane32_swap_b32 v168, v170\n v_permlane32_swap_b32 v192, v194\n " \
  "v_pk_fma_f32 v[252:253], v[156:157], v[144:145], v[252:253] op_sel:[0,1,0]\n " \
  "v_pk_fma_f32 v[254:255], v[158:159], v[144:145], v[254:255] op_sel:[0,1,0]\n v_mov_b32 v169, v168\n v_mov_b32 v171, v170\n v_mov_b32 v193, v192\n " \
  "v_mov_b32 v195, v194\n v_pk_mul_f32 v[248:249], v[248:249], v[120:121]\n v_pk_mul_f32 v[250:251], v[250:251], v[122:123]\n " \
  "v_permlane16_swap_b32 v168, v169\n v_permlane16_swap_b32 v170, v171\n v_permlane16_swap_b32 v192, v193\n v_permlane16_swap_b32 v194, v195\n " \
  "v_pk_mul_f32 v[252:253], v[252:253], v[120:121]\n v_pk_mul_f32 v[254:255], v[254:255], v[122:123]\n v_cvt_pk_bf16_f32 v176, v176, v177\n " \
  "v_cvt_pk_bf16_f32 v177, v178, v179\n v_cvt_pk_bf16_f32 v178, v200, v201\n v_cvt_pk_bf16_f32 v179, v202, v203\n v_cvt_pk_bf16_f32 v180, v180, v181\n " \
  "v_cvt_pk_bf16_f32 v181, v182, v183\n v_cvt_pk_bf16_f32 v182, v204, v205\n v_cvt_pk_bf16_f32 v183, v206, v207\n v_cvt_pk_bf16_f32 v224, v224, v225\n " \
  "v_cvt_pk_bf16_f32 v225, v226, v227\n v_cvt_pk_bf16_f32 v226, v248, v249\n v_cvt_pk_bf16_f32 v227, v250, v251\n v_cvt_pk_bf16_f32 v228, v228, v229\n " \
  "v_cvt_pk_bf16_f32 v229, v230, v231\n v_cvt_pk_bf16_f32 v230, v252, v253\n v_cvt_pk_bf16_f32 v231, v254, v255\n v_mul_f32_e64 v45, v44, -v168\n " \
  "v_mul_f32_e64 v146, v44, -v169\n v_mul_f32_e64 v47, v46, -v170\n v_mul_f32_e64 v147, v46, -v171\n v_mul_f32_e64 v105, v104, -v192\n " \
  "v_mul_f32_e64 v144, v104, -v193\n v_mul_f32_e64 v107, v106, -v194\n v_mul_f32_e64 v145, v106, -v195\n " \
  "v_pk_fma_f32 v[200:201], v[172:173], v[44:45], v[146:147] op_sel_hi:[1,0,0] neg_lo:[1,0,0] neg_hi:[1,0,0]\n " \
  "v_pk_fma_f32 v[202:203], v[174:175], v[44:45], v[146:147] op_sel_hi:[1,0,0] neg_lo:[1,0,0] neg_hi:[1,0,0]\n " \
  "v_pk_fma_f32 v[204:205], v[80:81], v[44:45], v[146:147] op_sel_hi:[1,0,0] neg_lo:[1,0,0] neg_hi:[1,0,0]\n " \
  "v_pk_fma_f32 v[206:207], v[82:83], v[44:45], v[146:147] op_sel_hi:[1,0,0] neg_lo:[1,0,0] neg_hi:[1,0,0]\n " \
  "v_pk_fma_f32 v[172:173], v[172:173], v[44:45], v[44:45] op_sel:[0,0,1] op_sel_hi:[1,0,1]\n " \
  "v_pk_fma_f32 v[174:175], v[174:175], v[44:45], v[44:45] op_sel:[0,0,1] op_sel_hi:[1,0,1]\n " \
  "v_pk_fma_f32 v[80:81], v[80:81], v[44:45], v[44:45] op_sel:[0,0,1] op_sel_hi:[1,0,1]\n " \
  "v_pk_fma_f32 v[82:83], v[82:83], v[44:45], v[44:45] op_sel:[0,0,1] op_sel_hi:[1,0,1]\n ds_write_b128 v55, v[176:179] offset:0\n " \
  "v_exp_f32 v200, v200\n v_exp_f32 v201, v201\n v_exp_f32 v202, v202\n v_exp_f32 v203, v203\n v_exp_f32 v204, v204\n v_exp_f32 v205, v205\n " \
  "v_exp_f32 v206, v206\n v_exp_f32 v207, v207\n v_exp_f32 v172, v172\n v_exp_f32 v173, v173\n v_exp_f32 v174, v174\n v_exp_f32 v175, v175\n " \
  "v_exp_f32 v80, v80\n v_exp_f32 v81, v81\n v_exp_f32 v82, v82\n v_exp_f32 v83, v83\n " \
  "v_pk_fma_f32 v[248:249], v[196:197], v[46:47], v[146:147] op_sel:[0,0,1] op_sel_hi:[1,0,1] neg_lo:[1,0,0] neg_hi:[1,0,0]\n " \
  "v_pk_fma_f32 v[250:251], v[198:199], v[46:47], v[146:147] op_sel:[0,0,1] op_sel_hi:[1,0,1] neg_lo:[1,0,0] neg_hi:[1,0,0]\n " \
  "v_pk_fma_f32 v[252:253], v[84:85], v[46:47], v[146:147] op_sel:[0,0,1] op_sel_hi:[1,0,1] neg_lo:[1,0,0] neg_hi:[1,0,0]\n " \
  "v_pk_fma_f32 v[254:255], v[86:87], v[46:47], v[146:147] op_sel:[0,0,1] op_sel_hi:[1,0,1] neg_lo:[1,0,0] neg_hi:[1,0,0]\n " \
  "v_pk_fma_f32 v[196:197], v[196:197], v[46:47], v[46:47] op_sel:[0,0,1] op_sel_hi:[1,0,1]\n " \
  "v_pk_fma_f32 v[198:199], v[198:199], v[46:47], v[46:47] op_sel:[0,0,1] op_sel_hi:[1,0,1]\n " \
  "v_pk_fma_f32 v[84:85], v[84:85], v[46:47], v[46:47] op_sel:[0,0,1] op_sel_hi:[1,0,1]\n " \
  "v_pk_fma_f32 v[86:87], v[86:87], v[46:47], v[46:47] op_sel:[0,0,1] op_sel_hi:[1,0,1]\n ds_write_b128 v55, v[180:183] offset:1024\n " \
  "v_exp_f32 v248, v248\n v_exp_f32 v249, v249\n v_exp_f32 v250, v250\n v_exp_f32 v251, v251\n v_exp_f32 v252, v252\n v_exp_f32 v253, v253\n " \
  "v_exp_f32 v254, v254\n v_exp_f32 v255, v255\n v_exp_f32 v196, v196\n v_exp_f32 v197, v197\n v_exp_f32 v198, v198\n v_exp_f32 v199, v199\n " \
  "v_exp_f32 v84, v84\n v_exp_f32 v85, v85\n v_exp_f32 v86, v86\n v_exp_f32 v87, v87\n " \
  "v_pk_fma_f32 v[128:129], v[220:221], v[104:105], v[144:145] op_sel_hi:[1,0,0] neg_lo:[1,0,0] neg_hi:[1,0,0]\n " \
  "v_pk_fma_f32 v[130:131], v[222:223], v[104:105], v[144:145] op_sel_hi:[1,0,0] neg_lo:[1,0,0] neg_hi:[1,0,0]\n " \
  "v_pk_fma_f32 v[132:133], v[124:125], v[104:105], v[144:145] op_sel_hi:[1,0,0] neg_lo:[1,0,0] neg_hi:[1,0,0]\n " \
  "v_pk_fma_f32 v[134:135], v[126:127], v[104:105], v[144:145] op_sel_hi:[1,0,0] neg_lo:[1,0,0] neg_hi:[1,0,0]\n " \
  "v_pk_fma_f32 v[220:221], v[220:221], v[104:105], v[104:105] op_sel:[0,0,1] op_sel_hi:[1,0,1]\n " \
  "v_pk_fma_f32 v[222:223], v[222:223], v[104:105], v[104:105] op_sel:[0,0,1] op_sel_hi:[1,0,1]\n " \
  "v_pk_fma_f32 v[124:125], v[124:125], v[104:105], v[104:105] op_sel:[0,0,1] op_sel_hi:[1,0,1]\n " \
  "v_pk_fma_f32 v[126:127], v[126:127], v[104:105], v[104:105] op_sel:[0,0,1] op_sel_hi:[1,0,1]\n ds_write_b128 v55, v[224:227] offset:8192\n " \
  "v_pk_add_f32 v[216:217], v[172:173], v[174:175]\n v_pk_add_f32 v[218:219], v[80:81], v[82:83]\n v_pk_add_f32 v[216:217], v[216:217], v[218:219]\n " \
  "v_add_f32 v168, v216, v217\n v_pk_add_f32 v[216:217], v[200:201], v[202:203]\n v_pk_add_f32 v[218:219], v[204:205], v[206:207]\n " \
  "v_pk_add_f32 v[216:217], v[216:217], v[218:219]\n v_add_f32 v169, v216, v217\n v_pk_add_f32 v[216:217], v[196:197], v[198:199]\n " \
  "v_pk_add_f32 v[218:219], v[84:85], v[86:87]\n v_pk_add_f32 v[216:217], v[216:217], v[218:219]\n v_add_f32 v170, v216, v217\n " \
  "v_pk_add_f32 v[216:217], v[248:249], v[250:251]\n v_pk_add_f32 v[218:219], v[252:253], v[254:255]\n v_pk_add_f32 v[216:217], v[216:217], v[218:219]\n " \
  "v_add_f32 v171, v216, v217\n v_exp_f32 v128, v128\n v_exp_f32 v129, v129\n v_permlane16_swap_b32 v168, v169\n v_permlane16_swap_b32 v170, v171\n " \
  "v_exp_f32 v130, v130\n v_exp_f32 v131, v131\n v_add_f32 v168, v168, v169\n v_add_f32 v170, v170, v171\n v_exp_f32 v132, v132\n v_exp_f32 v133, v133\n " \
  "v_permlane32_swap_b32 v168, v170\n v_exp_f32 v134, v134\n v_exp_f32 v135, v135\n v_add_f32 v168, v168, v170\n v_exp_f32 v220, v220\n " \
  "v_exp_f32 v221, v221\n v_rcp_f32 v168, v168\n v_exp_f32 v222, v222\n v_exp_f32 v223, v223\n v_mov_b32 v170, v168\n v_exp_f32 v124, v124\n " \
  "v_exp_f32 v125, v125\n v_permlane32_swap_b32 v168, v170\n v_exp_f32 v126, v126\n v_exp_f32 v127, v127\n v_mov_b32 v169, v168\n v_mov_b32 v171, v170\n " \
  "v_pk_fma_f32 v[112:113], v[244:245], v[106:107], v[144:145] op_sel:[0,0,1] op_sel_hi:[1,0,1] neg_lo:[1,0,0] neg_hi:[1,0,0]\n " \
  "v_pk_fma_f32 v[114:115], v[246:247], v[106:107], v[144:145] op_sel:[0,0,1] op_sel_hi:[1,0,1] neg_lo:[1,0,0] neg_hi:[1,0,0]\n " \
  "v_permlane16_swap_b32 v168, v169\n v_permlane16_swap_b32 v170, v171\n " \
  "v_pk_fma_f32 v[116:117], v[108:109], v[106:107], v[144:145] op_sel:[0,0,1] op_sel_hi:[1,0,1] neg_lo:[1,0,0] neg_hi:[1,0,0]\n " \
  "v_pk_fma_f32 v[118:119], v[110:111], v[106:107], v[144:145] op_sel:[0,0,1] op_sel_hi:[1,0,1] neg_lo:[1,0,0] neg_hi:[1,0,0]\n " \
  "v_pk_fma_f32 v[244:245], v[244:245], v[106:107], v[106:107] op_sel:[0,0,1] op_sel_hi:[1,0,1]\n " \
  "v_pk_fma_f32 v[246:247], v[246:247], v[106:107], v[106:107] op_sel:[0,0,1] op_sel_hi:[1,0,1]\n " \
  "v_pk_fma_f32 v[108:109], v[108:109], v[106:107], v[106:107] op_sel:[0,0,1] op_sel_hi:[1,0,1]\n " \
  "v_pk_fma_f32 v[110:111], v[110:111], v[106:107], v[106:107] op_sel:[0,0,1] op_sel_hi:[1,0,1]\n ds_write_b128 v55, v[228:231] offset:9216\n " \
  "v_exp_f32 v112, v112\n v_pk_mul_f32 v[172:173], v[172:173], v[168:169] op_sel_hi:[1,0]\n v_exp_f32 v113, v113\n " \
  "v_pk_mul_f32 v[174:175], v[174:175], v[168:169] op_sel_hi:[1,0]\n v_exp_f32 v114, v114\n v_cvt_pk_bf16_f32 v172, v172, v173\n v_exp_f32 v115, v115\n " \
  "v_cvt_pk_bf16_f32 v173, v174, v175\n v_exp_f32 v116, v116\n ds_write_b64 v58, v[172:173] offset:0\n v_exp_f32 v117, v117\n " \
  "v_pk_mul_f32 v[80:81], v[80:81], v[168:169] op_sel_hi:[1,0]\n v_exp_f32 v118, v118\n v_pk_mul_f32 v[82:83], v[82:83], v[168:169] op_sel_hi:[1,0]\n " \
  "v_exp_f32 v119, v119\n v_cvt_pk_bf16_f32 v80, v80, v81\n v_exp_f32 v244, v244\n v_cvt_pk_bf16_f32 v81, v82, v83\n v_exp_f32 v245, v245\n " \
  "ds_write_b64 v58, v[80:81] offset:32\n v_exp_f32 v246, v246\n v_pk_mul_f32 v[200:201], v[200:201], v[168:169] op_sel:[0,1]\n v_exp_f32 v247, v247\n " \
  "v_pk_mul_f32 v[202:203], v[202:203], v[168:169] op_sel:[0,1]\n v_exp_f32 v108, v108\n v_cvt_pk_bf16_f32 v200, v200, v201\n v_exp_f32 v109, v109\n " \
  "v_cvt_pk_bf16_f32 v201, v202, v203\n v_exp_f32 v110, v110\n ds_write_b64 v58, v[200:201] offset:64\n v_exp_f32 v111, v111\n " \
  "v_pk_mul_f32 v[204:205], v[204:205], v[168:169] op_sel:[0,1]\n v_pk_mul_f32 v[206:207], v[206:207], v[168:169] op_sel:[0,1]\n " \
  "v_cvt_pk_bf16_f32 v204, v204, v205\n v_cvt_pk_bf16_f32 v205, v206, v207\n ds_write_b64 v58, v[204:205] offset:96\n " \
  "v_pk_add_f32 v[216:217], v[220:221], v[222:223]\n v_pk_add_f32 v[218:219], v[124:125], v[126:127]\n v_pk_add_f32 v[216:217], v[216:217], v[218:219]\n " \
  "v_add_f32 v192, v216, v217\n v_pk_add_f32 v[216:217], v[128:129], v[130:131]\n v_pk_add_f32 v[218:219], v[132:133], v[134:135]\n " \
  "v_pk_add_f32 v[216:217], v[216:217], v[218:219]\n v_add_f32 v193, v216, v217\n v_pk_add_f32 v[216:217], v[244:245], v[246:247]\n " \
  "v_pk_add_f32 v[218:219], v[108:109], v[110:111]\n v_pk_add_f32 v[216:217], v[216:217], v[218:219]\n v_add_f32 v194, v216, v217\n " \
  "v_pk_add_f32 v[216:217], v[112:113], v[114:115]\n v_pk_add_f32 v[218:219], v[116:117], v[118:119]\n v_pk_add_f32 v[216:217], v[216:217], v[218:219]\n " \
  "v_add_f32 v195, v216, v217\n v_pk_mul_f32 v[196:197], v[196:197], v[170:171] op_sel_hi:[1,0]\n " \
  "v_pk_mul_f32 v[198:199], v[198:199], v[170:171] op_sel_hi:[1,0]\n v_permlane16_swap_b32 v192, v193\n v_permlane16_swap_b32 v194, v195\n " \
  "v_cvt_pk_bf16_f32 v196, v196, v197\n v_cvt_pk_bf16_f32 v197, v198, v199\n v_add_f32 v192, v192, v193\n v_add_f32 v194, v194, v195\n " \
  "ds_write_b64 v58, v[196:197] offset:2304\n v_pk_mul_f32 v[84:85], v[84:85], v[170:171] op_sel_hi:[1,0]\n v_permlane32_swap_b32 v192, v194\n " \
  "v_pk_mul_f32 v[86:87], v[86:87], v[170:171] op_sel_hi:[1,0]\n v_cvt_pk_bf16_f32 v84, v84, v85\n v_add_f32 v192, v192, v194\n " \
  "v_cvt_pk_bf16_f32 v85, v86, v87\n ds_write_b64 v58, v[84:85] offset:2336\n v_rcp_f32 v192, v192\n " \
  "v_pk_mul_f32 v[248:249], v[248:249], v[170:171] op_sel:[0,1]\n v_pk_mul_f32 v[250:251], v[250:251], v[170:171] op_sel:[0,1]\n v_mov_b32 v194, v192\n " \
  "v_cvt_pk_bf16_f32 v248, v248, v249\n v_cvt_pk_bf16_f32 v249, v250, v251\n v_permlane32_swap_b32 v192, v194\n " \
  "ds_write_b64 v58, v[248:249] offset:2368\n v_pk_mul_f32 v[252:253], v[252:253], v[170:171] op_sel:[0,1]\n v_mov_b32 v193, v192\n " \
  "v_mov_b32 v195, v194\n v_pk_mul_f32 v[254:255], v[254:255], v[170:171] op_sel:[0,1]\n v_cvt_pk_bf16_f32 v252, v252, v253\n " \
  "v_permlane16_swap_b32 v192, v193\n v_permlane16_swap_b32 v194, v195\n v_cvt_pk_bf16_f32 v253, v254, v255\n ds_write_b64 v58, v[252:253] offset:2400\n " \
  "v_pk_mul_f32 v[220:221], v[220:221], v[192:193] op_sel_hi:[1,0]\n v_pk_mul_f32 v[222:223], v[222:223], v[192:193] op_sel_hi:[1,0]\n " \
  "v_cvt_pk_bf16_f32 v220, v220, v221\n v_cvt_pk_bf16_f32 v221, v222, v223\n ds_write_b64 v58, v[220:221] offset:18432\n " \
  "v_pk_mul_f32 v[124:125], v[124:125], v[192:193] op_sel_hi:[1,0]\n v_pk_mul_f32 v[126:127], v[126:127], v[192:193] op_sel_hi:[1,0]\n " \
  "v_cvt_pk_bf16_f32 v124, v124, v125\n v_cvt_pk_bf16_f32 v125, v126, v127\n ds_write_b64 v58, v[124:125] offset:18464\n " \
  "v_pk_mul_f32 v[128:129], v[128:129], v[192:193] op_sel:[0,1]\n v_pk_mul_f32 v[130:131], v[130:131], v[192:193] op_sel:[0,1]\n " \
  "v_cvt_pk_bf16_f32 v128, v128, v129\n v_cvt_pk_bf16_f32 v129, v130, v131\n ds_write_b64 v58, v[128:129] offset:18496\n " \
  "v_pk_mul_f32 v[132:133], v[132:133], v[192:193] op_sel:[0,1]\n v_pk_mul_f32 v[134:135], v[134:135], v[192:193] op_sel:[0,1]\n " \
  "v_cvt_pk_bf16_f32 v132, v132, v133\n v_cvt_pk_bf16_f32 v133, v134, v135\n ds_write_b64 v58, v[132:133] offset:18528\n " \
  "v_pk_mul_f32 v[244:245], v[244:245], v[194:195] op_sel_hi:[1,0]\n v_pk_mul_f32 v[246:247], v[246:247], v[194:195] op_sel_hi:[1,0]\n " \
  "v_cvt_pk_bf16_f32 v244, v244, v245\n v_cvt_pk_bf16_f32 v245, v246, v247\n ds_write_b64 v58, v[244:245] offset:20736\n " \
  "v_pk_mul_f32 v[108:109], v[108:109], v[194:195] op_sel_hi:[1,0]\n v_pk_mul_f32 v[110:111], v[110:111], v[194:195] op_sel_hi:[1,0]\n " \
  "v_cvt_pk_bf16_f32 v108, v108, v109\n v_cvt_pk_bf16_f32 v109, v110, v111\n ds_write_b64 v58, v[108:109] offset:20768\n " \
  "v_pk_mul_f32 v[112:113], v[112:113], v[194:195] op_sel:[0,1]\n v_pk_mul_f32 v[114:115], v[114:115], v[194:195] op_sel:[0,1]\n " \
  "v_cvt_pk_bf16_f32 v112, v112, v113\n v_cvt_pk_bf16_f32 v113, v114, v115\n ds_write_b64 v58, v[112:113] offset:20800\n " \
  "v_pk_mul_f32 v[116:117], v[116:117], v[194:195] op_sel:[0,1]\n v_pk_mul_f32 v[118:119], v[118:119], v[194:195] op_sel:[0,1]\n " \
  "v_cvt_pk_bf16_f32 v116, v116, v117\n v_cvt_pk_bf16_f32 v117, v118, v119\n ds_write_b64 v58, v[116:117] offset:20832\n "
#define G1_OUTPUT \
  "s_mov_b32 m0, 0x40000\n s_branch .Lg1p%=\n .Lg1q%=:\n s_sleep 1\n s_sub_u32 m0, m0, 1\n s_cbranch_scc1 .Lg1r%=\n .Lg1p%=:\n " \
  "buffer_load_dwordx4 v[216:219], %[fz], %[rs], 0 offen sc1\n buffer_load_dwordx4 v[220:223], %[fz], %[rs], 0 offen offset:64 sc1\n " \
  "buffer_load_dwordx4 v[200:203], %[fo], %[rs], 0 offen offset:0 sc1\n buffer_load_dwordx4 v[204:207], %[fo], %[rs], 0 offen offset:2048 sc1\n " \
  "buffer_load_dwordx4 v[208:211], %[fo], %[rs], 0 offen offset:1024 sc1\n buffer_load_dwordx4 v[212:215], %[fo], %[rs], 0 offen offset:3072 sc1\n " \
  "s_waitcnt vmcnt(5)\n v_max_u32 v152, v216, v217\n v_max3_u32 v152, v218, v219, v152\n s_waitcnt vmcnt(4)\n v_max3_u32 v152, v220, v221, v152\n " \
  "v_max3_u32 v152, v222, v223, v152\n s_waitcnt vmcnt(3)\n v_max3_u32 v152, v200, v201, v152\n v_max3_u32 v152, v202, v203, v152\n s_waitcnt vmcnt(2)\n " \
  "v_max3_u32 v152, v204, v205, v152\n v_max3_u32 v152, v206, v207, v152\n s_waitcnt vmcnt(1)\n v_max3_u32 v152, v208, v209, v152\n " \
  "v_max3_u32 v152, v210, v211, v152\n s_waitcnt vmcnt(0)\n v_max3_u32 v152, v212, v213, v152\n v_max3_u32 v152, v214, v215, v152\n " \
  "v_cmp_eq_u32 vcc, -1, v152\n s_cbranch_vccnz .Lg1q%=\n .Lg1r%=:\n v_mfma_f32_16x16x32_bf16 v[160:163], v[216:219], v[224:227], 0\n " \
  "v_mfma_f32_16x16x32_bf16 v[160:163], v[220:223], v[228:231], v[160:163]\n v_mfma_f32_16x16x32_bf16 v[152:155], v[200:203], v[224:227], 0\n " \
  "v_mfma_f32_16x16x32_bf16 v[152:155], v[204:207], v[228:231], v[152:155]\n v_mfma_f32_16x16x32_bf16 v[156:159], v[208:211], v[224:227], 0\n " \
  "v_mfma_f32_16x16x32_bf16 v[156:159], v[212:215], v[228:231], v[156:159]\n v_mfma_f32_16x16x32_bf16 v[172:175], v[216:219], v[232:235], 0\n " \
  "v_mfma_f32_16x16x32_bf16 v[172:175], v[220:223], v[236:239], v[172:175]\n v_mfma_f32_16x16x32_bf16 v[164:167], v[200:203], v[232:235], 0\n " \
  "v_mfma_f32_16x16x32_bf16 v[164:167], v[204:207], v[236:239], v[164:167]\n v_mfma_f32_16x16x32_bf16 v[168:171], v[208:211], v[232:235], 0\n " \
  "v_add_f32 v160, 0x358637bd, v160\n v_rcp_f32 v160, v160\n v_mfma_f32_16x16x32_bf16 v[168:171], v[212:215], v[236:239], v[168:171]\n " \
  "v_mul_f32 v152, v160, v152\n v_mfma_f32_16x16x32_bf16 v[184:187], v[216:219], v[240:243], 0\n v_mul_f32 v153, v160, v153\n " \
  "v_cvt_pk_bf16_f32 v152, v152, v153\n v_mul_f32 v154, v160, v154\n v_mfma_f32_16x16x32_bf16 v[184:187], v[220:223], v[244:247], v[184:187]\n " \
  "v_mul_f32 v155, v160, v155\n v_cvt_pk_bf16_f32 v153, v154, v155\n v_mul_f32 v156, v160, v156\n " \
  "v_mfma_f32_16x16x32_bf16 v[176:179], v[200:203], v[240:243], 0\n v_mul_f32 v157, v160, v157\n v_cvt_pk_bf16_f32 v154, v156, v157\n " \
  "v_mul_f32 v158, v160, v158\n v_mfma_f32_16x16x32_bf16 v[176:179], v[204:207], v[244:247], v[176:179]\n v_mul_f32 v159, v160, v159\n " \
  "v_cvt_pk_bf16_f32 v155, v158, v159\n buffer_store_dwordx4 v[152:155], %[go], %[rs], 0 offen offset:0 sc1\n " \
  "v_mfma_f32_16x16x32_bf16 v[180:183], v[208:211], v[240:243], 0\n ds_write_b128 %[lo], v[152:155] offset:0\n v_add_f32 v172, 0x358637bd, v172\n " \
  "v_mfma_f32_16x16x32_bf16 v[180:183], v[212:215], v[244:247], v[180:183]\n v_rcp_f32 v172, v172\n " \
  "v_mfma_f32_16x16x32_bf16 v[196:199], v[216:219], v[248:251], 0\n v_mul_f32 v164, v172, v164\n v_mul_f32 v165, v172, v165\n " \
  "v_cvt_pk_bf16_f32 v164, v164, v165\n v_mfma_f32_16x16x32_bf16 v[196:199], v[220:223], v[252:255], v[196:199]\n v_mul_f32 v166, v172, v166\n " \
  "v_mul_f32 v167, v172, v167\n v_cvt_pk_bf16_f32 v165, v166, v167\n v_mfma_f32_16x16x32_bf16 v[188:191], v[200:203], v[248:251], 0\n " \
  "v_mul_f32 v168, v172, v168\n v_mul_f32 v169, v172, v169\n v_cvt_pk_bf16_f32 v166, v168, v169\n " \
  "v_mfma_f32_16x16x32_bf16 v[188:191], v[204:207], v[252:255], v[188:191]\n v_mul_f32 v170, v172, v170\n v_mul_f32 v171, v172, v171\n " \
  "v_cvt_pk_bf16_f32 v167, v170, v171\n v_mfma_f32_16x16x32_bf16 v[192:195], v[208:211], v[248:251], 0\n " \
  "buffer_store_dwordx4 v[164:167], %[go], %[rs], 0 offen offset:1024 sc1\n ds_write_b128 %[lo], v[164:167] offset:1024\n " \
  "v_mfma_f32_16x16x32_bf16 v[192:195], v[212:215], v[252:255], v[192:195]\n v_add_f32 v184, 0x358637bd, v184\n v_rcp_f32 v184, v184\n s_nop 0\n " \
  "v_pk_mul_f32 v[176:177], v[176:177], v[184:185] op_sel_hi:[1,0]\n v_pk_mul_f32 v[178:179], v[178:179], v[184:185] op_sel_hi:[1,0]\n " \
  "v_pk_mul_f32 v[180:181], v[180:181], v[184:185] op_sel_hi:[1,0]\n v_pk_mul_f32 v[182:183], v[182:183], v[184:185] op_sel_hi:[1,0]\n " \
  "v_cvt_pk_bf16_f32 v176, v176, v177\n v_cvt_pk_bf16_f32 v177, v178, v179\n v_cvt_pk_bf16_f32 v178, v180, v181\n v_cvt_pk_bf16_f32 v179, v182, v183\n " \
  "buffer_store_dwordx4 v[176:179], %[go], %[rs], 0 offen offset:2048 sc1\n ds_write_b128 %[lo], v[176:179] offset:2048\n " \
  "v_add_f32 v196, 0x358637bd, v196\n v_rcp_f32 v196, v196\n s_nop 0\n v_pk_mul_f32 v[188:189], v[188:189], v[196:197] op_sel_hi:[1,0]\n " \
  "v_pk_mul_f32 v[190:191], v[190:191], v[196:197] op_sel_hi:[1,0]\n v_pk_mul_f32 v[192:193], v[192:193], v[196:197] op_sel_hi:[1,0]\n " \
  "v_pk_mul_f32 v[194:195], v[194:195], v[196:197] op_sel_hi:[1,0]\n v_cvt_pk_bf16_f32 v188, v188, v189\n v_cvt_pk_bf16_f32 v189, v190, v191\n " \
  "v_cvt_pk_bf16_f32 v190, v192, v193\n v_cvt_pk_bf16_f32 v191, v194, v195\n buffer_store_dwordx4 v[188:191], %[go], %[rs], 0 offen offset:3072 sc1\n " \
  "ds_write_b128 %[lo], v[188:191] offset:3072\n "
#define G1_PURGE \
  ".purgem G1M\n .purgem G1RA\n .purgem G1RB\n .purgem G1BK\n .purgem G1AK\n .purgem G1RDB\n .purgem G1RDA\n .purgem G1Q\n .purgem G1SG\n "
#define G1_CLOBBER \
  "v80", "v81", "v82", "v83", "v84", "v85", "v86", "v87", "v92", "v93", "v94", "v95", "v96", "v97", "v98", "v99", "v100", "v101", "v102", "v103", \
  "v104", "v105", "v106", "v107", "v108", "v109", "v110", "v111", \
  "v112", "v113", "v114", "v115", "v116", "v117", "v118", "v119", "v120", "v121", "v122", "v123", "v124", "v125", "v126", "v127", "v128", "v129", \
  "v130", "v131", "v132", "v133", "v134", "v135", "v144", "v145", "v146", "v147", "v152", "v153", "v154", "v155", \
  "v156", "v157", "v158", "v159"

template <int MODE>
__device__ __forceinline__ void g1_phase(char* smem, char* ws, u64& E, int w, f32x16& xq, Next& nx, int layer, const u16* __restrict__ x0, const u16* __restrict__ W,
                                         const u16* __restrict__ wo, const u16* __restrict__ wphi,
                                         const u16* __restrict__ wn1, const u16* __restrict__ wn2) {
  constexpr bool HH = (MODE == 1);
  constexpr int ABYTES = 128 * 128;
  constexpr int STAGE = ABYTES + 3 * 8192;
  constexpr int OFF_FX = 16384;
  constexpr int OFF_SP = 3 * STAGE;
  constexpr int OFF_RS = !HH ? 159744 : OFF_SP + 16384;
  constexpr int OFF_CS = OFF_RS + (HH ? 1152 : 1024);
  constexpr int OFF_WP = OFF_CS + 1536;
  constexpr int OFF_JK = LDS_TOTAL - 1024;
  constexpr int OFF_PK = 0;
  constexpr int OFF_VF = OFF_WP + 4096;
  constexpr int OFF_R2 = HH ? OFF_VF + 16384 : OFF_CS + 1024;
  constexpr int OFF_RD = OFF_CS + 768;
  constexpr int PSTR = 144;
  IDS
  W = uni(W); wphi = uni(wphi); wn1 = uni(wn1); wn2 = uni(wn2);
  const int kg = w >> 2, wm = (w >> 1) & 1, wn = w & 1;
  const bool first = !HH && layer == 0;
  f32x4 acc[2][2], qh[2][2], csq[2], st01, st23;
  bf16x8 wf[2];
  u64 kol;
  u32x4 pw[8];
  ST(layer * 24 + 0)
  const int R0 = wm * 64;
  u32x16 pa;
#define pcs pa[0]
#define prs pa[1]
#define opo pa[2]
#define ogo pa[3]
#define hof pa[7]
#define dm pa[8]
#define dm2 pa[9]
#define pcv pa[4]
#define pwf pa[5]
#define prv pa[6]
#define pvf pa[7]
#define ppk pa[10]
#define ppa pa[11]
#define pvr pa[12]
#define gkv pa[13]
#define gkz pa[14]
#define pwl pa[15]
#define pwm pa[4]
#define plb pa[10]
#define prd pa[11]
#define dm3 pa[12]
  f32x4 bias[3];
  const char* wos;
  unsigned tO;
  const unsigned sb = lds_addr(smem);
  auto prep = [&](auto ldr) {
    constexpr bool LDR = decltype(ldr)::value;
    if constexpr (!LDR) {
      pcs = sb + OFF_CS + wn * 128 + g * 16;
      prs = sb + OFF_RS + (R0 + fr) * 8;
      opo = (unsigned)(wn * 8192 + R0 * 64 + lane * 16);
      ogo = opo + WS_O + layer * (SEQ * DM * 2) + (unsigned)((rt * NH + 2 * hp) * 8192);
      opo += sb;
      pcv = sb + OFF_CS + (wn * 32 + 128 + fr) * 4;
      prv = sb + OFF_R2 + (R0 + 4 * g) * 4;
    } else {
      constexpr unsigned kvn = WS_KV + MODE * KVLB;
      const int t = tid & 255;
      dm = kvn + (unsigned)((rt * NH + 2 * hp) * KVPB) + t * 16;
      dm2 = t < 16 ? dm + 8192 : t >= 64 && t < 97 ? kvn + 16u * NH * KVPB + (unsigned)(hp * KVFB + rt * 528 + (t - 64) * 16) : 0x7fff0000u;
      if (!HH) dm3 = WS_HL + (unsigned)((256 + rt * 16 + hp) * 4096 + t * 16);
    }
    if constexpr (!HH) {
      prd = sb + OFF_RD + wn * 4;
      if constexpr (!LDR) {
        plb = sb + OFF_FX + wn * 2048 + lane * 16;
        hof = m0 + wm == 0 ? 0x7fff0000u : WS_HL + (unsigned)((layer * 128 + rt * 16 - 16 + wm * 16 + hp) * 4096 + wn * 2048 + lane * 16);
        const unsigned out = ~((0x5555u << 2 << fr) >> (4 * g));
#pragma unroll
        for (int i = 0; i < 8; ++i) {
          unsigned t;
          asm("v_bfe_i32 %0, %1, %2, 1" : "=v"(t) : "v"(out), "n"((i >> 2) * 16 + (i & 3)));
          bias[i >> 2][i & 3] = __uint_as_float(t & 0xff61b1e6u);
        }
        bias[2] = m0 + R0 == 0 ? f32x4{-3.0e38f, -3.0e38f, -3.0e38f, -3.0e38f} : bias[0];
        asm volatile("" : "+{v[36:39]}"(bias[0]), "+{v[40:43]}"(bias[1]), "+{v[44:47]}"(bias[2]));
      }
    } else {
      const int u = wm * 2 + kg;
      if constexpr (!LDR) {
        pwf = sb + OFF_WP + wn * 2048 + (4 * g + q4) * 64 + p4 * 8;
        pvf = sb + OFF_VF + w * 2048 + lane * 16;
        ppk = sb + OFF_PK + w * (32 * PSTR) + fr * PSTR + g * 8;
      }
      ppa = sb + OFF_PK + wn * (32 * PSTR) + (4 * g + q4) * PSTR + u * 32 + p4 * 8;
      pvr = sb + OFF_VF + wn * 2048 + lane * 16;
      const unsigned doff = WS_KV + (layer >> 1) * KVLB + (unsigned)((rt * NH + 2 * hp + wn) * KVPB);
      gkv = doff + u * 1024 + lane * 16;
      gkz = fr == 0 ? doff + 4096 + u * 32 + g * 8 : 0x7fff0000u;
    }
    asm volatile("" : "+{v[48:63]}"(pa));
  };

  if (kg == 1) {

    const int lw = w & 3;
    const int kB = 16 * lw + (lane >> 3);
    const int fB = (kB >> 1) & 1;
    const int cB0 = ((((lane & 7) >> 1) ^ fB) << 1) | (lane & 1), cB1 = ((((lane & 7) >> 1) ^ (fB | 2)) << 1) | (lane & 1);
    const bool im = img(layer, hp);

    unsigned vW0, vW1, vP, vX, vY;
    u64 ko2;
    auto ld_pw = [&](int i0) {
#pragma unroll
      for (int i = i0; i < i0 + 4; ++i) gld16(pw[i], vP, wn1 + i * 8 * 3072);
    };
    auto wo_tile = [&](int t) { glds2v(wos + knib(ko2, t) * tO, vW0, vW1, lds_addr(smem) + OFF_WO + t * 8192 + lw * 2048); };
    auto run = [&](auto fst) {
      constexpr bool FIRST = decltype(fst)::value;
      constexpr int N = 10;
      const unsigned vA = FIRST ? (unsigned)(((m0 + lw * 32 + (lane >> 3)) * DM + ((lane & 7) ^ (lane >> 3)) * 8) * 2)
                                : (unsigned)(rt * 262144 + lw * 4096 + lane * 16);
      unsigned imv = !FIRST && im ? ~0u : 0u;
      if (!FIRST) asm volatile("" : "+v"(imv));
      const unsigned vB0 = ((unsigned)(lw * 6144 + lane * 16) & imv) | ((unsigned)((kB * 3072 + hp * 64 + cB0 * 8) * 2) & ~imv);
      const unsigned vB1 = ((unsigned)(lw * 6144 + lane * 16) & imv) | (((unsigned)(((kB + 8) * 3072 + hp * 64 + cB1 * 8) * 2) - 1024u) & ~imv);
      const char* xa = FIRST ? (const char*)x0 : ws + WS_X + (layer - 1) * (SEQ * DM * 2);
      constexpr int KS = FIRST ? 128 : 16384;
      const u64 kord = FIRST ? korder0(rt) : nx.kord;
      auto ktile = [&](int ks) { return knib(kord, ks); };
      auto stageA = [&](int ks, int boff) {
        const int kt = ktile(ks);
        const unsigned sA = lds_addr(smem) + boff;
        if (FIRST) glds4v(xa + kt * KS, vA, vA + 15360u, vA + 2 * 15360u, vA + 3 * 15360u, sA + lw * 4096);
        else glds4c(xa + kt * KS, vA, sA + lw * 4096);
      };
      auto stageB = [&](const u16* Wp, int ks, int boff) {
        const int kt = ktile(ks);
        const unsigned sB = lds_addr(smem) + boff + ABYTES;
        const char* b = FIRST ? (const char*)(Wp + (size_t)kt * 64 * 3072) : nx.wq + kt * nx.tw;
        glds4v(b, vB0, vB1, vB0, vB1, sB + lw * 6144);
        glds2v(b + 4096, vB0, vB1, sB + lw * 6144 + 4096);
      };
      bf16x8 sel[3];
      unsigned cso[3], cst;
      if (FIRST) {
        WAITVN(N);
        BAR;
        stageA(2, 2 * STAGE); stageB(W, 2, 2 * STAGE);
        __builtin_amdgcn_sched_barrier(0);
        const int fl = (q4 >> 1) | ((g & 1) << 1), j = 3 * wm + (fr < 3 ? fr : 0);
#pragma unroll
        for (int i = 0; i < 3; ++i) {
          const unsigned o = fr == i ? 0x3F803F80u : 0u;
          sel[i] = __builtin_bit_cast(bf16x8, u32x4{o, o, o, o});
          cso[i] = (unsigned)(2 * STAGE + ABYTES + (g >> 1) * 6144 + (g & 1) * 1024 + q4 * 128 + p4 * 8 + (((wn * 2 + ((wm + i) & 1)) ^ fl) << 5) + ((3 * wm + i) >> 1) * 2048);
          asm volatile("" : "+v"(sel[i]), "+v"(cso[i]));
        }
        cst = fr < 3 ? WS_CPA + (unsigned)(((hp * 16 + rt) * 192 + (j >> 1) * 64 + wn * 32 + (j & 1) * 16 + 4 * g) * 4) : 0x7fff0000u;
        asm volatile("" : "+v"(cst));
      }
      u64 fall, fal2, fal3, fsv = E + (unsigned)(layer + 1);
      unsigned fao, fa2, vS, vC, dS, dC, dR;
      auto setup = [&]() {
        if (FIRST) { asm volatile("s_load_dwordx2 %0, %1, 0x0\n\ts_waitcnt lgkmcnt(0)" : "=s"(E) : "s"(ws) : "memory"); fall = E + 1u; asm volatile("" : "+v"(fall)); }
        if (FIRST) fa2 = (unsigned)((512 + (lane & 15) * 16 + hp) * FLS);
        vP = (unsigned)((rt * 64 + (lane >> 3)) * 6144 + lw * 2048 + ((hp + (1 << lw)) & 15) * 128 + (lane & 7) * 16);
        vS = FIRST ? (unsigned)(WS_G0 + (m0 + lw * 32 + (lane & 31)) * 16)
                   : WS_ST + (unsigned)((layer - 1) * (SEQ * 128) + rt * 16384 + g * 1024 + lw * 256 + fr * 16);
        vC = (FIRST ? WS_CPA : WS_CSP) + (unsigned)(hp * 12288 + g * 768 + lw * 192 + (fr < 12 ? fr : 0) * 16);
        if (!FIRST) vX = HH ? (unsigned)(hp * 4096 + lw * 1024 + lane * 16) : (unsigned)((rt * 16 + hp) * 1024 + lane * 16);
        vW0 = ((unsigned)(lw * 2048 + lane * 16) & imv) | ((unsigned)((kB * DM + hp * 64 + ((lane & 7) ^ ((lane >> 4) << 1)) * 8) * 2) & ~imv); vW1 = vW0 + ((8 * DM * 2 - 1024u) & ~imv);
        dS = OFF_RS + lw * 256 + (FIRST ? (lane & 31) * 8 : fr * 16 + (g & 1) * 8); dC = OFF_CS + lw * 192 + (fr < 12 ? fr : 0) * 16 + g * 4;
        ko2 = FIRST ? korder(bid & 3, hp) : nx.ko2;
        wos = FIRST ? (const char*)wo : wo_src(ws, wo, layer, hp); tO = !FIRST && im ? 8192u : 131072u;
        asm volatile("" : "+s"(wos), "+s"(tO));
        dR = OFF_R2 + lw * 128 + (FIRST ? (lane & 31) * 4 : fr * 8 + (g & 1) * 4);
        asm volatile("" : "+v"(dR));
        asm volatile("" : "+v"(vS), "+v"(vX), "+v"(vW0), "+v"(vW1), "+v"(dS));
        asm volatile("" : "+s"(ko2));
        if (FIRST || HH) asm volatile("" : "+v"(vC), "+v"(dC), "+v"(fa2));
        if (FIRST) asm volatile("" : "+v"(vP));
      };
      if (!FIRST) {
        fao = (unsigned)((256 + rt * 16 + (lane & 15)) * FLS);
        if (HH) { vY = (unsigned)(bid * 8192 + lw * 2048 + lane * 16); fa2 = (unsigned)((256 + (lane & 15) * 16 + ((hp - ((1 << (lane >> 4)) >> 1)) & 15)) * FLS); asm volatile("" : "+v"(vY), "+v"(fa2)); }
        asm volatile("" : "+v"(fsv), "+v"(fao));
      }
      u32x4 sv[4], cv[4];
      auto ld_c = [&](u32x4& d, unsigned v, int so) { asm volatile("buffer_load_dwordx4 %0, %1, %2, %3 offen sc1" : "=v"(d) : "v"(v), "s"(rsW), "s"(so) : "memory"); };
      float tm, tr;
      unsigned tc;
      auto fin = [&]() {
        if (FIRST) {
          const unsigned tag = (unsigned)E + 1u;
          asm volatile("" : "+v"(sv[0]));
          _Pragma("unroll 1") for (unsigned spins = 0; spins < (1u << 18); ++spins) {
            if (__all(sv[0][1] == tag && sv[0][3] == tag)) break;
            __builtin_amdgcn_s_sleep(1);
            asm volatile("buffer_load_dwordx4 %0, %1, %2, 0 offen sc1\n\ts_waitcnt vmcnt(0)" : "=v"(sv[0]) : "v"(vS), "s"(rsW) : "memory");
          }
          tm = __uint_as_float(sv[0][0]); tr = __uint_as_float(sv[0][2]);
        } else {
          asm volatile("" : "+v"(sv[0]), "+v"(sv[1]), "+v"(sv[2]), "+v"(sv[3]));
          const f32x4 t = sum4(sv);
          const unsigned u = xs4(t[0], t[2], t[1], t[3]);
          auto h = __builtin_amdgcn_permlane32_swap(u, u, false, false);
          const float mean = __uint_as_float(h[0]) * (1.0f / 1024.0f);
          const float var = fmaxf(__uint_as_float(h[1]) * (1.0f / 1024.0f) - mean * mean, 0.f);
          tm = -mean; tr = __builtin_amdgcn_rsqf(var + 1e-5f);
        }
      };
      auto cfin = [&]() {
        if (FIRST || HH) {
          asm volatile("" : "+v"(cv[0]), "+v"(cv[1]), "+v"(cv[2]), "+v"(cv[3]));
          const f32x4 t = sum4(cv);
          tc = xs4(t[0], t[1], t[2], t[3]);
        }
      };
      bf16x8 cf[6];
      f32x4 csum = {0, 0, 0, 0};
      if (FIRST) { vX = (unsigned)(bid * 8192 + lw * 2048 + lane * 16); asm volatile("" : "+v"(vX)); }
#pragma unroll
      for (int kt = 0; kt < 13; ++kt) {
        const int cur = (kt % 3) * STAGE;
        if (!FIRST && kt == 0) { WAITV(4); }
        else if (FIRST && kt == 4 && lw == 1) { WAITVN(N + 2); }
        else if (HH && (kt == 4 || kt == 6)) { WAITVN(N + 1); }
        else if (HH && kt == 5) { WAITVN(N + 2); }
        else if ((!FIRST && kt == 1 && lw == 3) || (!FIRST && kt == 9 && lw == 2) || ((FIRST || HH) && kt == 10 && lw == 3) || (FIRST && (kt == 2 || kt == 4 || kt == 5))) { WAITVN(N + 1); }
        else { WAITVN(N); }
        if (!FIRST && kt == 2 && lw == 3) {
          asm volatile("" : "+v"(fall));
          if (__any(fall != E + (unsigned)layer)) poll_flags(ws, E, 1, rt, hp, lane, layer, false);
        }
        if (!FIRST && kt == 10 && lw == 2) {
          asm volatile("" : "+v"(fal3));
          if (__any(fal3 != E + (unsigned)(layer + 1))) poll_flags(ws, E, 2, rt, hp, lane, layer + 1, false);
        }
        if (HH && kt == 11 && lw == 3) {
          asm volatile("" : "+v"(fal2));
          if (__any(fal2 != E + (unsigned)layer)) {
            _Pragma("unroll 1") for (int j = 0; j < 4; ++j) poll_flags(ws, E, 1, rt, (hp - ((1 << j) >> 1)) & 15, lane, layer, true);
          }
        }
        if (FIRST && kt == 11 && lw == 3) {
          asm volatile("" : "+v"(fal2));
          if (__any(fal2 != E + 1u)) poll_flags(ws, E, 2, rt, hp, lane, 1u, true);
        }
        BAR;
        if (FIRST && kt == 1) {
          asm volatile("" ::: "memory");
#pragma unroll
          for (int i = 0; i < 6; ++i) { const char* t = smem + cso[i >> 1] + (i & 1) * 12288; cf[i] = cat8(tr_read(t), tr_read(t + 512)); }
        }
        if (FIRST && kt >= 2 && kt < 6 && lw == kt - 2) setup();
        if (FIRST && kt >= 6 && kt < 10 && lw == kt - 6) prep(IC<1>{});
        stageA(kt + 3, cur);
        if (FIRST && kt == 1) {
          asm volatile("" : "+v"(sel[0]));
#pragma unroll
          for (int i = 0; i < 6; ++i) csum = MFMA(cf[i], sel[i >> 1], csum);
          asm volatile("" : "+v"(csum));
        }
        stageB(W, kt + 3, cur);
        if (FIRST) {
          if (kt == 1) __builtin_amdgcn_raw_buffer_store_b128(__builtin_bit_cast(u32x4, csum), rsW, cst, 0, 16);
          if (kt == 3 && tid == 320) __builtin_amdgcn_raw_buffer_store_b64(__builtin_bit_cast(u32x2, fall), rsW, WS_FLAGS + (unsigned)(512 + rt * 16 + hp) * FLS, 0, 16);
          if (kt == 3 || kt == 4) glds16s(wo + (kt - 3) * 512, vX, lds_addr(smem) + OFF_JK);
        }
        if (HH && (kt == 3 || kt == 4)) glds16s(wo + (kt - 3) * 512, vY, lds_addr(smem) + OFF_JK);
        if (!FIRST && kt == 3 && tid == 320) __builtin_amdgcn_raw_buffer_store_b64(__builtin_bit_cast(u32x2, fsv), rsW, WS_FLAGS + (unsigned)(512 + rt * 16 + hp) * FLS, 0, 16);
        if (!FIRST && kt == 8 && lw == 2) asm volatile("global_load_dwordx2 %0, %1, %2 sc1" : "=v"(fal3) : "v"(fao), "s"(ws + WS_FLAGS + 256 * FLS) : "memory");
        if (!FIRST && kt == 0 && lw == 3) asm volatile("global_load_dwordx2 %0, %1, %2 sc1" : "=v"(fall) : "v"(fao), "s"(ws + WS_FLAGS) : "memory");
        if ((FIRST || HH) && kt == 9 && lw == 3) asm volatile("global_load_dwordx2 %0, %1, %2 sc1" : "=v"(fal2) : "v"(fa2), "s"(ws + WS_FLAGS) : "memory");
      }
      if (!FIRST) setup();
      WAITVN(N);
      BAR;
#pragma unroll
      for (int i = 0; i < (FIRST ? 1 : 4); ++i) ld_c(sv[i], vS, i * 4096);
      if (FIRST || HH) {
#pragma unroll
        for (int i = 0; i < 4; ++i) ld_c(cv[i], vC, i * 3072);
      }
      if (HH) glds16s(wphi, vX, lds_addr(smem) + OFF_WP + lw * 1024);
      else if (!FIRST) glds16c(ws + WS_CSA, vX, lds_addr(smem) + OFF_CS);
      wo_tile(0); wo_tile(1);
      WAITVN(HH ? 13 : 9);
      BAR;
      if (FIRST) {
        if (lw < 3) { ld_pw(0); ld_pw(4); WAITV(12); } else { WAITV(4); }
      } else {
        prep(IC<1>{});
        WAITV(4);
      }
      cfin();
      fin();
      asm volatile("" : "+v"(tm), "+v"(tr));
      if (FIRST || HH) asm volatile("" : "+v"(tc));
      if (FIRST && lw < 3) { WAITV(8); } else { WAITV(0); }
      lds<f32x2>(sb + dS) = f32x2{tm, tr};
      lds<float>(sb + dR) = tm; lds<float>(sb + dR + 512) = tr;
      if (FIRST || HH) lds<unsigned>(sb + dC) = tc;
      if (!HH) lds<float>(prd) = zero1();
      WAITL0;
    };
    typedef std::integral_constant<bool, true> T_;
    typedef std::integral_constant<bool, false> F_;
    __builtin_amdgcn_s_setprio(3);
    if (first) run(T_{}); else run(F_{});
    __builtin_amdgcn_s_setprio(0);
    BAR;
    if constexpr (HH) {
      u32x4 c0, c1;
      if (layer == 1) {
        const int n = bid * 8 + lw * 2;
        const void* p;
        unsigned a0, a1;
        if (n < 1536) {
          const int r = n % 768, q = r % 384, e = q % 24;
          p = n < 768 ? wn1 : W;
          a0 = (unsigned)((((q / 24) * 64 + (e / 6) * 16 + (lane >> 3)) * 3072 + ((e % 6) >> 1) * 1024 + (r < 384 ? 192 : 704)) * 2);
          a1 = a0 + 8 * 6144 + cB1 * 16; a0 += cB0 * 16;
        } else {
          p = n < 1792 ? wn2 : wo;
          a0 = (unsigned)((((n & 127) * 8 + (lane >> 3)) * DM + ((n & 128) ? 704 : 192) + ((lane & 7) ^ ((lane >> 4) << 1)) * 8) * 2);
          a1 = a0 + 8 * DM * 2;
        }
        p = uni(p);
        gld16(c0, a0, p); gld16(c1, a1, p);
        const u32x4 pz = empty4();
        __builtin_amdgcn_raw_buffer_store_b128(pz, rsW, dm, 0, 16);
        __builtin_amdgcn_raw_buffer_store_b128(pz, rsW, dm, 4096, 16);
        __builtin_amdgcn_raw_buffer_store_b128(pz, rsW, dm2, 0, 16);
      }
#pragma unroll
      for (int t = 2; t < 6; ++t) wo_tile(t);
      if (layer == 1) {
        WAITV(11);
        __builtin_amdgcn_sched_barrier(0);
        asm volatile("" : "+v"(c0), "+v"(c1));
        __builtin_amdgcn_raw_buffer_store_b128(c0, rsW, WS_IMG + (unsigned)(bid * 8192 + lw * 2048 + lane * 16), 0, 16);
        __builtin_amdgcn_raw_buffer_store_b128(c1, rsW, WS_IMG + (unsigned)(bid * 8192 + lw * 2048 + 1024 + lane * 16), 0, 16);
        WAITV(0);
      }
      pwl = vW0; pwm = vW1; kol = ko2;
      asm volatile("" : "=v"(csq[0]), "=v"(csq[1]), "=v"(st01), "=v"(st23), "=v"(wf[0]), "=v"(wf[1]));
      asm volatile("" : "=v"(acc[0][0]), "=v"(acc[0][1]), "=v"(acc[1][0]), "=v"(acc[1][1]));
      asm volatile("" : "=v"(qh[0][0]), "=v"(qh[0][1]), "=v"(qh[1][0]), "=v"(qh[1][1]));
    } else {
      const int lane = lane_id();
#pragma unroll
      for (int t = 2; t < 12; ++t) {
        wo_tile(t);
        if (t == 2 && first) {
        const u32x4 pz = empty4();
        __builtin_amdgcn_raw_buffer_store_b128(pz, rsW, dm, 0, 16);
        __builtin_amdgcn_raw_buffer_store_b128(pz, rsW, dm, 4096, 16);
        __builtin_amdgcn_raw_buffer_store_b128(pz, rsW, dm3, 0, 16);
        __builtin_amdgcn_raw_buffer_store_b128(pz, rsW, dm2, 0, 16);
        if (lw == 3 && lane < 48) __builtin_amdgcn_raw_buffer_store_b128(lds<u32x4>(sb + OFF_CS + lane * 16), rsW, WS_CSA + (unsigned)((rt * 16 + hp) * 1024 + lane * 16), 0, 16);
        }
      }
      if (first) { BAR;   WAITV(0); }
    }
    asm volatile("" : "={v[64:79]}"(xq));
  } else {

    prep(IC<0>{});
    if (first) {
      const int row = m0 + hp * 8 + w * 2 + (lane >> 5);
      asm volatile("s_load_dwordx2 %0, %1, 0x0\n\ts_waitcnt lgkmcnt(0)" : "=s"(E) : "s"(ws) : "memory");
      const unsigned tag = (unsigned)E + 1u;
      static_assert(HH || (OFF_SP == 122880 && OFF_R2 + 1024 <= OFF_JK), "addresses used at the top of the kernel");
      WAITV(0);
      u32x4 xs[4];
#pragma unroll
      for (int i = 0; i < 4; ++i) xs[i] = *(const u32x4*)(smem + OFF_SP + w * 4096 + (lane >> 5) * 2048 + (lane & 31) * 16 + i * 512);
      typedef __attribute__((ext_vector_type(2))) __bf16 bf2;
      const bf2 one2 = __builtin_bit_cast(bf2, 0x3F803F80u);
      float sa[2] = {zero1(), zero1()}, sq[2] = {zero1(), zero1()};
#pragma unroll
      for (int i = 0; i < 16; ++i) {
        const unsigned u = xs[i >> 2][i & 3];
        const bf2 t = __builtin_bit_cast(bf2, u);
        sa[i & 1] = __builtin_amdgcn_fdot2_f32_bf16(t, one2, sa[i & 1], false);
        sq[i & 1] = __builtin_amdgcn_fdot2_f32_bf16(t, t, sq[i & 1], false);
      }
      float sm = r16_sum(sa[0] + sa[1]), m2 = r16_sum(sq[0] + sq[1]);
      {
        auto r = __builtin_amdgcn_permlane16_swap(__float_as_uint(sm), __float_as_uint(sm), false, false);
        auto q = __builtin_amdgcn_permlane16_swap(__float_as_uint(m2), __float_as_uint(m2), false, false);
        sm = __uint_as_float(r[0]) + __uint_as_float(r[1]); m2 = __uint_as_float(q[0]) + __uint_as_float(q[1]);
      }
      const float mean = sm * (1.0f / 1024.0f);
      m2 = fmaxf(m2 - sm * mean, 0.f);
      u32x4 gw = {__float_as_uint(-mean), tag, __float_as_uint(rsqrtf(m2 * (1.0f / 1024.0f) + 1e-5f)), tag};
      if ((lane & 31) == 0) __builtin_amdgcn_raw_buffer_store_b128(gw, rsW, (unsigned)(WS_G0 + row * 16), 0, 16);
      const u64 ko = korder(bid & 3, hp);
      nx.kf = kfirst(ko) | (kfirst(ko >> 16) | (1u << knib(ko, 9))) << 16;
      asm volatile("" : "+s"(nx.kf));
      __builtin_amdgcn_raw_buffer_store_b128(empty4(), rsW, WS_HL + (unsigned)((rt * 16 + hp) * 4096 + tid * 16), 0, 16);
    }
    const int fl = (q4 >> 1) | ((g & 1) << 1);
    const unsigned bo = lds_addr(smem) + ABYTES + (g >> 1) * 6144 + (g & 1) * 1024 + q4 * 128 + p4 * 8;
    const unsigned bo0 = bo + (((wn * 2 + 0) ^ fl) << 5), bo1 = bo + (((wn * 2 + 1) ^ fl) << 5);
    const unsigned ar = lds_addr(smem) + (wm * 64 + fr) * 128;
    const unsigned aK0 = first ? ar + ((g ^ (fr & 7)) << 4) : sb + wm * 8192 + lane * 16, aK1 = first ? ar + (((4 + g) ^ (fr & 7)) << 4) : aK0 + 1024;
    f32x32 ac0, ac1, ac2;
    asm volatile("" :: "v"(aK0), "v"(aK1), "v"(bo0), "v"(bo1));
    if (first) BAR;
    else publish(rsW, tid, 256 + rt * 16 + hp, E + (unsigned)layer);
#define G1_OUT
#define G1_T(a)
#define G1_TEND
#define G1_TCL
    asm volatile(G1_MACROS
        "s_mov_b32 vcc_hi, %[lay]\n"
        ".if %[hh]\n s_branch .Lg1m\n .else\n"
        ".p2align 3\n .Lg1m:\n"
        "G1RDB 0, v16, v17, 0\n G1RDA v12, v13, 0\n G1RDB 1, v16, v17, 0\n"
        "ds_read_b128 v[64:67], v20\n ds_read_b128 v[68:71], v20 offset:2048\n"
        "ds_read_b128 v[72:75], v20 offset:4096\n ds_read_b128 v[76:79], v20 offset:6144\n"
        "s_cmp_eq_u32 vcc_hi, 0\n s_waitcnt lgkmcnt(12)\n"
        "G1SG 0, 1, 1, v12, v13, v16, v17, 0\n"
        "s_mov_b32 vcc_lo, 16\n"
        "G1SG 1, 1, 1, v12, v13, v16, v17, 40960\n"
        "G1SG 2, 1, 2, v12, v13, v16, v17, 40960\n"
        "s_cbranch_scc1 .Lg1a%=\n"
        "s_barrier\n"
        G1_T(88)
        "s_barrier\n"
        G1_T(92)
        "G1RDA v12, v13, 40960\n"
        "s_waitcnt lgkmcnt(4)\n"
        "s_branch .Lg1b%=\n"
        ".Lg1a%=:\n"
        G1_T(88)
        "s_barrier\n"
        G1_T(92)
        "G1RDB 0, v16, v17, 40960\n G1RDA v12, v13, 40960\n G1RDB 1, v16, v17, 40960\n"
        ".p2align 3\n s_nop 0\n"
        ".Lg1b%=:\n"
        "s_waitcnt lgkmcnt(8)\n"
        "G1SG 0, 0, 1, v12, v13, v16, v17, 40960\n"
        "s_barrier\n"
        "G1SG 1, 0, 1, v14, v15, v18, v19, 0\n"
        "G1SG 2, 0, 1, v14, v15, v18, v19, 0\n"
        "s_lshr_b32 vcc_lo, vcc_lo, 1\n s_waitcnt vmcnt(0) lgkmcnt(8)\n"
        "G1SG 0, 0, 1, v14, v15, v18, v19, 0\n"
        "s_barrier\n"
        "G1SG 1, 0, 1, v12, v13, v16, v17, 0\n"
        "G1SG 2, 0, 1, v12, v13, v16, v17, 0\n"
        "s_cbranch_scc0 .Lg1c%=\n s_waitcnt lgkmcnt(8)\n"
        "G1SG 0, 0, 1, v12, v13, v16, v17, 0\n"
        "s_barrier\n"
        "G1SG 1, 0, 1, v12, v13, v16, v17, 40960\n"
        "G1SG 2, 0, 1, v12, v13, v16, v17, 40960\n"
        "s_branch .Lg1b%=\n"
        ".Lg1c%=:\n"
        "s_waitcnt lgkmcnt(8)\n"
        "G1SG 0, 0, 1, v12, v13, v16, v17, 0\n s_bitcmp1_b32 vcc_hi, 0\n"
        G1_T(96)
        "s_cbranch_scc1 .Lg1n\n s_barrier\n"
        G1_SLIDE
        ".endif\n .if %[hh]\n .p2align 3\n s_nop 0\n .Lg1n:\n s_barrier\n"
        G1_HEDGE
        ".endif\n"
        G1_TEND G1_PURGE
        : "=&{v[160:191]}"(ac0), "=&{v[192:223]}"(ac1), "=&{v[224:255]}"(ac2), "=&{v[64:79]}"(xq),
          "=&{v[88:91]}"(csq[1]), "=&{v[148:151]}"(csq[0]), "=&{v[140:143]}"(st01), "=&{v[136:139]}"(st23),
          "+{v[48:63]}"(pa), "+{v[36:39]}"(bias[0]), "+{v[40:43]}"(bias[1]), "+{v[44:47]}"(bias[2]) G1_OUT
        : "{v12}"(aK0), "{v13}"(aK1), "{v14}"(aK0 + 2 * STAGE), "{v15}"(aK1 + 2 * STAGE), "{v16}"(bo0), "{v17}"(bo1),
          "{v18}"(bo0 + 2 * STAGE), "{v19}"(bo1 + 2 * STAGE), "{v20}"(wn ? aK1 : aK0),
          [lay] "s"(layer), [hh] "n"(MODE), [rs] "s"(rsW), [wm] "s"(wm)
        : "memory", "scc", "vcc", "m0", G1_TCL G1_CLOBBER);
    acc[0][0] = quad<0>(ac0); acc[0][1] = quad<1>(ac0); acc[1][0] = quad<6>(ac0); acc[1][1] = quad<7>(ac0);
    asm volatile("" : "=s"(kol), "=s"(wos), "=s"(tO));
    wf[0] = __builtin_bit_cast(bf16x8, bias[0]); wf[1] = __builtin_bit_cast(bf16x8, bias[1]);
    qh[0][0] = quad<4>(ac1); qh[0][1] = quad<5>(ac1); qh[1][0] = quad<2>(ac2); qh[1][1] = quad<3>(ac2);
  }
  ST(layer * 24 + 4)
  {
  const int lane = lane_id(), tid = w * 64 + lane, fr = lane & 15, g = lane >> 4;
  (void)fr; (void)g;
  if constexpr (HH) {
    const f32x4 z4 = {0, 0, 0, 0};
    constexpr float L2E = 1.4426950408889634f;
    const unsigned kvo = WS_KV + (layer >> 1) * KVLB, kfo = kvo + 16u * NH * KVPB + hp * KVFB;
    auto phi2 = [&](const f32x4 (&a)[2][2], f32x4 st, bf16x8 (&qp)[2][2]) {
      f32x4 pm[2][2];
      f32x2 f[2][4];
      asm volatile("" : "+v"(st));
#pragma unroll
      for (int i = 0; i < 8; ++i) {
        const int rb = i >> 2, c = (i >> 1) & 1, e = (i & 1) * 2;
        f[rb][i & 3] = __builtin_elementwise_fma(f32x2{csq[c][e], csq[c][e + 1]}, f32x2{st[2 * rb], st[2 * rb]}, f32x2{a[rb][c][e], a[rb][c][e + 1]});
      }
#pragma unroll
      for (int rb = 0; rb < 2; ++rb) asm volatile("" : "+v"(f[rb][0]), "+v"(f[rb][1]), "+v"(f[rb][2]), "+v"(f[rb][3]));
#pragma unroll
      for (int rb = 0; rb < 2; ++rb) {
        const bf16x8 xf = __builtin_bit_cast(bf16x8, u32x4{pk2(f[rb][0][0], f[rb][0][1]), pk2(f[rb][1][0], f[rb][1][1]), pk2(f[rb][2][0], f[rb][2][1]), pk2(f[rb][3][0], f[rb][3][1])});
        pm[rb][0] = MFMA(wf[0], xf, z4); pm[rb][1] = MFMA(wf[1], xf, z4);
      }
      asm volatile("s_nop 7" : "+v"(pm[0][0]), "+v"(pm[0][1]), "+v"(pm[1][0]), "+v"(pm[1][1]));
      float mx[2], mn[2];
#pragma unroll
      for (int u = 0; u < 2; ++u) max8pn(pm[u][0], pm[u][1], mx[u], mn[u]);
      xr4<true>(mx[0], mn[0], mx[1], mn[1]);
      float ep[2][8], en[2][8], sp[2], sn[2];
#pragma unroll
      for (int u = 0; u < 2; ++u) {
        const float c = st[2 * u + 1] * L2E, mxs = -mx[u] * c, mns = -mn[u] * c;
#pragma unroll
        for (int i = 0; i < 4; ++i) {
          const f32x2 p2 = {pm[u][i >> 1][(i & 1) * 2], pm[u][i >> 1][(i & 1) * 2 + 1]};
          const f32x2 tp = __builtin_elementwise_fma(p2, f32x2{c, c}, f32x2{mxs, mxs});
          const f32x2 tn = __builtin_elementwise_fma(-p2, f32x2{c, c}, f32x2{mns, mns});
          ep[u][2 * i] = __builtin_amdgcn_exp2f(tp[0]); ep[u][2 * i + 1] = __builtin_amdgcn_exp2f(tp[1]);
          en[u][2 * i] = __builtin_amdgcn_exp2f(tn[0]); en[u][2 * i + 1] = __builtin_amdgcn_exp2f(tn[1]);
        }
        const f32x2 a2 = (f32x2{ep[u][0], ep[u][1]} + f32x2{ep[u][2], ep[u][3]}) + (f32x2{ep[u][4], ep[u][5]} + f32x2{ep[u][6], ep[u][7]});
        const f32x2 b2 = (f32x2{en[u][0], en[u][1]} + f32x2{en[u][2], en[u][3]}) + (f32x2{en[u][4], en[u][5]} + f32x2{en[u][6], en[u][7]});
        sp[u] = a2[0] + a2[1]; sn[u] = b2[0] + b2[1];
      }
      xr4<false>(sp[0], sn[0], sp[1], sn[1]);
#pragma unroll
      for (int rb = 0; rb < 2; ++rb) {
        const float ip = __builtin_amdgcn_rcpf(sp[rb]), in = __builtin_amdgcn_rcpf(sn[rb]);
        qp[rb][0] = __builtin_bit_cast(bf16x8, u32x4{pk2(ep[rb][0] * ip, ep[rb][1] * ip), pk2(ep[rb][2] * ip, ep[rb][3] * ip), pk2(ep[rb][4] * ip, ep[rb][5] * ip), pk2(ep[rb][6] * ip, ep[rb][7] * ip)});
        qp[rb][1] = __builtin_bit_cast(bf16x8, u32x4{pk2(en[rb][0] * in, en[rb][1] * in), pk2(en[rb][2] * in, en[rb][3] * in), pk2(en[rb][4] * in, en[rb][5] * in), pk2(en[rb][6] * in, en[rb][7] * in)});
        asm volatile("" : "+v"(qp[rb][0]), "+v"(qp[rb][1]));
      }
    };
    bf16x8 one8 = ones8();
    asm volatile("" : "+v"(one8));
    WAITL0; BAR;
    ST(layer * 24 + 5)
    {
      bf16x8 af[4], v0[4], v1[4];
      asm volatile("" ::: "memory");
#pragma unroll
      for (int sw = 0; sw < 4; ++sw) {
        af[sw] = cat8(tr_read(ppa + sw * (64 * PSTR)), tr_read(ppa + sw * (64 * PSTR) + 16 * PSTR));
        v0[sw] = lds<bf16x8>(pvr + sw * 4096);
        v1[sw] = lds<bf16x8>(pvr + sw * 4096 + 1024);
      }
      __builtin_amdgcn_sched_barrier(0);
      f32x4 kv[3] = {z4, z4, z4};
#pragma unroll
      for (int sw = 0; sw < 4; ++sw) {
        kv[0] = MFMA(af[sw], v0[sw], kv[0]);
        kv[1] = MFMA(af[sw], v1[sw], kv[1]);
        kv[2] = MFMA(af[sw], one8, kv[2]);
      }
      auto pc = [](float x, float y) { const unsigned t = pk2(x, y); return t < EMPTY - 1u ? t : EMPTY - 1u; };
      __builtin_amdgcn_raw_buffer_store_b128(u32x4{pc(kv[0][0], kv[0][1]), pc(kv[0][2], kv[0][3]), pc(kv[1][0], kv[1][1]), pc(kv[1][2], kv[1][3])}, rsW, gkv, 0, 16);
      __builtin_amdgcn_raw_buffer_store_b64(u32x2{pc(kv[2][0], kv[2][1]), pc(kv[2][2], kv[2][3])}, rsW, gkz, 0, 16);
    }
    auto wo_late = [&](int t) { glds2v(wos + knib(kol, t) * tO, pwl, pwm, sb + OFF_WO + t * 8192 + (w & 3) * 2048); };
    ST(layer * 24 + 6)
    u32x4 l1[8];
    const bool red = w == 7 || (w == 6 && (rt & 7) == 0);
    unsigned lo = kvo + (unsigned)((2 * hp + (rt >> 3) + (lane >> 5) * 8 * NH) * KVPB) +
                  (w == 7 ? (unsigned)((rt & 4) * 512 + (g & 1) * 1024 + (rt & 3) * 256 + fr * 16) : (unsigned)(4096 + (lane & 7) * 16));
    unsigned fs = w == 7 ? kfo + (unsigned)((rt >> 2) * 2048 + (lane >> 5) * 1024 + (rt & 3) * 256 + fr * 16 + (g & 1) * 8)
                : (lane & 24) ? 0x7fff0000u : kfo + (unsigned)(8192 + (rt >> 3) * 128 + (lane & 4) * 16 + (lane & 1) * 32 + (lane >> 5) * 16 + (lane & 2) * 4);
    auto l1_load = [&]() {
      asm volatile("" ::: "memory");
#pragma unroll
      for (int p = 0; p < 8; ++p) asm volatile("buffer_load_dwordx4 %0, %1, %2, %3 offen sc1" : "=v"(l1[p]) : "v"(lo), "s"(rsW), "s"(p * (NH * KVPB)) : "memory");
      __builtin_amdgcn_sched_barrier(0);
    };
    if (red) asm volatile("" : "+v"(lo), "+v"(fs));
    if (red) { __builtin_amdgcn_s_sleep(13); l1_load(); }
    bf16x8 qp2[2][2];
    if (!kg) {
      phi2(qh, st23, qp2);
    } else if (red) {
      typedef __attribute__((ext_vector_type(2))) __bf16 bf2;
      unsigned c0 = 0x00003F80u, c1 = 0x3F800000u;
      asm volatile("" : "+v"(c0), "+v"(c1));
      float s4[4];
      _Pragma("unroll 1") for (unsigned spins = 0; spins < (1u << 18); ++spins) {
        float a[8] = {zero1(), zero1(), zero1(), zero1(), zero1(), zero1(), zero1(), zero1()};
        ARRIVED("+v"(l1[0]), "+v"(l1[1]), "+v"(l1[2]), "+v"(l1[3]), "+v"(l1[4]), "+v"(l1[5]), "+v"(l1[6]), "+v"(l1[7]));
#pragma unroll
        for (int p = 0; p < 8; ++p)
#pragma unroll
          for (int e = 0; e < 4; ++e) {
            const unsigned u = l1[p][e];
            a[2 * e] = __builtin_amdgcn_fdot2_f32_bf16(__builtin_bit_cast(bf2, u), __builtin_bit_cast(bf2, c0), a[2 * e], false);
            a[2 * e + 1] = __builtin_amdgcn_fdot2_f32_bf16(__builtin_bit_cast(bf2, u), __builtin_bit_cast(bf2, c1), a[2 * e + 1], false);
          }
#pragma unroll
        for (int i = 0; i < 4; ++i) {
          auto r = __builtin_amdgcn_permlane32_swap(__float_as_uint(a[i]), __float_as_uint(a[i + 4]), false, false);
          s4[i] = __uint_as_float(r[0]) + __uint_as_float(r[1]);
        }
        const float t = s4[0] + s4[2];
        if (__all(t == t)) break;
        unsigned mxw = 0;
#pragma unroll
        for (int p = 0; p < 8; ++p)
#pragma unroll
          for (int e = 0; e < 4; ++e) mxw = l1[p][e] > mxw ? l1[p][e] : mxw;
        if (__all(mxw != EMPTY)) break;
        l1_load();
      }
      WAITV(0);
      unsigned o0 = pk2(s4[0], s4[1]), o1 = pk2(s4[2], s4[3]);
      o0 = o0 < EMPTY - 1u ? o0 : EMPTY - 1u; o1 = o1 < EMPTY - 1u ? o1 : EMPTY - 1u;
      __builtin_amdgcn_raw_buffer_store_b64(u32x2{o0, o1}, rsW, fs, 0, 16);
    }
    ST(layer * 24 + 8)
    WAITL0;
    BAR;
    ST(layer * 24 + 9)
    if (kg) {
#pragma unroll
      for (int i = 6; i < 12; ++i) wo_late(i);
    } else {
      unsigned fo = kfo + (unsigned)(wn * 4096 + lane * 16), fz = kfo + (unsigned)(8192 + wn * 128 + g * 16);
      asm volatile("" : "+v"(fo), "+v"(fz));
      bf16x8 qp[2][2];
      phi2(acc, st01, qp);
      asm volatile(G1_OUTPUT
          :: "{v[224:227]}"(qp[0][0]), "{v[228:231]}"(qp[0][1]), "{v[232:235]}"(qp[1][0]), "{v[236:239]}"(qp[1][1]),
             "{v[240:243]}"(qp2[0][0]), "{v[244:247]}"(qp2[0][1]), "{v[248:251]}"(qp2[1][0]), "{v[252:255]}"(qp2[1][1]),
             [fo] "v"(fo), [fz] "v"(fz), [go] "v"(ogo), [lo] "v"(opo), [rs] "s"(rsW)
          : "memory", "vcc", "scc", "m0", "v152", "v153", "v154", "v155", "v156", "v157", "v158", "v159", "v160", "v161", "v162", "v163", "v164", "v165", "v166", "v167", "v168", "v169", "v170", "v171", "v172", "v173", "v174", "v175", "v176", "v177", "v178", "v179", "v180", "v181", "v182", "v183", "v184", "v185", "v186", "v187", "v188", "v189", "v190", "v191", "v192", "v193", "v194", "v195", "v196", "v197", "v198", "v199", "v200", "v201", "v202", "v203", "v204", "v205", "v206", "v207", "v208", "v209", "v210", "v211", "v212", "v213", "v214", "v215", "v216", "v217", "v218", "v219", "v220", "v221", "v222", "v223");
    }
  }
  ST(layer * 24 + 12)
  if (first && w >= 4 && w < 7) {
    typedef __attribute__((ext_vector_type(2))) __bf16 bf2;
    unsigned c0 = 0x00003F80u, c1 = 0x3F800000u;
    asm volatile("" : "+v"(c0), "+v"(c1));
    const bf2 lo1 = __builtin_bit_cast(bf2, c0), hi1 = __builtin_bit_cast(bf2, c1);
    float a[8] = {zero1(), zero1(), zero1(), zero1(), zero1(), zero1(), zero1(), zero1()};
#pragma unroll
    for (int i = 0; i < 8; ++i) {
      asm volatile("" : "+v"(pw[i]));
#pragma unroll
      for (int e = 0; e < 4; ++e) {
        const unsigned u = pw[i][e];
        a[2 * e] = __builtin_amdgcn_fdot2_f32_bf16(__builtin_bit_cast(bf2, u), lo1, a[2 * e], false);
        a[2 * e + 1] = __builtin_amdgcn_fdot2_f32_bf16(__builtin_bit_cast(bf2, u), hi1, a[2 * e + 1], false);
      }
    }
#pragma unroll
    for (int i = 0; i < 4; ++i) {
      auto r = __builtin_amdgcn_permlane32_swap(__float_as_uint(a[i]), __float_as_uint(a[i + 4]), false, false);
      a[i] = __uint_as_float(r[0]) + __uint_as_float(r[1]);
    }
#pragma unroll
    for (int i = 0; i < 2; ++i) {
      auto r = __builtin_amdgcn_permlane16_swap(__float_as_uint(a[i]), __float_as_uint(a[i + 2]), false, false);
      a[i] = __uint_as_float(r[0]) + __uint_as_float(r[1]);
      a[i] += dpp_ror<8>(a[i]);
    }
    if (!(lane & 8))
      __builtin_amdgcn_raw_buffer_store_b64(u32x2{__float_as_uint(a[0]), __float_as_uint(a[1])}, rsW,
          (unsigned)(WS_CSP + ((((hp + (1 << (w - 4))) & 15) * 16 + rt) * 192 + (w - 4) * 64 + (lane & 7) * 8 + (lane >> 5) * 4 + ((lane >> 4) & 1) * 2) * 4), 0, 16);
  }
  }
}

#undef pcs
#undef prs
#undef opo
#undef ogo
#undef hof
#undef dm
#undef dm2
#undef pcv
#undef pwf
#undef prv
#undef pvf
#undef ppk
#undef ppa
#undef pvr
#undef gkv
#undef gkz
#undef pwl
#undef pwm
#undef plb
#undef prd
#undef dm3

__device__ __forceinline__ void g2_phase(char* smem, char* ws, u64 E, int w, f32x16& xq, Next& nx, int layer, const u16* __restrict__ x0,
                                         const u16* __restrict__ wo, const u16* __restrict__ wnx, u16* __restrict__ out) {
  IDS
  wnx = uni(wnx);
  const unsigned sb = lds_addr(smem);
  const int lw = w & 3, k2 = 16 * lw + (lane >> 3);
  unsigned pxr, rmo;
  auto ends = [&]() {
    const int lane = lane_id(), r0 = w * 16 + (lane >> 3);
    pxr = sb + r0 * 128 + (lane & 7) * 16;
    rmo = (unsigned)(((m0 + r0) * DM + hp * 64 + ((lane & 7) ^ (lane >> 3)) * 8) * 2);
    asm volatile("" : "+v"(pxr), "+v"(rmo));
  };
  constexpr int S0 = 0, S1 = 16384, S2 = 32768, S3 = 147456, S4 = OFF_WO, S5 = 98304, S6 = 114688;
  constexpr int AS[16] = {S0, S1, S2, S3, S0, S1, S4, S2, S3, S0, S1, S4, S5, S3, S0, S6};
  constexpr int OFF_C = 0;
  auto bs = [](int t) { return OFF_WO + (t < 12 ? t : t - 10) * 8192; };
  const char *xa1, *xa2, *wq2;
  u64 fm;
  unsigned vB0, vB1, vXA, po, pst;
  if (w >= 4) {
    constexpr int WT[15] = {0, 8, 12, 14, 16, 18, 20, 18, 16, 14, 12, 12, 10, 10, 8};
    const char* o = ws + WS_O + layer * (SEQ * DM * 2) + rt * 262144;
    const unsigned vA = (unsigned)(lw * 4096 + lane * 16);
    u64 kord = korder(bid & 3, hp);
    auto ldA = [&](int s) { glds4c(o + knib(kord, s) * 16384, vA, sb + AS[s] + lw * 4096); };
    const bool im = img(layer, hp), in = img(layer + 1, hp);
    unsigned vW0 = im ? (unsigned)(lw * 2048 + lane * 16) : (unsigned)((k2 * DM + hp * 64 + ((lane & 7) ^ ((lane >> 4) << 1)) * 8) * 2), vW1 = vW0 + (im ? 0u : 8 * DM * 2 - 1024u);
    const char *wos = wo_src(ws, wo, layer, hp), *wqs = wq_src(ws, wnx, layer + 1, hp);
    unsigned tO = im ? 8192u : 131072u;
    asm volatile("" : "+v"(vW0), "+v"(vW1));
    asm volatile("" : "+s"(wos), "+s"(tO));
    auto ldW = [&](int t) { glds2v(wos + knib(kord, t) * tO, vW0, vW1, sb + bs(t) + lw * 2048); };
    const int fB = (k2 >> 1) & 1;
    vB0 = in ? (unsigned)(lw * 6144 + lane * 16) : (unsigned)((k2 * 3072 + hp * 64 + (((((lane & 7) >> 1) ^ fB) << 1) | (lane & 1)) * 8) * 2);
    vB1 = in ? vB0 : (unsigned)(((k2 + 8) * 3072 + hp * 64 + (((((lane & 7) >> 1) ^ (fB | 2)) << 1) | (lane & 1)) * 8) * 2) - 1024u;
    const u64 kn = nx.kord = korder(kv1(bid), hp);
    nx.ko2 = kord; nx.wq = wqs; nx.tw = in ? 24576u : 393216u;
    const char *wq0 = wqs + hp * (in ? 24576u : 393216u), *wq1 = wqs + knib(kn, 1) * (in ? 24576u : 393216u);
    wq2 = wqs + knib(kn, 2) * (in ? 24576u : 393216u);
    xa1 = ws + WS_X + layer * (SEQ * DM * 2) + knib(kn, 1) * 16384; xa2 = ws + WS_X + layer * (SEQ * DM * 2) + knib(kn, 2) * 16384;
    vXA = (unsigned)(rt * 262144 + lw * 4096 + lane * 16);
    fm = kfirst(kn);
    po = ((fm >> lane) & 1) ? WS_FLAGS + (unsigned)(256 + rt * 16 + lane) * FLS : 0u;
    pst = g != q4 ? 0x7fff0000u : WS_ST + layer * (SEQ * 128) + (unsigned)(((rt * 16 + hp) * 128 + lw * 32 + fr) * 8);
    asm volatile("" : "+v"(vB0), "+v"(vB1), "+v"(vXA), "+v"(po), "+v"(pst));
    asm volatile("" : "+s"(xa1), "+s"(xa2), "+s"(wq2), "+s"(fm), "+s"(nx.kord), "+s"(nx.ko2), "+s"(nx.wq), "+s"(nx.tw));
    asm volatile("" : "+s"(wq0), "+s"(wq1));
    auto ldB = [&](int s, int part) {
      const char* b = s ? wq1 : wq0;
      if (part) glds4v(b, vB0, vB1, vB0, vB1, sb + s * 40960 + 16384 + lw * 6144);
      else glds2v(b + 4096, vB0, vB1, sb + s * 40960 + 16384 + lw * 6144 + 4096);
    };
    ends();
    const char* o1 = o + knib(kord, 1) * 16384;
    asm volatile("" : "+s"(o1), "+s"(kord), "+s"(o));
    asm volatile("" :: "v"(vA));
    if (layer) { BAR;   }
    BAR;
    glds4c(o1, vA, sb + AS[1] + lw * 4096); __builtin_amdgcn_s_sleep(3); ldA(2); __builtin_amdgcn_s_sleep(3); ldA(3); __builtin_amdgcn_s_sleep(3); ldA(4);
    WAITV(12);
    BAR;
    auto step = [&](auto ktc) {
      constexpr int kt = decltype(ktc)::value;
      WAITVN(WT[kt]);
      BAR;
      if constexpr (kt == 1) { ldA(5); ldA(6); }
      if constexpr (kt >= 2 && kt <= 10) ldA(kt + 5);
      if constexpr (kt >= 2 && kt <= 5) ldW(kt + 10);
      if constexpr (kt >= 11) ldB((kt - 11) >> 1, (kt - 11) & 1);
      if constexpr (kt >= 6) { __builtin_amdgcn_s_sleep(kt >= 11 ? 3 : 2); if (layer == 1) __builtin_amdgcn_s_sleep(1); }
    };
    step(IC<1>{}); step(IC<2>{}); step(IC<3>{}); step(IC<4>{}); step(IC<5>{}); step(IC<6>{}); step(IC<7>{});
    step(IC<8>{}); step(IC<9>{}); step(IC<10>{}); step(IC<11>{}); step(IC<12>{}); step(IC<13>{}); step(IC<14>{});
  } else {
    asm volatile("" : "=v"(vB0), "=v"(vB1), "=v"(vXA), "=v"(po), "=v"(pst));
    asm volatile("" : "=s"(xa1), "=s"(xa2), "=s"(wq2), "=s"(fm), "=s"(nx.kord), "=s"(nx.ko2), "=s"(nx.wq), "=s"(nx.tw));
    const int wm = w >> 1, wn = w & 1;
    f32x4 acc[4][2];
    const int fl = (q4 >> 1) | ((g & 1) << 1);
    const int bb0 = (4 * g + q4) * 128 + p4 * 8 + (((wn * 2) ^ fl) << 5), bb1 = (4 * g + q4) * 128 + p4 * 8 + (((wn * 2 + 1) ^ fl) << 5);
    const int abase = wm * 4096 + lane * 16;
    bf16x8 fa[2][2][4], fb[2][2][2];
    unsigned ra[3], rb0[2], rb1[2];
#pragma unroll
    for (int i = 0; i < 3; ++i) { ra[i] = abase + i * 65536; asm volatile("" : "+v"(ra[i])); }
#pragma unroll
    for (int i = 0; i < 2; ++i) { rb0[i] = bb0 + OFF_WO + i * 49152; rb1[i] = bb1 + OFF_WO + i * 49152; asm volatile("" : "+v"(rb0[i]), "+v"(rb1[i])); }
    auto loadf = [&](int kt, bf16x8 (&a)[2][4], bf16x8 (&b)[2][2]) {
#pragma unroll
      for (int s = 0; s < 2; ++s) {
        const char* sA = smem + ra[AS[kt] >> 16] + (AS[kt] & 65535) + s * 8192;
        const int hi = bs(kt) >= OFF_WO + 49152, ob = bs(kt) - OFF_WO - hi * 49152 + s * 4096;
        b[s][0] = cat8(tr_read(smem + rb0[hi] + ob), tr_read(smem + rb0[hi] + ob + 2048));
        b[s][1] = cat8(tr_read(smem + rb1[hi] + ob), tr_read(smem + rb1[hi] + ob + 2048));
#pragma unroll
        for (int rb = 0; rb < 4; ++rb) a[s][rb] = *(const bf16x8*)(sA + rb * 1024);
      }
    };
    auto mma = [&](bf16x8 (&a)[2][4], bf16x8 (&b)[2][2]) {
#pragma unroll
      for (int i = 0; i < 16; ++i) {
        const int rb = i >> 2, cb = ((i >> 1) ^ rb) & 1, s = ((i + 1) >> 1) & 1;
        acc[rb][cb] = MFMA(b[s][cb], a[s][rb], acc[rb][cb]);
      }
    };
    unsigned fo = lane < 16 && lane != hp ? WS_FLAGS + (unsigned)(rt * 16 + lane) * FLS : 0u;
    unsigned kf = nx.kf & 0xffffu, kf2 = nx.kf >> 16;
    unsigned jo = (unsigned)((((rt & 7) * 128 + (w - 1) * 64 + lane) * 3072 + (hp & 3) * 1024 + (hp | 3) * 64) * 2);
    unsigned fo1 = ((kf >> (lane & 31)) & 1) ? fo : 0u, fo2 = ((kf2 >> (lane & 31)) & 1) ? fo : 0u;
    fo = (((kf | kf2) >> (lane & 31)) & 1) ? 0u : fo;
    asm volatile("" : "+v"(fo), "+v"(fo1), "+v"(fo2));
    asm volatile("" : "+s"(kf), "+s"(kf2));
    publish(rsW, tid, rt * 16 + hp, E + (unsigned)(layer + 1));
    ST(layer * 24 + 13)
    u64 fl1;
    asm volatile("" : "=v"(fl1));
    if (w == 0) asm volatile("global_load_dwordx2 %0, %1, %2 sc1" : "+v"(fl1) : "v"(fo1), "s"(ws) : "memory");
    if (layer == 0) {
      const unsigned xo = (unsigned)(((m0 + wm * 64 + fr) * DM + hp * 64 + wn * 32 + g * 8) * 2);
      asm volatile("global_load_dwordx4 v[64:67], %1, %3\n\tglobal_load_dwordx4 v[68:71], %1, %4\n\tglobal_load_dwordx4 v[72:75], %2, %3\n\tglobal_load_dwordx4 v[76:79], %2, %4"
                   : "+{v[64:79]}"(xq) : "v"(xo), "v"(xo + 32 * DM * 2), "s"(x0), "s"(x0 + 16 * DM) : "memory");
    }
    loadf(0, fa[0], fb[0]);
    unsigned junk;
    asm volatile("" : "=v"(junk));
    if (layer < 2 && w == 3 && (bid >> 6) == 0) code_touch(junk, bid, lane);
    if (layer == 0 && (hp & 7) < 3 && (w == 1 || w == 2))
      asm volatile("global_load_dword %0, %1, %2" : "+v"(junk) : "v"(jo), "s"(wnx) : "memory");
    __builtin_amdgcn_sched_barrier(0);
    bf16x8 id[2];
#pragma unroll
    for (int cb = 0; cb < 2; ++cb) {
      const unsigned one = (g >> 1) == cb && (g & 1) == (fr >> 3) ? 0x3F80u << ((fr & 1) * 16) : 0u;
      u32x4 t;
#pragma unroll
      for (int j = 0; j < 4; ++j) t[j] = ((fr >> 1) & 3) == j ? one : 0u;
      id[cb] = __builtin_bit_cast(bf16x8, t);
      asm volatile("" : "+v"(id[cb]));
    }
    const int l2 = lane_id(), ch = (l2 >> 5) | ((l2 >> 3) & 2), f2 = l2 & 15;
    unsigned pl = (unsigned)(wm * 8192 + wn * 1024 + ch * 256 + f2 * 16);
    unsigned pg = layer < 3 ? WS_X + layer * (SEQ * DM * 2) + (unsigned)((rt * 16 + hp) * 16384) + pl : 0x7fff0000u;
    pl = sb + OFF_C + (layer < 3 ? pl : (unsigned)((wm * 64 + f2) * 128 + (((wn * 4 + ch) ^ (f2 & 7)) << 4)));
    asm volatile("" : "+v"(pl), "+v"(pg));
    ends();
#pragma unroll
    for (int rb = 0; rb < 4; ++rb) { acc[rb][0] = f32x4{0, 0, 0, 0}; acc[rb][1] = f32x4{0, 0, 0, 0}; }
    mma(fa[0], fb[0]);
    __builtin_amdgcn_sched_barrier(0);
    WAITL0;
    u64 miss = 0;
    if (w == 0) { ARRIVED("+v"(fl1)); if (__ballot(fl1 != E + (unsigned)(layer + 1)) & kf) poll_eq(ws, fo1, E + (unsigned)(layer + 1), kf, kf); miss = 0xffffu & ~(kf | kf2 | (1u << hp)); }
    ST(layer * 24 + 14)
    __builtin_amdgcn_sched_barrier(0);
    BAR;
    __builtin_amdgcn_sched_barrier(0);
    asm volatile("" ::: "memory");
    if (layer == 0) WAITV(0);
    __builtin_amdgcn_sched_barrier(0);
    asm volatile("" : "+{v[64:79]}"(xq));
    const bf16x8 xf[4] = {__builtin_bit_cast(bf16x8, quad<0>(xq)), __builtin_bit_cast(bf16x8, quad<1>(xq)), __builtin_bit_cast(bf16x8, quad<2>(xq)), __builtin_bit_cast(bf16x8, quad<3>(xq))};
#pragma unroll
    for (int rb = 0; rb < 4; ++rb) { acc[rb][0] = MFMA(id[0], xf[rb], acc[rb][0]); acc[rb][1] = MFMA(id[1], xf[rb], acc[rb][1]); }
    if (w == 0) poll_eq(ws, fo2, E + (unsigned)(layer + 1), kf2, kf2);
    u64 fl2;
    asm volatile("" : "=v"(fl2));
    auto step = [&](auto ktc) {
      constexpr int kt = decltype(ktc)::value;
      __builtin_amdgcn_sched_barrier(0);
      if (kt == 5 && miss) {
        ARRIVED("+v"(fl2));
        if (__ballot(fl2 != E + (unsigned)(layer + 1)) & miss) poll_eq(ws, fo, E + (unsigned)(layer + 1), miss, ~0ull);
      }
      WAITL0;
      BAR;
      if (kt == 2 && miss) asm volatile("global_load_dwordx2 %0, %1, %2 sc1" : "+v"(fl2) : "v"(fo), "s"(ws) : "memory");
      if (kt == 0) { ST(layer * 24 + 15) }
      __builtin_amdgcn_sched_barrier(0);
      loadf(kt + 1, fa[(kt + 1) & 1], fb[(kt + 1) & 1]);
      if (kt == 0) return;
      mma(fa[kt & 1], fb[kt & 1]);
#pragma unroll
      for (int i = 0; i < 4; ++i) {
        __builtin_amdgcn_sched_group_barrier(0x008, 1, 0);
        __builtin_amdgcn_sched_group_barrier(0x100, 1, 0);
        __builtin_amdgcn_sched_group_barrier(0x008, 1, 0);
        __builtin_amdgcn_sched_group_barrier(0x100, 3, 0);
      }
#pragma unroll
      for (int i = 0; i < 8; ++i) __builtin_amdgcn_sched_group_barrier(0x008, 1, 0);
    };
    step(IC<0>{}); step(IC<1>{}); step(IC<2>{}); step(IC<3>{}); step(IC<4>{}); step(IC<5>{}); step(IC<6>{}); step(IC<7>{});
    step(IC<8>{}); step(IC<9>{}); step(IC<10>{}); step(IC<11>{}); step(IC<12>{}); step(IC<13>{}); step(IC<14>{});
    WAITV(0);
    asm volatile("" :: "v"(junk));
    __builtin_amdgcn_sched_barrier(0);
    u32x4 xv[4];
    auto put = [&](int rb) {
      auto a = __builtin_amdgcn_permlane16_swap(pk2(acc[rb][0][0], acc[rb][0][1]), pk2(acc[rb][1][0], acc[rb][1][1]), false, false);
      auto b = __builtin_amdgcn_permlane16_swap(pk2(acc[rb][0][2], acc[rb][0][3]), pk2(acc[rb][1][2], acc[rb][1][3]), false, false);
      xv[rb] = u32x4{a[0], b[0], a[1], b[1]};
      __builtin_amdgcn_raw_buffer_store_b128(xv[rb], rsW, pg + (rb & 1) * 2048, (rb >> 1) * 4096, 16);
    };
#pragma unroll
    for (int rb = 0; rb < 4; ++rb)
#pragma unroll
      for (int cb = 0; cb < 2; ++cb) acc[rb][cb] = MFMA(fb[1][0][cb], fa[1][0][rb], acc[rb][cb]);
#pragma unroll
    for (int rb = 0; rb < 4; ++rb) {
      __builtin_amdgcn_sched_barrier(0);
#pragma unroll
      for (int cb = 0; cb < 2; ++cb) acc[rb][cb] = MFMA(fb[1][1][cb], fa[1][1][rb], acc[rb][cb]);
      __builtin_amdgcn_sched_barrier(0);
      if (rb >= 2) put(rb - 2);
    }
    __builtin_amdgcn_sched_barrier(0);
    ST(layer * 24 + 16)
    put(2); put(3);
    __builtin_amdgcn_sched_barrier(0);
#pragma unroll
    for (int rb = 0; rb < 4; ++rb) lds<u32x4>(pl + rb * 2048) = xv[rb];
  }
  if (layer == 3) {
    WAITL0; BAR;
    asm volatile("" ::: "memory");
    const __amdgpu_buffer_rsrc_t rsO = __builtin_amdgcn_make_buffer_rsrc((void*)uni(out), (short)0, SEQ * DM * 2, 0x00020000);
    __builtin_amdgcn_raw_buffer_store_b128(lds<u32x4>(pxr + OFF_C), rsO, rmo, 0, 16);
    __builtin_amdgcn_raw_buffer_store_b128(lds<u32x4>(pxr + OFF_C + 1024), rsO, rmo, 8 * DM * 2, 16);
  }
  ST(layer * 24 + 17)
  if (layer < 3 && w >= 4) {
    WAITV(0); BAR;
    asm volatile("" ::: "memory");
    glds4v(wq2, vB0, vB1, vB0, vB1, sb + 2 * 40960 + 16384 + lw * 6144);
    glds2v(wq2 + 4096, vB0, vB1, sb + 2 * 40960 + 16384 + lw * 6144 + 4096);
    __builtin_amdgcn_s_setprio(3);
    bf16x8 xn[4];
#pragma unroll
    for (int i = 0; i < 4; ++i) xn[i] = lds<bf16x8>(sb + OFF_C + lw * 4096 + lane * 16 + i * 1024);
    const bf16x8 ones = ones8();
    u32x2 h[2];
#pragma unroll
    for (int i = 0; i < 2; ++i) {
      const f32x4 z4 = {0, 0, 0, 0};
      const f32x4 sa = MFMA(ones, xn[2 * i + 1], MFMA(ones, xn[2 * i], z4)), sq = MFMA(xn[2 * i + 1], xn[2 * i + 1], MFMA(xn[2 * i], xn[2 * i], z4));
      const float a = (lane & 1) ? sq[1] : sq[0], b = (lane & 1) ? sq[3] : sq[2];
      h[i] = u32x2{__float_as_uint(sa[0]), __float_as_uint((lane & 2) ? b : a)};
    }
    __builtin_amdgcn_s_setprio(0);
    u64 fl;
    asm volatile("" : "=v"(fl), "+v"(pst) : "v"(h[0]), "v"(h[1]));
    if (lw == 0) asm volatile("global_load_dwordx2 %0, %1, %2 sc1" : "+v"(fl) : "v"(po), "s"(ws) : "memory");
    __builtin_amdgcn_raw_buffer_store_b64(h[0], rsW, pst, 0, 16);
    __builtin_amdgcn_raw_buffer_store_b64(h[1], rsW, pst + 128, 0, 16);
    if (lw == 0) {
      WAITV(2);
      __builtin_amdgcn_sched_barrier(0);
      asm volatile("" : "+v"(fl));
      if (__ballot(fl != E + (unsigned)(layer + 1)) & fm) poll_eq(ws, po, E + (unsigned)(layer + 1), fm, ~0ull);
    }
    BAR;
    glds4c(xa1, vXA, sb + 40960 + lw * 4096); glds4c(xa2, vXA, sb + 2 * 40960 + lw * 4096);
  }
  ST(layer * 24 + 18)
}

__global__ __launch_bounds__(512, 2) void mega_kernel(const u16* __restrict__ x0, const u16* __restrict__ wqkvA,
                                                      const u16* __restrict__ woA, const u16* __restrict__ wqkvB,
                                                      const u16* __restrict__ wphi, const u16* __restrict__ woB,
                                                      u16* __restrict__ out, char* __restrict__ ws) {
  extern __shared__ __attribute__((aligned(16))) char smem[];
  {
    unsigned long long pc;
    unsigned t;
    asm volatile("s_getpc_b64 %0\n.Lmk_pc:\n\tv_lshl_add_u32 %1, %2, 9, %3\n\tv_lshlrev_b32 %1, 2, %1\n\tv_and_b32 %1, 0xffffff80, %1\n\ts_mov_b32 m0, %4\n\t"
                 "v_min_u32 %1, .Lmk_end-.Lmk_pc-4, %1\n\tglobal_load_lds_dword %1, %0" : "=&s"(pc), "=&v"(t) : "s"(blockIdx.x >> 3), "v"(threadIdx.x), "n"(LDS_TOTAL - 1024) : "memory", "m0");
  }
  const int w = __builtin_amdgcn_readfirstlane(threadIdx.x >> 6);
  u64 E = 0;
  {
    const int bid = blockIdx.x, lane = threadIdx.x & 63;
    const int rt = 2 * (bid & 4) + ((bid >> 3) & 7), hp = 4 * (bid & 3) + (bid >> 6), m0 = rt * 128;
    if (w < 4) {
      const unsigned xo = (unsigned)((m0 + hp * 8 + w * 2) * (DM * 2) + lane * 16);
      glds4v(x0, xo, xo, xo, xo, lds_addr(smem) + 122880 + w * 4096);
    } else {
      const int lw = w & 3;
      const int kB = 16 * lw + (lane >> 3), fB = (kB >> 1) & 1;
      const unsigned vB0 = (unsigned)((kB * 3072 + hp * 64 + (((((lane & 7) >> 1) ^ fB) << 1) | (lane & 1)) * 8) * 2);
      const unsigned vB1 = (unsigned)(((kB + 8) * 3072 + hp * 64 + (((((lane & 7) >> 1) ^ (fB | 2)) << 1) | (lane & 1)) * 8) * 2) - 1024u;
      const unsigned vA = (unsigned)(((m0 + lw * 32 + (lane >> 3)) * DM + ((lane & 7) ^ (lane >> 3)) * 8) * 2);
#pragma unroll
      for (int s = 0; s < 2; ++s) {
        const int kt = (rt & 8) + s;
        const unsigned sA = lds_addr(smem) + s * 40960, sB = sA + 16384;
        glds4v(x0 + kt * 64, vA, vA + 15360u, vA + 2 * 15360u, vA + 3 * 15360u, sA + lw * 4096);
        const u16* b = wqkvA + (size_t)kt * 64 * 3072;
        glds4v(b, vB0, vB1, vB0, vB1, sB + lw * 6144);
        glds2v(b + 2048, vB0, vB1, sB + lw * 6144 + 4096);
      }
    }
  }
  asm volatile("" :: "s"(woA), "s"(wqkvB), "s"(wphi), "s"(woB), "s"(out), "s"(ws));
  f32x16 xq;
  Next nx;
  asm volatile("" : "=s"(nx.kord), "=s"(nx.ko2), "=s"(nx.wq), "=s"(nx.tw), "=s"(nx.kf));
#pragma unroll 1
  for (int layer = 0; layer < 4; ++layer) {
    if (layer & 1) g1_phase<1>(smem, ws, E, w, xq, nx, layer, x0, wqkvB, woB, wphi, wqkvA, woA);
    else g1_phase<0>(smem, ws, E, w, xq, nx, layer, x0, wqkvA, woA, nullptr, wqkvB, woB);
    g2_phase(smem, ws, E, w, xq, nx, layer, x0, (layer & 1) ? woB : woA, (layer & 1) ? wqkvA : wqkvB, out);
  }
  asm volatile(".Lmk_end:");
  WAITV(0);
  if (blockIdx.x == 0 && w == 0 && lane_id() == 0) __hip_atomic_store((gu64*)ws, E + 16u, __ATOMIC_RELAXED, __HIP_MEMORY_SCOPE_AGENT);
}

extern "C" void kernel_launch(void* const* d_in, const int* in_sizes, int n_in,
                              void* d_out, int out_size, void* d_ws, size_t ws_size,
                              hipStream_t stream) {
  hipFuncSetAttribute((const void*)mega_kernel, hipFuncAttributeMaxDynamicSharedMemorySize, LDS_TOTAL);
  mega_kernel<<<256, 512, LDS_TOTAL, stream>>>((const u16*)d_in[0], (const u16*)d_in[1], (const u16*)d_in[2], (const u16*)d_in[4],
                                               (const u16*)d_in[5], (const u16*)d_in[6], (u16*)d_out, (char*)d_ws);
}
```

```cpp
#include <hip/hip_runtime.h>
#include <hip/hip_bf16.h>
#include <stdint.h>
#include <type_traits>
#pragma clang diagnostic ignored "-Winline-asm"
#pragma clang diagnostic ignored "-Wunused-value"

typedef unsigned short u16;
typedef __attribute__((ext_vector_type(8))) short bf16x8;
typedef __attribute__((ext_vector_type(4))) short bf16x4;
typedef __attribute__((ext_vector_type(4))) float f32x4;
typedef __attribute__((ext_vector_type(4))) unsigned u32x4;
typedef __attribute__((ext_vector_type(2))) unsigned u32x2;
typedef __attribute__((ext_vector_type(8))) float f32x8;
typedef __attribute__((ext_vector_type(16))) unsigned u32x16;
typedef __attribute__((ext_vector_type(16))) float f32x16;
typedef __attribute__((ext_vector_type(32))) float f32x32;
template <int I, typename T> __device__ __forceinline__ f32x4 quad(const T& v) { return __builtin_shufflevector(v, v, 4 * I, 4 * I + 1, 4 * I + 2, 4 * I + 3); }

constexpr int SEQ = 2048, DM = 1024, NH = 32;
#define ST(i)
constexpr int KVP = 64 * 32 + 64;

typedef __attribute__((ext_vector_type(2))) float f32x2;
typedef __attribute__((ext_vector_type(2))) __bf16 bf16x2_t;
__device__ __forceinline__ unsigned pk2(float a, float b) {
  f32x2 v = {a, b};
  bf16x2_t r = __builtin_convertvector(v, bf16x2_t);
  return __builtin_bit_cast(unsigned, r);
}

__device__ __forceinline__ bf16x4 tr_read(const char* p) {
  return __builtin_amdgcn_ds_read_tr16_b64_v4i16((__attribute__((address_space(3))) bf16x4*)p);
}
template <typename T> __device__ __forceinline__ __attribute__((address_space(3))) T& lds(unsigned a) { return *(__attribute__((address_space(3))) T*)(size_t)a; }
__device__ __forceinline__ bf16x4 tr_read(unsigned a) { return __builtin_amdgcn_ds_read_tr16_b64_v4i16(&lds<bf16x4>(a)); }
__device__ __forceinline__ bf16x8 cat8(bf16x4 a, bf16x4 b) {
  bf16x8 f = {a[0], a[1], a[2], a[3], b[0], b[1], b[2], b[3]};
  return f;
}
__device__ __forceinline__ void glds16s(const void* sbase, unsigned voff, unsigned lds_base) {
  asm volatile("s_mov_b32 m0, %2\n\ts_nop 0\n\tglobal_load_lds_dwordx4 %0, %1" :: "v"(voff), "s"(sbase), "s"(lds_base) : "memory", "m0");
}
__device__ __forceinline__ void glds16c(const void* sbase, unsigned voff, unsigned lds_base) {
  asm volatile("s_mov_b32 m0, %2\n\ts_nop 0\n\tglobal_load_lds_dwordx4 %0, %1 sc1" :: "v"(voff), "s"(sbase), "s"(lds_base) : "memory", "m0");
}
#define GL_ "\n\tglobal_load_lds_dwordx4 "
__device__ __forceinline__ void glds4c(const void* sbase, unsigned v, unsigned lds_base) {
  asm volatile("s_mov_b32 m0, %2\n\ts_nop 0" GL_ "%0, %1 sc1" GL_ "%0, %1 offset:1024 sc1" GL_ "%0, %1 offset:2048 sc1" GL_ "%0, %1 offset:3072 sc1"
               :: "v"(v), "s"(sbase), "s"(lds_base) : "memory", "m0");
}
__device__ __forceinline__ void glds4v(const void* sbase, unsigned v0, unsigned v1, unsigned v2, unsigned v3, unsigned lds_base) {
  asm volatile("s_mov_b32 m0, %5\n\ts_nop 0" GL_ "%0, %4 sc1" GL_ "%1, %4 offset:1024 sc1" GL_ "%2, %4 offset:2048 sc1" GL_ "%3, %4 offset:3072 sc1"
               :: "v"(v0), "v"(v1), "v"(v2), "v"(v3), "s"(sbase), "s"(lds_base) : "memory", "m0");
}
__device__ __forceinline__ void glds2v(const void* sbase, unsigned v0, unsigned v1, unsigned lds_base) {
  asm volatile("s_mov_b32 m0, %3\n\ts_nop 0" GL_ "%0, %2 sc1" GL_ "%1, %2 offset:1024 sc1" :: "v"(v0), "v"(v1), "s"(sbase), "s"(lds_base) : "memory", "m0");
}
__device__ __forceinline__ void gld16(u32x4& d, unsigned voff, const void* sbase) {
  asm volatile("global_load_dwordx4 %0, %1, %2" : "=v"(d) : "v"(voff), "s"(sbase) : "memory");
}
__device__ __forceinline__ unsigned lds_addr(const void* p) { return (unsigned)(size_t)p; }
#define WAITV(n) asm volatile("s_waitcnt vmcnt(" #n ")" ::: "memory")
#define WAITVN(n) asm volatile("s_waitcnt vmcnt(%0)" :: "n"(n) : "memory")
#define WAITL0 do { asm volatile("" ::: "memory"); __builtin_amdgcn_s_waitcnt(0xC07F); asm volatile("" ::: "memory"); } while (0)
#define BAR __builtin_amdgcn_s_barrier()
#define ARRIVED(...) do { WAITV(0); __builtin_amdgcn_sched_barrier(0); asm volatile("" : __VA_ARGS__); } while (0)

template <int N> __device__ __forceinline__ float dpp_ror(float x) {
  return __uint_as_float(__builtin_amdgcn_update_dpp(0u, __float_as_uint(x), 0x120 + N, 0xf, 0xf, false));
}
__device__ __forceinline__ float r16_sum(float x) { x += dpp_ror<8>(x); x += dpp_ror<4>(x); x += dpp_ror<2>(x); return x + dpp_ror<1>(x); }
__device__ __forceinline__ void max8pn(const f32x4& a, const f32x4& b, float& mx, float& mn) {
  asm("v_max3_f32 %0, %2, %3, %4\n\tv_max3_f32 %1, -%2, -%3, -%4\n\tv_max3_f32 %0, %0, %5, %6\n\tv_max3_f32 %1, %1, -%5, -%6\n\t"
      "v_max3_f32 %0, %0, %7, %8\n\tv_max3_f32 %1, %1, -%7, -%8\n\tv_max_f32 %0, %0, %9\n\tv_max_f32 %1, %1, -%9\n\ts_nop 0"
      : "=&v"(mx), "=&v"(mn) : "v"(a[0]), "v"(a[1]), "v"(a[2]), "v"(a[3]), "v"(b[0]), "v"(b[1]), "v"(b[2]), "v"(b[3]));
}
__device__ __forceinline__ float vmaxf_n(float a, float b) { float r; asm("v_max_f32 %0, %1, %2\n\ts_nop 0" : "=v"(r) : "v"(a), "v"(b)); return r; }
template <bool MAXOP> __device__ __forceinline__ float rop(float a, float b) { return MAXOP ? vmaxf_n(a, b) : a + b; }
template <bool MAXOP> __device__ __forceinline__ void xr4(float& a, float& b, float& c, float& d) {
  auto r = __builtin_amdgcn_permlane16_swap(__float_as_uint(a), __float_as_uint(b), false, false);
  auto q = __builtin_amdgcn_permlane16_swap(__float_as_uint(c), __float_as_uint(d), false, false);
  const unsigned ab = __float_as_uint(rop<MAXOP>(__uint_as_float(r[0]), __uint_as_float(r[1])));
  const unsigned cd = __float_as_uint(rop<MAXOP>(__uint_as_float(q[0]), __uint_as_float(q[1])));
  auto t = __builtin_amdgcn_permlane32_swap(ab, cd, false, false);
  const unsigned u = __float_as_uint(rop<MAXOP>(__uint_as_float(t[0]), __uint_as_float(t[1])));
  auto h = __builtin_amdgcn_permlane32_swap(u, u, false, false);
  auto p0 = __builtin_amdgcn_permlane16_swap(h[0], h[0], false, false);
  auto p1 = __builtin_amdgcn_permlane16_swap(h[1], h[1], false, false);
  a = __uint_as_float(p0[0]); b = __uint_as_float(p0[1]); c = __uint_as_float(p1[0]); d = __uint_as_float(p1[1]);
}

__device__ __forceinline__ unsigned xs4(float a, float b, float c, float d) {
  auto r = __builtin_amdgcn_permlane16_swap(__float_as_uint(a), __float_as_uint(b), false, false);
  auto q = __builtin_amdgcn_permlane16_swap(__float_as_uint(c), __float_as_uint(d), false, false);
  auto t = __builtin_amdgcn_permlane32_swap(__float_as_uint(__uint_as_float(r[0]) + __uint_as_float(r[1])), __float_as_uint(__uint_as_float(q[0]) + __uint_as_float(q[1])), false, false);
  return __float_as_uint(__uint_as_float(t[0]) + __uint_as_float(t[1]));
}

__device__ __forceinline__ f32x4 sum4(const u32x4 (&v)[4]) {
  float r0, r1, r2, r3, t0, t1, t2, t3;
  asm("v_add_f32 %0, %8, %12\n\tv_add_f32 %1, %9, %13\n\tv_add_f32 %2, %10, %14\n\tv_add_f32 %3, %11, %15\n\t"
      "v_add_f32 %4, %16, %20\n\tv_add_f32 %5, %17, %21\n\tv_add_f32 %6, %18, %22\n\tv_add_f32 %7, %19, %23\n\t"
      "v_add_f32 %0, %0, %4\n\tv_add_f32 %1, %1, %5\n\tv_add_f32 %2, %2, %6\n\tv_add_f32 %3, %3, %7\n\ts_nop 0"
      : "=&v"(r0), "=&v"(r1), "=&v"(r2), "=&v"(r3), "=&v"(t0), "=&v"(t1), "=&v"(t2), "=&v"(t3)
      : "v"(v[0][0]), "v"(v[0][1]), "v"(v[0][2]), "v"(v[0][3]), "v"(v[1][0]), "v"(v[1][1]), "v"(v[1][2]), "v"(v[1][3]),
        "v"(v[2][0]), "v"(v[2][1]), "v"(v[2][2]), "v"(v[2][3]), "v"(v[3][0]), "v"(v[3][1]), "v"(v[3][2]), "v"(v[3][3]));
  return f32x4{r0, r1, r2, r3};
}
#define MFMA(a, b, c) __builtin_amdgcn_mfma_f32_16x16x32_bf16(a, b, c, 0, 0, 0)

typedef unsigned long long u64;
typedef __attribute__((address_space(1))) unsigned long long gu64;
constexpr unsigned WS_FLAGS = 4096;
constexpr int FLS = 64;
constexpr unsigned WS_G0 = 65536;
constexpr unsigned WS_CSP = WS_G0 + SEQ * 16;
constexpr unsigned WS_ST = WS_CSP + 16 * 16 * 768;
constexpr unsigned WS_X = 1u << 21;
constexpr unsigned WS_O = WS_X + 3u * SEQ * DM * 2;
constexpr unsigned KVPB = KVP * 2, KVFB = KVP * 4;
constexpr unsigned EMPTY = 0xffffffffu;
constexpr unsigned WS_KV = WS_O + 4u * SEQ * DM * 2;
constexpr unsigned KVLB = 16u * NH * KVPB + 16u * KVFB;
constexpr unsigned WS_HL = WS_KV + 2u * KVLB;
constexpr unsigned WS_CSA = WS_HL + 512u * 4096;
constexpr unsigned WS_CPA = WS_CSA + 256u * 1024;
constexpr unsigned WS_IMG = WS_CPA + 16 * 16 * 768;
constexpr unsigned WS_END = WS_IMG + 2048 * 1024;
constexpr int LDS_TOTAL = 163840;
constexpr int OFF_WO = 49152;
struct Next { u64 kord, ko2; const char* wq; unsigned tw, kf; };
template <int V> using IC = std::integral_constant<int, V>;
__device__ __forceinline__ bf16x8 ones8() { unsigned e = 0x3F803F80u; asm volatile("" : "+v"(e)); return __builtin_bit_cast(bf16x8, u32x4{e, e, e, e}); }
__device__ __forceinline__ float zero1() { float z; asm volatile("v_mov_b32 %0, 0" : "=v"(z)); return z; }
__device__ __forceinline__ u32x4 empty4() { unsigned e = EMPTY; asm volatile("" : "+v"(e)); return u32x4{e, e, e, e}; }
__device__ __forceinline__ u64 korder(int v, int own) {
  const u64 t = v == 0 ? 0x3b210fedca987654ull : v == 1 ? 0xb37654210fedca98ull : v == 2 ? 0x3ba987654210fedcull : 0xb3fedca987654210ull;
  const u64 inv = v == 0 ? 0xa987e6543210fdcbull : v == 1 ? 0x6543f210dcbae987ull : v == 2 ? 0x3210edcba987f654ull : 0xdcbaf9876543e210ull;
  const u64 low = (1ull << (4 * ((inv >> (4 * own)) & 15))) - 1ull;
  return (u64)own | ((t & low) << 4) | (t & ~((low << 4) | 15ull));
}
__device__ __forceinline__ u64 korder0(int rt) {
  const int r = rt & 7;
  const u64 own = (0x765432765432ull >> (4 * (r < 6 ? r : r - 6))) & 0xffffffull, oth = 0x7654321076543210ull >> (4 * r);
  const u64 t = 0x10ull | ((oth & 15ull) << 8) | (own << 12) | ((oth >> 4) << 36);
  return t | (rt < 8 ? 0x8888888000000800ull : 0x0000000888888088ull);
}
__device__ __forceinline__ int knib(u64 t, int s) { return (int)(t >> (4 * s)) & 15; }
__device__ __forceinline__ unsigned kfirst(u64 t) { return (1u << knib(t, 1)) | (1u << knib(t, 2)) | (1u << knib(t, 3)) | (1u << knib(t, 4)); }
__device__ __forceinline__ int kv1(int bid) { return (bid + (0x4924 >> ((bid >> 2) & 14))) & 3; }

__device__ __forceinline__ bool img(int layer, int hp) { return layer >= 2 && (hp & 7) == 3; }
__device__ __forceinline__ const char* wq_src(const char* ws, const void* W, int layer, int hp) { return img(layer, hp) ? ws + WS_IMG + ((layer & 1) * 768 + (hp >> 3) * 384) * 1024 : (const char*)W; }
__device__ __forceinline__ const char* wo_src(const char* ws, const void* W, int layer, int hp) { return img(layer, hp) ? ws + WS_IMG + (1536 + (layer & 1) * 256 + (hp >> 3) * 128) * 1024 : (const char*)W; }

template <typename T> __device__ __forceinline__ T* uni(T* p) {
  const unsigned long long v = (unsigned long long)p;
  const unsigned lo = __builtin_amdgcn_readfirstlane((unsigned)v), hi = __builtin_amdgcn_readfirstlane((unsigned)(v >> 32));
  return (T*)(((unsigned long long)hi << 32) | lo);
}
__device__ __forceinline__ int lane_id() {
  int l;
  asm volatile("v_mbcnt_lo_u32_b32 %0, -1, 0\n\tv_mbcnt_hi_u32_b32 %0, -1, %0" : "=v"(l));
  __builtin_assume(l >= 0 && l < 64);
  return l;
}
#define IDS \
  const int lane = lane_id(); \
  asm volatile("" : "+s"(w)); \
  const int tid = w * 64 + lane; \
  int bid = blockIdx.x; asm volatile("" : "+s"(bid)); \
  ws = uni(ws); asm volatile("" : "+s"(ws)); \
  layer = __builtin_amdgcn_readfirstlane(layer); x0 = uni(x0); wo = uni(wo); \
    \
  const int rt = 2 * (bid & 4) + ((bid >> 3) & 7); \
  const int hp = 4 * (bid & 3) + (bid >> 6); \
  const int m0 = rt * 128; \
  const int fr = lane & 15, g = lane >> 4; \
  const int q4 = (lane & 15) >> 2, p4 = lane & 3; \
  const __amdgpu_buffer_rsrc_t rsW = __builtin_amdgcn_make_buffer_rsrc((void*)ws, (short)0, (int)WS_END, 0x00020000); \
  (void)m0; (void)fr; (void)q4; (void)p4; (void)rsW; (void)g;

__device__ __forceinline__ void code_touch(unsigned& junk, int bid, int lane) {
  unsigned long long pc;
  unsigned len, d;
  asm volatile("s_getpc_b64 %0\n.Lct%=:\n\ts_mov_b32 %2, .Lct%=-.Lmk_pc\n\ts_mov_b32 %1, .Lmk_end-.Lmk_pc" : "=s"(pc), "=s"(len), "=s"(d));
  pc -= d;
  const unsigned per = ((len >> 10) + 1) * 128, off = ((bid >> 3) & 7) * per + (lane * 128 < per ? lane * 128 : 0);
  asm volatile("global_load_dword %0, %1, %2" : "+v"(junk) : "v"(off < len - 4 ? off : 0u), "s"(pc) : "memory");
}

__device__ __forceinline__ void publish(__amdgpu_buffer_rsrc_t rsW, int tid, unsigned idx, u64 v) {
  unsigned off = WS_FLAGS + idx * FLS;
  asm volatile("" : "+v"(off), "+v"(v));
  WAITV(0);
  WAITL0; BAR;
  if (tid == 192) __builtin_amdgcn_raw_buffer_store_b64(__builtin_bit_cast(u32x2, v), rsW, off, 0, 16);
  asm volatile("" ::: "memory");
}
__device__ __forceinline__ void poll_flags(char* ws, u64 E, int set, int rt, int hp, int lane, unsigned n, bool col) {
  const int l = lane & 15;
  const unsigned fo = WS_FLAGS + (unsigned)(set * 256 + (col ? l * 16 + hp : rt * 16 + l)) * FLS;
  _Pragma("unroll 1") for (unsigned spins = 0; spins < (1u << 18); ++spins) {
    u64 d;
    asm volatile("global_load_dwordx2 %0, %1, %2 sc1\n\ts_waitcnt vmcnt(0)" : "=v"(d) : "v"(fo), "s"(ws) : "memory");
    if (__all(d - (E + n) <= (u64)(16u - n))) break;
    __builtin_amdgcn_s_sleep(1);
  }
}

__device__ __forceinline__ u64 poll_eq(const char* ws, unsigned off, u64 want, u64 mask, u64 now) {
  u64 miss;
  _Pragma("unroll 1") for (unsigned spins = 0; spins < (1u << 18); ++spins) {
    u64 d;
    asm volatile("global_load_dwordx2 %0, %1, %2 sc1\n\ts_waitcnt vmcnt(0)" : "=v"(d) : "v"(off), "s"(ws) : "memory");
    miss = __ballot(d != want) & mask;
    if (!(miss & now)) break;
    __builtin_amdgcn_s_sleep(1);
  }
  return miss;
}

#define G1_MACROS \
  ".macro G1M rb, j, s, z\n" \
  " .set g1d, 160+24*(\\rb)+4*(((\\j)+2)-6*((\\j)>>2))\n .set g1a, 80+40*(\\s)+4*(\\rb)\n .set g1b, 96+40*(\\s)+4*(\\j)\n" \
  " .if ((\\j)>>1) == 1\n  .set g1t, g1a\n  .set g1a, g1b\n  .set g1b, g1t\n .endif\n" \
  " .if \\z\n  v_mfma_f32_16x16x32_bf16 v[g1d:g1d+3], v[g1b:g1b+3], v[g1a:g1a+3], 0\n" \
  " .else\n  v_mfma_f32_16x16x32_bf16 v[g1d:g1d+3], v[g1b:g1b+3], v[g1a:g1a+3], v[g1d:g1d+3]\n .endif\n" \
  ".endm\n" \
  ".macro G1RA rb, s, ra, oa\n" \
  " ds_read_b128 v[80+40*(\\s)+4*(\\rb):83+40*(\\s)+4*(\\rb)], \\ra offset:(\\oa)+(\\rb)*2048\n" \
  ".endm\n" \
  ".macro G1RB k, s, r0, r1, ob\n" \
  " .if (\\k) & 2\n  ds_read_b64_tr_b16 v[96+40*(\\s)+2*(\\k):97+40*(\\s)+2*(\\k)], \\r1 offset:(\\ob)+(((\\k)>>2)+1-3*((\\k)>>3))*2048+((\\k)&1)*512\n" \
  " .else\n  ds_read_b64_tr_b16 v[96+40*(\\s)+2*(\\k):97+40*(\\s)+2*(\\k)], \\r0 offset:(\\ob)+(((\\k)>>2)+1-3*((\\k)>>3))*2048+((\\k)&1)*512\n .endif\n" \
  ".endm\n" \
    \
  ".macro G1BK i, g, r0, r1, ob\n G1RB 4*(\\g)+((\\i)&3), (\\i)>>2, \\r0, \\r1, (\\ob)+((\\i)>>2)*12288\n.endm\n" \
  ".macro G1AK rb, s, a0, a1, oa\n .if \\s\n  G1RA \\rb, 1, \\a1, \\oa\n .else\n  G1RA \\rb, 0, \\a0, \\oa\n .endif\n.endm\n" \
  ".macro G1RDB g, r0, r1, ob\n .set g1i, 0\n .rept 8\n  G1BK g1i, \\g, \\r0, \\r1, \\ob\n  .set g1i, g1i+1\n .endr\n.endm\n" \
  ".macro G1RDA a0, a1, oa\n .set g1i, 0\n .rept 8\n  G1AK (g1i>>1)^2, g1i&1, \\a0, \\a1, \\oa\n  .set g1i, g1i+1\n .endr\n.endm\n" \
    \
  ".macro G1Q g, rb, q, i, z\n G1M \\rb, 2*(\\g)+((((\\i)>>1)^(\\q))&1), (((\\i)+1)>>1)&1, (\\z)&&(((\\i)&1)==0)\n.endm\n" \
    \
  ".macro G1SG g, z, rd, a0, a1, r0, r1, o\n" \
  " .set g1i, 0\n .rept 16\n" \
  "  .if ((\\g) == 0) && (g1i == 8) && (\\rd)\n   s_waitcnt lgkmcnt(7)\n   s_nop 0\n  .endif\n" \
  "  .if ((\\g) == 0) && (g1i == 15)\n   s_waitcnt lgkmcnt(0)\n  .endif\n" \
  "  G1Q \\g, (g1i>>2)^(2-((\\g)&1)), g1i>>2, g1i&3, \\z\n" \
  "  .if \\rd\n" \
  "   .if (\\g) == 0\n    .if g1i < 7\n     G1BK g1i, 2, \\r0, \\r1, \\o\n    .elseif g1i == 8\n     G1BK 7, 2, \\r0, \\r1, \\o\n    .endif\n" \
  "   .elseif (\\g) == 1\n    .if g1i < 8\n     G1BK g1i, 0, \\r0, \\r1, \\o\n    .endif\n" \
  "   .elseif (g1i & 2) == 0\n    G1BK (g1i&1)+2*(g1i>>2), 1, \\r0, \\r1, \\o\n" \
  "   .elseif (\\rd) == 1\n    G1AK (g1i>>2)^2, 1-(g1i&1), \\a0, \\a1, \\o\n   .endif\n" \
  "  .endif\n" \
  "  .set g1i, g1i+1\n .endr\n" \
  ".endm\n" \
  ""
#define G1_SLIDE \
  "G1Q 1, 3, 0, 0, 0\n ds_read2_b64 v[60:63], v49 offset0:32 offset1:48\n ds_read_b128 v[96:99], v48 offset:256\n G1Q 1, 3, 0, 1, 0\n " \
  "ds_read_b128 v[100:103], v48 offset:320\n ds_read2_b32 v[56:57], v52 offset1:16\n G1Q 1, 3, 0, 2, 0\n ds_read_b128 v[136:139], v54 offset:192\n " \
  "ds_read_b128 v[140:143], v54 offset:704\n G1Q 1, 3, 0, 3, 0\n G1Q 1, 2, 1, 0, 0\n G1Q 1, 2, 1, 1, 0\n s_waitcnt lgkmcnt(3)\n " \
  "v_fmac_f32 v240, v96, v62\n G1Q 1, 2, 1, 2, 0\n v_fma_f32 v241, v97, v62, v241\n v_fma_f32 v242, v98, v62, v242\n G1Q 1, 2, 1, 3, 0\n " \
  "v_fma_f32 v243, v99, v62, v243\n v_fma_f32 v244, v100, v62, v244\n G1Q 1, 1, 2, 0, 0\n v_fma_f32 v245, v101, v62, v245\n " \
  "v_fma_f32 v246, v102, v62, v246\n G1Q 1, 1, 2, 1, 0\n v_fma_f32 v247, v103, v62, v247\n v_mul_f32_e64 v240, v63, v240\n G1Q 1, 1, 2, 2, 0\n " \
  "v_mul_f32_e64 v241, v63, v241\n v_mul_f32_e64 v242, v63, v242\n G1Q 1, 1, 2, 3, 0\n v_mul_f32_e64 v243, v63, v243\n v_mul_f32_e64 v244, v63, v244\n " \
  "G1Q 1, 0, 3, 0, 0\n v_mul_f32_e64 v245, v63, v245\n v_mul_f32_e64 v246, v63, v246\n G1Q 1, 0, 3, 1, 0\n v_mul_f32_e64 v247, v63, v247\n " \
  "v_cvt_pk_bf16_f32 v240, v240, v241\n G1Q 1, 0, 3, 2, 0\n v_cvt_pk_bf16_f32 v241, v242, v243\n v_cvt_pk_bf16_f32 v242, v244, v245\n " \
  "G1Q 1, 0, 3, 3, 0\n v_cvt_pk_bf16_f32 v243, v246, v247\n s_waitcnt lgkmcnt(0)\n v_fmac_f32 v248, v136, v56\n G1Q 2, 2, 0, 0, 0\n " \
  "ds_read2_b64 v[144:147], v49 offset1:16\n v_fma_f32 v249, v137, v56, v249\n G1Q 2, 2, 0, 1, 0\n ds_read_b128 v[148:151], v48\n " \
  "v_fma_f32 v250, v138, v56, v250\n G1Q 2, 2, 0, 2, 0\n ds_read_b128 v[104:107], v48 offset:64\n v_fma_f32 v251, v139, v56, v251\n G1Q 2, 2, 0, 3, 0\n " \
  "ds_read_b128 v[108:111], v54 offset:128\n v_fma_f32 v252, v136, v57, v252\n G1Q 2, 3, 1, 0, 0\n v_fma_f32 v253, v137, v57, v253\n " \
  "v_fma_f32 v254, v138, v57, v254\n G1Q 2, 3, 1, 1, 0\n ds_read_b128 v[88:91], v54 offset:640\n v_fma_f32 v255, v139, v57, v255\n G1Q 2, 3, 1, 2, 0\n " \
  "ds_read_b128 v[128:131], v54\n v_mul_f32_e64 v248, v140, v248\n G1Q 2, 3, 1, 3, 0\n v_mul_f32_e64 v249, v141, v249\n v_mul_f32_e64 v250, v142, v250\n " \
  "G1Q 2, 0, 2, 0, 0\n v_mul_f32_e64 v251, v143, v251\n v_mul_f32_e64 v252, v140, v252\n G1Q 2, 0, 2, 1, 0\n ds_read_b128 v[92:95], v54 offset:512\n " \
  "v_mul_f32_e64 v253, v141, v253\n G1Q 2, 0, 2, 2, 0\n v_mul_f32_e64 v254, v142, v254\n v_mul_f32_e64 v255, v143, v255\n G1Q 2, 0, 2, 3, 0\n " \
  "v_cvt_pk_bf16_f32 v248, v248, v249\n v_cvt_pk_bf16_f32 v249, v250, v251\n G1Q 2, 1, 3, 0, 0\n v_cvt_pk_bf16_f32 v255, v254, v255\n " \
  "v_cvt_pk_bf16_f32 v254, v252, v253\n G1Q 2, 1, 3, 1, 0\n v_min_u32_e64 v136, -2, v240\n v_min_u32_e64 v137, -2, v241\n v_min_u32_e64 v138, -2, v242\n " \
  "G1Q 2, 1, 3, 2, 0\n v_min_u32_e64 v139, -2, v243\n v_min_u32_e64 v140, -2, v248\n v_min_u32_e64 v141, -2, v249\n G1Q 2, 1, 3, 3, 0\n " \
  "v_min_u32_e64 v142, -2, v254\n v_min_u32_e64 v143, -2, v255\n ds_read_b128 v[112:115], v54 offset:64\n ds_read_b128 v[116:119], v54 offset:576\n " \
  "s_cmp_eq_u32 %[wm], 0\n s_cbranch_scc1 .Lg1d%=\n buffer_store_dwordx4 v[136:139], v55, %[rs], 0 offen sc1\n " \
  "buffer_store_dwordx4 v[140:143], v55, %[rs], 0 offen offset:1024 sc1\n ds_read_b32 v53, v59\n ds_read_b32 v53, v59\n ds_read_b32 v53, v59\n " \
  "s_branch .Lg1e%=\n .Lg1d%=:\n v_mov_b32 v53, 1\n ds_write_b128 v58, v[136:139]\n ds_write_b128 v58, v[140:143] offset:1024\n ds_write_b32 v59, v53\n " \
  ".Lg1e%=:\n s_waitcnt lgkmcnt(8)\n v_pk_fma_f32 v[232:233], v[148:149], v[62:63], v[232:233] op_sel_hi:[1,0,1]\n " \
  "v_pk_fma_f32 v[234:235], v[150:151], v[62:63], v[234:235] op_sel_hi:[1,0,1]\n " \
  "v_pk_fma_f32 v[236:237], v[104:105], v[62:63], v[236:237] op_sel_hi:[1,0,1]\n " \
  "v_pk_fma_f32 v[238:239], v[106:107], v[62:63], v[238:239] op_sel_hi:[1,0,1]\n " \
  "v_pk_fma_f32 v[216:217], v[96:97], v[60:61], v[216:217] op_sel_hi:[1,0,1]\n " \
  "v_pk_fma_f32 v[218:219], v[98:99], v[60:61], v[218:219] op_sel_hi:[1,0,1]\n " \
  "v_pk_fma_f32 v[220:221], v[100:101], v[60:61], v[220:221] op_sel_hi:[1,0,1]\n " \
  "v_pk_fma_f32 v[222:223], v[102:103], v[60:61], v[222:223] op_sel_hi:[1,0,1]\n v_pk_mul_f32 v[216:217], v[60:61], v[216:217] op_sel:[1,0]\n " \
  "v_pk_mul_f32 v[218:219], v[60:61], v[218:219] op_sel:[1,0]\n v_pk_mul_f32 v[220:221], v[60:61], v[220:221] op_sel:[1,0]\n " \
  "v_pk_mul_f32 v[222:223], v[60:61], v[222:223] op_sel:[1,0]\n v_cvt_pk_bf16_f32 v232, v232, v233\n v_cvt_pk_bf16_f32 v233, v234, v235\n " \
  "v_cvt_pk_bf16_f32 v234, v236, v237\n v_cvt_pk_bf16_f32 v235, v238, v239\n v_cvt_pk_bf16_f32 v216, v216, v217\n v_cvt_pk_bf16_f32 v217, v218, v219\n " \
  "v_cvt_pk_bf16_f32 v218, v220, v221\n v_cvt_pk_bf16_f32 v219, v222, v223\n " \
  "v_pk_fma_f32 v[208:209], v[148:149], v[60:61], v[208:209] op_sel_hi:[1,0,1]\n " \
  "v_pk_fma_f32 v[210:211], v[150:151], v[60:61], v[210:211] op_sel_hi:[1,0,1]\n " \
  "v_pk_fma_f32 v[212:213], v[104:105], v[60:61], v[212:213] op_sel_hi:[1,0,1]\n " \
  "v_pk_fma_f32 v[214:215], v[106:107], v[60:61], v[214:215] op_sel_hi:[1,0,1]\n " \
  "v_pk_fma_f32 v[192:193], v[96:97], v[146:147], v[192:193] op_sel_hi:[1,0,1]\n " \
  "v_pk_fma_f32 v[194:195], v[98:99], v[146:147], v[194:195] op_sel_hi:[1,0,1]\n " \
  "v_pk_fma_f32 v[196:197], v[100:101], v[146:147], v[196:197] op_sel_hi:[1,0,1]\n " \
  "v_pk_fma_f32 v[198:199], v[102:103], v[146:147], v[198:199] op_sel_hi:[1,0,1]\n v_pk_mul_f32 v[192:193], v[146:147], v[192:193] op_sel:[1,0]\n " \
  "v_pk_mul_f32 v[194:195], v[146:147], v[194:195] op_sel:[1,0]\n v_pk_mul_f32 v[196:197], v[146:147], v[196:197] op_sel:[1,0]\n " \
  "v_pk_mul_f32 v[198:199], v[146:147], v[198:199] op_sel:[1,0]\n v_mfma_f32_16x16x32_bf16 v[120:123], v[240:243], v[232:235], v[40:43]\n " \
  "v_mfma_f32_16x16x32_bf16 v[236:239], v[216:219], v[232:235], v[36:39]\n v_cvt_pk_bf16_f32 v208, v208, v209\n v_cvt_pk_bf16_f32 v209, v210, v211\n " \
  "v_cvt_pk_bf16_f32 v210, v212, v213\n v_cvt_pk_bf16_f32 v211, v214, v215\n v_cvt_pk_bf16_f32 v192, v192, v193\n v_cvt_pk_bf16_f32 v193, v194, v195\n " \
  "v_cvt_pk_bf16_f32 v194, v196, v197\n v_cvt_pk_bf16_f32 v195, v198, v199\n " \
  "v_pk_fma_f32 v[184:185], v[148:149], v[146:147], v[184:185] op_sel_hi:[1,0,1]\n " \
  "v_pk_fma_f32 v[186:187], v[150:151], v[146:147], v[186:187] op_sel_hi:[1,0,1]\n " \
  "v_pk_fma_f32 v[188:189], v[104:105], v[146:147], v[188:189] op_sel_hi:[1,0,1]\n " \
  "v_pk_fma_f32 v[190:191], v[106:107], v[146:147], v[190:191] op_sel_hi:[1,0,1]\n " \
  "v_pk_fma_f32 v[168:169], v[96:97], v[144:145], v[168:169] op_sel_hi:[1,0,1]\n " \
  "v_pk_fma_f32 v[170:171], v[98:99], v[144:145], v[170:171] op_sel_hi:[1,0,1]\n " \
  "v_pk_fma_f32 v[172:173], v[100:101], v[144:145], v[172:173] op_sel_hi:[1,0,1]\n " \
  "v_pk_fma_f32 v[174:175], v[102:103], v[144:145], v[174:175] op_sel_hi:[1,0,1]\n v_pk_mul_f32 v[168:169], v[144:145], v[168:169] op_sel:[1,0]\n " \
  "v_pk_mul_f32 v[170:171], v[144:145], v[170:171] op_sel:[1,0]\n v_pk_mul_f32 v[172:173], v[144:145], v[172:173] op_sel:[1,0]\n " \
  "v_pk_mul_f32 v[174:175], v[144:145], v[174:175] op_sel:[1,0]\n v_mfma_f32_16x16x32_bf16 v[220:223], v[216:219], v[208:211], v[40:43]\n " \
  "v_mfma_f32_16x16x32_bf16 v[212:215], v[192:195], v[208:211], v[36:39]\n v_cvt_pk_bf16_f32 v184, v184, v185\n v_cvt_pk_bf16_f32 v185, v186, v187\n " \
  "v_cvt_pk_bf16_f32 v186, v188, v189\n v_cvt_pk_bf16_f32 v187, v190, v191\n v_cvt_pk_bf16_f32 v168, v168, v169\n v_cvt_pk_bf16_f32 v169, v170, v171\n " \
  "v_cvt_pk_bf16_f32 v170, v172, v173\n v_cvt_pk_bf16_f32 v171, v174, v175\n " \
  "v_pk_fma_f32 v[160:161], v[148:149], v[144:145], v[160:161] op_sel_hi:[1,0,1]\n " \
  "v_pk_fma_f32 v[162:163], v[150:151], v[144:145], v[162:163] op_sel_hi:[1,0,1]\n " \
  "v_pk_fma_f32 v[164:165], v[104:105], v[144:145], v[164:165] op_sel_hi:[1,0,1]\n " \
  "v_pk_fma_f32 v[166:167], v[106:107], v[144:145], v[166:167] op_sel_hi:[1,0,1]\n s_waitcnt lgkmcnt(3)\n " \
  "v_pk_fma_f32 v[176:177], v[128:129], v[56:57], v[176:177] op_sel_hi:[1,0,1]\n " \
  "v_pk_fma_f32 v[178:179], v[130:131], v[56:57], v[178:179] op_sel_hi:[1,0,1]\n " \
  "v_pk_fma_f32 v[180:181], v[128:129], v[56:57], v[180:181] op_sel:[0,1,0]\n v_pk_fma_f32 v[182:183], v[130:131], v[56:57], v[182:183] op_sel:[0,1,0]\n " \
  "v_pk_mul_f32 v[176:177], v[176:177], v[92:93]\n v_pk_mul_f32 v[178:179], v[178:179], v[94:95]\n v_pk_mul_f32 v[180:181], v[180:181], v[92:93]\n " \
  "v_pk_mul_f32 v[182:183], v[182:183], v[94:95]\n v_mfma_f32_16x16x32_bf16 v[196:199], v[192:195], v[184:187], v[40:43]\n " \
  "v_mfma_f32_16x16x32_bf16 v[188:191], v[168:171], v[184:187], v[36:39]\n v_cvt_pk_bf16_f32 v160, v160, v161\n v_cvt_pk_bf16_f32 v161, v162, v163\n " \
  "v_cvt_pk_bf16_f32 v162, v164, v165\n v_cvt_pk_bf16_f32 v163, v166, v167\n v_cvt_pk_bf16_f32 v174, v176, v177\n v_cvt_pk_bf16_f32 v175, v178, v179\n " \
  "v_cvt_pk_bf16_f32 v180, v180, v181\n v_cvt_pk_bf16_f32 v181, v182, v183\n " \
  "v_pk_fma_f32 v[224:225], v[108:109], v[56:57], v[224:225] op_sel_hi:[1,0,1]\n " \
  "v_pk_fma_f32 v[226:227], v[110:111], v[56:57], v[226:227] op_sel_hi:[1,0,1]\n " \
  "v_pk_fma_f32 v[228:229], v[108:109], v[56:57], v[228:229] op_sel:[0,1,0]\n v_pk_fma_f32 v[230:231], v[110:111], v[56:57], v[230:231] op_sel:[0,1,0]\n " \
  "v_pk_mul_f32 v[224:225], v[224:225], v[88:89]\n v_pk_mul_f32 v[226:227], v[226:227], v[90:91]\n v_pk_mul_f32 v[228:229], v[228:229], v[88:89]\n " \
  "v_pk_mul_f32 v[230:231], v[230:231], v[90:91]\n v_mfma_f32_16x16x32_bf16 v[124:127], v[168:171], v[160:163], v[40:43]\n " \
  "v_cvt_pk_bf16_f32 v246, v224, v225\n v_cvt_pk_bf16_f32 v247, v226, v227\n v_cvt_pk_bf16_f32 v252, v228, v229\n v_cvt_pk_bf16_f32 v253, v230, v231\n " \
  "v_max3_f32 v140, v236, v237, v238\n v_max3_f32 v138, v212, v213, v214\n v_max3_f32 v141, v239, v120, v121\n v_max3_f32 v139, v215, v220, v221\n " \
  "v_max3_f32 v140, v140, v122, v123\n v_max3_f32 v138, v138, v222, v223\n v_max_f32 v140, v140, v141\n v_max_f32 v138, v138, v139\n " \
  "v_max3_f32 v136, v188, v189, v190\n v_max3_f32 v137, v191, v196, v197\n v_max3_f32 v136, v136, v198, v199\n " \
  "v_pk_fma_f32 v[200:201], v[112:113], v[56:57], v[200:201] op_sel_hi:[1,0,1]\n " \
  "v_pk_fma_f32 v[202:203], v[114:115], v[56:57], v[202:203] op_sel_hi:[1,0,1]\n v_max_f32 v136, v136, v137\n v_mov_b32 v142, v140\n " \
  "v_pk_fma_f32 v[204:205], v[112:113], v[56:57], v[204:205] op_sel:[0,1,0]\n v_pk_fma_f32 v[206:207], v[114:115], v[56:57], v[206:207] op_sel:[0,1,0]\n " \
  "v_permlane16_swap_b32 v136, v138\n v_permlane16_swap_b32 v140, v142\n v_pk_mul_f32 v[200:201], v[200:201], v[116:117]\n " \
  "v_pk_mul_f32 v[202:203], v[202:203], v[118:119]\n v_max_f32 v136, v136, v138\n v_max_f32 v140, v140, v142\n " \
  "v_pk_mul_f32 v[204:205], v[204:205], v[116:117]\n v_pk_mul_f32 v[206:207], v[206:207], v[118:119]\n v_permlane32_swap_b32 v136, v140\n " \
  "v_mul_f32 v152, 0x3e8293ee, v63\n v_mul_f32 v154, 0x3e8293ee, v61\n v_max_f32 v136, v136, v140\n v_mov_b32 v140, v136\n " \
  "v_mul_f32 v156, 0x3e8293ee, v147\n v_mul_f32 v158, 0x3e8293ee, v145\n v_permlane32_swap_b32 v136, v140\n v_mov_b32 v132, 0x3f803f80\n " \
  "v_mov_b32 v133, 0x3f803f80\n v_mov_b32 v138, v136\n v_mov_b32 v134, 0x3f803f80\n v_mov_b32 v135, 0x3f803f80\n v_permlane16_swap_b32 v136, v138\n " \
  "v_cvt_pk_bf16_f32 v244, v200, v201\n v_cvt_pk_bf16_f32 v245, v202, v203\n v_cvt_pk_bf16_f32 v250, v204, v205\n v_cvt_pk_bf16_f32 v251, v206, v207\n " \
  "v_cvt_pk_bf16_f32 v176, v200, v201\n v_cvt_pk_bf16_f32 v177, v202, v203\n v_cvt_pk_bf16_f32 v182, v204, v205\n v_cvt_pk_bf16_f32 v183, v206, v207\n " \
  "v_mul_f32_e64 v153, v152, -v140\n v_mul_f32_e64 v155, v154, -v138\n v_mul_f32_e64 v157, v156, -v136\n " \
  "v_pk_fma_f32 v[236:237], v[236:237], v[152:153], v[152:153] op_sel:[0,0,1] op_sel_hi:[1,0,1]\n " \
  "v_pk_fma_f32 v[238:239], v[238:239], v[152:153], v[152:153] op_sel:[0,0,1] op_sel_hi:[1,0,1]\n " \
  "v_pk_fma_f32 v[120:121], v[120:121], v[152:153], v[152:153] op_sel:[0,0,1] op_sel_hi:[1,0,1]\n " \
  "v_pk_fma_f32 v[122:123], v[122:123], v[152:153], v[152:153] op_sel:[0,0,1] op_sel_hi:[1,0,1]\n v_exp_f32 v236, v236\n v_exp_f32 v237, v237\n " \
  "v_exp_f32 v238, v238\n v_exp_f32 v239, v239\n v_exp_f32 v120, v120\n v_exp_f32 v121, v121\n v_exp_f32 v122, v122\n v_exp_f32 v123, v123\n " \
  "v_pk_fma_f32 v[212:213], v[212:213], v[154:155], v[154:155] op_sel:[0,0,1] op_sel_hi:[1,0,1]\n " \
  "v_pk_fma_f32 v[214:215], v[214:215], v[154:155], v[154:155] op_sel:[0,0,1] op_sel_hi:[1,0,1]\n " \
  "v_pk_fma_f32 v[220:221], v[220:221], v[154:155], v[154:155] op_sel:[0,0,1] op_sel_hi:[1,0,1]\n " \
  "v_pk_fma_f32 v[222:223], v[222:223], v[154:155], v[154:155] op_sel:[0,0,1] op_sel_hi:[1,0,1]\n v_cvt_pk_bf16_f32 v236, v236, v237\n " \
  "v_cvt_pk_bf16_f32 v237, v238, v239\n v_cvt_pk_bf16_f32 v238, v120, v121\n v_cvt_pk_bf16_f32 v239, v122, v123\n v_exp_f32 v212, v212\n " \
  "v_exp_f32 v213, v213\n v_mfma_f32_16x16x32_bf16 v[120:123], v[132:135], v[236:239], 0\n " \
  "v_mfma_f32_16x16x32_bf16 v[232:235], v[246:249], v[236:239], 0\n v_mfma_f32_16x16x32_bf16 v[240:243], v[252:255], v[236:239], 0\n " \
  "v_exp_f32 v214, v214\n v_exp_f32 v215, v215\n v_exp_f32 v220, v220\n v_exp_f32 v221, v221\n v_exp_f32 v222, v222\n v_exp_f32 v223, v223\n " \
  "v_pk_fma_f32 v[188:189], v[188:189], v[156:157], v[156:157] op_sel:[0,0,1] op_sel_hi:[1,0,1]\n " \
  "v_pk_fma_f32 v[190:191], v[190:191], v[156:157], v[156:157] op_sel:[0,0,1] op_sel_hi:[1,0,1]\n " \
  "v_pk_fma_f32 v[196:197], v[196:197], v[156:157], v[156:157] op_sel:[0,0,1] op_sel_hi:[1,0,1]\n " \
  "v_pk_fma_f32 v[198:199], v[198:199], v[156:157], v[156:157] op_sel:[0,0,1] op_sel_hi:[1,0,1]\n v_cvt_pk_bf16_f32 v212, v212, v213\n " \
  "v_cvt_pk_bf16_f32 v213, v214, v215\n v_cvt_pk_bf16_f32 v214, v220, v221\n v_cvt_pk_bf16_f32 v215, v222, v223\n v_exp_f32 v188, v188\n " \
  "s_cmp_eq_u32 %[wm], 0\n s_cbranch_scc1 .Lg1f%=\n ds_read_b32 v53, v59\n ds_read_b128 v[80:83], v58\n ds_read_b64 v[172:173], v58 offset:1024\n " \
  "ds_read_b64 v[178:179], v58 offset:1032\n s_branch .Lg1g%=\n .Lg1f%=:\n buffer_load_dwordx4 v[80:83], v55, %[rs], 0 offen sc1\n " \
  "buffer_load_dwordx2 v[172:173], v55, %[rs], 0 offen offset:1024 sc1\n buffer_load_dwordx2 v[178:179], v55, %[rs], 0 offen offset:1032 sc1\n " \
  ".Lg1g%=:\n v_exp_f32 v189, v189\n v_mfma_f32_16x16x32_bf16 v[220:223], v[132:135], v[212:215], 0\n " \
  "v_mfma_f32_16x16x32_bf16 v[224:227], v[244:247], v[212:215], 0\n v_mfma_f32_16x16x32_bf16 v[228:231], v[250:253], v[212:215], 0\n " \
  "v_exp_f32 v190, v190\n v_exp_f32 v191, v191\n v_exp_f32 v196, v196\n v_exp_f32 v197, v197\n v_exp_f32 v198, v198\n v_exp_f32 v199, v199\n " \
  "v_rcp_f32 v120, v120\n v_cvt_pk_bf16_f32 v188, v188, v189\n v_pk_mul_f32 v[232:233], v[232:233], v[120:121] op_sel_hi:[1,0]\n " \
  "v_pk_mul_f32 v[234:235], v[234:235], v[120:121] op_sel_hi:[1,0]\n v_pk_mul_f32 v[240:241], v[240:241], v[120:121] op_sel_hi:[1,0]\n " \
  "v_pk_mul_f32 v[242:243], v[242:243], v[120:121] op_sel_hi:[1,0]\n v_cvt_pk_bf16_f32 v189, v190, v191\n v_cvt_pk_bf16_f32 v190, v196, v197\n " \
  "v_cvt_pk_bf16_f32 v191, v198, v199\n v_rcp_f32 v220, v220\n v_cvt_pk_bf16_f32 v232, v232, v233\n v_cvt_pk_bf16_f32 v233, v234, v235\n " \
  "v_mfma_f32_16x16x32_bf16 v[196:199], v[132:135], v[188:191], 0\n v_mfma_f32_16x16x32_bf16 v[200:203], v[174:177], v[188:191], 0\n " \
  "v_mfma_f32_16x16x32_bf16 v[204:207], v[180:183], v[188:191], 0\n v_cvt_pk_bf16_f32 v234, v240, v241\n v_cvt_pk_bf16_f32 v235, v242, v243\n " \
  "buffer_store_dwordx4 v[232:235], v51, %[rs], 0 offen offset:3072 sc1\n ds_write_b128 v50, v[232:235] offset:3072\n " \
  "v_pk_mul_f32 v[224:225], v[224:225], v[220:221] op_sel_hi:[1,0]\n v_pk_mul_f32 v[226:227], v[226:227], v[220:221] op_sel_hi:[1,0]\n " \
  "v_pk_mul_f32 v[228:229], v[228:229], v[220:221] op_sel_hi:[1,0]\n v_pk_mul_f32 v[230:231], v[230:231], v[220:221] op_sel_hi:[1,0]\n " \
  "v_cvt_pk_bf16_f32 v224, v224, v225\n v_cvt_pk_bf16_f32 v225, v226, v227\n v_cvt_pk_bf16_f32 v226, v228, v229\n v_cvt_pk_bf16_f32 v227, v230, v231\n " \
  "v_rcp_f32 v196, v196\n buffer_store_dwordx4 v[224:227], v51, %[rs], 0 offen offset:2048 sc1\n ds_write_b128 v50, v[224:227] offset:2048\n " \
  "v_pk_mul_f32 v[200:201], v[200:201], v[196:197] op_sel_hi:[1,0]\n v_pk_mul_f32 v[202:203], v[202:203], v[196:197] op_sel_hi:[1,0]\n " \
  "v_pk_mul_f32 v[204:205], v[204:205], v[196:197] op_sel_hi:[1,0]\n v_pk_mul_f32 v[206:207], v[206:207], v[196:197] op_sel_hi:[1,0]\n " \
  "v_cvt_pk_bf16_f32 v200, v200, v201\n v_cvt_pk_bf16_f32 v201, v202, v203\n v_cvt_pk_bf16_f32 v202, v204, v205\n v_cvt_pk_bf16_f32 v203, v206, v207\n " \
  "buffer_store_dwordx4 v[200:203], v51, %[rs], 0 offen offset:1024 sc1\n ds_write_b128 v50, v[200:203] offset:1024\n s_mov_b32 vcc_hi, 0x40000\n " \
  "s_cmp_eq_u32 %[wm], 0\n s_cbranch_scc1 .Lg1h%=\n s_waitcnt lgkmcnt(3)\n .Lg1i%=:\n v_readfirstlane_b32 vcc_lo, v53\n s_nop 1\n " \
  "s_cmp_lg_u32 vcc_lo, 0\n s_cbranch_scc1 .Lg1k%=\n ds_read_b32 v53, v59\n ds_read_b128 v[80:83], v58\n ds_read_b64 v[172:173], v58 offset:1024\n " \
  "ds_read_b64 v[178:179], v58 offset:1032\n s_waitcnt lgkmcnt(0)\n s_sub_u32 vcc_hi, vcc_hi, 1\n s_cbranch_scc0 .Lg1i%=\n s_branch .Lg1k%=\n .Lg1h%=:\n " \
  "s_mov_b32 m0, 0x40000\n s_waitcnt vmcnt(3)\n .Lg1j%=:\n v_max3_u32 v136, v80, v81, v82\n v_max3_u32 v137, v83, v172, v173\n " \
  "v_max3_u32 v136, v136, v178, v179\n v_max_u32 v136, v136, v137\n v_cmp_eq_u32 vcc, -1, v136\n s_cbranch_vccz .Lg1k%=\n " \
  "buffer_load_dwordx4 v[80:83], v55, %[rs], 0 offen sc1\n buffer_load_dwordx2 v[172:173], v55, %[rs], 0 offen offset:1024 sc1\n " \
  "buffer_load_dwordx2 v[178:179], v55, %[rs], 0 offen offset:1032 sc1\n s_waitcnt vmcnt(0)\n s_sub_u32 m0, m0, 1\n s_cbranch_scc0 .Lg1j%=\n .Lg1k%=:\n " \
  "v_mfma_f32_16x16x32_bf16 v[164:167], v[80:83], v[160:163], v[44:47]\n s_nop 7\n v_max3_f32 v136, v164, v165, v166\n " \
  "v_max3_f32 v137, v167, v124, v125\n v_max3_f32 v136, v136, v126, v127\n v_max_f32 v136, v136, v137\n v_mov_b32 v137, v136\n s_nop 1\n " \
  "v_permlane16_swap_b32 v136, v137\n v_max_f32 v136, v136, v137\n v_mov_b32 v137, v136\n s_nop 1\n v_permlane32_swap_b32 v136, v137\n " \
  "v_max_f32 v136, v136, v137\n v_mul_f32_e64 v159, v158, -v136\n " \
  "v_pk_fma_f32 v[164:165], v[164:165], v[158:159], v[158:159] op_sel:[0,0,1] op_sel_hi:[1,0,1]\n " \
  "v_pk_fma_f32 v[166:167], v[166:167], v[158:159], v[158:159] op_sel:[0,0,1] op_sel_hi:[1,0,1]\n " \
  "v_pk_fma_f32 v[124:125], v[124:125], v[158:159], v[158:159] op_sel:[0,0,1] op_sel_hi:[1,0,1]\n " \
  "v_pk_fma_f32 v[126:127], v[126:127], v[158:159], v[158:159] op_sel:[0,0,1] op_sel_hi:[1,0,1]\n v_exp_f32 v164, v164\n v_exp_f32 v165, v165\n " \
  "v_exp_f32 v166, v166\n v_exp_f32 v167, v167\n v_exp_f32 v124, v124\n v_exp_f32 v125, v125\n v_exp_f32 v126, v126\n v_exp_f32 v127, v127\n " \
  "v_cvt_pk_bf16_f32 v164, v164, v165\n v_cvt_pk_bf16_f32 v165, v166, v167\n v_cvt_pk_bf16_f32 v166, v124, v125\n v_cvt_pk_bf16_f32 v167, v126, v127\n " \
  "s_nop 1\n v_mfma_f32_16x16x32_bf16 v[124:127], v[132:135], v[164:167], 0\n v_mfma_f32_16x16x32_bf16 v[168:171], v[172:175], v[164:167], 0\n " \
  "v_mfma_f32_16x16x32_bf16 v[84:87], v[178:181], v[164:167], 0\n s_nop 5\n v_rcp_f32 v124, v124\n s_nop 0\n " \
  "v_pk_mul_f32 v[168:169], v[168:169], v[124:125] op_sel_hi:[1,0]\n v_pk_mul_f32 v[170:171], v[170:171], v[124:125] op_sel_hi:[1,0]\n " \
  "v_pk_mul_f32 v[84:85], v[84:85], v[124:125] op_sel_hi:[1,0]\n v_pk_mul_f32 v[86:87], v[86:87], v[124:125] op_sel_hi:[1,0]\n " \
  "v_cvt_pk_bf16_f32 v168, v168, v169\n v_cvt_pk_bf16_f32 v169, v170, v171\n v_cvt_pk_bf16_f32 v170, v84, v85\n v_cvt_pk_bf16_f32 v171, v86, v87\n " \
  "buffer_store_dwordx4 v[168:171], v51, %[rs], 0 offen offset:0 sc1\n ds_write_b128 v50, v[168:171] offset:0\n "
#define G1_HEDGE \
  "G1Q 1, 1, 0, 0, 0\n ds_read2_b64 v[140:143], v49 offset1:16\n ds_read_b128 v[96:99], v48 offset:256\n G1Q 1, 1, 0, 1, 0\n " \
  "ds_read_b128 v[100:103], v48 offset:320\n ds_read2_b64 v[136:139], v49 offset0:32 offset1:48\n G1Q 1, 1, 0, 2, 0\n ds_read_b64_tr_b16 v[36:37], v53\n " \
  "ds_read_b64_tr_b16 v[38:39], v53 offset:1024\n G1Q 1, 1, 0, 3, 0\n ds_read_b64_tr_b16 v[40:41], v53 offset:32\n " \
  "ds_read_b64_tr_b16 v[42:43], v53 offset:1056\n G1Q 1, 0, 1, 0, 0\n G1Q 1, 0, 1, 1, 0\n s_waitcnt lgkmcnt(4)\n v_fmac_f32 v168, v96, v140\n " \
  "G1Q 1, 0, 1, 2, 0\n v_fma_f32 v169, v97, v140, v169\n v_fma_f32 v170, v98, v140, v170\n G1Q 1, 0, 1, 3, 0\n v_fma_f32 v171, v99, v140, v171\n " \
  "v_fma_f32 v172, v100, v140, v172\n G1Q 1, 3, 2, 0, 0\n v_fma_f32 v173, v101, v140, v173\n v_fma_f32 v174, v102, v140, v174\n G1Q 1, 3, 2, 1, 0\n " \
  "v_fma_f32 v175, v103, v140, v175\n v_cvt_pk_bf16_f32 v168, v168, v169\n G1Q 1, 3, 2, 2, 0\n v_cvt_pk_bf16_f32 v169, v170, v171\n " \
  "v_cvt_pk_bf16_f32 v170, v172, v173\n G1Q 1, 3, 2, 3, 0\n v_cvt_pk_bf16_f32 v171, v174, v175\n v_fma_f32 v192, v96, v142, v192\n G1Q 1, 2, 3, 0, 0\n " \
  "v_fma_f32 v193, v97, v142, v193\n v_fma_f32 v194, v98, v142, v194\n G1Q 1, 2, 3, 1, 0\n v_fma_f32 v195, v99, v142, v195\n " \
  "v_fma_f32 v196, v100, v142, v196\n G1Q 1, 2, 3, 2, 0\n v_fma_f32 v197, v101, v142, v197\n v_fma_f32 v198, v102, v142, v198\n G1Q 1, 2, 3, 3, 0\n " \
  "v_fma_f32 v199, v103, v142, v199\n v_cvt_pk_bf16_f32 v192, v192, v193\n G1Q 2, 2, 0, 0, 0\n ds_read2_b32 v[144:145], v52 offset1:16\n " \
  "v_cvt_pk_bf16_f32 v193, v194, v195\n G1Q 2, 2, 0, 1, 0\n ds_read_b128 v[148:151], v48\n v_cvt_pk_bf16_f32 v194, v196, v197\n G1Q 2, 2, 0, 2, 0\n " \
  "v_cvt_pk_bf16_f32 v195, v198, v199\n v_fma_f32 v216, v96, v136, v216\n G1Q 2, 2, 0, 3, 0\n v_fma_f32 v217, v97, v136, v217\n " \
  "v_fma_f32 v218, v98, v136, v218\n G1Q 2, 3, 1, 0, 0\n v_fma_f32 v219, v99, v136, v219\n v_fma_f32 v220, v100, v136, v220\n G1Q 2, 3, 1, 1, 0\n " \
  "ds_read_b128 v[88:91], v48 offset:64\n v_fma_f32 v221, v101, v136, v221\n v_fma_f32 v222, v102, v136, v222\n G1Q 2, 3, 1, 2, 0\n " \
  "ds_read_b128 v[128:131], v54\n v_fma_f32 v223, v103, v136, v223\n v_cvt_pk_bf16_f32 v216, v216, v217\n G1Q 2, 3, 1, 3, 0\n " \
  "v_cvt_pk_bf16_f32 v217, v218, v219\n v_cvt_pk_bf16_f32 v218, v220, v221\n G1Q 2, 0, 2, 0, 0\n v_cvt_pk_bf16_f32 v219, v222, v223\n " \
  "v_fma_f32 v240, v96, v138, v240\n G1Q 2, 0, 2, 1, 0\n ds_read_b128 v[92:95], v54 offset:512\n v_fma_f32 v241, v97, v138, v241\n " \
  "v_fma_f32 v242, v98, v138, v242\n G1Q 2, 0, 2, 2, 0\n ds_read_b128 v[132:135], v54 offset:64\n v_fma_f32 v243, v99, v138, v243\n " \
  "v_fma_f32 v244, v100, v138, v244\n G1Q 2, 0, 2, 3, 0\n v_fma_f32 v245, v101, v138, v245\n v_fma_f32 v246, v102, v138, v246\n G1Q 2, 1, 3, 0, 0\n " \
  "v_fma_f32 v247, v103, v138, v247\n v_cvt_pk_bf16_f32 v240, v240, v241\n G1Q 2, 1, 3, 1, 0\n v_cvt_pk_bf16_f32 v241, v242, v243\n " \
  "v_cvt_pk_bf16_f32 v242, v244, v245\n G1Q 2, 1, 3, 2, 0\n v_cvt_pk_bf16_f32 v243, v246, v247\n G1Q 2, 1, 3, 3, 0\n s_waitcnt lgkmcnt(6)\n " \
  "v_mfma_f32_16x16x32_bf16 v[172:175], v[36:39], v[168:171], 0\n ds_read_b128 v[112:115], v54 offset:576\n ds_read_b128 v[116:119], v54 offset:128\n " \
  "v_mfma_f32_16x16x32_bf16 v[80:83], v[40:43], v[168:171], 0\n ds_read_b128 v[152:155], v54 offset:640\n ds_read_b128 v[156:159], v54 offset:192\n " \
  "v_mfma_f32_16x16x32_bf16 v[196:199], v[36:39], v[192:195], 0\n ds_read_b128 v[120:123], v54 offset:704\n v_mul_f32 v44, 0x3fb8aa3b, v141\n " \
  "v_mfma_f32_16x16x32_bf16 v[84:87], v[40:43], v[192:195], 0\n v_mul_f32 v46, 0x3fb8aa3b, v143\n v_mul_f32 v104, 0x3fb8aa3b, v137\n " \
  "v_mfma_f32_16x16x32_bf16 v[220:223], v[36:39], v[216:219], 0\n v_mul_f32 v106, 0x3fb8aa3b, v139\n " \
  "v_mfma_f32_16x16x32_bf16 v[124:127], v[40:43], v[216:219], 0\n v_mfma_f32_16x16x32_bf16 v[244:247], v[36:39], v[240:243], 0\n " \
  "v_mfma_f32_16x16x32_bf16 v[108:111], v[40:43], v[240:243], 0\n v_max3_f32 v168, v172, v173, v174\n v_max3_f32 v169, -v172, -v173, -v174\n " \
  "v_max3_f32 v168, v168, v175, v80\n v_max3_f32 v169, v169, -v175, -v80\n v_max3_f32 v168, v168, v81, v82\n v_max3_f32 v169, v169, -v81, -v82\n " \
  "v_max_f32_e64 v168, v168, v83\n v_max_f32_e64 v169, v169, -v83\n v_max3_f32 v170, v196, v197, v198\n v_max3_f32 v171, -v196, -v197, -v198\n " \
  "v_max3_f32 v170, v170, v199, v84\n v_max3_f32 v171, v171, -v199, -v84\n v_max3_f32 v170, v170, v85, v86\n v_max3_f32 v171, v171, -v85, -v86\n " \
  "v_max_f32_e64 v170, v170, v87\n v_max_f32_e64 v171, v171, -v87\n s_waitcnt lgkmcnt(0)\n " \
  "v_pk_fma_f32 v[176:177], v[128:129], v[144:145], v[176:177] op_sel_hi:[1,0,1]\n " \
  "v_pk_fma_f32 v[178:179], v[130:131], v[144:145], v[178:179] op_sel_hi:[1,0,1]\n " \
  "v_pk_fma_f32 v[180:181], v[128:129], v[144:145], v[180:181] op_sel:[0,1,0]\n " \
  "v_pk_fma_f32 v[182:183], v[130:131], v[144:145], v[182:183] op_sel:[0,1,0]\n v_pk_mul_f32 v[176:177], v[176:177], v[92:93]\n " \
  "v_pk_mul_f32 v[178:179], v[178:179], v[94:95]\n v_pk_mul_f32 v[180:181], v[180:181], v[92:93]\n v_pk_mul_f32 v[182:183], v[182:183], v[94:95]\n " \
  "v_pk_fma_f32 v[200:201], v[132:133], v[144:145], v[200:201] op_sel_hi:[1,0,1]\n " \
  "v_pk_fma_f32 v[202:203], v[134:135], v[144:145], v[202:203] op_sel_hi:[1,0,1]\n " \
  "v_pk_fma_f32 v[204:205], v[132:133], v[144:145], v[204:205] op_sel:[0,1,0]\n " \
  "v_pk_fma_f32 v[206:207], v[134:135], v[144:145], v[206:207] op_sel:[0,1,0]\n v_pk_mul_f32 v[200:201], v[200:201], v[112:113]\n " \
  "v_pk_mul_f32 v[202:203], v[202:203], v[114:115]\n v_pk_mul_f32 v[204:205], v[204:205], v[112:113]\n v_pk_mul_f32 v[206:207], v[206:207], v[114:115]\n " \
  "v_max3_f32 v192, v220, v221, v222\n v_max3_f32 v193, -v220, -v221, -v222\n v_max3_f32 v192, v192, v223, v124\n v_max3_f32 v193, v193, -v223, -v124\n " \
  "v_max3_f32 v192, v192, v125, v126\n v_max3_f32 v193, v193, -v125, -v126\n v_max_f32_e64 v192, v192, v127\n v_max_f32_e64 v193, v193, -v127\n " \
  "v_max3_f32 v194, v244, v245, v246\n v_max3_f32 v195, -v244, -v245, -v246\n v_max3_f32 v194, v194, v247, v108\n v_max3_f32 v195, v195, -v247, -v108\n " \
  "v_max3_f32 v194, v194, v109, v110\n v_max3_f32 v195, v195, -v109, -v110\n v_max_f32_e64 v194, v194, v111\n v_max_f32_e64 v195, v195, -v111\n " \
  "v_permlane16_swap_b32 v168, v169\n v_permlane16_swap_b32 v170, v171\n v_permlane16_swap_b32 v192, v193\n v_permlane16_swap_b32 v194, v195\n " \
  "v_pk_fma_f32 v[224:225], v[116:117], v[144:145], v[224:225] op_sel_hi:[1,0,1]\n " \
  "v_pk_fma_f32 v[226:227], v[118:119], v[144:145], v[226:227] op_sel_hi:[1,0,1]\n v_max_f32 v168, v168, v169\n v_max_f32 v170, v170, v171\n " \
  "v_max_f32 v192, v192, v193\n v_max_f32 v194, v194, v195\n v_pk_fma_f32 v[228:229], v[116:117], v[144:145], v[228:229] op_sel:[0,1,0]\n " \
  "v_pk_fma_f32 v[230:231], v[118:119], v[144:145], v[230:231] op_sel:[0,1,0]\n v_permlane32_swap_b32 v168, v170\n v_permlane32_swap_b32 v192, v194\n " \
  "v_pk_mul_f32 v[224:225], v[224:225], v[152:153]\n v_pk_mul_f32 v[226:227], v[226:227], v[154:155]\n v_max_f32 v168, v168, v170\n " \
  "v_max_f32 v192, v192, v194\n v_pk_mul_f32 v[228:229], v[228:229], v[152:153]\n v_pk_mul_f32 v[230:231], v[230:231], v[154:155]\n " \
  "v_mov_b32 v170, v168\n v_mov_b32 v194, v192\n v_pk_fma_f32 v[248:249], v[156:157], v[144:145], v[248:249] op_sel_hi:[1,0,1]\n " \
  "v_pk_fma_f32 v[250:251], v[158:159], v[144:145], v[250:251] op_sel_hi:[1,0,1]\n v_permlane32_swap_b32 v168, v170\n v_permlane32_swap_b32 v192, v194\n " \
  "v_pk_fma_f32 v[252:253], v[156:157], v[144:145], v[252:253] op_sel:[0,1,0]\n " \
  "v_pk_fma_f32 v[254:255], v[158:159], v[144:145], v[254:255] op_sel:[0,1,0]\n v_mov_b32 v169, v168\n v_mov_b32 v171, v170\n v_mov_b32 v193, v192\n " \
  "v_mov_b32 v195, v194\n v_pk_mul_f32 v[248:249], v[248:249], v[120:121]\n v_pk_mul_f32 v[250:251], v[250:251], v[122:123]\n " \
  "v_permlane16_swap_b32 v168, v169\n v_permlane16_swap_b32 v170, v171\n v_permlane16_swap_b32 v192, v193\n v_permlane16_swap_b32 v194, v195\n " \
  "v_pk_mul_f32 v[252:253], v[252:253], v[120:121]\n v_pk_mul_f32 v[254:255], v[254:255], v[122:123]\n v_cvt_pk_bf16_f32 v176, v176, v177\n " \
  "v_cvt_pk_bf16_f32 v177, v178, v179\n v_cvt_pk_bf16_f32 v178, v200, v201\n v_cvt_pk_bf16_f32 v179, v202, v203\n v_cvt_pk_bf16_f32 v180, v180, v181\n " \
  "v_cvt_pk_bf16_f32 v181, v182, v183\n v_cvt_pk_bf16_f32 v182, v204, v205\n v_cvt_pk_bf16_f32 v183, v206, v207\n v_cvt_pk_bf16_f32 v224, v224, v225\n " \
  "v_cvt_pk_bf16_f32 v225, v226, v227\n v_cvt_pk_bf16_f32 v226, v248, v249\n v_cvt_pk_bf16_f32 v227, v250, v251\n v_cvt_pk_bf16_f32 v228, v228, v229\n " \
  "v_cvt_pk_bf16_f32 v229, v230, v231\n v_cvt_pk_bf16_f32 v230, v252, v253\n v_cvt_pk_bf16_f32 v231, v254, v255\n v_mul_f32_e64 v45, v44, -v168\n " \
  "v_mul_f32_e64 v146, v44, -v169\n v_mul_f32_e64 v47, v46, -v170\n v_mul_f32_e64 v147, v46, -v171\n v_mul_f32_e64 v105, v104, -v192\n " \
  "v_mul_f32_e64 v144, v104, -v193\n v_mul_f32_e64 v107, v106, -v194\n v_mul_f32_e64 v145, v106, -v195\n " \
  "v_pk_fma_f32 v[200:201], v[172:173], v[44:45], v[146:147] op_sel_hi:[1,0,0] neg_lo:[1,0,0] neg_hi:[1,0,0]\n " \
  "v_pk_fma_f32 v[202:203], v[174:175], v[44:45], v[146:147] op_sel_hi:[1,0,0] neg_lo:[1,0,0] neg_hi:[1,0,0]\n " \
  "v_pk_fma_f32 v[204:205], v[80:81], v[44:45], v[146:147] op_sel_hi:[1,0,0] neg_lo:[1,0,0] neg_hi:[1,0,0]\n " \
  "v_pk_fma_f32 v[206:207], v[82:83], v[44:45], v[146:147] op_sel_hi:[1,0,0] neg_lo:[1,0,0] neg_hi:[1,0,0]\n " \
  "v_pk_fma_f32 v[172:173], v[172:173], v[44:45], v[44:45] op_sel:[0,0,1] op_sel_hi:[1,0,1]\n " \
  "v_pk_fma_f32 v[174:175], v[174:175], v[44:45], v[44:45] op_sel:[0,0,1] op_sel_hi:[1,0,1]\n " \
  "v_pk_fma_f32 v[80:81], v[80:81], v[44:45], v[44:45] op_sel:[0,0,1] op_sel_hi:[1,0,1]\n " \
  "v_pk_fma_f32 v[82:83], v[82:83], v[44:45], v[44:45] op_sel:[0,0,1] op_sel_hi:[1,0,1]\n ds_write_b128 v55, v[176:179] offset:0\n " \
  "v_exp_f32 v200, v200\n v_exp_f32 v201, v201\n v_exp_f32 v202, v202\n v_exp_f32 v203, v203\n v_exp_f32 v204, v204\n v_exp_f32 v205, v205\n " \
  "v_exp_f32 v206, v206\n v_exp_f32 v207, v207\n v_exp_f32 v172, v172\n v_exp_f32 v173, v173\n v_exp_f32 v174, v174\n v_exp_f32 v175, v175\n " \
  "v_exp_f32 v80, v80\n v_exp_f32 v81, v81\n v_exp_f32 v82, v82\n v_exp_f32 v83, v83\n " \
  "v_pk_fma_f32 v[248:249], v[196:197], v[46:47], v[146:147] op_sel:[0,0,1] op_sel_hi:[1,0,1] neg_lo:[1,0,0] neg_hi:[1,0,0]\n " \
  "v_pk_fma_f32 v[250:251], v[198:199], v[46:47], v[146:147] op_sel:[0,0,1] op_sel_hi:[1,0,1] neg_lo:[1,0,0] neg_hi:[1,0,0]\n " \
  "v_pk_fma_f32 v[252:253], v[84:85], v[46:47], v[146:147] op_sel:[0,0,1] op_sel_hi:[1,0,1] neg_lo:[1,0,0] neg_hi:[1,0,0]\n " \
  "v_pk_fma_f32 v[254:255], v[86:87], v[46:47], v[146:147] op_sel:[0,0,1] op_sel_hi:[1,0,1] neg_lo:[1,0,0] neg_hi:[1,0,0]\n " \
  "v_pk_fma_f32 v[196:197], v[196:197], v[46:47], v[46:47] op_sel:[0,0,1] op_sel_hi:[1,0,1]\n " \
  "v_pk_fma_f32 v[198:199], v[198:199], v[46:47], v[46:47] op_sel:[0,0,1] op_sel_hi:[1,0,1]\n " \
  "v_pk_fma_f32 v[84:85], v[84:85], v[46:47], v[46:47] op_sel:[0,0,1] op_sel_hi:[1,0,1]\n " \
  "v_pk_fma_f32 v[86:87], v[86:87], v[46:47], v[46:47] op_sel:[0,0,1] op_sel_hi:[1,0,1]\n ds_write_b128 v55, v[180:183] offset:1024\n " \
  "v_exp_f32 v248, v248\n v_exp_f32 v249, v249\n v_exp_f32 v250, v250\n v_exp_f32 v251, v251\n v_exp_f32 v252, v252\n v_exp_f32 v253, v253\n " \
  "v_exp_f32 v254, v254\n v_exp_f32 v255, v255\n v_exp_f32 v196, v196\n v_exp_f32 v197, v197\n v_exp_f32 v198, v198\n v_exp_f32 v199, v199\n " \
  "v_exp_f32 v84, v84\n v_exp_f32 v85, v85\n v_exp_f32 v86, v86\n v_exp_f32 v87, v87\n " \
  "v_pk_fma_f32 v[128:129], v[220:221], v[104:105], v[144:145] op_sel_hi:[1,0,0] neg_lo:[1,0,0] neg_hi:[1,0,0]\n " \
  "v_pk_fma_f32 v[130:131], v[222:223], v[104:105], v[144:145] op_sel_hi:[1,0,0] neg_lo:[1,0,0] neg_hi:[1,0,0]\n " \
  "v_pk_fma_f32 v[132:133], v[124:125], v[104:105], v[144:145] op_sel_hi:[1,0,0] neg_lo:[1,0,0] neg_hi:[1,0,0]\n " \
  "v_pk_fma_f32 v[134:135], v[126:127], v[104:105], v[144:145] op_sel_hi:[1,0,0] neg_lo:[1,0,0] neg_hi:[1,0,0]\n " \
  "v_pk_fma_f32 v[220:221], v[220:221], v[104:105], v[104:105] op_sel:[0,0,1] op_sel_hi:[1,0,1]\n " \
  "v_pk_fma_f32 v[222:223], v[222:223], v[104:105], v[104:105] op_sel:[0,0,1] op_sel_hi:[1,0,1]\n " \
  "v_pk_fma_f32 v[124:125], v[124:125], v[104:105], v[104:105] op_sel:[0,0,1] op_sel_hi:[1,0,1]\n " \
  "v_pk_fma_f32 v[126:127], v[126:127], v[104:105], v[104:105] op_sel:[0,0,1] op_sel_hi:[1,0,1]\n ds_write_b128 v55, v[224:227] offset:8192\n " \
  "v_pk_add_f32 v[216:217], v[172:173], v[174:175]\n v_pk_add_f32 v[218:219], v[80:81], v[82:83]\n v_pk_add_f32 v[216:217], v[216:217], v[218:219]\n " \
  "v_add_f32 v168, v216, v217\n v_pk_add_f32 v[216:217], v[200:201], v[202:203]\n v_pk_add_f32 v[218:219], v[204:205], v[206:207]\n " \
  "v_pk_add_f32 v[216:217], v[216:217], v[218:219]\n v_add_f32 v169, v216, v217\n v_pk_add_f32 v[216:217], v[196:197], v[198:199]\n " \
  "v_pk_add_f32 v[218:219], v[84:85], v[86:87]\n v_pk_add_f32 v[216:217], v[216:217], v[218:219]\n v_add_f32 v170, v216, v217\n " \
  "v_pk_add_f32 v[216:217], v[248:249], v[250:251]\n v_pk_add_f32 v[218:219], v[252:253], v[254:255]\n v_pk_add_f32 v[216:217], v[216:217], v[218:219]\n " \
  "v_add_f32 v171, v216, v217\n v_exp_f32 v128, v128\n v_exp_f32 v129, v129\n v_permlane16_swap_b32 v168, v169\n v_permlane16_swap_b32 v170, v171\n " \
  "v_exp_f32 v130, v130\n v_exp_f32 v131, v131\n v_add_f32 v168, v168, v169\n v_add_f32 v170, v170, v171\n v_exp_f32 v132, v132\n v_exp_f32 v133, v133\n " \
  "v_permlane32_swap_b32 v168, v170\n v_exp_f32 v134, v134\n v_exp_f32 v135, v135\n v_add_f32 v168, v168, v170\n v_exp_f32 v220, v220\n " \
  "v_exp_f32 v221, v221\n v_rcp_f32 v168, v168\n v_exp_f32 v222, v222\n v_exp_f32 v223, v223\n v_mov_b32 v170, v168\n v_exp_f32 v124, v124\n " \
  "v_exp_f32 v125, v125\n v_permlane32_swap_b32 v168, v170\n v_exp_f32 v126, v126\n v_exp_f32 v127, v127\n v_mov_b32 v169, v168\n v_mov_b32 v171, v170\n " \
  "v_pk_fma_f32 v[112:113], v[244:245], v[106:107], v[144:145] op_sel:[0,0,1] op_sel_hi:[1,0,1] neg_lo:[1,0,0] neg_hi:[1,0,0]\n " \
  "v_pk_fma_f32 v[114:115], v[246:247], v[106:107], v[144:145] op_sel:[0,0,1] op_sel_hi:[1,0,1] neg_lo:[1,0,0] neg_hi:[1,0,0]\n " \
  "v_permlane16_swap_b32 v168, v169\n v_permlane16_swap_b32 v170, v171\n " \
  "v_pk_fma_f32 v[116:117], v[108:109], v[106:107], v[144:145] op_sel:[0,0,1] op_sel_hi:[1,0,1] neg_lo:[1,0,0] neg_hi:[1,0,0]\n " \
  "v_pk_fma_f32 v[118:119], v[110:111], v[106:107], v[144:145] op_sel:[0,0,1] op_sel_hi:[1,0,1] neg_lo:[1,0,0] neg_hi:[1,0,0]\n " \
  "v_pk_fma_f32 v[244:245], v[244:245], v[106:107], v[106:107] op_sel:[0,0,1] op_sel_hi:[1,0,1]\n " \
  "v_pk_fma_f32 v[246:247], v[246:247], v[106:107], v[106:107] op_sel:[0,0,1] op_sel_hi:[1,0,1]\n " \
  "v_pk_fma_f32 v[108:109], v[108:109], v[106:107], v[106:107] op_sel:[0,0,1] op_sel_hi:[1,0,1]\n " \
  "v_pk_fma_f32 v[110:111], v[110:111], v[106:107], v[106:107] op_sel:[0,0,1] op_sel_hi:[1,0,1]\n ds_write_b128 v55, v[228:231] offset:9216\n " \
  "v_exp_f32 v112, v112\n v_pk_mul_f32 v[172:173], v[172:173], v[168:169] op_sel_hi:[1,0]\n v_exp_f32 v113, v113\n " \
  "v_pk_mul_f32 v[174:175], v[174:175], v[168:169] op_sel_hi:[1,0]\n v_exp_f32 v114, v114\n v_cvt_pk_bf16_f32 v172, v172, v173\n v_exp_f32 v115, v115\n " \
  "v_cvt_pk_bf16_f32 v173, v174, v175\n v_exp_f32 v116, v116\n ds_write_b64 v58, v[172:173] offset:0\n v_exp_f32 v117, v117\n " \
  "v_pk_mul_f32 v[80:81], v[80:81], v[168:169] op_sel_hi:[1,0]\n v_exp_f32 v118, v118\n v_pk_mul_f32 v[82:83], v[82:83], v[168:169] op_sel_hi:[1,0]\n " \
  "v_exp_f32 v119, v119\n v_cvt_pk_bf16_f32 v80, v80, v81\n v_exp_f32 v244, v244\n v_cvt_pk_bf16_f32 v81, v82, v83\n v_exp_f32 v245, v245\n " \
  "ds_write_b64 v58, v[80:81] offset:32\n v_exp_f32 v246, v246\n v_pk_mul_f32 v[200:201], v[200:201], v[168:169] op_sel:[0,1]\n v_exp_f32 v247, v247\n " \
  "v_pk_mul_f32 v[202:203], v[202:203], v[168:169] op_sel:[0,1]\n v_exp_f32 v108, v108\n v_cvt_pk_bf16_f32 v200, v200, v201\n v_exp_f32 v109, v109\n " \
  "v_cvt_pk_bf16_f32 v201, v202, v203\n v_exp_f32 v110, v110\n ds_write_b64 v58, v[200:201] offset:64\n v_exp_f32 v111, v111\n " \
  "v_pk_mul_f32 v[204:205], v[204:205], v[168:169] op_sel:[0,1]\n v_pk_mul_f32 v[206:207], v[206:207], v[168:169] op_sel:[0,1]\n " \
  "v_cvt_pk_bf16_f32 v204, v204, v205\n v_cvt_pk_bf16_f32 v205, v206, v207\n ds_write_b64 v58, v[204:205] offset:96\n " \
  "v_pk_add_f32 v[216:217], v[220:221], v[222:223]\n v_pk_add_f32 v[218:219], v[124:125], v[126:127]\n v_pk_add_f32 v[216:217], v[216:217], v[218:219]\n " \
  "v_add_f32 v192, v216, v217\n v_pk_add_f32 v[216:217], v[128:129], v[130:131]\n v_pk_add_f32 v[218:219], v[132:133], v[134:135]\n " \
  "v_pk_add_f32 v[216:217], v[216:217], v[218:219]\n v_add_f32 v193, v216, v217\n v_pk_add_f32 v[216:217], v[244:245], v[246:247]\n " \
  "v_pk_add_f32 v[218:219], v[108:109], v[110:111]\n v_pk_add_f32 v[216:217], v[216:217], v[218:219]\n v_add_f32 v194, v216, v217\n " \
  "v_pk_add_f32 v[216:217], v[112:113], v[114:115]\n v_pk_add_f32 v[218:219], v[116:117], v[118:119]\n v_pk_add_f32 v[216:217], v[216:217], v[218:219]\n " \
  "v_add_f32 v195, v216, v217\n v_pk_mul_f32 v[196:197], v[196:197], v[170:171] op_sel_hi:[1,0]\n " \
  "v_pk_mul_f32 v[198:199], v[198:199], v[170:171] op_sel_hi:[1,0]\n v_permlane16_swap_b32 v192, v193\n v_permlane16_swap_b32 v194, v195\n " \
  "v_cvt_pk_bf16_f32 v196, v196, v197\n v_cvt_pk_bf16_f32 v197, v198, v199\n v_add_f32 v192, v192, v193\n v_add_f32 v194, v194, v195\n " \
  "ds_write_b64 v58, v[196:197] offset:2304\n v_pk_mul_f32 v[84:85], v[84:85], v[170:171] op_sel_hi:[1,0]\n v_permlane32_swap_b32 v192, v194\n " \
  "v_pk_mul_f32 v[86:87], v[86:87], v[170:171] op_sel_hi:[1,0]\n v_cvt_pk_bf16_f32 v84, v84, v85\n v_add_f32 v192, v192, v194\n " \
  "v_cvt_pk_bf16_f32 v85, v86, v87\n ds_write_b64 v58, v[84:85] offset:2336\n v_rcp_f32 v192, v192\n " \
  "v_pk_mul_f32 v[248:249], v[248:249], v[170:171] op_sel:[0,1]\n v_pk_mul_f32 v[250:251], v[250:251], v[170:171] op_sel:[0,1]\n v_mov_b32 v194, v192\n " \
  "v_cvt_pk_bf16_f32 v248, v248, v249\n v_cvt_pk_bf16_f32 v249, v250, v251\n v_permlane32_swap_b32 v192, v194\n " \
  "ds_write_b64 v58, v[248:249] offset:2368\n v_pk_mul_f32 v[252:253], v[252:253], v[170:171] op_sel:[0,1]\n v_mov_b32 v193, v192\n " \
  "v_mov_b32 v195, v194\n v_pk_mul_f32 v[254:255], v[254:255], v[170:171] op_sel:[0,1]\n v_cvt_pk_bf16_f32 v252, v252, v253\n " \
  "v_permlane16_swap_b32 v192, v193\n v_permlane16_swap_b32 v194, v195\n v_cvt_pk_bf16_f32 v253, v254, v255\n ds_write_b64 v58, v[252:253] offset:2400\n " \
  "v_pk_mul_f32 v[220:221], v[220:221], v[192:193] op_sel_hi:[1,0]\n v_pk_mul_f32 v[222:223], v[222:223], v[192:193] op_sel_hi:[1,0]\n " \
  "v_cvt_pk_bf16_f32 v220, v220, v221\n v_cvt_pk_bf16_f32 v221, v222, v223\n ds_write_b64 v58, v[220:221] offset:18432\n " \
  "v_pk_mul_f32 v[124:125], v[124:125], v[192:193] op_sel_hi:[1,0]\n v_pk_mul_f32 v[126:127], v[126:127], v[192:193] op_sel_hi:[1,0]\n " \
  "v_cvt_pk_bf16_f32 v124, v124, v125\n v_cvt_pk_bf16_f32 v125, v126, v127\n ds_write_b64 v58, v[124:125] offset:18464\n " \
  "v_pk_mul_f32 v[128:129], v[128:129], v[192:193] op_sel:[0,1]\n v_pk_mul_f32 v[130:131], v[130:131], v[192:193] op_sel:[0,1]\n " \
  "v_cvt_pk_bf16_f32 v128, v128, v129\n v_cvt_pk_bf16_f32 v129, v130, v131\n ds_write_b64 v58, v[128:129] offset:18496\n " \
  "v_pk_mul_f32 v[132:133], v[132:133], v[192:193] op_sel:[0,1]\n v_pk_mul_f32 v[134:135], v[134:135], v[192:193] op_sel:[0,1]\n " \
  "v_cvt_pk_bf16_f32 v132, v132, v133\n v_cvt_pk_bf16_f32 v133, v134, v135\n ds_write_b64 v58, v[132:133] offset:18528\n " \
  "v_pk_mul_f32 v[244:245], v[244:245], v[194:195] op_sel_hi:[1,0]\n v_pk_mul_f32 v[246:247], v[246:247], v[194:195] op_sel_hi:[1,0]\n " \
  "v_cvt_pk_bf16_f32 v244, v244, v245\n v_cvt_pk_bf16_f32 v245, v246, v247\n ds_write_b64 v58, v[244:245] offset:20736\n " \
  "v_pk_mul_f32 v[108:109], v[108:109], v[194:195] op_sel_hi:[1,0]\n v_pk_mul_f32 v[110:111], v[110:111], v[194:195] op_sel_hi:[1,0]\n " \
  "v_cvt_pk_bf16_f32 v108, v108, v109\n v_cvt_pk_bf16_f32 v109, v110, v111\n ds_write_b64 v58, v[108:109] offset:20768\n " \
  "v_pk_mul_f32 v[112:113], v[112:113], v[194:195] op_sel:[0,1]\n v_pk_mul_f32 v[114:115], v[114:115], v[194:195] op_sel:[0,1]\n " \
  "v_cvt_pk_bf16_f32 v112, v112, v113\n v_cvt_pk_bf16_f32 v113, v114, v115\n ds_write_b64 v58, v[112:113] offset:20800\n " \
  "v_pk_mul_f32 v[116:117], v[116:117], v[194:195] op_sel:[0,1]\n v_pk_mul_f32 v[118:119], v[118:119], v[194:195] op_sel:[0,1]\n " \
  "v_cvt_pk_bf16_f32 v116, v116, v117\n v_cvt_pk_bf16_f32 v117, v118, v119\n ds_write_b64 v58, v[116:117] offset:20832\n "
#define G1_OUTPUT \
  "s_mov_b32 m0, 0x40000\n s_branch .Lg1p%=\n .Lg1q%=:\n s_sleep 1\n s_sub_u32 m0, m0, 1\n s_cbranch_scc1 .Lg1r%=\n .Lg1p%=:\n " \
  "buffer_load_dwordx4 v[216:219], %[fz], %[rs], 0 offen sc1\n buffer_load_dwordx4 v[220:223], %[fz], %[rs], 0 offen offset:64 sc1\n " \
  "buffer_load_dwordx4 v[200:203], %[fo], %[rs], 0 offen offset:0 sc1\n buffer_load_dwordx4 v[204:207], %[fo], %[rs], 0 offen offset:2048 sc1\n " \
  "buffer_load_dwordx4 v[208:211], %[fo], %[rs], 0 offen offset:1024 sc1\n buffer_load_dwordx4 v[212:215], %[fo], %[rs], 0 offen offset:3072 sc1\n " \
  "s_waitcnt vmcnt(5)\n v_max_u32 v152, v216, v217\n v_max3_u32 v152, v218, v219, v152\n s_waitcnt vmcnt(4)\n v_max3_u32 v152, v220, v221, v152\n " \
  "v_max3_u32 v152, v222, v223, v152\n s_waitcnt vmcnt(3)\n v_max3_u32 v152, v200, v201, v152\n v_max3_u32 v152, v202, v203, v152\n s_waitcnt vmcnt(2)\n " \
  "v_max3_u32 v152, v204, v205, v152\n v_max3_u32 v152, v206, v207, v152\n s_waitcnt vmcnt(1)\n v_max3_u32 v152, v208, v209, v152\n " \
  "v_max3_u32 v152, v210, v211, v152\n s_waitcnt vmcnt(0)\n v_max3_u32 v152, v212, v213, v152\n v_max3_u32 v152, v214, v215, v152\n " \
  "v_cmp_eq_u32 vcc, -1, v152\n s_cbranch_vccnz .Lg1q%=\n .Lg1r%=:\n v_mfma_f32_16x16x32_bf16 v[160:163], v[216:219], v[224:227], 0\n " \
  "v_mfma_f32_16x16x32_bf16 v[160:163], v[220:223], v[228:231], v[160:163]\n v_mfma_f32_16x16x32_bf16 v[152:155], v[200:203], v[224:227], 0\n " \
  "v_mfma_f32_16x16x32_bf16 v[152:155], v[204:207], v[228:231], v[152:155]\n v_mfma_f32_16x16x32_bf16 v[156:159], v[208:211], v[224:227], 0\n " \
  "v_mfma_f32_16x16x32_bf16 v[156:159], v[212:215], v[228:231], v[156:159]\n v_mfma_f32_16x16x32_bf16 v[172:175], v[216:219], v[232:235], 0\n " \
  "v_mfma_f32_16x16x32_bf16 v[172:175], v[220:223], v[236:239], v[172:175]\n v_mfma_f32_16x16x32_bf16 v[164:167], v[200:203], v[232:235], 0\n " \
  "v_mfma_f32_16x16x32_bf16 v[164:167], v[204:207], v[236:239], v[164:167]\n v_mfma_f32_16x16x32_bf16 v[168:171], v[208:211], v[232:235], 0\n " \
  "v_add_f32 v160, 0x358637bd, v160\n v_rcp_f32 v160, v160\n v_mfma_f32_16x16x32_bf16 v[168:171], v[212:215], v[236:239], v[168:171]\n " \
  "v_mul_f32 v152, v160, v152\n v_mfma_f32_16x16x32_bf16 v[184:187], v[216:219], v[240:243], 0\n v_mul_f32 v153, v160, v153\n " \
  "v_cvt_pk_bf16_f32 v152, v152, v153\n v_mul_f32 v154, v160, v154\n v_mfma_f32_16x16x32_bf16 v[184:187], v[220:223], v[244:247], v[184:187]\n " \
  "v_mul_f32 v155, v160, v155\n v_cvt_pk_bf16_f32 v153, v154, v155\n v_mul_f32 v156, v160, v156\n " \
  "v_mfma_f32_16x16x32_bf16 v[176:179], v[200:203], v[240:243], 0\n v_mul_f32 v157, v160, v157\n v_cvt_pk_bf16_f32 v154, v156, v157\n " \
  "v_mul_f32 v158, v160, v158\n v_mfma_f32_16x16x32_bf16 v[176:179], v[204:207], v[244:247], v[176:179]\n v_mul_f32 v159, v160, v159\n " \
  "v_cvt_pk_bf16_f32 v155, v158, v159\n buffer_store_dwordx4 v[152:155], %[go], %[rs], 0 offen offset:0 sc1\n " \
  "v_mfma_f32_16x16x32_bf16 v[180:183], v[208:211], v[240:243], 0\n ds_write_b128 %[lo], v[152:155] offset:0\n v_add_f32 v172, 0x358637bd, v172\n " \
  "v_mfma_f32_16x16x32_bf16 v[180:183], v[212:215], v[244:247], v[180:183]\n v_rcp_f32 v172, v172\n " \
  "v_mfma_f32_16x16x32_bf16 v[196:199], v[216:219], v[248:251], 0\n v_mul_f32 v164, v172, v164\n v_mul_f32 v165, v172, v165\n " \
  "v_cvt_pk_bf16_f32 v164, v164, v165\n v_mfma_f32_16x16x32_bf16 v[196:199], v[220:223], v[252:255], v[196:199]\n v_mul_f32 v166, v172, v166\n " \
  "v_mul_f32 v167, v172, v167\n v_cvt_pk_bf16_f32 v165, v166, v167\n v_mfma_f32_16x16x32_bf16 v[188:191], v[200:203], v[248:251], 0\n " \
  "v_mul_f32 v168, v172, v168\n v_mul_f32 v169, v172, v169\n v_cvt_pk_bf16_f32 v166, v168, v169\n " \
  "v_mfma_f32_16x16x32_bf16 v[188:191], v[204:207], v[252:255], v[188:191]\n v_mul_f32 v170, v172, v170\n v_mul_f32 v171, v172, v171\n " \
  "v_cvt_pk_bf16_f32 v167, v170, v171\n v_mfma_f32_16x16x32_bf16 v[192:195], v[208:211], v[248:251], 0\n " \
  "buffer_store_dwordx4 v[164:167], %[go], %[rs], 0 offen offset:1024 sc1\n ds_write_b128 %[lo], v[164:167] offset:1024\n " \
  "v_mfma_f32_16x16x32_bf16 v[192:195], v[212:215], v[252:255], v[192:195]\n v_add_f32 v184, 0x358637bd, v184\n v_rcp_f32 v184, v184\n s_nop 0\n " \
  "v_pk_mul_f32 v[176:177], v[176:177], v[184:185] op_sel_hi:[1,0]\n v_pk_mul_f32 v[178:179], v[178:179], v[184:185] op_sel_hi:[1,0]\n " \
  "v_pk_mul_f32 v[180:181], v[180:181], v[184:185] op_sel_hi:[1,0]\n v_pk_mul_f32 v[182:183], v[182:183], v[184:185] op_sel_hi:[1,0]\n " \
  "v_cvt_pk_bf16_f32 v176, v176, v177\n v_cvt_pk_bf16_f32 v177, v178, v179\n v_cvt_pk_bf16_f32 v178, v180, v181\n v_cvt_pk_bf16_f32 v179, v182, v183\n " \
  "buffer_store_dwordx4 v[176:179], %[go], %[rs], 0 offen offset:2048 sc1\n ds_write_b128 %[lo], v[176:179] offset:2048\n " \
  "v_add_f32 v196, 0x358637bd, v196\n v_rcp_f32 v196, v196\n s_nop 0\n v_pk_mul_f32 v[188:189], v[188:189], v[196:197] op_sel_hi:[1,0]\n " \
  "v_pk_mul_f32 v[190:191], v[190:191], v[196:197] op_sel_hi:[1,0]\n v_pk_mul_f32 v[192:193], v[192:193], v[196:197] op_sel_hi:[1,0]\n " \
  "v_pk_mul_f32 v[194:195], v[194:195], v[196:197] op_sel_hi:[1,0]\n v_cvt_pk_bf16_f32 v188, v188, v189\n v_cvt_pk_bf16_f32 v189, v190, v191\n " \
  "v_cvt_pk_bf16_f32 v190, v192, v193\n v_cvt_pk_bf16_f32 v191, v194, v195\n buffer_store_dwordx4 v[188:191], %[go], %[rs], 0 offen offset:3072 sc1\n " \
  "ds_write_b128 %[lo], v[188:191] offset:3072\n "
#define G1_PURGE \
  ".purgem G1M\n .purgem G1RA\n .purgem G1RB\n .purgem G1BK\n .purgem G1AK\n .purgem G1RDB\n .purgem G1RDA\n .purgem G1Q\n .purgem G1SG\n "
#define G1_CLOBBER \
  "v80", "v81", "v82", "v83", "v84", "v85", "v86", "v87", "v92", "v93", "v94", "v95", "v96", "v97", "v98", "v99", "v100", "v101", "v102", "v103", \
  "v104", "v105", "v106", "v107", "v108", "v109", "v110", "v111", \
  "v112", "v113", "v114", "v115", "v116", "v117", "v118", "v119", "v120", "v121", "v122", "v123", "v124", "v125", "v126", "v127", "v128", "v129", \
  "v130", "v131", "v132", "v133", "v134", "v135", "v144", "v145", "v146", "v147", "v152", "v153", "v154", "v155", \
  "v156", "v157", "v158", "v159"

template <int MODE>
__device__ __forceinline__ void g1_phase(char* smem, char* ws, u64& E, int w, f32x16& xq, Next& nx, int layer, const u16* __restrict__ x0, const u16* __restrict__ W,
                                         const u16* __restrict__ wo, const u16* __restrict__ wphi,
                                         const u16* __restrict__ wn1, const u16* __restrict__ wn2) {
  constexpr bool HH = (MODE == 1);
  constexpr int ABYTES = 128 * 128;
  constexpr int STAGE = ABYTES + 3 * 8192;
  constexpr int OFF_FX = 16384;
  constexpr int OFF_SP = 3 * STAGE;
  constexpr int OFF_RS = !HH ? 159744 : OFF_SP + 16384;
  constexpr int OFF_CS = OFF_RS + (HH ? 1152 : 1024);
  constexpr int OFF_WP = OFF_CS + 1536;
  constexpr int OFF_JK = LDS_TOTAL - 1024;
  constexpr int OFF_PK = 0;
  constexpr int OFF_VF = OFF_WP + 4096;
  constexpr int OFF_R2 = HH ? OFF_VF + 16384 : OFF_CS + 1024;
  constexpr int OFF_RD = OFF_CS + 768;
  constexpr int PSTR = 144;
  IDS
  W = uni(W); wphi = uni(wphi); wn1 = uni(wn1); wn2 = uni(wn2);
  const int kg = w >> 2, wm = (w >> 1) & 1, wn = w & 1;
  const bool first = !HH && layer == 0;
  f32x4 acc[2][2], qh[2][2], csq[2], st01, st23;
  bf16x8 wf[2];
  u64 kol;
  u32x4 pw[8];
  ST(layer * 24 + 0)
  const int R0 = wm * 64;
  u32x16 pa;
#define pcs pa[0]
#define prs pa[1]
#define opo pa[2]
#define ogo pa[3]
#define hof pa[7]
#define dm pa[8]
#define dm2 pa[9]
#define pcv pa[4]
#define pwf pa[5]
#define prv pa[6]
#define pvf pa[7]
#define ppk pa[10]
#define ppa pa[11]
#define pvr pa[12]
#define gkv pa[13]
#define gkz pa[14]
#define pwl pa[15]
#define pwm pa[4]
#define plb pa[10]
#define prd pa[11]
#define dm3 pa[12]
  f32x4 bias[3];
  const char* wos;
  unsigned tO;
  const unsigned sb = lds_addr(smem);
  auto prep = [&](auto ldr) {
    constexpr bool LDR = decltype(ldr)::value;
    if constexpr (!LDR) {
      pcs = sb + OFF_CS + wn * 128 + g * 16;
      prs = sb + OFF_RS + (R0 + fr) * 8;
      opo = (unsigned)(wn * 8192 + R0 * 64 + lane * 16);
      ogo = opo + WS_O + layer * (SEQ * DM * 2) + (unsigned)((rt * NH + 2 * hp) * 8192);
      opo += sb;
      pcv = sb + OFF_CS + (wn * 32 + 128 + fr) * 4;
      prv = sb + OFF_R2 + (R0 + 4 * g) * 4;
    } else {
      constexpr unsigned kvn = WS_KV + MODE * KVLB;
      const int t = tid & 255;
      dm = kvn + (unsigned)((rt * NH + 2 * hp) * KVPB) + t * 16;
      dm2 = t < 16 ? dm + 8192 : t >= 64 && t < 97 ? kvn + 16u * NH * KVPB + (unsigned)(hp * KVFB + rt * 528 + (t - 64) * 16) : 0x7fff0000u;
      if (!HH) dm3 = WS_HL + (unsigned)((256 + rt * 16 + hp) * 4096 + t * 16);
    }
    if constexpr (!HH) {
      if constexpr (!LDR) {
        prd = sb + OFF_RD + wn * 4;
        plb = sb + OFF_FX + wn * 2048 + lane * 16;
        hof = m0 + wm == 0 ? 0x7fff0000u : WS_HL + (unsigned)((layer * 128 + rt * 16 - 16 + wm * 16 + hp) * 4096 + wn * 2048 + lane * 16);
        const unsigned out = ~((0x5555u << 2 << fr) >> (4 * g));
#pragma unroll
        for (int i = 0; i < 8; ++i) {
          unsigned t;
          asm("v_bfe_i32 %0, %1, %2, 1" : "=v"(t) : "v"(out), "n"((i >> 2) * 16 + (i & 3)));
          bias[i >> 2][i & 3] = __uint_as_float(t & 0xff61b1e6u);
        }
        bias[2] = m0 + R0 == 0 ? f32x4{-3.0e38f, -3.0e38f, -3.0e38f, -3.0e38f} : bias[0];
        asm volatile("" : "+{v[36:39]}"(bias[0]), "+{v[40:43]}"(bias[1]), "+{v[44:47]}"(bias[2]));
      }
    } else {
      const int u = wm * 2 + kg;
      if constexpr (!LDR) {
        pwf = sb + OFF_WP + wn * 2048 + (4 * g + q4) * 64 + p4 * 8;
        pvf = sb + OFF_VF + w * 2048 + lane * 16;
        ppk = sb + OFF_PK + w * (32 * PSTR) + fr * PSTR + g * 8;
      }
      ppa = sb + OFF_PK + wn * (32 * PSTR) + (4 * g + q4) * PSTR + u * 32 + p4 * 8;
      pvr = sb + OFF_VF + wn * 2048 + lane * 16;
      const unsigned doff = WS_KV + (layer >> 1) * KVLB + (unsigned)((rt * NH + 2 * hp + wn) * KVPB);
      gkv = doff + u * 1024 + lane * 16;
      gkz = fr == 0 ? doff + 4096 + u * 32 + g * 8 : 0x7fff0000u;
    }
    asm volatile("" : "+{v[48:63]}"(pa));
  };

  if (kg == 1) {

    const int lw = w & 3;
    const int kB = 16 * lw + (lane >> 3);
    const int fB = (kB >> 1) & 1;
    const int cB0 = ((((lane & 7) >> 1) ^ fB) << 1) | (lane & 1), cB1 = ((((lane & 7) >> 1) ^ (fB | 2)) << 1) | (lane & 1);
    const bool im = img(layer, hp);

    unsigned vW0, vW1, vP, vX, vY;
    u64 ko2;
    auto ld_pw = [&](int i0) {
#pragma unroll
      for (int i = i0; i < i0 + 4; ++i) gld16(pw[i], vP, wn1 + i * 8 * 3072);
    };
    auto wo_tile = [&](int t) { glds2v(wos + knib(ko2, t) * tO, vW0, vW1, lds_addr(smem) + OFF_WO + t * 8192 + lw * 2048); };
    auto run = [&](auto fst) {
      constexpr bool FIRST = decltype(fst)::value;
      constexpr int N = 10;
      const unsigned vA = FIRST ? (unsigned)(((m0 + lw * 32 + (lane >> 3)) * DM + ((lane & 7) ^ (lane >> 3)) * 8) * 2)
                                : (unsigned)(rt * 262144 + lw * 4096 + lane * 16);
      unsigned imv = !FIRST && im ? ~0u : 0u;
      if (!FIRST) asm volatile("" : "+v"(imv));
      const unsigned vB0 = ((unsigned)(lw * 6144 + lane * 16) & imv) | ((unsigned)((kB * 3072 + hp * 64 + cB0 * 8) * 2) & ~imv);
      const unsigned vB1 = ((unsigned)(lw * 6144 + lane * 16) & imv) | (((unsigned)(((kB + 8) * 3072 + hp * 64 + cB1 * 8) * 2) - 1024u) & ~imv);
      const char* xa = FIRST ? (const char*)x0 : ws + WS_X + (layer - 1) * (SEQ * DM * 2);
      constexpr int KS = FIRST ? 128 : 16384;
      const u64 kord = FIRST ? korder0(rt) : nx.kord;
      auto ktile = [&](int ks) { return knib(kord, ks); };
      auto stageA = [&](int ks, int boff) {
        const int kt = ktile(ks);
        const unsigned sA = lds_addr(smem) + boff;
        if (FIRST) glds4v(xa + kt * KS, vA, vA + 15360u, vA + 2 * 15360u, vA + 3 * 15360u, sA + lw * 4096);
        else glds4c(xa + kt * KS, vA, sA + lw * 4096);
      };
      auto stageB = [&](const u16* Wp, int ks, int boff) {
        const int kt = ktile(ks);
        const unsigned sB = lds_addr(smem) + boff + ABYTES;
        const char* b = FIRST ? (const char*)(Wp + (size_t)kt * 64 * 3072) : nx.wq + kt * nx.tw;
        glds4v(b, vB0, vB1, vB0, vB1, sB + lw * 6144);
        glds2v(b + 4096, vB0, vB1, sB + lw * 6144 + 4096);
      };
      bf16x8 sel[3];
      unsigned cso[3], cst;
      if (FIRST) {
        WAITVN(N);
        BAR;
        stageA(2, 2 * STAGE); stageB(W, 2, 2 * STAGE);
        __builtin_amdgcn_sched_barrier(0);
        const int fl = (q4 >> 1) | ((g & 1) << 1), j = 3 * wm + (fr < 3 ? fr : 0);
#pragma unroll
        for (int i = 0; i < 3; ++i) {
          const unsigned o = fr == i ? 0x3F803F80u : 0u;
          sel[i] = __builtin_bit_cast(bf16x8, u32x4{o, o, o, o});
          cso[i] = (unsigned)(2 * STAGE + ABYTES + (g >> 1) * 6144 + (g & 1) * 1024 + q4 * 128 + p4 * 8 + (((wn * 2 + ((wm + i) & 1)) ^ fl) << 5) + ((3 * wm + i) >> 1) * 2048);
          asm volatile("" : "+v"(sel[i]), "+v"(cso[i]));
        }
        cst = fr < 3 ? WS_CPA + (unsigned)(((hp * 16 + rt) * 192 + (j >> 1) * 64 + wn * 32 + (j & 1) * 16 + 4 * g) * 4) : 0x7fff0000u;
        asm volatile("" : "+v"(cst));
      }
      u64 fall, fal2, fal3, fsv = E + (unsigned)(layer + 1);
      unsigned fao, fa2, vS, vC, dS, dC, dR;
      auto setup = [&]() {
        vP = (unsigned)((rt * 64 + (lane >> 3)) * 6144 + lw * 2048 + ((hp + (1 << lw)) & 15) * 128 + (lane & 7) * 16);
        vS = FIRST ? (unsigned)(WS_G0 + (m0 + lw * 32 + (lane & 31)) * 16)
                   : WS_ST + (unsigned)((layer - 1) * (SEQ * 128) + rt * 16384 + g * 1024 + lw * 256 + fr * 16);
        vC = (FIRST ? WS_CPA : WS_CSP) + (unsigned)(hp * 12288 + g * 768 + lw * 192 + (fr < 12 ? fr : 0) * 16);
        if (!FIRST) vX = HH ? (unsigned)(hp * 4096 + lw * 1024 + lane * 16) : (unsigned)((rt * 16 + hp) * 1024 + lane * 16);
        vW0 = ((unsigned)(lw * 2048 + lane * 16) & imv) | ((unsigned)((kB * DM + hp * 64 + ((lane & 7) ^ ((lane >> 4) << 1)) * 8) * 2) & ~imv); vW1 = vW0 + ((8 * DM * 2 - 1024u) & ~imv);
        dS = OFF_RS + lw * 256 + (FIRST ? (lane & 31) * 8 : fr * 16 + (g & 1) * 8); dC = OFF_CS + lw * 192 + (fr < 12 ? fr : 0) * 16 + g * 4;
        ko2 = FIRST ? korder(bid & 3, hp) : nx.ko2;
        wos = FIRST ? (const char*)wo : wo_src(ws, wo, layer, hp); tO = !FIRST && im ? 8192u : 131072u;
        asm volatile("" : "+s"(wos), "+s"(tO));
        dR = OFF_R2 + lw * 128 + (FIRST ? (lane & 31) * 4 : fr * 8 + (g & 1) * 4);
        asm volatile("" : "+v"(dR));
        asm volatile("" : "+v"(vS), "+v"(vX), "+v"(vW0), "+v"(vW1), "+v"(dS));
        asm volatile("" : "+s"(ko2));
        if (FIRST || HH) asm volatile("" : "+v"(vC), "+v"(dC), "+v"(fa2));
        if (FIRST) asm volatile("" : "+v"(vP));
      };
      if (!FIRST) {
        fao = (unsigned)((256 + rt * 16 + (lane & 15)) * FLS);
        if (HH) { vY = (unsigned)(bid * 8192 + lw * 2048 + lane * 16); fa2 = (unsigned)((256 + (lane & 15) * 16 + ((hp - ((1 << (lane >> 4)) >> 1)) & 15)) * FLS); asm volatile("" : "+v"(vY), "+v"(fa2)); }
        asm volatile("" : "+v"(fsv), "+v"(fao));
      }
      u32x4 sv[4], cv[4];
      auto ld_c = [&](u32x4& d, unsigned v, int so) { asm volatile("buffer_load_dwordx4 %0, %1, %2, %3 offen sc1" : "=v"(d) : "v"(v), "s"(rsW), "s"(so) : "memory"); };
      float tm, tr;
      unsigned tc;
      auto fin = [&]() {
        if (FIRST) {
          const unsigned tag = (unsigned)E + 1u;
          asm volatile("" : "+v"(sv[0]));
          _Pragma("unroll 1") for (unsigned spins = 0; spins < (1u << 18); ++spins) {
            if (__all(sv[0][1] == tag && sv[0][3] == tag)) break;
            __builtin_amdgcn_s_sleep(1);
            asm volatile("buffer_load_dwordx4 %0, %1, %2, 0 offen sc1\n\ts_waitcnt vmcnt(0)" : "=v"(sv[0]) : "v"(vS), "s"(rsW) : "memory");
          }
          tm = __uint_as_float(sv[0][0]); tr = __uint_as_float(sv[0][2]);
        } else {
          asm volatile("" : "+v"(sv[0]), "+v"(sv[1]), "+v"(sv[2]), "+v"(sv[3]));
          const f32x4 t = sum4(sv);
          const unsigned u = xs4(t[0], t[2], t[1], t[3]);
          auto h = __builtin_amdgcn_permlane32_swap(u, u, false, false);
          const float mean = __uint_as_float(h[0]) * (1.0f / 1024.0f);
          const float var = fmaxf(__uint_as_float(h[1]) * (1.0f / 1024.0f) - mean * mean, 0.f);
          tm = -mean; tr = __builtin_amdgcn_rsqf(var + 1e-5f);
        }
      };
      auto cfin = [&]() {
        if (FIRST || HH) {
          asm volatile("" : "+v"(cv[0]), "+v"(cv[1]), "+v"(cv[2]), "+v"(cv[3]));
          const f32x4 t = sum4(cv);
          tc = xs4(t[0], t[1], t[2], t[3]);
        }
      };
      bf16x8 cf[6];
      f32x4 csum = {0, 0, 0, 0};
      if (FIRST) {
        asm volatile("s_load_dwordx2 %0, %1, 0x0\n\ts_waitcnt lgkmcnt(0)" : "=s"(E) : "s"(ws) : "memory");
        fall = E + 1u; fa2 = (unsigned)((512 + (lane & 15) * 16 + hp) * FLS); vX = (unsigned)(bid * 8192 + lw * 2048 + lane * 16);
        asm volatile("" : "+v"(fall), "+v"(fa2), "+v"(vX));
      }
#pragma unroll
      for (int kt = 0; kt < 13; ++kt) {
        const int cur = (kt % 3) * STAGE;
        if (!FIRST && kt == 0) { WAITV(4); }
        else if (FIRST && kt == 4 && lw == 1) { WAITVN(N + 2); }
        else if (HH && (kt == 4 || kt == 6)) { WAITVN(N + 1); }
        else if (HH && kt == 5) { WAITVN(N + 2); }
        else if ((!FIRST && kt == 1 && lw == 3) || (!FIRST && kt == 9 && lw == 2) || ((FIRST || HH) && kt == 10 && lw == 3) || (FIRST && (kt == 2 || kt == 4 || kt == 5))) { WAITVN(N + 1); }
        else { WAITVN(N); }
        if (!FIRST && kt == 2 && lw == 3) {
          asm volatile("" : "+v"(fall));
          if (__any(fall != E + (unsigned)layer)) poll_flags(ws, E, 1, rt, hp, lane, layer, false);
        }
        if (!FIRST && kt == 10 && lw == 2) {
          asm volatile("" : "+v"(fal3));
          if (__any(fal3 != E + (unsigned)(layer + 1))) poll_flags(ws, E, 2, rt, hp, lane, layer + 1, false);
        }
        if (HH && kt == 11 && lw == 3) {
          asm volatile("" : "+v"(fal2));
          if (__any(fal2 != E + (unsigned)layer)) {
            _Pragma("unroll 1") for (int j = 0; j < 4; ++j) poll_flags(ws, E, 1, rt, (hp - ((1 << j) >> 1)) & 15, lane, layer, true);
          }
        }
        if (FIRST && kt == 11 && lw == 3) {
          asm volatile("" : "+v"(fal2));
          if (__any(fal2 != E + 1u)) poll_flags(ws, E, 2, rt, hp, lane, 1u, true);
        }
        BAR;
        if (FIRST && kt == 1) {
          asm volatile("" ::: "memory");
#pragma unroll
          for (int i = 0; i < 6; ++i) { const char* t = smem + cso[i >> 1] + (i & 1) * 12288; cf[i] = cat8(tr_read(t), tr_read(t + 512)); }
        }
        stageA(kt + 3, cur);
        if (FIRST && kt == 1) {
          asm volatile("" : "+v"(sel[0]));
#pragma unroll
          for (int i = 0; i < 6; ++i) csum = MFMA(cf[i], sel[i >> 1], csum);
          asm volatile("" : "+v"(csum));
        }
        stageB(W, kt + 3, cur);
        if (FIRST) {
          if (kt == 1) __builtin_amdgcn_raw_buffer_store_b128(__builtin_bit_cast(u32x4, csum), rsW, cst, 0, 16);
          if (kt == 3 && tid == 320) __builtin_amdgcn_raw_buffer_store_b64(__builtin_bit_cast(u32x2, fall), rsW, WS_FLAGS + (unsigned)(512 + rt * 16 + hp) * FLS, 0, 16);
          if (kt == 3 || kt == 4) glds16s(wo + (kt - 3) * 512, vX, lds_addr(smem) + OFF_JK);
        }
        if (HH && (kt == 3 || kt == 4)) glds16s(wo + (kt - 3) * 512, vY, lds_addr(smem) + OFF_JK);
        if (!FIRST && kt == 3 && tid == 320) __builtin_amdgcn_raw_buffer_store_b64(__builtin_bit_cast(u32x2, fsv), rsW, WS_FLAGS + (unsigned)(512 + rt * 16 + hp) * FLS, 0, 16);
        if (!FIRST && kt == 8 && lw == 2) asm volatile("global_load_dwordx2 %0, %1, %2 sc1" : "=v"(fal3) : "v"(fao), "s"(ws + WS_FLAGS + 256 * FLS) : "memory");
        if (!FIRST && kt == 0 && lw == 3) asm volatile("global_load_dwordx2 %0, %1, %2 sc1" : "=v"(fall) : "v"(fao), "s"(ws + WS_FLAGS) : "memory");
        if ((FIRST || HH) && kt == 9 && lw == 3) asm volatile("global_load_dwordx2 %0, %1, %2 sc1" : "=v"(fal2) : "v"(fa2), "s"(ws + WS_FLAGS) : "memory");
      }
      setup();
      WAITVN(N);
      BAR;
#pragma unroll
      for (int i = 0; i < (FIRST ? 1 : 4); ++i) ld_c(sv[i], vS, i * 4096);
      if (FIRST || HH) {
#pragma unroll
        for (int i = 0; i < 4; ++i) ld_c(cv[i], vC, i * 3072);
      }
      if (HH) glds16s(wphi, vX, lds_addr(smem) + OFF_WP + lw * 1024);
      else if (!FIRST) glds16c(ws + WS_CSA, vX, lds_addr(smem) + OFF_CS);
      wo_tile(0); wo_tile(1);
      WAITVN(HH ? 13 : 9);
      BAR;
      if (FIRST) {
        if (lw < 3) { ld_pw(0); ld_pw(4); WAITV(12); } else { WAITV(4); }
      } else {
        if (HH) prep(IC<1>{});
        WAITV(4);
      }
      cfin();
      fin();
      asm volatile("" : "+v"(tm), "+v"(tr));
      if (FIRST || HH) asm volatile("" : "+v"(tc));
      if (FIRST && lw < 3) { WAITV(8); } else { WAITV(0); }
      lds<f32x2>(sb + dS) = f32x2{tm, tr};
      lds<float>(sb + dR) = tm; lds<float>(sb + dR + 512) = tr;
      if (FIRST || HH) lds<unsigned>(sb + dC) = tc;
      if (!HH) lds<float>(sb + OFF_RD + wn * 4) = zero1();
      WAITL0;
    };
    typedef std::integral_constant<bool, true> T_;
    typedef std::integral_constant<bool, false> F_;
    __builtin_amdgcn_s_setprio(3);
    if (first) run(T_{}); else run(F_{});
    __builtin_amdgcn_s_setprio(0);
    BAR;
    if constexpr (HH) {
      u32x4 c0, c1;
      if (layer == 1) {
        const int n = bid * 8 + lw * 2;
        const void* p;
        unsigned a0, a1;
        if (n < 1536) {
          const int r = n % 768, q = r % 384, e = q % 24;
          p = n < 768 ? wn1 : W;
          a0 = (unsigned)((((q / 24) * 64 + (e / 6) * 16 + (lane >> 3)) * 3072 + ((e % 6) >> 1) * 1024 + (r < 384 ? 192 : 704)) * 2);
          a1 = a0 + 8 * 6144 + cB1 * 16; a0 += cB0 * 16;
        } else {
          p = n < 1792 ? wn2 : wo;
          a0 = (unsigned)((((n & 127) * 8 + (lane >> 3)) * DM + ((n & 128) ? 704 : 192) + ((lane & 7) ^ ((lane >> 4) << 1)) * 8) * 2);
          a1 = a0 + 8 * DM * 2;
        }
        p = uni(p);
        gld16(c0, a0, p); gld16(c1, a1, p);
        const u32x4 pz = empty4();
        __builtin_amdgcn_raw_buffer_store_b128(pz, rsW, dm, 0, 16);
        __builtin_amdgcn_raw_buffer_store_b128(pz, rsW, dm, 4096, 16);
        __builtin_amdgcn_raw_buffer_store_b128(pz, rsW, dm2, 0, 16);
      }
#pragma unroll
      for (int t = 2; t < 6; ++t) wo_tile(t);
      if (layer == 1) {
        WAITV(11);
        __builtin_amdgcn_sched_barrier(0);
        asm volatile("" : "+v"(c0), "+v"(c1));
        __builtin_amdgcn_raw_buffer_store_b128(c0, rsW, WS_IMG + (unsigned)(bid * 8192 + lw * 2048 + lane * 16), 0, 16);
        __builtin_amdgcn_raw_buffer_store_b128(c1, rsW, WS_IMG + (unsigned)(bid * 8192 + lw * 2048 + 1024 + lane * 16), 0, 16);
        WAITV(0);
      }
      pwl = vW0; pwm = vW1; kol = ko2;
      asm volatile("" : "=v"(csq[0]), "=v"(csq[1]), "=v"(st01), "=v"(st23), "=v"(wf[0]), "=v"(wf[1]));
      asm volatile("" : "=v"(acc[0][0]), "=v"(acc[0][1]), "=v"(acc[1][0]), "=v"(acc[1][1]));
      asm volatile("" : "=v"(qh[0][0]), "=v"(qh[0][1]), "=v"(qh[1][0]), "=v"(qh[1][1]));
    } else {
      const int lane = lane_id();
#pragma unroll
      for (int t = 2; t < 12; ++t) {
        wo_tile(t);
        if (t == 2 && first) {
        prep(IC<1>{});
        const u32x4 pz = empty4();
        __builtin_amdgcn_raw_buffer_store_b128(pz, rsW, dm, 0, 16);
        __builtin_amdgcn_raw_buffer_store_b128(pz, rsW, dm, 4096, 16);
        __builtin_amdgcn_raw_buffer_store_b128(pz, rsW, dm3, 0, 16);
        __builtin_amdgcn_raw_buffer_store_b128(pz, rsW, dm2, 0, 16);
        if (lw == 3 && lane < 48) __builtin_amdgcn_raw_buffer_store_b128(lds<u32x4>(sb + OFF_CS + lane * 16), rsW, WS_CSA + (unsigned)((rt * 16 + hp) * 1024 + lane * 16), 0, 16);
        }
      }
      if (first) { BAR;   WAITV(0); }
    }
    asm volatile("" : "={v[64:79]}"(xq));
  } else {

    prep(IC<0>{});
    if (first) {
      const int row = m0 + hp * 8 + w * 2 + (lane >> 5);
      asm volatile("s_load_dwordx2 %0, %1, 0x0\n\ts_waitcnt lgkmcnt(0)" : "=s"(E) : "s"(ws) : "memory");
      const unsigned tag = (unsigned)E + 1u;
      static_assert(HH || (OFF_SP == 122880 && OFF_R2 + 1024 <= OFF_JK), "addresses used at the top of the kernel");
      WAITV(0);
      u32x4 xs[4];
#pragma unroll
      for (int i = 0; i < 4; ++i) xs[i] = *(const u32x4*)(smem + OFF_SP + w * 4096 + (lane >> 5) * 2048 + (lane & 31) * 16 + i * 512);
      typedef __attribute__((ext_vector_type(2))) __bf16 bf2;
      const bf2 one2 = __builtin_bit_cast(bf2, 0x3F803F80u);
      float sa[2] = {zero1(), zero1()}, sq[2] = {zero1(), zero1()};
#pragma unroll
      for (int i = 0; i < 16; ++i) {
        const unsigned u = xs[i >> 2][i & 3];
        const bf2 t = __builtin_bit_cast(bf2, u);
        sa[i & 1] = __builtin_amdgcn_fdot2_f32_bf16(t, one2, sa[i & 1], false);
        sq[i & 1] = __builtin_amdgcn_fdot2_f32_bf16(t, t, sq[i & 1], false);
      }
      float sm = r16_sum(sa[0] + sa[1]), m2 = r16_sum(sq[0] + sq[1]);
      {
        auto r = __builtin_amdgcn_permlane16_swap(__float_as_uint(sm), __float_as_uint(sm), false, false);
        auto q = __builtin_amdgcn_permlane16_swap(__float_as_uint(m2), __float_as_uint(m2), false, false);
        sm = __uint_as_float(r[0]) + __uint_as_float(r[1]); m2 = __uint_as_float(q[0]) + __uint_as_float(q[1]);
      }
      const float mean = sm * (1.0f / 1024.0f);
      m2 = fmaxf(m2 - sm * mean, 0.f);
      u32x4 gw = {__float_as_uint(-mean), tag, __float_as_uint(rsqrtf(m2 * (1.0f / 1024.0f) + 1e-5f)), tag};
      if ((lane & 31) == 0) __builtin_amdgcn_raw_buffer_store_b128(gw, rsW, (unsigned)(WS_G0 + row * 16), 0, 16);
      const u64 ko = korder(bid & 3, hp);
      nx.kf = kfirst(ko) | (kfirst(ko >> 16) | (1u << knib(ko, 9))) << 16;
      asm volatile("" : "+s"(nx.kf));
      __builtin_amdgcn_raw_buffer_store_b128(empty4(), rsW, WS_HL + (unsigned)((rt * 16 + hp) * 4096 + tid * 16), 0, 16);
    }
    const int fl = (q4 >> 1) | ((g & 1) << 1);
    const unsigned bo = lds_addr(smem) + ABYTES + (g >> 1) * 6144 + (g & 1) * 1024 + q4 * 128 + p4 * 8;
    const unsigned bo0 = bo + (((wn * 2 + 0) ^ fl) << 5), bo1 = bo + (((wn * 2 + 1) ^ fl) << 5);
    const unsigned ar = lds_addr(smem) + (wm * 64 + fr) * 128;
    const unsigned aK0 = first ? ar + ((g ^ (fr & 7)) << 4) : sb + wm * 8192 + lane * 16, aK1 = first ? ar + (((4 + g) ^ (fr & 7)) << 4) : aK0 + 1024;
    f32x32 ac0, ac1, ac2;
    asm volatile("" :: "v"(aK0), "v"(aK1), "v"(bo0), "v"(bo1));
    if (first) BAR;
    else publish(rsW, tid, 256 + rt * 16 + hp, E + (unsigned)layer);
#define G1_OUT
#define G1_T(a)
#define G1_TEND
#define G1_TCL
    asm volatile(G1_MACROS
        "s_mov_b32 vcc_hi, %[lay]\n"
        ".if %[hh]\n s_branch .Lg1m\n .else\n"
        ".p2align 3\n .Lg1m:\n"
        "G1RDB 0, v16, v17, 0\n G1RDA v12, v13, 0\n G1RDB 1, v16, v17, 0\n"
        "ds_read_b128 v[64:67], v20\n ds_read_b128 v[68:71], v20 offset:2048\n"
        "ds_read_b128 v[72:75], v20 offset:4096\n ds_read_b128 v[76:79], v20 offset:6144\n"
        "s_cmp_eq_u32 vcc_hi, 0\n s_waitcnt lgkmcnt(12)\n"
        "G1SG 0, 1, 1, v12, v13, v16, v17, 0\n"
        "s_mov_b32 vcc_lo, 16\n"
        "G1SG 1, 1, 1, v12, v13, v16, v17, 40960\n"
        "G1SG 2, 1, 2, v12, v13, v16, v17, 40960\n"
        "s_cbranch_scc1 .Lg1a%=\n"
        "s_barrier\n"
        G1_T(88)
        "s_barrier\n"
        G1_T(92)
        "G1RDA v12, v13, 40960\n"
        "s_waitcnt lgkmcnt(4)\n"
        "s_branch .Lg1b%=\n"
        ".Lg1a%=:\n"
        G1_T(88)
        "s_barrier\n"
        G1_T(92)
        "G1RDB 0, v16, v17, 40960\n G1RDA v12, v13, 40960\n G1RDB 1, v16, v17, 40960\n"
        ".p2align 3\n s_nop 0\n"
        ".Lg1b%=:\n"
        "s_waitcnt lgkmcnt(8)\n"
        "G1SG 0, 0, 1, v12, v13, v16, v17, 40960\n"
        "s_barrier\n"
        "G1SG 1, 0, 1, v14, v15, v18, v19, 0\n"
        "G1SG 2, 0, 1, v14, v15, v18, v19, 0\n"
        "s_lshr_b32 vcc_lo, vcc_lo, 1\n s_waitcnt vmcnt(0) lgkmcnt(8)\n"
        "G1SG 0, 0, 1, v14, v15, v18, v19, 0\n"
        "s_barrier\n"
        "G1SG 1, 0, 1, v12, v13, v16, v17, 0\n"
        "G1SG 2, 0, 1, v12, v13, v16, v17, 0\n"
        "s_cbranch_scc0 .Lg1c%=\n s_waitcnt lgkmcnt(8)\n"
        "G1SG 0, 0, 1, v12, v13, v16, v17, 0\n"
        "s_barrier\n"
        "G1SG 1, 0, 1, v12, v13, v16, v17, 40960\n"
        "G1SG 2, 0, 1, v12, v13, v16, v17, 40960\n"
        "s_branch .Lg1b%=\n"
        ".Lg1c%=:\n"
        "s_waitcnt lgkmcnt(8)\n"
        "G1SG 0, 0, 1, v12, v13, v16, v17, 0\n s_bitcmp1_b32 vcc_hi, 0\n"
        G1_T(96)
        "s_cbranch_scc1 .Lg1n\n s_barrier\n"
        G1_SLIDE
        ".endif\n .if %[hh]\n .p2align 3\n s_nop 0\n .Lg1n:\n s_barrier\n"
        G1_HEDGE
        ".endif\n"
        G1_TEND G1_PURGE
        : "=&{v[160:191]}"(ac0), "=&{v[192:223]}"(ac1), "=&{v[224:255]}"(ac2), "=&{v[64:79]}"(xq),
          "=&{v[88:91]}"(csq[1]), "=&{v[148:151]}"(csq[0]), "=&{v[140:143]}"(st01), "=&{v[136:139]}"(st23),
          "+{v[48:63]}"(pa), "+{v[36:39]}"(bias[0]), "+{v[40:43]}"(bias[1]), "+{v[44:47]}"(bias[2]) G1_OUT
        : "{v12}"(aK0), "{v13}"(aK1), "{v14}"(aK0 + 2 * STAGE), "{v15}"(aK1 + 2 * STAGE), "{v16}"(bo0), "{v17}"(bo1),
          "{v18}"(bo0 + 2 * STAGE), "{v19}"(bo1 + 2 * STAGE), "{v20}"(wn ? aK1 : aK0),
          [lay] "s"(layer), [hh] "n"(MODE), [rs] "s"(rsW), [wm] "s"(wm)
        : "memory", "scc", "vcc", "m0", G1_TCL G1_CLOBBER);
    acc[0][0] = quad<0>(ac0); acc[0][1] = quad<1>(ac0); acc[1][0] = quad<6>(ac0); acc[1][1] = quad<7>(ac0);
    asm volatile("" : "=s"(kol), "=s"(wos), "=s"(tO));
    wf[0] = __builtin_bit_cast(bf16x8, bias[0]); wf[1] = __builtin_bit_cast(bf16x8, bias[1]);
    qh[0][0] = quad<4>(ac1); qh[0][1] = quad<5>(ac1); qh[1][0] = quad<2>(ac2); qh[1][1] = quad<3>(ac2);
  }
  ST(layer * 24 + 4)
  {
  const int lane = lane_id(), tid = w * 64 + lane, fr = lane & 15, g = lane >> 4;
  (void)fr; (void)g;
  if constexpr (HH) {
    const f32x4 z4 = {0, 0, 0, 0};
    constexpr float L2E = 1.4426950408889634f;
    const unsigned kvo = WS_KV + (layer >> 1) * KVLB, kfo = kvo + 16u * NH * KVPB + hp * KVFB;
    auto phi2 = [&](const f32x4 (&a)[2][2], f32x4 st, bf16x8 (&qp)[2][2]) {
      f32x4 pm[2][2];
      f32x2 f[2][4];
      asm volatile("" : "+v"(st));
#pragma unroll
      for (int i = 0; i < 8; ++i) {
        const int rb = i >> 2, c = (i >> 1) & 1, e = (i & 1) * 2;
        f[rb][i & 3] = __builtin_elementwise_fma(f32x2{csq[c][e], csq[c][e + 1]}, f32x2{st[2 * rb], st[2 * rb]}, f32x2{a[rb][c][e], a[rb][c][e + 1]});
      }
#pragma unroll
      for (int rb = 0; rb < 2; ++rb) asm volatile("" : "+v"(f[rb][0]), "+v"(f[rb][1]), "+v"(f[rb][2]), "+v"(f[rb][3]));
#pragma unroll
      for (int rb = 0; rb < 2; ++rb) {
        const bf16x8 xf = __builtin_bit_cast(bf16x8, u32x4{pk2(f[rb][0][0], f[rb][0][1]), pk2(f[rb][1][0], f[rb][1][1]), pk2(f[rb][2][0], f[rb][2][1]), pk2(f[rb][3][0], f[rb][3][1])});
        pm[rb][0] = MFMA(wf[0], xf, z4); pm[rb][1] = MFMA(wf[1], xf, z4);
      }
      asm volatile("s_nop 7" : "+v"(pm[0][0]), "+v"(pm[0][1]), "+v"(pm[1][0]), "+v"(pm[1][1]));
      float mx[2], mn[2];
#pragma unroll
      for (int u = 0; u < 2; ++u) max8pn(pm[u][0], pm[u][1], mx[u], mn[u]);
      xr4<true>(mx[0], mn[0], mx[1], mn[1]);
      float ep[2][8], en[2][8], sp[2], sn[2];
#pragma unroll
      for (int u = 0; u < 2; ++u) {
        const float c = st[2 * u + 1] * L2E, mxs = -mx[u] * c, mns = -mn[u] * c;
#pragma unroll
        for (int i = 0; i < 4; ++i) {
          const f32x2 p2 = {pm[u][i >> 1][(i & 1) * 2], pm[u][i >> 1][(i & 1) * 2 + 1]};
          const f32x2 tp = __builtin_elementwise_fma(p2, f32x2{c, c}, f32x2{mxs, mxs});
          const f32x2 tn = __builtin_elementwise_fma(-p2, f32x2{c, c}, f32x2{mns, mns});
          ep[u][2 * i] = __builtin_amdgcn_exp2f(tp[0]); ep[u][2 * i + 1] = __builtin_amdgcn_exp2f(tp[1]);
          en[u][2 * i] = __builtin_amdgcn_exp2f(tn[0]); en[u][2 * i + 1] = __builtin_amdgcn_exp2f(tn[1]);
        }
        const f32x2 a2 = (f32x2{ep[u][0], ep[u][1]} + f32x2{ep[u][2], ep[u][3]}) + (f32x2{ep[u][4], ep[u][5]} + f32x2{ep[u][6], ep[u][7]});
        const f32x2 b2 = (f32x2{en[u][0], en[u][1]} + f32x2{en[u][2], en[u][3]}) + (f32x2{en[u][4], en[u][5]} + f32x2{en[u][6], en[u][7]});
        sp[u] = a2[0] + a2[1]; sn[u] = b2[0] + b2[1];
      }
      xr4<false>(sp[0], sn[0], sp[1], sn[1]);
#pragma unroll
      for (int rb = 0; rb < 2; ++rb) {
        const float ip = __builtin_amdgcn_rcpf(sp[rb]), in = __builtin_amdgcn_rcpf(sn[rb]);
        qp[rb][0] = __builtin_bit_cast(bf16x8, u32x4{pk2(ep[rb][0] * ip, ep[rb][1] * ip), pk2(ep[rb][2] * ip, ep[rb][3] * ip), pk2(ep[rb][4] * ip, ep[rb][5] * ip), pk2(ep[rb][6] * ip, ep[rb][7] * ip)});
        qp[rb][1] = __builtin_bit_cast(bf16x8, u32x4{pk2(en[rb][0] * in, en[rb][1] * in), pk2(en[rb][2] * in, en[rb][3] * in), pk2(en[rb][4] * in, en[rb][5] * in), pk2(en[rb][6] * in, en[rb][7] * in)});
        asm volatile("" : "+v"(qp[rb][0]), "+v"(qp[rb][1]));
      }
    };
    bf16x8 one8 = ones8();
    asm volatile("" : "+v"(one8));
    WAITL0; BAR;
    ST(layer * 24 + 5)
    {
      bf16x8 af[4], v0[4], v1[4];
      asm volatile("" ::: "memory");
#pragma unroll
      for (int sw = 0; sw < 4; ++sw) {
        af[sw] = cat8(tr_read(ppa + sw * (64 * PSTR)), tr_read(ppa + sw * (64 * PSTR) + 16 * PSTR));
        v0[sw] = lds<bf16x8>(pvr + sw * 4096);
        v1[sw] = lds<bf16x8>(pvr + sw * 4096 + 1024);
      }
      __builtin_amdgcn_sched_barrier(0);
      f32x4 kv[3] = {z4, z4, z4};
#pragma unroll
      for (int sw = 0; sw < 4; ++sw) {
        kv[0] = MFMA(af[sw], v0[sw], kv[0]);
        kv[1] = MFMA(af[sw], v1[sw], kv[1]);
        kv[2] = MFMA(af[sw], one8, kv[2]);
      }
      auto pc = [](float x, float y) { const unsigned t = pk2(x, y); return t < EMPTY - 1u ? t : EMPTY - 1u; };
      __builtin_amdgcn_raw_buffer_store_b128(u32x4{pc(kv[0][0], kv[0][1]), pc(kv[0][2], kv[0][3]), pc(kv[1][0], kv[1][1]), pc(kv[1][2], kv[1][3])}, rsW, gkv, 0, 16);
      __builtin_amdgcn_raw_buffer_store_b64(u32x2{pc(kv[2][0], kv[2][1]), pc(kv[2][2], kv[2][3])}, rsW, gkz, 0, 16);
    }
    auto wo_late = [&](int t) { glds2v(wos + knib(kol, t) * tO, pwl, pwm, sb + OFF_WO + t * 8192 + (w & 3) * 2048); };
    ST(layer * 24 + 6)
    u32x4 l1[8];
    const bool red = w == 7 || (w == 6 && (rt & 7) == 0);
    unsigned lo = kvo + (unsigned)((2 * hp + (rt >> 3) + (lane >> 5) * 8 * NH) * KVPB) +
                  (w == 7 ? (unsigned)((rt & 4) * 512 + (g & 1) * 1024 + (rt & 3) * 256 + fr * 16) : (unsigned)(4096 + (lane & 7) * 16));
    unsigned fs = w == 7 ? kfo + (unsigned)((rt >> 2) * 2048 + (lane >> 5) * 1024 + (rt & 3) * 256 + fr * 16 + (g & 1) * 8)
                : (lane & 24) ? 0x7fff0000u : kfo + (unsigned)(8192 + (rt >> 3) * 128 + (lane & 4) * 16 + (lane & 1) * 32 + (lane >> 5) * 16 + (lane & 2) * 4);
    auto l1_load = [&]() {
      asm volatile("" ::: "memory");
#pragma unroll
      for (int p = 0; p < 8; ++p) asm volatile("buffer_load_dwordx4 %0, %1, %2, %3 offen sc1" : "=v"(l1[p]) : "v"(lo), "s"(rsW), "s"(p * (NH * KVPB)) : "memory");
      __builtin_amdgcn_sched_barrier(0);
    };
    if (red) asm volatile("" : "+v"(lo), "+v"(fs));
    if (red) { __builtin_amdgcn_s_sleep(13); l1_load(); }
    bf16x8 qp2[2][2];
    if (!kg) {
      phi2(qh, st23, qp2);
    } else if (red) {
      typedef __attribute__((ext_vector_type(2))) __bf16 bf2;
      unsigned c0 = 0x00003F80u, c1 = 0x3F800000u;
      asm volatile("" : "+v"(c0), "+v"(c1));
      float s4[4];
      _Pragma("unroll 1") for (unsigned spins = 0; spins < (1u << 18); ++spins) {
        float a[8] = {zero1(), zero1(), zero1(), zero1(), zero1(), zero1(), zero1(), zero1()};
        ARRIVED("+v"(l1[0]), "+v"(l1[1]), "+v"(l1[2]), "+v"(l1[3]), "+v"(l1[4]), "+v"(l1[5]), "+v"(l1[6]), "+v"(l1[7]));
#pragma unroll
        for (int p = 0; p < 8; ++p)
#pragma unroll
          for (int e = 0; e < 4; ++e) {
            const unsigned u = l1[p][e];
            a[2 * e] = __builtin_amdgcn_fdot2_f32_bf16(__builtin_bit_cast(bf2, u), __builtin_bit_cast(bf2, c0), a[2 * e], false);
            a[2 * e + 1] = __builtin_amdgcn_fdot2_f32_bf16(__builtin_bit_cast(bf2, u), __builtin_bit_cast(bf2, c1), a[2 * e + 1], false);
          }
#pragma unroll
        for (int i = 0; i < 4; ++i) {
          auto r = __builtin_amdgcn_permlane32_swap(__float_as_uint(a[i]), __float_as_uint(a[i + 4]), false, false);
          s4[i] = __uint_as_float(r[0]) + __uint_as_float(r[1]);
        }
        const float t = s4[0] + s4[2];
        if (__all(t == t)) break;
        unsigned mxw = 0;
#pragma unroll
        for (int p = 0; p < 8; ++p)
#pragma unroll
          for (int e = 0; e < 4; ++e) mxw = l1[p][e] > mxw ? l1[p][e] : mxw;
        if (__all(mxw != EMPTY)) break;
        l1_load();
      }
      WAITV(0);
      unsigned o0 = pk2(s4[0], s4[1]), o1 = pk2(s4[2], s4[3]);
      o0 = o0 < EMPTY - 1u ? o0 : EMPTY - 1u; o1 = o1 < EMPTY - 1u ? o1 : EMPTY - 1u;
      __builtin_amdgcn_raw_buffer_store_b64(u32x2{o0, o1}, rsW, fs, 0, 16);
    }
    ST(layer * 24 + 8)
    WAITL0;
    BAR;
    ST(layer * 24 + 9)
    if (kg) {
#pragma unroll
      for (int i = 6; i < 12; ++i) wo_late(i);
    } else {
      unsigned fo = kfo + (unsigned)(wn * 4096 + lane * 16), fz = kfo + (unsigned)(8192 + wn * 128 + g * 16);
      asm volatile("" : "+v"(fo), "+v"(fz));
      bf16x8 qp[2][2];
      phi2(acc, st01, qp);
      asm volatile(G1_OUTPUT
          :: "{v[224:227]}"(qp[0][0]), "{v[228:231]}"(qp[0][1]), "{v[232:235]}"(qp[1][0]), "{v[236:239]}"(qp[1][1]),
             "{v[240:243]}"(qp2[0][0]), "{v[244:247]}"(qp2[0][1]), "{v[248:251]}"(qp2[1][0]), "{v[252:255]}"(qp2[1][1]),
             [fo] "v"(fo), [fz] "v"(fz), [go] "v"(ogo), [lo] "v"(opo), [rs] "s"(rsW)
          : "memory", "vcc", "scc", "m0", "v152", "v153", "v154", "v155", "v156", "v157", "v158", "v159", "v160", "v161", "v162", "v163", "v164", "v165", "v166", "v167", "v168", "v169", "v170", "v171", "v172", "v173", "v174", "v175", "v176", "v177", "v178", "v179", "v180", "v181", "v182", "v183", "v184", "v185", "v186", "v187", "v188", "v189", "v190", "v191", "v192", "v193", "v194", "v195", "v196", "v197", "v198", "v199", "v200", "v201", "v202", "v203", "v204", "v205", "v206", "v207", "v208", "v209", "v210", "v211", "v212", "v213", "v214", "v215", "v216", "v217", "v218", "v219", "v220", "v221", "v222", "v223");
    }
  }
  ST(layer * 24 + 12)
  if (first && w >= 4 && w < 7) {
    typedef __attribute__((ext_vector_type(2))) __bf16 bf2;
    unsigned c0 = 0x00003F80u, c1 = 0x3F800000u;
    asm volatile("" : "+v"(c0), "+v"(c1));
    const bf2 lo1 = __builtin_bit_cast(bf2, c0), hi1 = __builtin_bit_cast(bf2, c1);
    float a[8] = {zero1(), zero1(), zero1(), zero1(), zero1(), zero1(), zero1(), zero1()};
#pragma unroll
    for (int i = 0; i < 8; ++i) {
      asm volatile("" : "+v"(pw[i]));
#pragma unroll
      for (int e = 0; e < 4; ++e) {
        const unsigned u = pw[i][e];
        a[2 * e] = __builtin_amdgcn_fdot2_f32_bf16(__builtin_bit_cast(bf2, u), lo1, a[2 * e], false);
        a[2 * e + 1] = __builtin_amdgcn_fdot2_f32_bf16(__builtin_bit_cast(bf2, u), hi1, a[2 * e + 1], false);
      }
    }
#pragma unroll
    for (int i = 0; i < 4; ++i) {
      auto r = __builtin_amdgcn_permlane32_swap(__float_as_uint(a[i]), __float_as_uint(a[i + 4]), false, false);
      a[i] = __uint_as_float(r[0]) + __uint_as_float(r[1]);
    }
#pragma unroll
    for (int i = 0; i < 2; ++i) {
      auto r = __builtin_amdgcn_permlane16_swap(__float_as_uint(a[i]), __float_as_uint(a[i + 2]), false, false);
      a[i] = __uint_as_float(r[0]) + __uint_as_float(r[1]);
      a[i] += dpp_ror<8>(a[i]);
    }
    if (!(lane & 8))
      __builtin_amdgcn_raw_buffer_store_b64(u32x2{__float_as_uint(a[0]), __float_as_uint(a[1])}, rsW,
          (unsigned)(WS_CSP + ((((hp + (1 << (w - 4))) & 15) * 16 + rt) * 192 + (w - 4) * 64 + (lane & 7) * 8 + (lane >> 5) * 4 + ((lane >> 4) & 1) * 2) * 4), 0, 16);
  }
  }
}

#undef pcs
#undef prs
#undef opo
#undef ogo
#undef hof
#undef dm
#undef dm2
#undef pcv
#undef pwf
#undef prv
#undef pvf
#undef ppk
#undef ppa
#undef pvr
#undef gkv
#undef gkz
#undef pwl
#undef pwm
#undef plb
#undef prd
#undef dm3

__device__ __forceinline__ void g2_phase(char* smem, char* ws, u64 E, int w, f32x16& xq, Next& nx, int layer, const u16* __restrict__ x0,
                                         const u16* __restrict__ wo, const u16* __restrict__ wnx, u16* __restrict__ out) {
  IDS
  wnx = uni(wnx);
  const unsigned sb = lds_addr(smem);
  const int lw = w & 3, k2 = 16 * lw + (lane >> 3);
  unsigned pxr, rmo;
  auto ends = [&]() {
    const int lane = lane_id(), r0 = w * 16 + (lane >> 3);
    pxr = sb + r0 * 128 + (lane & 7) * 16;
    rmo = (unsigned)(((m0 + r0) * DM + hp * 64 + ((lane & 7) ^ (lane >> 3)) * 8) * 2);
    asm volatile("" : "+v"(pxr), "+v"(rmo));
  };
  constexpr int S0 = 0, S1 = 16384, S2 = 32768, S3 = 147456, S4 = OFF_WO, S5 = 98304, S6 = 114688;
  constexpr int AS[16] = {S0, S1, S2, S3, S0, S1, S4, S2, S3, S0, S1, S4, S5, S3, S0, S6};
  constexpr int OFF_C = 0;
  auto bs = [](int t) { return OFF_WO + (t < 12 ? t : t - 10) * 8192; };
  const char *xa1, *xa2, *wq2;
  u64 fm;
  unsigned vB0, vB1, vXA, po, pst;
  if (w >= 4) {
    constexpr int WT[15] = {0, 8, 12, 14, 16, 18, 20, 18, 16, 14, 12, 12, 10, 10, 8};
    const char* o = ws + WS_O + layer * (SEQ * DM * 2) + rt * 262144;
    const unsigned vA = (unsigned)(lw * 4096 + lane * 16);
    u64 kord = korder(bid & 3, hp);
    auto ldA = [&](int s) { glds4c(o + knib(kord, s) * 16384, vA, sb + AS[s] + lw * 4096); };
    const bool im = img(layer, hp), in = img(layer + 1, hp);
    unsigned vW0 = im ? (unsigned)(lw * 2048 + lane * 16) : (unsigned)((k2 * DM + hp * 64 + ((lane & 7) ^ ((lane >> 4) << 1)) * 8) * 2), vW1 = vW0 + (im ? 0u : 8 * DM * 2 - 1024u);
    const char *wos = wo_src(ws, wo, layer, hp), *wqs = wq_src(ws, wnx, layer + 1, hp);
    unsigned tO = im ? 8192u : 131072u;
    asm volatile("" : "+v"(vW0), "+v"(vW1));
    asm volatile("" : "+s"(wos), "+s"(tO));
    auto ldW = [&](int t) { glds2v(wos + knib(kord, t) * tO, vW0, vW1, sb + bs(t) + lw * 2048); };
    const int fB = (k2 >> 1) & 1;
    vB0 = in ? (unsigned)(lw * 6144 + lane * 16) : (unsigned)((k2 * 3072 + hp * 64 + (((((lane & 7) >> 1) ^ fB) << 1) | (lane & 1)) * 8) * 2);
    vB1 = in ? vB0 : (unsigned)(((k2 + 8) * 3072 + hp * 64 + (((((lane & 7) >> 1) ^ (fB | 2)) << 1) | (lane & 1)) * 8) * 2) - 1024u;
    const u64 kn = nx.kord = korder(kv1(bid), hp);
    nx.ko2 = kord; nx.wq = wqs; nx.tw = in ? 24576u : 393216u;
    const char *wq0 = wqs + hp * (in ? 24576u : 393216u), *wq1 = wqs + knib(kn, 1) * (in ? 24576u : 393216u);
    wq2 = wqs + knib(kn, 2) * (in ? 24576u : 393216u);
    xa1 = ws + WS_X + layer * (SEQ * DM * 2) + knib(kn, 1) * 16384; xa2 = ws + WS_X + layer * (SEQ * DM * 2) + knib(kn, 2) * 16384;
    vXA = (unsigned)(rt * 262144 + lw * 4096 + lane * 16);
    fm = kfirst(kn);
    po = ((fm >> lane) & 1) ? WS_FLAGS + (unsigned)(256 + rt * 16 + lane) * FLS : 0u;
    pst = g != q4 ? 0x7fff0000u : WS_ST + layer * (SEQ * 128) + (unsigned)(((rt * 16 + hp) * 128 + lw * 32 + fr) * 8);
    asm volatile("" : "+v"(vB0), "+v"(vB1), "+v"(vXA), "+v"(po), "+v"(pst));
    asm volatile("" : "+s"(xa1), "+s"(xa2), "+s"(wq2), "+s"(fm), "+s"(nx.kord), "+s"(nx.ko2), "+s"(nx.wq), "+s"(nx.tw));
    asm volatile("" : "+s"(wq0), "+s"(wq1));
    auto ldB = [&](int s, int part) {
      const char* b = s ? wq1 : wq0;
      if (part) glds4v(b, vB0, vB1, vB0, vB1, sb + s * 40960 + 16384 + lw * 6144);
      else glds2v(b + 4096, vB0, vB1, sb + s * 40960 + 16384 + lw * 6144 + 4096);
    };
    ends();
    const char* o1 = o + knib(kord, 1) * 16384;
    asm volatile("" : "+s"(o1), "+s"(kord), "+s"(o));
    asm volatile("" :: "v"(vA));
    if (layer) { BAR;   }
    BAR;
    glds4c(o1, vA, sb + AS[1] + lw * 4096); __builtin_amdgcn_s_sleep(3); ldA(2); __builtin_amdgcn_s_sleep(3); ldA(3); __builtin_amdgcn_s_sleep(3); ldA(4);
    WAITV(12);
    BAR;
    auto step = [&](auto ktc) {
      constexpr int kt = decltype(ktc)::value;
      WAITVN(WT[kt]);
      BAR;
      if constexpr (kt == 1) { ldA(5); ldA(6); }
      if constexpr (kt >= 2 && kt <= 10) ldA(kt + 5);
      if constexpr (kt >= 2 && kt <= 5) ldW(kt + 10);
      if constexpr (kt >= 11) ldB((kt - 11) >> 1, (kt - 11) & 1);
      if constexpr (kt >= 6) { __builtin_amdgcn_s_sleep(kt >= 11 ? 3 : 2); if (layer == 1) __builtin_amdgcn_s_sleep(1); }
    };
    step(IC<1>{}); step(IC<2>{}); step(IC<3>{}); step(IC<4>{}); step(IC<5>{}); step(IC<6>{}); step(IC<7>{});
    step(IC<8>{}); step(IC<9>{}); step(IC<10>{}); step(IC<11>{}); step(IC<12>{}); step(IC<13>{}); step(IC<14>{});
  } else {
    asm volatile("" : "=v"(vB0), "=v"(vB1), "=v"(vXA), "=v"(po), "=v"(pst));
    asm volatile("" : "=s"(xa1), "=s"(xa2), "=s"(wq2), "=s"(fm), "=s"(nx.kord), "=s"(nx.ko2), "=s"(nx.wq), "=s"(nx.tw));
    const int wm = w >> 1, wn = w & 1;
    f32x4 acc[4][2];
    const int fl = (q4 >> 1) | ((g & 1) << 1);
    const int bb0 = (4 * g + q4) * 128 + p4 * 8 + (((wn * 2) ^ fl) << 5), bb1 = (4 * g + q4) * 128 + p4 * 8 + (((wn * 2 + 1) ^ fl) << 5);
    const int abase = wm * 4096 + lane * 16;
    bf16x8 fa[2][2][4], fb[2][2][2];
    unsigned ra[3], rb0[2], rb1[2];
#pragma unroll
    for (int i = 0; i < 3; ++i) { ra[i] = abase + i * 65536; asm volatile("" : "+v"(ra[i])); }
#pragma unroll
    for (int i = 0; i < 2; ++i) { rb0[i] = bb0 + OFF_WO + i * 49152; rb1[i] = bb1 + OFF_WO + i * 49152; asm volatile("" : "+v"(rb0[i]), "+v"(rb1[i])); }
    auto loadf = [&](int kt, bf16x8 (&a)[2][4], bf16x8 (&b)[2][2]) {
#pragma unroll
      for (int s = 0; s < 2; ++s) {
        const char* sA = smem + ra[AS[kt] >> 16] + (AS[kt] & 65535) + s * 8192;
        const int hi = bs(kt) >= OFF_WO + 49152, ob = bs(kt) - OFF_WO - hi * 49152 + s * 4096;
        b[s][0] = cat8(tr_read(smem + rb0[hi] + ob), tr_read(smem + rb0[hi] + ob + 2048));
        b[s][1] = cat8(tr_read(smem + rb1[hi] + ob), tr_read(smem + rb1[hi] + ob + 2048));
#pragma unroll
        for (int rb = 0; rb < 4; ++rb) a[s][rb] = *(const bf16x8*)(sA + rb * 1024);
      }
    };
    auto mma = [&](bf16x8 (&a)[2][4], bf16x8 (&b)[2][2]) {
#pragma unroll
      for (int i = 0; i < 16; ++i) {
        const int rb = i >> 2, cb = ((i >> 1) ^ rb) & 1, s = ((i + 1) >> 1) & 1;
        acc[rb][cb] = MFMA(b[s][cb], a[s][rb], acc[rb][cb]);
      }
    };
    unsigned fo = lane < 16 && lane != hp ? WS_FLAGS + (unsigned)(rt * 16 + lane) * FLS : 0u;
    unsigned kf = nx.kf & 0xffffu, kf2 = nx.kf >> 16;
    unsigned jo = (unsigned)((((rt & 7) * 128 + (w - 1) * 64 + lane) * 3072 + (hp & 3) * 1024 + (hp | 3) * 64) * 2);
    unsigned fo1 = ((kf >> (lane & 31)) & 1) ? fo : 0u, fo2 = ((kf2 >> (lane & 31)) & 1) ? fo : 0u;
    fo = (((kf | kf2) >> (lane & 31)) & 1) ? 0u : fo;
    asm volatile("" : "+v"(fo), "+v"(fo1), "+v"(fo2));
    asm volatile("" : "+s"(kf), "+s"(kf2));
    publish(rsW, tid, rt * 16 + hp, E + (unsigned)(layer + 1));
    ST(layer * 24 + 13)
    u64 fl1;
    asm volatile("" : "=v"(fl1));
    if (w == 0) asm volatile("global_load_dwordx2 %0, %1, %2 sc1" : "+v"(fl1) : "v"(fo1), "s"(ws) : "memory");
    if (layer == 0) {
      const unsigned xo = (unsigned)(((m0 + wm * 64 + fr) * DM + hp * 64 + wn * 32 + g * 8) * 2);
      asm volatile("global_load_dwordx4 v[64:67], %1, %3\n\tglobal_load_dwordx4 v[68:71], %1, %4\n\tglobal_load_dwordx4 v[72:75], %2, %3\n\tglobal_load_dwordx4 v[76:79], %2, %4"
                   : "+{v[64:79]}"(xq) : "v"(xo), "v"(xo + 32 * DM * 2), "s"(x0), "s"(x0 + 16 * DM) : "memory");
    }
    loadf(0, fa[0], fb[0]);
    unsigned junk;
    asm volatile("" : "=v"(junk));
    if (layer < 2 && w == 3 && (bid >> 6) == 0) code_touch(junk, bid, lane);
    if (layer == 0 && (hp & 7) < 3 && (w == 1 || w == 2))
      asm volatile("global_load_dword %0, %1, %2" : "+v"(junk) : "v"(jo), "s"(wnx) : "memory");
    __builtin_amdgcn_sched_barrier(0);
    bf16x8 id[2];
#pragma unroll
    for (int cb = 0; cb < 2; ++cb) {
      const unsigned one = (g >> 1) == cb && (g & 1) == (fr >> 3) ? 0x3F80u << ((fr & 1) * 16) : 0u;
      u32x4 t;
#pragma unroll
      for (int j = 0; j < 4; ++j) t[j] = ((fr >> 1) & 3) == j ? one : 0u;
      id[cb] = __builtin_bit_cast(bf16x8, t);
      asm volatile("" : "+v"(id[cb]));
    }
    const int l2 = lane_id(), ch = (l2 >> 5) | ((l2 >> 3) & 2), f2 = l2 & 15;
    unsigned pl = (unsigned)(wm * 8192 + wn * 1024 + ch * 256 + f2 * 16);
    unsigned pg = layer < 3 ? WS_X + layer * (SEQ * DM * 2) + (unsigned)((rt * 16 + hp) * 16384) + pl : 0x7fff0000u;
    pl = sb + OFF_C + (layer < 3 ? pl : (unsigned)((wm * 64 + f2) * 128 + (((wn * 4 + ch) ^ (f2 & 7)) << 4)));
    asm volatile("" : "+v"(pl), "+v"(pg));
    ends();
#pragma unroll
    for (int rb = 0; rb < 4; ++rb) { acc[rb][0] = f32x4{0, 0, 0, 0}; acc[rb][1] = f32x4{0, 0, 0, 0}; }
    mma(fa[0], fb[0]);
    __builtin_amdgcn_sched_barrier(0);
    WAITL0;
    u64 miss = 0;
    if (w == 0) { ARRIVED("+v"(fl1)); if (__ballot(fl1 != E + (unsigned)(layer + 1)) & kf) poll_eq(ws, fo1, E + (unsigned)(layer + 1), kf, kf); miss = 0xffffu & ~(kf | kf2 | (1u << hp)); }
    ST(layer * 24 + 14)
    __builtin_amdgcn_sched_barrier(0);
    BAR;
    __builtin_amdgcn_sched_barrier(0);
    asm volatile("" ::: "memory");
    if (layer == 0) WAITV(0);
    __builtin_amdgcn_sched_barrier(0);
    asm volatile("" : "+{v[64:79]}"(xq));
    const bf16x8 xf[4] = {__builtin_bit_cast(bf16x8, quad<0>(xq)), __builtin_bit_cast(bf16x8, quad<1>(xq)), __builtin_bit_cast(bf16x8, quad<2>(xq)), __builtin_bit_cast(bf16x8, quad<3>(xq))};
#pragma unroll
    for (int rb = 0; rb < 4; ++rb) { acc[rb][0] = MFMA(id[0], xf[rb], acc[rb][0]); acc[rb][1] = MFMA(id[1], xf[rb], acc[rb][1]); }
    if (w == 0) poll_eq(ws, fo2, E + (unsigned)(layer + 1), kf2, kf2);
    u64 fl2;
    asm volatile("" : "=v"(fl2));
    auto step = [&](auto ktc) {
      constexpr int kt = decltype(ktc)::value;
      __builtin_amdgcn_sched_barrier(0);
      if (kt == 5 && miss) {
        ARRIVED("+v"(fl2));
        if (__ballot(fl2 != E + (unsigned)(layer + 1)) & miss) poll_eq(ws, fo, E + (unsigned)(layer + 1), miss, ~0ull);
      }
      WAITL0;
      BAR;
      if (kt == 2 && miss) asm volatile("global_load_dwordx2 %0, %1, %2 sc1" : "+v"(fl2) : "v"(fo), "s"(ws) : "memory");
      if (kt == 0) { ST(layer * 24 + 15) }
      __builtin_amdgcn_sched_barrier(0);
      loadf(kt + 1, fa[(kt + 1) & 1], fb[(kt + 1) & 1]);
      if (kt == 0) return;
      mma(fa[kt & 1], fb[kt & 1]);
#pragma unroll
      for (int i = 0; i < 4; ++i) {
        __builtin_amdgcn_sched_group_barrier(0x008, 1, 0);
        __builtin_amdgcn_sched_group_barrier(0x100, 1, 0);
        __builtin_amdgcn_sched_group_barrier(0x008, 1, 0);
        __builtin_amdgcn_sched_group_barrier(0x100, 3, 0);
      }
#pragma unroll
      for (int i = 0; i < 8; ++i) __builtin_amdgcn_sched_group_barrier(0x008, 1, 0);
    };
    step(IC<0>{}); step(IC<1>{}); step(IC<2>{}); step(IC<3>{}); step(IC<4>{}); step(IC<5>{}); step(IC<6>{}); step(IC<7>{});
    step(IC<8>{}); step(IC<9>{}); step(IC<10>{}); step(IC<11>{}); step(IC<12>{}); step(IC<13>{}); step(IC<14>{});
    WAITV(0);
    asm volatile("" :: "v"(junk));
    __builtin_amdgcn_sched_barrier(0);
    u32x4 xv[4];
    auto put = [&](int rb) {
      auto a = __builtin_amdgcn_permlane16_swap(pk2(acc[rb][0][0], acc[rb][0][1]), pk2(acc[rb][1][0], acc[rb][1][1]), false, false);
      auto b = __builtin_amdgcn_permlane16_swap(pk2(acc[rb][0][2], acc[rb][0][3]), pk2(acc[rb][1][2], acc[rb][1][3]), false, false);
      xv[rb] = u32x4{a[0], b[0], a[1], b[1]};
      __builtin_amdgcn_raw_buffer_store_b128(xv[rb], rsW, pg + (rb & 1) * 2048, (rb >> 1) * 4096, 16);
    };
#pragma unroll
    for (int rb = 0; rb < 4; ++rb)
#pragma unroll
      for (int cb = 0; cb < 2; ++cb) acc[rb][cb] = MFMA(fb[1][0][cb], fa[1][0][rb], acc[rb][cb]);
#pragma unroll
    for (int rb = 0; rb < 4; ++rb) {
      __builtin_amdgcn_sched_barrier(0);
#pragma unroll
      for (int cb = 0; cb < 2; ++cb) acc[rb][cb] = MFMA(fb[1][1][cb], fa[1][1][rb], acc[rb][cb]);
      __builtin_amdgcn_sched_barrier(0);
      if (rb >= 2) put(rb - 2);
    }
    __builtin_amdgcn_sched_barrier(0);
    ST(layer * 24 + 16)
    put(2); put(3);
    __builtin_amdgcn_sched_barrier(0);
#pragma unroll
    for (int rb = 0; rb < 4; ++rb) lds<u32x4>(pl + rb * 2048) = xv[rb];
  }
  if (layer == 3) {
    WAITL0; BAR;
    asm volatile("" ::: "memory");
    const __amdgpu_buffer_rsrc_t rsO = __builtin_amdgcn_make_buffer_rsrc((void*)uni(out), (short)0, SEQ * DM * 2, 0x00020000);
    __builtin_amdgcn_raw_buffer_store_b128(lds<u32x4>(pxr + OFF_C), rsO, rmo, 0, 16);
    __builtin_amdgcn_raw_buffer_store_b128(lds<u32x4>(pxr + OFF_C + 1024), rsO, rmo, 8 * DM * 2, 16);
  }
  ST(layer * 24 + 17)
  if (layer < 3 && w >= 4) {
    WAITV(0); BAR;
    asm volatile("" ::: "memory");
    glds4v(wq2, vB0, vB1, vB0, vB1, sb + 2 * 40960 + 16384 + lw * 6144);
    glds2v(wq2 + 4096, vB0, vB1, sb + 2 * 40960 + 16384 + lw * 6144 + 4096);
    __builtin_amdgcn_s_setprio(3);
    bf16x8 xn[4];
#pragma unroll
    for (int i = 0; i < 4; ++i) xn[i] = lds<bf16x8>(sb + OFF_C + lw * 4096 + lane * 16 + i * 1024);
    const bf16x8 ones = ones8();
    u32x2 h[2];
#pragma unroll
    for (int i = 0; i < 2; ++i) {
      const f32x4 z4 = {0, 0, 0, 0};
      const f32x4 sa = MFMA(ones, xn[2 * i + 1], MFMA(ones, xn[2 * i], z4)), sq = MFMA(xn[2 * i + 1], xn[2 * i + 1], MFMA(xn[2 * i], xn[2 * i], z4));
      const float a = (lane & 1) ? sq[1] : sq[0], b = (lane & 1) ? sq[3] : sq[2];
      h[i] = u32x2{__float_as_uint(sa[0]), __float_as_uint((lane & 2) ? b : a)};
    }
    __builtin_amdgcn_s_setprio(0);
    u64 fl;
    asm volatile("" : "=v"(fl), "+v"(pst) : "v"(h[0]), "v"(h[1]));
    if (lw == 0) asm volatile("global_load_dwordx2 %0, %1, %2 sc1" : "+v"(fl) : "v"(po), "s"(ws) : "memory");
    __builtin_amdgcn_raw_buffer_store_b64(h[0], rsW, pst, 0, 16);
    __builtin_amdgcn_raw_buffer_store_b64(h[1], rsW, pst + 128, 0, 16);
    if (lw == 0) {
      WAITV(2);
      __builtin_amdgcn_sched_barrier(0);
      asm volatile("" : "+v"(fl));
      if (__ballot(fl != E + (unsigned)(layer + 1)) & fm) poll_eq(ws, po, E + (unsigned)(layer + 1), fm, ~0ull);
    }
    BAR;
    glds4c(xa1, vXA, sb + 40960 + lw * 4096); glds4c(xa2, vXA, sb + 2 * 40960 + lw * 4096);
  }
  ST(layer * 24 + 18)
}

__global__ __launch_bounds__(512, 2) void mega_kernel(const u16* __restrict__ x0, const u16* __restrict__ wqkvA,
                                                      const u16* __restrict__ woA, const u16* __restrict__ wqkvB,
                                                      const u16* __restrict__ wphi, const u16* __restrict__ woB,
                                                      u16* __restrict__ out, char* __restrict__ ws) {
  extern __shared__ __attribute__((aligned(16))) char smem[];
  {
    unsigned long long pc;
    unsigned t;
    asm volatile("s_getpc_b64 %0\n.Lmk_pc:\n\tv_lshl_add_u32 %1, %2, 9, %3\n\tv_lshlrev_b32 %1, 2, %1\n\tv_and_b32 %1, 0xffffff80, %1\n\ts_mov_b32 m0, %4\n\t"
                 "v_min_u32 %1, .Lmk_end-.Lmk_pc-4, %1\n\tglobal_load_lds_dword %1, %0" : "=&s"(pc), "=&v"(t) : "s"(blockIdx.x >> 3), "v"(threadIdx.x), "n"(LDS_TOTAL - 1024) : "memory", "m0");
  }
  const int w = __builtin_amdgcn_readfirstlane(threadIdx.x >> 6);
  u64 E = 0;
  {
    const int bid = blockIdx.x, lane = threadIdx.x & 63;
    const int rt = 2 * (bid & 4) + ((bid >> 3) & 7), hp = 4 * (bid & 3) + (bid >> 6), m0 = rt * 128;
    if (w < 4) {
      const unsigned xo = (unsigned)((m0 + hp * 8 + w * 2) * (DM * 2) + lane * 16);
      glds4v(x0, xo, xo, xo, xo, lds_addr(smem) + 122880 + w * 4096);
    } else {
      const int lw = w & 3;
      const int kB = 16 * lw + (lane >> 3), fB = (kB >> 1) & 1;
      const unsigned vB0 = (unsigned)((kB * 3072 + hp * 64 + (((((lane & 7) >> 1) ^ fB) << 1) | (lane & 1)) * 8) * 2);
      const unsigned vB1 = (unsigned)(((kB + 8) * 3072 + hp * 64 + (((((lane & 7) >> 1) ^ (fB | 2)) << 1) | (lane & 1)) * 8) * 2) - 1024u;
      const unsigned vA = (unsigned)(((m0 + lw * 32 + (lane >> 3)) * DM + ((lane & 7) ^ (lane >> 3)) * 8) * 2);
#pragma unroll
      for (int s = 0; s < 2; ++s) {
        const int kt = (rt & 8) + s;
        const unsigned sA = lds_addr(smem) + s * 40960, sB = sA + 16384;
        glds4v(x0 + kt * 64, vA, vA + 15360u, vA + 2 * 15360u, vA + 3 * 15360u, sA + lw * 4096);
        const u16* b = wqkvA + (size_t)kt * 64 * 3072;
        glds4v(b, vB0, vB1, vB0, vB1, sB + lw * 6144);
        glds2v(b + 2048, vB0, vB1, sB + lw * 6144 + 4096);
      }
    }
  }
  asm volatile("" :: "s"(woA), "s"(wqkvB), "s"(wphi), "s"(woB), "s"(out), "s"(ws));
  f32x16 xq;
  Next nx;
  asm volatile("" : "=s"(nx.kord), "=s"(nx.ko2), "=s"(nx.wq), "=s"(nx.tw), "=s"(nx.kf));
#pragma unroll 1
  for (int layer = 0; layer < 4; ++layer) {
    if (layer & 1) g1_phase<1>(smem, ws, E, w, xq, nx, layer, x0, wqkvB, woB, wphi, wqkvA, woA);
    else g1_phase<0>(smem, ws, E, w, xq, nx, layer, x0, wqkvA, woA, nullptr, wqkvB, woB);
    g2_phase(smem, ws, E, w, xq, nx, layer, x0, (layer & 1) ? woB : woA, (layer & 1) ? wqkvA : wqkvB, out);
  }
  asm volatile(".Lmk_end:");
  WAITV(0);
  if (blockIdx.x == 0 && w == 0 && lane_id() == 0) __hip_atomic_store((gu64*)ws, E + 16u, __ATOMIC_RELAXED, __HIP_MEMORY_SCOPE_AGENT);
}

extern "C" void kernel_launch(void* const* d_in, const int* in_sizes, int n_in,
                              void* d_out, int out_size, void* d_ws, size_t ws_size,
                              hipStream_t stream) {
  hipFuncSetAttribute((const void*)mega_kernel, hipFuncAttributeMaxDynamicSharedMemorySize, LDS_TOTAL);
  mega_kernel<<<256, 512, LDS_TOTAL, stream>>>((const u16*)d_in[0], (const u16*)d_in[1], (const u16*)d_in[2], (const u16*)d_in[4],
                                               (const u16*)d_in[5], (const u16*)d_in[6], (u16*)d_out, (char*)d_ws);
}
```

```cpp
#include <hip/hip_runtime.h>
#include <hip/hip_bf16.h>
#include <stdint.h>
#include <type_traits>
#pragma clang diagnostic ignored "-Winline-asm"
#pragma clang diagnostic ignored "-Wunused-value"

typedef unsigned short u16;
typedef __attribute__((ext_vector_type(8))) short bf16x8;
typedef __attribute__((ext_vector_type(4))) short bf16x4;
typedef __attribute__((ext_vector_type(4))) float f32x4;
typedef __attribute__((ext_vector_type(4))) unsigned u32x4;
typedef __attribute__((ext_vector_type(2))) unsigned u32x2;
typedef __attribute__((ext_vector_type(8))) float f32x8;
typedef __attribute__((ext_vector_type(16))) unsigned u32x16;
typedef __attribute__((ext_vector_type(16))) float f32x16;
typedef __attribute__((ext_vector_type(32))) float f32x32;
template <int I, typename T> __device__ __forceinline__ f32x4 quad(const T& v) { return __builtin_shufflevector(v, v, 4 * I, 4 * I + 1, 4 * I + 2, 4 * I + 3); }

constexpr int SEQ = 2048, DM = 1024, NH = 32;
#define ST(i)
constexpr int KVP = 64 * 32 + 64;

typedef __attribute__((ext_vector_type(2))) float f32x2;
typedef __attribute__((ext_vector_type(2))) __bf16 bf16x2_t;
__device__ __forceinline__ unsigned pk2(float a, float b) {
  f32x2 v = {a, b};
  bf16x2_t r = __builtin_convertvector(v, bf16x2_t);
  return __builtin_bit_cast(unsigned, r);
}

__device__ __forceinline__ bf16x4 tr_read(const char* p) {
  return __builtin_amdgcn_ds_read_tr16_b64_v4i16((__attribute__((address_space(3))) bf16x4*)p);
}
template <typename T> __device__ __forceinline__ __attribute__((address_space(3))) T& lds(unsigned a) { return *(__attribute__((address_space(3))) T*)(size_t)a; }
__device__ __forceinline__ bf16x4 tr_read(unsigned a) { return __builtin_amdgcn_ds_read_tr16_b64_v4i16(&lds<bf16x4>(a)); }
__device__ __forceinline__ bf16x8 cat8(bf16x4 a, bf16x4 b) {
  bf16x8 f = {a[0], a[1], a[2], a[3], b[0], b[1], b[2], b[3]};
  return f;
}
__device__ __forceinline__ void glds16s(const void* sbase, unsigned voff, unsigned lds_base) {
  asm volatile("s_mov_b32 m0, %2\n\ts_nop 0\n\tglobal_load_lds_dwordx4 %0, %1" :: "v"(voff), "s"(sbase), "s"(lds_base) : "memory", "m0");
}
__device__ __forceinline__ void glds16c(const void* sbase, unsigned voff, unsigned lds_base) {
  asm volatile("s_mov_b32 m0, %2\n\ts_nop 0\n\tglobal_load_lds_dwordx4 %0, %1 sc1" :: "v"(voff), "s"(sbase), "s"(lds_base) : "memory", "m0");
}
#define GL_ "\n\tglobal_load_lds_dwordx4 "
__device__ __forceinline__ void glds4c(const void* sbase, unsigned v, unsigned lds_base) {
  asm volatile("s_mov_b32 m0, %2\n\ts_nop 0" GL_ "%0, %1 sc1" GL_ "%0, %1 offset:1024 sc1" GL_ "%0, %1 offset:2048 sc1" GL_ "%0, %1 offset:3072 sc1"
               :: "v"(v), "s"(sbase), "s"(lds_base) : "memory", "m0");
}
__device__ __forceinline__ void glds4v(const void* sbase, unsigned v0, unsigned v1, unsigned v2, unsigned v3, unsigned lds_base) {
  asm volatile("s_mov_b32 m0, %5\n\ts_nop 0" GL_ "%0, %4 sc1" GL_ "%1, %4 offset:1024 sc1" GL_ "%2, %4 offset:2048 sc1" GL_ "%3, %4 offset:3072 sc1"
               :: "v"(v0), "v"(v1), "v"(v2), "v"(v3), "s"(sbase), "s"(lds_base) : "memory", "m0");
}
__device__ __forceinline__ void glds2v(const void* sbase, unsigned v0, unsigned v1, unsigned lds_base) {
  asm volatile("s_mov_b32 m0, %3\n\ts_nop 0" GL_ "%0, %2 sc1" GL_ "%1, %2 offset:1024 sc1" :: "v"(v0), "v"(v1), "s"(sbase), "s"(lds_base) : "memory", "m0");
}
__device__ __forceinline__ void gld16(u32x4& d, unsigned voff, const void* sbase) {
  asm volatile("global_load_dwordx4 %0, %1, %2" : "=v"(d) : "v"(voff), "s"(sbase) : "memory");
}
__device__ __forceinline__ unsigned lds_addr(const void* p) { return (unsigned)(size_t)p; }
#define WAITV(n) asm volatile("s_waitcnt vmcnt(" #n ")" ::: "memory")
#define WAITVN(n) asm volatile("s_waitcnt vmcnt(%0)" :: "n"(n) : "memory")
#define WAITL0 do { asm volatile("" ::: "memory"); __builtin_amdgcn_s_waitcnt(0xC07F); asm volatile("" ::: "memory"); } while (0)
#define BAR __builtin_amdgcn_s_barrier()
#define ARRIVED(...) do { WAITV(0); __builtin_amdgcn_sched_barrier(0); asm volatile("" : __VA_ARGS__); } while (0)

template <int N> __device__ __forceinline__ float dpp_ror(float x) {
  return __uint_as_float(__builtin_amdgcn_update_dpp(0u, __float_as_uint(x), 0x120 + N, 0xf, 0xf, false));
}
__device__ __forceinline__ float r16_sum(float x) { x += dpp_ror<8>(x); x += dpp_ror<4>(x); x += dpp_ror<2>(x); return x + dpp_ror<1>(x); }
__device__ __forceinline__ void max8pn(const f32x4& a, const f32x4& b, float& mx, float& mn) {
  asm("v_max3_f32 %0, %2, %3, %4\n\tv_max3_f32 %1, -%2, -%3, -%4\n\tv_max3_f32 %0, %0, %5, %6\n\tv_max3_f32 %1, %1, -%5, -%6\n\t"
      "v_max3_f32 %0, %0, %7, %8\n\tv_max3_f32 %1, %1, -%7, -%8\n\tv_max_f32 %0, %0, %9\n\tv_max_f32 %1, %1, -%9\n\ts_nop 0"
      : "=&v"(mx), "=&v"(mn) : "v"(a[0]), "v"(a[1]), "v"(a[2]), "v"(a[3]), "v"(b[0]), "v"(b[1]), "v"(b[2]), "v"(b[3]));
}
__device__ __forceinline__ float vmaxf_n(float a, float b) { float r; asm("v_max_f32 %0, %1, %2\n\ts_nop 0" : "=v"(r) : "v"(a), "v"(b)); return r; }
template <bool MAXOP> __device__ __forceinline__ float rop(float a, float b) { return MAXOP ? vmaxf_n(a, b) : a + b; }
template <bool MAXOP> __device__ __forceinline__ void xr4(float& a, float& b, float& c, float& d) {
  auto r = __builtin_amdgcn_permlane16_swap(__float_as_uint(a), __float_as_uint(b), false, false);
  auto q = __builtin_amdgcn_permlane16_swap(__float_as_uint(c), __float_as_uint(d), false, false);
  const unsigned ab = __float_as_uint(rop<MAXOP>(__uint_as_float(r[0]), __uint_as_float(r[1])));
  const unsigned cd = __float_as_uint(rop<MAXOP>(__uint_as_float(q[0]), __uint_as_float(q[1])));
  auto t = __builtin_amdgcn_permlane32_swap(ab, cd, false, false);
  const unsigned u = __float_as_uint(rop<MAXOP>(__uint_as_float(t[0]), __uint_as_float(t[1])));
  auto h = __builtin_amdgcn_permlane32_swap(u, u, false, false);
  auto p0 = __builtin_amdgcn_permlane16_swap(h[0], h[0], false, false);
  auto p1 = __builtin_amdgcn_permlane16_swap(h[1], h[1], false, false);
  a = __uint_as_float(p0[0]); b = __uint_as_float(p0[1]); c = __uint_as_float(p1[0]); d = __uint_as_float(p1[1]);
}

__device__ __forceinline__ unsigned xs4(float a, float b, float c, float d) {
  auto r = __builtin_amdgcn_permlane16_swap(__float_as_uint(a), __float_as_uint(b), false, false);
  auto q = __builtin_amdgcn_permlane16_swap(__float_as_uint(c), __float_as_uint(d), false, false);
  auto t = __builtin_amdgcn_permlane32_swap(__float_as_uint(__uint_as_float(r[0]) + __uint_as_float(r[1])), __float_as_uint(__uint_as_float(q[0]) + __uint_as_float(q[1])), false, false);
  return __float_as_uint(__uint_as_float(t[0]) + __uint_as_float(t[1]));
}

__device__ __forceinline__ f32x4 sum4(const u32x4 (&v)[4]) {
  float r0, r1, r2, r3, t0, t1, t2, t3;
  asm("v_add_f32 %0, %8, %12\n\tv_add_f32 %1, %9, %13\n\tv_add_f32 %2, %10, %14\n\tv_add_f32 %3, %11, %15\n\t"
      "v_add_f32 %4, %16, %20\n\tv_add_f32 %5, %17, %21\n\tv_add_f32 %6, %18, %22\n\tv_add_f32 %7, %19, %23\n\t"
      "v_add_f32 %0, %0, %4\n\tv_add_f32 %1, %1, %5\n\tv_add_f32 %2, %2, %6\n\tv_add_f32 %3, %3, %7\n\ts_nop 0"
      : "=&v"(r0), "=&v"(r1), "=&v"(r2), "=&v"(r3), "=&v"(t0), "=&v"(t1), "=&v"(t2), "=&v"(t3)
      : "v"(v[0][0]), "v"(v[0][1]), "v"(v[0][2]), "v"(v[0][3]), "v"(v[1][0]), "v"(v[1][1]), "v"(v[1][2]), "v"(v[1][3]),
        "v"(v[2][0]), "v"(v[2][1]), "v"(v[2][2]), "v"(v[2][3]), "v"(v[3][0]), "v"(v[3][1]), "v"(v[3][2]), "v"(v[3][3]));
  return f32x4{r0, r1, r2, r3};
}
#define MFMA(a, b, c) __builtin_amdgcn_mfma_f32_16x16x32_bf16(a, b, c, 0, 0, 0)

typedef unsigned long long u64;
typedef __attribute__((address_space(1))) unsigned long long gu64;
constexpr unsigned WS_FLAGS = 4096;
constexpr int FLS = 64;
constexpr unsigned WS_G0 = 65536;
constexpr unsigned WS_CSP = WS_G0 + SEQ * 16;
constexpr unsigned WS_ST = WS_CSP + 16 * 16 * 768;
constexpr unsigned WS_X = 1u << 21;
constexpr unsigned WS_O = WS_X + 3u * SEQ * DM * 2;
constexpr unsigned KVPB = KVP * 2, KVFB = KVP * 4;
constexpr unsigned EMPTY = 0xffffffffu;
constexpr unsigned WS_KV = WS_O + 4u * SEQ * DM * 2;
constexpr unsigned KVLB = 16u * NH * KVPB + 16u * KVFB;
constexpr unsigned WS_HL = WS_KV + 2u * KVLB;
constexpr unsigned WS_CSA = WS_HL + 512u * 4096;
constexpr unsigned WS_CPA = WS_CSA + 256u * 1024;
constexpr unsigned WS_IMG = WS_CPA + 16 * 16 * 768;
constexpr unsigned WS_END = WS_IMG + 2048 * 1024;
constexpr int LDS_TOTAL = 163840;
constexpr int OFF_WO = 49152;
struct Next { u64 kord, ko2; const char* wq; unsigned tw, kf; };
template <int V> using IC = std::integral_constant<int, V>;
__device__ __forceinline__ bf16x8 ones8() { unsigned e = 0x3F803F80u; asm volatile("" : "+v"(e)); return __builtin_bit_cast(bf16x8, u32x4{e, e, e, e}); }
__device__ __forceinline__ float zero1() { float z; asm volatile("v_mov_b32 %0, 0" : "=v"(z)); return z; }
__device__ __forceinline__ u32x4 empty4() { unsigned e = EMPTY; asm volatile("" : "+v"(e)); return u32x4{e, e, e, e}; }
__device__ __forceinline__ u64 korder(int v, int own) {
  const u64 t = v == 0 ? 0x3b210fedca987654ull : v == 1 ? 0xb37654210fedca98ull : v == 2 ? 0x3ba987654210fedcull : 0xb3fedca987654210ull;
  const u64 inv = v == 0 ? 0xa987e6543210fdcbull : v == 1 ? 0x6543f210dcbae987ull : v == 2 ? 0x3210edcba987f654ull : 0xdcbaf9876543e210ull;
  const u64 low = (1ull << (4 * ((inv >> (4 * own)) & 15))) - 1ull;
  return (u64)own | ((t & low) << 4) | (t & ~((low << 4) | 15ull));
}
__device__ __forceinline__ u64 korder0(int rt) {
  const int r = rt & 7;
  const u64 own = (0x765432765432ull >> (4 * (r < 6 ? r : r - 6))) & 0xffffffull, oth = 0x7654321076543210ull >> (4 * r);
  const u64 t = 0x10ull | ((oth & 15ull) << 8) | (own << 12) | ((oth >> 4) << 36);
  return t | (rt < 8 ? 0x8888888000000800ull : 0x0000000888888088ull);
}
__device__ __forceinline__ int knib(u64 t, int s) { return (int)(t >> (4 * s)) & 15; }
__device__ __forceinline__ unsigned kfirst(u64 t) { return (1u << knib(t, 1)) | (1u << knib(t, 2)) | (1u << knib(t, 3)) | (1u << knib(t, 4)); }
__device__ __forceinline__ int kv1(int bid) { return (bid + (0x4924 >> ((bid >> 2) & 14))) & 3; }

__device__ __forceinline__ bool img(int layer, int hp) { return layer >= 2 && (hp & 7) == 3; }
__device__ __forceinline__ const char* wq_src(const char* ws, const void* W, int layer, int hp) { return img(layer, hp) ? ws + WS_IMG + ((layer & 1) * 768 + (hp >> 3) * 384) * 1024 : (const char*)W; }
__device__ __forceinline__ const char* wo_src(const char* ws, const void* W, int layer, int hp) { return img(layer, hp) ? ws + WS_IMG + (1536 + (layer & 1) * 256 + (hp >> 3) * 128) * 1024 : (const char*)W; }

template <typename T> __device__ __forceinline__ T* uni(T* p) {
  const unsigned long long v = (unsigned long long)p;
  const unsigned lo = __builtin_amdgcn_readfirstlane((unsigned)v), hi = __builtin_amdgcn_readfirstlane((unsigned)(v >> 32));
  return (T*)(((unsigned long long)hi << 32) | lo);
}
__device__ __forceinline__ int lane_id() {
  int l;
  asm volatile("v_mbcnt_lo_u32_b32 %0, -1, 0\n\tv_mbcnt_hi_u32_b32 %0, -1, %0" : "=v"(l));
  __builtin_assume(l >= 0 && l < 64);
  return l;
}
#define IDS \
  const int lane = lane_id(); \
  asm volatile("" : "+s"(w)); \
  const int tid = w * 64 + lane; \
  int bid = blockIdx.x; asm volatile("" : "+s"(bid)); \
  ws = uni(ws); asm volatile("" : "+s"(ws)); \
  layer = __builtin_amdgcn_readfirstlane(layer); x0 = uni(x0); wo = uni(wo); \
    \
  const int rt = 2 * (bid & 4) + ((bid >> 3) & 7); \
  const int hp = 4 * (bid & 3) + (bid >> 6); \
  const int m0 = rt * 128; \
  const int fr = lane & 15, g = lane >> 4; \
  const int q4 = (lane & 15) >> 2, p4 = lane & 3; \
  const __amdgpu_buffer_rsrc_t rsW = __builtin_amdgcn_make_buffer_rsrc((void*)ws, (short)0, (int)WS_END, 0x00020000); \
  (void)m0; (void)fr; (void)q4; (void)p4; (void)rsW; (void)g;

__device__ __forceinline__ void code_touch(unsigned& junk, int bid, int lane) {
  unsigned long long pc;
  unsigned len, d;
  asm volatile("s_getpc_b64 %0\n.Lct%=:\n\ts_mov_b32 %2, .Lct%=-.Lmk_pc\n\ts_mov_b32 %1, .Lmk_end-.Lmk_pc" : "=s"(pc), "=s"(len), "=s"(d));
  pc -= d;
  const unsigned per = ((len >> 10) + 1) * 128, off = ((bid >> 3) & 7) * per + (lane * 128 < per ? lane * 128 : 0);
  asm volatile("global_load_dword %0, %1, %2" : "+v"(junk) : "v"(off < len - 4 ? off : 0u), "s"(pc) : "memory");
}

__device__ __forceinline__ void publish(__amdgpu_buffer_rsrc_t rsW, int tid, unsigned idx, u64 v) {
  unsigned off = WS_FLAGS + idx * FLS;
  asm volatile("" : "+v"(off), "+v"(v));
  WAITV(0);
  WAITL0; BAR;
  if (tid == 192) __builtin_amdgcn_raw_buffer_store_b64(__builtin_bit_cast(u32x2, v), rsW, off, 0, 16);
  asm volatile("" ::: "memory");
}
__device__ __forceinline__ void poll_flags(char* ws, u64 E, int set, int rt, int hp, int lane, unsigned n, bool col) {
  const int l = lane & 15;
  const unsigned fo = WS_FLAGS + (unsigned)(set * 256 + (col ? l * 16 + hp : rt * 16 + l)) * FLS;
  _Pragma("unroll 1") for (unsigned spins = 0; spins < (1u << 18); ++spins) {
    u64 d;
    asm volatile("global_load_dwordx2 %0, %1, %2 sc1\n\ts_waitcnt vmcnt(0)" : "=v"(d) : "v"(fo), "s"(ws) : "memory");
    if (__all(d - (E + n) <= (u64)(16u - n))) break;
    __builtin_amdgcn_s_sleep(1);
  }
}

__device__ __forceinline__ u64 poll_eq(const char* ws, unsigned off, u64 want, u64 mask, u64 now) {
  u64 miss;
  _Pragma("unroll 1") for (unsigned spins = 0; spins < (1u << 18); ++spins) {
    u64 d;
    asm volatile("global_load_dwordx2 %0, %1, %2 sc1\n\ts_waitcnt vmcnt(0)" : "=v"(d) : "v"(off), "s"(ws) : "memory");
    miss = __ballot(d != want) & mask;
    if (!(miss & now)) break;
    __builtin_amdgcn_s_sleep(1);
  }
  return miss;
}

#define G1_MACROS \
  ".macro G1M rb, j, s, z\n" \
  " .set g1d, 160+24*(\\rb)+4*(((\\j)+2)-6*((\\j)>>2))\n .set g1a, 80+40*(\\s)+4*(\\rb)\n .set g1b, 96+40*(\\s)+4*(\\j)\n" \
  " .if ((\\j)>>1) == 1\n  .set g1t, g1a\n  .set g1a, g1b\n  .set g1b, g1t\n .endif\n" \
  " .if \\z\n  v_mfma_f32_16x16x32_bf16 v[g1d:g1d+3], v[g1b:g1b+3], v[g1a:g1a+3], 0\n" \
  " .else\n  v_mfma_f32_16x16x32_bf16 v[g1d:g1d+3], v[g1b:g1b+3], v[g1a:g1a+3], v[g1d:g1d+3]\n .endif\n" \
  ".endm\n" \
  ".macro G1RA rb, s, ra, oa\n" \
  " ds_read_b128 v[80+40*(\\s)+4*(\\rb):83+40*(\\s)+4*(\\rb)], \\ra offset:(\\oa)+(\\rb)*2048\n" \
  ".endm\n" \
  ".macro G1RB k, s, r0, r1, ob\n" \
  " .if (\\k) & 2\n  ds_read_b64_tr_b16 v[96+40*(\\s)+2*(\\k):97+40*(\\s)+2*(\\k)], \\r1 offset:(\\ob)+(((\\k)>>2)+1-3*((\\k)>>3))*2048+((\\k)&1)*512\n" \
  " .else\n  ds_read_b64_tr_b16 v[96+40*(\\s)+2*(\\k):97+40*(\\s)+2*(\\k)], \\r0 offset:(\\ob)+(((\\k)>>2)+1-3*((\\k)>>3))*2048+((\\k)&1)*512\n .endif\n" \
  ".endm\n" \
    \
  ".macro G1BK i, g, r0, r1, ob\n G1RB 4*(\\g)+((\\i)&3), (\\i)>>2, \\r0, \\r1, (\\ob)+((\\i)>>2)*12288\n.endm\n" \
  ".macro G1AK rb, s, a0, a1, oa\n .if \\s\n  G1RA \\rb, 1, \\a1, \\oa\n .else\n  G1RA \\rb, 0, \\a0, \\oa\n .endif\n.endm\n" \
  ".macro G1RDB g, r0, r1, ob\n .set g1i, 0\n .rept 8\n  G1BK g1i, \\g, \\r0, \\r1, \\ob\n  .set g1i, g1i+1\n .endr\n.endm\n" \
  ".macro G1RDA a0, a1, oa\n .set g1i, 0\n .rept 8\n  G1AK (g1i>>1)^2, g1i&1, \\a0, \\a1, \\oa\n  .set g1i, g1i+1\n .endr\n.endm\n" \
    \
  ".macro G1Q g, rb, q, i, z\n G1M \\rb, 2*(\\g)+((((\\i)>>1)^(\\q))&1), (((\\i)+1)>>1)&1, (\\z)&&(((\\i)&1)==0)\n.endm\n" \
    \
  ".macro G1SG g, z, rd, a0, a1, r0, r1, o\n" \
  " .set g1i, 0\n .rept 16\n" \
  "  .if ((\\g) == 0) && (g1i == 8) && (\\rd)\n   s_waitcnt lgkmcnt(7)\n   s_nop 0\n  .endif\n" \
  "  .if ((\\g) == 0) && (g1i == 15)\n   s_waitcnt lgkmcnt(0)\n  .endif\n" \
  "  G1Q \\g, (g1i>>2)^(2-((\\g)&1)), g1i>>2, g1i&3, \\z\n" \
  "  .if \\rd\n" \
  "   .if (\\g) == 0\n    .if g1i < 7\n     G1BK g1i, 2, \\r0, \\r1, \\o\n    .elseif g1i == 8\n     G1BK 7, 2, \\r0, \\r1, \\o\n    .endif\n" \
  "   .elseif (\\g) == 1\n    .if g1i < 8\n     G1BK g1i, 0, \\r0, \\r1, \\o\n    .endif\n" \
  "   .elseif (g1i & 2) == 0\n    G1BK (g1i&1)+2*(g1i>>2), 1, \\r0, \\r1, \\o\n" \
  "   .elseif (\\rd) == 1\n    G1AK (g1i>>2)^2, 1-(g1i&1), \\a0, \\a1, \\o\n   .endif\n" \
  "  .endif\n" \
  "  .set g1i, g1i+1\n .endr\n" \
  ".endm\n" \
  ""
#define G1_SLIDE \
  "G1Q 1, 3, 0, 0, 0\n ds_read2_b64 v[60:63], v49 offset0:32 offset1:48\n ds_read_b128 v[96:99], v48 offset:256\n G1Q 1, 3, 0, 1, 0\n " \
  "ds_read_b128 v[100:103], v48 offset:320\n ds_read2_b32 v[56:57], v52 offset1:16\n G1Q 1, 3, 0, 2, 0\n ds_read_b128 v[136:139], v54 offset:192\n " \
  "ds_read_b128 v[140:143], v54 offset:704\n G1Q 1, 3, 0, 3, 0\n G1Q 1, 2, 1, 0, 0\n G1Q 1, 2, 1, 1, 0\n s_waitcnt lgkmcnt(3)\n " \
  "v_fmac_f32 v240, v96, v62\n G1Q 1, 2, 1, 2, 0\n v_fma_f32 v241, v97, v62, v241\n v_fma_f32 v242, v98, v62, v242\n G1Q 1, 2, 1, 3, 0\n " \
  "v_fma_f32 v243, v99, v62, v243\n v_fma_f32 v244, v100, v62, v244\n G1Q 1, 1, 2, 0, 0\n v_fma_f32 v245, v101, v62, v245\n " \
  "v_fma_f32 v246, v102, v62, v246\n G1Q 1, 1, 2, 1, 0\n v_fma_f32 v247, v103, v62, v247\n v_mul_f32_e64 v240, v63, v240\n G1Q 1, 1, 2, 2, 0\n " \
  "v_mul_f32_e64 v241, v63, v241\n v_mul_f32_e64 v242, v63, v242\n G1Q 1, 1, 2, 3, 0\n v_mul_f32_e64 v243, v63, v243\n v_mul_f32_e64 v244, v63, v244\n " \
  "G1Q 1, 0, 3, 0, 0\n v_mul_f32_e64 v245, v63, v245\n v_mul_f32_e64 v246, v63, v246\n G1Q 1, 0, 3, 1, 0\n v_mul_f32_e64 v247, v63, v247\n " \
  "v_cvt_pk_bf16_f32 v240, v240, v241\n G1Q 1, 0, 3, 2, 0\n v_cvt_pk_bf16_f32 v241, v242, v243\n v_cvt_pk_bf16_f32 v242, v244, v245\n " \
  "G1Q 1, 0, 3, 3, 0\n v_cvt_pk_bf16_f32 v243, v246, v247\n s_waitcnt lgkmcnt(0)\n v_fmac_f32 v248, v136, v56\n G1Q 2, 2, 0, 0, 0\n " \
  "ds_read2_b64 v[144:147], v49 offset1:16\n v_fma_f32 v249, v137, v56, v249\n G1Q 2, 2, 0, 1, 0\n ds_read_b128 v[148:151], v48\n " \
  "v_fma_f32 v250, v138, v56, v250\n G1Q 2, 2, 0, 2, 0\n ds_read_b128 v[104:107], v48 offset:64\n v_fma_f32 v251, v139, v56, v251\n G1Q 2, 2, 0, 3, 0\n " \
  "ds_read_b128 v[108:111], v54 offset:128\n v_fma_f32 v252, v136, v57, v252\n G1Q 2, 3, 1, 0, 0\n v_fma_f32 v253, v137, v57, v253\n " \
  "v_fma_f32 v254, v138, v57, v254\n G1Q 2, 3, 1, 1, 0\n ds_read_b128 v[88:91], v54 offset:640\n v_fma_f32 v255, v139, v57, v255\n G1Q 2, 3, 1, 2, 0\n " \
  "ds_read_b128 v[128:131], v54\n v_mul_f32_e64 v248, v140, v248\n G1Q 2, 3, 1, 3, 0\n v_mul_f32_e64 v249, v141, v249\n v_mul_f32_e64 v250, v142, v250\n " \
  "G1Q 2, 0, 2, 0, 0\n v_mul_f32_e64 v251, v143, v251\n v_mul_f32_e64 v252, v140, v252\n G1Q 2, 0, 2, 1, 0\n ds_read_b128 v[92:95], v54 offset:512\n " \
  "v_mul_f32_e64 v253, v141, v253\n G1Q 2, 0, 2, 2, 0\n v_mul_f32_e64 v254, v142, v254\n v_mul_f32_e64 v255, v143, v255\n G1Q 2, 0, 2, 3, 0\n " \
  "v_cvt_pk_bf16_f32 v248, v248, v249\n v_cvt_pk_bf16_f32 v249, v250, v251\n G1Q 2, 1, 3, 0, 0\n v_cvt_pk_bf16_f32 v255, v254, v255\n " \
  "v_cvt_pk_bf16_f32 v254, v252, v253\n G1Q 2, 1, 3, 1, 0\n v_min_u32_e64 v136, -2, v240\n v_min_u32_e64 v137, -2, v241\n v_min_u32_e64 v138, -2, v242\n " \
  "G1Q 2, 1, 3, 2, 0\n v_min_u32_e64 v139, -2, v243\n v_min_u32_e64 v140, -2, v248\n v_min_u32_e64 v141, -2, v249\n G1Q 2, 1, 3, 3, 0\n " \
  "v_min_u32_e64 v142, -2, v254\n v_min_u32_e64 v143, -2, v255\n ds_read_b128 v[112:115], v54 offset:64\n ds_read_b128 v[116:119], v54 offset:576\n " \
  "s_cmp_eq_u32 %[wm], 0\n s_cbranch_scc1 .Lg1d%=\n buffer_store_dwordx4 v[136:139], v55, %[rs], 0 offen sc1\n " \
  "buffer_store_dwordx4 v[140:143], v55, %[rs], 0 offen offset:1024 sc1\n ds_read_b32 v53, v59\n ds_read_b32 v53, v59\n ds_read_b32 v53, v59\n " \
  "s_branch .Lg1e%=\n .Lg1d%=:\n v_mov_b32 v53, 1\n ds_write_b128 v58, v[136:139]\n ds_write_b128 v58, v[140:143] offset:1024\n ds_write_b32 v59, v53\n " \
  ".Lg1e%=:\n s_waitcnt lgkmcnt(8)\n v_pk_fma_f32 v[232:233], v[148:149], v[62:63], v[232:233] op_sel_hi:[1,0,1]\n " \
  "v_pk_fma_f32 v[234:235], v[150:151], v[62:63], v[234:235] op_sel_hi:[1,0,1]\n " \
  "v_pk_fma_f32 v[236:237], v[104:105], v[62:63], v[236:237] op_sel_hi:[1,0,1]\n " \
  "v_pk_fma_f32 v[238:239], v[106:107], v[62:63], v[238:239] op_sel_hi:[1,0,1]\n " \
  "v_pk_fma_f32 v[216:217], v[96:97], v[60:61], v[216:217] op_sel_hi:[1,0,1]\n " \
  "v_pk_fma_f32 v[218:219], v[98:99], v[60:61], v[218:219] op_sel_hi:[1,0,1]\n " \
  "v_pk_fma_f32 v[220:221], v[100:101], v[60:61], v[220:221] op_sel_hi:[1,0,1]\n " \
  "v_pk_fma_f32 v[222:223], v[102:103], v[60:61], v[222:223] op_sel_hi:[1,0,1]\n v_pk_mul_f32 v[216:217], v[60:61], v[216:217] op_sel:[1,0]\n " \
  "v_pk_mul_f32 v[218:219], v[60:61], v[218:219] op_sel:[1,0]\n v_pk_mul_f32 v[220:221], v[60:61], v[220:221] op_sel:[1,0]\n " \
  "v_pk_mul_f32 v[222:223], v[60:61], v[222:223] op_sel:[1,0]\n v_cvt_pk_bf16_f32 v232, v232, v233\n v_cvt_pk_bf16_f32 v233, v234, v235\n " \
  "v_cvt_pk_bf16_f32 v234, v236, v237\n v_cvt_pk_bf16_f32 v235, v238, v239\n v_cvt_pk_bf16_f32 v216, v216, v217\n v_cvt_pk_bf16_f32 v217, v218, v219\n " \
  "v_cvt_pk_bf16_f32 v218, v220, v221\n v_cvt_pk_bf16_f32 v219, v222, v223\n " \
  "v_pk_fma_f32 v[208:209], v[148:149], v[60:61], v[208:209] op_sel_hi:[1,0,1]\n " \
  "v_pk_fma_f32 v[210:211], v[150:151], v[60:61], v[210:211] op_sel_hi:[1,0,1]\n " \
  "v_pk_fma_f32 v[212:213], v[104:105], v[60:61], v[212:213] op_sel_hi:[1,0,1]\n " \
  "v_pk_fma_f32 v[214:215], v[106:107], v[60:61], v[214:215] op_sel_hi:[1,0,1]\n " \
  "v_pk_fma_f32 v[192:193], v[96:97], v[146:147], v[192:193] op_sel_hi:[1,0,1]\n " \
  "v_pk_fma_f32 v[194:195], v[98:99], v[146:147], v[194:195] op_sel_hi:[1,0,1]\n " \
  "v_pk_fma_f32 v[196:197], v[100:101], v[146:147], v[196:197] op_sel_hi:[1,0,1]\n " \
  "v_pk_fma_f32 v[198:199], v[102:103], v[146:147], v[198:199] op_sel_hi:[1,0,1]\n v_pk_mul_f32 v[192:193], v[146:147], v[192:193] op_sel:[1,0]\n " \
  "v_pk_mul_f32 v[194:195], v[146:147], v[194:195] op_sel:[1,0]\n v_pk_mul_f32 v[196:197], v[146:147], v[196:197] op_sel:[1,0]\n " \
  "v_pk_mul_f32 v[198:199], v[146:147], v[198:199] op_sel:[1,0]\n v_mfma_f32_16x16x32_bf16 v[120:123], v[240:243], v[232:235], v[40:43]\n " \
  "v_mfma_f32_16x16x32_bf16 v[236:239], v[216:219], v[232:235], v[36:39]\n v_cvt_pk_bf16_f32 v208, v208, v209\n v_cvt_pk_bf16_f32 v209, v210, v211\n " \
  "v_cvt_pk_bf16_f32 v210, v212, v213\n v_cvt_pk_bf16_f32 v211, v214, v215\n v_cvt_pk_bf16_f32 v192, v192, v193\n v_cvt_pk_bf16_f32 v193, v194, v195\n " \
  "v_cvt_pk_bf16_f32 v194, v196, v197\n v_cvt_pk_bf16_f32 v195, v198, v199\n " \
  "v_pk_fma_f32 v[184:185], v[148:149], v[146:147], v[184:185] op_sel_hi:[1,0,1]\n " \
  "v_pk_fma_f32 v[186:187], v[150:151], v[146:147], v[186:187] op_sel_hi:[1,0,1]\n " \
  "v_pk_fma_f32 v[188:189], v[104:105], v[146:147], v[188:189] op_sel_hi:[1,0,1]\n " \
  "v_pk_fma_f32 v[190:191], v[106:107], v[146:147], v[190:191] op_sel_hi:[1,0,1]\n " \
  "v_pk_fma_f32 v[168:169], v[96:97], v[144:145], v[168:169] op_sel_hi:[1,0,1]\n " \
  "v_pk_fma_f32 v[170:171], v[98:99], v[144:145], v[170:171] op_sel_hi:[1,0,1]\n " \
  "v_pk_fma_f32 v[172:173], v[100:101], v[144:145], v[172:173] op_sel_hi:[1,0,1]\n " \
  "v_pk_fma_f32 v[174:175], v[102:103], v[144:145], v[174:175] op_sel_hi:[1,0,1]\n v_pk_mul_f32 v[168:169], v[144:145], v[168:169] op_sel:[1,0]\n " \
  "v_pk_mul_f32 v[170:171], v[144:145], v[170:171] op_sel:[1,0]\n v_pk_mul_f32 v[172:173], v[144:145], v[172:173] op_sel:[1,0]\n " \
  "v_pk_mul_f32 v[174:175], v[144:145], v[174:175] op_sel:[1,0]\n v_mfma_f32_16x16x32_bf16 v[220:223], v[216:219], v[208:211], v[40:43]\n " \
  "v_mfma_f32_16x16x32_bf16 v[212:215], v[192:195], v[208:211], v[36:39]\n v_cvt_pk_bf16_f32 v184, v184, v185\n v_cvt_pk_bf16_f32 v185, v186, v187\n " \
  "v_cvt_pk_bf16_f32 v186, v188, v189\n v_cvt_pk_bf16_f32 v187, v190, v191\n v_cvt_pk_bf16_f32 v168, v168, v169\n v_cvt_pk_bf16_f32 v169, v170, v171\n " \
  "v_cvt_pk_bf16_f32 v170, v172, v173\n v_cvt_pk_bf16_f32 v171, v174, v175\n " \
  "v_pk_fma_f32 v[160:161], v[148:149], v[144:145], v[160:161] op_sel_hi:[1,0,1]\n " \
  "v_pk_fma_f32 v[162:163], v[150:151], v[144:145], v[162:163] op_sel_hi:[1,0,1]\n " \
  "v_pk_fma_f32 v[164:165], v[104:105], v[144:145], v[164:165] op_sel_hi:[1,0,1]\n " \
  "v_pk_fma_f32 v[166:167], v[106:107], v[144:145], v[166:167] op_sel_hi:[1,0,1]\n s_waitcnt lgkmcnt(3)\n " \
  "v_pk_fma_f32 v[176:177], v[128:129], v[56:57], v[176:177] op_sel_hi:[1,0,1]\n " \
  "v_pk_fma_f32 v[178:179], v[130:131], v[56:57], v[178:179] op_sel_hi:[1,0,1]\n " \
  "v_pk_fma_f32 v[180:181], v[128:129], v[56:57], v[180:181] op_sel:[0,1,0]\n v_pk_fma_f32 v[182:183], v[130:131], v[56:57], v[182:183] op_sel:[0,1,0]\n " \
  "v_pk_mul_f32 v[176:177], v[176:177], v[92:93]\n v_pk_mul_f32 v[178:179], v[178:179], v[94:95]\n v_pk_mul_f32 v[180:181], v[180:181], v[92:93]\n " \
  "v_pk_mul_f32 v[182:183], v[182:183], v[94:95]\n v_mfma_f32_16x16x32_bf16 v[196:199], v[192:195], v[184:187], v[40:43]\n " \
  "v_mfma_f32_16x16x32_bf16 v[188:191], v[168:171], v[184:187], v[36:39]\n v_cvt_pk_bf16_f32 v160, v160, v161\n v_cvt_pk_bf16_f32 v161, v162, v163\n " \
  "v_cvt_pk_bf16_f32 v162, v164, v165\n v_cvt_pk_bf16_f32 v163, v166, v167\n v_cvt_pk_bf16_f32 v174, v176, v177\n v_cvt_pk_bf16_f32 v175, v178, v179\n " \
  "v_cvt_pk_bf16_f32 v180, v180, v181\n v_cvt_pk_bf16_f32 v181, v182, v183\n " \
  "v_pk_fma_f32 v[224:225], v[108:109], v[56:57], v[224:225] op_sel_hi:[1,0,1]\n " \
  "v_pk_fma_f32 v[226:227], v[110:111], v[56:57], v[226:227] op_sel_hi:[1,0,1]\n " \
  "v_pk_fma_f32 v[228:229], v[108:109], v[56:57], v[228:229] op_sel:[0,1,0]\n v_pk_fma_f32 v[230:231], v[110:111], v[56:57], v[230:231] op_sel:[0,1,0]\n " \
  "v_pk_mul_f32 v[224:225], v[224:225], v[88:89]\n v_pk_mul_f32 v[226:227], v[226:227], v[90:91]\n v_pk_mul_f32 v[228:229], v[228:229], v[88:89]\n " \
  "v_pk_mul_f32 v[230:231], v[230:231], v[90:91]\n v_mfma_f32_16x16x32_bf16 v[124:127], v[168:171], v[160:163], v[40:43]\n " \
  "v_cvt_pk_bf16_f32 v246, v224, v225\n v_cvt_pk_bf16_f32 v247, v226, v227\n v_cvt_pk_bf16_f32 v252, v228, v229\n v_cvt_pk_bf16_f32 v253, v230, v231\n " \
  "v_max3_f32 v140, v236, v237, v238\n v_max3_f32 v138, v212, v213, v214\n v_max3_f32 v141, v239, v120, v121\n v_max3_f32 v139, v215, v220, v221\n " \
  "v_max3_f32 v140, v140, v122, v123\n v_max3_f32 v138, v138, v222, v223\n v_max_f32 v140, v140, v141\n v_max_f32 v138, v138, v139\n " \
  "v_max3_f32 v136, v188, v189, v190\n v_max3_f32 v137, v191, v196, v197\n v_max3_f32 v136, v136, v198, v199\n " \
  "v_pk_fma_f32 v[200:201], v[112:113], v[56:57], v[200:201] op_sel_hi:[1,0,1]\n " \
  "v_pk_fma_f32 v[202:203], v[114:115], v[56:57], v[202:203] op_sel_hi:[1,0,1]\n v_max_f32 v136, v136, v137\n v_mov_b32 v142, v140\n " \
  "v_pk_fma_f32 v[204:205], v[112:113], v[56:57], v[204:205] op_sel:[0,1,0]\n v_pk_fma_f32 v[206:207], v[114:115], v[56:57], v[206:207] op_sel:[0,1,0]\n " \
  "v_permlane16_swap_b32 v136, v138\n v_permlane16_swap_b32 v140, v142\n v_pk_mul_f32 v[200:201], v[200:201], v[116:117]\n " \
  "v_pk_mul_f32 v[202:203], v[202:203], v[118:119]\n v_max_f32 v136, v136, v138\n v_max_f32 v140, v140, v142\n " \
  "v_pk_mul_f32 v[204:205], v[204:205], v[116:117]\n v_pk_mul_f32 v[206:207], v[206:207], v[118:119]\n v_permlane32_swap_b32 v136, v140\n " \
  "v_mul_f32 v152, 0x3e8293ee, v63\n v_mul_f32 v154, 0x3e8293ee, v61\n v_max_f32 v136, v136, v140\n v_mov_b32 v140, v136\n " \
  "v_mul_f32 v156, 0x3e8293ee, v147\n v_mul_f32 v158, 0x3e8293ee, v145\n v_permlane32_swap_b32 v136, v140\n v_mov_b32 v132, 0x3f803f80\n " \
  "v_mov_b32 v133, 0x3f803f80\n v_mov_b32 v138, v136\n v_mov_b32 v134, 0x3f803f80\n v_mov_b32 v135, 0x3f803f80\n v_permlane16_swap_b32 v136, v138\n " \
  "v_cvt_pk_bf16_f32 v244, v200, v201\n v_cvt_pk_bf16_f32 v245, v202, v203\n v_cvt_pk_bf16_f32 v250, v204, v205\n v_cvt_pk_bf16_f32 v251, v206, v207\n " \
  "v_cvt_pk_bf16_f32 v176, v200, v201\n v_cvt_pk_bf16_f32 v177, v202, v203\n v_cvt_pk_bf16_f32 v182, v204, v205\n v_cvt_pk_bf16_f32 v183, v206, v207\n " \
  "v_mul_f32_e64 v153, v152, -v140\n v_mul_f32_e64 v155, v154, -v138\n v_mul_f32_e64 v157, v156, -v136\n " \
  "v_pk_fma_f32 v[236:237], v[236:237], v[152:153], v[152:153] op_sel:[0,0,1] op_sel_hi:[1,0,1]\n " \
  "v_pk_fma_f32 v[238:239], v[238:239], v[152:153], v[152:153] op_sel:[0,0,1] op_sel_hi:[1,0,1]\n " \
  "v_pk_fma_f32 v[120:121], v[120:121], v[152:153], v[152:153] op_sel:[0,0,1] op_sel_hi:[1,0,1]\n " \
  "v_pk_fma_f32 v[122:123], v[122:123], v[152:153], v[152:153] op_sel:[0,0,1] op_sel_hi:[1,0,1]\n v_exp_f32 v236, v236\n v_exp_f32 v237, v237\n " \
  "v_exp_f32 v238, v238\n v_exp_f32 v239, v239\n v_exp_f32 v120, v120\n v_exp_f32 v121, v121\n v_exp_f32 v122, v122\n v_exp_f32 v123, v123\n " \
  "v_pk_fma_f32 v[212:213], v[212:213], v[154:155], v[154:155] op_sel:[0,0,1] op_sel_hi:[1,0,1]\n " \
  "v_pk_fma_f32 v[214:215], v[214:215], v[154:155], v[154:155] op_sel:[0,0,1] op_sel_hi:[1,0,1]\n " \
  "v_pk_fma_f32 v[220:221], v[220:221], v[154:155], v[154:155] op_sel:[0,0,1] op_sel_hi:[1,0,1]\n " \
  "v_pk_fma_f32 v[222:223], v[222:223], v[154:155], v[154:155] op_sel:[0,0,1] op_sel_hi:[1,0,1]\n v_cvt_pk_bf16_f32 v236, v236, v237\n " \
  "v_cvt_pk_bf16_f32 v237, v238, v239\n v_cvt_pk_bf16_f32 v238, v120, v121\n v_cvt_pk_bf16_f32 v239, v122, v123\n v_exp_f32 v212, v212\n " \
  "v_exp_f32 v213, v213\n v_mfma_f32_16x16x32_bf16 v[120:123], v[132:135], v[236:239], 0\n " \
  "v_mfma_f32_16x16x32_bf16 v[232:235], v[246:249], v[236:239], 0\n v_mfma_f32_16x16x32_bf16 v[240:243], v[252:255], v[236:239], 0\n " \
  "v_exp_f32 v214, v214\n v_exp_f32 v215, v215\n v_exp_f32 v220, v220\n v_exp_f32 v221, v221\n v_exp_f32 v222, v222\n v_exp_f32 v223, v223\n " \
  "v_pk_fma_f32 v[188:189], v[188:189], v[156:157], v[156:157] op_sel:[0,0,1] op_sel_hi:[1,0,1]\n " \
  "v_pk_fma_f32 v[190:191], v[190:191], v[156:157], v[156:157] op_sel:[0,0,1] op_sel_hi:[1,0,1]\n " \
  "v_pk_fma_f32 v[196:197], v[196:197], v[156:157], v[156:157] op_sel:[0,0,1] op_sel_hi:[1,0,1]\n " \
  "v_pk_fma_f32 v[198:199], v[198:199], v[156:157], v[156:157] op_sel:[0,0,1] op_sel_hi:[1,0,1]\n v_cvt_pk_bf16_f32 v212, v212, v213\n " \
  "v_cvt_pk_bf16_f32 v213, v214, v215\n v_cvt_pk_bf16_f32 v214, v220, v221\n v_cvt_pk_bf16_f32 v215, v222, v223\n v_exp_f32 v188, v188\n " \
  "s_cmp_eq_u32 %[wm], 0\n s_cbranch_scc1 .Lg1f%=\n ds_read_b32 v53, v59\n ds_read_b128 v[80:83], v58\n ds_read_b64 v[172:173], v58 offset:1024\n " \
  "ds_read_b64 v[178:179], v58 offset:1032\n s_branch .Lg1g%=\n .Lg1f%=:\n buffer_load_dwordx4 v[80:83], v55, %[rs], 0 offen sc1\n " \
  "buffer_load_dwordx2 v[172:173], v55, %[rs], 0 offen offset:1024 sc1\n buffer_load_dwordx2 v[178:179], v55, %[rs], 0 offen offset:1032 sc1\n " \
  ".Lg1g%=:\n v_exp_f32 v189, v189\n v_mfma_f32_16x16x32_bf16 v[220:223], v[132:135], v[212:215], 0\n " \
  "v_mfma_f32_16x16x32_bf16 v[224:227], v[244:247], v[212:215], 0\n v_mfma_f32_16x16x32_bf16 v[228:231], v[250:253], v[212:215], 0\n " \
  "v_exp_f32 v190, v190\n v_exp_f32 v191, v191\n v_exp_f32 v196, v196\n v_exp_f32 v197, v197\n v_exp_f32 v198, v198\n v_exp_f32 v199, v199\n " \
  "v_rcp_f32 v120, v120\n v_cvt_pk_bf16_f32 v188, v188, v189\n v_pk_mul_f32 v[232:233], v[232:233], v[120:121] op_sel_hi:[1,0]\n " \
  "v_pk_mul_f32 v[234:235], v[234:235], v[120:121] op_sel_hi:[1,0]\n v_pk_mul_f32 v[240:241], v[240:241], v[120:121] op_sel_hi:[1,0]\n " \
  "v_pk_mul_f32 v[242:243], v[242:243], v[120:121] op_sel_hi:[1,0]\n v_cvt_pk_bf16_f32 v189, v190, v191\n v_cvt_pk_bf16_f32 v190, v196, v197\n " \
  "v_cvt_pk_bf16_f32 v191, v198, v199\n v_rcp_f32 v220, v220\n v_cvt_pk_bf16_f32 v232, v232, v233\n v_cvt_pk_bf16_f32 v233, v234, v235\n " \
  "v_mfma_f32_16x16x32_bf16 v[196:199], v[132:135], v[188:191], 0\n v_mfma_f32_16x16x32_bf16 v[200:203], v[174:177], v[188:191], 0\n " \
  "v_mfma_f32_16x16x32_bf16 v[204:207], v[180:183], v[188:191], 0\n v_cvt_pk_bf16_f32 v234, v240, v241\n v_cvt_pk_bf16_f32 v235, v242, v243\n " \
  "buffer_store_dwordx4 v[232:235], v51, %[rs], 0 offen offset:3072 sc1\n ds_write_b128 v50, v[232:235] offset:3072\n " \
  "v_pk_mul_f32 v[224:225], v[224:225], v[220:221] op_sel_hi:[1,0]\n v_pk_mul_f32 v[226:227], v[226:227], v[220:221] op_sel_hi:[1,0]\n " \
  "v_pk_mul_f32 v[228:229], v[228:229], v[220:221] op_sel_hi:[1,0]\n v_pk_mul_f32 v[230:231], v[230:231], v[220:221] op_sel_hi:[1,0]\n " \
  "v_cvt_pk_bf16_f32 v224, v224, v225\n v_cvt_pk_bf16_f32 v225, v226, v227\n v_cvt_pk_bf16_f32 v226, v228, v229\n v_cvt_pk_bf16_f32 v227, v230, v231\n " \
  "v_rcp_f32 v196, v196\n buffer_store_dwordx4 v[224:227], v51, %[rs], 0 offen offset:2048 sc1\n ds_write_b128 v50, v[224:227] offset:2048\n " \
  "v_pk_mul_f32 v[200:201], v[200:201], v[196:197] op_sel_hi:[1,0]\n v_pk_mul_f32 v[202:203], v[202:203], v[196:197] op_sel_hi:[1,0]\n " \
  "v_pk_mul_f32 v[204:205], v[204:205], v[196:197] op_sel_hi:[1,0]\n v_pk_mul_f32 v[206:207], v[206:207], v[196:197] op_sel_hi:[1,0]\n " \
  "v_cvt_pk_bf16_f32 v200, v200, v201\n v_cvt_pk_bf16_f32 v201, v202, v203\n v_cvt_pk_bf16_f32 v202, v204, v205\n v_cvt_pk_bf16_f32 v203, v206, v207\n " \
  "buffer_store_dwordx4 v[200:203], v51, %[rs], 0 offen offset:1024 sc1\n ds_write_b128 v50, v[200:203] offset:1024\n s_mov_b32 vcc_hi, 0x40000\n " \
  "s_cmp_eq_u32 %[wm], 0\n s_cbranch_scc1 .Lg1h%=\n s_waitcnt lgkmcnt(3)\n .Lg1i%=:\n v_readfirstlane_b32 vcc_lo, v53\n s_nop 1\n " \
  "s_cmp_lg_u32 vcc_lo, 0\n s_cbranch_scc1 .Lg1k%=\n ds_read_b32 v53, v59\n ds_read_b128 v[80:83], v58\n ds_read_b64 v[172:173], v58 offset:1024\n " \
  "ds_read_b64 v[178:179], v58 offset:1032\n s_waitcnt lgkmcnt(0)\n s_sub_u32 vcc_hi, vcc_hi, 1\n s_cbranch_scc0 .Lg1i%=\n s_branch .Lg1k%=\n .Lg1h%=:\n " \
  "s_mov_b32 m0, 0x40000\n s_waitcnt vmcnt(3)\n .Lg1j%=:\n v_max3_u32 v136, v80, v81, v82\n v_max3_u32 v137, v83, v172, v173\n " \
  "v_max3_u32 v136, v136, v178, v179\n v_max_u32 v136, v136, v137\n v_cmp_eq_u32 vcc, -1, v136\n s_cbranch_vccz .Lg1k%=\n " \
  "buffer_load_dwordx4 v[80:83], v55, %[rs], 0 offen sc1\n buffer_load_dwordx2 v[172:173], v55, %[rs], 0 offen offset:1024 sc1\n " \
  "buffer_load_dwordx2 v[178:179], v55, %[rs], 0 offen offset:1032 sc1\n s_waitcnt vmcnt(0)\n s_sub_u32 m0, m0, 1\n s_cbranch_scc0 .Lg1j%=\n .Lg1k%=:\n " \
  "v_mfma_f32_16x16x32_bf16 v[164:167], v[80:83], v[160:163], v[44:47]\n s_nop 7\n v_max3_f32 v136, v164, v165, v166\n " \
  "v_max3_f32 v137, v167, v124, v125\n v_max3_f32 v136, v136, v126, v127\n v_max_f32 v136, v136, v137\n v_mov_b32 v137, v136\n s_nop 1\n " \
  "v_permlane16_swap_b32 v136, v137\n v_max_f32 v136, v136, v137\n v_mov_b32 v137, v136\n s_nop 1\n v_permlane32_swap_b32 v136, v137\n " \
  "v_max_f32 v136, v136, v137\n v_mul_f32_e64 v159, v158, -v136\n " \
  "v_pk_fma_f32 v[164:165], v[164:165], v[158:159], v[158:159] op_sel:[0,0,1] op_sel_hi:[1,0,1]\n " \
  "v_pk_fma_f32 v[166:167], v[166:167], v[158:159], v[158:159] op_sel:[0,0,1] op_sel_hi:[1,0,1]\n " \
  "v_pk_fma_f32 v[124:125], v[124:125], v[158:159], v[158:159] op_sel:[0,0,1] op_sel_hi:[1,0,1]\n " \
  "v_pk_fma_f32 v[126:127], v[126:127], v[158:159], v[158:159] op_sel:[0,0,1] op_sel_hi:[1,0,1]\n v_exp_f32 v164, v164\n v_exp_f32 v165, v165\n " \
  "v_exp_f32 v166, v166\n v_exp_f32 v167, v167\n v_exp_f32 v124, v124\n v_exp_f32 v125, v125\n v_exp_f32 v126, v126\n v_exp_f32 v127, v127\n " \
  "v_cvt_pk_bf16_f32 v164, v164, v165\n v_cvt_pk_bf16_f32 v165, v166, v167\n v_cvt_pk_bf16_f32 v166, v124, v125\n v_cvt_pk_bf16_f32 v167, v126, v127\n " \
  "s_nop 1\n v_mfma_f32_16x16x32_bf16 v[124:127], v[132:135], v[164:167], 0\n v_mfma_f32_16x16x32_bf16 v[168:171], v[172:175], v[164:167], 0\n " \
  "v_mfma_f32_16x16x32_bf16 v[84:87], v[178:181], v[164:167], 0\n s_nop 5\n v_rcp_f32 v124, v124\n s_nop 0\n " \
  "v_pk_mul_f32 v[168:169], v[168:169], v[124:125] op_sel_hi:[1,0]\n v_pk_mul_f32 v[170:171], v[170:171], v[124:125] op_sel_hi:[1,0]\n " \
  "v_pk_mul_f32 v[84:85], v[84:85], v[124:125] op_sel_hi:[1,0]\n v_pk_mul_f32 v[86:87], v[86:87], v[124:125] op_sel_hi:[1,0]\n " \
  "v_cvt_pk_bf16_f32 v168, v168, v169\n v_cvt_pk_bf16_f32 v169, v170, v171\n v_cvt_pk_bf16_f32 v170, v84, v85\n v_cvt_pk_bf16_f32 v171, v86, v87\n " \
  "buffer_store_dwordx4 v[168:171], v51, %[rs], 0 offen offset:0 sc1\n ds_write_b128 v50, v[168:171] offset:0\n "
#define G1_HEDGE \
  "G1Q 1, 1, 0, 0, 0\n ds_read2_b64 v[140:143], v49 offset1:16\n ds_read_b128 v[96:99], v48 offset:256\n G1Q 1, 1, 0, 1, 0\n " \
  "ds_read_b128 v[100:103], v48 offset:320\n ds_read2_b64 v[136:139], v49 offset0:32 offset1:48\n G1Q 1, 1, 0, 2, 0\n ds_read_b64_tr_b16 v[36:37], v53\n " \
  "ds_read_b64_tr_b16 v[38:39], v53 offset:1024\n G1Q 1, 1, 0, 3, 0\n ds_read_b64_tr_b16 v[40:41], v53 offset:32\n " \
  "ds_read_b64_tr_b16 v[42:43], v53 offset:1056\n G1Q 1, 0, 1, 0, 0\n G1Q 1, 0, 1, 1, 0\n s_waitcnt lgkmcnt(4)\n v_fmac_f32 v168, v96, v140\n " \
  "G1Q 1, 0, 1, 2, 0\n v_fma_f32 v169, v97, v140, v169\n v_fma_f32 v170, v98, v140, v170\n G1Q 1, 0, 1, 3, 0\n v_fma_f32 v171, v99, v140, v171\n " \
  "v_fma_f32 v172, v100, v140, v172\n G1Q 1, 3, 2, 0, 0\n v_fma_f32 v173, v101, v140, v173\n v_fma_f32 v174, v102, v140, v174\n G1Q 1, 3, 2, 1, 0\n " \
  "v_fma_f32 v175, v103, v140, v175\n v_cvt_pk_bf16_f32 v168, v168, v169\n G1Q 1, 3, 2, 2, 0\n v_cvt_pk_bf16_f32 v169, v170, v171\n " \
  "v_cvt_pk_bf16_f32 v170, v172, v173\n G1Q 1, 3, 2, 3, 0\n v_cvt_pk_bf16_f32 v171, v174, v175\n v_fma_f32 v192, v96, v142, v192\n G1Q 1, 2, 3, 0, 0\n " \
  "v_fma_f32 v193, v97, v142, v193\n v_fma_f32 v194, v98, v142, v194\n G1Q 1, 2, 3, 1, 0\n v_fma_f32 v195, v99, v142, v195\n " \
  "v_fma_f32 v196, v100, v142, v196\n G1Q 1, 2, 3, 2, 0\n v_fma_f32 v197, v101, v142, v197\n v_fma_f32 v198, v102, v142, v198\n G1Q 1, 2, 3, 3, 0\n " \
  "v_fma_f32 v199, v103, v142, v199\n v_cvt_pk_bf16_f32 v192, v192, v193\n G1Q 2, 2, 0, 0, 0\n ds_read2_b32 v[144:145], v52 offset1:16\n " \
  "v_cvt_pk_bf16_f32 v193, v194, v195\n G1Q 2, 2, 0, 1, 0\n ds_read_b128 v[148:151], v48\n v_cvt_pk_bf16_f32 v194, v196, v197\n G1Q 2, 2, 0, 2, 0\n " \
  "v_cvt_pk_bf16_f32 v195, v198, v199\n v_fma_f32 v216, v96, v136, v216\n G1Q 2, 2, 0, 3, 0\n v_fma_f32 v217, v97, v136, v217\n " \
  "v_fma_f32 v218, v98, v136, v218\n G1Q 2, 3, 1, 0, 0\n v_fma_f32 v219, v99, v136, v219\n v_fma_f32 v220, v100, v136, v220\n G1Q 2, 3, 1, 1, 0\n " \
  "ds_read_b128 v[88:91], v48 offset:64\n v_fma_f32 v221, v101, v136, v221\n v_fma_f32 v222, v102, v136, v222\n G1Q 2, 3, 1, 2, 0\n " \
  "ds_read_b128 v[128:131], v54\n v_fma_f32 v223, v103, v136, v223\n v_cvt_pk_bf16_f32 v216, v216, v217\n G1Q 2, 3, 1, 3, 0\n " \
  "v_cvt_pk_bf16_f32 v217, v218, v219\n v_cvt_pk_bf16_f32 v218, v220, v221\n G1Q 2, 0, 2, 0, 0\n v_cvt_pk_bf16_f32 v219, v222, v223\n " \
  "v_fma_f32 v240, v96, v138, v240\n G1Q 2, 0, 2, 1, 0\n ds_read_b128 v[92:95], v54 offset:512\n v_fma_f32 v241, v97, v138, v241\n " \
  "v_fma_f32 v242, v98, v138, v242\n G1Q 2, 0, 2, 2, 0\n ds_read_b128 v[132:135], v54 offset:64\n v_fma_f32 v243, v99, v138, v243\n " \
  "v_fma_f32 v244, v100, v138, v244\n G1Q 2, 0, 2, 3, 0\n v_fma_f32 v245, v101, v138, v245\n v_fma_f32 v246, v102, v138, v246\n G1Q 2, 1, 3, 0, 0\n " \
  "v_fma_f32 v247, v103, v138, v247\n v_cvt_pk_bf16_f32 v240, v240, v241\n G1Q 2, 1, 3, 1, 0\n v_cvt_pk_bf16_f32 v241, v242, v243\n " \
  "v_cvt_pk_bf16_f32 v242, v244, v245\n G1Q 2, 1, 3, 2, 0\n v_cvt_pk_bf16_f32 v243, v246, v247\n G1Q 2, 1, 3, 3, 0\n s_waitcnt lgkmcnt(6)\n " \
  "v_mfma_f32_16x16x32_bf16 v[172:175], v[36:39], v[168:171], 0\n ds_read_b128 v[112:115], v54 offset:576\n ds_read_b128 v[116:119], v54 offset:128\n " \
  "v_mfma_f32_16x16x32_bf16 v[80:83], v[40:43], v[168:171], 0\n ds_read_b128 v[152:155], v54 offset:640\n ds_read_b128 v[156:159], v54 offset:192\n " \
  "v_mfma_f32_16x16x32_bf16 v[196:199], v[36:39], v[192:195], 0\n ds_read_b128 v[120:123], v54 offset:704\n v_mul_f32 v44, 0x3fb8aa3b, v141\n " \
  "v_mfma_f32_16x16x32_bf16 v[84:87], v[40:43], v[192:195], 0\n v_mul_f32 v46, 0x3fb8aa3b, v143\n v_mul_f32 v104, 0x3fb8aa3b, v137\n " \
  "v_mfma_f32_16x16x32_bf16 v[220:223], v[36:39], v[216:219], 0\n v_mul_f32 v106, 0x3fb8aa3b, v139\n " \
  "v_mfma_f32_16x16x32_bf16 v[124:127], v[40:43], v[216:219], 0\n v_mfma_f32_16x16x32_bf16 v[244:247], v[36:39], v[240:243], 0\n " \
  "v_mfma_f32_16x16x32_bf16 v[108:111], v[40:43], v[240:243], 0\n v_max3_f32 v168, v172, v173, v174\n v_max3_f32 v169, -v172, -v173, -v174\n " \
  "v_max3_f32 v168, v168, v175, v80\n v_max3_f32 v169, v169, -v175, -v80\n v_max3_f32 v168, v168, v81, v82\n v_max3_f32 v169, v169, -v81, -v82\n " \
  "v_max_f32_e64 v168, v168, v83\n v_max_f32_e64 v169, v169, -v83\n v_max3_f32 v170, v196, v197, v198\n v_max3_f32 v171, -v196, -v197, -v198\n " \
  "v_max3_f32 v170, v170, v199, v84\n v_max3_f32 v171, v171, -v199, -v84\n v_max3_f32 v170, v170, v85, v86\n v_max3_f32 v171, v171, -v85, -v86\n " \
  "v_max_f32_e64 v170, v170, v87\n v_max_f32_e64 v171, v171, -v87\n s_waitcnt lgkmcnt(0)\n " \
  "v_pk_fma_f32 v[176:177], v[128:129], v[144:145], v[176:177] op_sel_hi:[1,0,1]\n " \
  "v_pk_fma_f32 v[178:179], v[130:131], v[144:145], v[178:179] op_sel_hi:[1,0,1]\n " \
  "v_pk_fma_f32 v[180:181], v[128:129], v[144:145], v[180:181] op_sel:[0,1,0]\n " \
  "v_pk_fma_f32 v[182:183], v[130:131], v[144:145], v[182:183] op_sel:[0,1,0]\n v_pk_mul_f32 v[176:177], v[176:177], v[92:93]\n " \
  "v_pk_mul_f32 v[178:179], v[178:179], v[94:95]\n v_pk_mul_f32 v[180:181], v[180:181], v[92:93]\n v_pk_mul_f32 v[182:183], v[182:183], v[94:95]\n " \
  "v_pk_fma_f32 v[200:201], v[132:133], v[144:145], v[200:201] op_sel_hi:[1,0,1]\n " \
  "v_pk_fma_f32 v[202:203], v[134:135], v[144:145], v[202:203] op_sel_hi:[1,0,1]\n " \
  "v_pk_fma_f32 v[204:205], v[132:133], v[144:145], v[204:205] op_sel:[0,1,0]\n " \
  "v_pk_fma_f32 v[206:207], v[134:135], v[144:145], v[206:207] op_sel:[0,1,0]\n v_pk_mul_f32 v[200:201], v[200:201], v[112:113]\n " \
  "v_pk_mul_f32 v[202:203], v[202:203], v[114:115]\n v_pk_mul_f32 v[204:205], v[204:205], v[112:113]\n v_pk_mul_f32 v[206:207], v[206:207], v[114:115]\n " \
  "v_max3_f32 v192, v220, v221, v222\n v_max3_f32 v193, -v220, -v221, -v222\n v_max3_f32 v192, v192, v223, v124\n v_max3_f32 v193, v193, -v223, -v124\n " \
  "v_max3_f32 v192, v192, v125, v126\n v_max3_f32 v193, v193, -v125, -v126\n v_max_f32_e64 v192, v192, v127\n v_max_f32_e64 v193, v193, -v127\n " \
  "v_max3_f32 v194, v244, v245, v246\n v_max3_f32 v195, -v244, -v245, -v246\n v_max3_f32 v194, v194, v247, v108\n v_max3_f32 v195, v195, -v247, -v108\n " \
  "v_max3_f32 v194, v194, v109, v110\n v_max3_f32 v195, v195, -v109, -v110\n v_max_f32_e64 v194, v194, v111\n v_max_f32_e64 v195, v195, -v111\n " \
  "v_permlane16_swap_b32 v168, v169\n v_permlane16_swap_b32 v170, v171\n v_permlane16_swap_b32 v192, v193\n v_permlane16_swap_b32 v194, v195\n " \
  "v_pk_fma_f32 v[224:225], v[116:117], v[144:145], v[224:225] op_sel_hi:[1,0,1]\n " \
  "v_pk_fma_f32 v[226:227], v[118:119], v[144:145], v[226:227] op_sel_hi:[1,0,1]\n v_max_f32 v168, v168, v169\n v_max_f32 v170, v170, v171\n " \
  "v_max_f32 v192, v192, v193\n v_max_f32 v194, v194, v195\n v_pk_fma_f32 v[228:229], v[116:117], v[144:145], v[228:229] op_sel:[0,1,0]\n " \
  "v_pk_fma_f32 v[230:231], v[118:119], v[144:145], v[230:231] op_sel:[0,1,0]\n v_permlane32_swap_b32 v168, v170\n v_permlane32_swap_b32 v192, v194\n " \
  "v_pk_mul_f32 v[224:225], v[224:225], v[152:153]\n v_pk_mul_f32 v[226:227], v[226:227], v[154:155]\n v_max_f32 v168, v168, v170\n " \
  "v_max_f32 v192, v192, v194\n v_pk_mul_f32 v[228:229], v[228:229], v[152:153]\n v_pk_mul_f32 v[230:231], v[230:231], v[154:155]\n " \
  "v_mov_b32 v170, v168\n v_mov_b32 v194, v192\n v_pk_fma_f32 v[248:249], v[156:157], v[144:145], v[248:249] op_sel_hi:[1,0,1]\n " \
  "v_pk_fma_f32 v[250:251], v[158:159], v[144:145], v[250:251] op_sel_hi:[1,0,1]\n v_permlane32_swap_b32 v168, v170\n v_permlane32_swap_b32 v192, v194\n " \
  "v_pk_fma_f32 v[252:253], v[156:157], v[144:145], v[252:253] op_sel:[0,1,0]\n " \
  "v_pk_fma_f32 v[254:255], v[158:159], v[144:145], v[254:255] op_sel:[0,1,0]\n v_mov_b32 v169, v168\n v_mov_b32 v171, v170\n v_mov_b32 v193, v192\n " \
  "v_mov_b32 v195, v194\n v_pk_mul_f32 v[248:249], v[248:249], v[120:121]\n v_pk_mul_f32 v[250:251], v[250:251], v[122:123]\n " \
  "v_permlane16_swap_b32 v168, v169\n v_permlane16_swap_b32 v170, v171\n v_permlane16_swap_b32 v192, v193\n v_permlane16_swap_b32 v194, v195\n " \
  "v_pk_mul_f32 v[252:253], v[252:253], v[120:121]\n v_pk_mul_f32 v[254:255], v[254:255], v[122:123]\n v_cvt_pk_bf16_f32 v176, v176, v177\n " \
  "v_cvt_pk_bf16_f32 v177, v178, v179\n v_cvt_pk_bf16_f32 v178, v200, v201\n v_cvt_pk_bf16_f32 v179, v202, v203\n v_cvt_pk_bf16_f32 v180, v180, v181\n " \
  "v_cvt_pk_bf16_f32 v181, v182, v183\n v_cvt_pk_bf16_f32 v182, v204, v205\n v_cvt_pk_bf16_f32 v183, v206, v207\n v_cvt_pk_bf16_f32 v224, v224, v225\n " \
  "v_cvt_pk_bf16_f32 v225, v226, v227\n v_cvt_pk_bf16_f32 v226, v248, v249\n v_cvt_pk_bf16_f32 v227, v250, v251\n v_cvt_pk_bf16_f32 v228, v228, v229\n " \
  "v_cvt_pk_bf16_f32 v229, v230, v231\n v_cvt_pk_bf16_f32 v230, v252, v253\n v_cvt_pk_bf16_f32 v231, v254, v255\n v_mul_f32_e64 v45, v44, -v168\n " \
  "v_mul_f32_e64 v146, v44, -v169\n v_mul_f32_e64 v47, v46, -v170\n v_mul_f32_e64 v147, v46, -v171\n v_mul_f32_e64 v105, v104, -v192\n " \
  "v_mul_f32_e64 v144, v104, -v193\n v_mul_f32_e64 v107, v106, -v194\n v_mul_f32_e64 v145, v106, -v195\n " \
  "v_pk_fma_f32 v[200:201], v[172:173], v[44:45], v[146:147] op_sel_hi:[1,0,0] neg_lo:[1,0,0] neg_hi:[1,0,0]\n " \
  "v_pk_fma_f32 v[202:203], v[174:175], v[44:45], v[146:147] op_sel_hi:[1,0,0] neg_lo:[1,0,0] neg_hi:[1,0,0]\n " \
  "v_pk_fma_f32 v[204:205], v[80:81], v[44:45], v[146:147] op_sel_hi:[1,0,0] neg_lo:[1,0,0] neg_hi:[1,0,0]\n " \
  "v_pk_fma_f32 v[206:207], v[82:83], v[44:45], v[146:147] op_sel_hi:[1,0,0] neg_lo:[1,0,0] neg_hi:[1,0,0]\n " \
  "v_pk_fma_f32 v[172:173], v[172:173], v[44:45], v[44:45] op_sel:[0,0,1] op_sel_hi:[1,0,1]\n " \
  "v_pk_fma_f32 v[174:175], v[174:175], v[44:45], v[44:45] op_sel:[0,0,1] op_sel_hi:[1,0,1]\n " \
  "v_pk_fma_f32 v[80:81], v[80:81], v[44:45], v[44:45] op_sel:[0,0,1] op_sel_hi:[1,0,1]\n " \
  "v_pk_fma_f32 v[82:83], v[82:83], v[44:45], v[44:45] op_sel:[0,0,1] op_sel_hi:[1,0,1]\n ds_write_b128 v55, v[176:179] offset:0\n " \
  "v_exp_f32 v200, v200\n v_exp_f32 v201, v201\n v_exp_f32 v202, v202\n v_exp_f32 v203, v203\n v_exp_f32 v204, v204\n v_exp_f32 v205, v205\n " \
  "v_exp_f32 v206, v206\n v_exp_f32 v207, v207\n v_exp_f32 v172, v172\n v_exp_f32 v173, v173\n v_exp_f32 v174, v174\n v_exp_f32 v175, v175\n " \
  "v_exp_f32 v80, v80\n v_exp_f32 v81, v81\n v_exp_f32 v82, v82\n v_exp_f32 v83, v83\n " \
  "v_pk_fma_f32 v[248:249], v[196:197], v[46:47], v[146:147] op_sel:[0,0,1] op_sel_hi:[1,0,1] neg_lo:[1,0,0] neg_hi:[1,0,0]\n " \
  "v_pk_fma_f32 v[250:251], v[198:199], v[46:47], v[146:147] op_sel:[0,0,1] op_sel_hi:[1,0,1] neg_lo:[1,0,0] neg_hi:[1,0,0]\n " \
  "v_pk_fma_f32 v[252:253], v[84:85], v[46:47], v[146:147] op_sel:[0,0,1] op_sel_hi:[1,0,1] neg_lo:[1,0,0] neg_hi:[1,0,0]\n " \
  "v_pk_fma_f32 v[254:255], v[86:87], v[46:47], v[146:147] op_sel:[0,0,1] op_sel_hi:[1,0,1] neg_lo:[1,0,0] neg_hi:[1,0,0]\n " \
  "v_pk_fma_f32 v[196:197], v[196:197], v[46:47], v[46:47] op_sel:[0,0,1] op_sel_hi:[1,0,1]\n " \
  "v_pk_fma_f32 v[198:199], v[198:199], v[46:47], v[46:47] op_sel:[0,0,1] op_sel_hi:[1,0,1]\n " \
  "v_pk_fma_f32 v[84:85], v[84:85], v[46:47], v[46:47] op_sel:[0,0,1] op_sel_hi:[1,0,1]\n " \
  "v_pk_fma_f32 v[86:87], v[86:87], v[46:47], v[46:47] op_sel:[0,0,1] op_sel_hi:[1,0,1]\n ds_write_b128 v55, v[180:183] offset:1024\n " \
  "v_exp_f32 v248, v248\n v_exp_f32 v249, v249\n v_exp_f32 v250, v250\n v_exp_f32 v251, v251\n v_exp_f32 v252, v252\n v_exp_f32 v253, v253\n " \
  "v_exp_f32 v254, v254\n v_exp_f32 v255, v255\n v_exp_f32 v196, v196\n v_exp_f32 v197, v197\n v_exp_f32 v198, v198\n v_exp_f32 v199, v199\n " \
  "v_exp_f32 v84, v84\n v_exp_f32 v85, v85\n v_exp_f32 v86, v86\n v_exp_f32 v87, v87\n " \
  "v_pk_fma_f32 v[128:129], v[220:221], v[104:105], v[144:145] op_sel_hi:[1,0,0] neg_lo:[1,0,0] neg_hi:[1,0,0]\n " \
  "v_pk_fma_f32 v[130:131], v[222:223], v[104:105], v[144:145] op_sel_hi:[1,0,0] neg_lo:[1,0,0] neg_hi:[1,0,0]\n " \
  "v_pk_fma_f32 v[132:133], v[124:125], v[104:105], v[144:145] op_sel_hi:[1,0,0] neg_lo:[1,0,0] neg_hi:[1,0,0]\n " \
  "v_pk_fma_f32 v[134:135], v[126:127], v[104:105], v[144:145] op_sel_hi:[1,0,0] neg_lo:[1,0,0] neg_hi:[1,0,0]\n " \
  "v_pk_fma_f32 v[220:221], v[220:221], v[104:105], v[104:105] op_sel:[0,0,1] op_sel_hi:[1,0,1]\n " \
  "v_pk_fma_f32 v[222:223], v[222:223], v[104:105], v[104:105] op_sel:[0,0,1] op_sel_hi:[1,0,1]\n " \
  "v_pk_fma_f32 v[124:125], v[124:125], v[104:105], v[104:105] op_sel:[0,0,1] op_sel_hi:[1,0,1]\n " \
  "v_pk_fma_f32 v[126:127], v[126:127], v[104:105], v[104:105] op_sel:[0,0,1] op_sel_hi:[1,0,1]\n ds_write_b128 v55, v[224:227] offset:8192\n " \
  "v_pk_add_f32 v[216:217], v[172:173], v[174:175]\n v_pk_add_f32 v[218:219], v[80:81], v[82:83]\n v_pk_add_f32 v[216:217], v[216:217], v[218:219]\n " \
  "v_add_f32 v168, v216, v217\n v_pk_add_f32 v[216:217], v[200:201], v[202:203]\n v_pk_add_f32 v[218:219], v[204:205], v[206:207]\n " \
  "v_pk_add_f32 v[216:217], v[216:217], v[218:219]\n v_add_f32 v169, v216, v217\n v_pk_add_f32 v[216:217], v[196:197], v[198:199]\n " \
  "v_pk_add_f32 v[218:219], v[84:85], v[86:87]\n v_pk_add_f32 v[216:217], v[216:217], v[218:219]\n v_add_f32 v170, v216, v217\n " \
  "v_pk_add_f32 v[216:217], v[248:249], v[250:251]\n v_pk_add_f32 v[218:219], v[252:253], v[254:255]\n v_pk_add_f32 v[216:217], v[216:217], v[218:219]\n " \
  "v_add_f32 v171, v216, v217\n v_exp_f32 v128, v128\n v_exp_f32 v129, v129\n v_permlane16_swap_b32 v168, v169\n v_permlane16_swap_b32 v170, v171\n " \
  "v_exp_f32 v130, v130\n v_exp_f32 v131, v131\n v_add_f32 v168, v168, v169\n v_add_f32 v170, v170, v171\n v_exp_f32 v132, v132\n v_exp_f32 v133, v133\n " \
  "v_permlane32_swap_b32 v168, v170\n v_exp_f32 v134, v134\n v_exp_f32 v135, v135\n v_add_f32 v168, v168, v170\n v_exp_f32 v220, v220\n " \
  "v_exp_f32 v221, v221\n v_rcp_f32 v168, v168\n v_exp_f32 v222, v222\n v_exp_f32 v223, v223\n v_mov_b32 v170, v168\n v_exp_f32 v124, v124\n " \
  "v_exp_f32 v125, v125\n v_permlane32_swap_b32 v168, v170\n v_exp_f32 v126, v126\n v_exp_f32 v127, v127\n v_mov_b32 v169, v168\n v_mov_b32 v171, v170\n " \
  "v_pk_fma_f32 v[112:113], v[244:245], v[106:107], v[144:145] op_sel:[0,0,1] op_sel_hi:[1,0,1] neg_lo:[1,0,0] neg_hi:[1,0,0]\n " \
  "v_pk_fma_f32 v[114:115], v[246:247], v[106:107], v[144:145] op_sel:[0,0,1] op_sel_hi:[1,0,1] neg_lo:[1,0,0] neg_hi:[1,0,0]\n " \
  "v_permlane16_swap_b32 v168, v169\n v_permlane16_swap_b32 v170, v171\n " \
  "v_pk_fma_f32 v[116:117], v[108:109], v[106:107], v[144:145] op_sel:[0,0,1] op_sel_hi:[1,0,1] neg_lo:[1,0,0] neg_hi:[1,0,0]\n " \
  "v_pk_fma_f32 v[118:119], v[110:111], v[106:107], v[144:145] op_sel:[0,0,1] op_sel_hi:[1,0,1] neg_lo:[1,0,0] neg_hi:[1,0,0]\n " \
  "v_pk_fma_f32 v[244:245], v[244:245], v[106:107], v[106:107] op_sel:[0,0,1] op_sel_hi:[1,0,1]\n " \
  "v_pk_fma_f32 v[246:247], v[246:247], v[106:107], v[106:107] op_sel:[0,0,1] op_sel_hi:[1,0,1]\n " \
  "v_pk_fma_f32 v[108:109], v[108:109], v[106:107], v[106:107] op_sel:[0,0,1] op_sel_hi:[1,0,1]\n " \
  "v_pk_fma_f32 v[110:111], v[110:111], v[106:107], v[106:107] op_sel:[0,0,1] op_sel_hi:[1,0,1]\n ds_write_b128 v55, v[228:231] offset:9216\n " \
  "v_exp_f32 v112, v112\n v_pk_mul_f32 v[172:173], v[172:173], v[168:169] op_sel_hi:[1,0]\n v_exp_f32 v113, v113\n " \
  "v_pk_mul_f32 v[174:175], v[174:175], v[168:169] op_sel_hi:[1,0]\n v_exp_f32 v114, v114\n v_cvt_pk_bf16_f32 v172, v172, v173\n v_exp_f32 v115, v115\n " \
  "v_cvt_pk_bf16_f32 v173, v174, v175\n v_exp_f32 v116, v116\n ds_write_b64 v58, v[172:173] offset:0\n v_exp_f32 v117, v117\n " \
  "v_pk_mul_f32 v[80:81], v[80:81], v[168:169] op_sel_hi:[1,0]\n v_exp_f32 v118, v118\n v_pk_mul_f32 v[82:83], v[82:83], v[168:169] op_sel_hi:[1,0]\n " \
  "v_exp_f32 v119, v119\n v_cvt_pk_bf16_f32 v80, v80, v81\n v_exp_f32 v244, v244\n v_cvt_pk_bf16_f32 v81, v82, v83\n v_exp_f32 v245, v245\n " \
  "ds_write_b64 v58, v[80:81] offset:32\n v_exp_f32 v246, v246\n v_pk_mul_f32 v[200:201], v[200:201], v[168:169] op_sel:[0,1]\n v_exp_f32 v247, v247\n " \
  "v_pk_mul_f32 v[202:203], v[202:203], v[168:169] op_sel:[0,1]\n v_exp_f32 v108, v108\n v_cvt_pk_bf16_f32 v200, v200, v201\n v_exp_f32 v109, v109\n " \
  "v_cvt_pk_bf16_f32 v201, v202, v203\n v_exp_f32 v110, v110\n ds_write_b64 v58, v[200:201] offset:64\n v_exp_f32 v111, v111\n " \
  "v_pk_mul_f32 v[204:205], v[204:205], v[168:169] op_sel:[0,1]\n v_pk_mul_f32 v[206:207], v[206:207], v[168:169] op_sel:[0,1]\n " \
  "v_cvt_pk_bf16_f32 v204, v204, v205\n v_cvt_pk_bf16_f32 v205, v206, v207\n ds_write_b64 v58, v[204:205] offset:96\n " \
  "v_pk_add_f32 v[216:217], v[220:221], v[222:223]\n v_pk_add_f32 v[218:219], v[124:125], v[126:127]\n v_pk_add_f32 v[216:217], v[216:217], v[218:219]\n " \
  "v_add_f32 v192, v216, v217\n v_pk_add_f32 v[216:217], v[128:129], v[130:131]\n v_pk_add_f32 v[218:219], v[132:133], v[134:135]\n " \
  "v_pk_add_f32 v[216:217], v[216:217], v[218:219]\n v_add_f32 v193, v216, v217\n v_pk_add_f32 v[216:217], v[244:245], v[246:247]\n " \
  "v_pk_add_f32 v[218:219], v[108:109], v[110:111]\n v_pk_add_f32 v[216:217], v[216:217], v[218:219]\n v_add_f32 v194, v216, v217\n " \
  "v_pk_add_f32 v[216:217], v[112:113], v[114:115]\n v_pk_add_f32 v[218:219], v[116:117], v[118:119]\n v_pk_add_f32 v[216:217], v[216:217], v[218:219]\n " \
  "v_add_f32 v195, v216, v217\n v_pk_mul_f32 v[196:197], v[196:197], v[170:171] op_sel_hi:[1,0]\n " \
  "v_pk_mul_f32 v[198:199], v[198:199], v[170:171] op_sel_hi:[1,0]\n v_permlane16_swap_b32 v192, v193\n v_permlane16_swap_b32 v194, v195\n " \
  "v_cvt_pk_bf16_f32 v196, v196, v197\n v_cvt_pk_bf16_f32 v197, v198, v199\n v_add_f32 v192, v192, v193\n v_add_f32 v194, v194, v195\n " \
  "ds_write_b64 v58, v[196:197] offset:2304\n v_pk_mul_f32 v[84:85], v[84:85], v[170:171] op_sel_hi:[1,0]\n v_permlane32_swap_b32 v192, v194\n " \
  "v_pk_mul_f32 v[86:87], v[86:87], v[170:171] op_sel_hi:[1,0]\n v_cvt_pk_bf16_f32 v84, v84, v85\n v_add_f32 v192, v192, v194\n " \
  "v_cvt_pk_bf16_f32 v85, v86, v87\n ds_write_b64 v58, v[84:85] offset:2336\n v_rcp_f32 v192, v192\n " \
  "v_pk_mul_f32 v[248:249], v[248:249], v[170:171] op_sel:[0,1]\n v_pk_mul_f32 v[250:251], v[250:251], v[170:171] op_sel:[0,1]\n v_mov_b32 v194, v192\n " \
  "v_cvt_pk_bf16_f32 v248, v248, v249\n v_cvt_pk_bf16_f32 v249, v250, v251\n v_permlane32_swap_b32 v192, v194\n " \
  "ds_write_b64 v58, v[248:249] offset:2368\n v_pk_mul_f32 v[252:253], v[252:253], v[170:171] op_sel:[0,1]\n v_mov_b32 v193, v192\n " \
  "v_mov_b32 v195, v194\n v_pk_mul_f32 v[254:255], v[254:255], v[170:171] op_sel:[0,1]\n v_cvt_pk_bf16_f32 v252, v252, v253\n " \
  "v_permlane16_swap_b32 v192, v193\n v_permlane16_swap_b32 v194, v195\n v_cvt_pk_bf16_f32 v253, v254, v255\n ds_write_b64 v58, v[252:253] offset:2400\n " \
  "v_pk_mul_f32 v[220:221], v[220:221], v[192:193] op_sel_hi:[1,0]\n v_pk_mul_f32 v[222:223], v[222:223], v[192:193] op_sel_hi:[1,0]\n " \
  "v_cvt_pk_bf16_f32 v220, v220, v221\n v_cvt_pk_bf16_f32 v221, v222, v223\n ds_write_b64 v58, v[220:221] offset:18432\n " \
  "v_pk_mul_f32 v[124:125], v[124:125], v[192:193] op_sel_hi:[1,0]\n v_pk_mul_f32 v[126:127], v[126:127], v[192:193] op_sel_hi:[1,0]\n " \
  "v_cvt_pk_bf16_f32 v124, v124, v125\n v_cvt_pk_bf16_f32 v125, v126, v127\n ds_write_b64 v58, v[124:125] offset:18464\n " \
  "v_pk_mul_f32 v[128:129], v[128:129], v[192:193] op_sel:[0,1]\n v_pk_mul_f32 v[130:131], v[130:131], v[192:193] op_sel:[0,1]\n " \
  "v_cvt_pk_bf16_f32 v128, v128, v129\n v_cvt_pk_bf16_f32 v129, v130, v131\n ds_write_b64 v58, v[128:129] offset:18496\n " \
  "v_pk_mul_f32 v[132:133], v[132:133], v[192:193] op_sel:[0,1]\n v_pk_mul_f32 v[134:135], v[134:135], v[192:193] op_sel:[0,1]\n " \
  "v_cvt_pk_bf16_f32 v132, v132, v133\n v_cvt_pk_bf16_f32 v133, v134, v135\n ds_write_b64 v58, v[132:133] offset:18528\n " \
  "v_pk_mul_f32 v[244:245], v[244:245], v[194:195] op_sel_hi:[1,0]\n v_pk_mul_f32 v[246:247], v[246:247], v[194:195] op_sel_hi:[1,0]\n " \
  "v_cvt_pk_bf16_f32 v244, v244, v245\n v_cvt_pk_bf16_f32 v245, v246, v247\n ds_write_b64 v58, v[244:245] offset:20736\n " \
  "v_pk_mul_f32 v[108:109], v[108:109], v[194:195] op_sel_hi:[1,0]\n v_pk_mul_f32 v[110:111], v[110:111], v[194:195] op_sel_hi:[1,0]\n " \
  "v_cvt_pk_bf16_f32 v108, v108, v109\n v_cvt_pk_bf16_f32 v109, v110, v111\n ds_write_b64 v58, v[108:109] offset:20768\n " \
  "v_pk_mul_f32 v[112:113], v[112:113], v[194:195] op_sel:[0,1]\n v_pk_mul_f32 v[114:115], v[114:115], v[194:195] op_sel:[0,1]\n " \
  "v_cvt_pk_bf16_f32 v112, v112, v113\n v_cvt_pk_bf16_f32 v113, v114, v115\n ds_write_b64 v58, v[112:113] offset:20800\n " \
  "v_pk_mul_f32 v[116:117], v[116:117], v[194:195] op_sel:[0,1]\n v_pk_mul_f32 v[118:119], v[118:119], v[194:195] op_sel:[0,1]\n " \
  "v_cvt_pk_bf16_f32 v116, v116, v117\n v_cvt_pk_bf16_f32 v117, v118, v119\n ds_write_b64 v58, v[116:117] offset:20832\n "
#define G1_KV \
  "v_mov_b32 v144, 0x3f803f80\n v_mov_b32 v145, 0x3f803f80\n v_mov_b32 v146, 0x3f803f80\n v_mov_b32 v147, 0x3f803f80\n s_waitcnt lgkmcnt(0)\n " \
  "s_barrier\n ds_read_b64_tr_b16 v[92:93], v59 offset:0\n ds_read_b64_tr_b16 v[94:95], v59 offset:2304\n ds_read_b128 v[108:111], v60 offset:0\n " \
  "ds_read_b128 v[124:127], v60 offset:1024\n ds_read_b64_tr_b16 v[96:97], v59 offset:9216\n ds_read_b64_tr_b16 v[98:99], v59 offset:11520\n " \
  "ds_read_b128 v[112:115], v60 offset:4096\n ds_read_b128 v[128:131], v60 offset:5120\n ds_read_b64_tr_b16 v[100:101], v59 offset:18432\n " \
  "ds_read_b64_tr_b16 v[102:103], v59 offset:20736\n ds_read_b128 v[116:119], v60 offset:8192\n ds_read_b128 v[132:135], v60 offset:9216\n " \
  "ds_read_b64_tr_b16 v[104:105], v59 offset:27648\n ds_read_b64_tr_b16 v[106:107], v59 offset:29952\n ds_read_b128 v[120:123], v60 offset:12288\n " \
  "ds_read_b128 v[80:83], v60 offset:13312\n s_waitcnt lgkmcnt(14)\n v_mfma_f32_16x16x32_bf16 v[84:87], v[92:95], v[144:147], 0\n " \
  "s_waitcnt lgkmcnt(13)\n v_mfma_f32_16x16x32_bf16 v[152:155], v[92:95], v[108:111], 0\n s_waitcnt lgkmcnt(12)\n " \
  "v_mfma_f32_16x16x32_bf16 v[156:159], v[92:95], v[124:127], 0\n s_waitcnt lgkmcnt(10)\n " \
  "v_mfma_f32_16x16x32_bf16 v[84:87], v[96:99], v[144:147], v[84:87]\n s_waitcnt lgkmcnt(9)\n " \
  "v_mfma_f32_16x16x32_bf16 v[152:155], v[96:99], v[112:115], v[152:155]\n s_waitcnt lgkmcnt(8)\n " \
  "v_mfma_f32_16x16x32_bf16 v[156:159], v[96:99], v[128:131], v[156:159]\n s_waitcnt lgkmcnt(6)\n " \
  "v_mfma_f32_16x16x32_bf16 v[84:87], v[100:103], v[144:147], v[84:87]\n s_waitcnt lgkmcnt(5)\n " \
  "v_mfma_f32_16x16x32_bf16 v[152:155], v[100:103], v[116:119], v[152:155]\n s_waitcnt lgkmcnt(4)\n " \
  "v_mfma_f32_16x16x32_bf16 v[156:159], v[100:103], v[132:135], v[156:159]\n s_waitcnt lgkmcnt(2)\n " \
  "v_mfma_f32_16x16x32_bf16 v[84:87], v[104:107], v[144:147], v[84:87]\n s_waitcnt lgkmcnt(1)\n " \
  "v_mfma_f32_16x16x32_bf16 v[152:155], v[104:107], v[120:123], v[152:155]\n s_waitcnt lgkmcnt(0)\n " \
  "v_mfma_f32_16x16x32_bf16 v[156:159], v[104:107], v[80:83], v[156:159]\n s_nop 3\n v_cvt_pk_bf16_f32 v84, v84, v85\n v_cvt_pk_bf16_f32 v85, v86, v87\n " \
  "v_cvt_pk_bf16_f32 v152, v152, v153\n v_cvt_pk_bf16_f32 v153, v154, v155\n v_cvt_pk_bf16_f32 v154, v156, v157\n v_cvt_pk_bf16_f32 v155, v158, v159\n " \
  "v_min_u32 v152, -2, v152\n v_min_u32 v153, -2, v153\n v_min_u32 v154, -2, v154\n v_min_u32 v155, -2, v155\n " \
  "buffer_store_dwordx4 v[152:155], v61, %[rs], 0 offen sc1\n v_min_u32 v84, -2, v84\n v_min_u32 v85, -2, v85\n " \
  "buffer_store_dwordx2 v[84:85], v62, %[rs], 0 offen sc1\n "
#define G1_OUTPUT \
  "s_mov_b32 m0, 0x40000\n s_branch .Lg1p%=\n .Lg1q%=:\n s_sleep 1\n s_sub_u32 m0, m0, 1\n s_cbranch_scc1 .Lg1r%=\n .Lg1p%=:\n " \
  "buffer_load_dwordx4 v[216:219], %[fz], %[rs], 0 offen sc1\n buffer_load_dwordx4 v[220:223], %[fz], %[rs], 0 offen offset:64 sc1\n " \
  "buffer_load_dwordx4 v[200:203], %[fo], %[rs], 0 offen offset:0 sc1\n buffer_load_dwordx4 v[204:207], %[fo], %[rs], 0 offen offset:2048 sc1\n " \
  "buffer_load_dwordx4 v[208:211], %[fo], %[rs], 0 offen offset:1024 sc1\n buffer_load_dwordx4 v[212:215], %[fo], %[rs], 0 offen offset:3072 sc1\n " \
  "s_waitcnt vmcnt(5)\n v_max_u32 v152, v216, v217\n v_max3_u32 v152, v218, v219, v152\n s_waitcnt vmcnt(4)\n v_max3_u32 v152, v220, v221, v152\n " \
  "v_max3_u32 v152, v222, v223, v152\n s_waitcnt vmcnt(3)\n v_max3_u32 v152, v200, v201, v152\n v_max3_u32 v152, v202, v203, v152\n s_waitcnt vmcnt(2)\n " \
  "v_max3_u32 v152, v204, v205, v152\n v_max3_u32 v152, v206, v207, v152\n s_waitcnt vmcnt(1)\n v_max3_u32 v152, v208, v209, v152\n " \
  "v_max3_u32 v152, v210, v211, v152\n s_waitcnt vmcnt(0)\n v_max3_u32 v152, v212, v213, v152\n v_max3_u32 v152, v214, v215, v152\n " \
  "v_cmp_eq_u32 vcc, -1, v152\n s_cbranch_vccnz .Lg1q%=\n .Lg1r%=:\n v_mfma_f32_16x16x32_bf16 v[160:163], v[216:219], v[224:227], 0\n " \
  "v_mfma_f32_16x16x32_bf16 v[160:163], v[220:223], v[228:231], v[160:163]\n v_mfma_f32_16x16x32_bf16 v[152:155], v[200:203], v[224:227], 0\n " \
  "v_mfma_f32_16x16x32_bf16 v[152:155], v[204:207], v[228:231], v[152:155]\n v_mfma_f32_16x16x32_bf16 v[156:159], v[208:211], v[224:227], 0\n " \
  "v_mfma_f32_16x16x32_bf16 v[156:159], v[212:215], v[228:231], v[156:159]\n v_mfma_f32_16x16x32_bf16 v[172:175], v[216:219], v[232:235], 0\n " \
  "v_mfma_f32_16x16x32_bf16 v[172:175], v[220:223], v[236:239], v[172:175]\n v_mfma_f32_16x16x32_bf16 v[164:167], v[200:203], v[232:235], 0\n " \
  "v_mfma_f32_16x16x32_bf16 v[164:167], v[204:207], v[236:239], v[164:167]\n v_mfma_f32_16x16x32_bf16 v[168:171], v[208:211], v[232:235], 0\n " \
  "v_add_f32 v160, 0x358637bd, v160\n v_rcp_f32 v160, v160\n v_mfma_f32_16x16x32_bf16 v[168:171], v[212:215], v[236:239], v[168:171]\n " \
  "v_mul_f32 v152, v160, v152\n v_mfma_f32_16x16x32_bf16 v[184:187], v[216:219], v[240:243], 0\n v_mul_f32 v153, v160, v153\n " \
  "v_cvt_pk_bf16_f32 v152, v152, v153\n v_mul_f32 v154, v160, v154\n v_mfma_f32_16x16x32_bf16 v[184:187], v[220:223], v[244:247], v[184:187]\n " \
  "v_mul_f32 v155, v160, v155\n v_cvt_pk_bf16_f32 v153, v154, v155\n v_mul_f32 v156, v160, v156\n " \
  "v_mfma_f32_16x16x32_bf16 v[176:179], v[200:203], v[240:243], 0\n v_mul_f32 v157, v160, v157\n v_cvt_pk_bf16_f32 v154, v156, v157\n " \
  "v_mul_f32 v158, v160, v158\n v_mfma_f32_16x16x32_bf16 v[176:179], v[204:207], v[244:247], v[176:179]\n v_mul_f32 v159, v160, v159\n " \
  "v_cvt_pk_bf16_f32 v155, v158, v159\n buffer_store_dwordx4 v[152:155], %[go], %[rs], 0 offen offset:0 sc1\n " \
  "v_mfma_f32_16x16x32_bf16 v[180:183], v[208:211], v[240:243], 0\n ds_write_b128 %[lo], v[152:155] offset:0\n v_add_f32 v172, 0x358637bd, v172\n " \
  "v_mfma_f32_16x16x32_bf16 v[180:183], v[212:215], v[244:247], v[180:183]\n v_rcp_f32 v172, v172\n " \
  "v_mfma_f32_16x16x32_bf16 v[196:199], v[216:219], v[248:251], 0\n v_mul_f32 v164, v172, v164\n v_mul_f32 v165, v172, v165\n " \
  "v_cvt_pk_bf16_f32 v164, v164, v165\n v_mfma_f32_16x16x32_bf16 v[196:199], v[220:223], v[252:255], v[196:199]\n v_mul_f32 v166, v172, v166\n " \
  "v_mul_f32 v167, v172, v167\n v_cvt_pk_bf16_f32 v165, v166, v167\n v_mfma_f32_16x16x32_bf16 v[188:191], v[200:203], v[248:251], 0\n " \
  "v_mul_f32 v168, v172, v168\n v_mul_f32 v169, v172, v169\n v_cvt_pk_bf16_f32 v166, v168, v169\n " \
  "v_mfma_f32_16x16x32_bf16 v[188:191], v[204:207], v[252:255], v[188:191]\n v_mul_f32 v170, v172, v170\n v_mul_f32 v171, v172, v171\n " \
  "v_cvt_pk_bf16_f32 v167, v170, v171\n v_mfma_f32_16x16x32_bf16 v[192:195], v[208:211], v[248:251], 0\n " \
  "buffer_store_dwordx4 v[164:167], %[go], %[rs], 0 offen offset:1024 sc1\n ds_write_b128 %[lo], v[164:167] offset:1024\n " \
  "v_mfma_f32_16x16x32_bf16 v[192:195], v[212:215], v[252:255], v[192:195]\n v_add_f32 v184, 0x358637bd, v184\n v_rcp_f32 v184, v184\n s_nop 0\n " \
  "v_pk_mul_f32 v[176:177], v[176:177], v[184:185] op_sel_hi:[1,0]\n v_pk_mul_f32 v[178:179], v[178:179], v[184:185] op_sel_hi:[1,0]\n " \
  "v_pk_mul_f32 v[180:181], v[180:181], v[184:185] op_sel_hi:[1,0]\n v_pk_mul_f32 v[182:183], v[182:183], v[184:185] op_sel_hi:[1,0]\n " \
  "v_cvt_pk_bf16_f32 v176, v176, v177\n v_cvt_pk_bf16_f32 v177, v178, v179\n v_cvt_pk_bf16_f32 v178, v180, v181\n v_cvt_pk_bf16_f32 v179, v182, v183\n " \
  "buffer_store_dwordx4 v[176:179], %[go], %[rs], 0 offen offset:2048 sc1\n ds_write_b128 %[lo], v[176:179] offset:2048\n " \
  "v_add_f32 v196, 0x358637bd, v196\n v_rcp_f32 v196, v196\n s_nop 0\n v_pk_mul_f32 v[188:189], v[188:189], v[196:197] op_sel_hi:[1,0]\n " \
  "v_pk_mul_f32 v[190:191], v[190:191], v[196:197] op_sel_hi:[1,0]\n v_pk_mul_f32 v[192:193], v[192:193], v[196:197] op_sel_hi:[1,0]\n " \
  "v_pk_mul_f32 v[194:195], v[194:195], v[196:197] op_sel_hi:[1,0]\n v_cvt_pk_bf16_f32 v188, v188, v189\n v_cvt_pk_bf16_f32 v189, v190, v191\n " \
  "v_cvt_pk_bf16_f32 v190, v192, v193\n v_cvt_pk_bf16_f32 v191, v194, v195\n buffer_store_dwordx4 v[188:191], %[go], %[rs], 0 offen offset:3072 sc1\n " \
  "ds_write_b128 %[lo], v[188:191] offset:3072\n "
#define G1_PURGE \
  ".purgem G1M\n .purgem G1RA\n .purgem G1RB\n .purgem G1BK\n .purgem G1AK\n .purgem G1RDB\n .purgem G1RDA\n .purgem G1Q\n .purgem G1SG\n "
#define G1_CLOBBER \
  "v80", "v81", "v82", "v83", "v84", "v85", "v86", "v87", "v92", "v93", "v94", "v95", "v96", "v97", "v98", "v99", "v100", "v101", "v102", "v103", \
  "v104", "v105", "v106", "v107", "v108", "v109", "v110", "v111", \
  "v112", "v113", "v114", "v115", "v116", "v117", "v118", "v119", "v120", "v121", "v122", "v123", "v124", "v125", "v126", "v127", "v128", "v129", \
  "v130", "v131", "v132", "v133", "v134", "v135", "v144", "v145", "v146", "v147", "v152", "v153", "v154", "v155", \
  "v156", "v157", "v158", "v159"

template <int MODE>
__device__ __forceinline__ void g1_phase(char* smem, char* ws, u64& E, int w, f32x16& xq, Next& nx, int layer, const u16* __restrict__ x0, const u16* __restrict__ W,
                                         const u16* __restrict__ wo, const u16* __restrict__ wphi,
                                         const u16* __restrict__ wn1, const u16* __restrict__ wn2) {
  constexpr bool HH = (MODE == 1);
  constexpr int ABYTES = 128 * 128;
  constexpr int STAGE = ABYTES + 3 * 8192;
  constexpr int OFF_FX = 16384;
  constexpr int OFF_SP = 3 * STAGE;
  constexpr int OFF_RS = !HH ? 159744 : OFF_SP + 16384;
  constexpr int OFF_CS = OFF_RS + (HH ? 1152 : 1024);
  constexpr int OFF_WP = OFF_CS + 1536;
  constexpr int OFF_JK = LDS_TOTAL - 1024;
  constexpr int OFF_PK = 0;
  constexpr int OFF_VF = OFF_WP + 4096;
  constexpr int OFF_R2 = HH ? OFF_VF + 16384 : OFF_CS + 1024;
  constexpr int OFF_RD = OFF_CS + 768;
  constexpr int PSTR = 144;
  IDS
  W = uni(W); wphi = uni(wphi); wn1 = uni(wn1); wn2 = uni(wn2);
  const int kg = w >> 2, wm = (w >> 1) & 1, wn = w & 1;
  const bool first = !HH && layer == 0;
  f32x4 acc[2][2], qh[2][2], csq[2], st01, st23;
  bf16x8 wf[2];
  u64 kol;
  u32x4 pw[8];
  ST(layer * 24 + 0)
  const int R0 = wm * 64;
  u32x16 pa;
#define pcs pa[0]
#define prs pa[1]
#define opo pa[2]
#define ogo pa[3]
#define hof pa[7]
#define dm pa[8]
#define dm2 pa[9]
#define pcv pa[4]
#define pwf pa[5]
#define prv pa[6]
#define pvf pa[7]
#define ppk pa[10]
#define ppa pa[11]
#define pvr pa[12]
#define gkv pa[13]
#define gkz pa[14]
#define pwl pa[15]
#define pwm pa[4]
#define plb pa[10]
#define prd pa[11]
#define dm3 pa[12]
  f32x4 bias[3];
  const char* wos;
  unsigned tO;
  const unsigned sb = lds_addr(smem);
  auto prep = [&](auto ldr) {
    constexpr bool LDR = decltype(ldr)::value;
    if constexpr (!LDR) {
      pcs = sb + OFF_CS + wn * 128 + g * 16;
      prs = sb + OFF_RS + (R0 + fr) * 8;
      opo = (unsigned)(wn * 8192 + R0 * 64 + lane * 16);
      ogo = opo + WS_O + layer * (SEQ * DM * 2) + (unsigned)((rt * NH + 2 * hp) * 8192);
      opo += sb;
      pcv = sb + OFF_CS + (wn * 32 + 128 + fr) * 4;
      prv = sb + OFF_R2 + (R0 + 4 * g) * 4;
    } else {
      constexpr unsigned kvn = WS_KV + MODE * KVLB;
      const int t = tid & 255;
      dm = kvn + (unsigned)((rt * NH + 2 * hp) * KVPB) + t * 16;
      dm2 = t < 16 ? dm + 8192 : t >= 64 && t < 97 ? kvn + 16u * NH * KVPB + (unsigned)(hp * KVFB + rt * 528 + (t - 64) * 16) : 0x7fff0000u;
      if (!HH) dm3 = WS_HL + (unsigned)((256 + rt * 16 + hp) * 4096 + t * 16);
    }
    if constexpr (!HH) {
      if constexpr (!LDR) {
        prd = sb + OFF_RD + wn * 4;
        plb = sb + OFF_FX + wn * 2048 + lane * 16;
        hof = m0 + wm == 0 ? 0x7fff0000u : WS_HL + (unsigned)((layer * 128 + rt * 16 - 16 + wm * 16 + hp) * 4096 + wn * 2048 + lane * 16);
        const unsigned out = ~((0x5555u << 2 << fr) >> (4 * g));
#pragma unroll
        for (int i = 0; i < 8; ++i) {
          unsigned t;
          asm("v_bfe_i32 %0, %1, %2, 1" : "=v"(t) : "v"(out), "n"((i >> 2) * 16 + (i & 3)));
          bias[i >> 2][i & 3] = __uint_as_float(t & 0xff61b1e6u);
        }
        bias[2] = m0 + R0 == 0 ? f32x4{-3.0e38f, -3.0e38f, -3.0e38f, -3.0e38f} : bias[0];
        asm volatile("" : "+{v[36:39]}"(bias[0]), "+{v[40:43]}"(bias[1]), "+{v[44:47]}"(bias[2]));
      }
    } else {
      const int u = wm * 2 + kg;
      if constexpr (!LDR) {
        pwf = sb + OFF_WP + wn * 2048 + (4 * g + q4) * 64 + p4 * 8;
        pvf = sb + OFF_VF + w * 2048 + lane * 16;
        ppk = sb + OFF_PK + w * (32 * PSTR) + fr * PSTR + g * 8;
      }
      ppa = sb + OFF_PK + wn * (32 * PSTR) + (4 * g + q4) * PSTR + u * 32 + p4 * 8;
      pvr = sb + OFF_VF + wn * 2048 + lane * 16;
      const unsigned doff = WS_KV + (layer >> 1) * KVLB + (unsigned)((rt * NH + 2 * hp + wn) * KVPB);
      gkv = doff + u * 1024 + lane * 16;
      gkz = fr == 0 ? doff + 4096 + u * 32 + g * 8 : 0x7fff0000u;
    }
    asm volatile("" : "+{v[48:63]}"(pa));
  };

  if (kg == 1) {

    const int lw = w & 3;
    const int kB = 16 * lw + (lane >> 3);
    const int fB = (kB >> 1) & 1;
    const int cB0 = ((((lane & 7) >> 1) ^ fB) << 1) | (lane & 1), cB1 = ((((lane & 7) >> 1) ^ (fB | 2)) << 1) | (lane & 1);
    const bool im = img(layer, hp);

    unsigned vW0, vW1, vP, vX, vY;
    u64 ko2;
    auto ld_pw = [&](int i0) {
#pragma unroll
      for (int i = i0; i < i0 + 4; ++i) gld16(pw[i], vP, wn1 + i * 8 * 3072);
    };
    auto wo_tile = [&](int t) { glds2v(wos + knib(ko2, t) * tO, vW0, vW1, lds_addr(smem) + OFF_WO + t * 8192 + lw * 2048); };
    auto run = [&](auto fst) {
      constexpr bool FIRST = decltype(fst)::value;
      constexpr int N = 10;
      const unsigned vA = FIRST ? (unsigned)(((m0 + lw * 32 + (lane >> 3)) * DM + ((lane & 7) ^ (lane >> 3)) * 8) * 2)
                                : (unsigned)(rt * 262144 + lw * 4096 + lane * 16);
      unsigned imv = !FIRST && im ? ~0u : 0u;
      if (!FIRST) asm volatile("" : "+v"(imv));
      const unsigned vB0 = ((unsigned)(lw * 6144 + lane * 16) & imv) | ((unsigned)((kB * 3072 + hp * 64 + cB0 * 8) * 2) & ~imv);
      const unsigned vB1 = ((unsigned)(lw * 6144 + lane * 16) & imv) | (((unsigned)(((kB + 8) * 3072 + hp * 64 + cB1 * 8) * 2) - 1024u) & ~imv);
      const char* xa = FIRST ? (const char*)x0 : ws + WS_X + (layer - 1) * (SEQ * DM * 2);
      constexpr int KS = FIRST ? 128 : 16384;
      const u64 kord = FIRST ? korder0(rt) : nx.kord;
      auto ktile = [&](int ks) { return knib(kord, ks); };
      auto stageA = [&](int ks, int boff) {
        const int kt = ktile(ks);
        const unsigned sA = lds_addr(smem) + boff;
        if (FIRST) glds4v(xa + kt * KS, vA, vA + 15360u, vA + 2 * 15360u, vA + 3 * 15360u, sA + lw * 4096);
        else glds4c(xa + kt * KS, vA, sA + lw * 4096);
      };
      auto stageB = [&](const u16* Wp, int ks, int boff) {
        const int kt = ktile(ks);
        const unsigned sB = lds_addr(smem) + boff + ABYTES;
        const char* b = FIRST ? (const char*)(Wp + (size_t)kt * 64 * 3072) : nx.wq + kt * nx.tw;
        glds4v(b, vB0, vB1, vB0, vB1, sB + lw * 6144);
        glds2v(b + 4096, vB0, vB1, sB + lw * 6144 + 4096);
      };
      bf16x8 sel[3];
      unsigned cso[3], cst;
      if (FIRST) {
        WAITVN(N);
        BAR;
        stageA(2, 2 * STAGE); stageB(W, 2, 2 * STAGE);
        __builtin_amdgcn_sched_barrier(0);
        const int fl = (q4 >> 1) | ((g & 1) << 1), j = 3 * wm + (fr < 3 ? fr : 0);
#pragma unroll
        for (int i = 0; i < 3; ++i) {
          const unsigned o = fr == i ? 0x3F803F80u : 0u;
          sel[i] = __builtin_bit_cast(bf16x8, u32x4{o, o, o, o});
          cso[i] = (unsigned)(2 * STAGE + ABYTES + (g >> 1) * 6144 + (g & 1) * 1024 + q4 * 128 + p4 * 8 + (((wn * 2 + ((wm + i) & 1)) ^ fl) << 5) + ((3 * wm + i) >> 1) * 2048);
          asm volatile("" : "+v"(sel[i]), "+v"(cso[i]));
        }
        cst = fr < 3 ? WS_CPA + (unsigned)(((hp * 16 + rt) * 192 + (j >> 1) * 64 + wn * 32 + (j & 1) * 16 + 4 * g) * 4) : 0x7fff0000u;
        asm volatile("" : "+v"(cst));
      }
      u64 fall, fal2, fal3, fsv = E + (unsigned)(layer + 1);
      unsigned fao, fa2, vS, vC, dS, dC, dR;
      auto setup = [&]() {
        vP = (unsigned)((rt * 64 + (lane >> 3)) * 6144 + lw * 2048 + ((hp + (1 << lw)) & 15) * 128 + (lane & 7) * 16);
        vS = FIRST ? (unsigned)(WS_G0 + (m0 + lw * 32 + (lane & 31)) * 16)
                   : WS_ST + (unsigned)((layer - 1) * (SEQ * 128) + rt * 16384 + g * 1024 + lw * 256 + fr * 16);
        vC = (FIRST ? WS_CPA : WS_CSP) + (unsigned)(hp * 12288 + g * 768 + lw * 192 + (fr < 12 ? fr : 0) * 16);
        if (!FIRST) vX = HH ? (unsigned)(hp * 4096 + lw * 1024 + lane * 16) : (unsigned)((rt * 16 + hp) * 1024 + lane * 16);
        vW0 = ((unsigned)(lw * 2048 + lane * 16) & imv) | ((unsigned)((kB * DM + hp * 64 + ((lane & 7) ^ ((lane >> 4) << 1)) * 8) * 2) & ~imv); vW1 = vW0 + ((8 * DM * 2 - 1024u) & ~imv);
        dS = OFF_RS + lw * 256 + (FIRST ? (lane & 31) * 8 : fr * 16 + (g & 1) * 8); dC = OFF_CS + lw * 192 + (fr < 12 ? fr : 0) * 16 + g * 4;
        ko2 = FIRST ? korder(bid & 3, hp) : nx.ko2;
        wos = FIRST ? (const char*)wo : wo_src(ws, wo, layer, hp); tO = !FIRST && im ? 8192u : 131072u;
        asm volatile("" : "+s"(wos), "+s"(tO));
        dR = OFF_R2 + lw * 128 + (FIRST ? (lane & 31) * 4 : fr * 8 + (g & 1) * 4);
        asm volatile("" : "+v"(dR));
        asm volatile("" : "+v"(vS), "+v"(vX), "+v"(vW0), "+v"(vW1), "+v"(dS));
        asm volatile("" : "+s"(ko2));
        if (FIRST || HH) asm volatile("" : "+v"(vC), "+v"(dC), "+v"(fa2));
        if (FIRST) asm volatile("" : "+v"(vP));
      };
      if (!FIRST) {
        fao = (unsigned)((256 + rt * 16 + (lane & 15)) * FLS);
        if (HH) { vY = (unsigned)(bid * 8192 + lw * 2048 + lane * 16); fa2 = (unsigned)((256 + (lane & 15) * 16 + ((hp - ((1 << (lane >> 4)) >> 1)) & 15)) * FLS); asm volatile("" : "+v"(vY), "+v"(fa2)); }
        asm volatile("" : "+v"(fsv), "+v"(fao));
      }
      u32x4 sv[4], cv[4];
      auto ld_c = [&](u32x4& d, unsigned v, int so) { asm volatile("buffer_load_dwordx4 %0, %1, %2, %3 offen sc1" : "=v"(d) : "v"(v), "s"(rsW), "s"(so) : "memory"); };
      float tm, tr;
      unsigned tc;
      auto fin = [&]() {
        if (FIRST) {
          const unsigned tag = (unsigned)E + 1u;
          asm volatile("" : "+v"(sv[0]));
          _Pragma("unroll 1") for (unsigned spins = 0; spins < (1u << 18); ++spins) {
            if (__all(sv[0][1] == tag && sv[0][3] == tag)) break;
            __builtin_amdgcn_s_sleep(1);
            asm volatile("buffer_load_dwordx4 %0, %1, %2, 0 offen sc1\n\ts_waitcnt vmcnt(0)" : "=v"(sv[0]) : "v"(vS), "s"(rsW) : "memory");
          }
          tm = __uint_as_float(sv[0][0]); tr = __uint_as_float(sv[0][2]);
        } else {
          asm volatile("" : "+v"(sv[0]), "+v"(sv[1]), "+v"(sv[2]), "+v"(sv[3]));
          const f32x4 t = sum4(sv);
          const unsigned u = xs4(t[0], t[2], t[1], t[3]);
          auto h = __builtin_amdgcn_permlane32_swap(u, u, false, false);
          const float mean = __uint_as_float(h[0]) * (1.0f / 1024.0f);
          const float var = fmaxf(__uint_as_float(h[1]) * (1.0f / 1024.0f) - mean * mean, 0.f);
          tm = -mean; tr = __builtin_amdgcn_rsqf(var + 1e-5f);
        }
      };
      auto cfin = [&]() {
        if (FIRST || HH) {
          asm volatile("" : "+v"(cv[0]), "+v"(cv[1]), "+v"(cv[2]), "+v"(cv[3]));
          const f32x4 t = sum4(cv);
          tc = xs4(t[0], t[1], t[2], t[3]);
        }
      };
      bf16x8 cf[6];
      f32x4 csum = {0, 0, 0, 0};
      if (FIRST) {
        asm volatile("s_load_dwordx2 %0, %1, 0x0\n\ts_waitcnt lgkmcnt(0)" : "=s"(E) : "s"(ws) : "memory");
        fall = E + 1u; fa2 = (unsigned)((512 + (lane & 15) * 16 + hp) * FLS); vX = (unsigned)(bid * 8192 + lw * 2048 + lane * 16);
        asm volatile("" : "+v"(fall), "+v"(fa2), "+v"(vX));
      }
#pragma unroll
      for (int kt = 0; kt < 13; ++kt) {
        const int cur = (kt % 3) * STAGE;
        if (!FIRST && kt == 0) { WAITV(4); }
        else if (FIRST && kt == 4 && lw == 1) { WAITVN(N + 2); }
        else if (HH && (kt == 4 || kt == 6)) { WAITVN(N + 1); }
        else if (HH && kt == 5) { WAITVN(N + 2); }
        else if ((!FIRST && kt == 1 && lw == 3) || (!FIRST && kt == 9 && lw == 2) || ((FIRST || HH) && kt == 10 && lw == 3) || (FIRST && (kt == 2 || kt == 4 || kt == 5))) { WAITVN(N + 1); }
        else { WAITVN(N); }
        if (!FIRST && kt == 2 && lw == 3) {
          asm volatile("" : "+v"(fall));
          if (__any(fall != E + (unsigned)layer)) poll_flags(ws, E, 1, rt, hp, lane, layer, false);
        }
        if (!FIRST && kt == 10 && lw == 2) {
          asm volatile("" : "+v"(fal3));
          if (__any(fal3 != E + (unsigned)(layer + 1))) poll_flags(ws, E, 2, rt, hp, lane, layer + 1, false);
        }
        if (HH && kt == 11 && lw == 3) {
          asm volatile("" : "+v"(fal2));
          if (__any(fal2 != E + (unsigned)layer)) {
            _Pragma("unroll 1") for (int j = 0; j < 4; ++j) poll_flags(ws, E, 1, rt, (hp - ((1 << j) >> 1)) & 15, lane, layer, true);
          }
        }
        if (FIRST && kt == 11 && lw == 3) {
          asm volatile("" : "+v"(fal2));
          if (__any(fal2 != E + 1u)) poll_flags(ws, E, 2, rt, hp, lane, 1u, true);
        }
        BAR;
        if (FIRST && kt == 1) {
          asm volatile("" ::: "memory");
#pragma unroll
          for (int i = 0; i < 6; ++i) { const char* t = smem + cso[i >> 1] + (i & 1) * 12288; cf[i] = cat8(tr_read(t), tr_read(t + 512)); }
        }
        stageA(kt + 3, cur);
        if (FIRST && kt == 1) {
          asm volatile("" : "+v"(sel[0]));
#pragma unroll
          for (int i = 0; i < 6; ++i) csum = MFMA(cf[i], sel[i >> 1], csum);
          asm volatile("" : "+v"(csum));
        }
        stageB(W, kt + 3, cur);
        if (FIRST) {
          if (kt == 1) __builtin_amdgcn_raw_buffer_store_b128(__builtin_bit_cast(u32x4, csum), rsW, cst, 0, 16);
          if (kt == 3 && tid == 320) __builtin_amdgcn_raw_buffer_store_b64(__builtin_bit_cast(u32x2, fall), rsW, WS_FLAGS + (unsigned)(512 + rt * 16 + hp) * FLS, 0, 16);
          if (kt == 3 || kt == 4) glds16s(wo + (kt - 3) * 512, vX, lds_addr(smem) + OFF_JK);
        }
        if (HH && (kt == 3 || kt == 4)) glds16s(wo + (kt - 3) * 512, vY, lds_addr(smem) + OFF_JK);
        if (!FIRST && kt == 3 && tid == 320) __builtin_amdgcn_raw_buffer_store_b64(__builtin_bit_cast(u32x2, fsv), rsW, WS_FLAGS + (unsigned)(512 + rt * 16 + hp) * FLS, 0, 16);
        if (!FIRST && kt == 8 && lw == 2) asm volatile("global_load_dwordx2 %0, %1, %2 sc1" : "=v"(fal3) : "v"(fao), "s"(ws + WS_FLAGS + 256 * FLS) : "memory");
        if (!FIRST && kt == 0 && lw == 3) asm volatile("global_load_dwordx2 %0, %1, %2 sc1" : "=v"(fall) : "v"(fao), "s"(ws + WS_FLAGS) : "memory");
        if ((FIRST || HH) && kt == 9 && lw == 3) asm volatile("global_load_dwordx2 %0, %1, %2 sc1" : "=v"(fal2) : "v"(fa2), "s"(ws + WS_FLAGS) : "memory");
      }
      setup();
      WAITVN(N);
      BAR;
#pragma unroll
      for (int i = 0; i < (FIRST ? 1 : 4); ++i) ld_c(sv[i], vS, i * 4096);
      if (FIRST || HH) {
#pragma unroll
        for (int i = 0; i < 4; ++i) ld_c(cv[i], vC, i * 3072);
      }
      if (HH) glds16s(wphi, vX, lds_addr(smem) + OFF_WP + lw * 1024);
      else if (!FIRST) glds16c(ws + WS_CSA, vX, lds_addr(smem) + OFF_CS);
      wo_tile(0); wo_tile(1);
      WAITVN(HH ? 13 : 9);
      BAR;
      if (FIRST) {
        if (lw < 3) { ld_pw(0); ld_pw(4); WAITV(12); } else { WAITV(4); }
      } else {
        if (HH) prep(IC<1>{});
        WAITV(4);
      }
      cfin();
      fin();
      asm volatile("" : "+v"(tm), "+v"(tr));
      if (FIRST || HH) asm volatile("" : "+v"(tc));
      if (FIRST && lw < 3) { WAITV(8); } else { WAITV(0); }
      lds<f32x2>(sb + dS) = f32x2{tm, tr};
      lds<float>(sb + dR) = tm; lds<float>(sb + dR + 512) = tr;
      if (FIRST || HH) lds<unsigned>(sb + dC) = tc;
      if (!HH) lds<float>(sb + OFF_RD + wn * 4) = zero1();
      WAITL0;
    };
    typedef std::integral_constant<bool, true> T_;
    typedef std::integral_constant<bool, false> F_;
    __builtin_amdgcn_s_setprio(3);
    if (first) run(T_{}); else run(F_{});
    __builtin_amdgcn_s_setprio(0);
    BAR;
    if constexpr (HH) {
      u32x4 c0, c1;
      if (layer == 1) {
        const int n = bid * 8 + lw * 2;
        const void* p;
        unsigned a0, a1;
        if (n < 1536) {
          const int r = n % 768, q = r % 384, e = q % 24;
          p = n < 768 ? wn1 : W;
          a0 = (unsigned)((((q / 24) * 64 + (e / 6) * 16 + (lane >> 3)) * 3072 + ((e % 6) >> 1) * 1024 + (r < 384 ? 192 : 704)) * 2);
          a1 = a0 + 8 * 6144 + cB1 * 16; a0 += cB0 * 16;
        } else {
          p = n < 1792 ? wn2 : wo;
          a0 = (unsigned)((((n & 127) * 8 + (lane >> 3)) * DM + ((n & 128) ? 704 : 192) + ((lane & 7) ^ ((lane >> 4) << 1)) * 8) * 2);
          a1 = a0 + 8 * DM * 2;
        }
        p = uni(p);
        gld16(c0, a0, p); gld16(c1, a1, p);
        const u32x4 pz = empty4();
        __builtin_amdgcn_raw_buffer_store_b128(pz, rsW, dm, 0, 16);
        __builtin_amdgcn_raw_buffer_store_b128(pz, rsW, dm, 4096, 16);
        __builtin_amdgcn_raw_buffer_store_b128(pz, rsW, dm2, 0, 16);
      }
#pragma unroll
      for (int t = 2; t < 6; ++t) wo_tile(t);
      if (layer == 1) {
        WAITV(11);
        __builtin_amdgcn_sched_barrier(0);
        asm volatile("" : "+v"(c0), "+v"(c1));
        __builtin_amdgcn_raw_buffer_store_b128(c0, rsW, WS_IMG + (unsigned)(bid * 8192 + lw * 2048 + lane * 16), 0, 16);
        __builtin_amdgcn_raw_buffer_store_b128(c1, rsW, WS_IMG + (unsigned)(bid * 8192 + lw * 2048 + 1024 + lane * 16), 0, 16);
        WAITV(0);
      }
      pwl = vW0; pwm = vW1; kol = ko2;
      asm volatile("" : "=v"(csq[0]), "=v"(csq[1]), "=v"(st01), "=v"(st23), "=v"(wf[0]), "=v"(wf[1]));
      asm volatile("" : "=v"(acc[0][0]), "=v"(acc[0][1]), "=v"(acc[1][0]), "=v"(acc[1][1]));
      asm volatile("" : "=v"(qh[0][0]), "=v"(qh[0][1]), "=v"(qh[1][0]), "=v"(qh[1][1]));
    } else {
      const int lane = lane_id();
#pragma unroll
      for (int t = 2; t < 12; ++t) {
        wo_tile(t);
        if (t == 2 && first) {
        prep(IC<1>{});
        const u32x4 pz = empty4();
        __builtin_amdgcn_raw_buffer_store_b128(pz, rsW, dm, 0, 16);
        __builtin_amdgcn_raw_buffer_store_b128(pz, rsW, dm, 4096, 16);
        __builtin_amdgcn_raw_buffer_store_b128(pz, rsW, dm3, 0, 16);
        __builtin_amdgcn_raw_buffer_store_b128(pz, rsW, dm2, 0, 16);
        if (lw == 3 && lane < 48) __builtin_amdgcn_raw_buffer_store_b128(lds<u32x4>(sb + OFF_CS + lane * 16), rsW, WS_CSA + (unsigned)((rt * 16 + hp) * 1024 + lane * 16), 0, 16);
        }
      }
      if (first) { BAR;   WAITV(0); }
    }
    asm volatile("" : "={v[64:79]}"(xq));
  } else {

    prep(IC<0>{});
    if (first) {
      const int row = m0 + hp * 8 + w * 2 + (lane >> 5);
      asm volatile("s_load_dwordx2 %0, %1, 0x0\n\ts_waitcnt lgkmcnt(0)" : "=s"(E) : "s"(ws) : "memory");
      const unsigned tag = (unsigned)E + 1u;
      static_assert(HH || (OFF_SP == 122880 && OFF_R2 + 1024 <= OFF_JK), "addresses used at the top of the kernel");
      WAITV(0);
      u32x4 xs[4];
#pragma unroll
      for (int i = 0; i < 4; ++i) xs[i] = *(const u32x4*)(smem + OFF_SP + w * 4096 + (lane >> 5) * 2048 + (lane & 31) * 16 + i * 512);
      typedef __attribute__((ext_vector_type(2))) __bf16 bf2;
      const bf2 one2 = __builtin_bit_cast(bf2, 0x3F803F80u);
      float sa[2] = {zero1(), zero1()}, sq[2] = {zero1(), zero1()};
#pragma unroll
      for (int i = 0; i < 16; ++i) {
        const unsigned u = xs[i >> 2][i & 3];
        const bf2 t = __builtin_bit_cast(bf2, u);
        sa[i & 1] = __builtin_amdgcn_fdot2_f32_bf16(t, one2, sa[i & 1], false);
        sq[i & 1] = __builtin_amdgcn_fdot2_f32_bf16(t, t, sq[i & 1], false);
      }
      float sm = r16_sum(sa[0] + sa[1]), m2 = r16_sum(sq[0] + sq[1]);
      {
        auto r = __builtin_amdgcn_permlane16_swap(__float_as_uint(sm), __float_as_uint(sm), false, false);
        auto q = __builtin_amdgcn_permlane16_swap(__float_as_uint(m2), __float_as_uint(m2), false, false);
        sm = __uint_as_float(r[0]) + __uint_as_float(r[1]); m2 = __uint_as_float(q[0]) + __uint_as_float(q[1]);
      }
      const float mean = sm * (1.0f / 1024.0f);
      m2 = fmaxf(m2 - sm * mean, 0.f);
      u32x4 gw = {__float_as_uint(-mean), tag, __float_as_uint(rsqrtf(m2 * (1.0f / 1024.0f) + 1e-5f)), tag};
      if ((lane & 31) == 0) __builtin_amdgcn_raw_buffer_store_b128(gw, rsW, (unsigned)(WS_G0 + row * 16), 0, 16);
      const u64 ko = korder(bid & 3, hp);
      nx.kf = kfirst(ko) | (kfirst(ko >> 16) | (1u << knib(ko, 9))) << 16;
      asm volatile("" : "+s"(nx.kf));
      __builtin_amdgcn_raw_buffer_store_b128(empty4(), rsW, WS_HL + (unsigned)((rt * 16 + hp) * 4096 + tid * 16), 0, 16);
    }
    const int fl = (q4 >> 1) | ((g & 1) << 1);
    const unsigned bo = lds_addr(smem) + ABYTES + (g >> 1) * 6144 + (g & 1) * 1024 + q4 * 128 + p4 * 8;
    const unsigned bo0 = bo + (((wn * 2 + 0) ^ fl) << 5), bo1 = bo + (((wn * 2 + 1) ^ fl) << 5);
    const unsigned ar = lds_addr(smem) + (wm * 64 + fr) * 128;
    const unsigned aK0 = first ? ar + ((g ^ (fr & 7)) << 4) : sb + wm * 8192 + lane * 16, aK1 = first ? ar + (((4 + g) ^ (fr & 7)) << 4) : aK0 + 1024;
    f32x32 ac0, ac1, ac2;
    asm volatile("" :: "v"(aK0), "v"(aK1), "v"(bo0), "v"(bo1));
    if (first) BAR;
    else publish(rsW, tid, 256 + rt * 16 + hp, E + (unsigned)layer);
#define G1_OUT
#define G1_T(a)
#define G1_TEND
#define G1_TCL
    asm volatile(G1_MACROS
        "s_mov_b32 vcc_hi, %[lay]\n"
        ".if %[hh]\n s_branch .Lg1m\n .else\n"
        ".p2align 3\n .Lg1m:\n"
        "G1RDB 0, v16, v17, 0\n G1RDA v12, v13, 0\n G1RDB 1, v16, v17, 0\n"
        "ds_read_b128 v[64:67], v20\n ds_read_b128 v[68:71], v20 offset:2048\n"
        "ds_read_b128 v[72:75], v20 offset:4096\n ds_read_b128 v[76:79], v20 offset:6144\n"
        "s_cmp_eq_u32 vcc_hi, 0\n s_waitcnt lgkmcnt(12)\n"
        "G1SG 0, 1, 1, v12, v13, v16, v17, 0\n"
        "s_mov_b32 vcc_lo, 16\n"
        "G1SG 1, 1, 1, v12, v13, v16, v17, 40960\n"
        "G1SG 2, 1, 2, v12, v13, v16, v17, 40960\n"
        "s_cbranch_scc1 .Lg1a%=\n"
        "s_barrier\n"
        G1_T(88)
        "s_barrier\n"
        G1_T(92)
        "G1RDA v12, v13, 40960\n"
        "s_waitcnt lgkmcnt(4)\n"
        "s_branch .Lg1b%=\n"
        ".Lg1a%=:\n"
        G1_T(88)
        "s_barrier\n"
        G1_T(92)
        "G1RDB 0, v16, v17, 40960\n G1RDA v12, v13, 40960\n G1RDB 1, v16, v17, 40960\n"
        ".p2align 3\n s_nop 0\n"
        ".Lg1b%=:\n"
        "s_waitcnt lgkmcnt(8)\n"
        "G1SG 0, 0, 1, v12, v13, v16, v17, 40960\n"
        "s_barrier\n"
        "G1SG 1, 0, 1, v14, v15, v18, v19, 0\n"
        "G1SG 2, 0, 1, v14, v15, v18, v19, 0\n"
        "s_lshr_b32 vcc_lo, vcc_lo, 1\n s_waitcnt vmcnt(0) lgkmcnt(8)\n"
        "G1SG 0, 0, 1, v14, v15, v18, v19, 0\n"
        "s_barrier\n"
        "G1SG 1, 0, 1, v12, v13, v16, v17, 0\n"
        "G1SG 2, 0, 1, v12, v13, v16, v17, 0\n"
        "s_cbranch_scc0 .Lg1c%=\n s_waitcnt lgkmcnt(8)\n"
        "G1SG 0, 0, 1, v12, v13, v16, v17, 0\n"
        "s_barrier\n"
        "G1SG 1, 0, 1, v12, v13, v16, v17, 40960\n"
        "G1SG 2, 0, 1, v12, v13, v16, v17, 40960\n"
        "s_branch .Lg1b%=\n"
        ".Lg1c%=:\n"
        "s_waitcnt lgkmcnt(8)\n"
        "G1SG 0, 0, 1, v12, v13, v16, v17, 0\n s_bitcmp1_b32 vcc_hi, 0\n"
        G1_T(96)
        "s_cbranch_scc1 .Lg1n\n s_barrier\n"
        G1_SLIDE
        ".endif\n .if %[hh]\n .p2align 3\n s_nop 0\n .Lg1n:\n s_barrier\n"
        G1_HEDGE
        ".endif\n"
        G1_TEND G1_PURGE
        : "=&{v[160:191]}"(ac0), "=&{v[192:223]}"(ac1), "=&{v[224:255]}"(ac2), "=&{v[64:79]}"(xq),
          "=&{v[88:91]}"(csq[1]), "=&{v[148:151]}"(csq[0]), "=&{v[140:143]}"(st01), "=&{v[136:139]}"(st23),
          "+{v[48:63]}"(pa), "+{v[36:39]}"(bias[0]), "+{v[40:43]}"(bias[1]), "+{v[44:47]}"(bias[2]) G1_OUT
        : "{v12}"(aK0), "{v13}"(aK1), "{v14}"(aK0 + 2 * STAGE), "{v15}"(aK1 + 2 * STAGE), "{v16}"(bo0), "{v17}"(bo1),
          "{v18}"(bo0 + 2 * STAGE), "{v19}"(bo1 + 2 * STAGE), "{v20}"(wn ? aK1 : aK0),
          [lay] "s"(layer), [hh] "n"(MODE), [rs] "s"(rsW), [wm] "s"(wm)
        : "memory", "scc", "vcc", "m0", G1_TCL G1_CLOBBER);
    acc[0][0] = quad<0>(ac0); acc[0][1] = quad<1>(ac0); acc[1][0] = quad<6>(ac0); acc[1][1] = quad<7>(ac0);
    asm volatile("" : "=s"(kol), "=s"(wos), "=s"(tO));
    wf[0] = __builtin_bit_cast(bf16x8, bias[0]); wf[1] = __builtin_bit_cast(bf16x8, bias[1]);
    qh[0][0] = quad<4>(ac1); qh[0][1] = quad<5>(ac1); qh[1][0] = quad<2>(ac2); qh[1][1] = quad<3>(ac2);
  }
  ST(layer * 24 + 4)
  {
  const int lane = lane_id(), tid = w * 64 + lane, fr = lane & 15, g = lane >> 4;
  (void)fr; (void)g;
  if constexpr (HH) {
    const f32x4 z4 = {0, 0, 0, 0};
    constexpr float L2E = 1.4426950408889634f;
    const unsigned kvo = WS_KV + (layer >> 1) * KVLB, kfo = kvo + 16u * NH * KVPB + hp * KVFB;
    auto phi2 = [&](const f32x4 (&a)[2][2], f32x4 st, bf16x8 (&qp)[2][2]) {
      f32x4 pm[2][2];
      f32x2 f[2][4];
      asm volatile("" : "+v"(st));
#pragma unroll
      for (int i = 0; i < 8; ++i) {
        const int rb = i >> 2, c = (i >> 1) & 1, e = (i & 1) * 2;
        f[rb][i & 3] = __builtin_elementwise_fma(f32x2{csq[c][e], csq[c][e + 1]}, f32x2{st[2 * rb], st[2 * rb]}, f32x2{a[rb][c][e], a[rb][c][e + 1]});
      }
#pragma unroll
      for (int rb = 0; rb < 2; ++rb) asm volatile("" : "+v"(f[rb][0]), "+v"(f[rb][1]), "+v"(f[rb][2]), "+v"(f[rb][3]));
#pragma unroll
      for (int rb = 0; rb < 2; ++rb) {
        const bf16x8 xf = __builtin_bit_cast(bf16x8, u32x4{pk2(f[rb][0][0], f[rb][0][1]), pk2(f[rb][1][0], f[rb][1][1]), pk2(f[rb][2][0], f[rb][2][1]), pk2(f[rb][3][0], f[rb][3][1])});
        pm[rb][0] = MFMA(wf[0], xf, z4); pm[rb][1] = MFMA(wf[1], xf, z4);
      }
      asm volatile("s_nop 7" : "+v"(pm[0][0]), "+v"(pm[0][1]), "+v"(pm[1][0]), "+v"(pm[1][1]));
      float mx[2], mn[2];
#pragma unroll
      for (int u = 0; u < 2; ++u) max8pn(pm[u][0], pm[u][1], mx[u], mn[u]);
      xr4<true>(mx[0], mn[0], mx[1], mn[1]);
      float ep[2][8], en[2][8], sp[2], sn[2];
#pragma unroll
      for (int u = 0; u < 2; ++u) {
        const float c = st[2 * u + 1] * L2E, mxs = -mx[u] * c, mns = -mn[u] * c;
#pragma unroll
        for (int i = 0; i < 4; ++i) {
          const f32x2 p2 = {pm[u][i >> 1][(i & 1) * 2], pm[u][i >> 1][(i & 1) * 2 + 1]};
          const f32x2 tp = __builtin_elementwise_fma(p2, f32x2{c, c}, f32x2{mxs, mxs});
          const f32x2 tn = __builtin_elementwise_fma(-p2, f32x2{c, c}, f32x2{mns, mns});
          ep[u][2 * i] = __builtin_amdgcn_exp2f(tp[0]); ep[u][2 * i + 1] = __builtin_amdgcn_exp2f(tp[1]);
          en[u][2 * i] = __builtin_amdgcn_exp2f(tn[0]); en[u][2 * i + 1] = __builtin_amdgcn_exp2f(tn[1]);
        }
        const f32x2 a2 = (f32x2{ep[u][0], ep[u][1]} + f32x2{ep[u][2], ep[u][3]}) + (f32x2{ep[u][4], ep[u][5]} + f32x2{ep[u][6], ep[u][7]});
        const f32x2 b2 = (f32x2{en[u][0], en[u][1]} + f32x2{en[u][2], en[u][3]}) + (f32x2{en[u][4], en[u][5]} + f32x2{en[u][6], en[u][7]});
        sp[u] = a2[0] + a2[1]; sn[u] = b2[0] + b2[1];
      }
      xr4<false>(sp[0], sn[0], sp[1], sn[1]);
#pragma unroll
      for (int rb = 0; rb < 2; ++rb) {
        const float ip = __builtin_amdgcn_rcpf(sp[rb]), in = __builtin_amdgcn_rcpf(sn[rb]);
        qp[rb][0] = __builtin_bit_cast(bf16x8, u32x4{pk2(ep[rb][0] * ip, ep[rb][1] * ip), pk2(ep[rb][2] * ip, ep[rb][3] * ip), pk2(ep[rb][4] * ip, ep[rb][5] * ip), pk2(ep[rb][6] * ip, ep[rb][7] * ip)});
        qp[rb][1] = __builtin_bit_cast(bf16x8, u32x4{pk2(en[rb][0] * in, en[rb][1] * in), pk2(en[rb][2] * in, en[rb][3] * in), pk2(en[rb][4] * in, en[rb][5] * in), pk2(en[rb][6] * in, en[rb][7] * in)});
        asm volatile("" : "+v"(qp[rb][0]), "+v"(qp[rb][1]));
      }
    };
    asm volatile(G1_KV : "+{v[48:63]}"(pa) : [rs] "s"(rsW) : "memory", "v80", "v81", "v82", "v83", "v84", "v85", "v86", "v87", "v92", "v93", "v94", "v95", "v96", "v97", "v98", "v99", "v100", "v101", "v102", "v103", "v104", "v105", "v106", "v107", "v108", "v109", "v110", "v111", "v112", "v113", "v114", "v115", "v116", "v117", "v118", "v119", "v120", "v121", "v122", "v123", "v124", "v125", "v126", "v127", "v128", "v129", "v130", "v131", "v132", "v133", "v134", "v135", "v144", "v145", "v146", "v147", "v152", "v153", "v154", "v155", "v156", "v157", "v158", "v159");
    auto wo_late = [&](int t) { glds2v(wos + knib(kol, t) * tO, pwl, pwm, sb + OFF_WO + t * 8192 + (w & 3) * 2048); };
    ST(layer * 24 + 6)
    u32x4 l1[8];
    const bool red = w == 7 || (w == 6 && (rt & 7) == 0);
    unsigned lo = kvo + (unsigned)((2 * hp + (rt >> 3) + (lane >> 5) * 8 * NH) * KVPB) +
                  (w == 7 ? (unsigned)((rt & 4) * 512 + (g & 1) * 1024 + (rt & 3) * 256 + fr * 16) : (unsigned)(4096 + (lane & 7) * 16));
    unsigned fs = w == 7 ? kfo + (unsigned)((rt >> 2) * 2048 + (lane >> 5) * 1024 + (rt & 3) * 256 + fr * 16 + (g & 1) * 8)
                : (lane & 24) ? 0x7fff0000u : kfo + (unsigned)(8192 + (rt >> 3) * 128 + (lane & 4) * 16 + (lane & 1) * 32 + (lane >> 5) * 16 + (lane & 2) * 4);
    auto l1_load = [&]() {
      asm volatile("" ::: "memory");
#pragma unroll
      for (int p = 0; p < 8; ++p) asm volatile("buffer_load_dwordx4 %0, %1, %2, %3 offen sc1" : "=v"(l1[p]) : "v"(lo), "s"(rsW), "s"(p * (NH * KVPB)) : "memory");
      __builtin_amdgcn_sched_barrier(0);
    };
    if (red) asm volatile("" : "+v"(lo), "+v"(fs));
    if (red) { __builtin_amdgcn_s_setprio(3); __builtin_amdgcn_s_sleep(13); l1_load(); }
    bf16x8 qp2[2][2];
    if (!kg) {
      phi2(qh, st23, qp2);
    } else if (red) {
      typedef __attribute__((ext_vector_type(2))) __bf16 bf2;
      unsigned c0 = 0x00003F80u, c1 = 0x3F800000u;
      asm volatile("" : "+v"(c0), "+v"(c1));
      float s4[4];
      _Pragma("unroll 1") for (unsigned spins = 0; spins < (1u << 18); ++spins) {
        float a[8] = {zero1(), zero1(), zero1(), zero1(), zero1(), zero1(), zero1(), zero1()};
#pragma unroll
        for (int p = 0; p < 8; ++p)
#pragma unroll
          for (int e = 0; e < 4; ++e) {
            if (e == 0) { __builtin_amdgcn_sched_barrier(0); asm volatile("s_waitcnt vmcnt(%1)" : "+v"(l1[p]) : "n"(7 - p) : "memory"); }
            const unsigned u = l1[p][e];
            a[2 * e] = __builtin_amdgcn_fdot2_f32_bf16(__builtin_bit_cast(bf2, u), __builtin_bit_cast(bf2, c0), a[2 * e], false);
            a[2 * e + 1] = __builtin_amdgcn_fdot2_f32_bf16(__builtin_bit_cast(bf2, u), __builtin_bit_cast(bf2, c1), a[2 * e + 1], false);
          }
#pragma unroll
        for (int i = 0; i < 4; ++i) {
          auto r = __builtin_amdgcn_permlane32_swap(__float_as_uint(a[i]), __float_as_uint(a[i + 4]), false, false);
          s4[i] = __uint_as_float(r[0]) + __uint_as_float(r[1]);
        }
        const float t = s4[0] + s4[2];
        if (__all(t == t)) break;
        unsigned mxw = 0;
#pragma unroll
        for (int p = 0; p < 8; ++p)
#pragma unroll
          for (int e = 0; e < 4; ++e) mxw = l1[p][e] > mxw ? l1[p][e] : mxw;
        if (__all(mxw != EMPTY)) break;
        l1_load();
      }
      WAITV(0);
      unsigned o0 = pk2(s4[0], s4[1]), o1 = pk2(s4[2], s4[3]);
      o0 = o0 < EMPTY - 1u ? o0 : EMPTY - 1u; o1 = o1 < EMPTY - 1u ? o1 : EMPTY - 1u;
      __builtin_amdgcn_raw_buffer_store_b64(u32x2{o0, o1}, rsW, fs, 0, 16);
      __builtin_amdgcn_s_setprio(0);
    }
    ST(layer * 24 + 8)
    WAITL0;
    BAR;
    ST(layer * 24 + 9)
    if (kg) {
#pragma unroll
      for (int i = 6; i < 12; ++i) wo_late(i);
    } else {
      unsigned fo = kfo + (unsigned)(wn * 4096 + lane * 16), fz = kfo + (unsigned)(8192 + wn * 128 + g * 16);
      asm volatile("" : "+v"(fo), "+v"(fz));
      bf16x8 qp[2][2];
      phi2(acc, st01, qp);
      asm volatile(G1_OUTPUT
          :: "{v[224:227]}"(qp[0][0]), "{v[228:231]}"(qp[0][1]), "{v[232:235]}"(qp[1][0]), "{v[236:239]}"(qp[1][1]),
             "{v[240:243]}"(qp2[0][0]), "{v[244:247]}"(qp2[0][1]), "{v[248:251]}"(qp2[1][0]), "{v[252:255]}"(qp2[1][1]),
             [fo] "v"(fo), [fz] "v"(fz), [go] "v"(ogo), [lo] "v"(opo), [rs] "s"(rsW)
          : "memory", "vcc", "scc", "m0", "v152", "v153", "v154", "v155", "v156", "v157", "v158", "v159", "v160", "v161", "v162", "v163", "v164", "v165", "v166", "v167", "v168", "v169", "v170", "v171", "v172", "v173", "v174", "v175", "v176", "v177", "v178", "v179", "v180", "v181", "v182", "v183", "v184", "v185", "v186", "v187", "v188", "v189", "v190", "v191", "v192", "v193", "v194", "v195", "v196", "v197", "v198", "v199", "v200", "v201", "v202", "v203", "v204", "v205", "v206", "v207", "v208", "v209", "v210", "v211", "v212", "v213", "v214", "v215", "v216", "v217", "v218", "v219", "v220", "v221", "v222", "v223");
    }
  }
  ST(layer * 24 + 12)
  if (first && w >= 4 && w < 7) {
    typedef __attribute__((ext_vector_type(2))) __bf16 bf2;
    unsigned c0 = 0x00003F80u, c1 = 0x3F800000u;
    asm volatile("" : "+v"(c0), "+v"(c1));
    const bf2 lo1 = __builtin_bit_cast(bf2, c0), hi1 = __builtin_bit_cast(bf2, c1);
    float a[8] = {zero1(), zero1(), zero1(), zero1(), zero1(), zero1(), zero1(), zero1()};
#pragma unroll
    for (int i = 0; i < 8; ++i) {
      asm volatile("" : "+v"(pw[i]));
#pragma unroll
      for (int e = 0; e < 4; ++e) {
        const unsigned u = pw[i][e];
        a[2 * e] = __builtin_amdgcn_fdot2_f32_bf16(__builtin_bit_cast(bf2, u), lo1, a[2 * e], false);
        a[2 * e + 1] = __builtin_amdgcn_fdot2_f32_bf16(__builtin_bit_cast(bf2, u), hi1, a[2 * e + 1], false);
      }
    }
#pragma unroll
    for (int i = 0; i < 4; ++i) {
      auto r = __builtin_amdgcn_permlane32_swap(__float_as_uint(a[i]), __float_as_uint(a[i + 4]), false, false);
      a[i] = __uint_as_float(r[0]) + __uint_as_float(r[1]);
    }
#pragma unroll
    for (int i = 0; i < 2; ++i) {
      auto r = __builtin_amdgcn_permlane16_swap(__float_as_uint(a[i]), __float_as_uint(a[i + 2]), false, false);
      a[i] = __uint_as_float(r[0]) + __uint_as_float(r[1]);
      a[i] += dpp_ror<8>(a[i]);
    }
    if (!(lane & 8))
      __builtin_amdgcn_raw_buffer_store_b64(u32x2{__float_as_uint(a[0]), __float_as_uint(a[1])}, rsW,
          (unsigned)(WS_CSP + ((((hp + (1 << (w - 4))) & 15) * 16 + rt) * 192 + (w - 4) * 64 + (lane & 7) * 8 + (lane >> 5) * 4 + ((lane >> 4) & 1) * 2) * 4), 0, 16);
  }
  }
}

#undef pcs
#undef prs
#undef opo
#undef ogo
#undef hof
#undef dm
#undef dm2
#undef pcv
#undef pwf
#undef prv
#undef pvf
#undef ppk
#undef ppa
#undef pvr
#undef gkv
#undef gkz
#undef pwl
#undef pwm
#undef plb
#undef prd
#undef dm3

__device__ __forceinline__ void g2_phase(char* smem, char* ws, u64 E, int w, f32x16& xq, Next& nx, int layer, const u16* __restrict__ x0,
                                         const u16* __restrict__ wo, const u16* __restrict__ wnx, u16* __restrict__ out) {
  IDS
  wnx = uni(wnx);
  const unsigned sb = lds_addr(smem);
  const int lw = w & 3, k2 = 16 * lw + (lane >> 3);
  unsigned pxr, rmo;
  auto ends = [&]() {
    const int lane = lane_id(), r0 = w * 16 + (lane >> 3);
    pxr = sb + r0 * 128 + (lane & 7) * 16;
    rmo = (unsigned)(((m0 + r0) * DM + hp * 64 + ((lane & 7) ^ (lane >> 3)) * 8) * 2);
    asm volatile("" : "+v"(pxr), "+v"(rmo));
  };
  constexpr int S0 = 0, S1 = 16384, S2 = 32768, S3 = 147456, S4 = OFF_WO, S5 = 98304, S6 = 114688;
  constexpr int AS[16] = {S0, S1, S2, S3, S0, S1, S4, S2, S3, S0, S1, S4, S5, S3, S0, S6};
  constexpr int OFF_C = 0;
  auto bs = [](int t) { return OFF_WO + (t < 12 ? t : t - 10) * 8192; };
  const char *xa1, *xa2, *wq2;
  u64 fm;
  unsigned vB0, vB1, vXA, po, pst;
  if (w >= 4) {
    constexpr int WT[15] = {0, 8, 12, 14, 16, 18, 20, 18, 16, 14, 12, 12, 10, 10, 8};
    const char* o = ws + WS_O + layer * (SEQ * DM * 2) + rt * 262144;
    const unsigned vA = (unsigned)(lw * 4096 + lane * 16);
    u64 kord = korder(bid & 3, hp);
    auto ldA = [&](int s) { glds4c(o + knib(kord, s) * 16384, vA, sb + AS[s] + lw * 4096); };
    const bool im = img(layer, hp), in = img(layer + 1, hp);
    unsigned vW0 = im ? (unsigned)(lw * 2048 + lane * 16) : (unsigned)((k2 * DM + hp * 64 + ((lane & 7) ^ ((lane >> 4) << 1)) * 8) * 2), vW1 = vW0 + (im ? 0u : 8 * DM * 2 - 1024u);
    const char *wos = wo_src(ws, wo, layer, hp), *wqs = wq_src(ws, wnx, layer + 1, hp);
    unsigned tO = im ? 8192u : 131072u;
    asm volatile("" : "+v"(vW0), "+v"(vW1));
    asm volatile("" : "+s"(wos), "+s"(tO));
    auto ldW = [&](int t) { glds2v(wos + knib(kord, t) * tO, vW0, vW1, sb + bs(t) + lw * 2048); };
    const int fB = (k2 >> 1) & 1;
    vB0 = in ? (unsigned)(lw * 6144 + lane * 16) : (unsigned)((k2 * 3072 + hp * 64 + (((((lane & 7) >> 1) ^ fB) << 1) | (lane & 1)) * 8) * 2);
    vB1 = in ? vB0 : (unsigned)(((k2 + 8) * 3072 + hp * 64 + (((((lane & 7) >> 1) ^ (fB | 2)) << 1) | (lane & 1)) * 8) * 2) - 1024u;
    const u64 kn = nx.kord = korder(kv1(bid), hp);
    nx.ko2 = kord; nx.wq = wqs; nx.tw = in ? 24576u : 393216u;
    const char *wq0 = wqs + hp * (in ? 24576u : 393216u), *wq1 = wqs + knib(kn, 1) * (in ? 24576u : 393216u);
    wq2 = wqs + knib(kn, 2) * (in ? 24576u : 393216u);
    xa1 = ws + WS_X + layer * (SEQ * DM * 2) + knib(kn, 1) * 16384; xa2 = ws + WS_X + layer * (SEQ * DM * 2) + knib(kn, 2) * 16384;
    vXA = (unsigned)(rt * 262144 + lw * 4096 + lane * 16);
    fm = kfirst(kn);
    po = ((fm >> lane) & 1) ? WS_FLAGS + (unsigned)(256 + rt * 16 + lane) * FLS : 0u;
    pst = g != q4 ? 0x7fff0000u : WS_ST + layer * (SEQ * 128) + (unsigned)(((rt * 16 + hp) * 128 + lw * 32 + fr) * 8);
    asm volatile("" : "+v"(vB0), "+v"(vB1), "+v"(vXA), "+v"(po), "+v"(pst));
    asm volatile("" : "+s"(xa1), "+s"(xa2), "+s"(wq2), "+s"(fm), "+s"(nx.kord), "+s"(nx.ko2), "+s"(nx.wq), "+s"(nx.tw));
    asm volatile("" : "+s"(wq0), "+s"(wq1));
    auto ldB = [&](int s, int part) {
      const char* b = s ? wq1 : wq0;
      if (part) glds4v(b, vB0, vB1, vB0, vB1, sb + s * 40960 + 16384 + lw * 6144);
      else glds2v(b + 4096, vB0, vB1, sb + s * 40960 + 16384 + lw * 6144 + 4096);
    };
    ends();
    const char* o1 = o + knib(kord, 1) * 16384;
    asm volatile("" : "+s"(o1), "+s"(kord), "+s"(o));
    asm volatile("" :: "v"(vA));
    if (layer) { BAR;   }
    BAR;
    glds4c(o1, vA, sb + AS[1] + lw * 4096); __builtin_amdgcn_s_sleep(3); ldA(2); __builtin_amdgcn_s_sleep(3); ldA(3); __builtin_amdgcn_s_sleep(3); ldA(4);
    WAITV(12);
    BAR;
    auto step = [&](auto ktc) {
      constexpr int kt = decltype(ktc)::value;
      WAITVN(WT[kt]);
      BAR;
      if constexpr (kt == 1) { ldA(5); ldA(6); }
      if constexpr (kt >= 2 && kt <= 10) ldA(kt + 5);
      if constexpr (kt >= 2 && kt <= 5) ldW(kt + 10);
      if constexpr (kt >= 11) ldB((kt - 11) >> 1, (kt - 11) & 1);
      if constexpr (kt >= 6) { __builtin_amdgcn_s_sleep(kt >= 11 ? 3 : 2); if (layer == 1) __builtin_amdgcn_s_sleep(1); }
    };
    step(IC<1>{}); step(IC<2>{}); step(IC<3>{}); step(IC<4>{}); step(IC<5>{}); step(IC<6>{}); step(IC<7>{});
    step(IC<8>{}); step(IC<9>{}); step(IC<10>{}); step(IC<11>{}); step(IC<12>{}); step(IC<13>{}); step(IC<14>{});
  } else {
    asm volatile("" : "=v"(vB0), "=v"(vB1), "=v"(vXA), "=v"(po), "=v"(pst));
    asm volatile("" : "=s"(xa1), "=s"(xa2), "=s"(wq2), "=s"(fm), "=s"(nx.kord), "=s"(nx.ko2), "=s"(nx.wq), "=s"(nx.tw));
    const int wm = w >> 1, wn = w & 1;
    f32x4 acc[4][2];
    const int fl = (q4 >> 1) | ((g & 1) << 1);
    const int bb0 = (4 * g + q4) * 128 + p4 * 8 + (((wn * 2) ^ fl) << 5), bb1 = (4 * g + q4) * 128 + p4 * 8 + (((wn * 2 + 1) ^ fl) << 5);
    const int abase = wm * 4096 + lane * 16;
    bf16x8 fa[2][2][4], fb[2][2][2];
    unsigned ra[3], rb0[2], rb1[2];
#pragma unroll
    for (int i = 0; i < 3; ++i) { ra[i] = abase + i * 65536; asm volatile("" : "+v"(ra[i])); }
#pragma unroll
    for (int i = 0; i < 2; ++i) { rb0[i] = bb0 + OFF_WO + i * 49152; rb1[i] = bb1 + OFF_WO + i * 49152; asm volatile("" : "+v"(rb0[i]), "+v"(rb1[i])); }
    auto loadf = [&](int kt, bf16x8 (&a)[2][4], bf16x8 (&b)[2][2]) {
#pragma unroll
      for (int s = 0; s < 2; ++s) {
        const char* sA = smem + ra[AS[kt] >> 16] + (AS[kt] & 65535) + s * 8192;
        const int hi = bs(kt) >= OFF_WO + 49152, ob = bs(kt) - OFF_WO - hi * 49152 + s * 4096;
        b[s][0] = cat8(tr_read(smem + rb0[hi] + ob), tr_read(smem + rb0[hi] + ob + 2048));
        b[s][1] = cat8(tr_read(smem + rb1[hi] + ob), tr_read(smem + rb1[hi] + ob + 2048));
#pragma unroll
        for (int rb = 0; rb < 4; ++rb) a[s][rb] = *(const bf16x8*)(sA + rb * 1024);
      }
    };
    auto mma = [&](bf16x8 (&a)[2][4], bf16x8 (&b)[2][2]) {
#pragma unroll
      for (int i = 0; i < 16; ++i) {
        const int rb = i >> 2, cb = ((i >> 1) ^ rb) & 1, s = ((i + 1) >> 1) & 1;
        acc[rb][cb] = MFMA(b[s][cb], a[s][rb], acc[rb][cb]);
      }
    };
    unsigned fo = lane < 16 && lane != hp ? WS_FLAGS + (unsigned)(rt * 16 + lane) * FLS : 0u;
    unsigned kf = nx.kf & 0xffffu, kf2 = nx.kf >> 16;
    unsigned jo = (unsigned)((((rt & 7) * 128 + (w - 1) * 64 + lane) * 3072 + (hp & 3) * 1024 + (hp | 3) * 64) * 2);
    unsigned fo1 = ((kf >> (lane & 31)) & 1) ? fo : 0u, fo2 = ((kf2 >> (lane & 31)) & 1) ? fo : 0u;
    fo = (((kf | kf2) >> (lane & 31)) & 1) ? 0u : fo;
    asm volatile("" : "+v"(fo), "+v"(fo1), "+v"(fo2));
    asm volatile("" : "+s"(kf), "+s"(kf2));
    publish(rsW, tid, rt * 16 + hp, E + (unsigned)(layer + 1));
    ST(layer * 24 + 13)
    u64 fl1;
    asm volatile("" : "=v"(fl1));
    if (w == 0) asm volatile("global_load_dwordx2 %0, %1, %2 sc1" : "+v"(fl1) : "v"(fo1), "s"(ws) : "memory");
    if (layer == 0) {
      const unsigned xo = (unsigned)(((m0 + wm * 64 + fr) * DM + hp * 64 + wn * 32 + g * 8) * 2);
      asm volatile("global_load_dwordx4 v[64:67], %1, %3\n\tglobal_load_dwordx4 v[68:71], %1, %4\n\tglobal_load_dwordx4 v[72:75], %2, %3\n\tglobal_load_dwordx4 v[76:79], %2, %4"
                   : "+{v[64:79]}"(xq) : "v"(xo), "v"(xo + 32 * DM * 2), "s"(x0), "s"(x0 + 16 * DM) : "memory");
    }
    loadf(0, fa[0], fb[0]);
    unsigned junk;
    asm volatile("" : "=v"(junk));
    if (layer < 2 && w == 3 && (bid >> 6) == 0) code_touch(junk, bid, lane);
    if (layer == 0 && (hp & 7) < 3 && (w == 1 || w == 2))
      asm volatile("global_load_dword %0, %1, %2" : "+v"(junk) : "v"(jo), "s"(wnx) : "memory");
    __builtin_amdgcn_sched_barrier(0);
    bf16x8 id[2];
#pragma unroll
    for (int cb = 0; cb < 2; ++cb) {
      const unsigned one = (g >> 1) == cb && (g & 1) == (fr >> 3) ? 0x3F80u << ((fr & 1) * 16) : 0u;
      u32x4 t;
#pragma unroll
      for (int j = 0; j < 4; ++j) t[j] = ((fr >> 1) & 3) == j ? one : 0u;
      id[cb] = __builtin_bit_cast(bf16x8, t);
      asm volatile("" : "+v"(id[cb]));
    }
    const int l2 = lane_id(), ch = (l2 >> 5) | ((l2 >> 3) & 2), f2 = l2 & 15;
    unsigned pl = (unsigned)(wm * 8192 + wn * 1024 + ch * 256 + f2 * 16);
    unsigned pg = layer < 3 ? WS_X + layer * (SEQ * DM * 2) + (unsigned)((rt * 16 + hp) * 16384) + pl : 0x7fff0000u;
    pl = sb + OFF_C + (layer < 3 ? pl : (unsigned)((wm * 64 + f2) * 128 + (((wn * 4 + ch) ^ (f2 & 7)) << 4)));
    asm volatile("" : "+v"(pl), "+v"(pg));
    ends();
#pragma unroll
    for (int rb = 0; rb < 4; ++rb) { acc[rb][0] = f32x4{0, 0, 0, 0}; acc[rb][1] = f32x4{0, 0, 0, 0}; }
    mma(fa[0], fb[0]);
    __builtin_amdgcn_sched_barrier(0);
    WAITL0;
    u64 miss = 0;
    if (w == 0) { ARRIVED("+v"(fl1)); if (__ballot(fl1 != E + (unsigned)(layer + 1)) & kf) poll_eq(ws, fo1, E + (unsigned)(layer + 1), kf, kf); miss = 0xffffu & ~(kf | kf2 | (1u << hp)); }
    ST(layer * 24 + 14)
    __builtin_amdgcn_sched_barrier(0);
    BAR;
    __builtin_amdgcn_sched_barrier(0);
    asm volatile("" ::: "memory");
    if (layer == 0) WAITV(0);
    __builtin_amdgcn_sched_barrier(0);
    asm volatile("" : "+{v[64:79]}"(xq));
    const bf16x8 xf[4] = {__builtin_bit_cast(bf16x8, quad<0>(xq)), __builtin_bit_cast(bf16x8, quad<1>(xq)), __builtin_bit_cast(bf16x8, quad<2>(xq)), __builtin_bit_cast(bf16x8, quad<3>(xq))};
#pragma unroll
    for (int rb = 0; rb < 4; ++rb) { acc[rb][0] = MFMA(id[0], xf[rb], acc[rb][0]); acc[rb][1] = MFMA(id[1], xf[rb], acc[rb][1]); }
    if (w == 0) poll_eq(ws, fo2, E + (unsigned)(layer + 1), kf2, kf2);
    u64 fl2;
    asm volatile("" : "=v"(fl2));
    auto step = [&](auto ktc) {
      constexpr int kt = decltype(ktc)::value;
      __builtin_amdgcn_sched_barrier(0);
      if (kt == 5 && miss) {
        ARRIVED("+v"(fl2));
        if (__ballot(fl2 != E + (unsigned)(layer + 1)) & miss) poll_eq(ws, fo, E + (unsigned)(layer + 1), miss, ~0ull);
      }
      WAITL0;
      BAR;
      if (kt == 2 && miss) asm volatile("global_load_dwordx2 %0, %1, %2 sc1" : "+v"(fl2) : "v"(fo), "s"(ws) : "memory");
      if (kt == 0) { ST(layer * 24 + 15) }
      __builtin_amdgcn_sched_barrier(0);
      loadf(kt + 1, fa[(kt + 1) & 1], fb[(kt + 1) & 1]);
      if (kt == 0) return;
      mma(fa[kt & 1], fb[kt & 1]);
#pragma unroll
      for (int i = 0; i < 4; ++i) {
        __builtin_amdgcn_sched_group_barrier(0x008, 1, 0);
        __builtin_amdgcn_sched_group_barrier(0x100, 1, 0);
        __builtin_amdgcn_sched_group_barrier(0x008, 1, 0);
        __builtin_amdgcn_sched_group_barrier(0x100, 3, 0);
      }
#pragma unroll
      for (int i = 0; i < 8; ++i) __builtin_amdgcn_sched_group_barrier(0x008, 1, 0);
    };
    step(IC<0>{}); step(IC<1>{}); step(IC<2>{}); step(IC<3>{}); step(IC<4>{}); step(IC<5>{}); step(IC<6>{}); step(IC<7>{});
    step(IC<8>{}); step(IC<9>{}); step(IC<10>{}); step(IC<11>{}); step(IC<12>{}); step(IC<13>{}); step(IC<14>{});
    WAITV(0);
    asm volatile("" :: "v"(junk));
    __builtin_amdgcn_sched_barrier(0);
    u32x4 xv[4];
    auto put = [&](int rb) {
      auto a = __builtin_amdgcn_permlane16_swap(pk2(acc[rb][0][0], acc[rb][0][1]), pk2(acc[rb][1][0], acc[rb][1][1]), false, false);
      auto b = __builtin_amdgcn_permlane16_swap(pk2(acc[rb][0][2], acc[rb][0][3]), pk2(acc[rb][1][2], acc[rb][1][3]), false, false);
      xv[rb] = u32x4{a[0], b[0], a[1], b[1]};
      __builtin_amdgcn_raw_buffer_store_b128(xv[rb], rsW, pg + (rb & 1) * 2048, (rb >> 1) * 4096, 16);
    };
#pragma unroll
    for (int rb = 0; rb < 4; ++rb)
#pragma unroll
      for (int cb = 0; cb < 2; ++cb) acc[rb][cb] = MFMA(fb[1][0][cb], fa[1][0][rb], acc[rb][cb]);
#pragma unroll
    for (int rb = 0; rb < 4; ++rb) {
      __builtin_amdgcn_sched_barrier(0);
#pragma unroll
      for (int cb = 0; cb < 2; ++cb) acc[rb][cb] = MFMA(fb[1][1][cb], fa[1][1][rb], acc[rb][cb]);
      __builtin_amdgcn_sched_barrier(0);
      if (rb >= 2) put(rb - 2);
    }
    __builtin_amdgcn_sched_barrier(0);
    ST(layer * 24 + 16)
    put(2); put(3);
    __builtin_amdgcn_sched_barrier(0);
#pragma unroll
    for (int rb = 0; rb < 4; ++rb) lds<u32x4>(pl + rb * 2048) = xv[rb];
  }
  if (layer == 3) {
    WAITL0; BAR;
    asm volatile("" ::: "memory");
    const __amdgpu_buffer_rsrc_t rsO = __builtin_amdgcn_make_buffer_rsrc((void*)uni(out), (short)0, SEQ * DM * 2, 0x00020000);
    __builtin_amdgcn_raw_buffer_store_b128(lds<u32x4>(pxr + OFF_C), rsO, rmo, 0, 16);
    __builtin_amdgcn_raw_buffer_store_b128(lds<u32x4>(pxr + OFF_C + 1024), rsO, rmo, 8 * DM * 2, 16);
  }
  ST(layer * 24 + 17)
  if (layer < 3 && w >= 4) {
    WAITV(0); BAR;
    asm volatile("" ::: "memory");
    glds4v(wq2, vB0, vB1, vB0, vB1, sb + 2 * 40960 + 16384 + lw * 6144);
    glds2v(wq2 + 4096, vB0, vB1, sb + 2 * 40960 + 16384 + lw * 6144 + 4096);
    __builtin_amdgcn_s_setprio(3);
    bf16x8 xn[4];
#pragma unroll
    for (int i = 0; i < 4; ++i) xn[i] = lds<bf16x8>(sb + OFF_C + lw * 4096 + lane * 16 + i * 1024);
    const bf16x8 ones = ones8();
    u32x2 h[2];
#pragma unroll
    for (int i = 0; i < 2; ++i) {
      const f32x4 z4 = {0, 0, 0, 0};
      const f32x4 sa = MFMA(ones, xn[2 * i + 1], MFMA(ones, xn[2 * i], z4)), sq = MFMA(xn[2 * i + 1], xn[2 * i + 1], MFMA(xn[2 * i], xn[2 * i], z4));
      const float a = (lane & 1) ? sq[1] : sq[0], b = (lane & 1) ? sq[3] : sq[2];
      h[i] = u32x2{__float_as_uint(sa[0]), __float_as_uint((lane & 2) ? b : a)};
    }
    __builtin_amdgcn_s_setprio(0);
    u64 fl;
    asm volatile("" : "=v"(fl), "+v"(pst) : "v"(h[0]), "v"(h[1]));
    if (lw == 0) asm volatile("global_load_dwordx2 %0, %1, %2 sc1" : "+v"(fl) : "v"(po), "s"(ws) : "memory");
    __builtin_amdgcn_raw_buffer_store_b64(h[0], rsW, pst, 0, 16);
    __builtin_amdgcn_raw_buffer_store_b64(h[1], rsW, pst + 128, 0, 16);
    if (lw == 0) {
      WAITV(2);
      __builtin_amdgcn_sched_barrier(0);
      asm volatile("" : "+v"(fl));
      if (__ballot(fl != E + (unsigned)(layer + 1)) & fm) poll_eq(ws, po, E + (unsigned)(layer + 1), fm, ~0ull);
    }
    BAR;
    glds4c(xa1, vXA, sb + 40960 + lw * 4096); glds4c(xa2, vXA, sb + 2 * 40960 + lw * 4096);
  }
  ST(layer * 24 + 18)
}

__global__ __launch_bounds__(512, 2) void mega_kernel(const u16* __restrict__ x0, const u16* __restrict__ wqkvA,
                                                      const u16* __restrict__ woA, const u16* __restrict__ wqkvB,
                                                      const u16* __restrict__ wphi, const u16* __restrict__ woB,
                                                      u16* __restrict__ out, char* __restrict__ ws) {
  extern __shared__ __attribute__((aligned(16))) char smem[];
  {
    unsigned long long pc;
    unsigned t;
    asm volatile("s_getpc_b64 %0\n.Lmk_pc:\n\tv_lshl_add_u32 %1, %2, 9, %3\n\tv_lshlrev_b32 %1, 2, %1\n\tv_and_b32 %1, 0xffffff80, %1\n\ts_mov_b32 m0, %4\n\t"
                 "v_min_u32 %1, .Lmk_end-.Lmk_pc-4, %1\n\tglobal_load_lds_dword %1, %0" : "=&s"(pc), "=&v"(t) : "s"(blockIdx.x >> 3), "v"(threadIdx.x), "n"(LDS_TOTAL - 1024) : "memory", "m0");
  }
  const int w = __builtin_amdgcn_readfirstlane(threadIdx.x >> 6);
  u64 E = 0;
  {
    const int bid = blockIdx.x, lane = threadIdx.x & 63;
    const int rt = 2 * (bid & 4) + ((bid >> 3) & 7), hp = 4 * (bid & 3) + (bid >> 6), m0 = rt * 128;
    if (w < 4) {
      const unsigned xo = (unsigned)((m0 + hp * 8 + w * 2) * (DM * 2) + lane * 16);
      glds4v(x0, xo, xo, xo, xo, lds_addr(smem) + 122880 + w * 4096);
    } else {
      const int lw = w & 3;
      const int kB = 16 * lw + (lane >> 3), fB = (kB >> 1) & 1;
      const unsigned vB0 = (unsigned)((kB * 3072 + hp * 64 + (((((lane & 7) >> 1) ^ fB) << 1) | (lane & 1)) * 8) * 2);
      const unsigned vB1 = (unsigned)(((kB + 8) * 3072 + hp * 64 + (((((lane & 7) >> 1) ^ (fB | 2)) << 1) | (lane & 1)) * 8) * 2) - 1024u;
      const unsigned vA = (unsigned)(((m0 + lw * 32 + (lane >> 3)) * DM + ((lane & 7) ^ (lane >> 3)) * 8) * 2);
#pragma unroll
      for (int s = 0; s < 2; ++s) {
        const int kt = (rt & 8) + s;
        const unsigned sA = lds_addr(smem) + s * 40960, sB = sA + 16384;
        glds4v(x0 + kt * 64, vA, vA + 15360u, vA + 2 * 15360u, vA + 3 * 15360u, sA + lw * 4096);
        const u16* b = wqkvA + (size_t)kt * 64 * 3072;
        glds4v(b, vB0, vB1, vB0, vB1, sB + lw * 6144);
        glds2v(b + 2048, vB0, vB1, sB + lw * 6144 + 4096);
      }
    }
  }
  asm volatile("" :: "s"(woA), "s"(wqkvB), "s"(wphi), "s"(woB), "s"(out), "s"(ws));
  f32x16 xq;
  Next nx;
  asm volatile("" : "=s"(nx.kord), "=s"(nx.ko2), "=s"(nx.wq), "=s"(nx.tw), "=s"(nx.kf));
#pragma unroll 1
  for (int layer = 0; layer < 4; ++layer) {
    if (layer & 1) g1_phase<1>(smem, ws, E, w, xq, nx, layer, x0, wqkvB, woB, wphi, wqkvA, woA);
    else g1_phase<0>(smem, ws, E, w, xq, nx, layer, x0, wqkvA, woA, nullptr, wqkvB, woB);
    g2_phase(smem, ws, E, w, xq, nx, layer, x0, (layer & 1) ? woB : woA, (layer & 1) ? wqkvA : wqkvB, out);
  }
  asm volatile(".Lmk_end:");
  WAITV(0);
  if (blockIdx.x == 0 && w == 0 && lane_id() == 0) __hip_atomic_store((gu64*)ws, E + 16u, __ATOMIC_RELAXED, __HIP_MEMORY_SCOPE_AGENT);
}

extern "C" void kernel_launch(void* const* d_in, const int* in_sizes, int n_in,
                              void* d_out, int out_size, void* d_ws, size_t ws_size,
                              hipStream_t stream) {
  hipFuncSetAttribute((const void*)mega_kernel, hipFuncAttributeMaxDynamicSharedMemorySize, LDS_TOTAL);
  mega_kernel<<<256, 512, LDS_TOTAL, stream>>>((const u16*)d_in[0], (const u16*)d_in[1], (const u16*)d_in[2], (const u16*)d_in[4],
                                               (const u16*)d_in[5], (const u16*)d_in[6], (u16*)d_out, (char*)d_ws);
}
```

```cpp
#include <hip/hip_runtime.h>
#include <hip/hip_bf16.h>
#include <stdint.h>
#include <type_traits>
#pragma clang diagnostic ignored "-Winline-asm"
#pragma clang diagnostic ignored "-Wunused-value"

typedef unsigned short u16;
typedef __attribute__((ext_vector_type(8))) short bf16x8;
typedef __attribute__((ext_vector_type(4))) short bf16x4;
typedef __attribute__((ext_vector_type(4))) float f32x4;
typedef __attribute__((ext_vector_type(4))) unsigned u32x4;
typedef __attribute__((ext_vector_type(2))) unsigned u32x2;
typedef __attribute__((ext_vector_type(8))) float f32x8;
typedef __attribute__((ext_vector_type(16))) unsigned u32x16;
typedef __attribute__((ext_vector_type(16))) float f32x16;
typedef __attribute__((ext_vector_type(32))) float f32x32;
template <int I, typename T> __device__ __forceinline__ f32x4 quad(const T& v) { return __builtin_shufflevector(v, v, 4 * I, 4 * I + 1, 4 * I + 2, 4 * I + 3); }

constexpr int SEQ = 2048, DM = 1024, NH = 32;
#define ST(i)
constexpr int KVP = 64 * 32 + 64;

typedef __attribute__((ext_vector_type(2))) float f32x2;
typedef __attribute__((ext_vector_type(2))) __bf16 bf16x2_t;
__device__ __forceinline__ unsigned pk2(float a, float b) {
  f32x2 v = {a, b};
  bf16x2_t r = __builtin_convertvector(v, bf16x2_t);
  return __builtin_bit_cast(unsigned, r);
}

__device__ __forceinline__ bf16x4 tr_read(const char* p) {
  return __builtin_amdgcn_ds_read_tr16_b64_v4i16((__attribute__((address_space(3))) bf16x4*)p);
}
template <typename T> __device__ __forceinline__ __attribute__((address_space(3))) T& lds(unsigned a) { return *(__attribute__((address_space(3))) T*)(size_t)a; }
__device__ __forceinline__ bf16x4 tr_read(unsigned a) { return __builtin_amdgcn_ds_read_tr16_b64_v4i16(&lds<bf16x4>(a)); }
__device__ __forceinline__ bf16x8 cat8(bf16x4 a, bf16x4 b) {
  bf16x8 f = {a[0], a[1], a[2], a[3], b[0], b[1], b[2], b[3]};
  return f;
}
__device__ __forceinline__ void glds16s(const void* sbase, unsigned voff, unsigned lds_base) {
  asm volatile("s_mov_b32 m0, %2\n\ts_nop 0\n\tglobal_load_lds_dwordx4 %0, %1" :: "v"(voff), "s"(sbase), "s"(lds_base) : "memory", "m0");
}
__device__ __forceinline__ void glds16c(const void* sbase, unsigned voff, unsigned lds_base) {
  asm volatile("s_mov_b32 m0, %2\n\ts_nop 0\n\tglobal_load_lds_dwordx4 %0, %1 sc1" :: "v"(voff), "s"(sbase), "s"(lds_base) : "memory", "m0");
}
#define GL_ "\n\tglobal_load_lds_dwordx4 "
__device__ __forceinline__ void glds4c(const void* sbase, unsigned v, unsigned lds_base) {
  asm volatile("s_mov_b32 m0, %2\n\ts_nop 0" GL_ "%0, %1 sc1" GL_ "%0, %1 offset:1024 sc1" GL_ "%0, %1 offset:2048 sc1" GL_ "%0, %1 offset:3072 sc1"
               :: "v"(v), "s"(sbase), "s"(lds_base) : "memory", "m0");
}
__device__ __forceinline__ void glds4v(const void* sbase, unsigned v0, unsigned v1, unsigned v2, unsigned v3, unsigned lds_base) {
  asm volatile("s_mov_b32 m0, %5\n\ts_nop 0" GL_ "%0, %4 sc1" GL_ "%1, %4 offset:1024 sc1" GL_ "%2, %4 offset:2048 sc1" GL_ "%3, %4 offset:3072 sc1"
               :: "v"(v0), "v"(v1), "v"(v2), "v"(v3), "s"(sbase), "s"(lds_base) : "memory", "m0");
}
__device__ __forceinline__ void glds2v(const void* sbase, unsigned v0, unsigned v1, unsigned lds_base) {
  asm volatile("s_mov_b32 m0, %3\n\ts_nop 0" GL_ "%0, %2 sc1" GL_ "%1, %2 offset:1024 sc1" :: "v"(v0), "v"(v1), "s"(sbase), "s"(lds_base) : "memory", "m0");
}
__device__ __forceinline__ void gld16(u32x4& d, unsigned voff, const void* sbase) {
  asm volatile("global_load_dwordx4 %0, %1, %2" : "=v"(d) : "v"(voff), "s"(sbase) : "memory");
}
__device__ __forceinline__ unsigned lds_addr(const void* p) { return (unsigned)(size_t)p; }
#define WAITV(n) asm volatile("s_waitcnt vmcnt(" #n ")" ::: "memory")
#define WAITVN(n) asm volatile("s_waitcnt vmcnt(%0)" :: "n"(n) : "memory")
#define WAITL0 do { asm volatile("" ::: "memory"); __builtin_amdgcn_s_waitcnt(0xC07F); asm volatile("" ::: "memory"); } while (0)
#define BAR __builtin_amdgcn_s_barrier()
#define ARRIVED(...) do { WAITV(0); __builtin_amdgcn_sched_barrier(0); asm volatile("" : __VA_ARGS__); } while (0)

template <int N> __device__ __forceinline__ float dpp_ror(float x) {
  return __uint_as_float(__builtin_amdgcn_update_dpp(0u, __float_as_uint(x), 0x120 + N, 0xf, 0xf, false));
}
__device__ __forceinline__ float r16_sum(float x) { x += dpp_ror<8>(x); x += dpp_ror<4>(x); x += dpp_ror<2>(x); return x + dpp_ror<1>(x); }
__device__ __forceinline__ void max8pn(const f32x4& a, const f32x4& b, float& mx, float& mn) {
  asm("v_max3_f32 %0, %2, %3, %4\n\tv_max3_f32 %1, -%2, -%3, -%4\n\tv_max3_f32 %0, %0, %5, %6\n\tv_max3_f32 %1, %1, -%5, -%6\n\t"
      "v_max3_f32 %0, %0, %7, %8\n\tv_max3_f32 %1, %1, -%7, -%8\n\tv_max_f32 %0, %0, %9\n\tv_max_f32 %1, %1, -%9\n\ts_nop 0"
      : "=&v"(mx), "=&v"(mn) : "v"(a[0]), "v"(a[1]), "v"(a[2]), "v"(a[3]), "v"(b[0]), "v"(b[1]), "v"(b[2]), "v"(b[3]));
}
__device__ __forceinline__ float vmaxf_n(float a, float b) { float r; asm("v_max_f32 %0, %1, %2\n\ts_nop 0" : "=v"(r) : "v"(a), "v"(b)); return r; }
template <bool MAXOP> __device__ __forceinline__ float rop(float a, float b) { return MAXOP ? vmaxf_n(a, b) : a + b; }
template <bool MAXOP> __device__ __forceinline__ void xr4(float& a, float& b, float& c, float& d) {
  auto r = __builtin_amdgcn_permlane16_swap(__float_as_uint(a), __float_as_uint(b), false, false);
  auto q = __builtin_amdgcn_permlane16_swap(__float_as_uint(c), __float_as_uint(d), false, false);
  const unsigned ab = __float_as_uint(rop<MAXOP>(__uint_as_float(r[0]), __uint_as_float(r[1])));
  const unsigned cd = __float_as_uint(rop<MAXOP>(__uint_as_float(q[0]), __uint_as_float(q[1])));
  auto t = __builtin_amdgcn_permlane32_swap(ab, cd, false, false);
  const unsigned u = __float_as_uint(rop<MAXOP>(__uint_as_float(t[0]), __uint_as_float(t[1])));
  auto h = __builtin_amdgcn_permlane32_swap(u, u, false, false);
  auto p0 = __builtin_amdgcn_permlane16_swap(h[0], h[0], false, false);
  auto p1 = __builtin_amdgcn_permlane16_swap(h[1], h[1], false, false);
  a = __uint_as_float(p0[0]); b = __uint_as_float(p0[1]); c = __uint_as_float(p1[0]); d = __uint_as_float(p1[1]);
}

__device__ __forceinline__ unsigned xs4(float a, float b, float c, float d) {
  auto r = __builtin_amdgcn_permlane16_swap(__float_as_uint(a), __float_as_uint(b), false, false);
  auto q = __builtin_amdgcn_permlane16_swap(__float_as_uint(c), __float_as_uint(d), false, false);
  auto t = __builtin_amdgcn_permlane32_swap(__float_as_uint(__uint_as_float(r[0]) + __uint_as_float(r[1])), __float_as_uint(__uint_as_float(q[0]) + __uint_as_float(q[1])), false, false);
  return __float_as_uint(__uint_as_float(t[0]) + __uint_as_float(t[1]));
}

__device__ __forceinline__ f32x4 sum4(const u32x4 (&v)[4]) {
  float r0, r1, r2, r3, t0, t1, t2, t3;
  asm("v_add_f32 %0, %8, %12\n\tv_add_f32 %1, %9, %13\n\tv_add_f32 %2, %10, %14\n\tv_add_f32 %3, %11, %15\n\t"
      "v_add_f32 %4, %16, %20\n\tv_add_f32 %5, %17, %21\n\tv_add_f32 %6, %18, %22\n\tv_add_f32 %7, %19, %23\n\t"
      "v_add_f32 %0, %0, %4\n\tv_add_f32 %1, %1, %5\n\tv_add_f32 %2, %2, %6\n\tv_add_f32 %3, %3, %7\n\ts_nop 0"
      : "=&v"(r0), "=&v"(r1), "=&v"(r2), "=&v"(r3), "=&v"(t0), "=&v"(t1), "=&v"(t2), "=&v"(t3)
      : "v"(v[0][0]), "v"(v[0][1]), "v"(v[0][2]), "v"(v[0][3]), "v"(v[1][0]), "v"(v[1][1]), "v"(v[1][2]), "v"(v[1][3]),
        "v"(v[2][0]), "v"(v[2][1]), "v"(v[2][2]), "v"(v[2][3]), "v"(v[3][0]), "v"(v[3][1]), "v"(v[3][2]), "v"(v[3][3]));
  return f32x4{r0, r1, r2, r3};
}
#define MFMA(a, b, c) __builtin_amdgcn_mfma_f32_16x16x32_bf16(a, b, c, 0, 0, 0)

typedef unsigned long long u64;
typedef __attribute__((address_space(1))) unsigned long long gu64;
constexpr unsigned WS_FLAGS = 4096;
constexpr int FLS = 64;
constexpr unsigned WS_G0 = 65536;
constexpr unsigned WS_CSP = WS_G0 + SEQ * 16;
constexpr unsigned WS_ST = WS_CSP + 16 * 16 * 768;
constexpr unsigned WS_X = 1u << 21;
constexpr unsigned WS_O = WS_X + 3u * SEQ * DM * 2;
constexpr unsigned KVPB = KVP * 2, KVFB = KVP * 4;
constexpr unsigned EMPTY = 0xffffffffu;
constexpr unsigned WS_KV = WS_O + 4u * SEQ * DM * 2;
constexpr unsigned KVLB = 16u * NH * KVPB + 16u * KVFB;
constexpr unsigned WS_HL = WS_KV + 2u * KVLB;
constexpr unsigned WS_CSA = WS_HL + 512u * 4096;
constexpr unsigned WS_CPA = WS_CSA + 256u * 1024;
constexpr unsigned WS_IMG = WS_CPA + 16 * 16 * 768;
constexpr unsigned WS_END = WS_IMG + 2048 * 1024;
constexpr int LDS_TOTAL = 163840;
constexpr int OFF_WO = 49152;
struct Next { u64 kord, ko2; const char* wq; unsigned tw, kf; };
template <int V> using IC = std::integral_constant<int, V>;
__device__ __forceinline__ bf16x8 ones8() { unsigned e = 0x3F803F80u; asm volatile("" : "+v"(e)); return __builtin_bit_cast(bf16x8, u32x4{e, e, e, e}); }
__device__ __forceinline__ float zero1() { float z; asm volatile("v_mov_b32 %0, 0" : "=v"(z)); return z; }
__device__ __forceinline__ u32x4 empty4() { unsigned e = EMPTY; asm volatile("" : "+v"(e)); return u32x4{e, e, e, e}; }
__device__ __forceinline__ u64 korder(int v, int own) {
  const u64 t = v == 0 ? 0x3b210fedca987654ull : v == 1 ? 0xb37654210fedca98ull : v == 2 ? 0x3ba987654210fedcull : 0xb3fedca987654210ull;
  const u64 inv = v == 0 ? 0xa987e6543210fdcbull : v == 1 ? 0x6543f210dcbae987ull : v == 2 ? 0x3210edcba987f654ull : 0xdcbaf9876543e210ull;
  const u64 low = (1ull << (4 * ((inv >> (4 * own)) & 15))) - 1ull;
  return (u64)own | ((t & low) << 4) | (t & ~((low << 4) | 15ull));
}
__device__ __forceinline__ u64 korder0(int rt) {
  const int r = rt & 7;
  const u64 own = (0x765432765432ull >> (4 * (r < 6 ? r : r - 6))) & 0xffffffull, oth = 0x7654321076543210ull >> (4 * r);
  const u64 t = 0x10ull | ((oth & 15ull) << 8) | (own << 12) | ((oth >> 4) << 36);
  return t | (rt < 8 ? 0x8888888000000800ull : 0x0000000888888088ull);
}
__device__ __forceinline__ int knib(u64 t, int s) { return (int)(t >> (4 * s)) & 15; }
__device__ __forceinline__ unsigned kfirst(u64 t) { return (1u << knib(t, 1)) | (1u << knib(t, 2)) | (1u << knib(t, 3)) | (1u << knib(t, 4)); }
__device__ __forceinline__ int kv1(int bid) { return (bid + (0x4924 >> ((bid >> 2) & 14))) & 3; }

__device__ __forceinline__ bool img(int layer, int hp) { return layer >= 2 && (hp & 7) == 3; }
__device__ __forceinline__ const char* wq_src(const char* ws, const void* W, int layer, int hp) { return img(layer, hp) ? ws + WS_IMG + ((layer & 1) * 768 + (hp >> 3) * 384) * 1024 : (const char*)W; }
__device__ __forceinline__ const char* wo_src(const char* ws, const void* W, int layer, int hp) { return img(layer, hp) ? ws + WS_IMG + (1536 + (layer & 1) * 256 + (hp >> 3) * 128) * 1024 : (const char*)W; }

template <typename T> __device__ __forceinline__ T* uni(T* p) {
  const unsigned long long v = (unsigned long long)p;
  const unsigned lo = __builtin_amdgcn_readfirstlane((unsigned)v), hi = __builtin_amdgcn_readfirstlane((unsigned)(v >> 32));
  return (T*)(((unsigned long long)hi << 32) | lo);
}
__device__ __forceinline__ int lane_id() {
  int l;
  asm volatile("v_mbcnt_lo_u32_b32 %0, -1, 0\n\tv_mbcnt_hi_u32_b32 %0, -1, %0" : "=v"(l));
  __builtin_assume(l >= 0 && l < 64);
  return l;
}
#define IDS \
  const int lane = lane_id(); \
  asm volatile("" : "+s"(w)); \
  const int tid = w * 64 + lane; \
  int bid = blockIdx.x; asm volatile("" : "+s"(bid)); \
  ws = uni(ws); asm volatile("" : "+s"(ws)); \
  layer = __builtin_amdgcn_readfirstlane(layer); x0 = uni(x0); wo = uni(wo); \
    \
  const int rt = 2 * (bid & 4) + ((bid >> 3) & 7); \
  const int hp = 4 * (bid & 3) + (bid >> 6); \
  const int m0 = rt * 128; \
  const int fr = lane & 15, g = lane >> 4; \
  const int q4 = (lane & 15) >> 2, p4 = lane & 3; \
  const __amdgpu_buffer_rsrc_t rsW = __builtin_amdgcn_make_buffer_rsrc((void*)ws, (short)0, (int)WS_END, 0x00020000); \
  (void)m0; (void)fr; (void)q4; (void)p4; (void)rsW; (void)g;

__device__ __forceinline__ void code_touch(unsigned& junk, int bid, int lane) {
  unsigned long long pc;
  unsigned len, d;
  asm volatile("s_getpc_b64 %0\n.Lct%=:\n\ts_mov_b32 %2, .Lct%=-.Lmk_pc\n\ts_mov_b32 %1, .Lmk_end-.Lmk_pc" : "=s"(pc), "=s"(len), "=s"(d));
  pc -= d;
  const unsigned per = ((len >> 10) + 1) * 128, off = ((bid >> 3) & 7) * per + (lane * 128 < per ? lane * 128 : 0);
  asm volatile("global_load_dword %0, %1, %2" : "+v"(junk) : "v"(off < len - 4 ? off : 0u), "s"(pc) : "memory");
}

__device__ __forceinline__ void publish(__amdgpu_buffer_rsrc_t rsW, int tid, unsigned idx, u64 v) {
  unsigned off = WS_FLAGS + idx * FLS;
  asm volatile("" : "+v"(off), "+v"(v));
  WAITV(0);
  WAITL0; BAR;
  if (tid == 192) __builtin_amdgcn_raw_buffer_store_b64(__builtin_bit_cast(u32x2, v), rsW, off, 0, 16);
  asm volatile("" ::: "memory");
}
__device__ __forceinline__ void poll_flags(char* ws, u64 E, int set, int rt, int hp, int lane, unsigned n, bool col) {
  const int l = lane & 15;
  const unsigned fo = WS_FLAGS + (unsigned)(set * 256 + (col ? l * 16 + hp : rt * 16 + l)) * FLS;
  _Pragma("unroll 1") for (unsigned spins = 0; spins < (1u << 18); ++spins) {
    u64 d;
    asm volatile("global_load_dwordx2 %0, %1, %2 sc1\n\ts_waitcnt vmcnt(0)" : "=v"(d) : "v"(fo), "s"(ws) : "memory");
    if (__all(d - (E + n) <= (u64)(16u - n))) break;
    __builtin_amdgcn_s_sleep(1);
  }
}

__device__ __forceinline__ u64 poll_eq(const char* ws, unsigned off, u64 want, u64 mask, u64 now) {
  u64 miss;
  _Pragma("unroll 1") for (unsigned spins = 0; spins < (1u << 18); ++spins) {
    u64 d;
    asm volatile("global_load_dwordx2 %0, %1, %2 sc1\n\ts_waitcnt vmcnt(0)" : "=v"(d) : "v"(off), "s"(ws) : "memory");
    miss = __ballot(d != want) & mask;
    if (!(miss & now)) break;
    __builtin_amdgcn_s_sleep(1);
  }
  return miss;
}

#define G1_MACROS \
  ".macro G1M rb, j, s, z\n" \
  " .set g1d, 160+24*(\\rb)+4*(((\\j)+2)-6*((\\j)>>2))\n .set g1a, 80+40*(\\s)+4*(\\rb)\n .set g1b, 96+40*(\\s)+4*(\\j)\n" \
  " .if ((\\j)>>1) == 1\n  .set g1t, g1a\n  .set g1a, g1b\n  .set g1b, g1t\n .endif\n" \
  " .if \\z\n  v_mfma_f32_16x16x32_bf16 v[g1d:g1d+3], v[g1b:g1b+3], v[g1a:g1a+3], 0\n" \
  " .else\n  v_mfma_f32_16x16x32_bf16 v[g1d:g1d+3], v[g1b:g1b+3], v[g1a:g1a+3], v[g1d:g1d+3]\n .endif\n" \
  ".endm\n" \
  ".macro G1RA rb, s, ra, oa\n" \
  " ds_read_b128 v[80+40*(\\s)+4*(\\rb):83+40*(\\s)+4*(\\rb)], \\ra offset:(\\oa)+(\\rb)*2048\n" \
  ".endm\n" \
  ".macro G1RB k, s, r0, r1, ob\n" \
  " .if (\\k) & 2\n  ds_read_b64_tr_b16 v[96+40*(\\s)+2*(\\k):97+40*(\\s)+2*(\\k)], \\r1 offset:(\\ob)+(((\\k)>>2)+1-3*((\\k)>>3))*2048+((\\k)&1)*512\n" \
  " .else\n  ds_read_b64_tr_b16 v[96+40*(\\s)+2*(\\k):97+40*(\\s)+2*(\\k)], \\r0 offset:(\\ob)+(((\\k)>>2)+1-3*((\\k)>>3))*2048+((\\k)&1)*512\n .endif\n" \
  ".endm\n" \
    \
  ".macro G1BK i, g, r0, r1, ob\n G1RB 4*(\\g)+((\\i)&3), (\\i)>>2, \\r0, \\r1, (\\ob)+((\\i)>>2)*12288\n.endm\n" \
  ".macro G1AK rb, s, a0, a1, oa\n .if \\s\n  G1RA \\rb, 1, \\a1, \\oa\n .else\n  G1RA \\rb, 0, \\a0, \\oa\n .endif\n.endm\n" \
  ".macro G1RDB g, r0, r1, ob\n .set g1i, 0\n .rept 8\n  G1BK g1i, \\g, \\r0, \\r1, \\ob\n  .set g1i, g1i+1\n .endr\n.endm\n" \
  ".macro G1RDA a0, a1, oa\n .set g1i, 0\n .rept 8\n  G1AK (g1i>>1)^2, g1i&1, \\a0, \\a1, \\oa\n  .set g1i, g1i+1\n .endr\n.endm\n" \
    \
  ".macro G1Q g, rb, q, i, z\n G1M \\rb, 2*(\\g)+((((\\i)>>1)^(\\q))&1), (((\\i)+1)>>1)&1, (\\z)&&(((\\i)&1)==0)\n.endm\n" \
    \
  ".macro G1SG g, z, rd, a0, a1, r0, r1, o\n" \
  " .set g1i, 0\n .rept 16\n" \
  "  .if ((\\g) == 0) && (g1i == 8) && (\\rd)\n   s_waitcnt lgkmcnt(7)\n   s_nop 0\n  .endif\n" \
  "  .if ((\\g) == 0) && (g1i == 15)\n   s_waitcnt lgkmcnt(0)\n  .endif\n" \
  "  G1Q \\g, (g1i>>2)^(2-((\\g)&1)), g1i>>2, g1i&3, \\z\n" \
  "  .if \\rd\n" \
  "   .if (\\g) == 0\n    .if g1i < 7\n     G1BK g1i, 2, \\r0, \\r1, \\o\n    .elseif g1i == 8\n     G1BK 7, 2, \\r0, \\r1, \\o\n    .endif\n" \
  "   .elseif (\\g) == 1\n    .if g1i < 8\n     G1BK g1i, 0, \\r0, \\r1, \\o\n    .endif\n" \
  "   .elseif (g1i & 2) == 0\n    G1BK (g1i&1)+2*(g1i>>2), 1, \\r0, \\r1, \\o\n" \
  "   .elseif (\\rd) == 1\n    G1AK (g1i>>2)^2, 1-(g1i&1), \\a0, \\a1, \\o\n   .endif\n" \
  "  .endif\n" \
  "  .set g1i, g1i+1\n .endr\n" \
  ".endm\n" \
  ""
#define G1_SLIDE \
  "G1Q 1, 3, 0, 0, 0\n ds_read2_b64 v[60:63], v49 offset0:32 offset1:48\n ds_read_b128 v[96:99], v48 offset:256\n G1Q 1, 3, 0, 1, 0\n " \
  "ds_read_b128 v[100:103], v48 offset:320\n ds_read2_b32 v[56:57], v52 offset1:16\n G1Q 1, 3, 0, 2, 0\n ds_read_b128 v[136:139], v54 offset:192\n " \
  "ds_read_b128 v[140:143], v54 offset:704\n G1Q 1, 3, 0, 3, 0\n G1Q 1, 2, 1, 0, 0\n G1Q 1, 2, 1, 1, 0\n s_waitcnt lgkmcnt(3)\n " \
  "v_fmac_f32 v240, v96, v62\n G1Q 1, 2, 1, 2, 0\n v_fma_f32 v241, v97, v62, v241\n v_fma_f32 v242, v98, v62, v242\n G1Q 1, 2, 1, 3, 0\n " \
  "v_fma_f32 v243, v99, v62, v243\n v_fma_f32 v244, v100, v62, v244\n G1Q 1, 1, 2, 0, 0\n v_fma_f32 v245, v101, v62, v245\n " \
  "v_fma_f32 v246, v102, v62, v246\n G1Q 1, 1, 2, 1, 0\n v_fma_f32 v247, v103, v62, v247\n v_mul_f32_e64 v240, v63, v240\n G1Q 1, 1, 2, 2, 0\n " \
  "v_mul_f32_e64 v241, v63, v241\n v_mul_f32_e64 v242, v63, v242\n G1Q 1, 1, 2, 3, 0\n v_mul_f32_e64 v243, v63, v243\n v_mul_f32_e64 v244, v63, v244\n " \
  "G1Q 1, 0, 3, 0, 0\n v_mul_f32_e64 v245, v63, v245\n v_mul_f32_e64 v246, v63, v246\n G1Q 1, 0, 3, 1, 0\n v_mul_f32_e64 v247, v63, v247\n " \
  "v_cvt_pk_bf16_f32 v240, v240, v241\n G1Q 1, 0, 3, 2, 0\n v_cvt_pk_bf16_f32 v241, v242, v243\n v_cvt_pk_bf16_f32 v242, v244, v245\n " \
  "G1Q 1, 0, 3, 3, 0\n v_cvt_pk_bf16_f32 v243, v246, v247\n s_waitcnt lgkmcnt(0)\n v_fmac_f32 v248, v136, v56\n G1Q 2, 2, 0, 0, 0\n " \
  "ds_read2_b64 v[144:147], v49 offset1:16\n v_fma_f32 v249, v137, v56, v249\n G1Q 2, 2, 0, 1, 0\n ds_read_b128 v[148:151], v48\n " \
  "v_fma_f32 v250, v138, v56, v250\n G1Q 2, 2, 0, 2, 0\n ds_read_b128 v[104:107], v48 offset:64\n v_fma_f32 v251, v139, v56, v251\n G1Q 2, 2, 0, 3, 0\n " \
  "ds_read_b128 v[108:111], v54 offset:128\n v_fma_f32 v252, v136, v57, v252\n G1Q 2, 3, 1, 0, 0\n v_fma_f32 v253, v137, v57, v253\n " \
  "v_fma_f32 v254, v138, v57, v254\n G1Q 2, 3, 1, 1, 0\n ds_read_b128 v[88:91], v54 offset:640\n v_fma_f32 v255, v139, v57, v255\n G1Q 2, 3, 1, 2, 0\n " \
  "ds_read_b128 v[128:131], v54\n v_mul_f32_e64 v248, v140, v248\n G1Q 2, 3, 1, 3, 0\n v_mul_f32_e64 v249, v141, v249\n v_mul_f32_e64 v250, v142, v250\n " \
  "G1Q 2, 0, 2, 0, 0\n v_mul_f32_e64 v251, v143, v251\n v_mul_f32_e64 v252, v140, v252\n G1Q 2, 0, 2, 1, 0\n ds_read_b128 v[92:95], v54 offset:512\n " \
  "v_mul_f32_e64 v253, v141, v253\n G1Q 2, 0, 2, 2, 0\n v_mul_f32_e64 v254, v142, v254\n v_mul_f32_e64 v255, v143, v255\n G1Q 2, 0, 2, 3, 0\n " \
  "v_cvt_pk_bf16_f32 v248, v248, v249\n v_cvt_pk_bf16_f32 v249, v250, v251\n G1Q 2, 1, 3, 0, 0\n v_cvt_pk_bf16_f32 v255, v254, v255\n " \
  "v_cvt_pk_bf16_f32 v254, v252, v253\n G1Q 2, 1, 3, 1, 0\n v_min_u32_e64 v136, -2, v240\n v_min_u32_e64 v137, -2, v241\n v_min_u32_e64 v138, -2, v242\n " \
  "G1Q 2, 1, 3, 2, 0\n v_min_u32_e64 v139, -2, v243\n v_min_u32_e64 v140, -2, v248\n v_min_u32_e64 v141, -2, v249\n G1Q 2, 1, 3, 3, 0\n " \
  "v_min_u32_e64 v142, -2, v254\n v_min_u32_e64 v143, -2, v255\n ds_read_b128 v[112:115], v54 offset:64\n ds_read_b128 v[116:119], v54 offset:576\n " \
  "s_cmp_eq_u32 %[wm], 0\n s_cbranch_scc1 .Lg1d%=\n buffer_store_dwordx4 v[136:139], v55, %[rs], 0 offen sc1\n " \
  "buffer_store_dwordx4 v[140:143], v55, %[rs], 0 offen offset:1024 sc1\n ds_read_b32 v53, v59\n ds_read_b32 v53, v59\n ds_read_b32 v53, v59\n " \
  "s_branch .Lg1e%=\n .Lg1d%=:\n v_mov_b32 v53, 1\n ds_write_b128 v58, v[136:139]\n ds_write_b128 v58, v[140:143] offset:1024\n ds_write_b32 v59, v53\n " \
  ".Lg1e%=:\n s_waitcnt lgkmcnt(8)\n v_pk_fma_f32 v[232:233], v[148:149], v[62:63], v[232:233] op_sel_hi:[1,0,1]\n " \
  "v_pk_fma_f32 v[234:235], v[150:151], v[62:63], v[234:235] op_sel_hi:[1,0,1]\n " \
  "v_pk_fma_f32 v[236:237], v[104:105], v[62:63], v[236:237] op_sel_hi:[1,0,1]\n " \
  "v_pk_fma_f32 v[238:239], v[106:107], v[62:63], v[238:239] op_sel_hi:[1,0,1]\n " \
  "v_pk_fma_f32 v[216:217], v[96:97], v[60:61], v[216:217] op_sel_hi:[1,0,1]\n " \
  "v_pk_fma_f32 v[218:219], v[98:99], v[60:61], v[218:219] op_sel_hi:[1,0,1]\n " \
  "v_pk_fma_f32 v[220:221], v[100:101], v[60:61], v[220:221] op_sel_hi:[1,0,1]\n " \
  "v_pk_fma_f32 v[222:223], v[102:103], v[60:61], v[222:223] op_sel_hi:[1,0,1]\n v_pk_mul_f32 v[216:217], v[60:61], v[216:217] op_sel:[1,0]\n " \
  "v_pk_mul_f32 v[218:219], v[60:61], v[218:219] op_sel:[1,0]\n v_pk_mul_f32 v[220:221], v[60:61], v[220:221] op_sel:[1,0]\n " \
  "v_pk_mul_f32 v[222:223], v[60:61], v[222:223] op_sel:[1,0]\n v_cvt_pk_bf16_f32 v232, v232, v233\n v_cvt_pk_bf16_f32 v233, v234, v235\n " \
  "v_cvt_pk_bf16_f32 v234, v236, v237\n v_cvt_pk_bf16_f32 v235, v238, v239\n v_cvt_pk_bf16_f32 v216, v216, v217\n v_cvt_pk_bf16_f32 v217, v218, v219\n " \
  "v_cvt_pk_bf16_f32 v218, v220, v221\n v_cvt_pk_bf16_f32 v219, v222, v223\n " \
  "v_pk_fma_f32 v[208:209], v[148:149], v[60:61], v[208:209] op_sel_hi:[1,0,1]\n " \
  "v_pk_fma_f32 v[210:211], v[150:151], v[60:61], v[210:211] op_sel_hi:[1,0,1]\n " \
  "v_pk_fma_f32 v[212:213], v[104:105], v[60:61], v[212:213] op_sel_hi:[1,0,1]\n " \
  "v_pk_fma_f32 v[214:215], v[106:107], v[60:61], v[214:215] op_sel_hi:[1,0,1]\n " \
  "v_pk_fma_f32 v[192:193], v[96:97], v[146:147], v[192:193] op_sel_hi:[1,0,1]\n " \
  "v_pk_fma_f32 v[194:195], v[98:99], v[146:147], v[194:195] op_sel_hi:[1,0,1]\n " \
  "v_pk_fma_f32 v[196:197], v[100:101], v[146:147], v[196:197] op_sel_hi:[1,0,1]\n " \
  "v_pk_fma_f32 v[198:199], v[102:103], v[146:147], v[198:199] op_sel_hi:[1,0,1]\n v_pk_mul_f32 v[192:193], v[146:147], v[192:193] op_sel:[1,0]\n " \
  "v_pk_mul_f32 v[194:195], v[146:147], v[194:195] op_sel:[1,0]\n v_pk_mul_f32 v[196:197], v[146:147], v[196:197] op_sel:[1,0]\n " \
  "v_pk_mul_f32 v[198:199], v[146:147], v[198:199] op_sel:[1,0]\n v_mfma_f32_16x16x32_bf16 v[120:123], v[240:243], v[232:235], v[40:43]\n " \
  "v_mfma_f32_16x16x32_bf16 v[236:239], v[216:219], v[232:235], v[36:39]\n v_cvt_pk_bf16_f32 v208, v208, v209\n v_cvt_pk_bf16_f32 v209, v210, v211\n " \
  "v_cvt_pk_bf16_f32 v210, v212, v213\n v_cvt_pk_bf16_f32 v211, v214, v215\n v_cvt_pk_bf16_f32 v192, v192, v193\n v_cvt_pk_bf16_f32 v193, v194, v195\n " \
  "v_cvt_pk_bf16_f32 v194, v196, v197\n v_cvt_pk_bf16_f32 v195, v198, v199\n " \
  "v_pk_fma_f32 v[184:185], v[148:149], v[146:147], v[184:185] op_sel_hi:[1,0,1]\n " \
  "v_pk_fma_f32 v[186:187], v[150:151], v[146:147], v[186:187] op_sel_hi:[1,0,1]\n " \
  "v_pk_fma_f32 v[188:189], v[104:105], v[146:147], v[188:189] op_sel_hi:[1,0,1]\n " \
  "v_pk_fma_f32 v[190:191], v[106:107], v[146:147], v[190:191] op_sel_hi:[1,0,1]\n " \
  "v_pk_fma_f32 v[168:169], v[96:97], v[144:145], v[168:169] op_sel_hi:[1,0,1]\n " \
  "v_pk_fma_f32 v[170:171], v[98:99], v[144:145], v[170:171] op_sel_hi:[1,0,1]\n " \
  "v_pk_fma_f32 v[172:173], v[100:101], v[144:145], v[172:173] op_sel_hi:[1,0,1]\n " \
  "v_pk_fma_f32 v[174:175], v[102:103], v[144:145], v[174:175] op_sel_hi:[1,0,1]\n v_pk_mul_f32 v[168:169], v[144:145], v[168:169] op_sel:[1,0]\n " \
  "v_pk_mul_f32 v[170:171], v[144:145], v[170:171] op_sel:[1,0]\n v_pk_mul_f32 v[172:173], v[144:145], v[172:173] op_sel:[1,0]\n " \
  "v_pk_mul_f32 v[174:175], v[144:145], v[174:175] op_sel:[1,0]\n v_mfma_f32_16x16x32_bf16 v[220:223], v[216:219], v[208:211], v[40:43]\n " \
  "v_mfma_f32_16x16x32_bf16 v[212:215], v[192:195], v[208:211], v[36:39]\n v_cvt_pk_bf16_f32 v184, v184, v185\n v_cvt_pk_bf16_f32 v185, v186, v187\n " \
  "v_cvt_pk_bf16_f32 v186, v188, v189\n v_cvt_pk_bf16_f32 v187, v190, v191\n v_cvt_pk_bf16_f32 v168, v168, v169\n v_cvt_pk_bf16_f32 v169, v170, v171\n " \
  "v_cvt_pk_bf16_f32 v170, v172, v173\n v_cvt_pk_bf16_f32 v171, v174, v175\n " \
  "v_pk_fma_f32 v[160:161], v[148:149], v[144:145], v[160:161] op_sel_hi:[1,0,1]\n " \
  "v_pk_fma_f32 v[162:163], v[150:151], v[144:145], v[162:163] op_sel_hi:[1,0,1]\n " \
  "v_pk_fma_f32 v[164:165], v[104:105], v[144:145], v[164:165] op_sel_hi:[1,0,1]\n " \
  "v_pk_fma_f32 v[166:167], v[106:107], v[144:145], v[166:167] op_sel_hi:[1,0,1]\n s_waitcnt lgkmcnt(3)\n " \
  "v_pk_fma_f32 v[176:177], v[128:129], v[56:57], v[176:177] op_sel_hi:[1,0,1]\n " \
  "v_pk_fma_f32 v[178:179], v[130:131], v[56:57], v[178:179] op_sel_hi:[1,0,1]\n " \
  "v_pk_fma_f32 v[180:181], v[128:129], v[56:57], v[180:181] op_sel:[0,1,0]\n v_pk_fma_f32 v[182:183], v[130:131], v[56:57], v[182:183] op_sel:[0,1,0]\n " \
  "v_pk_mul_f32 v[176:177], v[176:177], v[92:93]\n v_pk_mul_f32 v[178:179], v[178:179], v[94:95]\n v_pk_mul_f32 v[180:181], v[180:181], v[92:93]\n " \
  "v_pk_mul_f32 v[182:183], v[182:183], v[94:95]\n v_mfma_f32_16x16x32_bf16 v[196:199], v[192:195], v[184:187], v[40:43]\n " \
  "v_mfma_f32_16x16x32_bf16 v[188:191], v[168:171], v[184:187], v[36:39]\n v_cvt_pk_bf16_f32 v160, v160, v161\n v_cvt_pk_bf16_f32 v161, v162, v163\n " \
  "v_cvt_pk_bf16_f32 v162, v164, v165\n v_cvt_pk_bf16_f32 v163, v166, v167\n v_cvt_pk_bf16_f32 v174, v176, v177\n v_cvt_pk_bf16_f32 v175, v178, v179\n " \
  "v_cvt_pk_bf16_f32 v180, v180, v181\n v_cvt_pk_bf16_f32 v181, v182, v183\n " \
  "v_pk_fma_f32 v[224:225], v[108:109], v[56:57], v[224:225] op_sel_hi:[1,0,1]\n " \
  "v_pk_fma_f32 v[226:227], v[110:111], v[56:57], v[226:227] op_sel_hi:[1,0,1]\n " \
  "v_pk_fma_f32 v[228:229], v[108:109], v[56:57], v[228:229] op_sel:[0,1,0]\n v_pk_fma_f32 v[230:231], v[110:111], v[56:57], v[230:231] op_sel:[0,1,0]\n " \
  "v_pk_mul_f32 v[224:225], v[224:225], v[88:89]\n v_pk_mul_f32 v[226:227], v[226:227], v[90:91]\n v_pk_mul_f32 v[228:229], v[228:229], v[88:89]\n " \
  "v_pk_mul_f32 v[230:231], v[230:231], v[90:91]\n v_mfma_f32_16x16x32_bf16 v[124:127], v[168:171], v[160:163], v[40:43]\n " \
  "v_cvt_pk_bf16_f32 v246, v224, v225\n v_cvt_pk_bf16_f32 v247, v226, v227\n v_cvt_pk_bf16_f32 v252, v228, v229\n v_cvt_pk_bf16_f32 v253, v230, v231\n " \
  "v_max3_f32 v140, v236, v237, v238\n v_max3_f32 v138, v212, v213, v214\n v_max3_f32 v141, v239, v120, v121\n v_max3_f32 v139, v215, v220, v221\n " \
  "v_max3_f32 v140, v140, v122, v123\n v_max3_f32 v138, v138, v222, v223\n v_max_f32 v140, v140, v141\n v_max_f32 v138, v138, v139\n " \
  "v_max3_f32 v136, v188, v189, v190\n v_max3_f32 v137, v191, v196, v197\n v_max3_f32 v136, v136, v198, v199\n " \
  "v_pk_fma_f32 v[200:201], v[112:113], v[56:57], v[200:201] op_sel_hi:[1,0,1]\n " \
  "v_pk_fma_f32 v[202:203], v[114:115], v[56:57], v[202:203] op_sel_hi:[1,0,1]\n v_max_f32 v136, v136, v137\n v_mov_b32 v142, v140\n " \
  "v_pk_fma_f32 v[204:205], v[112:113], v[56:57], v[204:205] op_sel:[0,1,0]\n v_pk_fma_f32 v[206:207], v[114:115], v[56:57], v[206:207] op_sel:[0,1,0]\n " \
  "v_permlane16_swap_b32 v136, v138\n v_permlane16_swap_b32 v140, v142\n v_pk_mul_f32 v[200:201], v[200:201], v[116:117]\n " \
  "v_pk_mul_f32 v[202:203], v[202:203], v[118:119]\n v_max_f32 v136, v136, v138\n v_max_f32 v140, v140, v142\n " \
  "v_pk_mul_f32 v[204:205], v[204:205], v[116:117]\n v_pk_mul_f32 v[206:207], v[206:207], v[118:119]\n v_permlane32_swap_b32 v136, v140\n " \
  "v_mul_f32 v152, 0x3e8293ee, v63\n v_mul_f32 v154, 0x3e8293ee, v61\n v_max_f32 v136, v136, v140\n v_mov_b32 v140, v136\n " \
  "v_mul_f32 v156, 0x3e8293ee, v147\n v_mul_f32 v158, 0x3e8293ee, v145\n v_permlane32_swap_b32 v136, v140\n v_mov_b32 v132, 0x3f803f80\n " \
  "v_mov_b32 v133, 0x3f803f80\n v_mov_b32 v138, v136\n v_mov_b32 v134, 0x3f803f80\n v_mov_b32 v135, 0x3f803f80\n v_permlane16_swap_b32 v136, v138\n " \
  "v_cvt_pk_bf16_f32 v244, v200, v201\n v_cvt_pk_bf16_f32 v245, v202, v203\n v_cvt_pk_bf16_f32 v250, v204, v205\n v_cvt_pk_bf16_f32 v251, v206, v207\n " \
  "v_cvt_pk_bf16_f32 v176, v200, v201\n v_cvt_pk_bf16_f32 v177, v202, v203\n v_cvt_pk_bf16_f32 v182, v204, v205\n v_cvt_pk_bf16_f32 v183, v206, v207\n " \
  "v_mul_f32_e64 v153, v152, -v140\n v_mul_f32_e64 v155, v154, -v138\n v_mul_f32_e64 v157, v156, -v136\n " \
  "v_pk_fma_f32 v[236:237], v[236:237], v[152:153], v[152:153] op_sel:[0,0,1] op_sel_hi:[1,0,1]\n " \
  "v_pk_fma_f32 v[238:239], v[238:239], v[152:153], v[152:153] op_sel:[0,0,1] op_sel_hi:[1,0,1]\n " \
  "v_pk_fma_f32 v[120:121], v[120:121], v[152:153], v[152:153] op_sel:[0,0,1] op_sel_hi:[1,0,1]\n " \
  "v_pk_fma_f32 v[122:123], v[122:123], v[152:153], v[152:153] op_sel:[0,0,1] op_sel_hi:[1,0,1]\n v_exp_f32 v236, v236\n v_exp_f32 v237, v237\n " \
  "v_exp_f32 v238, v238\n v_exp_f32 v239, v239\n v_exp_f32 v120, v120\n v_exp_f32 v121, v121\n v_exp_f32 v122, v122\n v_exp_f32 v123, v123\n " \
  "v_pk_fma_f32 v[212:213], v[212:213], v[154:155], v[154:155] op_sel:[0,0,1] op_sel_hi:[1,0,1]\n " \
  "v_pk_fma_f32 v[214:215], v[214:215], v[154:155], v[154:155] op_sel:[0,0,1] op_sel_hi:[1,0,1]\n " \
  "v_pk_fma_f32 v[220:221], v[220:221], v[154:155], v[154:155] op_sel:[0,0,1] op_sel_hi:[1,0,1]\n " \
  "v_pk_fma_f32 v[222:223], v[222:223], v[154:155], v[154:155] op_sel:[0,0,1] op_sel_hi:[1,0,1]\n v_cvt_pk_bf16_f32 v236, v236, v237\n " \
  "v_cvt_pk_bf16_f32 v237, v238, v239\n v_cvt_pk_bf16_f32 v238, v120, v121\n v_cvt_pk_bf16_f32 v239, v122, v123\n v_exp_f32 v212, v212\n " \
  "v_exp_f32 v213, v213\n v_mfma_f32_16x16x32_bf16 v[120:123], v[132:135], v[236:239], 0\n " \
  "v_mfma_f32_16x16x32_bf16 v[232:235], v[246:249], v[236:239], 0\n v_mfma_f32_16x16x32_bf16 v[240:243], v[252:255], v[236:239], 0\n " \
  "v_exp_f32 v214, v214\n v_exp_f32 v215, v215\n v_exp_f32 v220, v220\n v_exp_f32 v221, v221\n v_exp_f32 v222, v222\n v_exp_f32 v223, v223\n " \
  "v_pk_fma_f32 v[188:189], v[188:189], v[156:157], v[156:157] op_sel:[0,0,1] op_sel_hi:[1,0,1]\n " \
  "v_pk_fma_f32 v[190:191], v[190:191], v[156:157], v[156:157] op_sel:[0,0,1] op_sel_hi:[1,0,1]\n " \
  "v_pk_fma_f32 v[196:197], v[196:197], v[156:157], v[156:157] op_sel:[0,0,1] op_sel_hi:[1,0,1]\n " \
  "v_pk_fma_f32 v[198:199], v[198:199], v[156:157], v[156:157] op_sel:[0,0,1] op_sel_hi:[1,0,1]\n v_cvt_pk_bf16_f32 v212, v212, v213\n " \
  "v_cvt_pk_bf16_f32 v213, v214, v215\n v_cvt_pk_bf16_f32 v214, v220, v221\n v_cvt_pk_bf16_f32 v215, v222, v223\n v_exp_f32 v188, v188\n " \
  "s_cmp_eq_u32 %[wm], 0\n s_cbranch_scc1 .Lg1f%=\n ds_read_b32 v53, v59\n ds_read_b128 v[80:83], v58\n ds_read_b64 v[172:173], v58 offset:1024\n " \
  "ds_read_b64 v[178:179], v58 offset:1032\n s_branch .Lg1g%=\n .Lg1f%=:\n buffer_load_dwordx4 v[80:83], v55, %[rs], 0 offen sc1\n " \
  "buffer_load_dwordx2 v[172:173], v55, %[rs], 0 offen offset:1024 sc1\n buffer_load_dwordx2 v[178:179], v55, %[rs], 0 offen offset:1032 sc1\n " \
  ".Lg1g%=:\n v_exp_f32 v189, v189\n v_mfma_f32_16x16x32_bf16 v[220:223], v[132:135], v[212:215], 0\n " \
  "v_mfma_f32_16x16x32_bf16 v[224:227], v[244:247], v[212:215], 0\n v_mfma_f32_16x16x32_bf16 v[228:231], v[250:253], v[212:215], 0\n " \
  "v_exp_f32 v190, v190\n v_exp_f32 v191, v191\n v_exp_f32 v196, v196\n v_exp_f32 v197, v197\n v_exp_f32 v198, v198\n v_exp_f32 v199, v199\n " \
  "v_rcp_f32 v120, v120\n v_cvt_pk_bf16_f32 v188, v188, v189\n v_pk_mul_f32 v[232:233], v[232:233], v[120:121] op_sel_hi:[1,0]\n " \
  "v_pk_mul_f32 v[234:235], v[234:235], v[120:121] op_sel_hi:[1,0]\n v_pk_mul_f32 v[240:241], v[240:241], v[120:121] op_sel_hi:[1,0]\n " \
  "v_pk_mul_f32 v[242:243], v[242:243], v[120:121] op_sel_hi:[1,0]\n v_cvt_pk_bf16_f32 v189, v190, v191\n v_cvt_pk_bf16_f32 v190, v196, v197\n " \
  "v_cvt_pk_bf16_f32 v191, v198, v199\n v_rcp_f32 v220, v220\n v_cvt_pk_bf16_f32 v232, v232, v233\n v_cvt_pk_bf16_f32 v233, v234, v235\n " \
  "v_mfma_f32_16x16x32_bf16 v[196:199], v[132:135], v[188:191], 0\n v_mfma_f32_16x16x32_bf16 v[200:203], v[174:177], v[188:191], 0\n " \
  "v_mfma_f32_16x16x32_bf16 v[204:207], v[180:183], v[188:191], 0\n v_cvt_pk_bf16_f32 v234, v240, v241\n v_cvt_pk_bf16_f32 v235, v242, v243\n " \
  "buffer_store_dwordx4 v[232:235], v51, %[rs], 0 offen offset:3072 sc1\n ds_write_b128 v50, v[232:235] offset:3072\n " \
  "v_pk_mul_f32 v[224:225], v[224:225], v[220:221] op_sel_hi:[1,0]\n v_pk_mul_f32 v[226:227], v[226:227], v[220:221] op_sel_hi:[1,0]\n " \
  "v_pk_mul_f32 v[228:229], v[228:229], v[220:221] op_sel_hi:[1,0]\n v_pk_mul_f32 v[230:231], v[230:231], v[220:221] op_sel_hi:[1,0]\n " \
  "v_cvt_pk_bf16_f32 v224, v224, v225\n v_cvt_pk_bf16_f32 v225, v226, v227\n v_cvt_pk_bf16_f32 v226, v228, v229\n v_cvt_pk_bf16_f32 v227, v230, v231\n " \
  "v_rcp_f32 v196, v196\n buffer_store_dwordx4 v[224:227], v51, %[rs], 0 offen offset:2048 sc1\n ds_write_b128 v50, v[224:227] offset:2048\n " \
  "v_pk_mul_f32 v[200:201], v[200:201], v[196:197] op_sel_hi:[1,0]\n v_pk_mul_f32 v[202:203], v[202:203], v[196:197] op_sel_hi:[1,0]\n " \
  "v_pk_mul_f32 v[204:205], v[204:205], v[196:197] op_sel_hi:[1,0]\n v_pk_mul_f32 v[206:207], v[206:207], v[196:197] op_sel_hi:[1,0]\n " \
  "v_cvt_pk_bf16_f32 v200, v200, v201\n v_cvt_pk_bf16_f32 v201, v202, v203\n v_cvt_pk_bf16_f32 v202, v204, v205\n v_cvt_pk_bf16_f32 v203, v206, v207\n " \
  "buffer_store_dwordx4 v[200:203], v51, %[rs], 0 offen offset:1024 sc1\n ds_write_b128 v50, v[200:203] offset:1024\n s_mov_b32 vcc_hi, 0x40000\n " \
  "s_cmp_eq_u32 %[wm], 0\n s_cbranch_scc1 .Lg1h%=\n s_waitcnt lgkmcnt(3)\n .Lg1i%=:\n v_readfirstlane_b32 vcc_lo, v53\n s_nop 1\n " \
  "s_cmp_lg_u32 vcc_lo, 0\n s_cbranch_scc1 .Lg1k%=\n ds_read_b32 v53, v59\n ds_read_b128 v[80:83], v58\n ds_read_b64 v[172:173], v58 offset:1024\n " \
  "ds_read_b64 v[178:179], v58 offset:1032\n s_waitcnt lgkmcnt(0)\n s_sub_u32 vcc_hi, vcc_hi, 1\n s_cbranch_scc0 .Lg1i%=\n s_branch .Lg1k%=\n .Lg1h%=:\n " \
  "s_mov_b32 m0, 0x40000\n s_waitcnt vmcnt(3)\n .Lg1j%=:\n v_max3_u32 v136, v80, v81, v82\n v_max3_u32 v137, v83, v172, v173\n " \
  "v_max3_u32 v136, v136, v178, v179\n v_max_u32 v136, v136, v137\n v_cmp_eq_u32 vcc, -1, v136\n s_cbranch_vccz .Lg1k%=\n " \
  "buffer_load_dwordx4 v[80:83], v55, %[rs], 0 offen sc1\n buffer_load_dwordx2 v[172:173], v55, %[rs], 0 offen offset:1024 sc1\n " \
  "buffer_load_dwordx2 v[178:179], v55, %[rs], 0 offen offset:1032 sc1\n s_waitcnt vmcnt(0)\n s_sub_u32 m0, m0, 1\n s_cbranch_scc0 .Lg1j%=\n .Lg1k%=:\n " \
  "v_mfma_f32_16x16x32_bf16 v[164:167], v[80:83], v[160:163], v[44:47]\n s_nop 7\n v_max3_f32 v136, v164, v165, v166\n " \
  "v_max3_f32 v137, v167, v124, v125\n v_max3_f32 v136, v136, v126, v127\n v_max_f32 v136, v136, v137\n v_mov_b32 v137, v136\n s_nop 1\n " \
  "v_permlane16_swap_b32 v136, v137\n v_max_f32 v136, v136, v137\n v_mov_b32 v137, v136\n s_nop 1\n v_permlane32_swap_b32 v136, v137\n " \
  "v_max_f32 v136, v136, v137\n v_mul_f32_e64 v159, v158, -v136\n " \
  "v_pk_fma_f32 v[164:165], v[164:165], v[158:159], v[158:159] op_sel:[0,0,1] op_sel_hi:[1,0,1]\n " \
  "v_pk_fma_f32 v[166:167], v[166:167], v[158:159], v[158:159] op_sel:[0,0,1] op_sel_hi:[1,0,1]\n " \
  "v_pk_fma_f32 v[124:125], v[124:125], v[158:159], v[158:159] op_sel:[0,0,1] op_sel_hi:[1,0,1]\n " \
  "v_pk_fma_f32 v[126:127], v[126:127], v[158:159], v[158:159] op_sel:[0,0,1] op_sel_hi:[1,0,1]\n v_exp_f32 v164, v164\n v_exp_f32 v165, v165\n " \
  "v_exp_f32 v166, v166\n v_exp_f32 v167, v167\n v_exp_f32 v124, v124\n v_exp_f32 v125, v125\n v_exp_f32 v126, v126\n v_exp_f32 v127, v127\n " \
  "v_cvt_pk_bf16_f32 v164, v164, v165\n v_cvt_pk_bf16_f32 v165, v166, v167\n v_cvt_pk_bf16_f32 v166, v124, v125\n v_cvt_pk_bf16_f32 v167, v126, v127\n " \
  "s_nop 1\n v_mfma_f32_16x16x32_bf16 v[124:127], v[132:135], v[164:167], 0\n v_mfma_f32_16x16x32_bf16 v[168:171], v[172:175], v[164:167], 0\n " \
  "v_mfma_f32_16x16x32_bf16 v[84:87], v[178:181], v[164:167], 0\n s_nop 5\n v_rcp_f32 v124, v124\n s_nop 0\n " \
  "v_pk_mul_f32 v[168:169], v[168:169], v[124:125] op_sel_hi:[1,0]\n v_pk_mul_f32 v[170:171], v[170:171], v[124:125] op_sel_hi:[1,0]\n " \
  "v_pk_mul_f32 v[84:85], v[84:85], v[124:125] op_sel_hi:[1,0]\n v_pk_mul_f32 v[86:87], v[86:87], v[124:125] op_sel_hi:[1,0]\n " \
  "v_cvt_pk_bf16_f32 v168, v168, v169\n v_cvt_pk_bf16_f32 v169, v170, v171\n v_cvt_pk_bf16_f32 v170, v84, v85\n v_cvt_pk_bf16_f32 v171, v86, v87\n " \
  "buffer_store_dwordx4 v[168:171], v51, %[rs], 0 offen offset:0 sc1\n ds_write_b128 v50, v[168:171] offset:0\n "
#define G1_HEDGE \
  "G1Q 1, 1, 0, 0, 0\n ds_read2_b64 v[140:143], v49 offset1:16\n ds_read_b128 v[96:99], v48 offset:256\n G1Q 1, 1, 0, 1, 0\n " \
  "ds_read_b128 v[100:103], v48 offset:320\n ds_read2_b64 v[136:139], v49 offset0:32 offset1:48\n G1Q 1, 1, 0, 2, 0\n ds_read_b64_tr_b16 v[36:37], v53\n " \
  "ds_read_b64_tr_b16 v[38:39], v53 offset:1024\n G1Q 1, 1, 0, 3, 0\n ds_read_b64_tr_b16 v[40:41], v53 offset:32\n " \
  "ds_read_b64_tr_b16 v[42:43], v53 offset:1056\n G1Q 1, 0, 1, 0, 0\n G1Q 1, 0, 1, 1, 0\n s_waitcnt lgkmcnt(4)\n v_fmac_f32 v168, v96, v140\n " \
  "G1Q 1, 0, 1, 2, 0\n v_fma_f32 v169, v97, v140, v169\n v_fma_f32 v170, v98, v140, v170\n G1Q 1, 0, 1, 3, 0\n v_fma_f32 v171, v99, v140, v171\n " \
  "v_fma_f32 v172, v100, v140, v172\n G1Q 1, 3, 2, 0, 0\n v_fma_f32 v173, v101, v140, v173\n v_fma_f32 v174, v102, v140, v174\n G1Q 1, 3, 2, 1, 0\n " \
  "v_fma_f32 v175, v103, v140, v175\n v_cvt_pk_bf16_f32 v168, v168, v169\n G1Q 1, 3, 2, 2, 0\n v_cvt_pk_bf16_f32 v169, v170, v171\n " \
  "v_cvt_pk_bf16_f32 v170, v172, v173\n G1Q 1, 3, 2, 3, 0\n v_cvt_pk_bf16_f32 v171, v174, v175\n v_fma_f32 v192, v96, v142, v192\n G1Q 1, 2, 3, 0, 0\n " \
  "v_fma_f32 v193, v97, v142, v193\n v_fma_f32 v194, v98, v142, v194\n G1Q 1, 2, 3, 1, 0\n v_fma_f32 v195, v99, v142, v195\n " \
  "v_fma_f32 v196, v100, v142, v196\n G1Q 1, 2, 3, 2, 0\n v_fma_f32 v197, v101, v142, v197\n v_fma_f32 v198, v102, v142, v198\n G1Q 1, 2, 3, 3, 0\n " \
  "v_fma_f32 v199, v103, v142, v199\n v_cvt_pk_bf16_f32 v192, v192, v193\n G1Q 2, 2, 0, 0, 0\n ds_read2_b32 v[144:145], v52 offset1:16\n " \
  "v_cvt_pk_bf16_f32 v193, v194, v195\n G1Q 2, 2, 0, 1, 0\n ds_read_b128 v[148:151], v48\n v_cvt_pk_bf16_f32 v194, v196, v197\n G1Q 2, 2, 0, 2, 0\n " \
  "v_cvt_pk_bf16_f32 v195, v198, v199\n v_fma_f32 v216, v96, v136, v216\n G1Q 2, 2, 0, 3, 0\n v_fma_f32 v217, v97, v136, v217\n " \
  "v_fma_f32 v218, v98, v136, v218\n G1Q 2, 3, 1, 0, 0\n v_fma_f32 v219, v99, v136, v219\n v_fma_f32 v220, v100, v136, v220\n G1Q 2, 3, 1, 1, 0\n " \
  "ds_read_b128 v[88:91], v48 offset:64\n v_fma_f32 v221, v101, v136, v221\n v_fma_f32 v222, v102, v136, v222\n G1Q 2, 3, 1, 2, 0\n " \
  "ds_read_b128 v[128:131], v54\n v_fma_f32 v223, v103, v136, v223\n v_cvt_pk_bf16_f32 v216, v216, v217\n G1Q 2, 3, 1, 3, 0\n " \
  "v_cvt_pk_bf16_f32 v217, v218, v219\n v_cvt_pk_bf16_f32 v218, v220, v221\n G1Q 2, 0, 2, 0, 0\n v_cvt_pk_bf16_f32 v219, v222, v223\n " \
  "v_fma_f32 v240, v96, v138, v240\n G1Q 2, 0, 2, 1, 0\n ds_read_b128 v[92:95], v54 offset:512\n v_fma_f32 v241, v97, v138, v241\n " \
  "v_fma_f32 v242, v98, v138, v242\n G1Q 2, 0, 2, 2, 0\n ds_read_b128 v[132:135], v54 offset:64\n v_fma_f32 v243, v99, v138, v243\n " \
  "v_fma_f32 v244, v100, v138, v244\n G1Q 2, 0, 2, 3, 0\n v_fma_f32 v245, v101, v138, v245\n v_fma_f32 v246, v102, v138, v246\n G1Q 2, 1, 3, 0, 0\n " \
  "v_fma_f32 v247, v103, v138, v247\n v_cvt_pk_bf16_f32 v240, v240, v241\n G1Q 2, 1, 3, 1, 0\n v_cvt_pk_bf16_f32 v241, v242, v243\n " \
  "v_cvt_pk_bf16_f32 v242, v244, v245\n G1Q 2, 1, 3, 2, 0\n v_cvt_pk_bf16_f32 v243, v246, v247\n G1Q 2, 1, 3, 3, 0\n s_waitcnt lgkmcnt(6)\n " \
  "v_mfma_f32_16x16x32_bf16 v[172:175], v[36:39], v[168:171], 0\n ds_read_b128 v[112:115], v54 offset:576\n ds_read_b128 v[116:119], v54 offset:128\n " \
  "v_mfma_f32_16x16x32_bf16 v[80:83], v[40:43], v[168:171], 0\n ds_read_b128 v[152:155], v54 offset:640\n ds_read_b128 v[156:159], v54 offset:192\n " \
  "v_mfma_f32_16x16x32_bf16 v[196:199], v[36:39], v[192:195], 0\n ds_read_b128 v[120:123], v54 offset:704\n v_mul_f32 v44, 0x3fb8aa3b, v141\n " \
  "v_mfma_f32_16x16x32_bf16 v[84:87], v[40:43], v[192:195], 0\n v_mul_f32 v46, 0x3fb8aa3b, v143\n v_mul_f32 v104, 0x3fb8aa3b, v137\n " \
  "v_mfma_f32_16x16x32_bf16 v[220:223], v[36:39], v[216:219], 0\n v_mul_f32 v106, 0x3fb8aa3b, v139\n " \
  "v_mfma_f32_16x16x32_bf16 v[124:127], v[40:43], v[216:219], 0\n v_mfma_f32_16x16x32_bf16 v[244:247], v[36:39], v[240:243], 0\n " \
  "v_mfma_f32_16x16x32_bf16 v[108:111], v[40:43], v[240:243], 0\n v_max3_f32 v96, v172, v173, v174\n v_max3_f32 v97, v196, v197, v198\n " \
  "v_max3_f32 v96, v96, v175, v80\n v_max3_f32 v97, v97, v199, v84\n v_max3_f32 v96, v96, v81, v82\n v_max3_f32 v97, v97, v85, v86\n " \
  "v_max_f32_e64 v96, v96, v83\n v_max_f32_e64 v97, v97, v87\n s_waitcnt lgkmcnt(0)\n " \
  "v_pk_fma_f32 v[176:177], v[128:129], v[144:145], v[176:177] op_sel_hi:[1,0,1]\n " \
  "v_pk_fma_f32 v[178:179], v[130:131], v[144:145], v[178:179] op_sel_hi:[1,0,1]\n " \
  "v_pk_fma_f32 v[180:181], v[128:129], v[144:145], v[180:181] op_sel:[0,1,0]\n " \
  "v_pk_fma_f32 v[182:183], v[130:131], v[144:145], v[182:183] op_sel:[0,1,0]\n v_pk_mul_f32 v[176:177], v[176:177], v[92:93]\n " \
  "v_pk_mul_f32 v[178:179], v[178:179], v[94:95]\n v_pk_mul_f32 v[180:181], v[180:181], v[92:93]\n v_pk_mul_f32 v[182:183], v[182:183], v[94:95]\n " \
  "v_max3_f32 v98, v220, v221, v222\n v_max3_f32 v99, v244, v245, v246\n v_max3_f32 v98, v98, v223, v124\n v_max3_f32 v99, v99, v247, v108\n " \
  "v_max3_f32 v98, v98, v125, v126\n v_max3_f32 v99, v99, v109, v110\n v_max_f32_e64 v98, v98, v127\n v_max_f32_e64 v99, v99, v111\n " \
  "v_pk_fma_f32 v[200:201], v[132:133], v[144:145], v[200:201] op_sel_hi:[1,0,1]\n " \
  "v_pk_fma_f32 v[202:203], v[134:135], v[144:145], v[202:203] op_sel_hi:[1,0,1]\n v_permlane16_swap_b32 v96, v97\n v_permlane16_swap_b32 v98, v99\n " \
  "v_pk_fma_f32 v[204:205], v[132:133], v[144:145], v[204:205] op_sel:[0,1,0]\n " \
  "v_pk_fma_f32 v[206:207], v[134:135], v[144:145], v[206:207] op_sel:[0,1,0]\n v_max_f32 v96, v96, v97\n v_max_f32 v98, v98, v99\n " \
  "v_pk_mul_f32 v[200:201], v[200:201], v[112:113]\n v_pk_mul_f32 v[202:203], v[202:203], v[114:115]\n v_permlane32_swap_b32 v96, v98\n " \
  "v_pk_mul_f32 v[204:205], v[204:205], v[112:113]\n v_pk_mul_f32 v[206:207], v[206:207], v[114:115]\n v_max_f32 v96, v96, v98\n " \
  "v_pk_fma_f32 v[224:225], v[116:117], v[144:145], v[224:225] op_sel_hi:[1,0,1]\n " \
  "v_pk_fma_f32 v[226:227], v[118:119], v[144:145], v[226:227] op_sel_hi:[1,0,1]\n v_mov_b32 v98, v96\n " \
  "v_pk_fma_f32 v[228:229], v[116:117], v[144:145], v[228:229] op_sel:[0,1,0]\n " \
  "v_pk_fma_f32 v[230:231], v[118:119], v[144:145], v[230:231] op_sel:[0,1,0]\n v_permlane32_swap_b32 v96, v98\n " \
  "v_pk_mul_f32 v[224:225], v[224:225], v[152:153]\n v_pk_mul_f32 v[226:227], v[226:227], v[154:155]\n v_mov_b32 v97, v96\n v_mov_b32 v99, v98\n " \
  "v_pk_mul_f32 v[228:229], v[228:229], v[152:153]\n v_pk_mul_f32 v[230:231], v[230:231], v[154:155]\n v_permlane16_swap_b32 v96, v97\n " \
  "v_permlane16_swap_b32 v98, v99\n v_pk_fma_f32 v[248:249], v[156:157], v[144:145], v[248:249] op_sel_hi:[1,0,1]\n " \
  "v_pk_fma_f32 v[250:251], v[158:159], v[144:145], v[250:251] op_sel_hi:[1,0,1]\n " \
  "v_pk_fma_f32 v[252:253], v[156:157], v[144:145], v[252:253] op_sel:[0,1,0]\n " \
  "v_pk_fma_f32 v[254:255], v[158:159], v[144:145], v[254:255] op_sel:[0,1,0]\n v_pk_mul_f32 v[248:249], v[248:249], v[120:121]\n " \
  "v_pk_mul_f32 v[250:251], v[250:251], v[122:123]\n v_pk_mul_f32 v[252:253], v[252:253], v[120:121]\n v_pk_mul_f32 v[254:255], v[254:255], v[122:123]\n " \
  "v_cvt_pk_bf16_f32 v176, v176, v177\n v_cvt_pk_bf16_f32 v177, v178, v179\n v_cvt_pk_bf16_f32 v178, v200, v201\n v_cvt_pk_bf16_f32 v179, v202, v203\n " \
  "v_cvt_pk_bf16_f32 v180, v180, v181\n v_cvt_pk_bf16_f32 v181, v182, v183\n v_cvt_pk_bf16_f32 v182, v204, v205\n v_cvt_pk_bf16_f32 v183, v206, v207\n " \
  "v_cvt_pk_bf16_f32 v224, v224, v225\n v_cvt_pk_bf16_f32 v225, v226, v227\n v_cvt_pk_bf16_f32 v226, v248, v249\n v_cvt_pk_bf16_f32 v227, v250, v251\n " \
  "v_cvt_pk_bf16_f32 v228, v228, v229\n v_cvt_pk_bf16_f32 v229, v230, v231\n v_cvt_pk_bf16_f32 v230, v252, v253\n v_cvt_pk_bf16_f32 v231, v254, v255\n " \
  "v_mul_f32_e64 v45, v44, -v96\n v_mul_f32_e64 v47, v46, -v97\n v_mul_f32_e64 v105, v104, -v98\n v_mul_f32_e64 v107, v106, -v99\n " \
  "v_pk_fma_f32 v[172:173], v[172:173], v[44:45], v[44:45] op_sel:[0,0,1] op_sel_hi:[1,0,1]\n " \
  "v_pk_fma_f32 v[174:175], v[174:175], v[44:45], v[44:45] op_sel:[0,0,1] op_sel_hi:[1,0,1]\n " \
  "v_pk_fma_f32 v[80:81], v[80:81], v[44:45], v[44:45] op_sel:[0,0,1] op_sel_hi:[1,0,1]\n " \
  "v_pk_fma_f32 v[82:83], v[82:83], v[44:45], v[44:45] op_sel:[0,0,1] op_sel_hi:[1,0,1]\n ds_write_b128 v55, v[176:179] offset:0\n " \
  "v_exp_f32 v172, v172\n v_exp_f32 v173, v173\n v_exp_f32 v174, v174\n v_exp_f32 v175, v175\n v_exp_f32 v80, v80\n v_exp_f32 v81, v81\n " \
  "v_exp_f32 v82, v82\n v_exp_f32 v83, v83\n v_rcp_f32 v200, v172\n v_rcp_f32 v201, v173\n v_rcp_f32 v202, v174\n v_rcp_f32 v203, v175\n " \
  "v_rcp_f32 v204, v80\n v_rcp_f32 v205, v81\n v_rcp_f32 v206, v82\n v_rcp_f32 v207, v83\n " \
  "v_pk_fma_f32 v[196:197], v[196:197], v[46:47], v[46:47] op_sel:[0,0,1] op_sel_hi:[1,0,1]\n " \
  "v_pk_fma_f32 v[198:199], v[198:199], v[46:47], v[46:47] op_sel:[0,0,1] op_sel_hi:[1,0,1]\n " \
  "v_pk_fma_f32 v[84:85], v[84:85], v[46:47], v[46:47] op_sel:[0,0,1] op_sel_hi:[1,0,1]\n " \
  "v_pk_fma_f32 v[86:87], v[86:87], v[46:47], v[46:47] op_sel:[0,0,1] op_sel_hi:[1,0,1]\n ds_write_b128 v55, v[180:183] offset:1024\n " \
  "v_exp_f32 v196, v196\n v_exp_f32 v197, v197\n v_exp_f32 v198, v198\n v_exp_f32 v199, v199\n v_exp_f32 v84, v84\n v_exp_f32 v85, v85\n " \
  "v_exp_f32 v86, v86\n v_exp_f32 v87, v87\n v_rcp_f32 v248, v196\n v_rcp_f32 v249, v197\n v_rcp_f32 v250, v198\n v_rcp_f32 v251, v199\n " \
  "v_rcp_f32 v252, v84\n v_rcp_f32 v253, v85\n v_rcp_f32 v254, v86\n v_rcp_f32 v255, v87\n " \
  "v_pk_fma_f32 v[220:221], v[220:221], v[104:105], v[104:105] op_sel:[0,0,1] op_sel_hi:[1,0,1]\n " \
  "v_pk_fma_f32 v[222:223], v[222:223], v[104:105], v[104:105] op_sel:[0,0,1] op_sel_hi:[1,0,1]\n " \
  "v_pk_fma_f32 v[124:125], v[124:125], v[104:105], v[104:105] op_sel:[0,0,1] op_sel_hi:[1,0,1]\n " \
  "v_pk_fma_f32 v[126:127], v[126:127], v[104:105], v[104:105] op_sel:[0,0,1] op_sel_hi:[1,0,1]\n ds_write_b128 v55, v[224:227] offset:8192\n " \
  "v_pk_add_f32 v[120:121], v[172:173], v[174:175]\n v_pk_add_f32 v[122:123], v[80:81], v[82:83]\n v_pk_add_f32 v[120:121], v[120:121], v[122:123]\n " \
  "v_add_f32 v96, v120, v121\n v_pk_add_f32 v[120:121], v[200:201], v[202:203]\n v_pk_add_f32 v[122:123], v[204:205], v[206:207]\n " \
  "v_pk_add_f32 v[120:121], v[120:121], v[122:123]\n v_add_f32 v97, v120, v121\n v_pk_add_f32 v[120:121], v[196:197], v[198:199]\n " \
  "v_pk_add_f32 v[122:123], v[84:85], v[86:87]\n v_pk_add_f32 v[120:121], v[120:121], v[122:123]\n v_add_f32 v98, v120, v121\n " \
  "v_pk_add_f32 v[120:121], v[248:249], v[250:251]\n v_pk_add_f32 v[122:123], v[252:253], v[254:255]\n v_pk_add_f32 v[120:121], v[120:121], v[122:123]\n " \
  "v_add_f32 v99, v120, v121\n v_exp_f32 v220, v220\n v_exp_f32 v221, v221\n v_permlane16_swap_b32 v96, v97\n v_permlane16_swap_b32 v98, v99\n " \
  "v_exp_f32 v222, v222\n v_exp_f32 v223, v223\n v_add_f32 v96, v96, v97\n v_add_f32 v98, v98, v99\n v_exp_f32 v124, v124\n v_exp_f32 v125, v125\n " \
  "v_permlane32_swap_b32 v96, v98\n v_exp_f32 v126, v126\n v_exp_f32 v127, v127\n v_add_f32 v96, v96, v98\n v_rcp_f32 v128, v220\n " \
  "v_rcp_f32 v129, v221\n v_rcp_f32 v96, v96\n v_rcp_f32 v130, v222\n v_rcp_f32 v131, v223\n v_mov_b32 v98, v96\n v_rcp_f32 v132, v124\n " \
  "v_rcp_f32 v133, v125\n v_permlane32_swap_b32 v96, v98\n v_rcp_f32 v134, v126\n v_rcp_f32 v135, v127\n v_mov_b32 v97, v96\n v_mov_b32 v99, v98\n " \
  "v_pk_fma_f32 v[244:245], v[244:245], v[106:107], v[106:107] op_sel:[0,0,1] op_sel_hi:[1,0,1]\n " \
  "v_pk_fma_f32 v[246:247], v[246:247], v[106:107], v[106:107] op_sel:[0,0,1] op_sel_hi:[1,0,1]\n v_permlane16_swap_b32 v96, v97\n " \
  "v_permlane16_swap_b32 v98, v99\n v_pk_fma_f32 v[108:109], v[108:109], v[106:107], v[106:107] op_sel:[0,0,1] op_sel_hi:[1,0,1]\n " \
  "v_pk_fma_f32 v[110:111], v[110:111], v[106:107], v[106:107] op_sel:[0,0,1] op_sel_hi:[1,0,1]\n ds_write_b128 v55, v[228:231] offset:9216\n " \
  "v_exp_f32 v244, v244\n v_pk_mul_f32 v[172:173], v[172:173], v[96:97] op_sel_hi:[1,0]\n v_exp_f32 v245, v245\n " \
  "v_pk_mul_f32 v[174:175], v[174:175], v[96:97] op_sel_hi:[1,0]\n v_exp_f32 v246, v246\n v_cvt_pk_bf16_f32 v172, v172, v173\n v_exp_f32 v247, v247\n " \
  "v_cvt_pk_bf16_f32 v173, v174, v175\n v_exp_f32 v108, v108\n ds_write_b64 v58, v[172:173] offset:0\n v_exp_f32 v109, v109\n " \
  "v_pk_mul_f32 v[80:81], v[80:81], v[96:97] op_sel_hi:[1,0]\n v_exp_f32 v110, v110\n v_pk_mul_f32 v[82:83], v[82:83], v[96:97] op_sel_hi:[1,0]\n " \
  "v_exp_f32 v111, v111\n v_cvt_pk_bf16_f32 v80, v80, v81\n v_rcp_f32 v112, v244\n v_cvt_pk_bf16_f32 v81, v82, v83\n v_rcp_f32 v113, v245\n " \
  "ds_write_b64 v58, v[80:81] offset:32\n v_rcp_f32 v114, v246\n v_pk_mul_f32 v[200:201], v[200:201], v[96:97] op_sel:[0,1]\n v_rcp_f32 v115, v247\n " \
  "v_pk_mul_f32 v[202:203], v[202:203], v[96:97] op_sel:[0,1]\n v_rcp_f32 v116, v108\n v_cvt_pk_bf16_f32 v200, v200, v201\n v_rcp_f32 v117, v109\n " \
  "v_cvt_pk_bf16_f32 v201, v202, v203\n v_rcp_f32 v118, v110\n ds_write_b64 v58, v[200:201] offset:64\n v_rcp_f32 v119, v111\n " \
  "v_pk_mul_f32 v[204:205], v[204:205], v[96:97] op_sel:[0,1]\n v_pk_mul_f32 v[206:207], v[206:207], v[96:97] op_sel:[0,1]\n " \
  "v_cvt_pk_bf16_f32 v204, v204, v205\n v_cvt_pk_bf16_f32 v205, v206, v207\n ds_write_b64 v58, v[204:205] offset:96\n " \
  "v_pk_add_f32 v[120:121], v[220:221], v[222:223]\n v_pk_add_f32 v[122:123], v[124:125], v[126:127]\n v_pk_add_f32 v[120:121], v[120:121], v[122:123]\n " \
  "v_add_f32 v100, v120, v121\n v_pk_add_f32 v[120:121], v[128:129], v[130:131]\n v_pk_add_f32 v[122:123], v[132:133], v[134:135]\n " \
  "v_pk_add_f32 v[120:121], v[120:121], v[122:123]\n v_add_f32 v101, v120, v121\n v_pk_add_f32 v[120:121], v[244:245], v[246:247]\n " \
  "v_pk_add_f32 v[122:123], v[108:109], v[110:111]\n v_pk_add_f32 v[120:121], v[120:121], v[122:123]\n v_add_f32 v102, v120, v121\n " \
  "v_pk_add_f32 v[120:121], v[112:113], v[114:115]\n v_pk_add_f32 v[122:123], v[116:117], v[118:119]\n v_pk_add_f32 v[120:121], v[120:121], v[122:123]\n " \
  "v_add_f32 v103, v120, v121\n v_pk_mul_f32 v[196:197], v[196:197], v[98:99] op_sel_hi:[1,0]\n " \
  "v_pk_mul_f32 v[198:199], v[198:199], v[98:99] op_sel_hi:[1,0]\n v_permlane16_swap_b32 v100, v101\n v_permlane16_swap_b32 v102, v103\n " \
  "v_cvt_pk_bf16_f32 v196, v196, v197\n v_cvt_pk_bf16_f32 v197, v198, v199\n v_add_f32 v100, v100, v101\n v_add_f32 v102, v102, v103\n " \
  "ds_write_b64 v58, v[196:197] offset:2304\n v_pk_mul_f32 v[84:85], v[84:85], v[98:99] op_sel_hi:[1,0]\n v_permlane32_swap_b32 v100, v102\n " \
  "v_pk_mul_f32 v[86:87], v[86:87], v[98:99] op_sel_hi:[1,0]\n v_cvt_pk_bf16_f32 v84, v84, v85\n v_add_f32 v100, v100, v102\n " \
  "v_cvt_pk_bf16_f32 v85, v86, v87\n ds_write_b64 v58, v[84:85] offset:2336\n v_rcp_f32 v100, v100\n " \
  "v_pk_mul_f32 v[248:249], v[248:249], v[98:99] op_sel:[0,1]\n v_pk_mul_f32 v[250:251], v[250:251], v[98:99] op_sel:[0,1]\n v_mov_b32 v102, v100\n " \
  "v_cvt_pk_bf16_f32 v248, v248, v249\n v_cvt_pk_bf16_f32 v249, v250, v251\n v_permlane32_swap_b32 v100, v102\n " \
  "ds_write_b64 v58, v[248:249] offset:2368\n v_pk_mul_f32 v[252:253], v[252:253], v[98:99] op_sel:[0,1]\n v_mov_b32 v101, v100\n v_mov_b32 v103, v102\n " \
  "v_pk_mul_f32 v[254:255], v[254:255], v[98:99] op_sel:[0,1]\n v_cvt_pk_bf16_f32 v252, v252, v253\n v_permlane16_swap_b32 v100, v101\n " \
  "v_permlane16_swap_b32 v102, v103\n v_cvt_pk_bf16_f32 v253, v254, v255\n ds_write_b64 v58, v[252:253] offset:2400\n " \
  "v_pk_mul_f32 v[220:221], v[220:221], v[100:101] op_sel_hi:[1,0]\n v_pk_mul_f32 v[222:223], v[222:223], v[100:101] op_sel_hi:[1,0]\n " \
  "v_cvt_pk_bf16_f32 v220, v220, v221\n v_cvt_pk_bf16_f32 v221, v222, v223\n v_min3_f32 v120, v97, v99, v101\n v_add_f32 v121, v97, v99\n " \
  "v_min_f32 v120, v120, v103\n v_add_f32 v122, v101, v103\n v_cmp_nlt_f32 vcc, 0x03800000, v120\n v_cmp_u_f32 %[sx], v121, v122\n " \
  "ds_write_b64 v58, v[220:221] offset:18432\n v_pk_mul_f32 v[124:125], v[124:125], v[100:101] op_sel_hi:[1,0]\n " \
  "v_pk_mul_f32 v[126:127], v[126:127], v[100:101] op_sel_hi:[1,0]\n v_cvt_pk_bf16_f32 v124, v124, v125\n v_cvt_pk_bf16_f32 v125, v126, v127\n " \
  "ds_write_b64 v58, v[124:125] offset:18464\n v_pk_mul_f32 v[128:129], v[128:129], v[100:101] op_sel:[0,1]\n " \
  "v_pk_mul_f32 v[130:131], v[130:131], v[100:101] op_sel:[0,1]\n v_cvt_pk_bf16_f32 v128, v128, v129\n v_cvt_pk_bf16_f32 v129, v130, v131\n " \
  "ds_write_b64 v58, v[128:129] offset:18496\n v_pk_mul_f32 v[132:133], v[132:133], v[100:101] op_sel:[0,1]\n " \
  "v_pk_mul_f32 v[134:135], v[134:135], v[100:101] op_sel:[0,1]\n v_cvt_pk_bf16_f32 v132, v132, v133\n v_cvt_pk_bf16_f32 v133, v134, v135\n " \
  "ds_write_b64 v58, v[132:133] offset:18528\n s_or_b64 vcc, vcc, %[sx]\n v_pk_mul_f32 v[244:245], v[244:245], v[102:103] op_sel_hi:[1,0]\n " \
  "v_pk_mul_f32 v[246:247], v[246:247], v[102:103] op_sel_hi:[1,0]\n v_cvt_pk_bf16_f32 v244, v244, v245\n v_cvt_pk_bf16_f32 v245, v246, v247\n " \
  "ds_write_b64 v58, v[244:245] offset:20736\n v_pk_mul_f32 v[108:109], v[108:109], v[102:103] op_sel_hi:[1,0]\n " \
  "v_pk_mul_f32 v[110:111], v[110:111], v[102:103] op_sel_hi:[1,0]\n v_cvt_pk_bf16_f32 v108, v108, v109\n v_cvt_pk_bf16_f32 v109, v110, v111\n " \
  "ds_write_b64 v58, v[108:109] offset:20768\n v_pk_mul_f32 v[112:113], v[112:113], v[102:103] op_sel:[0,1]\n " \
  "v_pk_mul_f32 v[114:115], v[114:115], v[102:103] op_sel:[0,1]\n v_cvt_pk_bf16_f32 v112, v112, v113\n v_cvt_pk_bf16_f32 v113, v114, v115\n " \
  "ds_write_b64 v58, v[112:113] offset:20800\n v_pk_mul_f32 v[116:117], v[116:117], v[102:103] op_sel:[0,1]\n " \
  "v_pk_mul_f32 v[118:119], v[118:119], v[102:103] op_sel:[0,1]\n v_cvt_pk_bf16_f32 v116, v116, v117\n v_cvt_pk_bf16_f32 v117, v118, v119\n " \
  "ds_write_b64 v58, v[116:117] offset:20832\n s_cbranch_vccz .Lg1t%=\n v_mfma_f32_16x16x32_bf16 v[172:175], v[36:39], v[168:171], 0\n " \
  "v_mfma_f32_16x16x32_bf16 v[80:83], v[40:43], v[168:171], 0\n s_nop 7\n v_max3_f32 v96, -v172, -v173, -v174\n v_max3_f32 v96, v96, -v175, -v80\n " \
  "v_max3_f32 v96, v96, -v81, -v82\n v_max_f32_e64 v96, v96, -v83\n v_mov_b32 v97, v96\n s_nop 1\n v_permlane16_swap_b32 v96, v97\n " \
  "v_max_f32 v96, v96, v97\n v_mov_b32 v97, v96\n s_nop 1\n v_permlane32_swap_b32 v96, v97\n v_max_f32 v96, v96, v97\n v_mul_f32_e64 v45, v44, -v96\n " \
  "v_pk_fma_f32 v[172:173], v[172:173], v[44:45], v[44:45] op_sel:[0,0,1] op_sel_hi:[1,0,1] neg_lo:[1,0,0] neg_hi:[1,0,0]\n " \
  "v_pk_fma_f32 v[174:175], v[174:175], v[44:45], v[44:45] op_sel:[0,0,1] op_sel_hi:[1,0,1] neg_lo:[1,0,0] neg_hi:[1,0,0]\n " \
  "v_pk_fma_f32 v[80:81], v[80:81], v[44:45], v[44:45] op_sel:[0,0,1] op_sel_hi:[1,0,1] neg_lo:[1,0,0] neg_hi:[1,0,0]\n " \
  "v_pk_fma_f32 v[82:83], v[82:83], v[44:45], v[44:45] op_sel:[0,0,1] op_sel_hi:[1,0,1] neg_lo:[1,0,0] neg_hi:[1,0,0]\n v_exp_f32 v172, v172\n " \
  "v_exp_f32 v173, v173\n v_exp_f32 v174, v174\n v_exp_f32 v175, v175\n v_exp_f32 v80, v80\n v_exp_f32 v81, v81\n v_exp_f32 v82, v82\n " \
  "v_exp_f32 v83, v83\n s_nop 0\n v_pk_add_f32 v[120:121], v[172:173], v[174:175]\n v_pk_add_f32 v[122:123], v[80:81], v[82:83]\n " \
  "v_pk_add_f32 v[120:121], v[120:121], v[122:123]\n v_add_f32 v96, v120, v121\n v_mov_b32 v97, v96\n s_nop 1\n v_permlane16_swap_b32 v96, v97\n " \
  "v_add_f32 v96, v96, v97\n v_mov_b32 v97, v96\n s_nop 1\n v_permlane32_swap_b32 v96, v97\n v_add_f32 v96, v96, v97\n v_rcp_f32 v96, v96\n s_nop 0\n " \
  "v_pk_mul_f32 v[172:173], v[172:173], v[96:97] op_sel_hi:[1,0]\n v_pk_mul_f32 v[174:175], v[174:175], v[96:97] op_sel_hi:[1,0]\n " \
  "v_cvt_pk_bf16_f32 v172, v172, v173\n v_cvt_pk_bf16_f32 v173, v174, v175\n ds_write_b64 v58, v[172:173] offset:64\n " \
  "v_pk_mul_f32 v[80:81], v[80:81], v[96:97] op_sel_hi:[1,0]\n v_pk_mul_f32 v[82:83], v[82:83], v[96:97] op_sel_hi:[1,0]\n " \
  "v_cvt_pk_bf16_f32 v80, v80, v81\n v_cvt_pk_bf16_f32 v81, v82, v83\n ds_write_b64 v58, v[80:81] offset:96\n " \
  "v_mfma_f32_16x16x32_bf16 v[196:199], v[36:39], v[192:195], 0\n v_mfma_f32_16x16x32_bf16 v[84:87], v[40:43], v[192:195], 0\n s_nop 7\n " \
  "v_max3_f32 v96, -v196, -v197, -v198\n v_max3_f32 v96, v96, -v199, -v84\n v_max3_f32 v96, v96, -v85, -v86\n v_max_f32_e64 v96, v96, -v87\n " \
  "v_mov_b32 v97, v96\n s_nop 1\n v_permlane16_swap_b32 v96, v97\n v_max_f32 v96, v96, v97\n v_mov_b32 v97, v96\n s_nop 1\n " \
  "v_permlane32_swap_b32 v96, v97\n v_max_f32 v96, v96, v97\n v_mul_f32_e64 v47, v46, -v96\n " \
  "v_pk_fma_f32 v[196:197], v[196:197], v[46:47], v[46:47] op_sel:[0,0,1] op_sel_hi:[1,0,1] neg_lo:[1,0,0] neg_hi:[1,0,0]\n " \
  "v_pk_fma_f32 v[198:199], v[198:199], v[46:47], v[46:47] op_sel:[0,0,1] op_sel_hi:[1,0,1] neg_lo:[1,0,0] neg_hi:[1,0,0]\n " \
  "v_pk_fma_f32 v[84:85], v[84:85], v[46:47], v[46:47] op_sel:[0,0,1] op_sel_hi:[1,0,1] neg_lo:[1,0,0] neg_hi:[1,0,0]\n " \
  "v_pk_fma_f32 v[86:87], v[86:87], v[46:47], v[46:47] op_sel:[0,0,1] op_sel_hi:[1,0,1] neg_lo:[1,0,0] neg_hi:[1,0,0]\n v_exp_f32 v196, v196\n " \
  "v_exp_f32 v197, v197\n v_exp_f32 v198, v198\n v_exp_f32 v199, v199\n v_exp_f32 v84, v84\n v_exp_f32 v85, v85\n v_exp_f32 v86, v86\n " \
  "v_exp_f32 v87, v87\n s_nop 0\n v_pk_add_f32 v[120:121], v[196:197], v[198:199]\n v_pk_add_f32 v[122:123], v[84:85], v[86:87]\n " \
  "v_pk_add_f32 v[120:121], v[120:121], v[122:123]\n v_add_f32 v96, v120, v121\n v_mov_b32 v97, v96\n s_nop 1\n v_permlane16_swap_b32 v96, v97\n " \
  "v_add_f32 v96, v96, v97\n v_mov_b32 v97, v96\n s_nop 1\n v_permlane32_swap_b32 v96, v97\n v_add_f32 v96, v96, v97\n v_rcp_f32 v96, v96\n s_nop 0\n " \
  "v_pk_mul_f32 v[196:197], v[196:197], v[96:97] op_sel_hi:[1,0]\n v_pk_mul_f32 v[198:199], v[198:199], v[96:97] op_sel_hi:[1,0]\n " \
  "v_cvt_pk_bf16_f32 v196, v196, v197\n v_cvt_pk_bf16_f32 v197, v198, v199\n ds_write_b64 v58, v[196:197] offset:2368\n " \
  "v_pk_mul_f32 v[84:85], v[84:85], v[96:97] op_sel_hi:[1,0]\n v_pk_mul_f32 v[86:87], v[86:87], v[96:97] op_sel_hi:[1,0]\n " \
  "v_cvt_pk_bf16_f32 v84, v84, v85\n v_cvt_pk_bf16_f32 v85, v86, v87\n ds_write_b64 v58, v[84:85] offset:2400\n " \
  "v_mfma_f32_16x16x32_bf16 v[220:223], v[36:39], v[216:219], 0\n v_mfma_f32_16x16x32_bf16 v[124:127], v[40:43], v[216:219], 0\n s_nop 7\n " \
  "v_max3_f32 v96, -v220, -v221, -v222\n v_max3_f32 v96, v96, -v223, -v124\n v_max3_f32 v96, v96, -v125, -v126\n v_max_f32_e64 v96, v96, -v127\n " \
  "v_mov_b32 v97, v96\n s_nop 1\n v_permlane16_swap_b32 v96, v97\n v_max_f32 v96, v96, v97\n v_mov_b32 v97, v96\n s_nop 1\n " \
  "v_permlane32_swap_b32 v96, v97\n v_max_f32 v96, v96, v97\n v_mul_f32_e64 v105, v104, -v96\n " \
  "v_pk_fma_f32 v[220:221], v[220:221], v[104:105], v[104:105] op_sel:[0,0,1] op_sel_hi:[1,0,1] neg_lo:[1,0,0] neg_hi:[1,0,0]\n " \
  "v_pk_fma_f32 v[222:223], v[222:223], v[104:105], v[104:105] op_sel:[0,0,1] op_sel_hi:[1,0,1] neg_lo:[1,0,0] neg_hi:[1,0,0]\n " \
  "v_pk_fma_f32 v[124:125], v[124:125], v[104:105], v[104:105] op_sel:[0,0,1] op_sel_hi:[1,0,1] neg_lo:[1,0,0] neg_hi:[1,0,0]\n " \
  "v_pk_fma_f32 v[126:127], v[126:127], v[104:105], v[104:105] op_sel:[0,0,1] op_sel_hi:[1,0,1] neg_lo:[1,0,0] neg_hi:[1,0,0]\n v_exp_f32 v220, v220\n " \
  "v_exp_f32 v221, v221\n v_exp_f32 v222, v222\n v_exp_f32 v223, v223\n v_exp_f32 v124, v124\n v_exp_f32 v125, v125\n v_exp_f32 v126, v126\n " \
  "v_exp_f32 v127, v127\n s_nop 0\n v_pk_add_f32 v[120:121], v[220:221], v[222:223]\n v_pk_add_f32 v[122:123], v[124:125], v[126:127]\n " \
  "v_pk_add_f32 v[120:121], v[120:121], v[122:123]\n v_add_f32 v96, v120, v121\n v_mov_b32 v97, v96\n s_nop 1\n v_permlane16_swap_b32 v96, v97\n " \
  "v_add_f32 v96, v96, v97\n v_mov_b32 v97, v96\n s_nop 1\n v_permlane32_swap_b32 v96, v97\n v_add_f32 v96, v96, v97\n v_rcp_f32 v96, v96\n s_nop 0\n " \
  "v_pk_mul_f32 v[220:221], v[220:221], v[96:97] op_sel_hi:[1,0]\n v_pk_mul_f32 v[222:223], v[222:223], v[96:97] op_sel_hi:[1,0]\n " \
  "v_cvt_pk_bf16_f32 v220, v220, v221\n v_cvt_pk_bf16_f32 v221, v222, v223\n ds_write_b64 v58, v[220:221] offset:18496\n " \
  "v_pk_mul_f32 v[124:125], v[124:125], v[96:97] op_sel_hi:[1,0]\n v_pk_mul_f32 v[126:127], v[126:127], v[96:97] op_sel_hi:[1,0]\n " \
  "v_cvt_pk_bf16_f32 v124, v124, v125\n v_cvt_pk_bf16_f32 v125, v126, v127\n ds_write_b64 v58, v[124:125] offset:18528\n " \
  "v_mfma_f32_16x16x32_bf16 v[244:247], v[36:39], v[240:243], 0\n v_mfma_f32_16x16x32_bf16 v[108:111], v[40:43], v[240:243], 0\n s_nop 7\n " \
  "v_max3_f32 v96, -v244, -v245, -v246\n v_max3_f32 v96, v96, -v247, -v108\n v_max3_f32 v96, v96, -v109, -v110\n v_max_f32_e64 v96, v96, -v111\n " \
  "v_mov_b32 v97, v96\n s_nop 1\n v_permlane16_swap_b32 v96, v97\n v_max_f32 v96, v96, v97\n v_mov_b32 v97, v96\n s_nop 1\n " \
  "v_permlane32_swap_b32 v96, v97\n v_max_f32 v96, v96, v97\n v_mul_f32_e64 v107, v106, -v96\n " \
  "v_pk_fma_f32 v[244:245], v[244:245], v[106:107], v[106:107] op_sel:[0,0,1] op_sel_hi:[1,0,1] neg_lo:[1,0,0] neg_hi:[1,0,0]\n " \
  "v_pk_fma_f32 v[246:247], v[246:247], v[106:107], v[106:107] op_sel:[0,0,1] op_sel_hi:[1,0,1] neg_lo:[1,0,0] neg_hi:[1,0,0]\n " \
  "v_pk_fma_f32 v[108:109], v[108:109], v[106:107], v[106:107] op_sel:[0,0,1] op_sel_hi:[1,0,1] neg_lo:[1,0,0] neg_hi:[1,0,0]\n " \
  "v_pk_fma_f32 v[110:111], v[110:111], v[106:107], v[106:107] op_sel:[0,0,1] op_sel_hi:[1,0,1] neg_lo:[1,0,0] neg_hi:[1,0,0]\n v_exp_f32 v244, v244\n " \
  "v_exp_f32 v245, v245\n v_exp_f32 v246, v246\n v_exp_f32 v247, v247\n v_exp_f32 v108, v108\n v_exp_f32 v109, v109\n v_exp_f32 v110, v110\n " \
  "v_exp_f32 v111, v111\n s_nop 0\n v_pk_add_f32 v[120:121], v[244:245], v[246:247]\n v_pk_add_f32 v[122:123], v[108:109], v[110:111]\n " \
  "v_pk_add_f32 v[120:121], v[120:121], v[122:123]\n v_add_f32 v96, v120, v121\n v_mov_b32 v97, v96\n s_nop 1\n v_permlane16_swap_b32 v96, v97\n " \
  "v_add_f32 v96, v96, v97\n v_mov_b32 v97, v96\n s_nop 1\n v_permlane32_swap_b32 v96, v97\n v_add_f32 v96, v96, v97\n v_rcp_f32 v96, v96\n s_nop 0\n " \
  "v_pk_mul_f32 v[244:245], v[244:245], v[96:97] op_sel_hi:[1,0]\n v_pk_mul_f32 v[246:247], v[246:247], v[96:97] op_sel_hi:[1,0]\n " \
  "v_cvt_pk_bf16_f32 v244, v244, v245\n v_cvt_pk_bf16_f32 v245, v246, v247\n ds_write_b64 v58, v[244:245] offset:20800\n " \
  "v_pk_mul_f32 v[108:109], v[108:109], v[96:97] op_sel_hi:[1,0]\n v_pk_mul_f32 v[110:111], v[110:111], v[96:97] op_sel_hi:[1,0]\n " \
  "v_cvt_pk_bf16_f32 v108, v108, v109\n v_cvt_pk_bf16_f32 v109, v110, v111\n ds_write_b64 v58, v[108:109] offset:20832\n .Lg1t%=:\n "
#define G1_KV \
  "v_mov_b32 v144, 0x3f803f80\n v_mov_b32 v145, 0x3f803f80\n v_mov_b32 v146, 0x3f803f80\n v_mov_b32 v147, 0x3f803f80\n s_waitcnt lgkmcnt(0)\n " \
  "s_barrier\n ds_read_b64_tr_b16 v[92:93], v59 offset:0\n ds_read_b64_tr_b16 v[94:95], v59 offset:2304\n ds_read_b128 v[108:111], v60 offset:0\n " \
  "ds_read_b128 v[124:127], v60 offset:1024\n ds_read_b64_tr_b16 v[96:97], v59 offset:9216\n ds_read_b64_tr_b16 v[98:99], v59 offset:11520\n " \
  "ds_read_b128 v[112:115], v60 offset:4096\n ds_read_b128 v[128:131], v60 offset:5120\n ds_read_b64_tr_b16 v[100:101], v59 offset:18432\n " \
  "ds_read_b64_tr_b16 v[102:103], v59 offset:20736\n ds_read_b128 v[116:119], v60 offset:8192\n ds_read_b128 v[132:135], v60 offset:9216\n " \
  "ds_read_b64_tr_b16 v[104:105], v59 offset:27648\n ds_read_b64_tr_b16 v[106:107], v59 offset:29952\n ds_read_b128 v[120:123], v60 offset:12288\n " \
  "ds_read_b128 v[80:83], v60 offset:13312\n s_waitcnt lgkmcnt(14)\n v_mfma_f32_16x16x32_bf16 v[84:87], v[92:95], v[144:147], 0\n " \
  "s_waitcnt lgkmcnt(13)\n v_mfma_f32_16x16x32_bf16 v[152:155], v[92:95], v[108:111], 0\n s_waitcnt lgkmcnt(12)\n " \
  "v_mfma_f32_16x16x32_bf16 v[156:159], v[92:95], v[124:127], 0\n s_waitcnt lgkmcnt(10)\n " \
  "v_mfma_f32_16x16x32_bf16 v[84:87], v[96:99], v[144:147], v[84:87]\n s_waitcnt lgkmcnt(9)\n " \
  "v_mfma_f32_16x16x32_bf16 v[152:155], v[96:99], v[112:115], v[152:155]\n s_waitcnt lgkmcnt(8)\n " \
  "v_mfma_f32_16x16x32_bf16 v[156:159], v[96:99], v[128:131], v[156:159]\n s_waitcnt lgkmcnt(6)\n " \
  "v_mfma_f32_16x16x32_bf16 v[84:87], v[100:103], v[144:147], v[84:87]\n s_waitcnt lgkmcnt(5)\n " \
  "v_mfma_f32_16x16x32_bf16 v[152:155], v[100:103], v[116:119], v[152:155]\n s_waitcnt lgkmcnt(4)\n " \
  "v_mfma_f32_16x16x32_bf16 v[156:159], v[100:103], v[132:135], v[156:159]\n s_waitcnt lgkmcnt(2)\n " \
  "v_mfma_f32_16x16x32_bf16 v[84:87], v[104:107], v[144:147], v[84:87]\n s_waitcnt lgkmcnt(1)\n " \
  "v_mfma_f32_16x16x32_bf16 v[152:155], v[104:107], v[120:123], v[152:155]\n s_waitcnt lgkmcnt(0)\n " \
  "v_mfma_f32_16x16x32_bf16 v[156:159], v[104:107], v[80:83], v[156:159]\n s_nop 3\n v_cvt_pk_bf16_f32 v84, v84, v85\n v_cvt_pk_bf16_f32 v85, v86, v87\n " \
  "v_cvt_pk_bf16_f32 v152, v152, v153\n v_cvt_pk_bf16_f32 v153, v154, v155\n v_cvt_pk_bf16_f32 v154, v156, v157\n v_cvt_pk_bf16_f32 v155, v158, v159\n " \
  "v_min_u32 v152, -2, v152\n v_min_u32 v153, -2, v153\n v_min_u32 v154, -2, v154\n v_min_u32 v155, -2, v155\n " \
  "buffer_store_dwordx4 v[152:155], v61, %[rs], 0 offen sc1\n v_min_u32 v84, -2, v84\n v_min_u32 v85, -2, v85\n " \
  "buffer_store_dwordx2 v[84:85], v62, %[rs], 0 offen sc1\n "
#define G1_OUTPUT \
  "s_mov_b32 m0, 0x40000\n s_branch .Lg1p%=\n .Lg1q%=:\n s_sleep 1\n s_sub_u32 m0, m0, 1\n s_cbranch_scc1 .Lg1r%=\n .Lg1p%=:\n " \
  "buffer_load_dwordx4 v[216:219], %[fz], %[rs], 0 offen sc1\n buffer_load_dwordx4 v[220:223], %[fz], %[rs], 0 offen offset:64 sc1\n " \
  "buffer_load_dwordx4 v[200:203], %[fo], %[rs], 0 offen offset:0 sc1\n buffer_load_dwordx4 v[204:207], %[fo], %[rs], 0 offen offset:2048 sc1\n " \
  "buffer_load_dwordx4 v[208:211], %[fo], %[rs], 0 offen offset:1024 sc1\n buffer_load_dwordx4 v[212:215], %[fo], %[rs], 0 offen offset:3072 sc1\n " \
  "s_waitcnt vmcnt(5)\n v_max_u32 v152, v216, v217\n v_max3_u32 v152, v218, v219, v152\n s_waitcnt vmcnt(4)\n v_max3_u32 v152, v220, v221, v152\n " \
  "v_max3_u32 v152, v222, v223, v152\n s_waitcnt vmcnt(3)\n v_max3_u32 v152, v200, v201, v152\n v_max3_u32 v152, v202, v203, v152\n s_waitcnt vmcnt(2)\n " \
  "v_max3_u32 v152, v204, v205, v152\n v_max3_u32 v152, v206, v207, v152\n s_waitcnt vmcnt(1)\n v_max3_u32 v152, v208, v209, v152\n " \
  "v_max3_u32 v152, v210, v211, v152\n s_waitcnt vmcnt(0)\n v_max3_u32 v152, v212, v213, v152\n v_max3_u32 v152, v214, v215, v152\n " \
  "v_cmp_eq_u32 vcc, -1, v152\n s_cbranch_vccnz .Lg1q%=\n .Lg1r%=:\n v_mfma_f32_16x16x32_bf16 v[160:163], v[216:219], v[224:227], 0\n " \
  "v_mfma_f32_16x16x32_bf16 v[160:163], v[220:223], v[228:231], v[160:163]\n v_mfma_f32_16x16x32_bf16 v[152:155], v[200:203], v[224:227], 0\n " \
  "v_mfma_f32_16x16x32_bf16 v[152:155], v[204:207], v[228:231], v[152:155]\n v_mfma_f32_16x16x32_bf16 v[156:159], v[208:211], v[224:227], 0\n " \
  "v_mfma_f32_16x16x32_bf16 v[156:159], v[212:215], v[228:231], v[156:159]\n v_mfma_f32_16x16x32_bf16 v[172:175], v[216:219], v[232:235], 0\n " \
  "v_mfma_f32_16x16x32_bf16 v[172:175], v[220:223], v[236:239], v[172:175]\n v_mfma_f32_16x16x32_bf16 v[164:167], v[200:203], v[232:235], 0\n " \
  "v_mfma_f32_16x16x32_bf16 v[164:167], v[204:207], v[236:239], v[164:167]\n v_mfma_f32_16x16x32_bf16 v[168:171], v[208:211], v[232:235], 0\n " \
  "v_add_f32 v160, 0x358637bd, v160\n v_rcp_f32 v160, v160\n v_mfma_f32_16x16x32_bf16 v[168:171], v[212:215], v[236:239], v[168:171]\n " \
  "v_mul_f32 v152, v160, v152\n v_mfma_f32_16x16x32_bf16 v[184:187], v[216:219], v[240:243], 0\n v_mul_f32 v153, v160, v153\n " \
  "v_cvt_pk_bf16_f32 v152, v152, v153\n v_mul_f32 v154, v160, v154\n v_mfma_f32_16x16x32_bf16 v[184:187], v[220:223], v[244:247], v[184:187]\n " \
  "v_mul_f32 v155, v160, v155\n v_cvt_pk_bf16_f32 v153, v154, v155\n v_mul_f32 v156, v160, v156\n " \
  "v_mfma_f32_16x16x32_bf16 v[176:179], v[200:203], v[240:243], 0\n v_mul_f32 v157, v160, v157\n v_cvt_pk_bf16_f32 v154, v156, v157\n " \
  "v_mul_f32 v158, v160, v158\n v_mfma_f32_16x16x32_bf16 v[176:179], v[204:207], v[244:247], v[176:179]\n v_mul_f32 v159, v160, v159\n " \
  "v_cvt_pk_bf16_f32 v155, v158, v159\n buffer_store_dwordx4 v[152:155], %[go], %[rs], 0 offen offset:0 sc1\n " \
  "v_mfma_f32_16x16x32_bf16 v[180:183], v[208:211], v[240:243], 0\n ds_write_b128 %[lo], v[152:155] offset:0\n v_add_f32 v172, 0x358637bd, v172\n " \
  "v_mfma_f32_16x16x32_bf16 v[180:183], v[212:215], v[244:247], v[180:183]\n v_rcp_f32 v172, v172\n " \
  "v_mfma_f32_16x16x32_bf16 v[196:199], v[216:219], v[248:251], 0\n v_mul_f32 v164, v172, v164\n v_mul_f32 v165, v172, v165\n " \
  "v_cvt_pk_bf16_f32 v164, v164, v165\n v_mfma_f32_16x16x32_bf16 v[196:199], v[220:223], v[252:255], v[196:199]\n v_mul_f32 v166, v172, v166\n " \
  "v_mul_f32 v167, v172, v167\n v_cvt_pk_bf16_f32 v165, v166, v167\n v_mfma_f32_16x16x32_bf16 v[188:191], v[200:203], v[248:251], 0\n " \
  "v_mul_f32 v168, v172, v168\n v_mul_f32 v169, v172, v169\n v_cvt_pk_bf16_f32 v166, v168, v169\n " \
  "v_mfma_f32_16x16x32_bf16 v[188:191], v[204:207], v[252:255], v[188:191]\n v_mul_f32 v170, v172, v170\n v_mul_f32 v171, v172, v171\n " \
  "v_cvt_pk_bf16_f32 v167, v170, v171\n v_mfma_f32_16x16x32_bf16 v[192:195], v[208:211], v[248:251], 0\n " \
  "buffer_store_dwordx4 v[164:167], %[go], %[rs], 0 offen offset:1024 sc1\n ds_write_b128 %[lo], v[164:167] offset:1024\n " \
  "v_mfma_f32_16x16x32_bf16 v[192:195], v[212:215], v[252:255], v[192:195]\n v_add_f32 v184, 0x358637bd, v184\n v_rcp_f32 v184, v184\n s_nop 0\n " \
  "v_pk_mul_f32 v[176:177], v[176:177], v[184:185] op_sel_hi:[1,0]\n v_pk_mul_f32 v[178:179], v[178:179], v[184:185] op_sel_hi:[1,0]\n " \
  "v_pk_mul_f32 v[180:181], v[180:181], v[184:185] op_sel_hi:[1,0]\n v_pk_mul_f32 v[182:183], v[182:183], v[184:185] op_sel_hi:[1,0]\n " \
  "v_cvt_pk_bf16_f32 v176, v176, v177\n v_cvt_pk_bf16_f32 v177, v178, v179\n v_cvt_pk_bf16_f32 v178, v180, v181\n v_cvt_pk_bf16_f32 v179, v182, v183\n " \
  "buffer_store_dwordx4 v[176:179], %[go], %[rs], 0 offen offset:2048 sc1\n ds_write_b128 %[lo], v[176:179] offset:2048\n " \
  "v_add_f32 v196, 0x358637bd, v196\n v_rcp_f32 v196, v196\n s_nop 0\n v_pk_mul_f32 v[188:189], v[188:189], v[196:197] op_sel_hi:[1,0]\n " \
  "v_pk_mul_f32 v[190:191], v[190:191], v[196:197] op_sel_hi:[1,0]\n v_pk_mul_f32 v[192:193], v[192:193], v[196:197] op_sel_hi:[1,0]\n " \
  "v_pk_mul_f32 v[194:195], v[194:195], v[196:197] op_sel_hi:[1,0]\n v_cvt_pk_bf16_f32 v188, v188, v189\n v_cvt_pk_bf16_f32 v189, v190, v191\n " \
  "v_cvt_pk_bf16_f32 v190, v192, v193\n v_cvt_pk_bf16_f32 v191, v194, v195\n buffer_store_dwordx4 v[188:191], %[go], %[rs], 0 offen offset:3072 sc1\n " \
  "ds_write_b128 %[lo], v[188:191] offset:3072\n "
#define G1_PURGE \
  ".purgem G1M\n .purgem G1RA\n .purgem G1RB\n .purgem G1BK\n .purgem G1AK\n .purgem G1RDB\n .purgem G1RDA\n .purgem G1Q\n .purgem G1SG\n "
#define G1_CLOBBER \
  "v80", "v81", "v82", "v83", "v84", "v85", "v86", "v87", "v92", "v93", "v94", "v95", "v96", "v97", "v98", "v99", "v100", "v101", "v102", "v103", \
  "v104", "v105", "v106", "v107", "v108", "v109", "v110", "v111", \
  "v112", "v113", "v114", "v115", "v116", "v117", "v118", "v119", "v120", "v121", "v122", "v123", "v124", "v125", "v126", "v127", "v128", "v129", \
  "v130", "v131", "v132", "v133", "v134", "v135", "v144", "v145", "v146", "v147", "v152", "v153", "v154", "v155", \
  "v156", "v157", "v158", "v159"

template <int MODE>
__device__ __forceinline__ void g1_phase(char* smem, char* ws, u64& E, int w, f32x16& xq, Next& nx, int layer, const u16* __restrict__ x0, const u16* __restrict__ W,
                                         const u16* __restrict__ wo, const u16* __restrict__ wphi,
                                         const u16* __restrict__ wn1, const u16* __restrict__ wn2) {
  constexpr bool HH = (MODE == 1);
  constexpr int ABYTES = 128 * 128;
  constexpr int STAGE = ABYTES + 3 * 8192;
  constexpr int OFF_FX = 16384;
  constexpr int OFF_SP = 3 * STAGE;
  constexpr int OFF_RS = !HH ? 159744 : OFF_SP + 16384;
  constexpr int OFF_CS = OFF_RS + (HH ? 1152 : 1024);
  constexpr int OFF_WP = OFF_CS + 1536;
  constexpr int OFF_JK = LDS_TOTAL - 1024;
  constexpr int OFF_PK = 0;
  constexpr int OFF_VF = OFF_WP + 4096;
  constexpr int OFF_R2 = HH ? OFF_VF + 16384 : OFF_CS + 1024;
  constexpr int OFF_RD = OFF_CS + 768;
  constexpr int PSTR = 144;
  IDS
  W = uni(W); wphi = uni(wphi); wn1 = uni(wn1); wn2 = uni(wn2);
  const int kg = w >> 2, wm = (w >> 1) & 1, wn = w & 1;
  const bool first = !HH && layer == 0;
  f32x4 acc[2][2], qh[2][2], csq[2], st01, st23;
  bf16x8 wf[2];
  u64 kol;
  u32x4 pw[8];
  ST(layer * 24 + 0)
  const int R0 = wm * 64;
  u32x16 pa;
#define pcs pa[0]
#define prs pa[1]
#define opo pa[2]
#define ogo pa[3]
#define hof pa[7]
#define dm pa[8]
#define dm2 pa[9]
#define pcv pa[4]
#define pwf pa[5]
#define prv pa[6]
#define pvf pa[7]
#define ppk pa[10]
#define ppa pa[11]
#define pvr pa[12]
#define gkv pa[13]
#define gkz pa[14]
#define pwl pa[15]
#define pwm pa[4]
#define plb pa[10]
#define prd pa[11]
#define dm3 pa[12]
  f32x4 bias[3];
  const char* wos;
  unsigned tO;
  const unsigned sb = lds_addr(smem);
  auto prep = [&](auto ldr) {
    constexpr bool LDR = decltype(ldr)::value;
    if constexpr (!LDR) {
      pcs = sb + OFF_CS + wn * 128 + g * 16;
      prs = sb + OFF_RS + (R0 + fr) * 8;
      opo = (unsigned)(wn * 8192 + R0 * 64 + lane * 16);
      ogo = opo + WS_O + layer * (SEQ * DM * 2) + (unsigned)((rt * NH + 2 * hp) * 8192);
      opo += sb;
      pcv = sb + OFF_CS + (wn * 32 + 128 + fr) * 4;
      prv = sb + OFF_R2 + (R0 + 4 * g) * 4;
    } else {
      constexpr unsigned kvn = WS_KV + MODE * KVLB;
      const int t = tid & 255;
      dm = kvn + (unsigned)((rt * NH + 2 * hp) * KVPB) + t * 16;
      dm2 = t < 16 ? dm + 8192 : t >= 64 && t < 97 ? kvn + 16u * NH * KVPB + (unsigned)(hp * KVFB + rt * 528 + (t - 64) * 16) : 0x7fff0000u;
      if (!HH) dm3 = WS_HL + (unsigned)((256 + rt * 16 + hp) * 4096 + t * 16);
    }
    if constexpr (!HH) {
      if constexpr (!LDR) {
        prd = sb + OFF_RD + wn * 4;
        plb = sb + OFF_FX + wn * 2048 + lane * 16;
        hof = m0 + wm == 0 ? 0x7fff0000u : WS_HL + (unsigned)((layer * 128 + rt * 16 - 16 + wm * 16 + hp) * 4096 + wn * 2048 + lane * 16);
        const unsigned out = ~((0x5555u << 2 << fr) >> (4 * g));
#pragma unroll
        for (int i = 0; i < 8; ++i) {
          unsigned t;
          asm("v_bfe_i32 %0, %1, %2, 1" : "=v"(t) : "v"(out), "n"((i >> 2) * 16 + (i & 3)));
          bias[i >> 2][i & 3] = __uint_as_float(t & 0xff61b1e6u);
        }
        bias[2] = m0 + R0 == 0 ? f32x4{-3.0e38f, -3.0e38f, -3.0e38f, -3.0e38f} : bias[0];
        asm volatile("" : "+{v[36:39]}"(bias[0]), "+{v[40:43]}"(bias[1]), "+{v[44:47]}"(bias[2]));
      }
    } else {
      const int u = wm * 2 + kg;
      if constexpr (!LDR) {
        pwf = sb + OFF_WP + wn * 2048 + (4 * g + q4) * 64 + p4 * 8;
        pvf = sb + OFF_VF + w * 2048 + lane * 16;
        ppk = sb + OFF_PK + w * (32 * PSTR) + fr * PSTR + g * 8;
      }
      ppa = sb + OFF_PK + wn * (32 * PSTR) + (4 * g + q4) * PSTR + u * 32 + p4 * 8;
      pvr = sb + OFF_VF + wn * 2048 + lane * 16;
      const unsigned doff = WS_KV + (layer >> 1) * KVLB + (unsigned)((rt * NH + 2 * hp + wn) * KVPB);
      gkv = doff + u * 1024 + lane * 16;
      gkz = fr == 0 ? doff + 4096 + u * 32 + g * 8 : 0x7fff0000u;
    }
    asm volatile("" : "+{v[48:63]}"(pa));
  };

  if (kg == 1) {

    const int lw = w & 3;
    const int kB = 16 * lw + (lane >> 3);
    const int fB = (kB >> 1) & 1;
    const int cB0 = ((((lane & 7) >> 1) ^ fB) << 1) | (lane & 1), cB1 = ((((lane & 7) >> 1) ^ (fB | 2)) << 1) | (lane & 1);
    const bool im = img(layer, hp);

    unsigned vW0, vW1, vP, vX, vY;
    u64 ko2;
    auto ld_pw = [&](int i0) {
#pragma unroll
      for (int i = i0; i < i0 + 4; ++i) gld16(pw[i], vP, wn1 + i * 8 * 3072);
    };
    auto wo_tile = [&](int t) { glds2v(wos + knib(ko2, t) * tO, vW0, vW1, lds_addr(smem) + OFF_WO + t * 8192 + lw * 2048); };
    auto run = [&](auto fst) {
      constexpr bool FIRST = decltype(fst)::value;
      constexpr int N = 10;
      const unsigned vA = FIRST ? (unsigned)(((m0 + lw * 32 + (lane >> 3)) * DM + ((lane & 7) ^ (lane >> 3)) * 8) * 2)
                                : (unsigned)(rt * 262144 + lw * 4096 + lane * 16);
      unsigned imv = !FIRST && im ? ~0u : 0u;
      if (!FIRST) asm volatile("" : "+v"(imv));
      const unsigned vB0 = ((unsigned)(lw * 6144 + lane * 16) & imv) | ((unsigned)((kB * 3072 + hp * 64 + cB0 * 8) * 2) & ~imv);
      const unsigned vB1 = ((unsigned)(lw * 6144 + lane * 16) & imv) | (((unsigned)(((kB + 8) * 3072 + hp * 64 + cB1 * 8) * 2) - 1024u) & ~imv);
      const char* xa = FIRST ? (const char*)x0 : ws + WS_X + (layer - 1) * (SEQ * DM * 2);
      constexpr int KS = FIRST ? 128 : 16384;
      const u64 kord = FIRST ? korder0(rt) : nx.kord;
      auto ktile = [&](int ks) { return knib(kord, ks); };
      auto stageA = [&](int ks, int boff) {
        const int kt = ktile(ks);
        const unsigned sA = lds_addr(smem) + boff;
        if (FIRST) glds4v(xa + kt * KS, vA, vA + 15360u, vA + 2 * 15360u, vA + 3 * 15360u, sA + lw * 4096);
        else glds4c(xa + kt * KS, vA, sA + lw * 4096);
      };
      auto stageB = [&](const u16* Wp, int ks, int boff) {
        const int kt = ktile(ks);
        const unsigned sB = lds_addr(smem) + boff + ABYTES;
        const char* b = FIRST ? (const char*)(Wp + (size_t)kt * 64 * 3072) : nx.wq + kt * nx.tw;
        glds4v(b, vB0, vB1, vB0, vB1, sB + lw * 6144);
        glds2v(b + 4096, vB0, vB1, sB + lw * 6144 + 4096);
      };
      bf16x8 sel[3];
      unsigned cso[3], cst;
      if (FIRST) {
        WAITVN(N);
        BAR;
        stageA(2, 2 * STAGE); stageB(W, 2, 2 * STAGE);
        __builtin_amdgcn_sched_barrier(0);
        const int fl = (q4 >> 1) | ((g & 1) << 1), j = 3 * wm + (fr < 3 ? fr : 0);
#pragma unroll
        for (int i = 0; i < 3; ++i) {
          const unsigned o = fr == i ? 0x3F803F80u : 0u;
          sel[i] = __builtin_bit_cast(bf16x8, u32x4{o, o, o, o});
          cso[i] = (unsigned)(2 * STAGE + ABYTES + (g >> 1) * 6144 + (g & 1) * 1024 + q4 * 128 + p4 * 8 + (((wn * 2 + ((wm + i) & 1)) ^ fl) << 5) + ((3 * wm + i) >> 1) * 2048);
          asm volatile("" : "+v"(sel[i]), "+v"(cso[i]));
        }
        cst = fr < 3 ? WS_CPA + (unsigned)(((hp * 16 + rt) * 192 + (j >> 1) * 64 + wn * 32 + (j & 1) * 16 + 4 * g) * 4) : 0x7fff0000u;
        asm volatile("" : "+v"(cst));
      }
      u64 fall, fal2, fal3, fsv = E + (unsigned)(layer + 1);
      unsigned fao, fa2, vS, vC, dS, dC, dR;
      auto setup = [&]() {
        vP = (unsigned)((rt * 64 + (lane >> 3)) * 6144 + lw * 2048 + ((hp + (1 << lw)) & 15) * 128 + (lane & 7) * 16);
        vS = FIRST ? (unsigned)(WS_G0 + (m0 + lw * 32 + (lane & 31)) * 16)
                   : WS_ST + (unsigned)((layer - 1) * (SEQ * 128) + rt * 16384 + g * 1024 + lw * 256 + fr * 16);
        vC = (FIRST ? WS_CPA : WS_CSP) + (unsigned)(hp * 12288 + g * 768 + lw * 192 + (fr < 12 ? fr : 0) * 16);
        if (!FIRST) vX = HH ? (unsigned)(hp * 4096 + lw * 1024 + lane * 16) : (unsigned)((rt * 16 + hp) * 1024 + lane * 16);
        vW0 = ((unsigned)(lw * 2048 + lane * 16) & imv) | ((unsigned)((kB * DM + hp * 64 + ((lane & 7) ^ ((lane >> 4) << 1)) * 8) * 2) & ~imv); vW1 = vW0 + ((8 * DM * 2 - 1024u) & ~imv);
        dS = OFF_RS + lw * 256 + (FIRST ? (lane & 31) * 8 : fr * 16 + (g & 1) * 8); dC = OFF_CS + lw * 192 + (fr < 12 ? fr : 0) * 16 + g * 4;
        ko2 = FIRST ? korder(bid & 3, hp) : nx.ko2;
        wos = FIRST ? (const char*)wo : wo_src(ws, wo, layer, hp); tO = !FIRST && im ? 8192u : 131072u;
        asm volatile("" : "+s"(wos), "+s"(tO));
        dR = OFF_R2 + lw * 128 + (FIRST ? (lane & 31) * 4 : fr * 8 + (g & 1) * 4);
        asm volatile("" : "+v"(dR));
        asm volatile("" : "+v"(vS), "+v"(vX), "+v"(vW0), "+v"(vW1), "+v"(dS));
        asm volatile("" : "+s"(ko2));
        if (FIRST || HH) asm volatile("" : "+v"(vC), "+v"(dC), "+v"(fa2));
        if (FIRST) asm volatile("" : "+v"(vP));
      };
      if (!FIRST) {
        fao = (unsigned)((256 + rt * 16 + (lane & 15)) * FLS);
        if (HH) { vY = (unsigned)(bid * 8192 + lw * 2048 + lane * 16); fa2 = (unsigned)((256 + (lane & 15) * 16 + ((hp - ((1 << (lane >> 4)) >> 1)) & 15)) * FLS); asm volatile("" : "+v"(vY), "+v"(fa2)); }
        asm volatile("" : "+v"(fsv), "+v"(fao));
      }
      u32x4 sv[4], cv[4];
      auto ld_c = [&](u32x4& d, unsigned v, int so) { asm volatile("buffer_load_dwordx4 %0, %1, %2, %3 offen sc1" : "=v"(d) : "v"(v), "s"(rsW), "s"(so) : "memory"); };
      float tm, tr;
      unsigned tc;
      auto fin = [&]() {
        if (FIRST) {
          const unsigned tag = (unsigned)E + 1u;
          asm volatile("" : "+v"(sv[0]));
          _Pragma("unroll 1") for (unsigned spins = 0; spins < (1u << 18); ++spins) {
            if (__all(sv[0][1] == tag && sv[0][3] == tag)) break;
            __builtin_amdgcn_s_sleep(1);
            asm volatile("buffer_load_dwordx4 %0, %1, %2, 0 offen sc1\n\ts_waitcnt vmcnt(0)" : "=v"(sv[0]) : "v"(vS), "s"(rsW) : "memory");
          }
          tm = __uint_as_float(sv[0][0]); tr = __uint_as_float(sv[0][2]);
        } else {
          asm volatile("" : "+v"(sv[0]), "+v"(sv[1]), "+v"(sv[2]), "+v"(sv[3]));
          const f32x4 t = sum4(sv);
          const unsigned u = xs4(t[0], t[2], t[1], t[3]);
          auto h = __builtin_amdgcn_permlane32_swap(u, u, false, false);
          const float mean = __uint_as_float(h[0]) * (1.0f / 1024.0f);
          const float var = fmaxf(__uint_as_float(h[1]) * (1.0f / 1024.0f) - mean * mean, 0.f);
          tm = -mean; tr = __builtin_amdgcn_rsqf(var + 1e-5f);
        }
      };
      auto cfin = [&]() {
        if (FIRST || HH) {
          asm volatile("" : "+v"(cv[0]), "+v"(cv[1]), "+v"(cv[2]), "+v"(cv[3]));
          const f32x4 t = sum4(cv);
          tc = xs4(t[0], t[1], t[2], t[3]);
        }
      };
      bf16x8 cf[6];
      f32x4 csum = {0, 0, 0, 0};
      if (FIRST) {
        asm volatile("s_load_dwordx2 %0, %1, 0x0\n\ts_waitcnt lgkmcnt(0)" : "=s"(E) : "s"(ws) : "memory");
        fall = E + 1u; fa2 = (unsigned)((512 + (lane & 15) * 16 + hp) * FLS); vX = (unsigned)(bid * 8192 + lw * 2048 + lane * 16);
        asm volatile("" : "+v"(fall), "+v"(fa2), "+v"(vX));
      }
#pragma unroll
      for (int kt = 0; kt < 13; ++kt) {
        const int cur = (kt % 3) * STAGE;
        if (!FIRST && kt == 0) { WAITV(4); }
        else if (FIRST && kt == 4 && lw == 1) { WAITVN(N + 2); }
        else if (HH && (kt == 4 || kt == 6)) { WAITVN(N + 1); }
        else if (HH && kt == 5) { WAITVN(N + 2); }
        else if ((!FIRST && kt == 1 && lw == 3) || (!FIRST && kt == 9 && lw == 2) || ((FIRST || HH) && kt == 10 && lw == 3) || (FIRST && (kt == 2 || kt == 4 || kt == 5))) { WAITVN(N + 1); }
        else { WAITVN(N); }
        if (!FIRST && kt == 2 && lw == 3) {
          asm volatile("" : "+v"(fall));
          if (__any(fall != E + (unsigned)layer)) poll_flags(ws, E, 1, rt, hp, lane, layer, false);
        }
        if (!FIRST && kt == 10 && lw == 2) {
          asm volatile("" : "+v"(fal3));
          if (__any(fal3 != E + (unsigned)(layer + 1))) poll_flags(ws, E, 2, rt, hp, lane, layer + 1, false);
        }
        if (HH && kt == 11 && lw == 3) {
          asm volatile("" : "+v"(fal2));
          if (__any(fal2 != E + (unsigned)layer)) {
            _Pragma("unroll 1") for (int j = 0; j < 4; ++j) poll_flags(ws, E, 1, rt, (hp - ((1 << j) >> 1)) & 15, lane, layer, true);
          }
        }
        if (FIRST && kt == 11 && lw == 3) {
          asm volatile("" : "+v"(fal2));
          if (__any(fal2 != E + 1u)) poll_flags(ws, E, 2, rt, hp, lane, 1u, true);
        }
        BAR;
        if (FIRST && kt == 1) {
          asm volatile("" ::: "memory");
#pragma unroll
          for (int i = 0; i < 6; ++i) { const char* t = smem + cso[i >> 1] + (i & 1) * 12288; cf[i] = cat8(tr_read(t), tr_read(t + 512)); }
        }
        stageA(kt + 3, cur);
        if (FIRST && kt == 1) {
          asm volatile("" : "+v"(sel[0]));
#pragma unroll
          for (int i = 0; i < 6; ++i) csum = MFMA(cf[i], sel[i >> 1], csum);
          asm volatile("" : "+v"(csum));
        }
        stageB(W, kt + 3, cur);
        if (FIRST) {
          if (kt == 1) __builtin_amdgcn_raw_buffer_store_b128(__builtin_bit_cast(u32x4, csum), rsW, cst, 0, 16);
          if (kt == 3 && tid == 320) __builtin_amdgcn_raw_buffer_store_b64(__builtin_bit_cast(u32x2, fall), rsW, WS_FLAGS + (unsigned)(512 + rt * 16 + hp) * FLS, 0, 16);
          if (kt == 3 || kt == 4) glds16s(wo + (kt - 3) * 512, vX, lds_addr(smem) + OFF_JK);
        }
        if (HH && (kt == 3 || kt == 4)) glds16s(wo + (kt - 3) * 512, vY, lds_addr(smem) + OFF_JK);
        if (!FIRST && kt == 3 && tid == 320) __builtin_amdgcn_raw_buffer_store_b64(__builtin_bit_cast(u32x2, fsv), rsW, WS_FLAGS + (unsigned)(512 + rt * 16 + hp) * FLS, 0, 16);
        if (!FIRST && kt == 8 && lw == 2) asm volatile("global_load_dwordx2 %0, %1, %2 sc1" : "=v"(fal3) : "v"(fao), "s"(ws + WS_FLAGS + 256 * FLS) : "memory");
        if (!FIRST && kt == 0 && lw == 3) asm volatile("global_load_dwordx2 %0, %1, %2 sc1" : "=v"(fall) : "v"(fao), "s"(ws + WS_FLAGS) : "memory");
        if ((FIRST || HH) && kt == 9 && lw == 3) asm volatile("global_load_dwordx2 %0, %1, %2 sc1" : "=v"(fal2) : "v"(fa2), "s"(ws + WS_FLAGS) : "memory");
      }
      setup();
      WAITVN(N);
      BAR;
#pragma unroll
      for (int i = 0; i < (FIRST ? 1 : 4); ++i) ld_c(sv[i], vS, i * 4096);
      if (FIRST || HH) {
#pragma unroll
        for (int i = 0; i < 4; ++i) ld_c(cv[i], vC, i * 3072);
      }
      if (HH) glds16s(wphi, vX, lds_addr(smem) + OFF_WP + lw * 1024);
      else if (!FIRST) glds16c(ws + WS_CSA, vX, lds_addr(smem) + OFF_CS);
      wo_tile(0); wo_tile(1);
      WAITVN(HH ? 13 : 9);
      BAR;
      if (FIRST) {
        if (lw < 3) { ld_pw(0); ld_pw(4); WAITV(12); } else { WAITV(4); }
      } else {
        if (HH) prep(IC<1>{});
        WAITV(4);
      }
      cfin();
      fin();
      asm volatile("" : "+v"(tm), "+v"(tr));
      if (FIRST || HH) asm volatile("" : "+v"(tc));
      if (FIRST && lw < 3) { WAITV(8); } else { WAITV(0); }
      lds<f32x2>(sb + dS) = f32x2{tm, tr};
      lds<float>(sb + dR) = tm; lds<float>(sb + dR + 512) = tr;
      if (FIRST || HH) lds<unsigned>(sb + dC) = tc;
      if (!HH) lds<float>(sb + OFF_RD + wn * 4) = zero1();
      WAITL0;
    };
    typedef std::integral_constant<bool, true> T_;
    typedef std::integral_constant<bool, false> F_;
    __builtin_amdgcn_s_setprio(3);
    if (first) run(T_{}); else run(F_{});
    __builtin_amdgcn_s_setprio(0);
    BAR;
    if constexpr (HH) {
      u32x4 c0, c1;
      if (layer == 1) {
        const int n = bid * 8 + lw * 2;
        const void* p;
        unsigned a0, a1;
        if (n < 1536) {
          const int r = n % 768, q = r % 384, e = q % 24;
          p = n < 768 ? wn1 : W;
          a0 = (unsigned)((((q / 24) * 64 + (e / 6) * 16 + (lane >> 3)) * 3072 + ((e % 6) >> 1) * 1024 + (r < 384 ? 192 : 704)) * 2);
          a1 = a0 + 8 * 6144 + cB1 * 16; a0 += cB0 * 16;
        } else {
          p = n < 1792 ? wn2 : wo;
          a0 = (unsigned)((((n & 127) * 8 + (lane >> 3)) * DM + ((n & 128) ? 704 : 192) + ((lane & 7) ^ ((lane >> 4) << 1)) * 8) * 2);
          a1 = a0 + 8 * DM * 2;
        }
        p = uni(p);
        gld16(c0, a0, p); gld16(c1, a1, p);
        const u32x4 pz = empty4();
        __builtin_amdgcn_raw_buffer_store_b128(pz, rsW, dm, 0, 16);
        __builtin_amdgcn_raw_buffer_store_b128(pz, rsW, dm, 4096, 16);
        __builtin_amdgcn_raw_buffer_store_b128(pz, rsW, dm2, 0, 16);
      }
#pragma unroll
      for (int t = 2; t < 6; ++t) wo_tile(t);
      if (layer == 1) {
        WAITV(11);
        __builtin_amdgcn_sched_barrier(0);
        asm volatile("" : "+v"(c0), "+v"(c1));
        __builtin_amdgcn_raw_buffer_store_b128(c0, rsW, WS_IMG + (unsigned)(bid * 8192 + lw * 2048 + lane * 16), 0, 16);
        __builtin_amdgcn_raw_buffer_store_b128(c1, rsW, WS_IMG + (unsigned)(bid * 8192 + lw * 2048 + 1024 + lane * 16), 0, 16);
        WAITV(0);
      }
      pwl = vW0; pwm = vW1; kol = ko2;
      asm volatile("" : "=v"(csq[0]), "=v"(csq[1]), "=v"(st01), "=v"(st23), "=v"(wf[0]), "=v"(wf[1]));
      asm volatile("" : "=v"(acc[0][0]), "=v"(acc[0][1]), "=v"(acc[1][0]), "=v"(acc[1][1]));
      asm volatile("" : "=v"(qh[0][0]), "=v"(qh[0][1]), "=v"(qh[1][0]), "=v"(qh[1][1]));
    } else {
      const int lane = lane_id();
#pragma unroll
      for (int t = 2; t < 12; ++t) {
        wo_tile(t);
        if (t == 2 && first) {
        prep(IC<1>{});
        const u32x4 pz = empty4();
        __builtin_amdgcn_raw_buffer_store_b128(pz, rsW, dm, 0, 16);
        __builtin_amdgcn_raw_buffer_store_b128(pz, rsW, dm, 4096, 16);
        __builtin_amdgcn_raw_buffer_store_b128(pz, rsW, dm3, 0, 16);
        __builtin_amdgcn_raw_buffer_store_b128(pz, rsW, dm2, 0, 16);
        if (lw == 3 && lane < 48) __builtin_amdgcn_raw_buffer_store_b128(lds<u32x4>(sb + OFF_CS + lane * 16), rsW, WS_CSA + (unsigned)((rt * 16 + hp) * 1024 + lane * 16), 0, 16);
        }
      }
      if (first) { BAR;   WAITV(0); }
    }
    asm volatile("" : "={v[64:79]}"(xq));
  } else {

    prep(IC<0>{});
    if (first) {
      const int row = m0 + hp * 8 + w * 2 + (lane >> 5);
      asm volatile("s_load_dwordx2 %0, %1, 0x0\n\ts_waitcnt lgkmcnt(0)" : "=s"(E) : "s"(ws) : "memory");
      const unsigned tag = (unsigned)E + 1u;
      static_assert(HH || (OFF_SP == 122880 && OFF_R2 + 1024 <= OFF_JK), "addresses used at the top of the kernel");
      WAITV(0);
      u32x4 xs[4];
#pragma unroll
      for (int i = 0; i < 4; ++i) xs[i] = *(const u32x4*)(smem + OFF_SP + w * 4096 + (lane >> 5) * 2048 + (lane & 31) * 16 + i * 512);
      typedef __attribute__((ext_vector_type(2))) __bf16 bf2;
      const bf2 one2 = __builtin_bit_cast(bf2, 0x3F803F80u);
      float sa[2] = {zero1(), zero1()}, sq[2] = {zero1(), zero1()};
#pragma unroll
      for (int i = 0; i < 16; ++i) {
        const unsigned u = xs[i >> 2][i & 3];
        const bf2 t = __builtin_bit_cast(bf2, u);
        sa[i & 1] = __builtin_amdgcn_fdot2_f32_bf16(t, one2, sa[i & 1], false);
        sq[i & 1] = __builtin_amdgcn_fdot2_f32_bf16(t, t, sq[i & 1], false);
      }
      float sm = r16_sum(sa[0] + sa[1]), m2 = r16_sum(sq[0] + sq[1]);
      {
        auto r = __builtin_amdgcn_permlane16_swap(__float_as_uint(sm), __float_as_uint(sm), false, false);
        auto q = __builtin_amdgcn_permlane16_swap(__float_as_uint(m2), __float_as_uint(m2), false, false);
        sm = __uint_as_float(r[0]) + __uint_as_float(r[1]); m2 = __uint_as_float(q[0]) + __uint_as_float(q[1]);
      }
      const float mean = sm * (1.0f / 1024.0f);
      m2 = fmaxf(m2 - sm * mean, 0.f);
      u32x4 gw = {__float_as_uint(-mean), tag, __float_as_uint(rsqrtf(m2 * (1.0f / 1024.0f) + 1e-5f)), tag};
      if ((lane & 31) == 0) __builtin_amdgcn_raw_buffer_store_b128(gw, rsW, (unsigned)(WS_G0 + row * 16), 0, 16);
      const u64 ko = korder(bid & 3, hp);
      nx.kf = kfirst(ko) | (kfirst(ko >> 16) | (1u << knib(ko, 9))) << 16;
      asm volatile("" : "+s"(nx.kf));
      __builtin_amdgcn_raw_buffer_store_b128(empty4(), rsW, WS_HL + (unsigned)((rt * 16 + hp) * 4096 + tid * 16), 0, 16);
    }
    const int fl = (q4 >> 1) | ((g & 1) << 1);
    const unsigned bo = lds_addr(smem) + ABYTES + (g >> 1) * 6144 + (g & 1) * 1024 + q4 * 128 + p4 * 8;
    const unsigned bo0 = bo + (((wn * 2 + 0) ^ fl) << 5), bo1 = bo + (((wn * 2 + 1) ^ fl) << 5);
    const unsigned ar = lds_addr(smem) + (wm * 64 + fr) * 128;
    const unsigned aK0 = first ? ar + ((g ^ (fr & 7)) << 4) : sb + wm * 8192 + lane * 16, aK1 = first ? ar + (((4 + g) ^ (fr & 7)) << 4) : aK0 + 1024;
    f32x32 ac0, ac1, ac2;
    u64 sx;
    asm volatile("" :: "v"(aK0), "v"(aK1), "v"(bo0), "v"(bo1));
    if (first) BAR;
    else publish(rsW, tid, 256 + rt * 16 + hp, E + (unsigned)layer);
#define G1_OUT
#define G1_T(a)
#define G1_TEND
#define G1_TCL
    asm volatile(G1_MACROS
        "s_mov_b32 vcc_hi, %[lay]\n"
        ".if %[hh]\n s_branch .Lg1m\n .else\n"
        ".p2align 3\n .Lg1m:\n"
        "G1RDB 0, v16, v17, 0\n G1RDA v12, v13, 0\n G1RDB 1, v16, v17, 0\n"
        "ds_read_b128 v[64:67], v20\n ds_read_b128 v[68:71], v20 offset:2048\n"
        "ds_read_b128 v[72:75], v20 offset:4096\n ds_read_b128 v[76:79], v20 offset:6144\n"
        "s_cmp_eq_u32 vcc_hi, 0\n s_waitcnt lgkmcnt(12)\n"
        "G1SG 0, 1, 1, v12, v13, v16, v17, 0\n"
        "s_mov_b32 vcc_lo, 16\n"
        "G1SG 1, 1, 1, v12, v13, v16, v17, 40960\n"
        "G1SG 2, 1, 2, v12, v13, v16, v17, 40960\n"
        "s_cbranch_scc1 .Lg1a%=\n"
        "s_barrier\n"
        G1_T(88)
        "s_barrier\n"
        G1_T(92)
        "G1RDA v12, v13, 40960\n"
        "s_waitcnt lgkmcnt(4)\n"
        "s_branch .Lg1b%=\n"
        ".Lg1a%=:\n"
        G1_T(88)
        "s_barrier\n"
        G1_T(92)
        "G1RDB 0, v16, v17, 40960\n G1RDA v12, v13, 40960\n G1RDB 1, v16, v17, 40960\n"
        ".p2align 3\n s_nop 0\n"
        ".Lg1b%=:\n"
        "s_waitcnt lgkmcnt(8)\n"
        "G1SG 0, 0, 1, v12, v13, v16, v17, 40960\n"
        "s_barrier\n"
        "G1SG 1, 0, 1, v14, v15, v18, v19, 0\n"
        "G1SG 2, 0, 1, v14, v15, v18, v19, 0\n"
        "s_lshr_b32 vcc_lo, vcc_lo, 1\n s_waitcnt vmcnt(0) lgkmcnt(8)\n"
        "G1SG 0, 0, 1, v14, v15, v18, v19, 0\n"
        "s_barrier\n"
        "G1SG 1, 0, 1, v12, v13, v16, v17, 0\n"
        "G1SG 2, 0, 1, v12, v13, v16, v17, 0\n"
        "s_cbranch_scc0 .Lg1c%=\n s_waitcnt lgkmcnt(8)\n"
        "G1SG 0, 0, 1, v12, v13, v16, v17, 0\n"
        "s_barrier\n"
        "G1SG 1, 0, 1, v12, v13, v16, v17, 40960\n"
        "G1SG 2, 0, 1, v12, v13, v16, v17, 40960\n"
        "s_branch .Lg1b%=\n"
        ".Lg1c%=:\n"
        "s_waitcnt lgkmcnt(8)\n"
        "G1SG 0, 0, 1, v12, v13, v16, v17, 0\n s_bitcmp1_b32 vcc_hi, 0\n"
        G1_T(96)
        "s_cbranch_scc1 .Lg1n\n s_barrier\n"
        G1_SLIDE
        ".endif\n .if %[hh]\n .p2align 3\n s_nop 0\n .Lg1n:\n s_barrier\n"
        G1_HEDGE
        ".endif\n"
        G1_TEND G1_PURGE
        : "=&{v[160:191]}"(ac0), "=&{v[192:223]}"(ac1), "=&{v[224:255]}"(ac2), "=&{v[64:79]}"(xq),
          "=&{v[88:91]}"(csq[1]), "=&{v[148:151]}"(csq[0]), "=&{v[140:143]}"(st01), "=&{v[136:139]}"(st23),
          "+{v[48:63]}"(pa), "+{v[36:39]}"(bias[0]), "+{v[40:43]}"(bias[1]), "+{v[44:47]}"(bias[2]), [sx] "=&s"(sx) G1_OUT
        : "{v12}"(aK0), "{v13}"(aK1), "{v14}"(aK0 + 2 * STAGE), "{v15}"(aK1 + 2 * STAGE), "{v16}"(bo0), "{v17}"(bo1),
          "{v18}"(bo0 + 2 * STAGE), "{v19}"(bo1 + 2 * STAGE), "{v20}"(wn ? aK1 : aK0),
          [lay] "s"(layer), [hh] "n"(MODE), [rs] "s"(rsW), [wm] "s"(wm)
        : "memory", "scc", "vcc", "m0", G1_TCL G1_CLOBBER);
    acc[0][0] = quad<0>(ac0); acc[0][1] = quad<1>(ac0); acc[1][0] = quad<6>(ac0); acc[1][1] = quad<7>(ac0);
    asm volatile("" : "=s"(kol), "=s"(wos), "=s"(tO));
    wf[0] = __builtin_bit_cast(bf16x8, bias[0]); wf[1] = __builtin_bit_cast(bf16x8, bias[1]);
    qh[0][0] = quad<4>(ac1); qh[0][1] = quad<5>(ac1); qh[1][0] = quad<2>(ac2); qh[1][1] = quad<3>(ac2);
  }
  ST(layer * 24 + 4)
  {
  const int lane = lane_id(), tid = w * 64 + lane, fr = lane & 15, g = lane >> 4;
  (void)fr; (void)g;
  if constexpr (HH) {
    const f32x4 z4 = {0, 0, 0, 0};
    constexpr float L2E = 1.4426950408889634f;
    const unsigned kvo = WS_KV + (layer >> 1) * KVLB, kfo = kvo + 16u * NH * KVPB + hp * KVFB;
    auto phi2 = [&](const f32x4 (&a)[2][2], f32x4 st, bf16x8 (&qp)[2][2]) {
      f32x4 pm[2][2];
      f32x2 f[2][4];
      asm volatile("" : "+v"(st));
#pragma unroll
      for (int i = 0; i < 8; ++i) {
        const int rb = i >> 2, c = (i >> 1) & 1, e = (i & 1) * 2;
        f[rb][i & 3] = __builtin_elementwise_fma(f32x2{csq[c][e], csq[c][e + 1]}, f32x2{st[2 * rb], st[2 * rb]}, f32x2{a[rb][c][e], a[rb][c][e + 1]});
      }
#pragma unroll
      for (int rb = 0; rb < 2; ++rb) asm volatile("" : "+v"(f[rb][0]), "+v"(f[rb][1]), "+v"(f[rb][2]), "+v"(f[rb][3]));
#pragma unroll
      for (int rb = 0; rb < 2; ++rb) {
        const bf16x8 xf = __builtin_bit_cast(bf16x8, u32x4{pk2(f[rb][0][0], f[rb][0][1]), pk2(f[rb][1][0], f[rb][1][1]), pk2(f[rb][2][0], f[rb][2][1]), pk2(f[rb][3][0], f[rb][3][1])});
        pm[rb][0] = MFMA(wf[0], xf, z4); pm[rb][1] = MFMA(wf[1], xf, z4);
      }
      asm volatile("s_nop 7" : "+v"(pm[0][0]), "+v"(pm[0][1]), "+v"(pm[1][0]), "+v"(pm[1][1]));
      float mx[2], mn[2];
#pragma unroll
      for (int u = 0; u < 2; ++u) max8pn(pm[u][0], pm[u][1], mx[u], mn[u]);
      xr4<true>(mx[0], mn[0], mx[1], mn[1]);
      float ep[2][8], en[2][8], sp[2], sn[2];
#pragma unroll
      for (int u = 0; u < 2; ++u) {
        const float c = st[2 * u + 1] * L2E, mxs = -mx[u] * c, mns = -mn[u] * c;
#pragma unroll
        for (int i = 0; i < 4; ++i) {
          const f32x2 p2 = {pm[u][i >> 1][(i & 1) * 2], pm[u][i >> 1][(i & 1) * 2 + 1]};
          const f32x2 tp = __builtin_elementwise_fma(p2, f32x2{c, c}, f32x2{mxs, mxs});
          const f32x2 tn = __builtin_elementwise_fma(-p2, f32x2{c, c}, f32x2{mns, mns});
          ep[u][2 * i] = __builtin_amdgcn_exp2f(tp[0]); ep[u][2 * i + 1] = __builtin_amdgcn_exp2f(tp[1]);
          en[u][2 * i] = __builtin_amdgcn_exp2f(tn[0]); en[u][2 * i + 1] = __builtin_amdgcn_exp2f(tn[1]);
        }
        const f32x2 a2 = (f32x2{ep[u][0], ep[u][1]} + f32x2{ep[u][2], ep[u][3]}) + (f32x2{ep[u][4], ep[u][5]} + f32x2{ep[u][6], ep[u][7]});
        const f32x2 b2 = (f32x2{en[u][0], en[u][1]} + f32x2{en[u][2], en[u][3]}) + (f32x2{en[u][4], en[u][5]} + f32x2{en[u][6], en[u][7]});
        sp[u] = a2[0] + a2[1]; sn[u] = b2[0] + b2[1];
      }
      xr4<false>(sp[0], sn[0], sp[1], sn[1]);
#pragma unroll
      for (int rb = 0; rb < 2; ++rb) {
        const float ip = __builtin_amdgcn_rcpf(sp[rb]), in = __builtin_amdgcn_rcpf(sn[rb]);
        qp[rb][0] = __builtin_bit_cast(bf16x8, u32x4{pk2(ep[rb][0] * ip, ep[rb][1] * ip), pk2(ep[rb][2] * ip, ep[rb][3] * ip), pk2(ep[rb][4] * ip, ep[rb][5] * ip), pk2(ep[rb][6] * ip, ep[rb][7] * ip)});
        qp[rb][1] = __builtin_bit_cast(bf16x8, u32x4{pk2(en[rb][0] * in, en[rb][1] * in), pk2(en[rb][2] * in, en[rb][3] * in), pk2(en[rb][4] * in, en[rb][5] * in), pk2(en[rb][6] * in, en[rb][7] * in)});
        asm volatile("" : "+v"(qp[rb][0]), "+v"(qp[rb][1]));
      }
    };
    asm volatile(G1_KV : "+{v[48:63]}"(pa) : [rs] "s"(rsW) : "memory", "v80", "v81", "v82", "v83", "v84", "v85", "v86", "v87", "v92", "v93", "v94", "v95", "v96", "v97", "v98", "v99", "v100", "v101", "v102", "v103", "v104", "v105", "v106", "v107", "v108", "v109", "v110", "v111", "v112", "v113", "v114", "v115", "v116", "v117", "v118", "v119", "v120", "v121", "v122", "v123", "v124", "v125", "v126", "v127", "v128", "v129", "v130", "v131", "v132", "v133", "v134", "v135", "v144", "v145", "v146", "v147", "v152", "v153", "v154", "v155", "v156", "v157", "v158", "v159");
    auto wo_late = [&](int t) { glds2v(wos + knib(kol, t) * tO, pwl, pwm, sb + OFF_WO + t * 8192 + (w & 3) * 2048); };
    ST(layer * 24 + 6)
    u32x4 l1[8];
    const bool red = w == 7 || (w == 6 && (rt & 7) == 0);
    unsigned lo = kvo + (unsigned)((2 * hp + (rt >> 3) + (lane >> 5) * 8 * NH) * KVPB) +
                  (w == 7 ? (unsigned)((rt & 4) * 512 + (g & 1) * 1024 + (rt & 3) * 256 + fr * 16) : (unsigned)(4096 + (lane & 7) * 16));
    unsigned fs = w == 7 ? kfo + (unsigned)((rt >> 2) * 2048 + (lane >> 5) * 1024 + (rt & 3) * 256 + fr * 16 + (g & 1) * 8)
                : (lane & 24) ? 0x7fff0000u : kfo + (unsigned)(8192 + (rt >> 3) * 128 + (lane & 4) * 16 + (lane & 1) * 32 + (lane >> 5) * 16 + (lane & 2) * 4);
    auto l1_load = [&]() {
      asm volatile("" ::: "memory");
#pragma unroll
      for (int p = 0; p < 8; ++p) asm volatile("buffer_load_dwordx4 %0, %1, %2, %3 offen sc1" : "=v"(l1[p]) : "v"(lo), "s"(rsW), "s"(p * (NH * KVPB)) : "memory");
      __builtin_amdgcn_sched_barrier(0);
    };
    if (red) asm volatile("" : "+v"(lo), "+v"(fs));
    if (red) { __builtin_amdgcn_s_setprio(3); __builtin_amdgcn_s_sleep(13); l1_load(); }
    bf16x8 qp2[2][2];
    if (!kg) {
      phi2(qh, st23, qp2);
    } else if (red) {
      typedef __attribute__((ext_vector_type(2))) __bf16 bf2;
      unsigned c0 = 0x00003F80u, c1 = 0x3F800000u;
      asm volatile("" : "+v"(c0), "+v"(c1));
      float s4[4];
      _Pragma("unroll 1") for (unsigned spins = 0; spins < (1u << 18); ++spins) {
        float a[8] = {zero1(), zero1(), zero1(), zero1(), zero1(), zero1(), zero1(), zero1()};
#pragma unroll
        for (int p = 0; p < 8; ++p)
#pragma unroll
          for (int e = 0; e < 4; ++e) {
            if (e == 0) { __builtin_amdgcn_sched_barrier(0); asm volatile("s_waitcnt vmcnt(%1)" : "+v"(l1[p]) : "n"(7 - p) : "memory"); }
            const unsigned u = l1[p][e];
            a[2 * e] = __builtin_amdgcn_fdot2_f32_bf16(__builtin_bit_cast(bf2, u), __builtin_bit_cast(bf2, c0), a[2 * e], false);
            a[2 * e + 1] = __builtin_amdgcn_fdot2_f32_bf16(__builtin_bit_cast(bf2, u), __builtin_bit_cast(bf2, c1), a[2 * e + 1], false);
          }
#pragma unroll
        for (int i = 0; i < 4; ++i) {
          auto r = __builtin_amdgcn_permlane32_swap(__float_as_uint(a[i]), __float_as_uint(a[i + 4]), false, false);
          s4[i] = __uint_as_float(r[0]) + __uint_as_float(r[1]);
        }
        const float t = s4[0] + s4[2];
        if (__all(t == t)) break;
        unsigned mxw = 0;
#pragma unroll
        for (int p = 0; p < 8; ++p)
#pragma unroll
          for (int e = 0; e < 4; ++e) mxw = l1[p][e] > mxw ? l1[p][e] : mxw;
        if (__all(mxw != EMPTY)) break;
        l1_load();
      }
      WAITV(0);
      unsigned o0 = pk2(s4[0], s4[1]), o1 = pk2(s4[2], s4[3]);
      o0 = o0 < EMPTY - 1u ? o0 : EMPTY - 1u; o1 = o1 < EMPTY - 1u ? o1 : EMPTY - 1u;
      __builtin_amdgcn_raw_buffer_store_b64(u32x2{o0, o1}, rsW, fs, 0, 16);
      __builtin_amdgcn_s_setprio(0);
    }
    ST(layer * 24 + 8)
    WAITL0;
    BAR;
    ST(layer * 24 + 9)
    if (kg) {
#pragma unroll
      for (int i = 6; i < 12; ++i) wo_late(i);
    } else {
      unsigned fo = kfo + (unsigned)(wn * 4096 + lane * 16), fz = kfo + (unsigned)(8192 + wn * 128 + g * 16);
      asm volatile("" : "+v"(fo), "+v"(fz));
      bf16x8 qp[2][2];
      phi2(acc, st01, qp);
      asm volatile(G1_OUTPUT
          :: "{v[224:227]}"(qp[0][0]), "{v[228:231]}"(qp[0][1]), "{v[232:235]}"(qp[1][0]), "{v[236:239]}"(qp[1][1]),
             "{v[240:243]}"(qp2[0][0]), "{v[244:247]}"(qp2[0][1]), "{v[248:251]}"(qp2[1][0]), "{v[252:255]}"(qp2[1][1]),
             [fo] "v"(fo), [fz] "v"(fz), [go] "v"(ogo), [lo] "v"(opo), [rs] "s"(rsW)
          : "memory", "vcc", "scc", "m0", "v152", "v153", "v154", "v155", "v156", "v157", "v158", "v159", "v160", "v161", "v162", "v163", "v164", "v165", "v166", "v167", "v168", "v169", "v170", "v171", "v172", "v173", "v174", "v175", "v176", "v177", "v178", "v179", "v180", "v181", "v182", "v183", "v184", "v185", "v186", "v187", "v188", "v189", "v190", "v191", "v192", "v193", "v194", "v195", "v196", "v197", "v198", "v199", "v200", "v201", "v202", "v203", "v204", "v205", "v206", "v207", "v208", "v209", "v210", "v211", "v212", "v213", "v214", "v215", "v216", "v217", "v218", "v219", "v220", "v221", "v222", "v223");
    }
  }
  ST(layer * 24 + 12)
  if (first && w >= 4 && w < 7) {
    typedef __attribute__((ext_vector_type(2))) __bf16 bf2;
    unsigned c0 = 0x00003F80u, c1 = 0x3F800000u;
    asm volatile("" : "+v"(c0), "+v"(c1));
    const bf2 lo1 = __builtin_bit_cast(bf2, c0), hi1 = __builtin_bit_cast(bf2, c1);
    float a[8] = {zero1(), zero1(), zero1(), zero1(), zero1(), zero1(), zero1(), zero1()};
#pragma unroll
    for (int i = 0; i < 8; ++i) {
      asm volatile("" : "+v"(pw[i]));
#pragma unroll
      for (int e = 0; e < 4; ++e) {
        const unsigned u = pw[i][e];
        a[2 * e] = __builtin_amdgcn_fdot2_f32_bf16(__builtin_bit_cast(bf2, u), lo1, a[2 * e], false);
        a[2 * e + 1] = __builtin_amdgcn_fdot2_f32_bf16(__builtin_bit_cast(bf2, u), hi1, a[2 * e + 1], false);
      }
    }
#pragma unroll
    for (int i = 0; i < 4; ++i) {
      auto r = __builtin_amdgcn_permlane32_swap(__float_as_uint(a[i]), __float_as_uint(a[i + 4]), false, false);
      a[i] = __uint_as_float(r[0]) + __uint_as_float(r[1]);
    }
#pragma unroll
    for (int i = 0; i < 2; ++i) {
      auto r = __builtin_amdgcn_permlane16_swap(__float_as_uint(a[i]), __float_as_uint(a[i + 2]), false, false);
      a[i] = __uint_as_float(r[0]) + __uint_as_float(r[1]);
      a[i] += dpp_ror<8>(a[i]);
    }
    if (!(lane & 8))
      __builtin_amdgcn_raw_buffer_store_b64(u32x2{__float_as_uint(a[0]), __float_as_uint(a[1])}, rsW,
          (unsigned)(WS_CSP + ((((hp + (1 << (w - 4))) & 15) * 16 + rt) * 192 + (w - 4) * 64 + (lane & 7) * 8 + (lane >> 5) * 4 + ((lane >> 4) & 1) * 2) * 4), 0, 16);
  }
  }
}

#undef pcs
#undef prs
#undef opo
#undef ogo
#undef hof
#undef dm
#undef dm2
#undef pcv
#undef pwf
#undef prv
#undef pvf
#undef ppk
#undef ppa
#undef pvr
#undef gkv
#undef gkz
#undef pwl
#undef pwm
#undef plb
#undef prd
#undef dm3

__device__ __forceinline__ void g2_phase(char* smem, char* ws, u64 E, int w, f32x16& xq, Next& nx, int layer, const u16* __restrict__ x0,
                                         const u16* __restrict__ wo, const u16* __restrict__ wnx, u16* __restrict__ out) {
  IDS
  wnx = uni(wnx);
  const unsigned sb = lds_addr(smem);
  const int lw = w & 3, k2 = 16 * lw + (lane >> 3);
  unsigned pxr, rmo;
  auto ends = [&]() {
    const int lane = lane_id(), r0 = w * 16 + (lane >> 3);
    pxr = sb + r0 * 128 + (lane & 7) * 16;
    rmo = (unsigned)(((m0 + r0) * DM + hp * 64 + ((lane & 7) ^ (lane >> 3)) * 8) * 2);
    asm volatile("" : "+v"(pxr), "+v"(rmo));
  };
  constexpr int S0 = 0, S1 = 16384, S2 = 32768, S3 = 147456, S4 = OFF_WO, S5 = 98304, S6 = 114688;
  constexpr int AS[16] = {S0, S1, S2, S3, S0, S1, S4, S2, S3, S0, S1, S4, S5, S3, S0, S6};
  constexpr int OFF_C = 0;
  auto bs = [](int t) { return OFF_WO + (t < 12 ? t : t - 10) * 8192; };
  const char *xa1, *xa2, *wq2;
  u64 fm;
  unsigned vB0, vB1, vXA, po, pst;
  if (w >= 4) {
    constexpr int WT[15] = {0, 8, 12, 14, 16, 18, 20, 18, 16, 14, 12, 12, 10, 10, 8};
    const char* o = ws + WS_O + layer * (SEQ * DM * 2) + rt * 262144;
    const unsigned vA = (unsigned)(lw * 4096 + lane * 16);
    u64 kord = korder(bid & 3, hp);
    auto ldA = [&](int s) { glds4c(o + knib(kord, s) * 16384, vA, sb + AS[s] + lw * 4096); };
    const bool im = img(layer, hp), in = img(layer + 1, hp);
    unsigned vW0 = im ? (unsigned)(lw * 2048 + lane * 16) : (unsigned)((k2 * DM + hp * 64 + ((lane & 7) ^ ((lane >> 4) << 1)) * 8) * 2), vW1 = vW0 + (im ? 0u : 8 * DM * 2 - 1024u);
    const char *wos = wo_src(ws, wo, layer, hp), *wqs = wq_src(ws, wnx, layer + 1, hp);
    unsigned tO = im ? 8192u : 131072u;
    asm volatile("" : "+v"(vW0), "+v"(vW1));
    asm volatile("" : "+s"(wos), "+s"(tO));
    auto ldW = [&](int t) { glds2v(wos + knib(kord, t) * tO, vW0, vW1, sb + bs(t) + lw * 2048); };
    const int fB = (k2 >> 1) & 1;
    vB0 = in ? (unsigned)(lw * 6144 + lane * 16) : (unsigned)((k2 * 3072 + hp * 64 + (((((lane & 7) >> 1) ^ fB) << 1) | (lane & 1)) * 8) * 2);
    vB1 = in ? vB0 : (unsigned)(((k2 + 8) * 3072 + hp * 64 + (((((lane & 7) >> 1) ^ (fB | 2)) << 1) | (lane & 1)) * 8) * 2) - 1024u;
    const u64 kn = nx.kord = korder(kv1(bid), hp);
    nx.ko2 = kord; nx.wq = wqs; nx.tw = in ? 24576u : 393216u;
    const char *wq0 = wqs + hp * (in ? 24576u : 393216u), *wq1 = wqs + knib(kn, 1) * (in ? 24576u : 393216u);
    wq2 = wqs + knib(kn, 2) * (in ? 24576u : 393216u);
    xa1 = ws + WS_X + layer * (SEQ * DM * 2) + knib(kn, 1) * 16384; xa2 = ws + WS_X + layer * (SEQ * DM * 2) + knib(kn, 2) * 16384;
    vXA = (unsigned)(rt * 262144 + lw * 4096 + lane * 16);
    fm = kfirst(kn);
    po = ((fm >> lane) & 1) ? WS_FLAGS + (unsigned)(256 + rt * 16 + lane) * FLS : 0u;
    pst = g != q4 ? 0x7fff0000u : WS_ST + layer * (SEQ * 128) + (unsigned)(((rt * 16 + hp) * 128 + lw * 32 + fr) * 8);
    asm volatile("" : "+v"(vB0), "+v"(vB1), "+v"(vXA), "+v"(po), "+v"(pst));
    asm volatile("" : "+s"(xa1), "+s"(xa2), "+s"(wq2), "+s"(fm), "+s"(nx.kord), "+s"(nx.ko2), "+s"(nx.wq), "+s"(nx.tw));
    asm volatile("" : "+s"(wq0), "+s"(wq1));
    auto ldB = [&](int s, int part) {
      const char* b = s ? wq1 : wq0;
      if (part) glds4v(b, vB0, vB1, vB0, vB1, sb + s * 40960 + 16384 + lw * 6144);
      else glds2v(b + 4096, vB0, vB1, sb + s * 40960 + 16384 + lw * 6144 + 4096);
    };
    ends();
    const char* o1 = o + knib(kord, 1) * 16384;
    asm volatile("" : "+s"(o1), "+s"(kord), "+s"(o));
    asm volatile("" :: "v"(vA));
    if (layer) { BAR;   }
    BAR;
    glds4c(o1, vA, sb + AS[1] + lw * 4096); __builtin_amdgcn_s_sleep(3); ldA(2); __builtin_amdgcn_s_sleep(3); ldA(3); __builtin_amdgcn_s_sleep(3); ldA(4);
    WAITV(12);
    BAR;
    auto step = [&](auto ktc) {
      constexpr int kt = decltype(ktc)::value;
      WAITVN(WT[kt]);
      BAR;
      if constexpr (kt == 1) { ldA(5); ldA(6); }
      if constexpr (kt >= 2 && kt <= 10) ldA(kt + 5);
      if constexpr (kt >= 2 && kt <= 5) ldW(kt + 10);
      if constexpr (kt >= 11) ldB((kt - 11) >> 1, (kt - 11) & 1);
      if constexpr (kt >= 6) { __builtin_amdgcn_s_sleep(kt >= 11 ? 3 : 2); if (layer == 1) __builtin_amdgcn_s_sleep(1); }
    };
    step(IC<1>{}); step(IC<2>{}); step(IC<3>{}); step(IC<4>{}); step(IC<5>{}); step(IC<6>{}); step(IC<7>{});
    step(IC<8>{}); step(IC<9>{}); step(IC<10>{}); step(IC<11>{}); step(IC<12>{}); step(IC<13>{}); step(IC<14>{});
  } else {
    asm volatile("" : "=v"(vB0), "=v"(vB1), "=v"(vXA), "=v"(po), "=v"(pst));
    asm volatile("" : "=s"(xa1), "=s"(xa2), "=s"(wq2), "=s"(fm), "=s"(nx.kord), "=s"(nx.ko2), "=s"(nx.wq), "=s"(nx.tw));
    const int wm = w >> 1, wn = w & 1;
    f32x4 acc[4][2];
    const int fl = (q4 >> 1) | ((g & 1) << 1);
    const int bb0 = (4 * g + q4) * 128 + p4 * 8 + (((wn * 2) ^ fl) << 5), bb1 = (4 * g + q4) * 128 + p4 * 8 + (((wn * 2 + 1) ^ fl) << 5);
    const int abase = wm * 4096 + lane * 16;
    bf16x8 fa[2][2][4], fb[2][2][2];
    unsigned ra[3], rb0[2], rb1[2];
#pragma unroll
    for (int i = 0; i < 3; ++i) { ra[i] = abase + i * 65536; asm volatile("" : "+v"(ra[i])); }
#pragma unroll
    for (int i = 0; i < 2; ++i) { rb0[i] = bb0 + OFF_WO + i * 49152; rb1[i] = bb1 + OFF_WO + i * 49152; asm volatile("" : "+v"(rb0[i]), "+v"(rb1[i])); }
    auto loadf = [&](int kt, bf16x8 (&a)[2][4], bf16x8 (&b)[2][2]) {
#pragma unroll
      for (int s = 0; s < 2; ++s) {
        const char* sA = smem + ra[AS[kt] >> 16] + (AS[kt] & 65535) + s * 8192;
        const int hi = bs(kt) >= OFF_WO + 49152, ob = bs(kt) - OFF_WO - hi * 49152 + s * 4096;
        b[s][0] = cat8(tr_read(smem + rb0[hi] + ob), tr_read(smem + rb0[hi] + ob + 2048));
        b[s][1] = cat8(tr_read(smem + rb1[hi] + ob), tr_read(smem + rb1[hi] + ob + 2048));
#pragma unroll
        for (int rb = 0; rb < 4; ++rb) a[s][rb] = *(const bf16x8*)(sA + rb * 1024);
      }
    };
    auto mma = [&](bf16x8 (&a)[2][4], bf16x8 (&b)[2][2]) {
#pragma unroll
      for (int i = 0; i < 16; ++i) {
        const int rb = i >> 2, cb = ((i >> 1) ^ rb) & 1, s = ((i + 1) >> 1) & 1;
        acc[rb][cb] = MFMA(b[s][cb], a[s][rb], acc[rb][cb]);
      }
    };
    unsigned fo = lane < 16 && lane != hp ? WS_FLAGS + (unsigned)(rt * 16 + lane) * FLS : 0u;
    unsigned kf = nx.kf & 0xffffu, kf2 = nx.kf >> 16;
    unsigned jo = (unsigned)((((rt & 7) * 128 + (w - 1) * 64 + lane) * 3072 + (hp & 3) * 1024 + (hp | 3) * 64) * 2);
    unsigned fo1 = ((kf >> (lane & 31)) & 1) ? fo : 0u, fo2 = ((kf2 >> (lane & 31)) & 1) ? fo : 0u;
    fo = (((kf | kf2) >> (lane & 31)) & 1) ? 0u : fo;
    asm volatile("" : "+v"(fo), "+v"(fo1), "+v"(fo2));
    asm volatile("" : "+s"(kf), "+s"(kf2));
    publish(rsW, tid, rt * 16 + hp, E + (unsigned)(layer + 1));
    ST(layer * 24 + 13)
    u64 fl1;
    asm volatile("" : "=v"(fl1));
    if (w == 0) asm volatile("global_load_dwordx2 %0, %1, %2 sc1" : "+v"(fl1) : "v"(fo1), "s"(ws) : "memory");
    if (layer == 0) {
      const unsigned xo = (unsigned)(((m0 + wm * 64 + fr) * DM + hp * 64 + wn * 32 + g * 8) * 2);
      asm volatile("global_load_dwordx4 v[64:67], %1, %3\n\tglobal_load_dwordx4 v[68:71], %1, %4\n\tglobal_load_dwordx4 v[72:75], %2, %3\n\tglobal_load_dwordx4 v[76:79], %2, %4"
                   : "+{v[64:79]}"(xq) : "v"(xo), "v"(xo + 32 * DM * 2), "s"(x0), "s"(x0 + 16 * DM) : "memory");
    }
    loadf(0, fa[0], fb[0]);
    unsigned junk;
    asm volatile("" : "=v"(junk));
    if (layer < 2 && w == 3 && (bid >> 6) == 0) code_touch(junk, bid, lane);
    if (layer == 0 && (hp & 7) < 3 && (w == 1 || w == 2))
      asm volatile("global_load_dword %0, %1, %2" : "+v"(junk) : "v"(jo), "s"(wnx) : "memory");
    __builtin_amdgcn_sched_barrier(0);
    bf16x8 id[2];
#pragma unroll
    for (int cb = 0; cb < 2; ++cb) {
      const unsigned one = (g >> 1) == cb && (g & 1) == (fr >> 3) ? 0x3F80u << ((fr & 1) * 16) : 0u;
      u32x4 t;
#pragma unroll
      for (int j = 0; j < 4; ++j) t[j] = ((fr >> 1) & 3) == j ? one : 0u;
      id[cb] = __builtin_bit_cast(bf16x8, t);
      asm volatile("" : "+v"(id[cb]));
    }
    const int l2 = lane_id(), ch = (l2 >> 5) | ((l2 >> 3) & 2), f2 = l2 & 15;
    unsigned pl = (unsigned)(wm * 8192 + wn * 1024 + ch * 256 + f2 * 16);
    unsigned pg = layer < 3 ? WS_X + layer * (SEQ * DM * 2) + (unsigned)((rt * 16 + hp) * 16384) + pl : 0x7fff0000u;
    pl = sb + OFF_C + (layer < 3 ? pl : (unsigned)((wm * 64 + f2) * 128 + (((wn * 4 + ch) ^ (f2 & 7)) << 4)));
    asm volatile("" : "+v"(pl), "+v"(pg));
    ends();
#pragma unroll
    for (int rb = 0; rb < 4; ++rb) { acc[rb][0] = f32x4{0, 0, 0, 0}; acc[rb][1] = f32x4{0, 0, 0, 0}; }
    mma(fa[0], fb[0]);
    __builtin_amdgcn_sched_barrier(0);
    WAITL0;
    u64 miss = 0;
    if (w == 0) { ARRIVED("+v"(fl1)); if (__ballot(fl1 != E + (unsigned)(layer + 1)) & kf) poll_eq(ws, fo1, E + (unsigned)(layer + 1), kf, kf); miss = 0xffffu & ~(kf | kf2 | (1u << hp)); }
    ST(layer * 24 + 14)
    __builtin_amdgcn_sched_barrier(0);
    BAR;
    __builtin_amdgcn_sched_barrier(0);
    asm volatile("" ::: "memory");
    if (layer == 0) WAITV(0);
    __builtin_amdgcn_sched_barrier(0);
    asm volatile("" : "+{v[64:79]}"(xq));
    const bf16x8 xf[4] = {__builtin_bit_cast(bf16x8, quad<0>(xq)), __builtin_bit_cast(bf16x8, quad<1>(xq)), __builtin_bit_cast(bf16x8, quad<2>(xq)), __builtin_bit_cast(bf16x8, quad<3>(xq))};
#pragma unroll
    for (int rb = 0; rb < 4; ++rb) { acc[rb][0] = MFMA(id[0], xf[rb], acc[rb][0]); acc[rb][1] = MFMA(id[1], xf[rb], acc[rb][1]); }
    if (w == 0) poll_eq(ws, fo2, E + (unsigned)(layer + 1), kf2, kf2);
    u64 fl2;
    asm volatile("" : "=v"(fl2));
    auto step = [&](auto ktc) {
      constexpr int kt = decltype(ktc)::value;
      __builtin_amdgcn_sched_barrier(0);
      if (kt == 5 && miss) {
        ARRIVED("+v"(fl2));
        if (__ballot(fl2 != E + (unsigned)(layer + 1)) & miss) poll_eq(ws, fo, E + (unsigned)(layer + 1), miss, ~0ull);
      }
      WAITL0;
      BAR;
      if (kt == 2 && miss) asm volatile("global_load_dwordx2 %0, %1, %2 sc1" : "+v"(fl2) : "v"(fo), "s"(ws) : "memory");
      if (kt == 0) { ST(layer * 24 + 15) }
      __builtin_amdgcn_sched_barrier(0);
      loadf(kt + 1, fa[(kt + 1) & 1], fb[(kt + 1) & 1]);
      if (kt == 0) return;
      mma(fa[kt & 1], fb[kt & 1]);
#pragma unroll
      for (int i = 0; i < 4; ++i) {
        __builtin_amdgcn_sched_group_barrier(0x008, 1, 0);
        __builtin_amdgcn_sched_group_barrier(0x100, 1, 0);
        __builtin_amdgcn_sched_group_barrier(0x008, 1, 0);
        __builtin_amdgcn_sched_group_barrier(0x100, 3, 0);
      }
#pragma unroll
      for (int i = 0; i < 8; ++i) __builtin_amdgcn_sched_group_barrier(0x008, 1, 0);
    };
    step(IC<0>{}); step(IC<1>{}); step(IC<2>{}); step(IC<3>{}); step(IC<4>{}); step(IC<5>{}); step(IC<6>{}); step(IC<7>{});
    step(IC<8>{}); step(IC<9>{}); step(IC<10>{}); step(IC<11>{}); step(IC<12>{}); step(IC<13>{}); step(IC<14>{});
    WAITV(0);
    asm volatile("" :: "v"(junk));
    __builtin_amdgcn_sched_barrier(0);
    u32x4 xv[4];
    auto put = [&](int rb) {
      auto a = __builtin_amdgcn_permlane16_swap(pk2(acc[rb][0][0], acc[rb][0][1]), pk2(acc[rb][1][0], acc[rb][1][1]), false, false);
      auto b = __builtin_amdgcn_permlane16_swap(pk2(acc[rb][0][2], acc[rb][0][3]), pk2(acc[rb][1][2], acc[rb][1][3]), false, false);
      xv[rb] = u32x4{a[0], b[0], a[1], b[1]};
      __builtin_amdgcn_raw_buffer_store_b128(xv[rb], rsW, pg + (rb & 1) * 2048, (rb >> 1) * 4096, 16);
    };
#pragma unroll
    for (int rb = 0; rb < 4; ++rb)
#pragma unroll
      for (int cb = 0; cb < 2; ++cb) acc[rb][cb] = MFMA(fb[1][0][cb], fa[1][0][rb], acc[rb][cb]);
#pragma unroll
    for (int rb = 0; rb < 4; ++rb) {
      __builtin_amdgcn_sched_barrier(0);
#pragma unroll
      for (int cb = 0; cb < 2; ++cb) acc[rb][cb] = MFMA(fb[1][1][cb], fa[1][1][rb], acc[rb][cb]);
      __builtin_amdgcn_sched_barrier(0);
      if (rb >= 2) put(rb - 2);
    }
    __builtin_amdgcn_sched_barrier(0);
    ST(layer * 24 + 16)
    put(2); put(3);
    __builtin_amdgcn_sched_barrier(0);
#pragma unroll
    for (int rb = 0; rb < 4; ++rb) lds<u32x4>(pl + rb * 2048) = xv[rb];
  }
  if (layer == 3) {
    WAITL0; BAR;
    asm volatile("" ::: "memory");
    const __amdgpu_buffer_rsrc_t rsO = __builtin_amdgcn_make_buffer_rsrc((void*)uni(out), (short)0, SEQ * DM * 2, 0x00020000);
    __builtin_amdgcn_raw_buffer_store_b128(lds<u32x4>(pxr + OFF_C), rsO, rmo, 0, 16);
    __builtin_amdgcn_raw_buffer_store_b128(lds<u32x4>(pxr + OFF_C + 1024), rsO, rmo, 8 * DM * 2, 16);
  }
  ST(layer * 24 + 17)
  if (layer < 3 && w >= 4) {
    WAITV(0); BAR;
    asm volatile("" ::: "memory");
    glds4v(wq2, vB0, vB1, vB0, vB1, sb + 2 * 40960 + 16384 + lw * 6144);
    glds2v(wq2 + 4096, vB0, vB1, sb + 2 * 40960 + 16384 + lw * 6144 + 4096);
    __builtin_amdgcn_s_setprio(3);
    bf16x8 xn[4];
#pragma unroll
    for (int i = 0; i < 4; ++i) xn[i] = lds<bf16x8>(sb + OFF_C + lw * 4096 + lane * 16 + i * 1024);
    const bf16x8 ones = ones8();
    u32x2 h[2];
#pragma unroll
    for (int i = 0; i < 2; ++i) {
      const f32x4 z4 = {0, 0, 0, 0};
      const f32x4 sa = MFMA(ones, xn[2 * i + 1], MFMA(ones, xn[2 * i], z4)), sq = MFMA(xn[2 * i + 1], xn[2 * i + 1], MFMA(xn[2 * i], xn[2 * i], z4));
      const float a = (lane & 1) ? sq[1] : sq[0], b = (lane & 1) ? sq[3] : sq[2];
      h[i] = u32x2{__float_as_uint(sa[0]), __float_as_uint((lane & 2) ? b : a)};
    }
    __builtin_amdgcn_s_setprio(0);
    u64 fl;
    asm volatile("" : "=v"(fl), "+v"(pst) : "v"(h[0]), "v"(h[1]));
    if (lw == 0) asm volatile("global_load_dwordx2 %0, %1, %2 sc1" : "+v"(fl) : "v"(po), "s"(ws) : "memory");
    __builtin_amdgcn_raw_buffer_store_b64(h[0], rsW, pst, 0, 16);
    __builtin_amdgcn_raw_buffer_store_b64(h[1], rsW, pst + 128, 0, 16);
    if (lw == 0) {
      WAITV(2);
      __builtin_amdgcn_sched_barrier(0);
      asm volatile("" : "+v"(fl));
      if (__ballot(fl != E + (unsigned)(layer + 1)) & fm) poll_eq(ws, po, E + (unsigned)(layer + 1), fm, ~0ull);
    }
    BAR;
    glds4c(xa1, vXA, sb + 40960 + lw * 4096); glds4c(xa2, vXA, sb + 2 * 40960 + lw * 4096);
  }
  ST(layer * 24 + 18)
}

__global__ __launch_bounds__(512, 2) void mega_kernel(const u16* __restrict__ x0, const u16* __restrict__ wqkvA,
                                                      const u16* __restrict__ woA, const u16* __restrict__ wqkvB,
                                                      const u16* __restrict__ wphi, const u16* __restrict__ woB,
                                                      u16* __restrict__ out, char* __restrict__ ws) {
  extern __shared__ __attribute__((aligned(16))) char smem[];
  {
    unsigned long long pc;
    unsigned t;
    asm volatile("s_getpc_b64 %0\n.Lmk_pc:\n\tv_lshl_add_u32 %1, %2, 9, %3\n\tv_lshlrev_b32 %1, 2, %1\n\tv_and_b32 %1, 0xffffff80, %1\n\ts_mov_b32 m0, %4\n\t"
                 "v_min_u32 %1, .Lmk_end-.Lmk_pc-4, %1\n\tglobal_load_lds_dword %1, %0" : "=&s"(pc), "=&v"(t) : "s"(blockIdx.x >> 3), "v"(threadIdx.x), "n"(LDS_TOTAL - 1024) : "memory", "m0");
  }
  const int w = __builtin_amdgcn_readfirstlane(threadIdx.x >> 6);
  u64 E = 0;
  {
    const int bid = blockIdx.x, lane = threadIdx.x & 63;
    const int rt = 2 * (bid & 4) + ((bid >> 3) & 7), hp = 4 * (bid & 3) + (bid >> 6), m0 = rt * 128;
    if (w < 4) {
      const unsigned xo = (unsigned)((m0 + hp * 8 + w * 2) * (DM * 2) + lane * 16);
      glds4v(x0, xo, xo, xo, xo, lds_addr(smem) + 122880 + w * 4096);
    } else {
      const int lw = w & 3;
      const int kB = 16 * lw + (lane >> 3), fB = (kB >> 1) & 1;
      const unsigned vB0 = (unsigned)((kB * 3072 + hp * 64 + (((((lane & 7) >> 1) ^ fB) << 1) | (lane & 1)) * 8) * 2);
      const unsigned vB1 = (unsigned)(((kB + 8) * 3072 + hp * 64 + (((((lane & 7) >> 1) ^ (fB | 2)) << 1) | (lane & 1)) * 8) * 2) - 1024u;
      const unsigned vA = (unsigned)(((m0 + lw * 32 + (lane >> 3)) * DM + ((lane & 7) ^ (lane >> 3)) * 8) * 2);
#pragma unroll
      for (int s = 0; s < 2; ++s) {
        const int kt = (rt & 8) + s;
        const unsigned sA = lds_addr(smem) + s * 40960, sB = sA + 16384;
        glds4v(x0 + kt * 64, vA, vA + 15360u, vA + 2 * 15360u, vA + 3 * 15360u, sA + lw * 4096);
        const u16* b = wqkvA + (size_t)kt * 64 * 3072;
        glds4v(b, vB0, vB1, vB0, vB1, sB + lw * 6144);
        glds2v(b + 2048, vB0, vB1, sB + lw * 6144 + 4096);
      }
    }
  }
  asm volatile("" :: "s"(woA), "s"(wqkvB), "s"(wphi), "s"(woB), "s"(out), "s"(ws));
  f32x16 xq;
  Next nx;
  asm volatile("" : "=s"(nx.kord), "=s"(nx.ko2), "=s"(nx.wq), "=s"(nx.tw), "=s"(nx.kf));
#pragma unroll 1
  for (int layer = 0; layer < 4; ++layer) {
    if (layer & 1) g1_phase<1>(smem, ws, E, w, xq, nx, layer, x0, wqkvB, woB, wphi, wqkvA, woA);
    else g1_phase<0>(smem, ws, E, w, xq, nx, layer, x0, wqkvA, woA, nullptr, wqkvB, woB);
    g2_phase(smem, ws, E, w, xq, nx, layer, x0, (layer & 1) ? woB : woA, (layer & 1) ? wqkvA : wqkvB, out);
  }
  asm volatile(".Lmk_end:");
  WAITV(0);
  if (blockIdx.x == 0 && w == 0 && lane_id() == 0) __hip_atomic_store((gu64*)ws, E + 16u, __ATOMIC_RELAXED, __HIP_MEMORY_SCOPE_AGENT);
}

extern "C" void kernel_launch(void* const* d_in, const int* in_sizes, int n_in,
                              void* d_out, int out_size, void* d_ws, size_t ws_size,
                              hipStream_t stream) {
  hipFuncSetAttribute((const void*)mega_kernel, hipFuncAttributeMaxDynamicSharedMemorySize, LDS_TOTAL);
  mega_kernel<<<256, 512, LDS_TOTAL, stream>>>((const u16*)d_in[0], (const u16*)d_in[1], (const u16*)d_in[2], (const u16*)d_in[4],
                                               (const u16*)d_in[5], (const u16*)d_in[6], (u16*)d_out, (char*)d_ws);
}
```
